```python
import torch

def _softmax(x, axis=-1):
    return torch.softmax(x, dim=axis)
def _gelu(x):
    return torch.nn.functional.gelu(x, approximate="tanh")
def _silu(x):
    return torch.nn.functional.silu(x)
def _sigmoid(x):
    return torch.sigmoid(x)
def _layernorm(x, axis=-1, eps=1e-5):
    m = x.mean(dim=axis, keepdim=True)
    v = x.var(dim=axis, keepdim=True, unbiased=False)
    return (x - m) / torch.sqrt(v + eps)
def _scatter_add(tgt, idx, src, axis=0):
    return tgt.clone().index_add_(axis, idx, src)
def _segment_sum(src, seg, n):
    out = torch.zeros((n,) + src.shape[1:], dtype=src.dtype)
    return out.index_add_(0, seg, src)
def _pad(x, pw):
    flat = tuple(v for pair in reversed(pw) for v in pair)
    return torch.nn.functional.pad(x, flat)

kernel_name = 'standalone__hybrid_graph_tf_graphgps__graph'

def setup_inputs():

    _w = (lambda a: (lambda t: t.to(torch.bfloat16) if t.is_floating_point() else t)(torch.as_tensor(a)))
    N, deg, D = 10000, 16, 256
    E = N * deg
    src = (lambda lo, hi, *s: torch.randint(lo, hi, s))(0, N, E).to(torch.int64)
    dst = (lambda lo, hi, *s: torch.randint(lo, hi, s))(0, N, E).to(torch.int64)
    deg_cnt = torch.bincount(dst, minlength=N).to(float) + 1.0
    sc = 1.0 / float(D) ** 0.5
    return {
        "x": _w((lambda *s: torch.randn(*s, dtype=torch.bfloat16) * 0.05)(N, D)),
        "src": _w(src),
        "dst": _w(dst),
        "deg_inv": _w((1.0 / deg_cnt).to(float)),
        "w_msg": _w((lambda *s: torch.randn(*s, dtype=torch.bfloat16) * 0.2)(D, D) * sc),
        "wq": _w((lambda *s: torch.randn(*s, dtype=torch.bfloat16) * 1.6)(D, D) * sc),
        "wk": _w((lambda *s: torch.randn(*s, dtype=torch.bfloat16) * 1.6)(D, D) * sc),
        "wv": _w((lambda *s: torch.randn(*s, dtype=torch.bfloat16) * 0.2)(D, D) * sc),
        "w_out": _w((lambda *s: torch.randn(*s, dtype=torch.bfloat16) * 0.2)(D, D) * sc),
    }

def reference(x, src, dst, deg_inv, w_msg, wq, wk, wv, w_out):
    N, D = x.shape[0], x.shape[1]
    h = _layernorm(x, axis=-1)
    m_e = torch.einsum('nd,de->ne', h, w_msg)
    h_local = _segment_sum(m_e[src], dst, N) * deg_inv[:, None]
    q = torch.einsum('nd,de->ne', h, wq)
    k = torch.einsum('nd,de->ne', h, wk)
    v = torch.einsum('nd,de->ne', h, wv)
    logits = torch.einsum('id,jd->ij', q, k) / (float(D) ** 0.5)
    attn = _softmax(logits, axis=-1)
    h_global = torch.einsum('ij,jd->id', attn, v)
    y = torch.einsum('nd,de->ne', h_local + h_global, w_out)
    return x + y
```

```cpp
#include <hip/hip_runtime.h>
#include <hip/hip_bf16.h>
#include <stdint.h>
#pragma clang diagnostic ignored "-Winline-asm"

typedef unsigned short u16;
typedef __attribute__((ext_vector_type(8))) short bf16x8;
typedef __attribute__((ext_vector_type(4))) short bf16x4;
typedef __attribute__((ext_vector_type(4))) float f32x4;
typedef __attribute__((ext_vector_type(2))) float f32x2;
typedef __attribute__((ext_vector_type(4))) unsigned u32x4;
typedef __attribute__((ext_vector_type(2))) unsigned u32x2;
#define DI __device__ __forceinline__

constexpr int NN = 10000, DD = 256, EE = 160000;
constexpr int NATT = 252, NSP = 4;

constexpr unsigned WS_H = 0;
constexpr unsigned WS_AF = WS_H + 10240 * 512;
constexpr unsigned WS_WV = WS_AF + 131072;
constexpr unsigned WS_WM = WS_WV + 131072;
constexpr unsigned WS_XF = WS_WM + 131072;
constexpr unsigned WS_HF = WS_XF + 256 * 64;
constexpr unsigned WS_BF = WS_HF + 4096;
constexpr unsigned WS_PF = WS_BF + 32768;
constexpr unsigned WS_WF = WS_PF + 4096;
constexpr unsigned WS_LF = WS_WF + 256;
constexpr unsigned WS_EP = WS_LF + 2048;
constexpr unsigned WS_G = WS_EP + 1792;
constexpr unsigned WS_QP = WS_G + 10240 * 512;
constexpr unsigned WS_XB = WS_QP + 10240 * 512;
constexpr unsigned WS_BL = WS_XB + NATT * 3 * 24832;
constexpr unsigned WS_GL = WS_BL + 16 * 5 * 260 * 64;
constexpr unsigned WS_DBG = WS_GL + 256 * 4096;
constexpr unsigned WS_END = WS_DBG + 1024 * 128 + 8192;

DI float bf2f(unsigned x) { return __uint_as_float(x << 16); }
DI float bfhi(unsigned x) { return __uint_as_float(x & 0xffff0000u); }
DI unsigned pk2(float lo, float hi) {
  typedef __attribute__((ext_vector_type(2))) __bf16 bf2_t;
  const bf2_t r = __builtin_convertvector((f32x2){lo, hi}, bf2_t);
  return __builtin_bit_cast(unsigned, r);
}
typedef __attribute__((address_space(3))) bf16x4* lds_bf16x4_p;
DI bf16x4 tr_read(unsigned p) { return __builtin_amdgcn_ds_read_tr16_b64_v4i16((lds_bf16x4_p)p); }
DI bf16x8 as_bf(u32x4 v) { return __builtin_bit_cast(bf16x8, v); }
#define SB() __builtin_amdgcn_sched_barrier(0)
#define MFMA16(a, b, c) __builtin_amdgcn_mfma_f32_16x16x32_bf16(a, b, c, 0, 0, 0)

typedef __amdgpu_buffer_rsrc_t srd_t;
DI void drain() { asm volatile("s_waitcnt vmcnt(0)" ::: "memory"); }
DI void st_flag(srd_t w, unsigned off, unsigned tag) { __builtin_amdgcn_raw_buffer_store_b32(tag, w, off, 0, 16); }
DI void wait_flag(srd_t w, unsigned off, unsigned tag) {
#pragma unroll 1
  for (int it = 0; it < (1 << 22); ++it) {
    unsigned v;
    asm volatile("buffer_load_dword %0, %1, %2, 0 offen sc1\n s_waitcnt vmcnt(0)" : "=v"(v) : "v"(off), "s"(w) : "memory");
    if (__ballot(v != tag) == 0) break;
    __builtin_amdgcn_s_sleep(2);
    asm volatile("" ::: "memory");
  }
  asm volatile("" ::: "memory");
}

DI void prod_store(const f32x4 (&acc)[2][2], int mat, int ks, int J, int lane, srd_t wsrd) {
#pragma unroll
  for (int s = 0; s < 2; ++s) {
    u32x4 o = {pk2(acc[0][s][0], acc[0][s][1]), pk2(acc[0][s][2], acc[0][s][3]), pk2(acc[1][s][0], acc[1][s][1]), pk2(acc[1][s][2], acc[1][s][3])};
    if (ks == 7 && lane >= 48) o[3] &= 0xffffu;
    const int T = 2 * J + s;
    const int at = mat ? 131072 + ((J & 1) ^ 1) * 16384 + ks * 2048 + s * 1024 : ks * 4096 + (T & 3) * 1024;
    __builtin_amdgcn_raw_buffer_store_b128(o, wsrd, WS_WV + (T >> 2) * 32768 + at + lane * 16, 0, 16);
  }
}
DI void corr(unsigned* dl, const unsigned (&lr)[16]) {
  float a0 = 0.f, a1 = 0.f;
#pragma unroll
  for (int r = 0; r < 16; ++r) { a0 += bf2f(lr[r] & 0xffffu); a1 += bfhi(lr[r]); }
  dl[0] = pk2(a0 * -0.0625f, a1 * -0.0625f);
  dl[128] = pk2((bf2f(lr[15] & 0xffffu) - 0.25f * a0) * (1.f / 3.f), (bfhi(lr[15]) - 0.25f * a1) * (1.f / 3.f));
}
DI void product_load(char* lds, int wid, int lane, int job, const u16* __restrict__ wv, const u16* __restrict__ wm, const u16* __restrict__ wo, bool left, bool right) {
  const unsigned ldsb = (unsigned)(size_t)lds;
  const int r = lane >> 3, sw = ((lane & 7) ^ (r & 6)) * 16 + wid * 128;
  const unsigned vp = (8 * (r >> 2) + (r & 3)) * 512 + sw, vn = r * 512 + sw;
  auto image = [&](int n, const u16* w, int row0, bool nat) {
#pragma unroll
    for (int i = 0; i < 8; ++i) {
      const int a = i & 3;
      asm volatile("s_mov_b32 m0, %2\n s_nop 0\n global_load_lds_dwordx4 %0, %1"
                   :: "v"(nat ? vn : vp), "s"(w + (row0 + (i >> 2) * 32 + (nat ? 8 * a : 16 * (a & 1) + 4 * (a >> 1))) * DD),
                      "s"(ldsb + n * 32768 + (i >> 2) * 16384 + wid * 4096 + a * 1024) : "m0", "memory");
    }
  };
  if (left) {
    image(2, wv, (job >> 2) * 64, true);
    image(3, wm, (job >> 2) * 64, false);
  }
  if (right)
#pragma unroll
  for (int k = 0; k < 8; ++k)
    asm volatile("s_mov_b32 m0, %2\n s_nop 0\n global_load_lds_dwordx4 %0, %1"
                 :: "v"((lane >> 3) * 512 + (lane & 7) * 16), "s"(wo + (k * 32 + wid * 8) * DD + (job & 3) * 64), "s"(ldsb + k * 4096 + wid * 1024) : "m0", "memory");
}
DI void product_w(char* lds, int wid, int lane, int job, srd_t wsrd) {
  const int m = lane & 15, g = lane >> 4;
  const int ks = 2 * (job >> 2) + (wid >> 1), J = 2 * (job & 3) + (wid & 1);
  const unsigned ldsb = (unsigned)(size_t)lds;
  unsigned* dl = reinterpret_cast<unsigned*>(lds + 0x26c00);
  const bool hi = (m >> 3) != 0;
  const unsigned ra = (m >> 3) * 1024 + (m & 7) * 128 + (g ^ (m & 6)) * 16;
  auto frag = [&](int n, int tl, int t, int k) {
    return *reinterpret_cast<const bf16x8*>(lds + n * 32768 + tl * 16384 + (k >> 1) * 4096 + t * 2048 + (ra ^ ((k & 1) * 64)));
  };
  f32x4 av[2][2] = {}, am[2][2] = {}, ad[2] = {};
  const unsigned tb = ldsb + (job & 1) * 32768 + (8 * g + ((lane >> 2) & 3)) * 128 + (wid & 1) * 64 + (lane & 3) * 16;
  bf16x8 d0[8], d1[8], d2[8], rr[8][2], vf[8][2], mf[8][2];
#pragma unroll
  for (int kk = 0; kk < 9; ++kk) {
    if (kk < 8) {
      d0[kk] = *reinterpret_cast<const bf16x8*>(dl + 512 + 16 * kk + 4 * g); d1[kk] = *reinterpret_cast<const bf16x8*>(dl + 512 + (ks == 7 ? 128 : 0) + 16 * kk + 4 * g);
      d2[kk] = *reinterpret_cast<const bf16x8*>(dl + 768 + (ks == 7 && hi ? 128 : 0) + 16 * kk + 4 * g);
#pragma unroll
      for (int s = 0; s < 2; ++s) {
        rr[kk][s] = __builtin_shufflevector(tr_read(tb + kk * 4096 + s * 8), tr_read(tb + kk * 4096 + s * 8 + 512), 0, 1, 2, 3, 4, 5, 6, 7);
        vf[kk][s] = frag(2, wid >> 1, s, kk); mf[kk][s] = frag(3, wid >> 1, s, kk);
      }
    }
    SB();
    if (kk) {
      const int k = kk - 1;
#pragma unroll
      for (int s = 0; s < 2; ++s) {
        av[0][s] = MFMA16(d0[k], rr[k][s], av[0][s]); av[1][s] = MFMA16(d1[k], rr[k][s], av[1][s]); ad[s] = MFMA16(d2[k], rr[k][s], ad[s]);
#pragma unroll
        for (int t = 0; t < 2; ++t) { av[t][s] = MFMA16(vf[k][t], rr[k][s], av[t][s]); am[t][s] = MFMA16(mf[k][t], rr[k][s], am[t][s]); }
      }
    }
    SB();
  }
#pragma unroll
  for (int t = 0; t < 2; ++t)
#pragma unroll
    for (int s = 0; s < 2; ++s) am[t][s] += ad[s];
  prod_store(av, 0, ks, J, lane, wsrd);
  prod_store(am, 1, ks, J, lane, wsrd);
}

constexpr unsigned CODE_BYTES = 84480;
DI void code_touch(unsigned i, unsigned ldsdst) {
  unsigned long long a; int o_;
  asm volatile("s_getpc_b64 %0\n s_mov_b32 %1, _Z11attn_kernelPcPKiS1_PKtS3_PtS3_S3_S3_S3_S3_@rel32@lo+4" : "=&s"(a), "=&s"(o_));
  a += (long long)o_;
  asm volatile("s_mov_b32 m0, %2\n s_nop 0\n global_load_lds_dword %0, %1" :: "v"(min(i * 128u, CODE_BYTES - 4u)), "s"(a), "s"(ldsdst) : "m0", "memory");
}

constexpr int LONGS = 1056;
typedef __attribute__((address_space(3))) unsigned* lds_u32_p;
typedef __attribute__((address_space(3))) u16* lds_u16_p;
DI void local_store(srd_t srd, int node, int lane, f32x4 acc, float di) {
  const int n = lane & 15, g = lane >> 4;
  const u32x2 o = {pk2(acc[0] * di, acc[1] * di), pk2(acc[2] * di, acc[3] * di)};
  __builtin_amdgcn_raw_buffer_store_b64(o, srd, WS_G + node * 512 + (g >> 1) * 256 + n * 16 + (g & 1) * 8, 0, 16);
}
DI void list_branch(char* lds, int j, int wid, const int* __restrict__ src, const int* __restrict__ dst, const u16* __restrict__ deg_inv,
                    const u16* wv, const u16* wm, const u16* wo, char* ws) {
  const unsigned rec = (unsigned)(size_t)lds + wid * 33280, ext = (unsigned)(size_t)lds + 0x20800 + wid * 6912;
  int lane; asm volatile("v_mbcnt_lo_u32_b32 %0, -1, 0\n\tv_mbcnt_hi_u32_b32 %0, -1, %0" : "=v"(lane));
  const int tid = wid * 64 + lane, bin = j * 4 + wid, lo = bin * 640;
  code_touch(CODE_BYTES / 128 - tid, (unsigned)(size_t)lds + 0x27f00u);
  const srd_t msrd = __builtin_amdgcn_make_buffer_rsrc(ws + WS_H, 0, 10240 * 512, 0x00020000);
  const srd_t wsrd = __builtin_amdgcn_make_buffer_rsrc(ws, 0, WS_END, 0x00020000);
  const unsigned tag = __builtin_amdgcn_raw_buffer_load_b32(wsrd, WS_EP, 0, 0) + 2u;
  unsigned lr[16];
#pragma unroll
  for (int r = 0; r < 16; ++r) lr[r] = reinterpret_cast<const unsigned*>((wid >= 2 ? wm : wv) + (240 + r) * DD)[tid & 127];
  unsigned dv[10];
#pragma unroll
  for (int i = 0; i < 10; ++i) dv[i] = (unsigned)deg_inv[min(lo + 64 * i + lane, NN - 1)];
#pragma unroll
  for (int i = 0; i < 10; ++i) ((lds_u32_p)ext)[64 * i + lane] = dv[i] << 16 | (lo + 64 * i + lane < NN ? lo + 64 * i + lane : 0xffff);
  corr(reinterpret_cast<unsigned*>(lds + 0x26c00) + 512 + (wid >> 1) * 256 + (tid & 127), lr);
  const u32x4 none = {~0u, ~0u, ~0u, ~0u};
  auto clear = [&]() {
    unsigned a = rec + lane * 16, r = rec + lane * 52;
    asm volatile("" : "+v"(a), "+v"(r));
#pragma unroll
    for (int i = 0; i < 33; ++i) *(__attribute__((address_space(3))) u32x4*)(a + (i < 32 ? i * 1024 : 32256)) = none;
#pragma unroll
    for (int i = 0; i < 10; ++i) ((lds_u32_p)(r + 64 * 52 * i))[12] = 46;
  };
  clear();
  wait_flag(wsrd, WS_BF + min(tid, NATT - 1) * 128, tag);
  __syncthreads();
  auto take = [&](const u32x4& e0, const u32x4& e1, unsigned (&a)[8], unsigned (&p)[8]) {
#pragma unroll
    for (int r = 0; r < 8; ++r) asm("v_mad_u32_u16 %0, %1, 52, %2 op_sel:[1,0,0,0]" : "=v"(a[r]) : "v"(r < 4 ? e0[r & 3] : e1[r & 3]), "s"(rec));
    SB();
#pragma unroll
    for (int r = 0; r < 8; ++r) p[r] = __atomic_fetch_add((lds_u32_p)a[r] + 12, -2u, __ATOMIC_RELAXED);
    SB();
  };
  auto fill = [&](const u32x4& e0, const u32x4& e1, unsigned (&a)[8], const unsigned (&p)[8]) {
#pragma unroll
    for (int r = 7; r >= 0; --r) a[r] = __builtin_elementwise_add_sat(a[r], p[r]);
    SB();
#pragma unroll
    for (int r = 7; r >= 0; --r) *(lds_u16_p)a[r] = r < 4 ? e0[r & 3] : e1[r & 3];
    SB();
  };
  auto add8 = [&](u32x4 e0, u32x4 e1) { unsigned a[8], p[8]; take(e0, e1, a, p); fill(e0, e1, a, p); };
  const unsigned bo = WS_BL + bin * 5 * 260 * 64 + lane * 16;
  auto ld = [&](int g, int k) { return __builtin_amdgcn_raw_buffer_load_b128(wsrd, bo + (g * 260 + k * 16) * 64, 0, 16); };
  auto fix = [&](u32x4 e, unsigned last) { return e | (unsigned)(__builtin_amdgcn_mov_dpp(last, 0xff, 15, 15, 0) >> 31); };
  bool ovf = false;
  unsigned big = 0; int off[10], tot = 0;
#pragma unroll 1
  for (int pass = 0; pass < 2; ++pass) {
    if (!ovf) {
      u32x4 c[64];
#pragma unroll
      for (int k = 0; k < 16; ++k) c[k] = ld(0, k);
      unsigned m = ~0u, a[2][8], p[2][8];
#pragma unroll
      for (int k = 0; k < 66; k += 2) {
        SB();
        if (k < 64) {
          if (k >= 48) { c[k] = fix(c[k], c[k - 16][3]); c[k + 1] = fix(c[k + 1], c[k - 15][3]); }
          if ((k & 15) == 14 && lane >= 48) c[k + 1] = none;
          if (k >= 48) m &= c[k][3] & c[k + 1][3];
          take(c[k], c[k + 1], a[k >> 1 & 1], p[k >> 1 & 1]);
        }
        if (k < 16) {
#pragma unroll
          for (int i = 16 + 2 * k; i < 20 + 2 * k; ++i) c[i] = ld(i >> 4, i & 15);
        }
        if (k >= 24 && k < 40) {
#pragma unroll
          for (int i = k - 24; i < k - 22; ++i)
            c[48 + i] = __builtin_amdgcn_raw_buffer_load_b128(wsrd, bo + (780 + i * 16) * 64 + (__builtin_amdgcn_mov_dpp(c[32 + i][3], 0xff, 15, 15, 0) & 0x40000000), 0, 16);
        }
        SB();
        if (k) fill(c[k - 2], c[k - 1], a[~k >> 1 & 1], p[~k >> 1 & 1]);
      }
      SB();
      if (__ballot((lane & 3) == 3 && m != ~0u)) {
        unsigned more = 0;
#pragma unroll
        for (int k = 0; k < 16; ++k) more |= __ballot((lane & 3) == 3 && c[48 + k][3] != ~0u) ? 1u << k : 0u;
#pragma unroll 1
        for (; more; more &= more - 1) {
          const int k = __builtin_ctz(more);
          u32x4 e = ld(4, k);
          if (k == 15 && lane >= 48) e = none;
          ovf |= __ballot((lane & 3) == 3 && e[3] != ~0u) != 0;
          add8(e, none);
        }
      }
      if (ovf) clear();
    }
    if (ovf) {
      for (int q = lane; q < EE / 4; q += 64) {
        const u32x4 d = reinterpret_cast<const u32x4*>(dst)[q], s = reinterpret_cast<const u32x4*>(src)[q];
        u32x4 e;
#pragma unroll
        for (int r = 0; r < 4; ++r) e[r] = d[r] - lo < 640u ? (d[r] - lo) << 16 | s[r] : ~0u;
        add8(e, none);
      }
    }
    if (pass) break;
    asm volatile("" : "+v"(lane));
    const int qd = lane & 3, nl = lane >> 2;
    int inc = 0;
#pragma unroll
    for (int i = 0; i < 10; ++i) off[i] = 46 - (int)((lds_u32_p)(rec + (64 * i + lane) * 52))[12];
#pragma unroll
    for (int i = 0; i < 10; ++i) {
      const int cn = off[i];
      off[i] = cn > 48 ? cn : 0; inc += off[i];
      if (cn > 48) { big |= 1u << i; ((lds_u16_p)ext)[2 * (64 * i + lane)] = 0xffff; }
    }
    __builtin_amdgcn_wave_barrier();
    auto put = [&](int odd) {
#pragma unroll 2
      for (int bl = 0; bl < 4; ++bl) {
        u32x4 o[10];
#pragma unroll
        for (int T = 0; T < 10; ++T) if (((2 * T + 1) / 5 & 1) == odd) {
          const lds_u32_p r = (lds_u32_p)(rec + (bl * 160 + T * 16 + nl) * 52);
          o[T] = (u32x4){r[3 * qd], r[1 + 3 * qd], r[2 + 3 * qd], ((lds_u32_p)ext)[bl * 160 + T * 16 + nl]};
        }
#pragma unroll
        for (int T = 0; T < 10; ++T) if (((2 * T + 1) / 5 & 1) == odd) {
          const int c = (2 * T + 1) / 5, ro = 16 * (T - (5 * c >> 1)) + nl, w = c & 1 ? ro * 43 >> 9 : ro >> 3;
          __builtin_amdgcn_raw_buffer_store_b128(o[T], wsrd, WS_GL + c * 4096 + w * (c & 1 ? 256 : 512) + ro * 64 + qd * 16, (bin * 4 + bl) * 16384, 16);
        }
      }
    };
    put(1);
    asm volatile("" ::: "memory");
    put(0);
    asm volatile("s_waitcnt vmcnt(16)" ::: "memory");
    if (lane == 0) st_flag(wsrd, WS_LF + bin * 128 + 64, tag);
#pragma unroll
    for (int o = 1; o < 64; o <<= 1) { const int y = __shfl_up(inc, o); if (lane >= o) inc += y; }
    tot = __builtin_amdgcn_readlane(inc, 63);
    drain();
    if (lane == 0) st_flag(wsrd, WS_LF + bin * 128, tag);
    __syncthreads();
    int j2 = j * 4, w2 = wid, l2 = lane;
    asm volatile("" : "+s"(j2), "+s"(w2), "+v"(l2));
    product_load(lds, w2, l2, j2, wv, wm, wo, true, true);
#pragma unroll 1
    for (int job = 0; job < 4; ++job) {
      if (job) asm volatile("s_waitcnt vmcnt(4)" ::: "memory"); else drain();
      __syncthreads();
      asm volatile("" : "+s"(w2));
      if (job < 3) product_load(lds + (~job & 1) * 32768, w2, l2, j2 + job + 1, wv, wm, wo, false, true);
      product_w(lds, w2, l2, j2 + job, wsrd);
    }
    if (w2 == 0) *(lds_u32_p)(ext - 8) = 0;
    __syncthreads();
    if (tot == 0 || tot > 2 * LONGS) break;
#pragma unroll
    for (int i = 0; i < 10; ++i) {
      const unsigned r = rec + (64 * i + lane) * 52;
      ((lds_u32_p)r)[12] = off[i] ? ext + 2558 + inc - r : -2u;
      inc -= off[i]; off[i] = off[i] << 15 | inc;
    }
  }
  __syncthreads();
  const unsigned ex0 = (unsigned)(size_t)lds + 0x20800;
  const lds_u32_p lng = (lds_u32_p)(size_t)(unsigned)(size_t)lds;
#pragma unroll
  for (int i = 0; i < 10; ++i)
    if (big >> i & 1) {
      const unsigned q = __atomic_fetch_add((lds_u32_p)(ex0 - 8), 1u, __ATOMIC_RELAXED);
      lng[2 * q] = tot > 2 * LONGS ? ~0u : off[i] + wid * 6912 + 2560; lng[2 * q + 1] = (((lds_u32_p)ext)[64 * i + lane] & 0xffff0000u) | (lo + 64 * i + lane);
    }
  __syncthreads();
  const int nl_ = *(lds_u32_p)(ex0 - 8);
  bf16x8 b1;
#pragma unroll
  for (int e = 0; e < 8; ++e) b1[e] = (lane & 15) == e + ((lane >> 1) & 8) ? 0x3f80 : 0;
  const unsigned lo16 = (lane & 31) * 16;
  {
    u32x4 d[3][16]; unsigned nn[3]; int mode[3], q = wid, hf = 0;
    auto issue = [&](u32x4 (&v)[16], unsigned& n, int& md) {
      const unsigned w = __builtin_amdgcn_readfirstlane(lng[2 * q]);
      n = lng[2 * q + 1];
      const int dg = q < nl_ && w >> 16 <= 64 ? w >> 16 : 0, g = dg - hf - (lane >> 5);
      const lds_u16_p f = (lds_u16_p)(ex0 + (w & 0xffffu)) + hf + (lane >> 5);
      md = q >= nl_ ? -1 : dg == 0 ? 0 : dg - hf > 32 ? 1 : 2;
      hf = md == 1 ? 32 : 0; q += md == 1 ? 0 : 4;
      unsigned id[16];
#pragma unroll
      for (int t = 0; t < 16; ++t) id[t] = f[2 * t];
#pragma unroll
      for (int t = 0; t < 16; ++t) { asm volatile("" : "+v"(id[t])); v[t] = __builtin_amdgcn_raw_buffer_load_b128(msrd, (2 * t < g ? id[t] : 0xffffu) * 512u + lo16, 0, 16); }
    };
    issue(d[0], nn[0], mode[0]); issue(d[1], nn[1], mode[1]);
    f32x4 keep = {};
#pragma unroll 1
    for (bool go = true; go;) {
#pragma unroll
      for (int s = 0; s < 3; ++s) {
        if (mode[s] < 0) { go = false; break; }
        issue(d[(s + 2) % 3], nn[(s + 2) % 3], mode[(s + 2) % 3]); SB();
        f32x4 acc = {};
#pragma unroll
        for (int t = 0; t < 16; ++t) acc = MFMA16(b1, as_bf(d[s][t]), acc);
        local_store(wsrd, mode[s] == 2 ? nn[s] & 0xffffu : 0x200000u, lane, acc + keep, bfhi(nn[s]));
        keep = mode[s] == 1 ? acc : (f32x4){0.f, 0.f, 0.f, 0.f}; SB();
      }
    }
  }
#pragma unroll 1
  for (int q0 = 0; q0 < nl_; q0 += 256)
#pragma unroll 1
  for (unsigned long long m = __ballot(q0 + 4 * lane + 3 - wid < nl_ && lng[2 * (q0 + 4 * lane + 3 - wid)] >> 16 > 64); m; m &= m - 1) {
    const int q = q0 + 4 * __builtin_ctzll(m) + 3 - wid;
    const unsigned w = __builtin_amdgcn_readfirstlane(lng[2 * q]), n = __builtin_amdgcn_readfirstlane(lng[2 * q + 1]); const int dg = w >> 16;
    f32x4 acc = {};
    if (w == ~0u) {
      for (int e0 = 0; e0 < EE; e0 += 64) {
        unsigned long long h = __ballot(dst[e0 + lane] == (n & 0xffffu));
        while (h) {
          const int e = e0 + __builtin_ctzll(h); h &= h - 1;
          const unsigned vo = lane < 32 ? (unsigned)src[e] * 512 + lo16 : 0x7fffff00u;
          acc = MFMA16(b1, as_bf(__builtin_amdgcn_raw_buffer_load_b128(msrd, vo, 0, 16)), acc);
        }
      }
    } else {
      const lds_u16_p f = (lds_u16_p)(ex0 + (w & 0xffffu)), u = (lds_u16_p)(ext + 2624 + 2 * LONGS);
      for (int x = lane; x < dg; x += 64) {
        const int key = f[x]; int p = 0;
#pragma unroll 8
        for (int t = 0; t < dg; ++t) { const int v = f[t]; p += v < key || (v == key && t < x); }
        u[p] = key;
      }
      __builtin_amdgcn_wave_barrier();
      for (int x = lane >> 5; x < dg + (lane >> 5); x += 32) {
        u32x4 d[16]; unsigned id[16]; f32x4 a2 = {};
#pragma unroll
        for (int t = 0; t < 16; ++t) id[t] = u[min(x + 2 * t, dg)];
#pragma unroll
        for (int t = 0; t < 16; ++t) { asm volatile("" : "+v"(id[t])); d[t] = __builtin_amdgcn_raw_buffer_load_b128(msrd, (x + 2 * t < dg ? id[t] : 0xffffu) * 512u + lo16, 0, 16); }
#pragma unroll
        for (int t = 0; t < 16; ++t) a2 = MFMA16(b1, as_bf(d[t]), a2);
        acc += a2;
      }
      __builtin_amdgcn_wave_barrier();
    }
    local_store(wsrd, n & 0xffffu, lane, acc, bfhi(n));
  }
  drain();
  __syncthreads();
  if (wid == 0 && lane == 0) st_flag(wsrd, WS_WF + j * 4, tag);
}

DI void exact_rows(char* lds, int b, int wid, char* ws, const u16* x, u16* out) {
  int lane; asm volatile("v_mbcnt_lo_u32_b32 %0, -1, 0\n\tv_mbcnt_hi_u32_b32 %0, -1, %0" : "=v"(lane));
  const srd_t w = __builtin_amdgcn_make_buffer_rsrc(ws, 0, WS_END, 0x00020000);
  const srd_t xs = __builtin_amdgcn_make_buffer_rsrc(const_cast<u16*>(x), 0, NN * 512, 0x00020000);
  const srd_t os = __builtin_amdgcn_make_buffer_rsrc(out, 0, NN * 512, 0x00020000);
  const int n = lane & 15, g = lane >> 4, c = b & 3;
  f32x4* red = reinterpret_cast<f32x4*>(lds);
  float* mx = reinterpret_cast<float*>(lds + 65536);
  auto ld = [&](unsigned off) { return as_bf(__builtin_amdgcn_raw_buffer_load_b128(w, off, 0, 16)); };
#pragma unroll 1
  for (int i = 0; i < 2 + (c & 1); ++i) {
    const int tile = (b >> 2) * 10 + (5 * c >> 1) + i;
    bf16x8 q[8];
#pragma unroll
    for (int ks = 0; ks < 8; ++ks) q[ks] = ld(WS_QP + tile * 8192 + ks * 1024 + lane * 16);
    auto score = [&](int kt) {
      f32x4 s = {};
#pragma unroll
      for (int ks = 0; ks < 8; ++ks) s = MFMA16(ld(WS_H + (kt * 16 + n) * 512 + ks * 64 + g * 16), q[ks], s);
      return s;
    };
    float m = -__builtin_inff();
#pragma unroll 1
    for (int kt = wid; kt < NN / 16; kt += 4) { const f32x4 s = score(kt); m = fmaxf(fmaxf(m, fmaxf(s[0], s[1])), fmaxf(s[2], s[3])); }
    __syncthreads();
    mx[wid * 64 + lane] = m;
    __syncthreads();
#pragma unroll
    for (int r = 0; r < 16; ++r) m = fmaxf(m, mx[r * 16 + n]);
    f32x4 acc[16] = {};
#pragma unroll 1
    for (int kt = wid; kt < NN / 16; kt += 4) {
      const f32x4 s = score(kt);
      const u32x2 p = {pk2(__builtin_amdgcn_exp2f(s[0] - m), __builtin_amdgcn_exp2f(s[1] - m)), pk2(__builtin_amdgcn_exp2f(s[2] - m), __builtin_amdgcn_exp2f(s[3] - m))};
#pragma unroll
      for (int dt = 0; dt < 16; ++dt) {
        bf16x4 v;
#pragma unroll
        for (int e = 0; e < 4; ++e) v[e] = __builtin_amdgcn_raw_buffer_load_b16(w, WS_H + (kt * 16 + 4 * g + e) * 512 + n * 2, dt * 32, 16);
        acc[dt] = __builtin_amdgcn_mfma_f32_16x16x16bf16_1k(v, __builtin_bit_cast(bf16x4, p), acc[dt], 0, 0, 0);
      }
    }
#pragma unroll
    for (int dt = 0; dt < 16; ++dt) red[(wid * 16 + dt) * 64 + lane] = acc[dt];
    __syncthreads();
    float l = 0.f;
#pragma unroll
    for (int r = 0; r < 4; ++r) l += red[(r * 16 + 15) * 64 + 48 + n][3];
    l = 1.f / l;
    f32x4 y[4] = {};
#pragma unroll 1
    for (int ks = 0; ks < 8; ++ks) {
      f32x4 o[2];
#pragma unroll
      for (int h = 0; h < 2; ++h) o[h] = (red[(2 * ks + h) * 64 + lane] + red[(16 + 2 * ks + h) * 64 + lane] + red[(32 + 2 * ks + h) * 64 + lane] + red[(48 + 2 * ks + h) * 64 + lane]) * l;
      const bf16x8 of = as_bf((u32x4){pk2(o[0][0], o[0][1]), pk2(o[0][2], o[0][3]), pk2(o[1][0], o[1][1]), pk2(o[1][2], o[1][3])});
      const bf16x8 gf = ld(WS_G + (tile * 16 + n) * 512 + ks * 64 + g * 16);
#pragma unroll
      for (int et = 0; et < 4; ++et) {
        y[et] = MFMA16(ld(WS_WV + wid * 32768 + ks * 4096 + et * 1024 + lane * 16), of, y[et]);
        y[et] = MFMA16(ld(WS_WM + wid * 32768 + (et < 2 ? 16384 : 0) + ks * 2048 + (et & 1) * 1024 + lane * 16), gf, y[et]);
      }
    }
#pragma unroll
    for (int jj = 0; jj < 2; ++jj) {
      const unsigned at = (tile * 16 + n) * 512 + wid * 128 + jj * 64 + g * 16;
      const u32x4 xv = __builtin_amdgcn_raw_buffer_load_b128(xs, at, 0, 0);
      u32x4 o;
#pragma unroll
      for (int r = 0; r < 4; ++r) o[r] = pk2(y[2 * jj + (r >> 1)][2 * (r & 1)] + bf2f(xv[r] & 0xffffu), y[2 * jj + (r >> 1)][2 * (r & 1) + 1] + bfhi(xv[r]));
      __builtin_amdgcn_raw_buffer_store_b128(o, os, at, 0, 0);
    }
  }
}

__global__ __launch_bounds__(256, 1) void attn_kernel(char* ws, const int* __restrict__ src, const int* __restrict__ dst,
                                                      const u16* __restrict__ deg_inv, const u16* __restrict__ x, u16* out,
                                                      const u16* __restrict__ wq, const u16* __restrict__ wk, const u16* __restrict__ wv,
                                                      const u16* __restrict__ wm, const u16* __restrict__ wo) {
  extern __shared__ __attribute__((aligned(16))) char lds[];
  asm volatile("s_load_dword s99, %0, 0" :: "s"(__builtin_amdgcn_kernarg_segment_ptr()) : "s99");
  int b = (blockIdx.x & 6) << 5 | blockIdx.x >> 3 << 1 | (blockIdx.x & 1);
  code_touch((blockIdx.x >> 3 & 1) * 256 + threadIdx.x, (unsigned)(size_t)lds + 0x27f00u);
  int wid = __builtin_amdgcn_readfirstlane(threadIdx.x >> 6);
  static_assert(WS_END == 37475328 && WS_EP == 5695744, "workspace layout of the generated code");
  auto ka = __builtin_amdgcn_kernarg_segment_ptr();
  asm volatile(
    "s_cmpk_ge_u32 %1, 252\n"
    "s_cbranch_scc1 .Lat_end\n"
    "s_load_dwordx2 s[20:21], %0, 0x30\n"
    "s_load_dwordx2 s[24:25], %0, 0x38\n"
    "s_load_dwordx2 s[12:13], %0, 0x20\n"
    "s_load_dwordx2 s[8:9], %0, 0x0\n"
    "s_load_dwordx2 s[16:17], %0, 0x28\n"
    "s_load_dwordx4 s[28:31], %0, 0x8\n"
    "v_mbcnt_lo_u32_b32 v24, -1, 0\n"
    "v_mbcnt_hi_u32_b32 v24, -1, v24\n"
    "s_and_b32 s50, %2, 1\n"
    "s_lshr_b32 s51, %2, 1\n"
    "v_lshrrev_b32_e32 v25, 3, v24\n"
    "v_and_b32_e32 v26, 6, v25\n"
    "v_and_b32_e32 v27, 7, v24\n"
    "v_xor_b32_e32 v26, v26, v27\n"
    "v_lshlrev_b32_e32 v26, 4, v26\n"
    "v_and_b32_e32 v27, 4, v25\n"
    "v_add_u32_e32 v27, v27, v25\n"
    "v_lshl_add_u32 v254, v27, 9, v26\n"
    "v_add_u32_e32 v255, 0x1c00, v254\n"
    "s_mov_b32 s22, 0x20000\n"
    "s_mov_b32 s23, 0x20000\n"
    "s_mov_b32 s26, 0x20000\n"
    "s_mov_b32 s27, 0x20000\n"
    "s_lshr_b32 s36, %1, 7\n"
    "s_mul_i32 s36, s36, 3\n"
    "s_add_u32 s36, s36, %1\n"
    "s_and_b32 s36, s36, 127\n"
    "s_lshl_b32 s45, %2, 7\n"
    "s_lshl_b32 s46, %2, 12\n"
    "s_add_u32 s46, s46, %3\n"
    "s_and_b32 s47, s36, 7\n"
    "s_lshl_b32 s47, s47, 14\n"
    "s_add_u32 m0, s46, 32768\n"
    "s_add_u32 s47, s47, s45\n"
    "s_and_b32 s32, %1, 3\n"
    "s_lshr_b32 s33, %1, 2\n"
    "s_mul_i32 s38, s32, 5\n"
    "s_lshr_b32 s38, s38, 1\n"
    "s_mul_i32 s35, s33, 10\n"
    "s_add_u32 s35, s35, s38\n"
    "s_lshl_b32 s35, s35, 13\n"
    "s_mov_b32 s14, 5120000\n"
    "s_mov_b32 s15, 0x20000\n"
    "s_waitcnt lgkmcnt(0)\n"
    "buffer_load_dwordx4 v254, s[20:23], s47 offen offset:0 lds\n"
    "s_add_u32 s48, s47, 1024\n"
    "buffer_load_dwordx4 v255, s[20:23], s47 offen offset:3072 lds\n"
    "s_add_u32 s47, s47, 0x1800\n"
    "buffer_load_dwordx4 v254, s[20:23], s48 offen offset:1024 lds\n"
    "buffer_load_dwordx4 v254, s[20:23], s47 offen offset:2048 lds\n"
    "s_lshr_b32 s47, s36, 4\n"
    "s_lshl_b32 s47, s47, 14\n"
    "s_and_b32 s48, s36, 8\n"
    "s_lshl_b32 s48, s48, 8\n"
    "s_add_u32 s47, s47, s48\n"
    "s_add_u32 m0, s46, 49152\n"
    "s_add_u32 s47, s47, s45\n"
    "buffer_load_dwordx4 v254, s[24:27], s47 offen offset:0 lds\n"
    "buffer_load_dwordx4 v255, s[24:27], s47 offen offset:1024 lds\n"
    "s_add_u32 s47, s45, 120832\n"
    "buffer_load_dwordx4 v254, s[24:27], s47 offen offset:2048 lds\n"
    "s_add_u32 s47, s47, 1024\n"
    "buffer_load_dwordx4 v254, s[24:27], s47 offen offset:3072 lds\n"
    "s_mul_i32 s46, %1, 159\n"
    "s_lshl_b32 s45, s50, 6\n"
    "v_add_u32_e32 v111, s45, v24\n"
    "v_add_u32_e32 v108, s46, v111\n"
    "v_min_u32_e32 v108, 39999, v108\n"
    "v_lshlrev_b32_e32 v108, 4, v108\n"
    "s_lshl1_add_u32 s47, s46, 256\n"
    "v_add_u32_e32 v109, s47, v111\n"
    "v_min_u32_e32 v109, 79999, v109\n"
    "v_lshlrev_b32_e32 v109, 3, v109\n"
    "s_cmp_lg_u32 s51, 0\n"
    "s_cselect_b64 exec, -1, 1\n"
    "global_load_dwordx4 v[96:99], v108, s[30:31]\n"
    "global_load_dwordx2 v[92:93], v109, s[30:31]\n"
    "global_load_dwordx4 v[100:103], v108, s[28:29]\n"
    "global_load_dwordx2 v[94:95], v109, s[28:29]\n"
    "s_mov_b64 exec, -1\n"
    "s_lshl_b32 s45, %2, 6\n"
    "v_add_u32_e32 v108, s45, v24\n"
    "s_sub_u32 s59, 40000, s46\n"
    "s_lshl1_add_u32 s61, s59, -256\n"
    "s_min_i32 s61, s61, 62\n"
    "s_lshl_b32 s45, %2, 2\n"
    "s_add_u32 s54, s35, 0x2000\n"
    "s_add_u32 s55, s35, 0x4000\n"
    "v_lshlrev_b32_e32 v253, 4, v24\n"
    "v_lshrrev_b32_e32 v28, 4, v24\n"
    "v_add_u32_e32 v28, s45, v28\n"
    "v_and_b32_e32 v104, 0xf0, v253\n"
    "v_lshl_add_u32 v104, v28, 9, v104\n"
    "buffer_load_dwordx4 v[0:3], v104, s[12:15], s35 offen offset:0\n"
    "buffer_load_dwordx4 v[4:7], v104, s[12:15], s35 offen offset:256\n"
    "s_bitcmp1_b32 %1, 0\n"
    "s_cselect_b32 s60, s55, 2.0\n"
    "buffer_load_dwordx4 v[8:11], v104, s[12:15], s54 offen offset:0\n"
    "buffer_load_dwordx4 v[12:15], v104, s[12:15], s54 offen offset:256\n"
    "buffer_load_dwordx4 v[16:19], v104, s[12:15], s55 offen offset:0\n"
    "buffer_load_dwordx4 v[20:23], v104, s[12:15], s55 offen offset:256\n"
    "s_mov_b32 s10, 37475328\n"
    "s_mov_b32 s11, 0x20000\n"
    "s_mov_b32 s37, 5695744\n"
    "s_buffer_load_dword s39, s[8:11], s37\n"
    "s_bfe_u32 s37, %1, 0x20001\n"
    "v_and_b32_e32 v27, 1, v25\n"
    "v_lshl_add_u32 v248, v27, 9, v26\n"
    "v_lshrrev_b32_e32 v27, 1, v25\n"
    "v_lshl_add_u32 v248, v27, 12, v248\n"
    "s_mul_i32 s40, s32, 39\n"
    "s_and_b32 s34, s32, 1\n"
    "s_add_u32 s40, s40, s34\n"
    "s_lshl1_add_u32 s40, s40, s51\n"
    "s_lshl_b32 s40, s40, 14\n"
    "s_lshl_b32 s47, s50, 8\n"
    "s_add_u32 s40, s40, s47\n"
    "s_add_u32 s41, s40, 128\n"
    "s_lshl_b32 s34, s34, 15\n"
    "s_xor_b32 s43, s50, s51\n"
    "s_lshl3_add_u32 s43, s50, s43\n"
    "s_lshl_b32 s43, s43, 13\n"
    "s_add_u32 s47, %3, 32768\n"
    "s_add_u32 s43, s43, s47\n"
    "v_bfe_u32 v25, v24, 2, 3\n"
    "v_lshlrev_b32_e32 v244, 7, v25\n"
    "v_lshrrev_b32_e32 v26, 5, v24\n"
    "v_lshl_add_u32 v244, v26, 10, v244\n"
    "v_and_b32_e32 v25, 6, v25\n"
    "v_bfe_u32 v26, v24, 1, 1\n"
    "v_xor_b32_e32 v25, v25, v26\n"
    "v_lshl_add_u32 v244, v25, 4, v244\n"
    "v_and_b32_e32 v26, 1, v24\n"
    "v_lshl_add_u32 v244, v26, 3, v244\n"
    "s_lshl_b32 s48, s51, 16\n"
    "s_add_u32 s48, s48, s47\n"
    "v_add_u32_e32 v244, s48, v244\n"
    "v_xor_b32_e32 v245, 32, v244\n"
    "v_xor_b32_e32 v246, 64, v244\n"
    "v_xor_b32_e32 v247, 0x60, v244\n"
    "v_and_b32_e32 v25, 7, v24\n"
    "v_lshlrev_b32_e32 v27, 7, v25\n"
    "v_bfe_u32 v26, v24, 3, 1\n"
    "v_lshl_add_u32 v27, v26, 10, v27\n"
    "v_and_b32_e32 v25, 6, v24\n"
    "v_lshrrev_b32_e32 v26, 4, v24\n"
    "v_xor_b32_e32 v25, v25, v26\n"
    "v_lshl_add_u32 v27, v25, 4, v27\n"
    "s_lshl_b32 s48, s51, 13\n"
    "s_add_u32 s48, s48, s47\n"
    "v_add_u32_e32 v240, s48, v27\n"
    "v_xor_b32_e32 v241, 64, v240\n"
    "v_and_b32_e32 v29, 15, v24\n"
    "s_cmp_ge_u32 s36, 112\n"
    "s_cselect_b32 s47, 8, 16\n"
    "v_cmp_le_u32_e32 vcc, s47, v29\n"
    "v_mov_b32_e32 v64, 0xbd80bd80\n"
    "v_mov_b32_e32 v65, 0xbdabbdab\n"
    "v_mov_b32_e32 v66, 0x392b392b\n"
    "v_cndmask_b32_e32 v64, v64, v65, vcc\n"
    "v_cndmask_b32_e32 v66, 0, v66, vcc\n"
    "s_cselect_b32 s47, 56, 64\n"
    "v_cmp_le_u32_e32 vcc, s47, v24\n"
    "v_mov_b32_e32 v65, 0x3e80bdab\n"
    "v_mov_b32_e32 v67, 0x392b\n"
    "v_cndmask_b32_e32 v65, v64, v65, vcc\n"
    "v_cndmask_b32_e32 v67, v66, v67, vcc\n"
    "s_cmp_ge_u32 s36, 120\n"
    "s_cselect_b32 s47, 15, 16\n"
    "v_cmp_ne_u32_e32 vcc, s47, v29\n"
    "v_mov_b32_e32 v68, 0x3db8aa3b\n"
    "s_lshr_b32 s47, s36, 5\n"
    "v_cndmask_b32_e32 v68, 0, v68, vcc\n"
    "s_lshl_b32 s47, s47, 15\n"
    "s_and_b32 s48, s36, 7\n"
    "s_lshl_b32 s48, s48, 11\n"
    "s_add_u32 s47, s47, s48\n"
    "s_and_b32 s48, s36, 8\n"
    "s_lshl_b32 s48, s48, 7\n"
    "s_add_u32 s47, s47, s48\n"
    "s_and_b32 s48, s36, 16\n"
    "s_lshl_b32 s48, s48, 10\n"
    "s_add_u32 s47, s47, s48\n"
    "s_add_u32 s52, s47, 5242880\n"
    "s_lshl_b32 s48, s50, 9\n"
    "s_add_u32 s52, s52, s48\n"
    "s_lshl_b32 s48, s50, 11\n"
    "v_add_u32_e32 v72, s48, v240\n"
    "v_add_u32_e32 v73, s48, v241\n"
    "v_and_b32_e32 v74, 0x1f0, v253\n"
    "v_lshrrev_b32_e32 v29, 5, v24\n"
    "v_lshl_add_u32 v74, v29, 3, v74\n"
    "s_lshl_b32 s53, s36, 1\n"
    "s_add_u32 s53, s53, s50\n"
    "s_lshl_b32 s53, s53, 4\n"
    "s_add_u32 s53, s53, 5689344\n"
    "s_cmp_eq_u32 s51, 0\n"
    "s_cselect_b32 s52, s52, 2.0\n"
    "s_cselect_b32 s53, s53, 2.0\n"
    "v_lshrrev_b32_e32 v109, 2, v108\n"
    "v_mul_u32_u24_e32 v109, 0x4100, v109\n"
    "v_and_b32_e32 v110, 48, v253\n"
    "v_or_b32_e32 v109, v109, v110\n"
    "v_add_u32_e32 v110, 0x104000, v109\n"
    "v_cmp_gt_u32_e32 vcc, 64, v108\n"
    "s_add_u32 s44, %3, 24576\n"
    "v_lshl_add_u32 v108, v108, 4, s44\n"
    "v_cndmask_b32_e32 v110, 2.0, v110, vcc\n"
    "v_mov_b32_e32 v32, -1\n"
    "v_mov_b32_e32 v33, -1\n"
    "v_mov_b32_e32 v34, -1\n"
    "v_mov_b32_e32 v35, -1\n"
    "ds_write_b128 v108, v[32:35] offset:0\n"
    "ds_write_b128 v108, v[32:35] offset:1280\n"
    "v_and_b32_e32 v29, 6, v28\n"
    "v_xor_b32_e32 v29, v29, v24\n"
    "v_and_b32_e32 v29, 7, v29\n"
    "v_lshlrev_b32_e32 v105, 7, v28\n"
    "v_lshl_add_u32 v105, v29, 4, v105\n"
    "v_bfe_u32 v29, v24, 3, 1\n"
    "v_subrev_u32_e32 v29, s37, v29\n"
    "v_and_b32_e32 v29, 3, v29\n"
    "v_lshl_add_u32 v105, v29, 11, v105\n"
    "s_add_u32 s45, %3, 65536\n"
    "v_add_u32_e32 v105, s45, v105\n"
    "v_xor_b32_e32 v106, 0x1000, v105\n"
    "v_and_b32_e32 v29, 15, v24\n"
    "v_cmp_eq_u32_e32 vcc, 15, v29\n"
    "v_mov_b32_e32 v107, 0xffff\n"
    "s_mov_b32 s58, 0x3f803f80\n"
    "s_mov_b32 s57, 0x43c00000\n"
    "v_cndmask_b32_e32 v107, -1, v107, vcc\n"
    "s_mov_b32 s56, 0xc3c00000\n"
    "s_mov_b32 s62, 0xc000c000\n"
    "s_mov_b32 s63, 0xc000c000\n"
    "v_add_u32_e32 v250, s45, v27\n"
    "v_xor_b32_e32 v251, 64, v250\n"
    "s_lshl_b32 s47, %2, 10\n"
    "s_add_u32 s47, s47, 5689344\n"
    "v_lshl_add_u32 v252, v24, 4, s47\n"
    "s_waitcnt vmcnt(10)\n"
    "s_waitcnt lgkmcnt(0)\n"
    "s_barrier\n"
    "s_add_u32 s39, s39, 2\n"
    "s_cmp_lg_u32 s51, 0\n"
    "s_cbranch_scc1 .Lat_bin\n"
    "ds_read_b128 v[112:115], v72 offset:0\n"
    "ds_read_b128 v[208:211], v240 offset:18432\n"
    "ds_read_b128 v[176:179], v240 offset:16384\n"
    "ds_read_b128 v[116:119], v73 offset:0\n"
    "ds_read_b128 v[212:215], v241 offset:18432\n"
    "ds_read_b128 v[180:183], v241 offset:16384\n"
    "ds_read_b128 v[120:123], v72 offset:4096\n"
    "ds_read_b128 v[216:219], v240 offset:22528\n"
    "ds_read_b128 v[184:187], v240 offset:20480\n"
    "ds_read_b128 v[124:127], v73 offset:4096\n"
    "ds_read_b128 v[220:223], v241 offset:22528\n"
    "ds_read_b128 v[188:191], v241 offset:20480\n"
    "ds_read_b128 v[128:131], v72 offset:8192\n"
    "ds_read_b128 v[224:227], v240 offset:26624\n"
    "ds_read_b128 v[192:195], v240 offset:24576\n"
    "s_waitcnt lgkmcnt(12)\n"
    "v_mfma_f32_16x16x32_bf16 v[56:59], v[208:211], v[112:115], 0\n"
    "ds_read_b128 v[132:135], v73 offset:8192\n"
    "ds_read_b128 v[228:231], v241 offset:26624\n"
    "v_mfma_f32_16x16x32_bf16 v[48:51], v[112:115], v[176:179], 0\n"
    "ds_read_b128 v[196:199], v241 offset:24576\n"
    "s_waitcnt lgkmcnt(12)\n"
    "v_mfma_f32_16x16x32_bf16 v[56:59], v[212:215], v[116:119], v[56:59]\n"
    "ds_read_b128 v[136:139], v72 offset:12288\n"
    "ds_read_b128 v[232:235], v240 offset:30720\n"
    "v_mfma_f32_16x16x32_bf16 v[48:51], v[116:119], v[180:183], v[48:51]\n"
    "ds_read_b128 v[200:203], v240 offset:28672\n"
    "s_waitcnt lgkmcnt(12)\n"
    "v_mfma_f32_16x16x32_bf16 v[56:59], v[216:219], v[120:123], v[56:59]\n"
    "ds_read_b128 v[140:143], v73 offset:12288\n"
    "ds_read_b128 v[236:239], v241 offset:30720\n"
    "v_mfma_f32_16x16x32_bf16 v[48:51], v[120:123], v[184:187], v[48:51]\n"
    "ds_read_b128 v[204:207], v241 offset:28672\n"
    "s_waitcnt lgkmcnt(12)\n"
    "v_mfma_f32_16x16x32_bf16 v[56:59], v[220:223], v[124:127], v[56:59]\n"
    "v_mfma_f32_16x16x32_bf16 v[48:51], v[124:127], v[188:191], v[48:51]\n"
    "s_waitcnt lgkmcnt(9)\n"
    "v_mfma_f32_16x16x32_bf16 v[56:59], v[224:227], v[128:131], v[56:59]\n"
    "v_mfma_f32_16x16x32_bf16 v[48:51], v[128:131], v[192:195], v[48:51]\n"
    "s_waitcnt lgkmcnt(6)\n"
    "v_mfma_f32_16x16x32_bf16 v[56:59], v[228:231], v[132:135], v[56:59]\n"
    "v_mfma_f32_16x16x32_bf16 v[48:51], v[132:135], v[196:199], v[48:51]\n"
    "s_waitcnt lgkmcnt(3)\n"
    "v_mfma_f32_16x16x32_bf16 v[56:59], v[232:235], v[136:139], v[56:59]\n"
    "v_mfma_f32_16x16x32_bf16 v[48:51], v[136:139], v[200:203], v[48:51]\n"
    "s_waitcnt lgkmcnt(0)\n"
    "v_mfma_f32_16x16x32_bf16 v[56:59], v[236:239], v[140:143], v[56:59]\n"
    "v_mfma_f32_16x16x32_bf16 v[48:51], v[140:143], v[204:207], v[48:51]\n"
    "s_nop 3\n"
    "v_cvt_pk_bf16_f32 v56, v56, v57\n"
    "v_cvt_pk_bf16_f32 v57, v58, v59\n"
    "s_nop 1\n"
    "v_mfma_f32_16x16x16_bf16 v[48:51], v[56:57], v[64:65], v[48:51]\n"
    "v_mfma_f32_16x16x16_bf16 v[48:51], v[56:57], v[66:67], v[48:51]\n"
    "s_nop 7\n"
    "v_mul_f32_e32 v48, v48, v68\n"
    "v_mul_f32_e32 v49, v49, v68\n"
    "v_mul_f32_e32 v50, v50, v68\n"
    "v_mul_f32_e32 v51, v51, v68\n"
    "v_cvt_pk_bf16_f32 v48, v48, v49\n"
    "v_cvt_pk_bf16_f32 v49, v50, v51\n"
    "s_branch .Lat_noj2\n"
    ".Lat_bin:\n"
    "s_movk_i32 s45, -640\n"
    "s_movk_i32 s48, 80\n"
    "v_mov_b32_e32 v60, 1\n"
    "v_cmp_gt_u32_e32 vcc, s59, v111\n"
    "v_cmp_gt_i32_e64 s[46:47], s61, v111\n"
    "s_waitcnt vmcnt(8)\n"
    "v_mul_u32_u24_e32 v48, 0x6667, v96\n"
    "v_mul_u32_u24_e32 v49, 0x6667, v97\n"
    "v_mul_u32_u24_e32 v50, 0x6667, v98\n"
    "v_mul_u32_u24_e32 v51, 0x6667, v99\n"
    "v_mul_u32_u24_e32 v52, 0x6667, v92\n"
    "v_mul_u32_u24_e32 v53, 0x6667, v93\n"
    "v_lshrrev_b32_e32 v48, 24, v48\n"
    "v_lshrrev_b32_e32 v49, 24, v49\n"
    "v_lshrrev_b32_e32 v50, 24, v50\n"
    "v_lshrrev_b32_e32 v51, 24, v51\n"
    "v_lshrrev_b32_e32 v52, 24, v52\n"
    "v_lshrrev_b32_e32 v53, 24, v53\n"
    "v_min_u32_e32 v48, 15, v48\n"
    "v_min_u32_e32 v49, 15, v49\n"
    "v_min_u32_e32 v50, 15, v50\n"
    "v_min_u32_e32 v51, 15, v51\n"
    "v_min_u32_e32 v52, 15, v52\n"
    "v_min_u32_e32 v53, 15, v53\n"
    "v_lshl_add_u32 v54, v48, 2, s44\n"
    "v_lshl_add_u32 v55, v49, 2, s44\n"
    "v_lshl_add_u32 v56, v50, 2, s44\n"
    "v_lshl_add_u32 v57, v51, 2, s44\n"
    "v_lshl_add_u32 v58, v52, 2, s44\n"
    "v_lshl_add_u32 v59, v53, 2, s44\n"
    "s_mov_b64 exec, vcc\n"
    "ds_add_rtn_u32 v54, v54, v60 offset:5248\n"
    "ds_add_rtn_u32 v55, v55, v60 offset:5248\n"
    "ds_add_rtn_u32 v56, v56, v60 offset:5248\n"
    "ds_add_rtn_u32 v57, v57, v60 offset:5248\n"
    "s_mov_b64 exec, s[46:47]\n"
    "ds_add_rtn_u32 v58, v58, v60 offset:5248\n"
    "ds_add_rtn_u32 v59, v59, v60 offset:5248\n"
    "s_mov_b64 exec, -1\n"
    "s_waitcnt vmcnt(6)\n"
    "v_mad_i32_i24 v96, v48, s45, v96\n"
    "v_mad_i32_i24 v97, v49, s45, v97\n"
    "v_mad_i32_i24 v98, v50, s45, v98\n"
    "v_mad_i32_i24 v99, v51, s45, v99\n"
    "v_mad_i32_i24 v92, v52, s45, v92\n"
    "v_mad_i32_i24 v93, v53, s45, v93\n"
    "v_lshl_or_b32 v96, v96, 16, v100\n"
    "v_lshl_or_b32 v97, v97, 16, v101\n"
    "v_lshl_or_b32 v98, v98, 16, v102\n"
    "v_lshl_or_b32 v99, v99, 16, v103\n"
    "v_lshl_or_b32 v92, v92, 16, v94\n"
    "v_lshl_or_b32 v93, v93, 16, v95\n"
    "s_mov_b64 exec, vcc\n"
    "s_waitcnt lgkmcnt(0)\n"
    "v_min_i32_e32 v54, 78, v54\n"
    "v_min_i32_e32 v55, 78, v55\n"
    "v_min_i32_e32 v56, 78, v56\n"
    "v_min_i32_e32 v57, 78, v57\n"
    "v_mad_u32_u24 v48, v48, s48, v54\n"
    "v_mad_u32_u24 v49, v49, s48, v55\n"
    "v_mad_u32_u24 v50, v50, s48, v56\n"
    "v_mad_u32_u24 v51, v51, s48, v57\n"
    "v_lshl_add_u32 v48, v48, 2, s44\n"
    "v_lshl_add_u32 v49, v49, 2, s44\n"
    "v_lshl_add_u32 v50, v50, 2, s44\n"
    "v_lshl_add_u32 v51, v51, 2, s44\n"
    "ds_write_b32 v48, v96 offset:4\n"
    "ds_write_b32 v49, v97 offset:4\n"
    "ds_write_b32 v50, v98 offset:4\n"
    "ds_write_b32 v51, v99 offset:4\n"
    "s_mov_b64 exec, s[46:47]\n"
    "v_min_i32_e32 v58, 78, v58\n"
    "v_min_i32_e32 v59, 78, v59\n"
    "v_mad_u32_u24 v52, v52, s48, v58\n"
    "v_mad_u32_u24 v53, v53, s48, v59\n"
    "v_lshl_add_u32 v52, v52, 2, s44\n"
    "v_lshl_add_u32 v53, v53, 2, s44\n"
    "ds_write_b32 v52, v92 offset:4\n"
    "ds_write_b32 v53, v93 offset:4\n"
    "s_mov_b64 exec, -1\n"
    ".Lat_noj2:\n"
    "buffer_store_dwordx2 v[48:49], v74, s[8:11], s52 offen offset:0 sc1\n"
    "v_mov_b32_e32 v112, s39\n"
    "v_mov_b32_e32 v113, 0\n"
    "s_waitcnt vmcnt(1)\n"
    "v_dot2_f32_bf16 v24, v0, s58, 0\n"
    "v_dot2_f32_bf16 v32, v8, s58, 0\n"
    "v_dot2_f32_bf16 v40, v16, s58, 0\n"
    "v_dot2_f32_bf16 v25, v4, s58, 0\n"
    "v_dot2_f32_bf16 v33, v12, s58, 0\n"
    "v_dot2_f32_bf16 v41, v20, s58, 0\n"
    "v_dot2_f32_bf16 v26, v0, v0, 0\n"
    "v_dot2_f32_bf16 v34, v8, v8, 0\n"
    "v_dot2_f32_bf16 v42, v16, v16, 0\n"
    "v_dot2_f32_bf16 v27, v4, v4, 0\n"
    "v_dot2_f32_bf16 v35, v12, v12, 0\n"
    "v_dot2_f32_bf16 v43, v20, v20, 0\n"
    "v_dot2_f32_bf16 v24, v1, s58, v24\n"
    "v_dot2_f32_bf16 v32, v9, s58, v32\n"
    "v_dot2_f32_bf16 v40, v17, s58, v40\n"
    "v_dot2_f32_bf16 v25, v5, s58, v25\n"
    "v_dot2_f32_bf16 v33, v13, s58, v33\n"
    "v_dot2_f32_bf16 v41, v21, s58, v41\n"
    "v_dot2_f32_bf16 v26, v1, v1, v26\n"
    "v_dot2_f32_bf16 v34, v9, v9, v34\n"
    "v_dot2_f32_bf16 v42, v17, v17, v42\n"
    "v_dot2_f32_bf16 v27, v5, v5, v27\n"
    "v_dot2_f32_bf16 v35, v13, v13, v35\n"
    "v_dot2_f32_bf16 v43, v21, v21, v43\n"
    "v_dot2_f32_bf16 v24, v2, s58, v24\n"
    "v_dot2_f32_bf16 v32, v10, s58, v32\n"
    "v_dot2_f32_bf16 v40, v18, s58, v40\n"
    "v_dot2_f32_bf16 v25, v6, s58, v25\n"
    "v_dot2_f32_bf16 v33, v14, s58, v33\n"
    "v_dot2_f32_bf16 v41, v22, s58, v41\n"
    "v_dot2_f32_bf16 v26, v2, v2, v26\n"
    "v_dot2_f32_bf16 v34, v10, v10, v34\n"
    "v_dot2_f32_bf16 v42, v18, v18, v42\n"
    "v_dot2_f32_bf16 v27, v6, v6, v27\n"
    "v_dot2_f32_bf16 v35, v14, v14, v35\n"
    "v_dot2_f32_bf16 v43, v22, v22, v43\n"
    "v_dot2_f32_bf16 v24, v3, s58, v24\n"
    "v_dot2_f32_bf16 v32, v11, s58, v32\n"
    "v_dot2_f32_bf16 v40, v19, s58, v40\n"
    "v_dot2_f32_bf16 v25, v7, s58, v25\n"
    "v_dot2_f32_bf16 v33, v15, s58, v33\n"
    "v_dot2_f32_bf16 v41, v23, s58, v41\n"
    "v_dot2_f32_bf16 v26, v3, v3, v26\n"
    "v_dot2_f32_bf16 v34, v11, v11, v34\n"
    "v_dot2_f32_bf16 v42, v19, v19, v42\n"
    "v_dot2_f32_bf16 v27, v7, v7, v27\n"
    "v_dot2_f32_bf16 v35, v15, v15, v35\n"
    "v_dot2_f32_bf16 v43, v23, v23, v43\n"
    "v_and_b32_e32 v30, 0xffff0000, v7\n"
    "v_and_b32_e32 v38, 0xffff0000, v15\n"
    "v_and_b32_e32 v46, 0xffff0000, v23\n"
    "v_add_f32_e32 v24, v24, v25\n"
    "v_add_f32_e32 v32, v32, v33\n"
    "v_add_f32_e32 v40, v40, v41\n"
    "v_add_f32_e32 v26, v26, v27\n"
    "v_add_f32_e32 v34, v34, v35\n"
    "v_add_f32_e32 v42, v42, v43\n"
    "v_add_f32_dpp v25, v25, v25 quad_perm:[1,0,3,2] row_mask:0xf bank_mask:0xf bound_ctrl:1\n"
    "v_lshlrev_b32_e32 v48, 16, v0\n"
    "v_add_f32_dpp v33, v33, v33 quad_perm:[1,0,3,2] row_mask:0xf bank_mask:0xf bound_ctrl:1\n"
    "v_and_b32_e32 v49, 0xffff0000, v0\n"
    "v_add_f32_dpp v41, v41, v41 quad_perm:[1,0,3,2] row_mask:0xf bank_mask:0xf bound_ctrl:1\n"
    "v_lshlrev_b32_e32 v50, 16, v1\n"
    "v_mov_b32_dpp v30, v30 quad_perm:[3,3,3,3] row_mask:0xf bank_mask:0xf bound_ctrl:1\n"
    "v_and_b32_e32 v51, 0xffff0000, v1\n"
    "v_mov_b32_dpp v38, v38 quad_perm:[3,3,3,3] row_mask:0xf bank_mask:0xf bound_ctrl:1\n"
    "v_lshlrev_b32_e32 v52, 16, v2\n"
    "v_mov_b32_dpp v46, v46 quad_perm:[3,3,3,3] row_mask:0xf bank_mask:0xf bound_ctrl:1\n"
    "v_and_b32_e32 v53, 0xffff0000, v2\n"
    "v_add_f32_dpp v24, v24, v24 quad_perm:[1,0,3,2] row_mask:0xf bank_mask:0xf bound_ctrl:1\n"
    "v_lshlrev_b32_e32 v54, 16, v3\n"
    "v_add_f32_dpp v32, v32, v32 quad_perm:[1,0,3,2] row_mask:0xf bank_mask:0xf bound_ctrl:1\n"
    "v_and_b32_e32 v55, 0xffff0000, v3\n"
    "v_add_f32_dpp v40, v40, v40 quad_perm:[1,0,3,2] row_mask:0xf bank_mask:0xf bound_ctrl:1\n"
    "v_lshlrev_b32_e32 v56, 16, v4\n"
    "v_add_f32_dpp v26, v26, v26 quad_perm:[1,0,3,2] row_mask:0xf bank_mask:0xf bound_ctrl:1\n"
    "v_and_b32_e32 v57, 0xffff0000, v4\n"
    "v_add_f32_dpp v34, v34, v34 quad_perm:[1,0,3,2] row_mask:0xf bank_mask:0xf bound_ctrl:1\n"
    "v_lshlrev_b32_e32 v58, 16, v5\n"
    "s_waitcnt vmcnt(0)\n"
    "buffer_store_dword v112, v113, s[8:11], s53 offen offset:0 sc1\n"
    "v_add_f32_dpp v42, v42, v42 quad_perm:[1,0,3,2] row_mask:0xf bank_mask:0xf bound_ctrl:1\n"
    "v_and_b32_e32 v59, 0xffff0000, v5\n"
    "v_add_f32_dpp v24, v24, v24 quad_perm:[2,3,0,1] row_mask:0xf bank_mask:0xf bound_ctrl:1\n"
    "v_lshlrev_b32_e32 v60, 16, v6\n"
    "v_add_f32_dpp v32, v32, v32 quad_perm:[2,3,0,1] row_mask:0xf bank_mask:0xf bound_ctrl:1\n"
    "v_and_b32_e32 v61, 0xffff0000, v6\n"
    "s_waitcnt lgkmcnt(0)\n"
    "s_barrier\n"
    "ds_read_b128 v[96:99], v108 offset:0\n"
    "ds_read_b128 v[100:103], v108 offset:4096\n"
    "s_lshl_b32 s45, %1, 6\n"
    "s_add_u32 s45, s45, 34956288\n"
    "v_add_f32_dpp v40, v40, v40 quad_perm:[2,3,0,1] row_mask:0xf bank_mask:0xf bound_ctrl:1\n"
    "v_lshlrev_b32_e32 v62, 16, v7\n"
    "v_add_f32_dpp v26, v26, v26 quad_perm:[2,3,0,1] row_mask:0xf bank_mask:0xf bound_ctrl:1\n"
    "v_and_b32_e32 v63, 0xffff0000, v7\n"
    "v_add_f32_dpp v34, v34, v34 quad_perm:[2,3,0,1] row_mask:0xf bank_mask:0xf bound_ctrl:1\n"
    "v_lshlrev_b32_e32 v64, 16, v8\n"
    "v_add_f32_dpp v42, v42, v42 quad_perm:[2,3,0,1] row_mask:0xf bank_mask:0xf bound_ctrl:1\n"
    "v_and_b32_e32 v65, 0xffff0000, v8\n"
    "v_add_f32_dpp v24, v24, v24 row_half_mirror row_mask:0xf bank_mask:0xf bound_ctrl:1\n"
    "v_lshlrev_b32_e32 v66, 16, v9\n"
    "v_add_f32_dpp v32, v32, v32 row_half_mirror row_mask:0xf bank_mask:0xf bound_ctrl:1\n"
    "v_and_b32_e32 v67, 0xffff0000, v9\n"
    "v_add_f32_dpp v40, v40, v40 row_half_mirror row_mask:0xf bank_mask:0xf bound_ctrl:1\n"
    "v_lshlrev_b32_e32 v68, 16, v10\n"
    "v_add_f32_dpp v26, v26, v26 row_half_mirror row_mask:0xf bank_mask:0xf bound_ctrl:1\n"
    "v_and_b32_e32 v69, 0xffff0000, v10\n"
    "v_add_f32_dpp v34, v34, v34 row_half_mirror row_mask:0xf bank_mask:0xf bound_ctrl:1\n"
    "v_lshlrev_b32_e32 v70, 16, v11\n"
    "v_add_f32_dpp v42, v42, v42 row_half_mirror row_mask:0xf bank_mask:0xf bound_ctrl:1\n"
    "v_and_b32_e32 v71, 0xffff0000, v11\n"
    "v_add_f32_dpp v24, v24, v24 row_mirror row_mask:0xf bank_mask:0xf bound_ctrl:1\n"
    "v_lshlrev_b32_e32 v72, 16, v12\n"
    "v_add_f32_dpp v32, v32, v32 row_mirror row_mask:0xf bank_mask:0xf bound_ctrl:1\n"
    "v_and_b32_e32 v73, 0xffff0000, v12\n"
    "v_add_f32_dpp v40, v40, v40 row_mirror row_mask:0xf bank_mask:0xf bound_ctrl:1\n"
    "v_lshlrev_b32_e32 v74, 16, v13\n"
    "v_add_f32_dpp v26, v26, v26 row_mirror row_mask:0xf bank_mask:0xf bound_ctrl:1\n"
    "v_and_b32_e32 v75, 0xffff0000, v13\n"
    "v_add_f32_dpp v34, v34, v34 row_mirror row_mask:0xf bank_mask:0xf bound_ctrl:1\n"
    "v_lshlrev_b32_e32 v76, 16, v14\n"
    "s_waitcnt lgkmcnt(0)\n"
    "buffer_store_dwordx4 v[96:99], v109, s[8:11], s45 offen offset:0 sc1\n"
    "buffer_store_dwordx4 v[100:103], v110, s[8:11], s45 offen offset:0 sc1\n"
    "v_add_f32_dpp v42, v42, v42 row_mirror row_mask:0xf bank_mask:0xf bound_ctrl:1\n"
    "v_and_b32_e32 v77, 0xffff0000, v14\n"
    "v_mul_f32_e32 v24, 0x3b800000, v24\n"
    "v_lshlrev_b32_e32 v78, 16, v15\n"
    "v_mul_f32_e32 v32, 0x3b800000, v32\n"
    "v_and_b32_e32 v79, 0xffff0000, v15\n"
    "v_mul_f32_e32 v40, 0x3b800000, v40\n"
    "v_lshlrev_b32_e32 v80, 16, v16\n"
    "v_mul_f32_e32 v25, 0.25, v25\n"
    "v_and_b32_e32 v81, 0xffff0000, v16\n"
    "v_mul_f32_e32 v33, 0.25, v33\n"
    "v_lshlrev_b32_e32 v82, 16, v17\n"
    "v_mul_f32_e32 v41, 0.25, v41\n"
    "v_and_b32_e32 v83, 0xffff0000, v17\n"
    "v_mul_f32_e64 v27, v24, -v24\n"
    "v_lshlrev_b32_e32 v84, 16, v18\n"
    "v_mul_f32_e64 v35, v32, -v32\n"
    "v_and_b32_e32 v85, 0xffff0000, v18\n"
    "v_mul_f32_e64 v43, v40, -v40\n"
    "v_lshlrev_b32_e32 v86, 16, v19\n"
    "v_sub_f32_e32 v25, v25, v30\n"
    "v_and_b32_e32 v87, 0xffff0000, v19\n"
    "v_sub_f32_e32 v33, v33, v38\n"
    "v_lshlrev_b32_e32 v88, 16, v20\n"
    "v_sub_f32_e32 v41, v41, v46\n"
    "v_and_b32_e32 v89, 0xffff0000, v20\n"
    "v_fmac_f32_e32 v27, 0x3b800000, v26\n"
    "v_lshlrev_b32_e32 v90, 16, v21\n"
    "v_fmac_f32_e32 v35, 0x3b800000, v34\n"
    "v_and_b32_e32 v91, 0xffff0000, v21\n"
    "v_fmac_f32_e32 v43, 0x3b800000, v42\n"
    "v_lshlrev_b32_e32 v92, 16, v22\n"
    "v_mul_f32_e32 v25, 0x3eaaaaab, v25\n"
    "v_and_b32_e32 v93, 0xffff0000, v22\n"
    "v_mul_f32_e32 v33, 0x3eaaaaab, v33\n"
    "v_lshlrev_b32_e32 v94, 16, v23\n"
    "v_mul_f32_e32 v41, 0x3eaaaaab, v41\n"
    "v_and_b32_e32 v95, 0xffff0000, v23\n"
    "v_max_f32_e32 v27, 0, v27\n"
    "v_max_f32_e32 v35, 0, v35\n"
    "v_max_f32_e32 v43, 0, v43\n"
    "v_cndmask_b32_e64 v25, v24, v25, s[62:63]\n"
    "v_cndmask_b32_e64 v33, v32, v33, s[62:63]\n"
    "v_cndmask_b32_e64 v41, v40, v41, s[62:63]\n"
    "v_add_f32_e32 v27, 0x3727c5ac, v27\n"
    "v_add_f32_e32 v35, 0x3727c5ac, v35\n"
    "v_add_f32_e32 v43, 0x3727c5ac, v43\n"
    "v_rsq_f32_e32 v28, v27\n"
    "v_rsq_f32_e32 v36, v35\n"
    "v_rsq_f32_e32 v44, v43\n"
    "v_fma_f32 v29, -v24, v28, s57\n"
    "v_fma_f32 v37, -v32, v36, s57\n"
    "v_fma_f32 v45, -v40, v44, s57\n"
    "v_fma_f32 v31, -v25, v28, s57\n"
    "v_fma_f32 v39, -v33, v36, s57\n"
    "v_fma_f32 v47, -v41, v44, s57\n"
    "v_sub_f32_e32 v24, v29, v31\n"
    "v_sub_f32_e32 v32, v37, v39\n"
    "v_sub_f32_e32 v40, v45, v47\n"
    "v_pk_fma_f32 v[48:49], v[48:49], v[28:29], v[28:29] op_sel:[0,0,1] op_sel_hi:[1,0,1]\n"
    "v_pk_fma_f32 v[50:51], v[50:51], v[28:29], v[28:29] op_sel:[0,0,1] op_sel_hi:[1,0,1]\n"
    "v_pk_fma_f32 v[52:53], v[52:53], v[28:29], v[28:29] op_sel:[0,0,1] op_sel_hi:[1,0,1]\n"
    "v_pk_fma_f32 v[54:55], v[54:55], v[28:29], v[28:29] op_sel:[0,0,1] op_sel_hi:[1,0,1]\n"
    "v_pk_fma_f32 v[56:57], v[56:57], v[28:29], v[30:31] op_sel:[0,0,1] op_sel_hi:[1,0,1]\n"
    "v_pk_fma_f32 v[58:59], v[58:59], v[28:29], v[30:31] op_sel:[0,0,1] op_sel_hi:[1,0,1]\n"
    "v_pk_fma_f32 v[60:61], v[60:61], v[28:29], v[30:31] op_sel:[0,0,1] op_sel_hi:[1,0,1]\n"
    "v_pk_fma_f32 v[62:63], v[62:63], v[28:29], v[30:31] op_sel:[0,0,1] op_sel_hi:[1,0,1]\n"
    "v_pk_add_f32 v[48:49], v[48:49], s[56:57] op_sel_hi:[1,0]\n"
    "v_pk_add_f32 v[50:51], v[50:51], s[56:57] op_sel_hi:[1,0]\n"
    "v_pk_add_f32 v[52:53], v[52:53], s[56:57] op_sel_hi:[1,0]\n"
    "v_pk_add_f32 v[54:55], v[54:55], s[56:57] op_sel_hi:[1,0]\n"
    "v_pk_add_f32 v[56:57], v[56:57], s[56:57] op_sel_hi:[1,0]\n"
    "v_pk_add_f32 v[58:59], v[58:59], s[56:57] op_sel_hi:[1,0]\n"
    "v_pk_add_f32 v[60:61], v[60:61], s[56:57] op_sel_hi:[1,0]\n"
    "v_pk_add_f32 v[62:63], v[62:63], s[56:57] op_sel_hi:[1,0]\n"
    "v_cvt_pk_bf16_f32 v0, v48, v49\n"
    "v_cvt_pk_bf16_f32 v1, v50, v51\n"
    "v_cvt_pk_bf16_f32 v2, v52, v53\n"
    "v_cvt_pk_bf16_f32 v3, v54, v55\n"
    "v_cvt_pk_bf16_f32 v4, v56, v57\n"
    "v_cvt_pk_bf16_f32 v5, v58, v59\n"
    "v_cvt_pk_bf16_f32 v6, v60, v61\n"
    "v_cvt_pk_bf16_f32 v7, v62, v63\n"
    "buffer_store_dwordx4 v[0:3], v104, s[8:11], s35 offen offset:0 sc1\n"
    "v_bfi_b32 v7, v107, v7, 1.0\n"
    "ds_write_b128 v105, v[0:3] offset:0\n"
    "buffer_store_dwordx4 v[4:7], v104, s[8:11], s35 offen offset:256 sc1\n"
    "v_pk_fma_f32 v[64:65], v[64:65], v[36:37], v[36:37] op_sel:[0,0,1] op_sel_hi:[1,0,1]\n"
    "v_pk_fma_f32 v[66:67], v[66:67], v[36:37], v[36:37] op_sel:[0,0,1] op_sel_hi:[1,0,1]\n"
    "v_pk_fma_f32 v[68:69], v[68:69], v[36:37], v[36:37] op_sel:[0,0,1] op_sel_hi:[1,0,1]\n"
    "v_pk_fma_f32 v[70:71], v[70:71], v[36:37], v[36:37] op_sel:[0,0,1] op_sel_hi:[1,0,1]\n"
    "v_pk_fma_f32 v[72:73], v[72:73], v[36:37], v[38:39] op_sel:[0,0,1] op_sel_hi:[1,0,1]\n"
    "v_pk_fma_f32 v[74:75], v[74:75], v[36:37], v[38:39] op_sel:[0,0,1] op_sel_hi:[1,0,1]\n"
    "v_pk_fma_f32 v[76:77], v[76:77], v[36:37], v[38:39] op_sel:[0,0,1] op_sel_hi:[1,0,1]\n"
    "v_pk_fma_f32 v[78:79], v[78:79], v[36:37], v[38:39] op_sel:[0,0,1] op_sel_hi:[1,0,1]\n"
    "v_pk_add_f32 v[64:65], v[64:65], s[56:57] op_sel_hi:[1,0]\n"
    "v_pk_add_f32 v[66:67], v[66:67], s[56:57] op_sel_hi:[1,0]\n"
    "v_pk_add_f32 v[68:69], v[68:69], s[56:57] op_sel_hi:[1,0]\n"
    "v_pk_add_f32 v[70:71], v[70:71], s[56:57] op_sel_hi:[1,0]\n"
    "v_pk_add_f32 v[72:73], v[72:73], s[56:57] op_sel_hi:[1,0]\n"
    "v_pk_add_f32 v[74:75], v[74:75], s[56:57] op_sel_hi:[1,0]\n"
    "v_pk_add_f32 v[76:77], v[76:77], s[56:57] op_sel_hi:[1,0]\n"
    "v_pk_add_f32 v[78:79], v[78:79], s[56:57] op_sel_hi:[1,0]\n"
    "v_cvt_pk_bf16_f32 v8, v64, v65\n"
    "v_cvt_pk_bf16_f32 v9, v66, v67\n"
    "v_cvt_pk_bf16_f32 v10, v68, v69\n"
    "v_cvt_pk_bf16_f32 v11, v70, v71\n"
    "v_cvt_pk_bf16_f32 v12, v72, v73\n"
    "v_cvt_pk_bf16_f32 v13, v74, v75\n"
    "v_cvt_pk_bf16_f32 v14, v76, v77\n"
    "v_cvt_pk_bf16_f32 v15, v78, v79\n"
    "buffer_store_dwordx4 v[8:11], v104, s[8:11], s54 offen offset:0 sc1\n"
    "v_bfi_b32 v15, v107, v15, 1.0\n"
    "ds_write_b128 v105, v[8:11] offset:8192\n"
    "buffer_store_dwordx4 v[12:15], v104, s[8:11], s54 offen offset:256 sc1\n"
    "v_pk_fma_f32 v[80:81], v[80:81], v[44:45], v[44:45] op_sel:[0,0,1] op_sel_hi:[1,0,1]\n"
    "v_pk_fma_f32 v[82:83], v[82:83], v[44:45], v[44:45] op_sel:[0,0,1] op_sel_hi:[1,0,1]\n"
    "v_pk_fma_f32 v[84:85], v[84:85], v[44:45], v[44:45] op_sel:[0,0,1] op_sel_hi:[1,0,1]\n"
    "v_pk_fma_f32 v[86:87], v[86:87], v[44:45], v[44:45] op_sel:[0,0,1] op_sel_hi:[1,0,1]\n"
    "v_pk_fma_f32 v[88:89], v[88:89], v[44:45], v[46:47] op_sel:[0,0,1] op_sel_hi:[1,0,1]\n"
    "v_pk_fma_f32 v[90:91], v[90:91], v[44:45], v[46:47] op_sel:[0,0,1] op_sel_hi:[1,0,1]\n"
    "v_pk_fma_f32 v[92:93], v[92:93], v[44:45], v[46:47] op_sel:[0,0,1] op_sel_hi:[1,0,1]\n"
    "v_pk_fma_f32 v[94:95], v[94:95], v[44:45], v[46:47] op_sel:[0,0,1] op_sel_hi:[1,0,1]\n"
    "v_pk_add_f32 v[80:81], v[80:81], s[56:57] op_sel_hi:[1,0]\n"
    "v_pk_add_f32 v[82:83], v[82:83], s[56:57] op_sel_hi:[1,0]\n"
    "v_pk_add_f32 v[84:85], v[84:85], s[56:57] op_sel_hi:[1,0]\n"
    "v_pk_add_f32 v[86:87], v[86:87], s[56:57] op_sel_hi:[1,0]\n"
    "v_pk_add_f32 v[88:89], v[88:89], s[56:57] op_sel_hi:[1,0]\n"
    "v_pk_add_f32 v[90:91], v[90:91], s[56:57] op_sel_hi:[1,0]\n"
    "v_pk_add_f32 v[92:93], v[92:93], s[56:57] op_sel_hi:[1,0]\n"
    "v_pk_add_f32 v[94:95], v[94:95], s[56:57] op_sel_hi:[1,0]\n"
    "v_cvt_pk_bf16_f32 v16, v80, v81\n"
    "v_cvt_pk_bf16_f32 v17, v82, v83\n"
    "v_cvt_pk_bf16_f32 v18, v84, v85\n"
    "v_cvt_pk_bf16_f32 v19, v86, v87\n"
    "v_cvt_pk_bf16_f32 v20, v88, v89\n"
    "v_cvt_pk_bf16_f32 v21, v90, v91\n"
    "v_cvt_pk_bf16_f32 v22, v92, v93\n"
    "v_cvt_pk_bf16_f32 v23, v94, v95\n"
    "buffer_store_dwordx4 v[16:19], v104, s[8:11], s60 offen offset:0 sc1\n"
    "v_bfi_b32 v23, v107, v23, 1.0\n"
    "ds_write_b128 v105, v[16:19] offset:16384\n"
    "buffer_store_dwordx4 v[20:23], v104, s[8:11], s60 offen offset:256 sc1\n"
    "v_pk_add_f32 v[56:57], v[56:57], v[24:25] op_sel_hi:[1,0]\n"
    "v_pk_add_f32 v[58:59], v[58:59], v[24:25] op_sel_hi:[1,0]\n"
    "v_pk_add_f32 v[60:61], v[60:61], v[24:25] op_sel_hi:[1,0]\n"
    "v_pk_add_f32 v[62:63], v[62:63], v[24:25] op_sel_hi:[1,0]\n"
    "v_cvt_pk_bf16_f32 v56, v56, v57\n"
    "v_cvt_pk_bf16_f32 v57, v58, v59\n"
    "v_cvt_pk_bf16_f32 v58, v60, v61\n"
    "v_cvt_pk_bf16_f32 v59, v62, v63\n"
    "ds_write_b128 v106, v[56:59] offset:0\n"
    "v_pk_add_f32 v[72:73], v[72:73], v[32:33] op_sel_hi:[1,0]\n"
    "v_pk_add_f32 v[74:75], v[74:75], v[32:33] op_sel_hi:[1,0]\n"
    "v_pk_add_f32 v[76:77], v[76:77], v[32:33] op_sel_hi:[1,0]\n"
    "v_pk_add_f32 v[78:79], v[78:79], v[32:33] op_sel_hi:[1,0]\n"
    "v_cvt_pk_bf16_f32 v72, v72, v73\n"
    "v_cvt_pk_bf16_f32 v73, v74, v75\n"
    "v_cvt_pk_bf16_f32 v74, v76, v77\n"
    "v_cvt_pk_bf16_f32 v75, v78, v79\n"
    "ds_write_b128 v106, v[72:75] offset:8192\n"
    "v_pk_add_f32 v[88:89], v[88:89], v[40:41] op_sel_hi:[1,0]\n"
    "v_pk_add_f32 v[90:91], v[90:91], v[40:41] op_sel_hi:[1,0]\n"
    "v_pk_add_f32 v[92:93], v[92:93], v[40:41] op_sel_hi:[1,0]\n"
    "v_pk_add_f32 v[94:95], v[94:95], v[40:41] op_sel_hi:[1,0]\n"
    "v_cvt_pk_bf16_f32 v88, v88, v89\n"
    "v_cvt_pk_bf16_f32 v89, v90, v91\n"
    "v_cvt_pk_bf16_f32 v90, v92, v93\n"
    "v_cvt_pk_bf16_f32 v91, v94, v95\n"
    "ds_write_b128 v106, v[88:91] offset:16384\n"
    "s_waitcnt lgkmcnt(0)\n"
    "s_barrier\n"
    "ds_read_b128 a[0:3], v250 offset:0\n"
    "ds_read_b128 a[32:35], v250 offset:8192\n"
    "ds_read_b128 a[64:67], v250 offset:16384\n"
    "ds_read_b128 a[4:7], v251 offset:0\n"
    "ds_read_b128 a[36:39], v251 offset:8192\n"
    "ds_read_b128 a[68:71], v251 offset:16384\n"
    "ds_read_b128 a[8:11], v250 offset:2048\n"
    "ds_read_b128 a[40:43], v250 offset:10240\n"
    "ds_read_b128 a[72:75], v250 offset:18432\n"
    "ds_read_b128 a[12:15], v251 offset:2048\n"
    "ds_read_b128 a[44:47], v251 offset:10240\n"
    "ds_read_b128 a[76:79], v251 offset:18432\n"
    "buffer_load_dword v255, v252, s[8:11], 0 offen offset:0 sc1\n"
    "ds_read_b128 a[16:19], v250 offset:4096\n"
    "ds_read_b128 a[48:51], v250 offset:12288\n"
    "ds_read_b128 a[80:83], v250 offset:20480\n"
    "ds_read_b128 a[20:23], v251 offset:4096\n"
    "ds_read_b128 a[52:55], v251 offset:12288\n"
    "ds_read_b128 a[84:87], v251 offset:20480\n"
    "ds_read_b128 a[24:27], v250 offset:6144\n"
    "ds_read_b128 a[56:59], v250 offset:14336\n"
    "ds_read_b128 a[88:91], v250 offset:22528\n"
    "ds_read_b128 a[28:31], v251 offset:6144\n"
    "ds_read_b128 a[60:63], v251 offset:14336\n"
    "ds_read_b128 a[92:95], v251 offset:22528\n"
    "s_lshl_b32 s52, %2, 15\n"
    "s_add_u32 s52, s52, 5242880\n"
    "s_lshl_b32 s61, s37, 12\n"
    "s_lshl_b32 s46, %1, 4\n"
    "s_lshl_b32 s47, %1, 7\n"
    "s_add_u32 s47, s47, 4096\n"
    "s_cmp_eq_u32 s50, 0\n"
    "s_cselect_b32 s46, s46, s47\n"
    "s_add_u32 s46, s46, 5652480\n"
    "s_cmp_eq_u32 s51, 0\n"
    "s_cselect_b32 s46, s46, 2.0\n"
    "v_mov_b32_e32 v40, s39\n"
    "v_mov_b32_e32 v41, 0\n"
    "s_waitcnt vmcnt(1)\n"
    "s_waitcnt lgkmcnt(0)\n"
    "s_barrier\n"
    "buffer_store_dword v40, v41, s[8:11], s46 offen offset:0 sc1\n"
    "s_waitcnt vmcnt(1)\n"
    "v_cmp_ne_u32_e32 vcc, s39, v255\n"
    "s_nop 4\n"
    "s_cbranch_vccnz .Lat_apoll\n"
    ".Lat_apoll_ret:\n"
    "s_add_u32 s47, s52, s61\n"
    "buffer_load_dwordx4 v[112:115], v253, s[8:11], s47 offen offset:0 sc1\n"
    "buffer_load_dwordx4 v[116:119], v253, s[8:11], s47 offen offset:1024 sc1\n"
    "buffer_load_dwordx4 v[128:131], v253, s[8:11], s47 offen offset:2048 sc1\n"
    "s_add_u32 s61, s61, 0x1000\n"
    "buffer_load_dwordx4 v[132:135], v253, s[8:11], s47 offen offset:3072 sc1\n"
    "s_bitset0_b32 s61, 14\n"
    "s_add_u32 s47, s52, s61\n"
    "buffer_load_dwordx4 v[144:147], v253, s[8:11], s47 offen offset:0 sc1\n"
    "buffer_load_dwordx4 v[148:151], v253, s[8:11], s47 offen offset:1024 sc1\n"
    "buffer_load_dwordx4 v[160:163], v253, s[8:11], s47 offen offset:2048 sc1\n"
    "s_add_u32 s61, s61, 0x1000\n"
    "buffer_load_dwordx4 v[164:167], v253, s[8:11], s47 offen offset:3072 sc1\n"
    "s_bitset0_b32 s61, 14\n"
    "s_add_u32 s47, s52, s61\n"
    "buffer_load_dwordx4 v[176:179], v253, s[8:11], s47 offen offset:0 sc1\n"
    "buffer_load_dwordx4 v[180:183], v253, s[8:11], s47 offen offset:1024 sc1\n"
    "buffer_load_dwordx4 v[192:195], v253, s[8:11], s47 offen offset:2048 sc1\n"
    "s_add_u32 s61, s61, 0x1000\n"
    "s_lshl_b32 s59, s32, 4\n"
    "s_lshr_b32 s48, s32, 1\n"
    "s_sub_u32 s59, s59, s48\n"
    "s_lshl_b32 s59, s59, 6\n"
    "s_sub_u32 s48, 3984, s59\n"
    "v_cmp_le_u32_e32 vcc, 0x40, v253\n"
    "v_mov_b32_e32 v0, s48\n"
    "s_cmp_eq_u32 s32, 2\n"
    "s_cselect_b32 s48, 1024, 0\n"
    "v_mov_b32_e32 v2, s48\n"
    "s_add_u32 s59, s59, 5652480\n"
    "v_cndmask_b32_e32 v3, v253, v0, vcc\n"
    "s_lshl_b32 s45, %2, 11\n"
    "s_add_u32 s48, s45, %3\n"
    "v_add_u32_e32 v9, s48, v253\n"
    "s_add_u32 s45, s45, 10940416\n"
    "v_add_u32_e32 v8, s45, v253\n"
    "v_lshrrev_b32_e32 v13, 7, v253\n"
    "v_and_b32_e32 v12, 0x70, v254\n"
    "v_lshl_add_u32 v12, v13, 9, v12\n"
    "v_add_u32_e32 v13, 0xc00, v12\n"
    "v_add_u32_e32 v14, 0x1800, v12\n"
    "v_add_u32_e32 v15, 0x2400, v12\n"
    "s_lshl_b32 s57, s50, 8\n"
    "s_add_u32 s57, s57, 5111808\n"
    "s_add_u32 s62, s43, s34\n"
    "s_xor_b32 s63, s34, 0xc000\n"
    "s_add_u32 s63, s63, s43\n"
    "s_waitcnt vmcnt(9)\n"
    "v_mfma_f32_16x16x32_bf16 v[24:27], v[112:115], a[0:3], 0\n"
    "buffer_load_dwordx4 v[196:199], v253, s[8:11], s47 offen offset:3072 sc1\n"
    "v_mfma_f32_16x16x32_bf16 v[28:31], v[116:119], a[0:3], 0\n"
    "s_bitset0_b32 s61, 14\n"
    "s_mov_b32 s18, 5120000\n"
    "s_mov_b32 s19, 0x20000\n"
    "v_mfma_f32_16x16x32_bf16 v[48:51], v[112:115], a[32:35], 0\n"
    "s_add_u32 s47, s52, s61\n"
    "v_xor_b32_e32 v242, 0x12000, v240\n"
    "v_xor_b32_e32 v243, 0x12000, v241\n"
    "v_mov_b32_e32 v80, 0\n"
    "v_mfma_f32_16x16x32_bf16 v[52:55], v[116:119], a[32:35], 0\n"
    "buffer_load_dwordx4 v[208:211], v253, s[8:11], s47 offen offset:0 sc1\n"
    "v_mfma_f32_16x16x32_bf16 v[64:67], v[112:115], a[64:67], 0\n"
    "s_cmp_eq_u32 %2, 3\n"
    "s_cselect_b32 s88, 0x2ff, -1\n"
    "v_mov_b32_e32 v10, 0xc2980000\n"
    "v_mov_b32_e32 v81, 0\n"
    "v_mfma_f32_16x16x32_bf16 v[68:71], v[116:119], a[64:67], 0\n"
    "s_waitcnt vmcnt(9)\n"
    "v_cmp_lt_u32_e32 vcc, s88, v253\n"
    "s_nop 1\n"
    "v_cndmask_b32_e32 v10, 0, v10, vcc\n"
    "v_mfma_f32_16x16x32_bf16 v[24:27], v[128:131], a[4:7], v[24:27]\n"
    "buffer_load_dwordx4 v[212:215], v253, s[8:11], s47 offen offset:1024 sc1\n"
    "v_mfma_f32_16x16x32_bf16 v[28:31], v[132:135], a[4:7], v[28:31]\n"
    "s_add_u32 s52, s52, 0x4000\n"
    "v_lshrrev_b32_e32 v5, 4, v253\n"
    "v_min_u32_e32 v5, 2, v5\n"
    "v_mfma_f32_16x16x32_bf16 v[48:51], v[128:131], a[36:39], v[48:51]\n"
    "s_add_u32 s88, s32, 1\n"
    "v_add_u32_e32 v5, s88, v5\n"
    "v_mov_b32_e32 v82, 0\n"
    "v_mfma_f32_16x16x32_bf16 v[52:55], v[132:135], a[36:39], v[52:55]\n"
    "buffer_load_dwordx4 v[224:227], v253, s[8:11], s47 offen offset:2048 sc1\n"
    "v_mfma_f32_16x16x32_bf16 v[64:67], v[128:131], a[68:71], v[64:67]\n"
    "s_add_u32 s61, s61, 0x1000\n"
    "v_and_b32_e32 v5, 3, v5\n"
    "v_lshlrev_b32_e32 v5, 7, v5\n"
    "v_mov_b32_e32 v83, 0\n"
    "v_mfma_f32_16x16x32_bf16 v[68:71], v[132:135], a[68:71], v[68:71]\n"
    "s_waitcnt vmcnt(9)\n"
    "s_lshl_b32 s58, %1, 7\n"
    "s_add_u32 s58, s58, 5656576\n"
    "s_cmp_eq_u32 %2, 2\n"
    "s_cselect_b32 s58, s58, 2.0\n"
    "v_mfma_f32_16x16x32_bf16 v[24:27], v[144:147], a[8:11], v[24:27]\n"
    "buffer_load_dwordx4 v[228:231], v253, s[8:11], s47 offen offset:3072 sc1\n"
    "v_mfma_f32_16x16x32_bf16 v[28:31], v[148:151], a[8:11], v[28:31]\n"
    "s_bitset0_b32 s61, 14\n"
    "s_lshl_b32 s88, s50, 13\n"
    "s_add_u32 s88, s88, %3\n"
    "v_mfma_f32_16x16x32_bf16 v[48:51], v[144:147], a[40:43], v[48:51]\n"
    "s_add_u32 s47, s52, s61\n"
    "v_add_u32_e32 v6, s88, v253\n"
    "v_add_u32_e32 v7, %3, v253\n"
    "v_mov_b32_e32 v84, 0\n"
    "v_mfma_f32_16x16x32_bf16 v[52:55], v[148:151], a[40:43], v[52:55]\n"
    "buffer_load_dwordx4 v[120:123], v253, s[8:11], s47 offen offset:0 sc1\n"
    "v_mfma_f32_16x16x32_bf16 v[64:67], v[144:147], a[72:75], v[64:67]\n"
    "s_mul_i32 s88, %2, 5120\n"
    "s_add_u32 s88, s88, %3\n"
    "v_lshrrev_b32_e32 v250, 2, v253\n"
    "v_mfma_f32_16x16x32_bf16 v[68:71], v[148:151], a[72:75], v[68:71]\n"
    "s_waitcnt vmcnt(9)\n"
    "v_add_u32_e32 v249, s88, v250\n"
    "s_mov_b32 s49, s88\n"
    "v_mov_b32_e32 v85, 0\n"
    "v_mfma_f32_16x16x32_bf16 v[24:27], v[160:163], a[12:15], v[24:27]\n"
    "buffer_load_dwordx4 v[124:127], v253, s[8:11], s47 offen offset:1024 sc1\n"
    "v_mfma_f32_16x16x32_bf16 v[28:31], v[164:167], a[12:15], v[28:31]\n"
    "s_lshl_b32 s81, s88, 2\n"
    "s_lshl2_add_u32 s90, %1, %2\n"
    "s_lshl_b32 s90, s90, 10\n"
    "v_mfma_f32_16x16x32_bf16 v[48:51], v[160:163], a[44:47], v[48:51]\n"
    "s_sub_u32 s81, s90, s81\n"
    "s_add_u32 s81, s81, 36287488\n"
    "s_xor_b32 s88, %2, 2\n"
    "v_mov_b32_e32 v86, 0\n"
    "v_mfma_f32_16x16x32_bf16 v[52:55], v[164:167], a[44:47], v[52:55]\n"
    "buffer_load_dwordx4 v[136:139], v253, s[8:11], s47 offen offset:2048 sc1\n"
    "v_mfma_f32_16x16x32_bf16 v[64:67], v[160:163], a[76:79], v[64:67]\n"
    "s_add_u32 s61, s61, 0x1000\n"
    "s_mul_i32 s88, s88, 5120\n"
    "s_add_u32 s88, s88, %3\n"
    "v_mfma_f32_16x16x32_bf16 v[68:71], v[164:167], a[76:79], v[68:71]\n"
    "s_waitcnt vmcnt(9)\n"
    "v_add_u32_e32 v250, s88, v250\n"
    "v_and_b32_e32 v251, 0x1f0, v253\n"
    "v_mov_b32_e32 v87, 0\n"
    "v_mfma_f32_16x16x32_bf16 v[24:27], v[176:179], a[16:19], v[24:27]\n"
    "buffer_load_dwordx4 v[140:143], v253, s[8:11], s47 offen offset:3072 sc1\n"
    "v_mfma_f32_16x16x32_bf16 v[28:31], v[180:183], a[16:19], v[28:31]\n"
    "s_bitset0_b32 s61, 14\n"
    "s_lshl_b32 s82, %2, 10\n"
    "s_add_u32 s82, s82, %3\n"
    "v_mfma_f32_16x16x32_bf16 v[48:51], v[176:179], a[48:51], v[48:51]\n"
    "s_add_u32 s47, s52, s61\n"
    "s_add_u32 s82, s82, 20480\n"
    "v_lshrrev_b32_e32 v252, 9, v253\n"
    "v_mov_b32_e32 v88, 0\n"
    "v_mfma_f32_16x16x32_bf16 v[52:55], v[180:183], a[48:51], v[52:55]\n"
    "buffer_load_dwordx4 v[152:155], v253, s[8:11], s47 offen offset:0 sc1\n"
    "v_mfma_f32_16x16x32_bf16 v[64:67], v[176:179], a[80:83], v[64:67]\n"
    "buffer_load_dword v0, v3, s[8:11], s59 offen offset:0 sc1\n"
    "v_mfma_f32_16x16x32_bf16 v[68:71], v[180:183], a[80:83], v[68:71]\n"
    "s_waitcnt vmcnt(10)\n"
    "s_lshr_b32 s80, %1, 4\n"
    "v_lshl_add_u32 v252, v252, 4, s82\n"
    "v_mov_b32_e32 v89, 0\n"
    "v_mfma_f32_16x16x32_bf16 v[24:27], v[192:195], a[20:23], v[24:27]\n"
    "buffer_load_dwordx4 v[156:159], v253, s[8:11], s47 offen offset:1024 sc1\n"
    "v_mfma_f32_16x16x32_bf16 v[28:31], v[196:199], a[20:23], v[28:31]\n"
    "s_lshl_b32 s80, s80, 7\n"
    "s_add_u32 s80, s80, 5693696\n"
    "s_lshr_b32 s88, s34, 9\n"
    "v_mfma_f32_16x16x32_bf16 v[48:51], v[192:195], a[52:55], v[48:51]\n"
    "s_add_u32 s80, s80, s88\n"
    "s_mov_b64 s[20:21], s[8:9]\n"
    "s_mov_b32 s22, 5242880\n"
    "v_mov_b32_e32 v90, 0\n"
    "v_mfma_f32_16x16x32_bf16 v[52:55], v[196:199], a[52:55], v[52:55]\n"
    "buffer_load_dwordx4 v[168:171], v253, s[8:11], s47 offen offset:2048 sc1\n"
    "v_mfma_f32_16x16x32_bf16 v[64:67], v[192:195], a[84:87], v[64:67]\n"
    "s_add_u32 s61, s61, 0x1000\n"
    "s_add_u32 s24, s8, 5697536\n"
    "s_addc_u32 s25, s9, 0\n"
    "v_mfma_f32_16x16x32_bf16 v[68:71], v[196:199], a[84:87], v[68:71]\n"
    "s_waitcnt vmcnt(10)\n"
    "s_mov_b32 s26, 5242880\n"
    "v_mov_b32_e32 v91, 0\n"
    "v_mfma_f32_16x16x32_bf16 v[24:27], v[208:211], a[24:27], v[24:27]\n"
    "buffer_load_dwordx4 v[172:175], v253, s[8:11], s47 offen offset:3072 sc1\n"
    "v_mfma_f32_16x16x32_bf16 v[28:31], v[212:215], a[24:27], v[28:31]\n"
    "s_bitset0_b32 s61, 14\n"
    "s_movk_i32 s42, 512\n"
    "s_mov_b32 s37, 0\n"
    "v_mfma_f32_16x16x32_bf16 v[48:51], v[208:211], a[56:59], v[48:51]\n"
    "s_add_u32 s47, s52, s61\n"
    "s_add_u32 s38, s38, s50\n"
    "v_bfe_u32 v16, v253, 4, 2\n"
    "v_mov_b32_e32 v92, 0\n"
    "v_mfma_f32_16x16x32_bf16 v[52:55], v[212:215], a[56:59], v[52:55]\n"
    "buffer_load_dwordx4 v[184:187], v253, s[8:11], s47 offen offset:0 sc1\n"
    "v_mfma_f32_16x16x32_bf16 v[64:67], v[208:211], a[88:91], v[64:67]\n"
    "v_bfe_u32 v17, v253, 6, 2\n"
    "v_mov_b32_e32 v18, 0x3f80\n"
    "v_mov_b32_e32 v93, 0\n"
    "v_mfma_f32_16x16x32_bf16 v[68:71], v[212:215], a[88:91], v[68:71]\n"
    "s_waitcnt vmcnt(10)\n"
    "v_cmp_eq_u32_e32 vcc, 3, v16\n"
    "s_nop 1\n"
    "v_cndmask_b32_e64 v18, v18, 1.0, vcc\n"
    "v_cndmask_b32_e64 v16, v16, 0, vcc\n"
    "v_mfma_f32_16x16x32_bf16 v[24:27], v[224:227], a[28:31], v[24:27]\n"
    "buffer_load_dwordx4 v[188:191], v253, s[8:11], s47 offen offset:1024 sc1\n"
    "v_mfma_f32_16x16x32_bf16 v[28:31], v[228:231], a[28:31], v[28:31]\n"
    "v_or_b32_e32 v16, 12, v16\n"
    "v_mul_u32_u24_e32 v16, 0x1111, v16\n"
    "v_mov_b32_e32 v94, 0\n"
    "v_mfma_f32_16x16x32_bf16 v[48:51], v[224:227], a[60:63], v[48:51]\n"
    "v_cmp_eq_u32_e32 vcc, 0, v17\n"
    "s_nop 1\n"
    "v_cndmask_b32_e32 v19, 0, v18, vcc\n"
    "v_mfma_f32_16x16x32_bf16 v[52:55], v[228:231], a[60:63], v[52:55]\n"
    "buffer_load_dwordx4 v[200:203], v253, s[8:11], s47 offen offset:2048 sc1\n"
    "v_mfma_f32_16x16x32_bf16 v[64:67], v[224:227], a[92:95], v[64:67]\n"
    "s_add_u32 s61, s61, 0x1000\n"
    "v_cvt_pk_bf16_f32 v24, v24, v25\n"
    "v_accvgpr_write_b32 a192, v19\n"
    "v_mov_b32_e32 v95, 0\n"
    "v_mfma_f32_16x16x32_bf16 v[68:71], v[228:231], a[92:95], v[68:71]\n"
    "s_waitcnt vmcnt(10)\n"
    "v_cvt_pk_bf16_f32 v25, v26, v27\n"
    "v_cmp_eq_u32_e32 vcc, 1, v17\n"
    "s_nop 1\n"
    "v_cndmask_b32_e32 v19, 0, v18, vcc\n"
    "v_mfma_f32_16x16x32_bf16 v[32:35], v[120:123], a[0:3], 0\n"
    "buffer_load_dwordx4 v[204:207], v253, s[8:11], s47 offen offset:3072 sc1\n"
    "v_mfma_f32_16x16x32_bf16 v[36:39], v[124:127], a[0:3], 0\n"
    "s_bitset0_b32 s61, 14\n"
    "v_cvt_pk_bf16_f32 v26, v28, v29\n"
    "v_accvgpr_write_b32 a193, v19\n"
    "v_mov_b32_e32 v96, 0\n"
    "v_mfma_f32_16x16x32_bf16 v[56:59], v[120:123], a[32:35], 0\n"
    "s_add_u32 s47, s52, s61\n"
    "v_cvt_pk_bf16_f32 v27, v30, v31\n"
    "v_cmp_eq_u32_e32 vcc, 2, v17\n"
    "s_nop 1\n"
    "v_cndmask_b32_e32 v19, 0, v18, vcc\n"
    "v_mfma_f32_16x16x32_bf16 v[60:63], v[124:127], a[32:35], 0\n"
    "buffer_load_dwordx4 v[216:219], v253, s[8:11], s47 offen offset:0 sc1\n"
    "v_mfma_f32_16x16x32_bf16 v[72:75], v[120:123], a[64:67], 0\n"
    "buffer_store_dwordx4 v[24:27], v8, s[8:11], s35 offen offset:0 sc1\n"
    "v_cvt_pk_bf16_f32 v48, v48, v49\n"
    "v_accvgpr_write_b32 a194, v19\n"
    "v_mov_b32_e32 v97, 0\n"
    "v_mfma_f32_16x16x32_bf16 v[76:79], v[124:127], a[64:67], 0\n"
    "s_waitcnt vmcnt(11)\n"
    "v_cvt_pk_bf16_f32 v49, v50, v51\n"
    "v_cmp_eq_u32_e32 vcc, 3, v17\n"
    "s_nop 1\n"
    "v_cndmask_b32_e32 v19, 0, v18, vcc\n"
    "v_mfma_f32_16x16x32_bf16 v[32:35], v[136:139], a[4:7], v[32:35]\n"
    "buffer_load_dwordx4 v[220:223], v253, s[8:11], s47 offen offset:1024 sc1\n"
    "v_mfma_f32_16x16x32_bf16 v[36:39], v[140:143], a[4:7], v[36:39]\n"
    "s_add_u32 s52, s52, 0x4000\n"
    "v_cvt_pk_bf16_f32 v50, v52, v53\n"
    "v_accvgpr_write_b32 a195, v19\n"
    "v_mfma_f32_16x16x32_bf16 v[56:59], v[136:139], a[36:39], v[56:59]\n"
    "s_add_u32 s28, s38, 2\n"
    "s_sub_u32 s88, s28, 10\n"
    "v_cvt_pk_bf16_f32 v51, v54, v55\n"
    "v_mov_b32_e32 v98, 0\n"
    "v_mfma_f32_16x16x32_bf16 v[60:63], v[140:143], a[36:39], v[60:63]\n"
    "s_waitcnt vmcnt(10)\n"
    "buffer_load_dwordx4 v[232:235], v253, s[8:11], s47 offen offset:2048 sc1\n"
    "buffer_store_dwordx4 v[48:51], v8, s[8:11], s54 offen offset:0 sc1\n"
    "v_cmp_ne_u32_e32 vcc, s39, v0\n"
    "v_mfma_f32_16x16x32_bf16 v[72:75], v[136:139], a[68:71], v[72:75]\n"
    "s_add_u32 s61, s61, 0x1000\n"
    "s_min_u32 s28, s28, s88\n"
    "v_cvt_pk_bf16_f32 v64, v64, v65\n"
    "v_mfma_f32_16x16x32_bf16 v[76:79], v[140:143], a[68:71], v[76:79]\n"
    "s_waitcnt vmcnt(11)\n"
    "s_lshl_b32 s88, s28, 0\n"
    "v_cvt_pk_bf16_f32 v65, v66, v67\n"
    "v_mov_b32_e32 v99, 0\n"
    "v_mfma_f32_16x16x32_bf16 v[32:35], v[152:155], a[8:11], v[32:35]\n"
    "buffer_load_dwordx4 v[236:239], v253, s[8:11], s47 offen offset:3072 sc1\n"
    "v_mfma_f32_16x16x32_bf16 v[36:39], v[156:159], a[8:11], v[36:39]\n"
    "s_bitset0_b32 s61, 14\n"
    "s_cbranch_vccnz .Lat_h1poll\n"
    ".Lat_h1poll_ret:\n"
    "v_mfma_f32_16x16x32_bf16 v[56:59], v[152:155], a[40:43], v[56:59]\n"
    "s_mov_b32 m0, s63\n"
    "v_cvt_pk_bf16_f32 v66, v68, v69\n"
    "v_cvt_pk_bf16_f32 v67, v70, v71\n"
    "v_mfma_f32_16x16x32_bf16 v[60:63], v[156:159], a[40:43], v[60:63]\n"
    "buffer_load_dwordx4 v254, s[8:11], s57 offen offset:0 sc1 lds\n"
    "buffer_store_dwordx4 v[64:67], v8, s[8:11], s60 offen offset:0 sc1\n"
    "v_mov_b32_e32 v100, 0\n"
    "v_mfma_f32_16x16x32_bf16 v[72:75], v[152:155], a[72:75], v[72:75]\n"
    "s_sub_u32 m0, s63, 1024\n"
    "s_or_b32 s36, s88, s88\n"
    "s_lshl1_add_u32 s88, s28, 1\n"
    "v_mfma_f32_16x16x32_bf16 v[76:79], v[156:159], a[72:75], v[76:79]\n"
    "s_waitcnt vmcnt(12)\n"
    "buffer_load_dwordx4 v254, s[8:11], s57 offen offset:2048 sc1 lds\n"
    "v_mfma_f32_16x16x32_bf16 v[32:35], v[168:171], a[12:15], v[32:35]\n"
    "s_add_u32 m0, s63, 3968\n"
    "s_mul_i32 s88, s88, 52\n"
    "s_lshr_b32 s88, s88, 8\n"
    "v_mfma_f32_16x16x32_bf16 v[36:39], v[172:175], a[12:15], v[36:39]\n"
    "buffer_load_dwordx4 v254, s[8:11], s57 offen offset:128 sc1 lds\n"
    "v_mfma_f32_16x16x32_bf16 v[56:59], v[168:171], a[44:47], v[56:59]\n"
    "s_add_u32 m0, s63, 2944\n"
    "ds_write_b128 v9, v[24:27] offset:0\n"
    "v_mfma_f32_16x16x32_bf16 v[60:63], v[172:175], a[44:47], v[60:63]\n"
    "buffer_load_dwordx4 v254, s[8:11], s57 offen offset:2176 sc1 lds\n"
    "v_mfma_f32_16x16x32_bf16 v[72:75], v[168:171], a[76:79], v[72:75]\n"
    "s_add_u32 m0, s62, 0\n"
    "s_mul_i32 s90, s88, 5\n"
    "s_lshr_b32 s90, s90, 1\n"
    "v_mfma_f32_16x16x32_bf16 v[76:79], v[172:175], a[76:79], v[76:79]\n"
    "s_waitcnt vmcnt(13)\n"
    "buffer_load_dwordx4 v12, s[8:11], s40 offen offset:0 sc1 lds\n"
    "v_mfma_f32_16x16x32_bf16 v[32:35], v[184:187], a[16:19], v[32:35]\n"
    "s_sub_u32 s28, s28, s90\n"
    "s_sub_u32 s90, s32, s88\n"
    "s_sub_u32 s90, s90, 1\n"
    "v_mfma_f32_16x16x32_bf16 v[36:39], v[188:191], a[16:19], v[36:39]\n"
    "buffer_load_dwordx4 v13, s[8:11], s40 offen offset:1024 sc1 lds\n"
    "v_mfma_f32_16x16x32_bf16 v[56:59], v[184:187], a[48:51], v[56:59]\n"
    "s_and_b32 s90, s90, 3\n"
    "s_lshl2_add_u32 s88, s33, s88\n"
    "s_mul_i32 s88, s88, 3\n"
    "v_mfma_f32_16x16x32_bf16 v[60:63], v[188:191], a[48:51], v[60:63]\n"
    "buffer_load_dwordx4 v14, s[8:11], s40 offen offset:2048 sc1 lds\n"
    "v_mfma_f32_16x16x32_bf16 v[72:75], v[184:187], a[80:83], v[72:75]\n"
    "s_add_u32 s88, s88, s90\n"
    "ds_write_b128 v9, v[48:51] offset:8192\n"
    "v_mfma_f32_16x16x32_bf16 v[76:79], v[188:191], a[80:83], v[76:79]\n"
    "s_waitcnt vmcnt(14)\n"
    "buffer_load_dwordx4 v15, s[8:11], s40 offen offset:3072 sc1 lds\n"
    "v_mfma_f32_16x16x32_bf16 v[32:35], v[200:203], a[20:23], v[32:35]\n"
    "s_add_u32 s40, s40, 0x8000\n"
    "s_add_u32 m0, s62, 4096\n"
    "v_mov_b32_e32 v101, 0\n"
    "v_mfma_f32_16x16x32_bf16 v[36:39], v[204:207], a[20:23], v[36:39]\n"
    "buffer_load_dwordx4 v12, s[8:11], s41 offen offset:0 sc1 lds\n"
    "v_mfma_f32_16x16x32_bf16 v[56:59], v[200:203], a[52:55], v[56:59]\n"
    "s_mul_i32 s88, s88, 24832\n"
    "s_add_u32 s88, s88, 16183296\n"
    "v_mov_b32_e32 v102, 0\n"
    "v_mfma_f32_16x16x32_bf16 v[60:63], v[204:207], a[52:55], v[60:63]\n"
    "buffer_load_dwordx4 v13, s[8:11], s41 offen offset:1024 sc1 lds\n"
    "v_mfma_f32_16x16x32_bf16 v[72:75], v[200:203], a[84:87], v[72:75]\n"
    "s_cmp_eq_u32 s90, 3\n"
    "s_cselect_b32 s88, 2.0, s88\n"
    "s_cselect_b32 s89, 2, 3\n"
    "v_mfma_f32_16x16x32_bf16 v[76:79], v[204:207], a[84:87], v[76:79]\n"
    "s_waitcnt vmcnt(14)\n"
    "buffer_load_dwordx4 v14, s[8:11], s41 offen offset:2048 sc1 lds\n"
    "v_mfma_f32_16x16x32_bf16 v[32:35], v[216:219], a[24:27], v[32:35]\n"
    "s_lshl1_add_u32 s83, s28, s51\n"
    "s_lshl_b32 s83, s83, 12\n"
    "s_add_u32 s83, s83, s88\n"
    "v_mfma_f32_16x16x32_bf16 v[36:39], v[220:223], a[24:27], v[36:39]\n"
    "buffer_load_dwordx4 v15, s[8:11], s41 offen offset:3072 sc1 lds\n"
    "v_mfma_f32_16x16x32_bf16 v[56:59], v[216:219], a[56:59], v[56:59]\n"
    "s_add_u32 s41, s41, 0x8000\n"
    "ds_write_b128 v9, v[64:67] offset:16384\n"
    "v_mfma_f32_16x16x32_bf16 v[60:63], v[220:223], a[56:59], v[60:63]\n"
    "s_lshl2_add_u32 s28, s28, s88\n"
    "s_add_u32 s29, s38, 4\n"
    "v_mov_b32_e32 v103, 0\n"
    "v_mfma_f32_16x16x32_bf16 v[72:75], v[216:219], a[88:91], v[72:75]\n"
    "s_sub_u32 s88, s29, 10\n"
    "s_min_u32 s29, s29, s88\n"
    "v_mov_b32_e32 v104, 0\n"
    "v_mfma_f32_16x16x32_bf16 v[76:79], v[220:223], a[88:91], v[76:79]\n"
    "s_waitcnt vmcnt(13)\n"
    "s_lshl_b32 s88, s29, 4\n"
    "v_mov_b32_e32 v105, 0\n"
    "v_mfma_f32_16x16x32_bf16 v[32:35], v[232:235], a[28:31], v[32:35]\n"
    "s_or_b32 s36, s88, s36\n"
    "s_lshl1_add_u32 s88, s29, 1\n"
    "v_mov_b32_e32 v106, 0\n"
    "v_mfma_f32_16x16x32_bf16 v[36:39], v[236:239], a[28:31], v[36:39]\n"
    "s_mul_i32 s88, s88, 52\n"
    "s_lshr_b32 s88, s88, 8\n"
    "v_mov_b32_e32 v107, 0\n"
    "v_mfma_f32_16x16x32_bf16 v[56:59], v[232:235], a[60:63], v[56:59]\n"
    "s_mul_i32 s90, s88, 5\n"
    "s_lshr_b32 s90, s90, 1\n"
    "v_mov_b32_e32 v108, 0\n"
    "v_mfma_f32_16x16x32_bf16 v[60:63], v[236:239], a[60:63], v[60:63]\n"
    "s_sub_u32 s29, s29, s90\n"
    "s_sub_u32 s90, s32, s88\n"
    "v_mov_b32_e32 v109, 0\n"
    "v_mfma_f32_16x16x32_bf16 v[72:75], v[232:235], a[92:95], v[72:75]\n"
    "s_sub_u32 s90, s90, 1\n"
    "s_and_b32 s90, s90, 3\n"
    "v_mov_b32_e32 v110, 0\n"
    "v_mfma_f32_16x16x32_bf16 v[76:79], v[236:239], a[92:95], v[76:79]\n"
    "s_lshl2_add_u32 s88, s33, s88\n"
    "s_mul_i32 s88, s88, 3\n"
    "v_mov_b32_e32 v111, 0\n"
    "v_cvt_pk_bf16_f32 v32, v32, v33\n"
    "v_cvt_pk_bf16_f32 v33, v34, v35\n"
    "v_cvt_pk_bf16_f32 v34, v36, v37\n"
    "v_cvt_pk_bf16_f32 v35, v38, v39\n"
    "v_or_b32_e32 v35, v10, v35\n"
    "ds_write_b128 v9, v[32:35] offset:1024\n"
    "buffer_store_dwordx4 v[32:35], v8, s[8:11], s35 offen offset:1024 sc1\n"
    "v_cvt_pk_bf16_f32 v56, v56, v57\n"
    "v_cvt_pk_bf16_f32 v57, v58, v59\n"
    "v_cvt_pk_bf16_f32 v58, v60, v61\n"
    "v_cvt_pk_bf16_f32 v59, v62, v63\n"
    "v_or_b32_e32 v59, v10, v59\n"
    "ds_write_b128 v9, v[56:59] offset:9216\n"
    "buffer_store_dwordx4 v[56:59], v8, s[8:11], s54 offen offset:1024 sc1\n"
    "v_cvt_pk_bf16_f32 v72, v72, v73\n"
    "v_cvt_pk_bf16_f32 v73, v74, v75\n"
    "v_cvt_pk_bf16_f32 v74, v76, v77\n"
    "v_cvt_pk_bf16_f32 v75, v78, v79\n"
    "v_or_b32_e32 v75, v10, v75\n"
    "ds_write_b128 v9, v[72:75] offset:17408\n"
    "buffer_store_dwordx4 v[72:75], v8, s[8:11], s60 offen offset:1024 sc1\n"
    "v_mov_b32_e32 v112, 0\n"
    "v_mov_b32_e32 v113, 0\n"
    "v_mov_b32_e32 v114, 0\n"
    "v_mov_b32_e32 v115, 0\n"
    "s_add_u32 m0, s62, 16384\n"
    "v_mov_b32_e32 v116, 0\n"
    "v_mov_b32_e32 v117, 0\n"
    "v_mov_b32_e32 v118, 0\n"
    "v_mov_b32_e32 v119, 0\n"
    "buffer_load_dwordx4 v12, s[8:11], s40 offen offset:0 sc1 lds\n"
    "v_mov_b32_e32 v120, 0\n"
    "v_mov_b32_e32 v121, 0\n"
    "v_mov_b32_e32 v122, 0\n"
    "v_mov_b32_e32 v123, 0\n"
    "buffer_load_dwordx4 v13, s[8:11], s40 offen offset:1024 sc1 lds\n"
    "v_mov_b32_e32 v124, 0\n"
    "v_mov_b32_e32 v125, 0\n"
    "v_mov_b32_e32 v126, 0\n"
    "v_mov_b32_e32 v127, 0\n"
    "buffer_load_dwordx4 v14, s[8:11], s40 offen offset:2048 sc1 lds\n"
    "v_mov_b32_e32 v128, 0\n"
    "v_mov_b32_e32 v129, 0\n"
    "v_mov_b32_e32 v130, 0\n"
    "v_mov_b32_e32 v131, 0\n"
    "buffer_load_dwordx4 v15, s[8:11], s40 offen offset:3072 sc1 lds\n"
    "v_mov_b32_e32 v132, 0\n"
    "v_mov_b32_e32 v133, 0\n"
    "v_mov_b32_e32 v134, 0\n"
    "v_mov_b32_e32 v135, 0\n"
    "s_add_u32 s40, s40, 0x8000\n"
    "v_mov_b32_e32 v136, 0\n"
    "v_mov_b32_e32 v137, 0\n"
    "v_mov_b32_e32 v138, 0\n"
    "v_mov_b32_e32 v139, 0\n"
    "s_add_u32 m0, s62, 20480\n"
    "v_mov_b32_e32 v140, 0\n"
    "v_mov_b32_e32 v141, 0\n"
    "v_mov_b32_e32 v142, 0\n"
    "v_mov_b32_e32 v143, 0\n"
    "buffer_load_dwordx4 v12, s[8:11], s41 offen offset:0 sc1 lds\n"
    "v_mov_b32_e32 v144, 0\n"
    "v_mov_b32_e32 v145, 0\n"
    "v_mov_b32_e32 v146, 0\n"
    "v_mov_b32_e32 v147, 0\n"
    "buffer_load_dwordx4 v13, s[8:11], s41 offen offset:1024 sc1 lds\n"
    "v_mov_b32_e32 v148, 0\n"
    "v_mov_b32_e32 v149, 0\n"
    "v_mov_b32_e32 v150, 0\n"
    "v_mov_b32_e32 v151, 0\n"
    "buffer_load_dwordx4 v14, s[8:11], s41 offen offset:2048 sc1 lds\n"
    "s_xor_b32 s45, s34, 0xc000\n"
    "v_add_u32_e32 v204, s45, v240\n"
    "v_add_u32_e32 v205, s45, v241\n"
    "v_add_u32_e32 v206, s45, v242\n"
    "buffer_load_dwordx4 v15, s[8:11], s41 offen offset:3072 sc1 lds\n"
    "v_add_u32_e32 v207, s45, v243\n"
    "v_add_u32_e32 v208, s45, v244\n"
    "v_add_u32_e32 v209, s45, v245\n"
    "v_add_u32_e32 v210, s45, v246\n"
    "s_add_u32 s41, s41, 0x8000\n"
    "buffer_load_dword v0, v253, s[8:11], s59 offen offset:0 sc1\n"
    "buffer_load_dword v1, v2, s[8:11], s59 offen offset:0 sc1\n"
    "v_add_u32_e32 v211, s45, v247\n"
    "v_add_u32_e32 v200, s34, v240\n"
    "v_add_u32_e32 v201, s34, v241\n"
    "v_add_u32_e32 v202, s34, v242\n"
    "v_add_u32_e32 v203, s34, v243\n"
    "v_mov_b32_e32 v152, v16\n"
    "v_and_b32_e32 v153, 0xf0, v253\n"
    "v_lshrrev_b32_e32 v154, 9, v253\n"
    "v_lshl_add_u32 v153, v154, 8, v153\n"
    "v_bfe_u32 v154, v253, 8, 1\n"
    "v_lshl_add_u32 v153, v154, 3, v153\n"
    "v_mov_b32_e32 v156, 0\n"
    "v_mov_b32_e32 v157, 0\n"
    "v_mov_b32_e32 v158, 0\n"
    "v_mov_b32_e32 v159, 0\n"
    "v_mov_b32_e32 v56, 0\n"
    "v_mov_b32_e32 v57, 0\n"
    "v_mov_b32_e32 v58, 0\n"
    "v_mov_b32_e32 v59, 0\n"
    "v_mov_b32_e32 v60, 0\n"
    "v_mov_b32_e32 v61, 0\n"
    "v_mov_b32_e32 v62, 0\n"
    "v_mov_b32_e32 v63, 0\n"
    "v_mov_b32_e32 v64, 0\n"
    "v_mov_b32_e32 v65, 0\n"
    "v_mov_b32_e32 v66, 0\n"
    "v_mov_b32_e32 v67, 0\n"
    "v_mov_b32_e32 v68, 0\n"
    "v_mov_b32_e32 v69, 0\n"
    "v_mov_b32_e32 v70, 0\n"
    "v_mov_b32_e32 v71, 0\n"
    "v_mov_b32_e32 v72, 0\n"
    "v_mov_b32_e32 v73, 0\n"
    "v_mov_b32_e32 v74, 0\n"
    "v_mov_b32_e32 v75, 0\n"
    "v_mov_b32_e32 v76, 0\n"
    "v_mov_b32_e32 v77, 0\n"
    "v_mov_b32_e32 v78, 0\n"
    "v_mov_b32_e32 v79, 0\n"
    "s_add_u32 s88, s88, s90\n"
    "s_mul_i32 s88, s88, 24832\n"
    "s_add_u32 s88, s88, 16183296\n"
    "s_lshl1_add_u32 s84, s29, s51\n"
    "s_lshl_b32 s84, s84, 12\n"
    "s_add_u32 s84, s84, s88\n"
    "s_lshl2_add_u32 s29, s29, s88\n"
    "s_add_u32 s30, s38, 6\n"
    "s_sub_u32 s88, s30, 10\n"
    "s_min_u32 s30, s30, s88\n"
    "s_lshl_b32 s88, s30, 8\n"
    "s_or_b32 s36, s88, s36\n"
    "s_lshl1_add_u32 s88, s30, 1\n"
    "s_mul_i32 s88, s88, 52\n"
    "s_lshr_b32 s88, s88, 8\n"
    "s_mul_i32 s90, s88, 5\n"
    "s_lshr_b32 s90, s90, 1\n"
    "s_sub_u32 s30, s30, s90\n"
    "s_sub_u32 s90, s32, s88\n"
    "s_sub_u32 s90, s90, 1\n"
    "s_and_b32 s90, s90, 3\n"
    "s_lshl2_add_u32 s88, s33, s88\n"
    "s_mul_i32 s88, s88, 3\n"
    "s_add_u32 s88, s88, s90\n"
    "s_mul_i32 s88, s88, 24832\n"
    "s_add_u32 s88, s88, 16183296\n"
    "s_lshl1_add_u32 s85, s30, s51\n"
    "s_lshl_b32 s85, s85, 12\n"
    "s_add_u32 s85, s85, s88\n"
    "s_lshl2_add_u32 s30, s30, s88\n"
    "s_add_u32 s31, s38, 8\n"
    "s_sub_u32 s88, s31, 10\n"
    "s_min_u32 s31, s31, s88\n"
    "s_lshl_b32 s88, s31, 12\n"
    "s_or_b32 s36, s88, s36\n"
    "s_lshl1_add_u32 s88, s31, 1\n"
    "s_mul_i32 s88, s88, 52\n"
    "s_lshr_b32 s88, s88, 8\n"
    "s_mul_i32 s90, s88, 5\n"
    "s_lshr_b32 s90, s90, 1\n"
    "s_sub_u32 s31, s31, s90\n"
    "s_sub_u32 s90, s32, s88\n"
    "s_sub_u32 s90, s90, 1\n"
    "s_and_b32 s90, s90, 3\n"
    "s_lshl2_add_u32 s88, s33, s88\n"
    "s_mul_i32 s88, s88, 3\n"
    "s_add_u32 s88, s88, s90\n"
    "s_mul_i32 s88, s88, 24832\n"
    "s_add_u32 s88, s88, 16183296\n"
    "s_lshl1_add_u32 s86, s31, s51\n"
    "s_lshl_b32 s86, s86, 12\n"
    "s_add_u32 s86, s86, s88\n"
    "s_lshl2_add_u32 s31, s31, s88\n"
    "s_lshl4_add_u32 s87, %1, %2\n"
    "s_lshl2_add_u32 s87, s87, 5636096\n"
    "s_or_b32 s88, %1, %2\n"
    "s_cselect_b32 s88, 2.0, 5695744\n"
    "s_lshl_b32 s47, s33, 9\n"
    "s_add_u32 s47, s47, 5656640\n"
    "v_lshlrev_b32_e32 v9, 3, v253\n"
    "s_cmp_eq_u32 s50, 0\n"
    "s_cselect_b32 s46, s35, s54\n"
    "s_cselect_b32 s45, s60, 2.0\n"
    "s_cmp_eq_u32 s51, 0\n"
    "s_cselect_b32 s46, s46, s45\n"
    "s_add_u32 s46, s46, 10940416\n"
    "s_waitcnt vmcnt(10)\n"
    "s_waitcnt lgkmcnt(0)\n"
    "s_barrier\n"
    "buffer_store_dword v40, v41, s[8:11], s58 offen offset:64 sc1\n"
    "buffer_load_dword v228, v9, s[8:11], s46 offen offset:0 sc1\n"
    "ds_read_b128 a[0:3], v6 offset:0\n"
    "ds_read_b128 a[160:163], v204 offset:0\n"
    "ds_read_b128 a[4:7], v6 offset:1024\n"
    "ds_read_b128 a[164:167], v205 offset:0\n"
    "ds_read_b128 a[8:11], v6 offset:2048\n"
    "ds_read_b128 a[168:171], v204 offset:4096\n"
    "ds_read_b128 a[12:15], v6 offset:3072\n"
    "ds_read_b128 a[172:175], v205 offset:4096\n"
    "ds_read_b128 a[16:19], v6 offset:4096\n"
    "ds_read_b128 a[176:179], v206 offset:0\n"
    "ds_read_b128 a[20:23], v6 offset:5120\n"
    "ds_read_b128 a[180:183], v207 offset:0\n"
    "ds_read_b128 a[24:27], v6 offset:6144\n"
    "ds_read_b128 a[184:187], v206 offset:4096\n"
    "ds_read_b128 a[28:31], v6 offset:7168\n"
    "ds_read_b128 a[188:191], v207 offset:4096\n"
    "s_waitcnt lgkmcnt(8)\n"
    "v_mfma_f32_16x16x32_bf16 v[220:223], a[160:163], a[0:3], 0\n"
    "ds_read_b128 a[32:35], v7 offset:16384\n"
    "ds_read_b128 a[36:39], v7 offset:17408\n"
    "v_mfma_f32_16x16x32_bf16 v[220:223], a[164:167], a[4:7], v[220:223]\n"
    "ds_read_b128 a[40:43], v7 offset:18432\n"
    "ds_read_b128 a[44:47], v7 offset:19456\n"
    "v_mfma_f32_16x16x32_bf16 v[220:223], a[168:171], a[8:11], v[220:223]\n"
    "ds_read_b128 a[48:51], v7 offset:20480\n"
    "ds_read_b128 a[52:55], v7 offset:21504\n"
    "v_mfma_f32_16x16x32_bf16 v[220:223], a[172:175], a[12:15], v[220:223]\n"
    "ds_read_b128 a[56:59], v7 offset:22528\n"
    "ds_read_b128 a[60:63], v7 offset:23552\n"
    "s_waitcnt lgkmcnt(8)\n"
    "v_mfma_f32_16x16x32_bf16 v[220:223], a[176:179], a[16:19], v[220:223]\n"
    "ds_read_b64_tr_b16 a[232:233], v208 offset:0\n"
    "ds_read_b64_tr_b16 a[234:235], v209 offset:0\n"
    "v_mfma_f32_16x16x32_bf16 v[220:223], a[180:183], a[20:23], v[220:223]\n"
    "ds_read_b64_tr_b16 a[236:237], v210 offset:0\n"
    "ds_read_b64_tr_b16 a[238:239], v211 offset:0\n"
    "v_mfma_f32_16x16x32_bf16 v[220:223], a[184:187], a[24:27], v[220:223]\n"
    "ds_read_b64_tr_b16 a[240:241], v208 offset:4096\n"
    "ds_read_b64_tr_b16 a[242:243], v209 offset:4096\n"
    "v_mfma_f32_16x16x32_bf16 v[220:223], a[188:191], a[28:31], v[220:223]\n"
    "buffer_load_dword v11, v5, s[8:11], s47 offen offset:0 sc1\n"
    "ds_read_b64_tr_b16 a[244:245], v210 offset:4096\n"
    "s_waitcnt lgkmcnt(11)\n"
    "v_mfma_f32_16x16x32_bf16 v[224:227], a[160:163], a[32:35], 0\n"
    "ds_read_b64_tr_b16 a[246:247], v211 offset:4096\n"
    "ds_read_b128 a[200:203], v203 offset:6144\n"
    "v_mfma_f32_16x16x32_bf16 v[224:227], a[164:167], a[36:39], v[224:227]\n"
    "ds_read_b128 a[204:207], v200 offset:2048\n"
    "ds_read_b128 a[208:211], v201 offset:2048\n"
    "v_mfma_f32_16x16x32_bf16 v[224:227], a[168:171], a[40:43], v[224:227]\n"
    "ds_read_b128 a[212:215], v200 offset:6144\n"
    "ds_read_b128 a[216:219], v201 offset:6144\n"
    "v_mfma_f32_16x16x32_bf16 v[224:227], a[172:175], a[44:47], v[224:227]\n"
    "ds_read_b128 a[220:223], v202 offset:2048\n"
    "ds_read_b128 a[224:227], v203 offset:2048\n"
    "s_waitcnt lgkmcnt(15)\n"
    "v_mfma_f32_16x16x32_bf16 v[224:227], a[176:179], a[48:51], v[224:227]\n"
    "ds_read_b128 a[228:231], v202 offset:6144\n"
    "v_mfma_f32_16x16x32_bf16 v[224:227], a[180:183], a[52:55], v[224:227]\n"
    "v_mfma_f32_16x16x32_bf16 v[224:227], a[184:187], a[56:59], v[224:227]\n"
    "v_mfma_f32_16x16x32_bf16 v[224:227], a[188:191], a[60:63], v[224:227]\n"
    "s_andn2_b32 s61, s32, s50\n"
    "s_bfe_i32 s61, s61, 0x10000\n"
    "ds_read_b128 a[160:163], v203 offset:4096\n"
    "ds_read_b128 a[164:167], v200 offset:0\n"
    "ds_read_b128 a[168:171], v201 offset:0\n"
    "ds_read_b128 a[172:175], v200 offset:4096\n"
    "ds_read_b128 a[176:179], v201 offset:4096\n"
    "ds_read_b128 a[180:183], v202 offset:0\n"
    "ds_read_b128 a[184:187], v203 offset:0\n"
    "ds_read_b128 a[188:191], v202 offset:4096\n"
    "s_and_b32 s62, s63, 0xffffdfff\n"
    "s_sub_u32 s62, s62, 16384\n"
    "v_add_u32_e32 v216, s62, v253\n"
    "s_lshl_b32 s45, s51, 12\n"
    "s_add_u32 s62, s62, s45\n"
    "s_mul_i32 s48, s33, 81920\n"
    "s_add_u32 s48, s48, s45\n"
    "s_add_u32 s48, s48, 10940416\n"
    "v_exp_f32_e32 v220, v220\n"
    "v_exp_f32_e32 v221, v221\n"
    "v_exp_f32_e32 v222, v222\n"
    "v_exp_f32_e32 v223, v223\n"
    "v_exp_f32_e32 v224, v224\n"
    "v_exp_f32_e32 v225, v225\n"
    "v_exp_f32_e32 v226, v226\n"
    "v_exp_f32_e32 v227, v227\n"
    "v_cvt_pk_bf16_f32 v220, v220, v221\n"
    "v_cvt_pk_bf16_f32 v221, v222, v223\n"
    "v_cvt_pk_bf16_f32 v222, v224, v225\n"
    "v_cvt_pk_bf16_f32 v223, v226, v227\n"
    "v_and_b32_e32 v222, s61, v222\n"
    "v_and_b32_e32 v223, s61, v223\n"
    "s_waitcnt vmcnt(3)\n"
    "s_waitcnt lgkmcnt(0)\n"
    "s_barrier\n"
    "v_xor_b32_e32 v1, s39, v1\n"
    "v_xor_b32_e32 v0, s39, v0\n"
    "v_or_b32_e32 v0, v0, v1\n"
    "v_cmp_ne_u32_e32 vcc, 0, v0\n"
    "s_nop 3\n"
    "s_cbranch_vccnz .Lat_hpoll\n"
    ".Lat_hpoll_ret:\n"
    "s_waitcnt vmcnt(0)\n"
    "v_cmp_ne_u32_e32 vcc, s39, v11\n"
    "s_cmp_eq_u32 s50, 0\n"
    "s_nop 2\n"
    "s_cbranch_vccnz .Lat_qpoll\n"
    ".Lat_qpoll_ret:\n"
    "s_cmp_eq_u32 s50, 0\n"
    "s_cbranch_scc1 .Lat_qx\n"
    "s_bfe_u32 s45, s36, 0x40000\n"
    "s_add_u32 m0, s62, 0\n"
    "s_lshl_b32 s45, s45, 13\n"
    "s_add_u32 s45, s45, s48\n"
    "buffer_load_dwordx4 v253, s[8:11], s45 offen offset:0 sc1 lds\n"
    "buffer_load_dwordx4 v253, s[8:11], s45 offen offset:1024 sc1 lds\n"
    "buffer_load_dwordx4 v253, s[8:11], s45 offen offset:2048 sc1 lds\n"
    "buffer_load_dwordx4 v253, s[8:11], s45 offen offset:3072 sc1 lds\n"
    ".Lat_qx:\n"
    "s_bfe_u32 s45, s36, 0x40004\n"
    "s_add_u32 m0, s62, 8192\n"
    "s_lshl_b32 s45, s45, 13\n"
    "s_add_u32 s45, s45, s48\n"
    "buffer_load_dwordx4 v253, s[8:11], s45 offen offset:0 sc1 lds\n"
    "buffer_load_dwordx4 v253, s[8:11], s45 offen offset:1024 sc1 lds\n"
    "buffer_load_dwordx4 v253, s[8:11], s45 offen offset:2048 sc1 lds\n"
    "buffer_load_dwordx4 v253, s[8:11], s45 offen offset:3072 sc1 lds\n"
    "v_mfma_f32_16x16x16_bf16 a[248:251], a[232:233], v[220:221], 0\n"
    "v_mfma_f32_16x16x16_bf16 a[252:255], a[232:233], v[222:223], 0\n"
    "s_bfe_u32 s45, s36, 0x40008\n"
    "s_add_u32 m0, s62, 16384\n"
    "s_lshl_b32 s45, s45, 13\n"
    "s_add_u32 s45, s45, s48\n"
    "v_mfma_f32_16x16x16_bf16 v[16:19], a[238:239], v[220:221], 0\n"
    "buffer_load_dwordx4 v253, s[8:11], s45 offen offset:0 sc1 lds\n"
    "v_mfma_f32_16x16x16_bf16 v[20:23], a[238:239], v[222:223], 0\n"
    "v_mfma_f32_16x16x16_bf16 v[24:27], a[240:241], v[220:221], 0\n"
    "buffer_load_dwordx4 v253, s[8:11], s45 offen offset:1024 sc1 lds\n"
    "v_mfma_f32_16x16x16_bf16 v[28:31], a[240:241], v[222:223], 0\n"
    "v_mfma_f32_16x16x16_bf16 v[32:35], a[242:243], v[220:221], 0\n"
    "buffer_load_dwordx4 v253, s[8:11], s45 offen offset:2048 sc1 lds\n"
    "v_mfma_f32_16x16x16_bf16 v[36:39], a[242:243], v[222:223], 0\n"
    "v_mfma_f32_16x16x16_bf16 v[40:43], a[244:245], v[220:221], 0\n"
    "buffer_load_dwordx4 v253, s[8:11], s45 offen offset:3072 sc1 lds\n"
    "v_mfma_f32_16x16x16_bf16 v[44:47], a[244:245], v[222:223], 0\n"
    "s_bfe_u32 s45, s36, 0x4000c\n"
    "s_add_u32 m0, s62, 24576\n"
    "s_lshl_b32 s45, s45, 13\n"
    "s_add_u32 s45, s45, s48\n"
    "v_mfma_f32_16x16x16_bf16 v[48:51], a[246:247], v[220:221], 0\n"
    "buffer_load_dwordx4 v253, s[8:11], s45 offen offset:0 sc1 lds\n"
    "v_mfma_f32_16x16x16_bf16 v[52:55], a[246:247], v[222:223], 0\n"
    "v_mfma_f32_16x16x16_bf16 v[0:3], a[234:235], v[220:221], 0\n"
    "buffer_load_dwordx4 v253, s[8:11], s45 offen offset:1024 sc1 lds\n"
    "v_mfma_f32_16x16x16_bf16 v[4:7], a[234:235], v[222:223], 0\n"
    "v_mfma_f32_16x16x16_bf16 v[8:11], a[236:237], v[220:221], 0\n"
    "buffer_load_dwordx4 v253, s[8:11], s45 offen offset:2048 sc1 lds\n"
    "v_mfma_f32_16x16x16_bf16 v[12:15], a[236:237], v[222:223], 0\n"
    "v_mfma_f32_16x16x32_bf16 v[160:163], a[160:163], a[28:31], 0\n"
    "buffer_load_dwordx4 v253, s[8:11], s45 offen offset:3072 sc1 lds\n"
    "v_mfma_f32_16x16x32_bf16 v[160:163], a[164:167], a[0:3], v[160:163]\n"
    "s_mov_b32 m0, s49\n"
    "v_mfma_f32_16x16x32_bf16 v[160:163], a[168:171], a[4:7], v[160:163]\n"
    "v_mfma_f32_16x16x32_bf16 v[160:163], a[172:175], a[8:11], v[160:163]\n"
    "v_mfma_f32_16x16x32_bf16 v[160:163], a[176:179], a[12:15], v[160:163]\n"
    "v_mfma_f32_16x16x32_bf16 v[160:163], a[180:183], a[16:19], v[160:163]\n"
    "v_mfma_f32_16x16x32_bf16 v[160:163], a[184:187], a[20:23], v[160:163]\n"
    "v_mfma_f32_16x16x32_bf16 v[160:163], a[188:191], a[24:27], v[160:163]\n"
    "v_mfma_f32_16x16x32_bf16 v[164:167], a[200:203], a[28:31], 0\n"
    "v_mfma_f32_16x16x32_bf16 v[164:167], a[204:207], a[0:3], v[164:167]\n"
    "v_mfma_f32_16x16x32_bf16 v[164:167], a[208:211], a[4:7], v[164:167]\n"
    "v_exp_f32_e32 v160, v160\n"
    "v_mfma_f32_16x16x32_bf16 v[164:167], a[212:215], a[8:11], v[164:167]\n"
    "v_exp_f32_e32 v161, v161\n"
    "v_mfma_f32_16x16x32_bf16 v[164:167], a[216:219], a[12:15], v[164:167]\n"
    "v_exp_f32_e32 v162, v162\n"
    "v_mfma_f32_16x16x32_bf16 v[164:167], a[220:223], a[16:19], v[164:167]\n"
    "v_exp_f32_e32 v163, v163\n"
    "v_mfma_f32_16x16x32_bf16 v[164:167], a[224:227], a[20:23], v[164:167]\n"
    "v_cvt_pk_bf16_f32 v160, v160, v161\n"
    "v_mfma_f32_16x16x32_bf16 v[164:167], a[228:231], a[24:27], v[164:167]\n"
    "s_waitcnt vmcnt(8)\n"
    "s_barrier\n"
    "s_cmp_eq_u32 s50, 0\n"
    "s_cbranch_scc1 .Lat_qy\n"
    "ds_read_b128 a[60:63], v216 offset:7168\n"
    "ds_read_b128 a[32:35], v216 offset:0\n"
    "ds_read_b128 a[36:39], v216 offset:1024\n"
    "ds_read_b128 a[40:43], v216 offset:2048\n"
    "ds_read_b128 a[44:47], v216 offset:3072\n"
    "ds_read_b128 a[48:51], v216 offset:4096\n"
    "ds_read_b128 a[52:55], v216 offset:5120\n"
    "ds_read_b128 a[56:59], v216 offset:6144\n"
    ".Lat_qy:\n"
    "ds_read_b128 a[92:95], v216 offset:15360\n"
    "ds_read_b128 a[64:67], v216 offset:8192\n"
    "s_waitcnt lgkmcnt(8)\n"
    "v_mfma_f32_16x16x32_bf16 v[168:171], a[160:163], a[60:63], 0\n"
    "v_cvt_pk_bf16_f32 v161, v162, v163\n"
    "v_mfma_f32_16x16x32_bf16 v[168:171], a[164:167], a[32:35], v[168:171]\n"
    "s_waitcnt lgkmcnt(6)\n"
    "ds_read_b128 a[68:71], v216 offset:9216\n"
    "v_mfma_f32_16x16x32_bf16 v[168:171], a[168:171], a[36:39], v[168:171]\n"
    "ds_read_b128 a[72:75], v216 offset:10240\n"
    "v_mfma_f32_16x16x32_bf16 v[168:171], a[172:175], a[40:43], v[168:171]\n"
    "s_waitcnt lgkmcnt(6)\n"
    "ds_read_b128 a[76:79], v216 offset:11264\n"
    "ds_write_addtid_b32 v160 offset:0\n"
    "v_mfma_f32_16x16x32_bf16 v[168:171], a[176:179], a[44:47], v[168:171]\n"
    "v_exp_f32_e32 v164, v164\n"
    "v_mfma_f32_16x16x32_bf16 v[168:171], a[180:183], a[48:51], v[168:171]\n"
    "s_waitcnt lgkmcnt(6)\n"
    "ds_read_b128 a[80:83], v216 offset:12288\n"
    "v_mfma_f32_16x16x32_bf16 v[168:171], a[184:187], a[52:55], v[168:171]\n"
    "ds_read_b128 a[84:87], v216 offset:13312\n"
    "v_mfma_f32_16x16x32_bf16 v[168:171], a[188:191], a[56:59], v[168:171]\n"
    "ds_read_b128 a[88:91], v216 offset:14336\n"
    "ds_write_addtid_b32 v161 offset:256\n"
    "v_mfma_f32_16x16x32_bf16 v[172:175], a[200:203], a[60:63], 0\n"
    "v_exp_f32_e32 v165, v165\n"
    "v_mfma_f32_16x16x32_bf16 v[172:175], a[204:207], a[32:35], v[172:175]\n"
    "v_exp_f32_e32 v166, v166\n"
    "v_mfma_f32_16x16x32_bf16 v[172:175], a[208:211], a[36:39], v[172:175]\n"
    "v_exp_f32_e32 v167, v167\n"
    "v_mfma_f32_16x16x32_bf16 v[172:175], a[212:215], a[40:43], v[172:175]\n"
    "v_cvt_pk_bf16_f32 v162, v164, v165\n"
    "v_cvt_pk_bf16_f32 v163, v166, v167\n"
    "v_mfma_f32_16x16x32_bf16 v[172:175], a[216:219], a[44:47], v[172:175]\n"
    "ds_write_addtid_b32 v162 offset:512\n"
    "v_exp_f32_e32 v168, v168\n"
    "v_mfma_f32_16x16x32_bf16 v[172:175], a[220:223], a[48:51], v[172:175]\n"
    "v_exp_f32_e32 v169, v169\n"
    "v_mfma_f32_16x16x32_bf16 v[172:175], a[224:227], a[52:55], v[172:175]\n"
    "v_exp_f32_e32 v170, v170\n"
    "v_mfma_f32_16x16x32_bf16 v[172:175], a[228:231], a[56:59], v[172:175]\n"
    "s_waitcnt lgkmcnt(9)\n"
    "v_exp_f32_e32 v171, v171\n"
    "v_mfma_f32_16x16x32_bf16 v[176:179], a[160:163], a[92:95], 0\n"
    "ds_write_addtid_b32 v163 offset:768\n"
    "v_cvt_pk_bf16_f32 v168, v168, v169\n"
    "v_mfma_f32_16x16x32_bf16 v[176:179], a[164:167], a[64:67], v[176:179]\n"
    "s_waitcnt lgkmcnt(8)\n"
    "ds_write_addtid_b32 v168 offset:1024\n"
    "v_cvt_pk_bf16_f32 v169, v170, v171\n"
    "v_mfma_f32_16x16x32_bf16 v[176:179], a[168:171], a[68:71], v[176:179]\n"
    "v_exp_f32_e32 v172, v172\n"
    "v_mfma_f32_16x16x32_bf16 v[176:179], a[172:175], a[72:75], v[176:179]\n"
    "s_waitcnt lgkmcnt(6)\n"
    "v_exp_f32_e32 v173, v173\n"
    "v_mfma_f32_16x16x32_bf16 v[176:179], a[176:179], a[76:79], v[176:179]\n"
    "v_exp_f32_e32 v174, v174\n"
    "v_mfma_f32_16x16x32_bf16 v[176:179], a[180:183], a[80:83], v[176:179]\n"
    "s_waitcnt vmcnt(0)\n"
    "s_barrier\n"
    "s_waitcnt lgkmcnt(4)\n"
    "v_mfma_f32_16x16x32_bf16 v[176:179], a[184:187], a[84:87], v[176:179]\n"
    "ds_write_addtid_b32 v169 offset:1280\n"
    "v_exp_f32_e32 v175, v175\n"
    "v_mfma_f32_16x16x32_bf16 v[176:179], a[188:191], a[88:91], v[176:179]\n"
    "ds_read_b128 a[124:127], v216 offset:23552\n"
    "v_cvt_pk_bf16_f32 v170, v172, v173\n"
    "v_mfma_f32_16x16x32_bf16 v[180:183], a[200:203], a[92:95], 0\n"
    "ds_read_b128 a[96:99], v216 offset:16384\n"
    "v_cvt_pk_bf16_f32 v171, v174, v175\n"
    "v_mfma_f32_16x16x32_bf16 v[180:183], a[204:207], a[64:67], v[180:183]\n"
    "ds_read_b128 a[100:103], v216 offset:17408\n"
    "v_exp_f32_e32 v176, v176\n"
    "v_mfma_f32_16x16x32_bf16 v[180:183], a[208:211], a[68:71], v[180:183]\n"
    "ds_read_b128 a[104:107], v216 offset:18432\n"
    "ds_write_addtid_b32 v170 offset:1536\n"
    "v_mfma_f32_16x16x32_bf16 v[180:183], a[212:215], a[72:75], v[180:183]\n"
    "ds_read_b128 a[108:111], v216 offset:19456\n"
    "v_exp_f32_e32 v177, v177\n"
    "v_mfma_f32_16x16x32_bf16 v[180:183], a[216:219], a[76:79], v[180:183]\n"
    "ds_read_b128 a[112:115], v216 offset:20480\n"
    "v_exp_f32_e32 v178, v178\n"
    "v_mfma_f32_16x16x32_bf16 v[180:183], a[220:223], a[80:83], v[180:183]\n"
    "ds_read_b128 a[116:119], v216 offset:21504\n"
    "ds_write_addtid_b32 v171 offset:1792\n"
    "v_mfma_f32_16x16x32_bf16 v[180:183], a[224:227], a[84:87], v[180:183]\n"
    "ds_read_b128 a[120:123], v216 offset:22528\n"
    "v_mfma_f32_16x16x32_bf16 v[180:183], a[228:231], a[88:91], v[180:183]\n"
    "s_waitcnt lgkmcnt(8)\n"
    "s_sub_u32 m0, s63, 16384\n"
    "v_exp_f32_e32 v179, v179\n"
    "v_mfma_f32_16x16x32_bf16 v[184:187], a[160:163], a[124:127], 0\n"
    "ds_read_b128 a[156:159], v216 offset:31744\n"
    "buffer_load_dwordx4 v248, s[8:11], s40 offen offset:0 sc1 lds\n"
    "v_cvt_pk_bf16_f32 v176, v176, v177\n"
    "v_mfma_f32_16x16x32_bf16 v[184:187], a[164:167], a[96:99], v[184:187]\n"
    "s_waitcnt lgkmcnt(7)\n"
    "ds_read_b128 a[128:131], v216 offset:24576\n"
    "v_cvt_pk_bf16_f32 v177, v178, v179\n"
    "v_mfma_f32_16x16x32_bf16 v[184:187], a[168:171], a[100:103], v[184:187]\n"
    "ds_read_b128 a[132:135], v216 offset:25600\n"
    "v_mfma_f32_16x16x32_bf16 v[184:187], a[172:175], a[104:107], v[184:187]\n"
    "s_waitcnt lgkmcnt(6)\n"
    "ds_read_b128 a[136:139], v216 offset:26624\n"
    "v_mfma_f32_16x16x32_bf16 v[184:187], a[176:179], a[108:111], v[184:187]\n"
    "ds_read_b128 a[140:143], v216 offset:27648\n"
    "buffer_load_dwordx4 v248, s[8:11], s40 offen offset:1024 sc1 lds\n"
    "v_mfma_f32_16x16x32_bf16 v[184:187], a[180:183], a[112:115], v[184:187]\n"
    "s_waitcnt lgkmcnt(5)\n"
    "ds_read_b128 a[144:147], v216 offset:28672\n"
    "v_mfma_f32_16x16x32_bf16 v[184:187], a[184:187], a[116:119], v[184:187]\n"
    "ds_read_b128 a[148:151], v216 offset:29696\n"
    "v_mfma_f32_16x16x32_bf16 v[184:187], a[188:191], a[120:123], v[184:187]\n"
    "ds_read_b128 a[152:155], v216 offset:30720\n"
    "v_mfma_f32_16x16x32_bf16 v[188:191], a[200:203], a[124:127], 0\n"
    "buffer_load_dwordx4 v248, s[8:11], s40 offen offset:2048 sc1 lds\n"
    "v_exp_f32_e32 v180, v180\n"
    "v_mfma_f32_16x16x32_bf16 v[188:191], a[204:207], a[96:99], v[188:191]\n"
    "ds_write_b32 v249, v176 offset:2048\n"
    "v_exp_f32_e32 v181, v181\n"
    "v_mfma_f32_16x16x32_bf16 v[188:191], a[208:211], a[100:103], v[188:191]\n"
    "ds_write_b32 v249, v177 offset:2304\n"
    "v_exp_f32_e32 v182, v182\n"
    "v_mfma_f32_16x16x32_bf16 v[188:191], a[212:215], a[104:107], v[188:191]\n"
    "v_exp_f32_e32 v183, v183\n"
    "v_mfma_f32_16x16x32_bf16 v[188:191], a[216:219], a[108:111], v[188:191]\n"
    "buffer_load_dwordx4 v248, s[8:11], s40 offen offset:3072 sc1 lds\n"
    "v_cvt_pk_bf16_f32 v178, v180, v181\n"
    "v_cvt_pk_bf16_f32 v179, v182, v183\n"
    "v_mfma_f32_16x16x32_bf16 v[188:191], a[220:223], a[112:115], v[188:191]\n"
    "s_add_u32 s40, s40, 0x8000\n"
    "ds_write_b32 v249, v178 offset:2560\n"
    "v_exp_f32_e32 v184, v184\n"
    "v_mfma_f32_16x16x32_bf16 v[188:191], a[224:227], a[116:119], v[188:191]\n"
    "s_sub_u32 m0, s63, 12288\n"
    "ds_write_b32 v249, v179 offset:2816\n"
    "v_exp_f32_e32 v185, v185\n"
    "v_mfma_f32_16x16x32_bf16 v[188:191], a[228:231], a[120:123], v[188:191]\n"
    "s_waitcnt lgkmcnt(10)\n"
    "v_exp_f32_e32 v186, v186\n"
    "v_mfma_f32_16x16x32_bf16 v[192:195], a[160:163], a[156:159], 0\n"
    "buffer_load_dwordx4 v248, s[8:11], s41 offen offset:0 sc1 lds\n"
    "v_exp_f32_e32 v187, v187\n"
    "v_mfma_f32_16x16x32_bf16 v[192:195], a[164:167], a[128:131], v[192:195]\n"
    "s_waitcnt lgkmcnt(8)\n"
    "ds_read_b128 a[160:163], v203 offset:20480\n"
    "v_cvt_pk_bf16_f32 v184, v184, v185\n"
    "v_mfma_f32_16x16x32_bf16 v[192:195], a[168:171], a[132:135], v[192:195]\n"
    "ds_read_b128 a[164:167], v200 offset:16384\n"
    "v_cvt_pk_bf16_f32 v185, v186, v187\n"
    "v_mfma_f32_16x16x32_bf16 v[192:195], a[172:175], a[136:139], v[192:195]\n"
    "s_waitcnt lgkmcnt(8)\n"
    "ds_read_b128 a[168:171], v201 offset:16384\n"
    "v_exp_f32_e32 v188, v188\n"
    "v_mfma_f32_16x16x32_bf16 v[192:195], a[176:179], a[140:143], v[192:195]\n"
    "ds_read_b128 a[172:175], v200 offset:20480\n"
    "buffer_load_dwordx4 v248, s[8:11], s41 offen offset:1024 sc1 lds\n"
    "v_exp_f32_e32 v189, v189\n"
    "v_mfma_f32_16x16x32_bf16 v[192:195], a[180:183], a[144:147], v[192:195]\n"
    "s_waitcnt lgkmcnt(8)\n"
    "ds_read_b128 a[176:179], v201 offset:20480\n"
    "v_exp_f32_e32 v190, v190\n"
    "v_mfma_f32_16x16x32_bf16 v[192:195], a[184:187], a[148:151], v[192:195]\n"
    "ds_read_b128 a[180:183], v202 offset:16384\n"
    "v_mfma_f32_16x16x32_bf16 v[192:195], a[188:191], a[152:155], v[192:195]\n"
    "ds_read_b128 a[184:187], v203 offset:16384\n"
    "v_mfma_f32_16x16x32_bf16 v[196:199], a[200:203], a[156:159], 0\n"
    "ds_read_b128 a[188:191], v202 offset:20480\n"
    "buffer_load_dwordx4 v248, s[8:11], s41 offen offset:2048 sc1 lds\n"
    "v_mfma_f32_16x16x32_bf16 v[196:199], a[204:207], a[128:131], v[196:199]\n"
    "ds_write_b32 v249, v184 offset:3072\n"
    "v_exp_f32_e32 v191, v191\n"
    "v_mfma_f32_16x16x32_bf16 v[196:199], a[208:211], a[132:135], v[196:199]\n"
    "ds_write_b32 v249, v185 offset:3328\n"
    "v_cvt_pk_bf16_f32 v186, v188, v189\n"
    "v_cvt_pk_bf16_f32 v187, v190, v191\n"
    "v_mfma_f32_16x16x32_bf16 v[196:199], a[212:215], a[136:139], v[196:199]\n"
    "ds_write_b32 v249, v186 offset:3584\n"
    "v_exp_f32_e32 v192, v192\n"
    "v_mfma_f32_16x16x32_bf16 v[196:199], a[216:219], a[140:143], v[196:199]\n"
    "buffer_load_dwordx4 v248, s[8:11], s41 offen offset:3072 sc1 lds\n"
    "v_exp_f32_e32 v193, v193\n"
    "v_mfma_f32_16x16x32_bf16 v[196:199], a[220:223], a[144:147], v[196:199]\n"
    "s_add_u32 s41, s41, 0x8000\n"
    "ds_write_b32 v249, v187 offset:3840\n"
    "v_exp_f32_e32 v194, v194\n"
    "v_mfma_f32_16x16x32_bf16 v[196:199], a[224:227], a[148:151], v[196:199]\n"
    "v_exp_f32_e32 v195, v195\n"
    "v_mfma_f32_16x16x32_bf16 v[196:199], a[228:231], a[152:155], v[196:199]\n"
    "v_cvt_pk_bf16_f32 v192, v192, v193\n"
    "ds_write_b32 v249, v192 offset:4096\n"
    "v_cvt_pk_bf16_f32 v193, v194, v195\n"
    "ds_write_b32 v249, v193 offset:4352\n"
    "s_mov_b32 s44, 2\n"
    "s_cmp_lg_u32 s34, 0\n"
    "s_waitcnt lgkmcnt(4)\n"
    "s_nop 0\n"
    "v_exp_f32_e32 v196, v196\n"
    "v_exp_f32_e32 v197, v197\n"
    "v_exp_f32_e32 v198, v198\n"
    "v_exp_f32_e32 v199, v199\n"
    "v_cvt_pk_bf16_f32 v194, v196, v197\n"
    "v_cvt_pk_bf16_f32 v195, v198, v199\n"
    "ds_write_b32 v249, v194 offset:4608\n"
    "ds_write_b32 v249, v195 offset:4864\n"
    "s_cbranch_scc1 .Lat_p2e\n"
    ".p2align 6\n"
    ".Lat_p:\n"
    "v_mfma_f32_16x16x32_bf16 v[200:203], a[160:163], a[28:31], 0\n"
    "v_mfma_f32_16x16x32_bf16 v[200:203], a[164:167], a[0:3], v[200:203]\n"
    "v_mfma_f32_16x16x32_bf16 v[208:211], a[164:167], a[32:35], 0\n"
    "v_mfma_f32_16x16x32_bf16 v[208:211], a[160:163], a[60:63], v[208:211]\n"
    "v_mfma_f32_16x16x32_bf16 v[216:219], a[160:163], a[92:95], 0\n"
    "v_mfma_f32_16x16x32_bf16 v[216:219], a[164:167], a[64:67], v[216:219]\n"
    "v_mfma_f32_16x16x32_bf16 v[224:227], a[164:167], a[96:99], 0\n"
    "s_add_u32 m0, s43, 49152\n"
    "v_mfma_f32_16x16x32_bf16 v[224:227], a[160:163], a[124:127], v[224:227]\n"
    "s_waitcnt lgkmcnt(0)\n"
    "s_barrier\n"
    "v_mfma_f32_16x16x32_bf16 v[232:235], a[160:163], a[156:159], 0\n"
    "ds_read_b128 a[160:163], v243 offset:22528\n"
    "v_mfma_f32_16x16x32_bf16 v[232:235], a[164:167], a[128:131], v[232:235]\n"
    "ds_read_b128 a[164:167], v240 offset:18432\n"
    "v_mfma_f32_16x16x32_bf16 v[232:235], a[168:171], a[132:135], v[232:235]\n"
    "buffer_load_dwordx4 v248, s[8:11], s40 offen offset:0 sc1 lds\n"
    "v_mfma_f32_16x16x32_bf16 v[232:235], a[172:175], a[136:139], v[232:235]\n"
    "ds_read2st64_b32 v[164:165], v250 offset0:0 offset1:1\n"
    "v_mfma_f32_16x16x32_bf16 v[224:227], a[172:175], a[104:107], v[224:227]\n"
    "v_mfma_f32_16x16x32_bf16 v[224:227], a[168:171], a[100:103], v[224:227]\n"
    "ds_read2st64_b32 v[166:167], v250 offset0:2 offset1:3\n"
    "v_mfma_f32_16x16x32_bf16 v[216:219], a[168:171], a[68:71], v[216:219]\n"
    "buffer_load_dwordx4 v248, s[8:11], s40 offen offset:1024 sc1 lds\n"
    "v_mfma_f32_16x16x32_bf16 v[216:219], a[172:175], a[72:75], v[216:219]\n"
    "v_mfma_f32_16x16x32_bf16 v[208:211], a[172:175], a[40:43], v[208:211]\n"
    "v_mfma_f32_16x16x32_bf16 v[208:211], a[168:171], a[36:39], v[208:211]\n"
    "ds_read2st64_b32 v[172:173], v250 offset0:4 offset1:5\n"
    "v_mfma_f32_16x16x32_bf16 v[200:203], a[168:171], a[4:7], v[200:203]\n"
    "ds_read_b128 a[168:171], v241 offset:18432\n"
    "buffer_load_dwordx4 v248, s[8:11], s40 offen offset:2048 sc1 lds\n"
    "v_mfma_f32_16x16x32_bf16 v[200:203], a[172:175], a[8:11], v[200:203]\n"
    "ds_read_b128 a[172:175], v240 offset:22528\n"
    "v_mfma_f32_16x16x32_bf16 v[200:203], a[176:179], a[12:15], v[200:203]\n"
    "v_mfma_f32_16x16x32_bf16 v[200:203], a[180:183], a[16:19], v[200:203]\n"
    "ds_read2st64_b32 v[174:175], v250 offset0:6 offset1:7\n"
    "v_mfma_f32_16x16x32_bf16 v[208:211], a[180:183], a[48:51], v[208:211]\n"
    "buffer_load_dwordx4 v248, s[8:11], s40 offen offset:3072 sc1 lds\n"
    "v_mfma_f32_16x16x32_bf16 v[208:211], a[176:179], a[44:47], v[208:211]\n"
    "s_add_u32 m0, s43, 53248\n"
    "v_mfma_f32_16x16x32_bf16 v[216:219], a[176:179], a[76:79], v[216:219]\n"
    "s_add_u32 s40, s40, 0x8000\n"
    "v_mfma_f32_16x16x32_bf16 v[216:219], a[180:183], a[80:83], v[216:219]\n"
    "ds_read2st64_b32 v[180:181], v250 offset0:8 offset1:9\n"
    "v_mfma_f32_16x16x32_bf16 v[224:227], a[180:183], a[112:115], v[224:227]\n"
    "buffer_load_dwordx4 v248, s[8:11], s41 offen offset:0 sc1 lds\n"
    "v_mfma_f32_16x16x32_bf16 v[224:227], a[176:179], a[108:111], v[224:227]\n"
    "ds_read2st64_b32 v[182:183], v250 offset0:10 offset1:11\n"
    "v_mfma_f32_16x16x32_bf16 v[232:235], a[176:179], a[140:143], v[232:235]\n"
    "ds_read_b128 a[176:179], v241 offset:22528\n"
    "v_mfma_f32_16x16x32_bf16 v[232:235], a[180:183], a[144:147], v[232:235]\n"
    "ds_read_b128 a[180:183], v242 offset:18432\n"
    "v_mfma_f32_16x16x32_bf16 v[232:235], a[184:187], a[148:151], v[232:235]\n"
    "buffer_load_dwordx4 v248, s[8:11], s41 offen offset:1024 sc1 lds\n"
    "v_mfma_f32_16x16x32_bf16 v[232:235], a[188:191], a[152:155], v[232:235]\n"
    "v_mfma_f32_16x16x32_bf16 v[224:227], a[188:191], a[120:123], v[224:227]\n"
    "ds_read2st64_b32 v[188:189], v250 offset0:12 offset1:13\n"
    "v_mfma_f32_16x16x32_bf16 v[224:227], a[184:187], a[116:119], v[224:227]\n"
    "v_mfma_f32_16x16x32_bf16 v[216:219], a[184:187], a[84:87], v[216:219]\n"
    "buffer_load_dwordx4 v248, s[8:11], s41 offen offset:2048 sc1 lds\n"
    "v_mfma_f32_16x16x32_bf16 v[216:219], a[188:191], a[88:91], v[216:219]\n"
    "v_exp_f32_e32 v232, v232\n"
    "v_mfma_f32_16x16x32_bf16 v[208:211], a[188:191], a[56:59], v[208:211]\n"
    "ds_read2st64_b32 v[190:191], v250 offset0:14 offset1:15\n"
    "v_mfma_f32_16x16x32_bf16 v[208:211], a[184:187], a[52:55], v[208:211]\n"
    "v_exp_f32_e32 v233, v233\n"
    "v_mfma_f32_16x16x32_bf16 v[200:203], a[184:187], a[20:23], v[200:203]\n"
    "ds_read_b128 a[184:187], v243 offset:18432\n"
    "buffer_load_dwordx4 v248, s[8:11], s41 offen offset:3072 sc1 lds\n"
    "v_mfma_f32_16x16x32_bf16 v[200:203], a[188:191], a[24:27], v[200:203]\n"
    "s_waitcnt lgkmcnt(8)\n"
    "s_add_u32 s41, s41, 0x8000\n"
    "ds_read_b128 a[188:191], v242 offset:22528\n"
    "v_mfma_f32_16x16x32_bf16 v[204:207], a[160:163], a[28:31], 0\n"
    "ds_read2st64_b32 v[196:197], v250 offset0:16 offset1:17\n"
    "v_mfma_f32_16x16x32_bf16 v[204:207], a[164:167], a[0:3], v[204:207]\n"
    "v_exp_f32_e32 v234, v234\n"
    "v_mfma_f32_16x16x32_bf16 v[212:215], a[164:167], a[32:35], 0\n"
    "v_exp_f32_e32 v235, v235\n"
    "v_mfma_f32_16x16x32_bf16 v[212:215], a[160:163], a[60:63], v[212:215]\n"
    "ds_read2st64_b32 v[198:199], v250 offset0:18 offset1:19\n"
    "v_mfma_f32_16x16x32_bf16 v[220:223], a[160:163], a[92:95], 0\n"
    "v_cvt_pk_bf16_f32 v232, v232, v233\n"
    "v_mfma_f32_16x16x32_bf16 v[220:223], a[164:167], a[64:67], v[220:223]\n"
    "v_exp_f32_e32 v224, v224\n"
    "v_mfma_f32_16x16x32_bf16 v[228:231], a[164:167], a[96:99], 0\n"
    "v_exp_f32_e32 v225, v225\n"
    "v_mfma_f32_16x16x32_bf16 v[228:231], a[160:163], a[124:127], v[228:231]\n"
    "v_cvt_pk_bf16_f32 v233, v234, v235\n"
    "v_mfma_f32_16x16x32_bf16 v[236:239], a[160:163], a[156:159], 0\n"
    "ds_read_b64_tr_b16 a[160:161], v244 offset:0\n"
    "v_mfma_f32_16x16x32_bf16 v[236:239], a[164:167], a[128:131], v[236:239]\n"
    "ds_read_b64_tr_b16 a[162:163], v244 offset:2048\n"
    "v_mfma_f32_16x16x32_bf16 v[236:239], a[168:171], a[132:135], v[236:239]\n"
    "ds_read_b64_tr_b16 a[164:165], v244 offset:8192\n"
    "v_mfma_f32_16x16x32_bf16 v[236:239], a[172:175], a[136:139], v[236:239]\n"
    "ds_read_b64_tr_b16 a[166:167], v244 offset:10240\n"
    "v_mfma_f32_16x16x32_bf16 v[228:231], a[172:175], a[104:107], v[228:231]\n"
    "v_exp_f32_e32 v226, v226\n"
    "v_mfma_f32_16x16x32_bf16 v[228:231], a[168:171], a[100:103], v[228:231]\n"
    "v_exp_f32_e32 v227, v227\n"
    "v_mfma_f32_16x16x32_bf16 v[220:223], a[168:171], a[68:71], v[220:223]\n"
    "v_cvt_pk_bf16_f32 v224, v224, v225\n"
    "v_mfma_f32_16x16x32_bf16 v[220:223], a[172:175], a[72:75], v[220:223]\n"
    "v_exp_f32_e32 v216, v216\n"
    "v_mfma_f32_16x16x32_bf16 v[212:215], a[172:175], a[40:43], v[212:215]\n"
    "v_exp_f32_e32 v217, v217\n"
    "v_mfma_f32_16x16x32_bf16 v[212:215], a[168:171], a[36:39], v[212:215]\n"
    "v_cvt_pk_bf16_f32 v225, v226, v227\n"
    "v_mfma_f32_16x16x32_bf16 v[204:207], a[168:171], a[4:7], v[204:207]\n"
    "ds_read_b64_tr_b16 a[168:169], v245 offset:0\n"
    "v_mfma_f32_16x16x32_bf16 v[204:207], a[172:175], a[8:11], v[204:207]\n"
    "s_waitcnt lgkmcnt(7)\n"
    "ds_read_b64_tr_b16 a[170:171], v245 offset:2048\n"
    "v_mfma_f32_16x16x32_bf16 v[204:207], a[176:179], a[12:15], v[204:207]\n"
    "ds_read_b64_tr_b16 a[172:173], v245 offset:8192\n"
    "v_mfma_f32_16x16x32_bf16 v[204:207], a[180:183], a[16:19], v[204:207]\n"
    "ds_read_b64_tr_b16 a[174:175], v245 offset:10240\n"
    "v_mfma_f32_16x16x32_bf16 v[212:215], a[180:183], a[48:51], v[212:215]\n"
    "v_exp_f32_e32 v218, v218\n"
    "v_mfma_f32_16x16x32_bf16 v[212:215], a[176:179], a[44:47], v[212:215]\n"
    "v_exp_f32_e32 v219, v219\n"
    "v_mfma_f32_16x16x32_bf16 v[220:223], a[176:179], a[76:79], v[220:223]\n"
    "v_cvt_pk_bf16_f32 v216, v216, v217\n"
    "v_mfma_f32_16x16x32_bf16 v[220:223], a[180:183], a[80:83], v[220:223]\n"
    "v_exp_f32_e32 v208, v208\n"
    "v_mfma_f32_16x16x32_bf16 v[228:231], a[180:183], a[112:115], v[228:231]\n"
    "v_exp_f32_e32 v209, v209\n"
    "v_mfma_f32_16x16x32_bf16 v[228:231], a[176:179], a[108:111], v[228:231]\n"
    "v_cvt_pk_bf16_f32 v217, v218, v219\n"
    "v_mfma_f32_16x16x32_bf16 v[236:239], a[176:179], a[140:143], v[236:239]\n"
    "ds_read_b64_tr_b16 a[176:177], v246 offset:0\n"
    "v_mfma_f32_16x16x32_bf16 v[236:239], a[180:183], a[144:147], v[236:239]\n"
    "ds_read_b64_tr_b16 a[178:179], v246 offset:2048\n"
    "v_mfma_f32_16x16x32_bf16 v[236:239], a[184:187], a[148:151], v[236:239]\n"
    "ds_read_b64_tr_b16 a[180:181], v246 offset:8192\n"
    "v_mfma_f32_16x16x32_bf16 v[236:239], a[188:191], a[152:155], v[236:239]\n"
    "ds_read_b64_tr_b16 a[182:183], v246 offset:10240\n"
    "v_mfma_f32_16x16x32_bf16 v[228:231], a[188:191], a[120:123], v[228:231]\n"
    "v_exp_f32_e32 v210, v210\n"
    "v_mfma_f32_16x16x32_bf16 v[228:231], a[184:187], a[116:119], v[228:231]\n"
    "v_exp_f32_e32 v211, v211\n"
    "v_mfma_f32_16x16x32_bf16 v[220:223], a[184:187], a[84:87], v[220:223]\n"
    "v_cvt_pk_bf16_f32 v208, v208, v209\n"
    "v_mfma_f32_16x16x32_bf16 v[220:223], a[188:191], a[88:91], v[220:223]\n"
    "v_exp_f32_e32 v200, v200\n"
    "v_mfma_f32_16x16x32_bf16 v[212:215], a[188:191], a[56:59], v[212:215]\n"
    "v_exp_f32_e32 v201, v201\n"
    "v_mfma_f32_16x16x32_bf16 v[212:215], a[184:187], a[52:55], v[212:215]\n"
    "v_cvt_pk_bf16_f32 v209, v210, v211\n"
    "v_mfma_f32_16x16x32_bf16 v[204:207], a[184:187], a[20:23], v[204:207]\n"
    "ds_read_b64_tr_b16 a[184:185], v247 offset:0\n"
    "v_mfma_f32_16x16x32_bf16 v[204:207], a[188:191], a[24:27], v[204:207]\n"
    "s_waitcnt lgkmcnt(5)\n"
    "ds_read_b64_tr_b16 a[186:187], v247 offset:2048\n"
    "v_mfma_f32_16x16x32_bf16 a[248:251], a[160:163], v[160:163], a[248:251]\n"
    "ds_read_b64_tr_b16 a[188:189], v247 offset:8192\n"
    "v_mfma_f32_16x16x32_bf16 a[248:251], a[164:167], v[164:167], a[248:251]\n"
    "ds_read_b64_tr_b16 a[190:191], v247 offset:10240\n"
    "v_mfma_f32_16x16x32_bf16 a[252:255], a[164:167], v[172:175], a[252:255]\n"
    "v_exp_f32_e32 v202, v202\n"
    "v_mfma_f32_16x16x32_bf16 a[252:255], a[160:163], v[168:171], a[252:255]\n"
    "v_exp_f32_e32 v203, v203\n"
    "v_mfma_f32_16x16x32_bf16 v[56:59], a[160:163], v[176:179], v[56:59]\n"
    "v_cvt_pk_bf16_f32 v200, v200, v201\n"
    "v_mfma_f32_16x16x32_bf16 v[56:59], a[164:167], v[180:183], v[56:59]\n"
    "v_mfma_f32_16x16x32_bf16 v[60:63], a[164:167], v[188:191], v[60:63]\n"
    "v_cvt_pk_bf16_f32 v201, v202, v203\n"
    "v_mfma_f32_16x16x32_bf16 v[60:63], a[160:163], v[184:187], v[60:63]\n"
    "v_exp_f32_e32 v236, v236\n"
    "v_mfma_f32_16x16x32_bf16 v[64:67], a[160:163], v[192:195], v[64:67]\n"
    "ds_read_b64_tr_b16 a[160:161], v244 offset:4096\n"
    "v_mfma_f32_16x16x32_bf16 v[64:67], a[164:167], v[196:199], v[64:67]\n"
    "ds_read_b64_tr_b16 a[162:163], v244 offset:6144\n"
    "v_mfma_f32_16x16x32_bf16 v[76:79], a[168:171], v[192:195], v[76:79]\n"
    "ds_read_b64_tr_b16 a[164:165], v244 offset:12288\n"
    "v_mfma_f32_16x16x32_bf16 v[76:79], a[172:175], v[196:199], v[76:79]\n"
    "ds_read_b64_tr_b16 a[166:167], v244 offset:14336\n"
    "v_mfma_f32_16x16x32_bf16 v[72:75], a[172:175], v[188:191], v[72:75]\n"
    "v_exp_f32_e32 v237, v237\n"
    "v_mfma_f32_16x16x32_bf16 v[72:75], a[168:171], v[184:187], v[72:75]\n"
    "v_exp_f32_e32 v238, v238\n"
    "v_mfma_f32_16x16x32_bf16 v[68:71], a[168:171], v[176:179], v[68:71]\n"
    "v_exp_f32_e32 v239, v239\n"
    "v_mfma_f32_16x16x32_bf16 v[68:71], a[172:175], v[180:183], v[68:71]\n"
    "v_cvt_pk_bf16_f32 v234, v236, v237\n"
    "v_mfma_f32_16x16x32_bf16 v[4:7], a[172:175], v[172:175], v[4:7]\n"
    "v_exp_f32_e32 v228, v228\n"
    "v_mfma_f32_16x16x32_bf16 v[4:7], a[168:171], v[168:171], v[4:7]\n"
    "v_exp_f32_e32 v229, v229\n"
    "v_mfma_f32_16x16x32_bf16 v[0:3], a[168:171], v[160:163], v[0:3]\n"
    "ds_read_b64_tr_b16 a[168:169], v245 offset:4096\n"
    "v_mfma_f32_16x16x32_bf16 v[0:3], a[172:175], v[164:167], v[0:3]\n"
    "s_waitcnt lgkmcnt(5)\n"
    "ds_read_b64_tr_b16 a[170:171], v245 offset:6144\n"
    "v_mfma_f32_16x16x32_bf16 v[8:11], a[176:179], v[160:163], v[8:11]\n"
    "ds_read_b64_tr_b16 a[172:173], v245 offset:12288\n"
    "v_mfma_f32_16x16x32_bf16 v[8:11], a[180:183], v[164:167], v[8:11]\n"
    "ds_read_b64_tr_b16 a[174:175], v245 offset:14336\n"
    "v_mfma_f32_16x16x32_bf16 v[12:15], a[180:183], v[172:175], v[12:15]\n"
    "v_cvt_pk_bf16_f32 v235, v238, v239\n"
    "v_mfma_f32_16x16x32_bf16 v[12:15], a[176:179], v[168:171], v[12:15]\n"
    "v_exp_f32_e32 v230, v230\n"
    "v_mfma_f32_16x16x32_bf16 v[80:83], a[176:179], v[176:179], v[80:83]\n"
    "v_exp_f32_e32 v231, v231\n"
    "v_mfma_f32_16x16x32_bf16 v[80:83], a[180:183], v[180:183], v[80:83]\n"
    "v_cvt_pk_bf16_f32 v226, v228, v229\n"
    "v_mfma_f32_16x16x32_bf16 v[84:87], a[180:183], v[188:191], v[84:87]\n"
    "v_exp_f32_e32 v220, v220\n"
    "v_mfma_f32_16x16x32_bf16 v[84:87], a[176:179], v[184:187], v[84:87]\n"
    "v_exp_f32_e32 v221, v221\n"
    "v_mfma_f32_16x16x32_bf16 v[88:91], a[176:179], v[192:195], v[88:91]\n"
    "ds_read_b64_tr_b16 a[176:177], v246 offset:4096\n"
    "v_mfma_f32_16x16x32_bf16 v[88:91], a[180:183], v[196:199], v[88:91]\n"
    "ds_read_b64_tr_b16 a[178:179], v246 offset:6144\n"
    "v_mfma_f32_16x16x32_bf16 v[100:103], a[184:187], v[192:195], v[100:103]\n"
    "ds_read_b64_tr_b16 a[180:181], v246 offset:12288\n"
    "v_mfma_f32_16x16x32_bf16 v[100:103], a[188:191], v[196:199], v[100:103]\n"
    "ds_read_b64_tr_b16 a[182:183], v246 offset:14336\n"
    "v_mfma_f32_16x16x32_bf16 v[96:99], a[188:191], v[188:191], v[96:99]\n"
    "v_cvt_pk_bf16_f32 v227, v230, v231\n"
    "v_mfma_f32_16x16x32_bf16 v[96:99], a[184:187], v[184:187], v[96:99]\n"
    "v_exp_f32_e32 v222, v222\n"
    "v_mfma_f32_16x16x32_bf16 v[92:95], a[184:187], v[176:179], v[92:95]\n"
    "v_exp_f32_e32 v223, v223\n"
    "v_mfma_f32_16x16x32_bf16 v[92:95], a[188:191], v[180:183], v[92:95]\n"
    "v_mfma_f32_16x16x32_bf16 v[20:23], a[188:191], v[172:175], v[20:23]\n"
    "v_cvt_pk_bf16_f32 v218, v220, v221\n"
    "v_mfma_f32_16x16x32_bf16 v[20:23], a[184:187], v[168:171], v[20:23]\n"
    "v_exp_f32_e32 v212, v212\n"
    "v_mfma_f32_16x16x32_bf16 v[16:19], a[184:187], v[160:163], v[16:19]\n"
    "ds_read_b64_tr_b16 a[184:185], v247 offset:4096\n"
    "v_mfma_f32_16x16x32_bf16 v[16:19], a[188:191], v[164:167], v[16:19]\n"
    "s_waitcnt lgkmcnt(5)\n"
    "s_mov_b32 m0, s49\n"
    "ds_read_b64_tr_b16 a[186:187], v247 offset:6144\n"
    "v_mfma_f32_16x16x32_bf16 v[24:27], a[160:163], v[160:163], v[24:27]\n"
    "ds_read_b64_tr_b16 a[188:189], v247 offset:12288\n"
    "v_mfma_f32_16x16x32_bf16 v[24:27], a[164:167], v[164:167], v[24:27]\n"
    "ds_read_b64_tr_b16 a[190:191], v247 offset:14336\n"
    "v_mfma_f32_16x16x32_bf16 v[28:31], a[164:167], v[172:175], v[28:31]\n"
    "v_exp_f32_e32 v213, v213\n"
    "v_mfma_f32_16x16x32_bf16 v[28:31], a[160:163], v[168:171], v[28:31]\n"
    "v_cvt_pk_bf16_f32 v219, v222, v223\n"
    "v_mfma_f32_16x16x32_bf16 v[104:107], a[160:163], v[176:179], v[104:107]\n"
    "v_exp_f32_e32 v214, v214\n"
    "v_mfma_f32_16x16x32_bf16 v[104:107], a[164:167], v[180:183], v[104:107]\n"
    "v_exp_f32_e32 v215, v215\n"
    "v_mfma_f32_16x16x32_bf16 v[108:111], a[164:167], v[188:191], v[108:111]\n"
    "v_cvt_pk_bf16_f32 v210, v212, v213\n"
    "v_mfma_f32_16x16x32_bf16 v[108:111], a[160:163], v[184:187], v[108:111]\n"
    "s_waitcnt vmcnt(8)\n"
    "s_barrier\n"
    "v_mfma_f32_16x16x32_bf16 v[112:115], a[160:163], v[192:195], v[112:115]\n"
    "ds_read_b128 a[160:163], v243 offset:36864\n"
    "v_mfma_f32_16x16x32_bf16 v[112:115], a[164:167], v[196:199], v[112:115]\n"
    "ds_read_b128 a[164:167], v240 offset:32768\n"
    "v_mfma_f32_16x16x32_bf16 v[124:127], a[168:171], v[192:195], v[124:127]\n"
    "v_exp_f32_e32 v204, v204\n"
    "v_mfma_f32_16x16x32_bf16 v[124:127], a[172:175], v[196:199], v[124:127]\n"
    "v_exp_f32_e32 v205, v205\n"
    "v_mfma_f32_16x16x32_bf16 v[120:123], a[172:175], v[188:191], v[120:123]\n"
    "ds_write_addtid_b32 v232 offset:4096\n"
    "v_cvt_pk_bf16_f32 v211, v214, v215\n"
    "v_mfma_f32_16x16x32_bf16 v[120:123], a[168:171], v[184:187], v[120:123]\n"
    "ds_write_addtid_b32 v233 offset:4352\n"
    "v_mfma_f32_16x16x32_bf16 v[116:119], a[168:171], v[176:179], v[116:119]\n"
    "v_exp_f32_e32 v206, v206\n"
    "v_mfma_f32_16x16x32_bf16 v[116:119], a[172:175], v[180:183], v[116:119]\n"
    "v_exp_f32_e32 v207, v207\n"
    "v_mfma_f32_16x16x32_bf16 v[36:39], a[172:175], v[172:175], v[36:39]\n"
    "ds_write_addtid_b32 v224 offset:3072\n"
    "v_cvt_pk_bf16_f32 v202, v204, v205\n"
    "v_mfma_f32_16x16x32_bf16 v[36:39], a[168:171], v[168:171], v[36:39]\n"
    "ds_write_addtid_b32 v225 offset:3328\n"
    "v_cvt_pk_bf16_f32 v203, v206, v207\n"
    "v_mfma_f32_16x16x32_bf16 v[32:35], a[168:171], v[160:163], v[32:35]\n"
    "ds_read_b128 a[168:171], v241 offset:32768\n"
    "v_mfma_f32_16x16x32_bf16 v[32:35], a[172:175], v[164:167], v[32:35]\n"
    "s_waitcnt lgkmcnt(7)\n"
    "ds_read_b128 a[172:175], v240 offset:36864\n"
    "v_mfma_f32_16x16x32_bf16 v[40:43], a[176:179], v[160:163], v[40:43]\n"
    "ds_write_addtid_b32 v216 offset:2048\n"
    "v_mfma_f32_16x16x32_bf16 v[40:43], a[180:183], v[164:167], v[40:43]\n"
    "ds_write_addtid_b32 v217 offset:2304\n"
    "v_mfma_f32_16x16x32_bf16 v[44:47], a[180:183], v[172:175], v[44:47]\n"
    "ds_write_addtid_b32 v208 offset:1024\n"
    "v_mfma_f32_16x16x32_bf16 v[44:47], a[176:179], v[168:171], v[44:47]\n"
    "ds_write_addtid_b32 v209 offset:1280\n"
    "v_mfma_f32_16x16x32_bf16 v[128:131], a[176:179], v[176:179], v[128:131]\n"
    "ds_write_addtid_b32 v200 offset:0\n"
    "v_mfma_f32_16x16x32_bf16 v[128:131], a[180:183], v[180:183], v[128:131]\n"
    "ds_write_addtid_b32 v201 offset:256\n"
    "v_mfma_f32_16x16x32_bf16 v[132:135], a[180:183], v[188:191], v[132:135]\n"
    "ds_write_addtid_b32 v234 offset:4608\n"
    "v_mfma_f32_16x16x32_bf16 v[132:135], a[176:179], v[184:187], v[132:135]\n"
    "ds_write_addtid_b32 v235 offset:4864\n"
    "v_mfma_f32_16x16x32_bf16 v[136:139], a[176:179], v[192:195], v[136:139]\n"
    "ds_read_b128 a[176:179], v241 offset:36864\n"
    "v_mfma_f32_16x16x32_bf16 v[136:139], a[180:183], v[196:199], v[136:139]\n"
    "ds_read_b128 a[180:183], v242 offset:32768\n"
    "v_mfma_f32_16x16x32_bf16 v[148:151], a[184:187], v[192:195], v[148:151]\n"
    "ds_write_addtid_b32 v226 offset:3584\n"
    "v_mfma_f32_16x16x32_bf16 v[148:151], a[188:191], v[196:199], v[148:151]\n"
    "ds_write_addtid_b32 v227 offset:3840\n"
    "v_mfma_f32_16x16x32_bf16 v[144:147], a[188:191], v[188:191], v[144:147]\n"
    "ds_write_addtid_b32 v218 offset:2560\n"
    "v_mfma_f32_16x16x32_bf16 v[144:147], a[184:187], v[184:187], v[144:147]\n"
    "ds_write_addtid_b32 v219 offset:2816\n"
    "v_mfma_f32_16x16x32_bf16 v[140:143], a[184:187], v[176:179], v[140:143]\n"
    "ds_write_addtid_b32 v210 offset:1536\n"
    "v_mfma_f32_16x16x32_bf16 v[140:143], a[188:191], v[180:183], v[140:143]\n"
    "ds_write_addtid_b32 v211 offset:1792\n"
    "v_mfma_f32_16x16x32_bf16 v[52:55], a[188:191], v[172:175], v[52:55]\n"
    "ds_write_addtid_b32 v202 offset:512\n"
    "v_mfma_f32_16x16x32_bf16 v[52:55], a[184:187], v[168:171], v[52:55]\n"
    "ds_write_addtid_b32 v203 offset:768\n"
    "v_mfma_f32_16x16x32_bf16 v[48:51], a[184:187], v[160:163], v[48:51]\n"
    "ds_read_b128 a[184:187], v243 offset:32768\n"
    "v_mfma_f32_16x16x32_bf16 v[48:51], a[188:191], v[164:167], v[48:51]\n"
    "s_waitcnt lgkmcnt(15)\n"
    "ds_read_b128 a[188:191], v242 offset:36864\n"
    "v_mfma_f32_16x16x32_bf16 v[160:163], a[160:163], a[28:31], 0\n"
    "v_mfma_f32_16x16x32_bf16 v[160:163], a[164:167], a[0:3], v[160:163]\n"
    "v_mfma_f32_16x16x32_bf16 v[168:171], a[164:167], a[32:35], 0\n"
    "v_mfma_f32_16x16x32_bf16 v[168:171], a[160:163], a[60:63], v[168:171]\n"
    "v_mfma_f32_16x16x32_bf16 v[176:179], a[160:163], a[92:95], 0\n"
    "v_mfma_f32_16x16x32_bf16 v[176:179], a[164:167], a[64:67], v[176:179]\n"
    "v_mfma_f32_16x16x32_bf16 v[184:187], a[164:167], a[96:99], 0\n"
    "s_add_u32 m0, s43, 0\n"
    "v_mfma_f32_16x16x32_bf16 v[184:187], a[160:163], a[124:127], v[184:187]\n"
    "s_waitcnt lgkmcnt(0)\n"
    "s_barrier\n"
    "v_mfma_f32_16x16x32_bf16 v[192:195], a[160:163], a[156:159], 0\n"
    "ds_read_b128 a[160:163], v243 offset:38912\n"
    "v_mfma_f32_16x16x32_bf16 v[192:195], a[164:167], a[128:131], v[192:195]\n"
    "ds_read_b128 a[164:167], v240 offset:34816\n"
    "v_mfma_f32_16x16x32_bf16 v[192:195], a[168:171], a[132:135], v[192:195]\n"
    "buffer_load_dwordx4 v248, s[8:11], s40 offen offset:0 sc1 lds\n"
    "v_mfma_f32_16x16x32_bf16 v[192:195], a[172:175], a[136:139], v[192:195]\n"
    "ds_read2st64_b32 v[204:205], v250 offset0:0 offset1:1\n"
    "v_mfma_f32_16x16x32_bf16 v[184:187], a[172:175], a[104:107], v[184:187]\n"
    "v_mfma_f32_16x16x32_bf16 v[184:187], a[168:171], a[100:103], v[184:187]\n"
    "ds_read2st64_b32 v[206:207], v250 offset0:2 offset1:3\n"
    "v_mfma_f32_16x16x32_bf16 v[176:179], a[168:171], a[68:71], v[176:179]\n"
    "buffer_load_dwordx4 v248, s[8:11], s40 offen offset:1024 sc1 lds\n"
    "v_mfma_f32_16x16x32_bf16 v[176:179], a[172:175], a[72:75], v[176:179]\n"
    "v_mfma_f32_16x16x32_bf16 v[168:171], a[172:175], a[40:43], v[168:171]\n"
    "v_mfma_f32_16x16x32_bf16 v[168:171], a[168:171], a[36:39], v[168:171]\n"
    "ds_read2st64_b32 v[212:213], v250 offset0:4 offset1:5\n"
    "v_mfma_f32_16x16x32_bf16 v[160:163], a[168:171], a[4:7], v[160:163]\n"
    "ds_read_b128 a[168:171], v241 offset:34816\n"
    "buffer_load_dwordx4 v248, s[8:11], s40 offen offset:2048 sc1 lds\n"
    "v_mfma_f32_16x16x32_bf16 v[160:163], a[172:175], a[8:11], v[160:163]\n"
    "ds_read_b128 a[172:175], v240 offset:38912\n"
    "v_mfma_f32_16x16x32_bf16 v[160:163], a[176:179], a[12:15], v[160:163]\n"
    "v_mfma_f32_16x16x32_bf16 v[160:163], a[180:183], a[16:19], v[160:163]\n"
    "ds_read2st64_b32 v[214:215], v250 offset0:6 offset1:7\n"
    "v_mfma_f32_16x16x32_bf16 v[168:171], a[180:183], a[48:51], v[168:171]\n"
    "buffer_load_dwordx4 v248, s[8:11], s40 offen offset:3072 sc1 lds\n"
    "v_mfma_f32_16x16x32_bf16 v[168:171], a[176:179], a[44:47], v[168:171]\n"
    "s_add_u32 m0, s43, 4096\n"
    "v_mfma_f32_16x16x32_bf16 v[176:179], a[176:179], a[76:79], v[176:179]\n"
    "s_add_u32 s40, s40, 0x8000\n"
    "v_mfma_f32_16x16x32_bf16 v[176:179], a[180:183], a[80:83], v[176:179]\n"
    "ds_read2st64_b32 v[220:221], v250 offset0:8 offset1:9\n"
    "v_mfma_f32_16x16x32_bf16 v[184:187], a[180:183], a[112:115], v[184:187]\n"
    "buffer_load_dwordx4 v248, s[8:11], s41 offen offset:0 sc1 lds\n"
    "v_mfma_f32_16x16x32_bf16 v[184:187], a[176:179], a[108:111], v[184:187]\n"
    "ds_read2st64_b32 v[222:223], v250 offset0:10 offset1:11\n"
    "v_mfma_f32_16x16x32_bf16 v[192:195], a[176:179], a[140:143], v[192:195]\n"
    "ds_read_b128 a[176:179], v241 offset:38912\n"
    "v_mfma_f32_16x16x32_bf16 v[192:195], a[180:183], a[144:147], v[192:195]\n"
    "ds_read_b128 a[180:183], v242 offset:34816\n"
    "v_mfma_f32_16x16x32_bf16 v[192:195], a[184:187], a[148:151], v[192:195]\n"
    "buffer_load_dwordx4 v248, s[8:11], s41 offen offset:1024 sc1 lds\n"
    "v_mfma_f32_16x16x32_bf16 v[192:195], a[188:191], a[152:155], v[192:195]\n"
    "v_mfma_f32_16x16x32_bf16 v[184:187], a[188:191], a[120:123], v[184:187]\n"
    "ds_read2st64_b32 v[228:229], v250 offset0:12 offset1:13\n"
    "v_mfma_f32_16x16x32_bf16 v[184:187], a[184:187], a[116:119], v[184:187]\n"
    "v_mfma_f32_16x16x32_bf16 v[176:179], a[184:187], a[84:87], v[176:179]\n"
    "buffer_load_dwordx4 v248, s[8:11], s41 offen offset:2048 sc1 lds\n"
    "v_mfma_f32_16x16x32_bf16 v[176:179], a[188:191], a[88:91], v[176:179]\n"
    "v_exp_f32_e32 v192, v192\n"
    "v_mfma_f32_16x16x32_bf16 v[168:171], a[188:191], a[56:59], v[168:171]\n"
    "ds_read2st64_b32 v[230:231], v250 offset0:14 offset1:15\n"
    "v_mfma_f32_16x16x32_bf16 v[168:171], a[184:187], a[52:55], v[168:171]\n"
    "v_exp_f32_e32 v193, v193\n"
    "v_mfma_f32_16x16x32_bf16 v[160:163], a[184:187], a[20:23], v[160:163]\n"
    "ds_read_b128 a[184:187], v243 offset:34816\n"
    "buffer_load_dwordx4 v248, s[8:11], s41 offen offset:3072 sc1 lds\n"
    "v_mfma_f32_16x16x32_bf16 v[160:163], a[188:191], a[24:27], v[160:163]\n"
    "s_waitcnt lgkmcnt(8)\n"
    "s_add_u32 s41, s41, 0x8000\n"
    "ds_read_b128 a[188:191], v242 offset:38912\n"
    "v_mfma_f32_16x16x32_bf16 v[164:167], a[160:163], a[28:31], 0\n"
    "ds_read2st64_b32 v[236:237], v250 offset0:16 offset1:17\n"
    "v_mfma_f32_16x16x32_bf16 v[164:167], a[164:167], a[0:3], v[164:167]\n"
    "v_exp_f32_e32 v194, v194\n"
    "v_mfma_f32_16x16x32_bf16 v[172:175], a[164:167], a[32:35], 0\n"
    "v_exp_f32_e32 v195, v195\n"
    "v_mfma_f32_16x16x32_bf16 v[172:175], a[160:163], a[60:63], v[172:175]\n"
    "ds_read2st64_b32 v[238:239], v250 offset0:18 offset1:19\n"
    "v_mfma_f32_16x16x32_bf16 v[180:183], a[160:163], a[92:95], 0\n"
    "v_cvt_pk_bf16_f32 v192, v192, v193\n"
    "v_mfma_f32_16x16x32_bf16 v[180:183], a[164:167], a[64:67], v[180:183]\n"
    "v_exp_f32_e32 v184, v184\n"
    "v_mfma_f32_16x16x32_bf16 v[188:191], a[164:167], a[96:99], 0\n"
    "v_exp_f32_e32 v185, v185\n"
    "v_mfma_f32_16x16x32_bf16 v[188:191], a[160:163], a[124:127], v[188:191]\n"
    "v_cvt_pk_bf16_f32 v193, v194, v195\n"
    "v_mfma_f32_16x16x32_bf16 v[196:199], a[160:163], a[156:159], 0\n"
    "ds_read_b64_tr_b16 a[160:161], v244 offset:16384\n"
    "v_mfma_f32_16x16x32_bf16 v[196:199], a[164:167], a[128:131], v[196:199]\n"
    "ds_read_b64_tr_b16 a[162:163], v244 offset:18432\n"
    "v_mfma_f32_16x16x32_bf16 v[196:199], a[168:171], a[132:135], v[196:199]\n"
    "ds_read_b64_tr_b16 a[164:165], v244 offset:24576\n"
    "v_mfma_f32_16x16x32_bf16 v[196:199], a[172:175], a[136:139], v[196:199]\n"
    "ds_read_b64_tr_b16 a[166:167], v244 offset:26624\n"
    "v_mfma_f32_16x16x32_bf16 v[188:191], a[172:175], a[104:107], v[188:191]\n"
    "v_exp_f32_e32 v186, v186\n"
    "v_mfma_f32_16x16x32_bf16 v[188:191], a[168:171], a[100:103], v[188:191]\n"
    "v_exp_f32_e32 v187, v187\n"
    "v_mfma_f32_16x16x32_bf16 v[180:183], a[168:171], a[68:71], v[180:183]\n"
    "v_cvt_pk_bf16_f32 v184, v184, v185\n"
    "v_mfma_f32_16x16x32_bf16 v[180:183], a[172:175], a[72:75], v[180:183]\n"
    "v_exp_f32_e32 v176, v176\n"
    "v_mfma_f32_16x16x32_bf16 v[172:175], a[172:175], a[40:43], v[172:175]\n"
    "v_exp_f32_e32 v177, v177\n"
    "v_mfma_f32_16x16x32_bf16 v[172:175], a[168:171], a[36:39], v[172:175]\n"
    "v_cvt_pk_bf16_f32 v185, v186, v187\n"
    "v_mfma_f32_16x16x32_bf16 v[164:167], a[168:171], a[4:7], v[164:167]\n"
    "ds_read_b64_tr_b16 a[168:169], v245 offset:16384\n"
    "v_mfma_f32_16x16x32_bf16 v[164:167], a[172:175], a[8:11], v[164:167]\n"
    "s_waitcnt lgkmcnt(7)\n"
    "ds_read_b64_tr_b16 a[170:171], v245 offset:18432\n"
    "v_mfma_f32_16x16x32_bf16 v[164:167], a[176:179], a[12:15], v[164:167]\n"
    "ds_read_b64_tr_b16 a[172:173], v245 offset:24576\n"
    "v_mfma_f32_16x16x32_bf16 v[164:167], a[180:183], a[16:19], v[164:167]\n"
    "ds_read_b64_tr_b16 a[174:175], v245 offset:26624\n"
    "v_mfma_f32_16x16x32_bf16 v[172:175], a[180:183], a[48:51], v[172:175]\n"
    "v_exp_f32_e32 v178, v178\n"
    "v_mfma_f32_16x16x32_bf16 v[172:175], a[176:179], a[44:47], v[172:175]\n"
    "v_exp_f32_e32 v179, v179\n"
    "v_mfma_f32_16x16x32_bf16 v[180:183], a[176:179], a[76:79], v[180:183]\n"
    "v_cvt_pk_bf16_f32 v176, v176, v177\n"
    "v_mfma_f32_16x16x32_bf16 v[180:183], a[180:183], a[80:83], v[180:183]\n"
    "v_exp_f32_e32 v168, v168\n"
    "v_mfma_f32_16x16x32_bf16 v[188:191], a[180:183], a[112:115], v[188:191]\n"
    "v_exp_f32_e32 v169, v169\n"
    "v_mfma_f32_16x16x32_bf16 v[188:191], a[176:179], a[108:111], v[188:191]\n"
    "v_cvt_pk_bf16_f32 v177, v178, v179\n"
    "v_mfma_f32_16x16x32_bf16 v[196:199], a[176:179], a[140:143], v[196:199]\n"
    "ds_read_b64_tr_b16 a[176:177], v246 offset:16384\n"
    "v_mfma_f32_16x16x32_bf16 v[196:199], a[180:183], a[144:147], v[196:199]\n"
    "ds_read_b64_tr_b16 a[178:179], v246 offset:18432\n"
    "v_mfma_f32_16x16x32_bf16 v[196:199], a[184:187], a[148:151], v[196:199]\n"
    "ds_read_b64_tr_b16 a[180:181], v246 offset:24576\n"
    "v_mfma_f32_16x16x32_bf16 v[196:199], a[188:191], a[152:155], v[196:199]\n"
    "ds_read_b64_tr_b16 a[182:183], v246 offset:26624\n"
    "v_mfma_f32_16x16x32_bf16 v[188:191], a[188:191], a[120:123], v[188:191]\n"
    "v_exp_f32_e32 v170, v170\n"
    "v_mfma_f32_16x16x32_bf16 v[188:191], a[184:187], a[116:119], v[188:191]\n"
    "v_exp_f32_e32 v171, v171\n"
    "v_mfma_f32_16x16x32_bf16 v[180:183], a[184:187], a[84:87], v[180:183]\n"
    "v_cvt_pk_bf16_f32 v168, v168, v169\n"
    "v_mfma_f32_16x16x32_bf16 v[180:183], a[188:191], a[88:91], v[180:183]\n"
    "v_exp_f32_e32 v160, v160\n"
    "v_mfma_f32_16x16x32_bf16 v[172:175], a[188:191], a[56:59], v[172:175]\n"
    "v_exp_f32_e32 v161, v161\n"
    "v_mfma_f32_16x16x32_bf16 v[172:175], a[184:187], a[52:55], v[172:175]\n"
    "v_cvt_pk_bf16_f32 v169, v170, v171\n"
    "v_mfma_f32_16x16x32_bf16 v[164:167], a[184:187], a[20:23], v[164:167]\n"
    "ds_read_b64_tr_b16 a[184:185], v247 offset:16384\n"
    "v_mfma_f32_16x16x32_bf16 v[164:167], a[188:191], a[24:27], v[164:167]\n"
    "s_waitcnt lgkmcnt(5)\n"
    "ds_read_b64_tr_b16 a[186:187], v247 offset:18432\n"
    "v_mfma_f32_16x16x32_bf16 a[248:251], a[160:163], v[200:203], a[248:251]\n"
    "ds_read_b64_tr_b16 a[188:189], v247 offset:24576\n"
    "v_mfma_f32_16x16x32_bf16 a[248:251], a[164:167], v[204:207], a[248:251]\n"
    "ds_read_b64_tr_b16 a[190:191], v247 offset:26624\n"
    "v_mfma_f32_16x16x32_bf16 a[252:255], a[164:167], v[212:215], a[252:255]\n"
    "v_exp_f32_e32 v162, v162\n"
    "v_mfma_f32_16x16x32_bf16 a[252:255], a[160:163], v[208:211], a[252:255]\n"
    "v_exp_f32_e32 v163, v163\n"
    "v_mfma_f32_16x16x32_bf16 v[56:59], a[160:163], v[216:219], v[56:59]\n"
    "v_cvt_pk_bf16_f32 v160, v160, v161\n"
    "v_mfma_f32_16x16x32_bf16 v[56:59], a[164:167], v[220:223], v[56:59]\n"
    "v_mfma_f32_16x16x32_bf16 v[60:63], a[164:167], v[228:231], v[60:63]\n"
    "v_cvt_pk_bf16_f32 v161, v162, v163\n"
    "v_mfma_f32_16x16x32_bf16 v[60:63], a[160:163], v[224:227], v[60:63]\n"
    "v_exp_f32_e32 v196, v196\n"
    "v_mfma_f32_16x16x32_bf16 v[64:67], a[160:163], v[232:235], v[64:67]\n"
    "ds_read_b64_tr_b16 a[160:161], v244 offset:20480\n"
    "v_mfma_f32_16x16x32_bf16 v[64:67], a[164:167], v[236:239], v[64:67]\n"
    "ds_read_b64_tr_b16 a[162:163], v244 offset:22528\n"
    "v_mfma_f32_16x16x32_bf16 v[76:79], a[168:171], v[232:235], v[76:79]\n"
    "ds_read_b64_tr_b16 a[164:165], v244 offset:28672\n"
    "v_mfma_f32_16x16x32_bf16 v[76:79], a[172:175], v[236:239], v[76:79]\n"
    "ds_read_b64_tr_b16 a[166:167], v244 offset:30720\n"
    "v_mfma_f32_16x16x32_bf16 v[72:75], a[172:175], v[228:231], v[72:75]\n"
    "v_exp_f32_e32 v197, v197\n"
    "v_mfma_f32_16x16x32_bf16 v[72:75], a[168:171], v[224:227], v[72:75]\n"
    "v_exp_f32_e32 v198, v198\n"
    "v_mfma_f32_16x16x32_bf16 v[68:71], a[168:171], v[216:219], v[68:71]\n"
    "v_exp_f32_e32 v199, v199\n"
    "v_mfma_f32_16x16x32_bf16 v[68:71], a[172:175], v[220:223], v[68:71]\n"
    "v_cvt_pk_bf16_f32 v194, v196, v197\n"
    "v_mfma_f32_16x16x32_bf16 v[4:7], a[172:175], v[212:215], v[4:7]\n"
    "v_exp_f32_e32 v188, v188\n"
    "v_mfma_f32_16x16x32_bf16 v[4:7], a[168:171], v[208:211], v[4:7]\n"
    "v_exp_f32_e32 v189, v189\n"
    "v_mfma_f32_16x16x32_bf16 v[0:3], a[168:171], v[200:203], v[0:3]\n"
    "ds_read_b64_tr_b16 a[168:169], v245 offset:20480\n"
    "v_mfma_f32_16x16x32_bf16 v[0:3], a[172:175], v[204:207], v[0:3]\n"
    "s_waitcnt lgkmcnt(5)\n"
    "ds_read_b64_tr_b16 a[170:171], v245 offset:22528\n"
    "v_mfma_f32_16x16x32_bf16 v[8:11], a[176:179], v[200:203], v[8:11]\n"
    "ds_read_b64_tr_b16 a[172:173], v245 offset:28672\n"
    "v_mfma_f32_16x16x32_bf16 v[8:11], a[180:183], v[204:207], v[8:11]\n"
    "ds_read_b64_tr_b16 a[174:175], v245 offset:30720\n"
    "v_mfma_f32_16x16x32_bf16 v[12:15], a[180:183], v[212:215], v[12:15]\n"
    "v_cvt_pk_bf16_f32 v195, v198, v199\n"
    "v_mfma_f32_16x16x32_bf16 v[12:15], a[176:179], v[208:211], v[12:15]\n"
    "v_exp_f32_e32 v190, v190\n"
    "v_mfma_f32_16x16x32_bf16 v[80:83], a[176:179], v[216:219], v[80:83]\n"
    "v_exp_f32_e32 v191, v191\n"
    "v_mfma_f32_16x16x32_bf16 v[80:83], a[180:183], v[220:223], v[80:83]\n"
    "v_cvt_pk_bf16_f32 v186, v188, v189\n"
    "v_mfma_f32_16x16x32_bf16 v[84:87], a[180:183], v[228:231], v[84:87]\n"
    "v_exp_f32_e32 v180, v180\n"
    "v_mfma_f32_16x16x32_bf16 v[84:87], a[176:179], v[224:227], v[84:87]\n"
    "v_exp_f32_e32 v181, v181\n"
    "v_mfma_f32_16x16x32_bf16 v[88:91], a[176:179], v[232:235], v[88:91]\n"
    "ds_read_b64_tr_b16 a[176:177], v246 offset:20480\n"
    "v_mfma_f32_16x16x32_bf16 v[88:91], a[180:183], v[236:239], v[88:91]\n"
    "ds_read_b64_tr_b16 a[178:179], v246 offset:22528\n"
    "v_mfma_f32_16x16x32_bf16 v[100:103], a[184:187], v[232:235], v[100:103]\n"
    "ds_read_b64_tr_b16 a[180:181], v246 offset:28672\n"
    "v_mfma_f32_16x16x32_bf16 v[100:103], a[188:191], v[236:239], v[100:103]\n"
    "ds_read_b64_tr_b16 a[182:183], v246 offset:30720\n"
    "v_mfma_f32_16x16x32_bf16 v[96:99], a[188:191], v[228:231], v[96:99]\n"
    "v_cvt_pk_bf16_f32 v187, v190, v191\n"
    "v_mfma_f32_16x16x32_bf16 v[96:99], a[184:187], v[224:227], v[96:99]\n"
    "v_exp_f32_e32 v182, v182\n"
    "v_mfma_f32_16x16x32_bf16 v[92:95], a[184:187], v[216:219], v[92:95]\n"
    "v_exp_f32_e32 v183, v183\n"
    "v_mfma_f32_16x16x32_bf16 v[92:95], a[188:191], v[220:223], v[92:95]\n"
    "v_mfma_f32_16x16x32_bf16 v[20:23], a[188:191], v[212:215], v[20:23]\n"
    "v_cvt_pk_bf16_f32 v178, v180, v181\n"
    "v_mfma_f32_16x16x32_bf16 v[20:23], a[184:187], v[208:211], v[20:23]\n"
    "v_exp_f32_e32 v172, v172\n"
    "v_mfma_f32_16x16x32_bf16 v[16:19], a[184:187], v[200:203], v[16:19]\n"
    "ds_read_b64_tr_b16 a[184:185], v247 offset:20480\n"
    "v_mfma_f32_16x16x32_bf16 v[16:19], a[188:191], v[204:207], v[16:19]\n"
    "s_waitcnt lgkmcnt(5)\n"
    "s_mov_b32 m0, s49\n"
    "ds_read_b64_tr_b16 a[186:187], v247 offset:22528\n"
    "v_mfma_f32_16x16x32_bf16 v[24:27], a[160:163], v[200:203], v[24:27]\n"
    "ds_read_b64_tr_b16 a[188:189], v247 offset:28672\n"
    "v_mfma_f32_16x16x32_bf16 v[24:27], a[164:167], v[204:207], v[24:27]\n"
    "ds_read_b64_tr_b16 a[190:191], v247 offset:30720\n"
    "v_mfma_f32_16x16x32_bf16 v[28:31], a[164:167], v[212:215], v[28:31]\n"
    "v_exp_f32_e32 v173, v173\n"
    "v_mfma_f32_16x16x32_bf16 v[28:31], a[160:163], v[208:211], v[28:31]\n"
    "v_cvt_pk_bf16_f32 v179, v182, v183\n"
    "v_mfma_f32_16x16x32_bf16 v[104:107], a[160:163], v[216:219], v[104:107]\n"
    "v_exp_f32_e32 v174, v174\n"
    "v_mfma_f32_16x16x32_bf16 v[104:107], a[164:167], v[220:223], v[104:107]\n"
    "v_exp_f32_e32 v175, v175\n"
    "v_mfma_f32_16x16x32_bf16 v[108:111], a[164:167], v[228:231], v[108:111]\n"
    "v_cvt_pk_bf16_f32 v170, v172, v173\n"
    "v_mfma_f32_16x16x32_bf16 v[108:111], a[160:163], v[224:227], v[108:111]\n"
    "s_waitcnt vmcnt(8)\n"
    "s_barrier\n"
    "v_mfma_f32_16x16x32_bf16 v[112:115], a[160:163], v[232:235], v[112:115]\n"
    "ds_read_b128 a[160:163], v243 offset:53248\n"
    "v_mfma_f32_16x16x32_bf16 v[112:115], a[164:167], v[236:239], v[112:115]\n"
    "ds_read_b128 a[164:167], v240 offset:49152\n"
    "v_mfma_f32_16x16x32_bf16 v[124:127], a[168:171], v[232:235], v[124:127]\n"
    "v_exp_f32_e32 v164, v164\n"
    "v_mfma_f32_16x16x32_bf16 v[124:127], a[172:175], v[236:239], v[124:127]\n"
    "v_exp_f32_e32 v165, v165\n"
    "v_mfma_f32_16x16x32_bf16 v[120:123], a[172:175], v[228:231], v[120:123]\n"
    "ds_write_addtid_b32 v192 offset:4096\n"
    "v_cvt_pk_bf16_f32 v171, v174, v175\n"
    "v_mfma_f32_16x16x32_bf16 v[120:123], a[168:171], v[224:227], v[120:123]\n"
    "ds_write_addtid_b32 v193 offset:4352\n"
    "v_mfma_f32_16x16x32_bf16 v[116:119], a[168:171], v[216:219], v[116:119]\n"
    "v_exp_f32_e32 v166, v166\n"
    "v_mfma_f32_16x16x32_bf16 v[116:119], a[172:175], v[220:223], v[116:119]\n"
    "v_exp_f32_e32 v167, v167\n"
    "v_mfma_f32_16x16x32_bf16 v[36:39], a[172:175], v[212:215], v[36:39]\n"
    "ds_write_addtid_b32 v184 offset:3072\n"
    "v_cvt_pk_bf16_f32 v162, v164, v165\n"
    "v_mfma_f32_16x16x32_bf16 v[36:39], a[168:171], v[208:211], v[36:39]\n"
    "ds_write_addtid_b32 v185 offset:3328\n"
    "v_cvt_pk_bf16_f32 v163, v166, v167\n"
    "v_mfma_f32_16x16x32_bf16 v[32:35], a[168:171], v[200:203], v[32:35]\n"
    "ds_read_b128 a[168:171], v241 offset:49152\n"
    "v_mfma_f32_16x16x32_bf16 v[32:35], a[172:175], v[204:207], v[32:35]\n"
    "s_waitcnt lgkmcnt(7)\n"
    "ds_read_b128 a[172:175], v240 offset:53248\n"
    "v_mfma_f32_16x16x32_bf16 v[40:43], a[176:179], v[200:203], v[40:43]\n"
    "ds_write_addtid_b32 v176 offset:2048\n"
    "v_mfma_f32_16x16x32_bf16 v[40:43], a[180:183], v[204:207], v[40:43]\n"
    "ds_write_addtid_b32 v177 offset:2304\n"
    "v_mfma_f32_16x16x32_bf16 v[44:47], a[180:183], v[212:215], v[44:47]\n"
    "ds_write_addtid_b32 v168 offset:1024\n"
    "v_mfma_f32_16x16x32_bf16 v[44:47], a[176:179], v[208:211], v[44:47]\n"
    "ds_write_addtid_b32 v169 offset:1280\n"
    "v_mfma_f32_16x16x32_bf16 v[128:131], a[176:179], v[216:219], v[128:131]\n"
    "ds_write_addtid_b32 v160 offset:0\n"
    "v_mfma_f32_16x16x32_bf16 v[128:131], a[180:183], v[220:223], v[128:131]\n"
    "ds_write_addtid_b32 v161 offset:256\n"
    "v_mfma_f32_16x16x32_bf16 v[132:135], a[180:183], v[228:231], v[132:135]\n"
    "ds_write_addtid_b32 v194 offset:4608\n"
    "v_mfma_f32_16x16x32_bf16 v[132:135], a[176:179], v[224:227], v[132:135]\n"
    "ds_write_addtid_b32 v195 offset:4864\n"
    "v_mfma_f32_16x16x32_bf16 v[136:139], a[176:179], v[232:235], v[136:139]\n"
    "ds_read_b128 a[176:179], v241 offset:53248\n"
    "v_mfma_f32_16x16x32_bf16 v[136:139], a[180:183], v[236:239], v[136:139]\n"
    "ds_read_b128 a[180:183], v242 offset:49152\n"
    "v_mfma_f32_16x16x32_bf16 v[148:151], a[184:187], v[232:235], v[148:151]\n"
    "ds_write_addtid_b32 v186 offset:3584\n"
    "v_mfma_f32_16x16x32_bf16 v[148:151], a[188:191], v[236:239], v[148:151]\n"
    "ds_write_addtid_b32 v187 offset:3840\n"
    "v_mfma_f32_16x16x32_bf16 v[144:147], a[188:191], v[228:231], v[144:147]\n"
    "ds_write_addtid_b32 v178 offset:2560\n"
    "v_mfma_f32_16x16x32_bf16 v[144:147], a[184:187], v[224:227], v[144:147]\n"
    "ds_write_addtid_b32 v179 offset:2816\n"
    "v_mfma_f32_16x16x32_bf16 v[140:143], a[184:187], v[216:219], v[140:143]\n"
    "ds_write_addtid_b32 v170 offset:1536\n"
    "v_mfma_f32_16x16x32_bf16 v[140:143], a[188:191], v[220:223], v[140:143]\n"
    "ds_write_addtid_b32 v171 offset:1792\n"
    "v_mfma_f32_16x16x32_bf16 v[52:55], a[188:191], v[212:215], v[52:55]\n"
    "ds_write_addtid_b32 v162 offset:512\n"
    "v_mfma_f32_16x16x32_bf16 v[52:55], a[184:187], v[208:211], v[52:55]\n"
    "ds_write_addtid_b32 v163 offset:768\n"
    "v_mfma_f32_16x16x32_bf16 v[48:51], a[184:187], v[200:203], v[48:51]\n"
    "ds_read_b128 a[184:187], v243 offset:49152\n"
    "v_mfma_f32_16x16x32_bf16 v[48:51], a[188:191], v[204:207], v[48:51]\n"
    "s_waitcnt lgkmcnt(15)\n"
    "ds_read_b128 a[188:191], v242 offset:53248\n"
    ".Lat_p2e:\n"
    "v_mfma_f32_16x16x32_bf16 v[200:203], a[160:163], a[28:31], 0\n"
    "v_mfma_f32_16x16x32_bf16 v[200:203], a[164:167], a[0:3], v[200:203]\n"
    "v_mfma_f32_16x16x32_bf16 v[208:211], a[164:167], a[32:35], 0\n"
    "v_mfma_f32_16x16x32_bf16 v[208:211], a[160:163], a[60:63], v[208:211]\n"
    "v_mfma_f32_16x16x32_bf16 v[216:219], a[160:163], a[92:95], 0\n"
    "v_mfma_f32_16x16x32_bf16 v[216:219], a[164:167], a[64:67], v[216:219]\n"
    "v_mfma_f32_16x16x32_bf16 v[224:227], a[164:167], a[96:99], 0\n"
    "s_add_u32 m0, s43, 16384\n"
    "v_mfma_f32_16x16x32_bf16 v[224:227], a[160:163], a[124:127], v[224:227]\n"
    "s_waitcnt lgkmcnt(0)\n"
    "s_barrier\n"
    "v_mfma_f32_16x16x32_bf16 v[232:235], a[160:163], a[156:159], 0\n"
    "ds_read_b128 a[160:163], v243 offset:55296\n"
    "v_mfma_f32_16x16x32_bf16 v[232:235], a[164:167], a[128:131], v[232:235]\n"
    "ds_read_b128 a[164:167], v240 offset:51200\n"
    "v_mfma_f32_16x16x32_bf16 v[232:235], a[168:171], a[132:135], v[232:235]\n"
    "buffer_load_dwordx4 v248, s[8:11], s40 offen offset:0 sc1 lds\n"
    "v_mfma_f32_16x16x32_bf16 v[232:235], a[172:175], a[136:139], v[232:235]\n"
    "ds_read2st64_b32 v[164:165], v250 offset0:0 offset1:1\n"
    "v_mfma_f32_16x16x32_bf16 v[224:227], a[172:175], a[104:107], v[224:227]\n"
    "v_mfma_f32_16x16x32_bf16 v[224:227], a[168:171], a[100:103], v[224:227]\n"
    "ds_read2st64_b32 v[166:167], v250 offset0:2 offset1:3\n"
    "v_mfma_f32_16x16x32_bf16 v[216:219], a[168:171], a[68:71], v[216:219]\n"
    "buffer_load_dwordx4 v248, s[8:11], s40 offen offset:1024 sc1 lds\n"
    "v_mfma_f32_16x16x32_bf16 v[216:219], a[172:175], a[72:75], v[216:219]\n"
    "v_mfma_f32_16x16x32_bf16 v[208:211], a[172:175], a[40:43], v[208:211]\n"
    "v_mfma_f32_16x16x32_bf16 v[208:211], a[168:171], a[36:39], v[208:211]\n"
    "ds_read2st64_b32 v[172:173], v250 offset0:4 offset1:5\n"
    "v_mfma_f32_16x16x32_bf16 v[200:203], a[168:171], a[4:7], v[200:203]\n"
    "ds_read_b128 a[168:171], v241 offset:51200\n"
    "buffer_load_dwordx4 v248, s[8:11], s40 offen offset:2048 sc1 lds\n"
    "v_mfma_f32_16x16x32_bf16 v[200:203], a[172:175], a[8:11], v[200:203]\n"
    "ds_read_b128 a[172:175], v240 offset:55296\n"
    "v_mfma_f32_16x16x32_bf16 v[200:203], a[176:179], a[12:15], v[200:203]\n"
    "v_mfma_f32_16x16x32_bf16 v[200:203], a[180:183], a[16:19], v[200:203]\n"
    "ds_read2st64_b32 v[174:175], v250 offset0:6 offset1:7\n"
    "v_mfma_f32_16x16x32_bf16 v[208:211], a[180:183], a[48:51], v[208:211]\n"
    "buffer_load_dwordx4 v248, s[8:11], s40 offen offset:3072 sc1 lds\n"
    "v_mfma_f32_16x16x32_bf16 v[208:211], a[176:179], a[44:47], v[208:211]\n"
    "s_add_u32 m0, s43, 20480\n"
    "v_mfma_f32_16x16x32_bf16 v[216:219], a[176:179], a[76:79], v[216:219]\n"
    "s_add_u32 s40, s40, 0x8000\n"
    "v_mfma_f32_16x16x32_bf16 v[216:219], a[180:183], a[80:83], v[216:219]\n"
    "ds_read2st64_b32 v[180:181], v250 offset0:8 offset1:9\n"
    "v_mfma_f32_16x16x32_bf16 v[224:227], a[180:183], a[112:115], v[224:227]\n"
    "buffer_load_dwordx4 v248, s[8:11], s41 offen offset:0 sc1 lds\n"
    "v_mfma_f32_16x16x32_bf16 v[224:227], a[176:179], a[108:111], v[224:227]\n"
    "ds_read2st64_b32 v[182:183], v250 offset0:10 offset1:11\n"
    "v_mfma_f32_16x16x32_bf16 v[232:235], a[176:179], a[140:143], v[232:235]\n"
    "ds_read_b128 a[176:179], v241 offset:55296\n"
    "v_mfma_f32_16x16x32_bf16 v[232:235], a[180:183], a[144:147], v[232:235]\n"
    "ds_read_b128 a[180:183], v242 offset:51200\n"
    "v_mfma_f32_16x16x32_bf16 v[232:235], a[184:187], a[148:151], v[232:235]\n"
    "buffer_load_dwordx4 v248, s[8:11], s41 offen offset:1024 sc1 lds\n"
    "v_mfma_f32_16x16x32_bf16 v[232:235], a[188:191], a[152:155], v[232:235]\n"
    "v_mfma_f32_16x16x32_bf16 v[224:227], a[188:191], a[120:123], v[224:227]\n"
    "ds_read2st64_b32 v[188:189], v250 offset0:12 offset1:13\n"
    "v_mfma_f32_16x16x32_bf16 v[224:227], a[184:187], a[116:119], v[224:227]\n"
    "v_mfma_f32_16x16x32_bf16 v[216:219], a[184:187], a[84:87], v[216:219]\n"
    "buffer_load_dwordx4 v248, s[8:11], s41 offen offset:2048 sc1 lds\n"
    "v_mfma_f32_16x16x32_bf16 v[216:219], a[188:191], a[88:91], v[216:219]\n"
    "v_exp_f32_e32 v232, v232\n"
    "v_mfma_f32_16x16x32_bf16 v[208:211], a[188:191], a[56:59], v[208:211]\n"
    "ds_read2st64_b32 v[190:191], v250 offset0:14 offset1:15\n"
    "v_mfma_f32_16x16x32_bf16 v[208:211], a[184:187], a[52:55], v[208:211]\n"
    "v_exp_f32_e32 v233, v233\n"
    "v_mfma_f32_16x16x32_bf16 v[200:203], a[184:187], a[20:23], v[200:203]\n"
    "ds_read_b128 a[184:187], v243 offset:51200\n"
    "buffer_load_dwordx4 v248, s[8:11], s41 offen offset:3072 sc1 lds\n"
    "v_mfma_f32_16x16x32_bf16 v[200:203], a[188:191], a[24:27], v[200:203]\n"
    "s_waitcnt lgkmcnt(8)\n"
    "s_add_u32 s41, s41, 0x8000\n"
    "ds_read_b128 a[188:191], v242 offset:55296\n"
    "v_mfma_f32_16x16x32_bf16 v[204:207], a[160:163], a[28:31], 0\n"
    "ds_read2st64_b32 v[196:197], v250 offset0:16 offset1:17\n"
    "v_mfma_f32_16x16x32_bf16 v[204:207], a[164:167], a[0:3], v[204:207]\n"
    "v_exp_f32_e32 v234, v234\n"
    "v_mfma_f32_16x16x32_bf16 v[212:215], a[164:167], a[32:35], 0\n"
    "v_exp_f32_e32 v235, v235\n"
    "v_mfma_f32_16x16x32_bf16 v[212:215], a[160:163], a[60:63], v[212:215]\n"
    "ds_read2st64_b32 v[198:199], v250 offset0:18 offset1:19\n"
    "v_mfma_f32_16x16x32_bf16 v[220:223], a[160:163], a[92:95], 0\n"
    "v_cvt_pk_bf16_f32 v232, v232, v233\n"
    "v_mfma_f32_16x16x32_bf16 v[220:223], a[164:167], a[64:67], v[220:223]\n"
    "v_exp_f32_e32 v224, v224\n"
    "v_mfma_f32_16x16x32_bf16 v[228:231], a[164:167], a[96:99], 0\n"
    "v_exp_f32_e32 v225, v225\n"
    "v_mfma_f32_16x16x32_bf16 v[228:231], a[160:163], a[124:127], v[228:231]\n"
    "v_cvt_pk_bf16_f32 v233, v234, v235\n"
    "v_mfma_f32_16x16x32_bf16 v[236:239], a[160:163], a[156:159], 0\n"
    "ds_read_b64_tr_b16 a[160:161], v244 offset:32768\n"
    "v_mfma_f32_16x16x32_bf16 v[236:239], a[164:167], a[128:131], v[236:239]\n"
    "ds_read_b64_tr_b16 a[162:163], v244 offset:34816\n"
    "v_mfma_f32_16x16x32_bf16 v[236:239], a[168:171], a[132:135], v[236:239]\n"
    "ds_read_b64_tr_b16 a[164:165], v244 offset:40960\n"
    "v_mfma_f32_16x16x32_bf16 v[236:239], a[172:175], a[136:139], v[236:239]\n"
    "ds_read_b64_tr_b16 a[166:167], v244 offset:43008\n"
    "v_mfma_f32_16x16x32_bf16 v[228:231], a[172:175], a[104:107], v[228:231]\n"
    "v_exp_f32_e32 v226, v226\n"
    "v_mfma_f32_16x16x32_bf16 v[228:231], a[168:171], a[100:103], v[228:231]\n"
    "v_exp_f32_e32 v227, v227\n"
    "v_mfma_f32_16x16x32_bf16 v[220:223], a[168:171], a[68:71], v[220:223]\n"
    "v_cvt_pk_bf16_f32 v224, v224, v225\n"
    "v_mfma_f32_16x16x32_bf16 v[220:223], a[172:175], a[72:75], v[220:223]\n"
    "v_exp_f32_e32 v216, v216\n"
    "v_mfma_f32_16x16x32_bf16 v[212:215], a[172:175], a[40:43], v[212:215]\n"
    "v_exp_f32_e32 v217, v217\n"
    "v_mfma_f32_16x16x32_bf16 v[212:215], a[168:171], a[36:39], v[212:215]\n"
    "v_cvt_pk_bf16_f32 v225, v226, v227\n"
    "v_mfma_f32_16x16x32_bf16 v[204:207], a[168:171], a[4:7], v[204:207]\n"
    "ds_read_b64_tr_b16 a[168:169], v245 offset:32768\n"
    "v_mfma_f32_16x16x32_bf16 v[204:207], a[172:175], a[8:11], v[204:207]\n"
    "s_waitcnt lgkmcnt(7)\n"
    "ds_read_b64_tr_b16 a[170:171], v245 offset:34816\n"
    "v_mfma_f32_16x16x32_bf16 v[204:207], a[176:179], a[12:15], v[204:207]\n"
    "ds_read_b64_tr_b16 a[172:173], v245 offset:40960\n"
    "v_mfma_f32_16x16x32_bf16 v[204:207], a[180:183], a[16:19], v[204:207]\n"
    "ds_read_b64_tr_b16 a[174:175], v245 offset:43008\n"
    "v_mfma_f32_16x16x32_bf16 v[212:215], a[180:183], a[48:51], v[212:215]\n"
    "v_exp_f32_e32 v218, v218\n"
    "v_mfma_f32_16x16x32_bf16 v[212:215], a[176:179], a[44:47], v[212:215]\n"
    "v_exp_f32_e32 v219, v219\n"
    "v_mfma_f32_16x16x32_bf16 v[220:223], a[176:179], a[76:79], v[220:223]\n"
    "v_cvt_pk_bf16_f32 v216, v216, v217\n"
    "v_mfma_f32_16x16x32_bf16 v[220:223], a[180:183], a[80:83], v[220:223]\n"
    "v_exp_f32_e32 v208, v208\n"
    "v_mfma_f32_16x16x32_bf16 v[228:231], a[180:183], a[112:115], v[228:231]\n"
    "v_exp_f32_e32 v209, v209\n"
    "v_mfma_f32_16x16x32_bf16 v[228:231], a[176:179], a[108:111], v[228:231]\n"
    "v_cvt_pk_bf16_f32 v217, v218, v219\n"
    "v_mfma_f32_16x16x32_bf16 v[236:239], a[176:179], a[140:143], v[236:239]\n"
    "ds_read_b64_tr_b16 a[176:177], v246 offset:32768\n"
    "v_mfma_f32_16x16x32_bf16 v[236:239], a[180:183], a[144:147], v[236:239]\n"
    "ds_read_b64_tr_b16 a[178:179], v246 offset:34816\n"
    "v_mfma_f32_16x16x32_bf16 v[236:239], a[184:187], a[148:151], v[236:239]\n"
    "ds_read_b64_tr_b16 a[180:181], v246 offset:40960\n"
    "v_mfma_f32_16x16x32_bf16 v[236:239], a[188:191], a[152:155], v[236:239]\n"
    "ds_read_b64_tr_b16 a[182:183], v246 offset:43008\n"
    "v_mfma_f32_16x16x32_bf16 v[228:231], a[188:191], a[120:123], v[228:231]\n"
    "v_exp_f32_e32 v210, v210\n"
    "v_mfma_f32_16x16x32_bf16 v[228:231], a[184:187], a[116:119], v[228:231]\n"
    "v_exp_f32_e32 v211, v211\n"
    "v_mfma_f32_16x16x32_bf16 v[220:223], a[184:187], a[84:87], v[220:223]\n"
    "v_cvt_pk_bf16_f32 v208, v208, v209\n"
    "v_mfma_f32_16x16x32_bf16 v[220:223], a[188:191], a[88:91], v[220:223]\n"
    "v_exp_f32_e32 v200, v200\n"
    "v_mfma_f32_16x16x32_bf16 v[212:215], a[188:191], a[56:59], v[212:215]\n"
    "v_exp_f32_e32 v201, v201\n"
    "v_mfma_f32_16x16x32_bf16 v[212:215], a[184:187], a[52:55], v[212:215]\n"
    "v_cvt_pk_bf16_f32 v209, v210, v211\n"
    "v_mfma_f32_16x16x32_bf16 v[204:207], a[184:187], a[20:23], v[204:207]\n"
    "ds_read_b64_tr_b16 a[184:185], v247 offset:32768\n"
    "v_mfma_f32_16x16x32_bf16 v[204:207], a[188:191], a[24:27], v[204:207]\n"
    "s_waitcnt lgkmcnt(5)\n"
    "ds_read_b64_tr_b16 a[186:187], v247 offset:34816\n"
    "v_mfma_f32_16x16x32_bf16 a[248:251], a[160:163], v[160:163], a[248:251]\n"
    "ds_read_b64_tr_b16 a[188:189], v247 offset:40960\n"
    "v_mfma_f32_16x16x32_bf16 a[248:251], a[164:167], v[164:167], a[248:251]\n"
    "ds_read_b64_tr_b16 a[190:191], v247 offset:43008\n"
    "v_mfma_f32_16x16x32_bf16 a[252:255], a[164:167], v[172:175], a[252:255]\n"
    "v_exp_f32_e32 v202, v202\n"
    "v_mfma_f32_16x16x32_bf16 a[252:255], a[160:163], v[168:171], a[252:255]\n"
    "v_exp_f32_e32 v203, v203\n"
    "v_mfma_f32_16x16x32_bf16 v[56:59], a[160:163], v[176:179], v[56:59]\n"
    "v_cvt_pk_bf16_f32 v200, v200, v201\n"
    "v_mfma_f32_16x16x32_bf16 v[56:59], a[164:167], v[180:183], v[56:59]\n"
    "v_mfma_f32_16x16x32_bf16 v[60:63], a[164:167], v[188:191], v[60:63]\n"
    "v_cvt_pk_bf16_f32 v201, v202, v203\n"
    "v_mfma_f32_16x16x32_bf16 v[60:63], a[160:163], v[184:187], v[60:63]\n"
    "v_exp_f32_e32 v236, v236\n"
    "v_mfma_f32_16x16x32_bf16 v[64:67], a[160:163], v[192:195], v[64:67]\n"
    "ds_read_b64_tr_b16 a[160:161], v244 offset:36864\n"
    "v_mfma_f32_16x16x32_bf16 v[64:67], a[164:167], v[196:199], v[64:67]\n"
    "ds_read_b64_tr_b16 a[162:163], v244 offset:38912\n"
    "v_mfma_f32_16x16x32_bf16 v[76:79], a[168:171], v[192:195], v[76:79]\n"
    "ds_read_b64_tr_b16 a[164:165], v244 offset:45056\n"
    "v_mfma_f32_16x16x32_bf16 v[76:79], a[172:175], v[196:199], v[76:79]\n"
    "ds_read_b64_tr_b16 a[166:167], v244 offset:47104\n"
    "v_mfma_f32_16x16x32_bf16 v[72:75], a[172:175], v[188:191], v[72:75]\n"
    "v_exp_f32_e32 v237, v237\n"
    "v_mfma_f32_16x16x32_bf16 v[72:75], a[168:171], v[184:187], v[72:75]\n"
    "v_exp_f32_e32 v238, v238\n"
    "v_mfma_f32_16x16x32_bf16 v[68:71], a[168:171], v[176:179], v[68:71]\n"
    "v_exp_f32_e32 v239, v239\n"
    "v_mfma_f32_16x16x32_bf16 v[68:71], a[172:175], v[180:183], v[68:71]\n"
    "v_cvt_pk_bf16_f32 v234, v236, v237\n"
    "v_mfma_f32_16x16x32_bf16 v[4:7], a[172:175], v[172:175], v[4:7]\n"
    "v_exp_f32_e32 v228, v228\n"
    "v_mfma_f32_16x16x32_bf16 v[4:7], a[168:171], v[168:171], v[4:7]\n"
    "v_exp_f32_e32 v229, v229\n"
    "v_mfma_f32_16x16x32_bf16 v[0:3], a[168:171], v[160:163], v[0:3]\n"
    "ds_read_b64_tr_b16 a[168:169], v245 offset:36864\n"
    "v_mfma_f32_16x16x32_bf16 v[0:3], a[172:175], v[164:167], v[0:3]\n"
    "s_waitcnt lgkmcnt(5)\n"
    "ds_read_b64_tr_b16 a[170:171], v245 offset:38912\n"
    "v_mfma_f32_16x16x32_bf16 v[8:11], a[176:179], v[160:163], v[8:11]\n"
    "ds_read_b64_tr_b16 a[172:173], v245 offset:45056\n"
    "v_mfma_f32_16x16x32_bf16 v[8:11], a[180:183], v[164:167], v[8:11]\n"
    "ds_read_b64_tr_b16 a[174:175], v245 offset:47104\n"
    "v_mfma_f32_16x16x32_bf16 v[12:15], a[180:183], v[172:175], v[12:15]\n"
    "s_or_b32 s45, s44, s37\n"
    "s_cselect_b32 s46, 2.0, s81\n"
    "s_cselect_b32 s45, 2.0, s80\n"
    "v_mfma_f32_16x16x32_bf16 v[12:15], a[176:179], v[168:171], v[12:15]\n"
    "v_cvt_pk_bf16_f32 v235, v238, v239\n"
    "v_mfma_f32_16x16x32_bf16 v[80:83], a[176:179], v[176:179], v[80:83]\n"
    "v_exp_f32_e32 v230, v230\n"
    "v_mfma_f32_16x16x32_bf16 v[80:83], a[180:183], v[180:183], v[80:83]\n"
    "v_exp_f32_e32 v231, v231\n"
    "v_mfma_f32_16x16x32_bf16 v[84:87], a[180:183], v[188:191], v[84:87]\n"
    "v_cvt_pk_bf16_f32 v226, v228, v229\n"
    "v_mfma_f32_16x16x32_bf16 v[84:87], a[176:179], v[184:187], v[84:87]\n"
    "v_exp_f32_e32 v220, v220\n"
    "v_mfma_f32_16x16x32_bf16 v[88:91], a[176:179], v[192:195], v[88:91]\n"
    "ds_read_b64_tr_b16 a[176:177], v246 offset:36864\n"
    "v_mfma_f32_16x16x32_bf16 v[88:91], a[180:183], v[196:199], v[88:91]\n"
    "ds_read_b64_tr_b16 a[178:179], v246 offset:38912\n"
    "v_mfma_f32_16x16x32_bf16 v[100:103], a[184:187], v[192:195], v[100:103]\n"
    "ds_read_b64_tr_b16 a[180:181], v246 offset:45056\n"
    "v_mfma_f32_16x16x32_bf16 v[100:103], a[188:191], v[196:199], v[100:103]\n"
    "ds_read_b64_tr_b16 a[182:183], v246 offset:47104\n"
    "v_mfma_f32_16x16x32_bf16 v[96:99], a[188:191], v[188:191], v[96:99]\n"
    "v_exp_f32_e32 v221, v221\n"
    "v_mfma_f32_16x16x32_bf16 v[96:99], a[184:187], v[184:187], v[96:99]\n"
    "v_cvt_pk_bf16_f32 v227, v230, v231\n"
    "v_mfma_f32_16x16x32_bf16 v[92:95], a[184:187], v[176:179], v[92:95]\n"
    "v_exp_f32_e32 v222, v222\n"
    "v_mfma_f32_16x16x32_bf16 v[92:95], a[188:191], v[180:183], v[92:95]\n"
    "v_exp_f32_e32 v223, v223\n"
    "v_mfma_f32_16x16x32_bf16 v[20:23], a[188:191], v[172:175], v[20:23]\n"
    "v_cvt_pk_bf16_f32 v218, v220, v221\n"
    "v_mfma_f32_16x16x32_bf16 v[20:23], a[184:187], v[168:171], v[20:23]\n"
    "v_exp_f32_e32 v212, v212\n"
    "v_mfma_f32_16x16x32_bf16 v[16:19], a[184:187], v[160:163], v[16:19]\n"
    "ds_read_b64_tr_b16 a[184:185], v247 offset:36864\n"
    "v_mfma_f32_16x16x32_bf16 v[16:19], a[188:191], v[164:167], v[16:19]\n"
    "s_waitcnt lgkmcnt(5)\n"
    "s_mov_b32 m0, s49\n"
    "ds_read_b64_tr_b16 a[186:187], v247 offset:38912\n"
    "v_mfma_f32_16x16x32_bf16 v[24:27], a[160:163], v[160:163], v[24:27]\n"
    "ds_read_b64_tr_b16 a[188:189], v247 offset:45056\n"
    "v_mfma_f32_16x16x32_bf16 v[24:27], a[164:167], v[164:167], v[24:27]\n"
    "ds_read_b64_tr_b16 a[190:191], v247 offset:47104\n"
    "v_mfma_f32_16x16x32_bf16 v[28:31], a[164:167], v[172:175], v[28:31]\n"
    "v_exp_f32_e32 v213, v213\n"
    "v_mfma_f32_16x16x32_bf16 v[28:31], a[160:163], v[168:171], v[28:31]\n"
    "v_cvt_pk_bf16_f32 v219, v222, v223\n"
    "v_mfma_f32_16x16x32_bf16 v[104:107], a[160:163], v[176:179], v[104:107]\n"
    "v_exp_f32_e32 v214, v214\n"
    "v_mfma_f32_16x16x32_bf16 v[104:107], a[164:167], v[180:183], v[104:107]\n"
    "v_exp_f32_e32 v215, v215\n"
    "v_mfma_f32_16x16x32_bf16 v[108:111], a[164:167], v[188:191], v[108:111]\n"
    "v_cvt_pk_bf16_f32 v210, v212, v213\n"
    "v_mfma_f32_16x16x32_bf16 v[108:111], a[160:163], v[184:187], v[108:111]\n"
    "s_waitcnt vmcnt(8)\n"
    "s_barrier\n"
    "v_mfma_f32_16x16x32_bf16 v[112:115], a[160:163], v[192:195], v[112:115]\n"
    "ds_read_b128 a[160:163], v243 offset:4096\n"
    "v_mfma_f32_16x16x32_bf16 v[112:115], a[164:167], v[196:199], v[112:115]\n"
    "ds_read_b128 a[164:167], v240 offset:0\n"
    "v_mfma_f32_16x16x32_bf16 v[124:127], a[168:171], v[192:195], v[124:127]\n"
    "v_exp_f32_e32 v204, v204\n"
    "v_mfma_f32_16x16x32_bf16 v[124:127], a[172:175], v[196:199], v[124:127]\n"
    "v_exp_f32_e32 v205, v205\n"
    "v_mfma_f32_16x16x32_bf16 v[120:123], a[172:175], v[188:191], v[120:123]\n"
    "ds_write_addtid_b32 v232 offset:4096\n"
    "v_cvt_pk_bf16_f32 v211, v214, v215\n"
    "v_mfma_f32_16x16x32_bf16 v[120:123], a[168:171], v[184:187], v[120:123]\n"
    "ds_write_addtid_b32 v233 offset:4352\n"
    "v_mfma_f32_16x16x32_bf16 v[116:119], a[168:171], v[176:179], v[116:119]\n"
    "v_exp_f32_e32 v206, v206\n"
    "v_mfma_f32_16x16x32_bf16 v[116:119], a[172:175], v[180:183], v[116:119]\n"
    "v_exp_f32_e32 v207, v207\n"
    "v_mfma_f32_16x16x32_bf16 v[36:39], a[172:175], v[172:175], v[36:39]\n"
    "ds_write_addtid_b32 v224 offset:3072\n"
    "v_cvt_pk_bf16_f32 v202, v204, v205\n"
    "v_mfma_f32_16x16x32_bf16 v[36:39], a[168:171], v[168:171], v[36:39]\n"
    "ds_write_addtid_b32 v225 offset:3328\n"
    "v_cvt_pk_bf16_f32 v203, v206, v207\n"
    "v_mfma_f32_16x16x32_bf16 v[32:35], a[168:171], v[160:163], v[32:35]\n"
    "ds_read_b128 a[168:171], v241 offset:0\n"
    "v_mfma_f32_16x16x32_bf16 v[32:35], a[172:175], v[164:167], v[32:35]\n"
    "s_waitcnt lgkmcnt(7)\n"
    "ds_read_b128 a[172:175], v240 offset:4096\n"
    "v_mfma_f32_16x16x32_bf16 v[40:43], a[176:179], v[160:163], v[40:43]\n"
    "buffer_load_dword v154, off, s[8:11], s45 sc1\n"
    "ds_write_addtid_b32 v216 offset:2048\n"
    "v_mfma_f32_16x16x32_bf16 v[40:43], a[180:183], v[164:167], v[40:43]\n"
    "ds_write_addtid_b32 v217 offset:2304\n"
    "v_mfma_f32_16x16x32_bf16 v[44:47], a[180:183], v[172:175], v[44:47]\n"
    "ds_write_addtid_b32 v208 offset:1024\n"
    "v_mfma_f32_16x16x32_bf16 v[44:47], a[176:179], v[168:171], v[44:47]\n"
    "ds_write_addtid_b32 v209 offset:1280\n"
    "v_mfma_f32_16x16x32_bf16 v[128:131], a[176:179], v[176:179], v[128:131]\n"
    "ds_write_addtid_b32 v200 offset:0\n"
    "v_mfma_f32_16x16x32_bf16 v[128:131], a[180:183], v[180:183], v[128:131]\n"
    "ds_write_addtid_b32 v201 offset:256\n"
    "v_mfma_f32_16x16x32_bf16 v[132:135], a[180:183], v[188:191], v[132:135]\n"
    "ds_write_addtid_b32 v234 offset:4608\n"
    "v_mfma_f32_16x16x32_bf16 v[132:135], a[176:179], v[184:187], v[132:135]\n"
    "ds_write_addtid_b32 v235 offset:4864\n"
    "v_mfma_f32_16x16x32_bf16 v[136:139], a[176:179], v[192:195], v[136:139]\n"
    "ds_read_b128 a[176:179], v241 offset:4096\n"
    "v_mfma_f32_16x16x32_bf16 v[136:139], a[180:183], v[196:199], v[136:139]\n"
    "ds_read_b128 a[180:183], v242 offset:0\n"
    "v_mfma_f32_16x16x32_bf16 v[148:151], a[184:187], v[192:195], v[148:151]\n"
    "ds_write_addtid_b32 v226 offset:3584\n"
    "v_mfma_f32_16x16x32_bf16 v[148:151], a[188:191], v[196:199], v[148:151]\n"
    "ds_write_addtid_b32 v227 offset:3840\n"
    "v_mfma_f32_16x16x32_bf16 v[144:147], a[188:191], v[188:191], v[144:147]\n"
    "ds_write_addtid_b32 v218 offset:2560\n"
    "v_mfma_f32_16x16x32_bf16 v[144:147], a[184:187], v[184:187], v[144:147]\n"
    "ds_write_addtid_b32 v219 offset:2816\n"
    "v_mfma_f32_16x16x32_bf16 v[140:143], a[184:187], v[176:179], v[140:143]\n"
    "ds_write_addtid_b32 v210 offset:1536\n"
    "v_mfma_f32_16x16x32_bf16 v[140:143], a[188:191], v[180:183], v[140:143]\n"
    "ds_write_addtid_b32 v211 offset:1792\n"
    "v_mfma_f32_16x16x32_bf16 v[52:55], a[188:191], v[172:175], v[52:55]\n"
    "ds_write_addtid_b32 v202 offset:512\n"
    "v_mfma_f32_16x16x32_bf16 v[52:55], a[184:187], v[168:171], v[52:55]\n"
    "ds_write_addtid_b32 v203 offset:768\n"
    "v_mfma_f32_16x16x32_bf16 v[48:51], a[184:187], v[160:163], v[48:51]\n"
    "ds_read_b128 a[184:187], v243 offset:0\n"
    "v_mfma_f32_16x16x32_bf16 v[48:51], a[188:191], v[164:167], v[48:51]\n"
    "s_waitcnt lgkmcnt(15)\n"
    "ds_read_b128 a[188:191], v242 offset:4096\n"
    "v_mfma_f32_16x16x32_bf16 v[160:163], a[160:163], a[28:31], 0\n"
    "v_lshlrev_b32_e32 v253, 2, v249\n"
    "v_mfma_f32_16x16x32_bf16 v[160:163], a[164:167], a[0:3], v[160:163]\n"
    "v_mfma_f32_16x16x32_bf16 v[168:171], a[164:167], a[32:35], 0\n"
    "v_mfma_f32_16x16x32_bf16 v[168:171], a[160:163], a[60:63], v[168:171]\n"
    "v_mfma_f32_16x16x32_bf16 v[176:179], a[160:163], a[92:95], 0\n"
    "v_mfma_f32_16x16x32_bf16 v[176:179], a[164:167], a[64:67], v[176:179]\n"
    "v_mfma_f32_16x16x32_bf16 v[184:187], a[164:167], a[96:99], 0\n"
    "s_add_u32 m0, s43, 32768\n"
    "v_mfma_f32_16x16x32_bf16 v[184:187], a[160:163], a[124:127], v[184:187]\n"
    "s_waitcnt lgkmcnt(0)\n"
    "s_barrier\n"
    "v_mfma_f32_16x16x32_bf16 v[192:195], a[160:163], a[156:159], 0\n"
    "ds_read_b128 a[160:163], v243 offset:6144\n"
    "v_mfma_f32_16x16x32_bf16 v[192:195], a[164:167], a[128:131], v[192:195]\n"
    "ds_read_b128 a[164:167], v240 offset:2048\n"
    "v_mfma_f32_16x16x32_bf16 v[192:195], a[168:171], a[132:135], v[192:195]\n"
    "buffer_load_dwordx4 v248, s[8:11], s40 offen offset:0 sc1 lds\n"
    "v_mfma_f32_16x16x32_bf16 v[192:195], a[172:175], a[136:139], v[192:195]\n"
    "ds_read2st64_b32 v[204:205], v250 offset0:0 offset1:1\n"
    "v_mfma_f32_16x16x32_bf16 v[184:187], a[172:175], a[104:107], v[184:187]\n"
    "v_mfma_f32_16x16x32_bf16 v[184:187], a[168:171], a[100:103], v[184:187]\n"
    "ds_read2st64_b32 v[206:207], v250 offset0:2 offset1:3\n"
    "v_mfma_f32_16x16x32_bf16 v[176:179], a[168:171], a[68:71], v[176:179]\n"
    "buffer_load_dwordx4 v248, s[8:11], s40 offen offset:1024 sc1 lds\n"
    "v_mfma_f32_16x16x32_bf16 v[176:179], a[172:175], a[72:75], v[176:179]\n"
    "v_mfma_f32_16x16x32_bf16 v[168:171], a[172:175], a[40:43], v[168:171]\n"
    "v_mfma_f32_16x16x32_bf16 v[168:171], a[168:171], a[36:39], v[168:171]\n"
    "ds_read2st64_b32 v[212:213], v250 offset0:4 offset1:5\n"
    "v_mfma_f32_16x16x32_bf16 v[160:163], a[168:171], a[4:7], v[160:163]\n"
    "ds_read_b128 a[168:171], v241 offset:2048\n"
    "buffer_load_dwordx4 v248, s[8:11], s40 offen offset:2048 sc1 lds\n"
    "v_mfma_f32_16x16x32_bf16 v[160:163], a[172:175], a[8:11], v[160:163]\n"
    "ds_read_b128 a[172:175], v240 offset:6144\n"
    "v_mfma_f32_16x16x32_bf16 v[160:163], a[176:179], a[12:15], v[160:163]\n"
    "v_mfma_f32_16x16x32_bf16 v[160:163], a[180:183], a[16:19], v[160:163]\n"
    "ds_read2st64_b32 v[214:215], v250 offset0:6 offset1:7\n"
    "v_mfma_f32_16x16x32_bf16 v[168:171], a[180:183], a[48:51], v[168:171]\n"
    "buffer_load_dwordx4 v248, s[8:11], s40 offen offset:3072 sc1 lds\n"
    "v_mfma_f32_16x16x32_bf16 v[168:171], a[176:179], a[44:47], v[168:171]\n"
    "s_add_u32 m0, s43, 36864\n"
    "v_mfma_f32_16x16x32_bf16 v[176:179], a[176:179], a[76:79], v[176:179]\n"
    "s_add_u32 s40, s40, 0x8000\n"
    "v_mfma_f32_16x16x32_bf16 v[176:179], a[180:183], a[80:83], v[176:179]\n"
    "ds_read2st64_b32 v[220:221], v250 offset0:8 offset1:9\n"
    "v_mfma_f32_16x16x32_bf16 v[184:187], a[180:183], a[112:115], v[184:187]\n"
    "buffer_load_dwordx4 v248, s[8:11], s41 offen offset:0 sc1 lds\n"
    "v_mfma_f32_16x16x32_bf16 v[184:187], a[176:179], a[108:111], v[184:187]\n"
    "ds_read2st64_b32 v[222:223], v250 offset0:10 offset1:11\n"
    "v_mfma_f32_16x16x32_bf16 v[192:195], a[176:179], a[140:143], v[192:195]\n"
    "ds_read_b128 a[176:179], v241 offset:6144\n"
    "v_mfma_f32_16x16x32_bf16 v[192:195], a[180:183], a[144:147], v[192:195]\n"
    "ds_read_b128 a[180:183], v242 offset:2048\n"
    "v_mfma_f32_16x16x32_bf16 v[192:195], a[184:187], a[148:151], v[192:195]\n"
    "buffer_load_dwordx4 v248, s[8:11], s41 offen offset:1024 sc1 lds\n"
    "v_mfma_f32_16x16x32_bf16 v[192:195], a[188:191], a[152:155], v[192:195]\n"
    "v_mfma_f32_16x16x32_bf16 v[184:187], a[188:191], a[120:123], v[184:187]\n"
    "ds_read2st64_b32 v[228:229], v250 offset0:12 offset1:13\n"
    "v_mfma_f32_16x16x32_bf16 v[184:187], a[184:187], a[116:119], v[184:187]\n"
    "v_mfma_f32_16x16x32_bf16 v[176:179], a[184:187], a[84:87], v[176:179]\n"
    "buffer_load_dwordx4 v248, s[8:11], s41 offen offset:2048 sc1 lds\n"
    "v_mfma_f32_16x16x32_bf16 v[176:179], a[188:191], a[88:91], v[176:179]\n"
    "v_exp_f32_e32 v192, v192\n"
    "v_mfma_f32_16x16x32_bf16 v[168:171], a[188:191], a[56:59], v[168:171]\n"
    "ds_read2st64_b32 v[230:231], v250 offset0:14 offset1:15\n"
    "v_mfma_f32_16x16x32_bf16 v[168:171], a[184:187], a[52:55], v[168:171]\n"
    "v_exp_f32_e32 v193, v193\n"
    "v_mfma_f32_16x16x32_bf16 v[160:163], a[184:187], a[20:23], v[160:163]\n"
    "ds_read_b128 a[184:187], v243 offset:2048\n"
    "buffer_load_dwordx4 v248, s[8:11], s41 offen offset:3072 sc1 lds\n"
    "v_mfma_f32_16x16x32_bf16 v[160:163], a[188:191], a[24:27], v[160:163]\n"
    "s_waitcnt lgkmcnt(8)\n"
    "s_add_u32 s41, s41, 0x8000\n"
    "ds_read_b128 a[188:191], v242 offset:6144\n"
    "v_mfma_f32_16x16x32_bf16 v[164:167], a[160:163], a[28:31], 0\n"
    "ds_read2st64_b32 v[236:237], v250 offset0:16 offset1:17\n"
    "v_mfma_f32_16x16x32_bf16 v[164:167], a[164:167], a[0:3], v[164:167]\n"
    "v_exp_f32_e32 v194, v194\n"
    "v_mfma_f32_16x16x32_bf16 v[172:175], a[164:167], a[32:35], 0\n"
    "v_exp_f32_e32 v195, v195\n"
    "v_mfma_f32_16x16x32_bf16 v[172:175], a[160:163], a[60:63], v[172:175]\n"
    "ds_read2st64_b32 v[238:239], v250 offset0:18 offset1:19\n"
    "v_mfma_f32_16x16x32_bf16 v[180:183], a[160:163], a[92:95], 0\n"
    "v_cvt_pk_bf16_f32 v192, v192, v193\n"
    "v_mfma_f32_16x16x32_bf16 v[180:183], a[164:167], a[64:67], v[180:183]\n"
    "v_exp_f32_e32 v184, v184\n"
    "v_mfma_f32_16x16x32_bf16 v[188:191], a[164:167], a[96:99], 0\n"
    "v_exp_f32_e32 v185, v185\n"
    "v_mfma_f32_16x16x32_bf16 v[188:191], a[160:163], a[124:127], v[188:191]\n"
    "v_cvt_pk_bf16_f32 v193, v194, v195\n"
    "v_mfma_f32_16x16x32_bf16 v[196:199], a[160:163], a[156:159], 0\n"
    "ds_read_b64_tr_b16 a[160:161], v244 offset:49152\n"
    "v_mfma_f32_16x16x32_bf16 v[196:199], a[164:167], a[128:131], v[196:199]\n"
    "ds_read_b64_tr_b16 a[162:163], v244 offset:51200\n"
    "v_mfma_f32_16x16x32_bf16 v[196:199], a[168:171], a[132:135], v[196:199]\n"
    "ds_read_b64_tr_b16 a[164:165], v244 offset:57344\n"
    "v_mfma_f32_16x16x32_bf16 v[196:199], a[172:175], a[136:139], v[196:199]\n"
    "ds_read_b64_tr_b16 a[166:167], v244 offset:59392\n"
    "v_mfma_f32_16x16x32_bf16 v[188:191], a[172:175], a[104:107], v[188:191]\n"
    "v_exp_f32_e32 v186, v186\n"
    "v_mfma_f32_16x16x32_bf16 v[188:191], a[168:171], a[100:103], v[188:191]\n"
    "v_exp_f32_e32 v187, v187\n"
    "v_mfma_f32_16x16x32_bf16 v[180:183], a[168:171], a[68:71], v[180:183]\n"
    "v_cvt_pk_bf16_f32 v184, v184, v185\n"
    "v_mfma_f32_16x16x32_bf16 v[180:183], a[172:175], a[72:75], v[180:183]\n"
    "v_exp_f32_e32 v176, v176\n"
    "v_mfma_f32_16x16x32_bf16 v[172:175], a[172:175], a[40:43], v[172:175]\n"
    "v_exp_f32_e32 v177, v177\n"
    "v_mfma_f32_16x16x32_bf16 v[172:175], a[168:171], a[36:39], v[172:175]\n"
    "v_cvt_pk_bf16_f32 v185, v186, v187\n"
    "v_mfma_f32_16x16x32_bf16 v[164:167], a[168:171], a[4:7], v[164:167]\n"
    "ds_read_b64_tr_b16 a[168:169], v245 offset:49152\n"
    "v_mfma_f32_16x16x32_bf16 v[164:167], a[172:175], a[8:11], v[164:167]\n"
    "s_waitcnt lgkmcnt(7)\n"
    "ds_read_b64_tr_b16 a[170:171], v245 offset:51200\n"
    "v_mfma_f32_16x16x32_bf16 v[164:167], a[176:179], a[12:15], v[164:167]\n"
    "s_mov_b32 m0, s82\n"
    "ds_read_b64_tr_b16 a[172:173], v245 offset:57344\n"
    "v_mfma_f32_16x16x32_bf16 v[164:167], a[180:183], a[16:19], v[164:167]\n"
    "s_waitcnt vmcnt(8)\n"
    "ds_read_b64_tr_b16 a[174:175], v245 offset:59392\n"
    "v_mfma_f32_16x16x32_bf16 v[172:175], a[180:183], a[48:51], v[172:175]\n"
    "buffer_load_dwordx4 v253, s[8:11], s46 offen offset:0 sc1 lds\n"
    "v_mfma_f32_16x16x32_bf16 v[172:175], a[176:179], a[44:47], v[172:175]\n"
    "v_exp_f32_e32 v178, v178\n"
    "v_mfma_f32_16x16x32_bf16 v[180:183], a[176:179], a[76:79], v[180:183]\n"
    "v_exp_f32_e32 v179, v179\n"
    "v_mfma_f32_16x16x32_bf16 v[180:183], a[180:183], a[80:83], v[180:183]\n"
    "v_cvt_pk_bf16_f32 v176, v176, v177\n"
    "v_mfma_f32_16x16x32_bf16 v[188:191], a[180:183], a[112:115], v[188:191]\n"
    "v_exp_f32_e32 v168, v168\n"
    "v_mfma_f32_16x16x32_bf16 v[188:191], a[176:179], a[108:111], v[188:191]\n"
    "v_exp_f32_e32 v169, v169\n"
    "v_mfma_f32_16x16x32_bf16 v[196:199], a[176:179], a[140:143], v[196:199]\n"
    "ds_read_b64_tr_b16 a[176:177], v246 offset:49152\n"
    "v_mfma_f32_16x16x32_bf16 v[196:199], a[180:183], a[144:147], v[196:199]\n"
    "ds_read_b64_tr_b16 a[178:179], v246 offset:51200\n"
    "v_mfma_f32_16x16x32_bf16 v[196:199], a[184:187], a[148:151], v[196:199]\n"
    "ds_read_b64_tr_b16 a[180:181], v246 offset:57344\n"
    "v_mfma_f32_16x16x32_bf16 v[196:199], a[188:191], a[152:155], v[196:199]\n"
    "ds_read_b64_tr_b16 a[182:183], v246 offset:59392\n"
    "v_mfma_f32_16x16x32_bf16 v[188:191], a[188:191], a[120:123], v[188:191]\n"
    "v_cvt_pk_bf16_f32 v177, v178, v179\n"
    "v_mfma_f32_16x16x32_bf16 v[188:191], a[184:187], a[116:119], v[188:191]\n"
    "v_exp_f32_e32 v170, v170\n"
    "v_mfma_f32_16x16x32_bf16 v[180:183], a[184:187], a[84:87], v[180:183]\n"
    "v_exp_f32_e32 v171, v171\n"
    "v_mfma_f32_16x16x32_bf16 v[180:183], a[188:191], a[88:91], v[180:183]\n"
    "v_cvt_pk_bf16_f32 v168, v168, v169\n"
    "v_mfma_f32_16x16x32_bf16 v[172:175], a[188:191], a[56:59], v[172:175]\n"
    "v_exp_f32_e32 v160, v160\n"
    "v_mfma_f32_16x16x32_bf16 v[172:175], a[184:187], a[52:55], v[172:175]\n"
    "v_exp_f32_e32 v161, v161\n"
    "v_mfma_f32_16x16x32_bf16 v[164:167], a[184:187], a[20:23], v[164:167]\n"
    "ds_read_b64_tr_b16 a[184:185], v247 offset:49152\n"
    "v_mfma_f32_16x16x32_bf16 v[164:167], a[188:191], a[24:27], v[164:167]\n"
    "s_waitcnt lgkmcnt(5)\n"
    "ds_read_b64_tr_b16 a[186:187], v247 offset:51200\n"
    "v_mfma_f32_16x16x32_bf16 a[248:251], a[160:163], v[200:203], a[248:251]\n"
    "ds_read_b64_tr_b16 a[188:189], v247 offset:57344\n"
    "v_mfma_f32_16x16x32_bf16 a[248:251], a[164:167], v[204:207], a[248:251]\n"
    "ds_read_b64_tr_b16 a[190:191], v247 offset:59392\n"
    "v_mfma_f32_16x16x32_bf16 a[252:255], a[164:167], v[212:215], a[252:255]\n"
    "v_cvt_pk_bf16_f32 v169, v170, v171\n"
    "v_mfma_f32_16x16x32_bf16 a[252:255], a[160:163], v[208:211], a[252:255]\n"
    "v_exp_f32_e32 v162, v162\n"
    "v_mfma_f32_16x16x32_bf16 v[56:59], a[160:163], v[216:219], v[56:59]\n"
    "v_exp_f32_e32 v163, v163\n"
    "v_mfma_f32_16x16x32_bf16 v[56:59], a[164:167], v[220:223], v[56:59]\n"
    "v_cvt_pk_bf16_f32 v160, v160, v161\n"
    "v_mfma_f32_16x16x32_bf16 v[60:63], a[164:167], v[228:231], v[60:63]\n"
    "v_cvt_pk_bf16_f32 v161, v162, v163\n"
    "v_mfma_f32_16x16x32_bf16 v[60:63], a[160:163], v[224:227], v[60:63]\n"
    "v_exp_f32_e32 v196, v196\n"
    "v_mfma_f32_16x16x32_bf16 v[64:67], a[160:163], v[232:235], v[64:67]\n"
    "ds_read_b64_tr_b16 a[160:161], v244 offset:53248\n"
    "v_mfma_f32_16x16x32_bf16 v[64:67], a[164:167], v[236:239], v[64:67]\n"
    "ds_read_b64_tr_b16 a[162:163], v244 offset:55296\n"
    "v_mfma_f32_16x16x32_bf16 v[76:79], a[168:171], v[232:235], v[76:79]\n"
    "ds_read_b64_tr_b16 a[164:165], v244 offset:61440\n"
    "v_mfma_f32_16x16x32_bf16 v[76:79], a[172:175], v[236:239], v[76:79]\n"
    "ds_read_b64_tr_b16 a[166:167], v244 offset:63488\n"
    "v_mfma_f32_16x16x32_bf16 v[72:75], a[172:175], v[228:231], v[72:75]\n"
    "v_exp_f32_e32 v197, v197\n"
    "v_mfma_f32_16x16x32_bf16 v[72:75], a[168:171], v[224:227], v[72:75]\n"
    "v_exp_f32_e32 v198, v198\n"
    "v_mfma_f32_16x16x32_bf16 v[68:71], a[168:171], v[216:219], v[68:71]\n"
    "v_exp_f32_e32 v199, v199\n"
    "v_mfma_f32_16x16x32_bf16 v[68:71], a[172:175], v[220:223], v[68:71]\n"
    "v_cvt_pk_bf16_f32 v194, v196, v197\n"
    "v_mfma_f32_16x16x32_bf16 v[4:7], a[172:175], v[212:215], v[4:7]\n"
    "v_exp_f32_e32 v188, v188\n"
    "v_mfma_f32_16x16x32_bf16 v[4:7], a[168:171], v[208:211], v[4:7]\n"
    "v_exp_f32_e32 v189, v189\n"
    "v_mfma_f32_16x16x32_bf16 v[0:3], a[168:171], v[200:203], v[0:3]\n"
    "ds_read_b64_tr_b16 a[168:169], v245 offset:53248\n"
    "v_mfma_f32_16x16x32_bf16 v[0:3], a[172:175], v[204:207], v[0:3]\n"
    "s_waitcnt lgkmcnt(5)\n"
    "ds_read_b64_tr_b16 a[170:171], v245 offset:55296\n"
    "v_mfma_f32_16x16x32_bf16 v[8:11], a[176:179], v[200:203], v[8:11]\n"
    "ds_read_b64_tr_b16 a[172:173], v245 offset:61440\n"
    "v_mfma_f32_16x16x32_bf16 v[8:11], a[180:183], v[204:207], v[8:11]\n"
    "ds_read_b64_tr_b16 a[174:175], v245 offset:63488\n"
    "v_mfma_f32_16x16x32_bf16 v[12:15], a[180:183], v[212:215], v[12:15]\n"
    "v_cvt_pk_bf16_f32 v195, v198, v199\n"
    "v_mfma_f32_16x16x32_bf16 v[12:15], a[176:179], v[208:211], v[12:15]\n"
    "v_exp_f32_e32 v190, v190\n"
    "v_mfma_f32_16x16x32_bf16 v[80:83], a[176:179], v[216:219], v[80:83]\n"
    "v_exp_f32_e32 v191, v191\n"
    "v_mfma_f32_16x16x32_bf16 v[80:83], a[180:183], v[220:223], v[80:83]\n"
    "v_cvt_pk_bf16_f32 v186, v188, v189\n"
    "v_mfma_f32_16x16x32_bf16 v[84:87], a[180:183], v[228:231], v[84:87]\n"
    "v_exp_f32_e32 v180, v180\n"
    "v_mfma_f32_16x16x32_bf16 v[84:87], a[176:179], v[224:227], v[84:87]\n"
    "v_exp_f32_e32 v181, v181\n"
    "v_mfma_f32_16x16x32_bf16 v[88:91], a[176:179], v[232:235], v[88:91]\n"
    "ds_read_b64_tr_b16 a[176:177], v246 offset:53248\n"
    "v_mfma_f32_16x16x32_bf16 v[88:91], a[180:183], v[236:239], v[88:91]\n"
    "ds_read_b64_tr_b16 a[178:179], v246 offset:55296\n"
    "v_mfma_f32_16x16x32_bf16 v[100:103], a[184:187], v[232:235], v[100:103]\n"
    "ds_read_b64_tr_b16 a[180:181], v246 offset:61440\n"
    "v_mfma_f32_16x16x32_bf16 v[100:103], a[188:191], v[236:239], v[100:103]\n"
    "ds_read_b64_tr_b16 a[182:183], v246 offset:63488\n"
    "v_mfma_f32_16x16x32_bf16 v[96:99], a[188:191], v[228:231], v[96:99]\n"
    "v_cvt_pk_bf16_f32 v187, v190, v191\n"
    "v_mfma_f32_16x16x32_bf16 v[96:99], a[184:187], v[224:227], v[96:99]\n"
    "v_exp_f32_e32 v182, v182\n"
    "v_mfma_f32_16x16x32_bf16 v[92:95], a[184:187], v[216:219], v[92:95]\n"
    "v_exp_f32_e32 v183, v183\n"
    "v_mfma_f32_16x16x32_bf16 v[92:95], a[188:191], v[220:223], v[92:95]\n"
    "v_mfma_f32_16x16x32_bf16 v[20:23], a[188:191], v[212:215], v[20:23]\n"
    "v_cvt_pk_bf16_f32 v178, v180, v181\n"
    "v_mfma_f32_16x16x32_bf16 v[20:23], a[184:187], v[208:211], v[20:23]\n"
    "v_exp_f32_e32 v172, v172\n"
    "v_mfma_f32_16x16x32_bf16 v[16:19], a[184:187], v[200:203], v[16:19]\n"
    "ds_read_b64_tr_b16 a[184:185], v247 offset:53248\n"
    "v_mfma_f32_16x16x32_bf16 v[16:19], a[188:191], v[204:207], v[16:19]\n"
    "s_waitcnt lgkmcnt(5)\n"
    "s_mov_b32 m0, s49\n"
    "ds_read_b64_tr_b16 a[186:187], v247 offset:55296\n"
    "v_mfma_f32_16x16x32_bf16 v[24:27], a[160:163], v[200:203], v[24:27]\n"
    "ds_read_b64_tr_b16 a[188:189], v247 offset:61440\n"
    "v_mfma_f32_16x16x32_bf16 v[24:27], a[164:167], v[204:207], v[24:27]\n"
    "ds_read_b64_tr_b16 a[190:191], v247 offset:63488\n"
    "v_mfma_f32_16x16x32_bf16 v[28:31], a[164:167], v[212:215], v[28:31]\n"
    "v_exp_f32_e32 v173, v173\n"
    "v_mfma_f32_16x16x32_bf16 v[28:31], a[160:163], v[208:211], v[28:31]\n"
    "v_cvt_pk_bf16_f32 v179, v182, v183\n"
    "v_mfma_f32_16x16x32_bf16 v[104:107], a[160:163], v[216:219], v[104:107]\n"
    "v_exp_f32_e32 v174, v174\n"
    "v_mfma_f32_16x16x32_bf16 v[104:107], a[164:167], v[220:223], v[104:107]\n"
    "v_exp_f32_e32 v175, v175\n"
    "v_mfma_f32_16x16x32_bf16 v[108:111], a[164:167], v[228:231], v[108:111]\n"
    "v_cvt_pk_bf16_f32 v170, v172, v173\n"
    "v_mfma_f32_16x16x32_bf16 v[108:111], a[160:163], v[224:227], v[108:111]\n"
    "s_waitcnt vmcnt(9)\n"
    "s_barrier\n"
    "v_mfma_f32_16x16x32_bf16 v[112:115], a[160:163], v[232:235], v[112:115]\n"
    "ds_read_b128 a[160:163], v243 offset:20480\n"
    "v_mfma_f32_16x16x32_bf16 v[112:115], a[164:167], v[236:239], v[112:115]\n"
    "ds_read_b128 a[164:167], v240 offset:16384\n"
    "v_mfma_f32_16x16x32_bf16 v[124:127], a[168:171], v[232:235], v[124:127]\n"
    "v_exp_f32_e32 v164, v164\n"
    "v_mfma_f32_16x16x32_bf16 v[124:127], a[172:175], v[236:239], v[124:127]\n"
    "v_exp_f32_e32 v165, v165\n"
    "v_mfma_f32_16x16x32_bf16 v[120:123], a[172:175], v[228:231], v[120:123]\n"
    "ds_write_addtid_b32 v192 offset:4096\n"
    "v_cvt_pk_bf16_f32 v171, v174, v175\n"
    "v_mfma_f32_16x16x32_bf16 v[120:123], a[168:171], v[224:227], v[120:123]\n"
    "ds_write_addtid_b32 v193 offset:4352\n"
    "v_mfma_f32_16x16x32_bf16 v[116:119], a[168:171], v[216:219], v[116:119]\n"
    "v_exp_f32_e32 v166, v166\n"
    "v_mfma_f32_16x16x32_bf16 v[116:119], a[172:175], v[220:223], v[116:119]\n"
    "v_exp_f32_e32 v167, v167\n"
    "v_mfma_f32_16x16x32_bf16 v[36:39], a[172:175], v[212:215], v[36:39]\n"
    "ds_write_addtid_b32 v184 offset:3072\n"
    "v_cvt_pk_bf16_f32 v162, v164, v165\n"
    "v_mfma_f32_16x16x32_bf16 v[36:39], a[168:171], v[208:211], v[36:39]\n"
    "ds_write_addtid_b32 v185 offset:3328\n"
    "v_cvt_pk_bf16_f32 v163, v166, v167\n"
    "v_mfma_f32_16x16x32_bf16 v[32:35], a[168:171], v[200:203], v[32:35]\n"
    "ds_read_b128 a[168:171], v241 offset:16384\n"
    "v_mfma_f32_16x16x32_bf16 v[32:35], a[172:175], v[204:207], v[32:35]\n"
    "s_waitcnt lgkmcnt(7)\n"
    "ds_read_b128 a[172:175], v240 offset:20480\n"
    "v_mfma_f32_16x16x32_bf16 v[40:43], a[176:179], v[200:203], v[40:43]\n"
    "ds_write_addtid_b32 v176 offset:2048\n"
    "v_mfma_f32_16x16x32_bf16 v[40:43], a[180:183], v[204:207], v[40:43]\n"
    "ds_write_addtid_b32 v177 offset:2304\n"
    "v_mfma_f32_16x16x32_bf16 v[44:47], a[180:183], v[212:215], v[44:47]\n"
    "ds_write_addtid_b32 v168 offset:1024\n"
    "v_mfma_f32_16x16x32_bf16 v[44:47], a[176:179], v[208:211], v[44:47]\n"
    "ds_write_addtid_b32 v169 offset:1280\n"
    "v_mfma_f32_16x16x32_bf16 v[128:131], a[176:179], v[216:219], v[128:131]\n"
    "ds_write_addtid_b32 v160 offset:0\n"
    "v_mfma_f32_16x16x32_bf16 v[128:131], a[180:183], v[220:223], v[128:131]\n"
    "ds_write_addtid_b32 v161 offset:256\n"
    "v_mfma_f32_16x16x32_bf16 v[132:135], a[180:183], v[228:231], v[132:135]\n"
    "ds_write_addtid_b32 v194 offset:4608\n"
    "v_mfma_f32_16x16x32_bf16 v[132:135], a[176:179], v[224:227], v[132:135]\n"
    "ds_write_addtid_b32 v195 offset:4864\n"
    "v_mfma_f32_16x16x32_bf16 v[136:139], a[176:179], v[232:235], v[136:139]\n"
    "ds_read_b128 a[176:179], v241 offset:20480\n"
    "v_mfma_f32_16x16x32_bf16 v[136:139], a[180:183], v[236:239], v[136:139]\n"
    "ds_read_b128 a[180:183], v242 offset:16384\n"
    "v_mfma_f32_16x16x32_bf16 v[148:151], a[184:187], v[232:235], v[148:151]\n"
    "ds_write_addtid_b32 v186 offset:3584\n"
    "v_mfma_f32_16x16x32_bf16 v[148:151], a[188:191], v[236:239], v[148:151]\n"
    "ds_write_addtid_b32 v187 offset:3840\n"
    "v_mfma_f32_16x16x32_bf16 v[144:147], a[188:191], v[228:231], v[144:147]\n"
    "ds_write_addtid_b32 v178 offset:2560\n"
    "v_mfma_f32_16x16x32_bf16 v[144:147], a[184:187], v[224:227], v[144:147]\n"
    "ds_write_addtid_b32 v179 offset:2816\n"
    "v_mfma_f32_16x16x32_bf16 v[140:143], a[184:187], v[216:219], v[140:143]\n"
    "s_sub_u32 s44, s44, 1\n"
    "ds_write_addtid_b32 v170 offset:1536\n"
    "v_mfma_f32_16x16x32_bf16 v[140:143], a[188:191], v[220:223], v[140:143]\n"
    "ds_write_addtid_b32 v171 offset:1792\n"
    "v_mfma_f32_16x16x32_bf16 v[52:55], a[188:191], v[212:215], v[52:55]\n"
    "ds_write_addtid_b32 v162 offset:512\n"
    "v_mfma_f32_16x16x32_bf16 v[52:55], a[184:187], v[208:211], v[52:55]\n"
    "ds_write_addtid_b32 v163 offset:768\n"
    "v_mfma_f32_16x16x32_bf16 v[48:51], a[184:187], v[200:203], v[48:51]\n"
    "ds_read_b128 a[184:187], v243 offset:16384\n"
    "v_mfma_f32_16x16x32_bf16 v[48:51], a[188:191], v[204:207], v[48:51]\n"
    "s_waitcnt lgkmcnt(15)\n"
    "ds_read_b128 a[188:191], v242 offset:20480\n"
    "s_cbranch_scc0 .Lat_p\n"
    "s_cmp_lg_u32 s37, 0\n"
    "s_cbranch_scc1 .Lat_tail\n"
    "s_waitcnt vmcnt(0)\n"
    "v_cmp_ne_u32_e32 vcc, s39, v154\n"
    "s_mov_b32 s37, 1\n"
    "s_lshr_b32 s44, s34, 14\n"
    "s_add_u32 s44, s44, 3\n"
    "s_nop 1\n"
    "s_cbranch_vccnz .Lat_lpoll\n"
    ".p2align 4\n"
    ".Lat_g:\n"
    "v_mfma_f32_16x16x32_bf16 v[200:203], a[160:163], a[28:31], 0\n"
    "v_mfma_f32_16x16x32_bf16 v[200:203], a[164:167], a[0:3], v[200:203]\n"
    "v_mfma_f32_16x16x32_bf16 v[208:211], a[164:167], a[32:35], 0\n"
    "v_mfma_f32_16x16x32_bf16 v[208:211], a[160:163], a[60:63], v[208:211]\n"
    "v_mfma_f32_16x16x32_bf16 v[216:219], a[160:163], a[92:95], 0\n"
    "v_mfma_f32_16x16x32_bf16 v[216:219], a[164:167], a[64:67], v[216:219]\n"
    "v_mfma_f32_16x16x32_bf16 v[224:227], a[164:167], a[96:99], 0\n"
    "s_add_u32 m0, s43, 49152\n"
    "v_mfma_f32_16x16x32_bf16 v[224:227], a[160:163], a[124:127], v[224:227]\n"
    "s_waitcnt lgkmcnt(0)\n"
    "s_barrier\n"
    "v_mfma_f32_16x16x32_bf16 v[232:235], a[160:163], a[156:159], 0\n"
    "ds_read_b128 a[160:163], v243 offset:22528\n"
    "v_mfma_f32_16x16x32_bf16 v[232:235], a[164:167], a[128:131], v[232:235]\n"
    "ds_read_b128 a[164:167], v240 offset:18432\n"
    "v_mfma_f32_16x16x32_bf16 v[232:235], a[168:171], a[132:135], v[232:235]\n"
    "buffer_load_dwordx4 v248, s[8:11], s40 offen offset:0 sc1 lds\n"
    "v_mfma_f32_16x16x32_bf16 v[232:235], a[172:175], a[136:139], v[232:235]\n"
    "ds_read2st64_b32 v[164:165], v250 offset0:0 offset1:1\n"
    "v_mfma_f32_16x16x32_bf16 v[224:227], a[172:175], a[104:107], v[224:227]\n"
    "v_mfma_f32_16x16x32_bf16 v[224:227], a[168:171], a[100:103], v[224:227]\n"
    "ds_read2st64_b32 v[166:167], v250 offset0:2 offset1:3\n"
    "v_mfma_f32_16x16x32_bf16 v[216:219], a[168:171], a[68:71], v[216:219]\n"
    "buffer_load_dwordx4 v248, s[8:11], s40 offen offset:1024 sc1 lds\n"
    "v_mfma_f32_16x16x32_bf16 v[216:219], a[172:175], a[72:75], v[216:219]\n"
    "v_mfma_f32_16x16x32_bf16 v[208:211], a[172:175], a[40:43], v[208:211]\n"
    "v_mfma_f32_16x16x32_bf16 v[208:211], a[168:171], a[36:39], v[208:211]\n"
    "ds_read2st64_b32 v[172:173], v250 offset0:4 offset1:5\n"
    "v_mfma_f32_16x16x32_bf16 v[200:203], a[168:171], a[4:7], v[200:203]\n"
    "ds_read_b128 a[168:171], v241 offset:18432\n"
    "buffer_load_dwordx4 v248, s[8:11], s40 offen offset:2048 sc1 lds\n"
    "v_mfma_f32_16x16x32_bf16 v[200:203], a[172:175], a[8:11], v[200:203]\n"
    "ds_read_b128 a[172:175], v240 offset:22528\n"
    "v_mfma_f32_16x16x32_bf16 v[200:203], a[176:179], a[12:15], v[200:203]\n"
    "v_mfma_f32_16x16x32_bf16 v[200:203], a[180:183], a[16:19], v[200:203]\n"
    "ds_read2st64_b32 v[174:175], v250 offset0:6 offset1:7\n"
    "v_mfma_f32_16x16x32_bf16 v[208:211], a[180:183], a[48:51], v[208:211]\n"
    "buffer_load_dwordx4 v248, s[8:11], s40 offen offset:3072 sc1 lds\n"
    "v_mfma_f32_16x16x32_bf16 v[208:211], a[176:179], a[44:47], v[208:211]\n"
    "s_add_u32 m0, s43, 53248\n"
    "v_mfma_f32_16x16x32_bf16 v[216:219], a[176:179], a[76:79], v[216:219]\n"
    "s_add_u32 s40, s40, 0x8000\n"
    "v_mfma_f32_16x16x32_bf16 v[216:219], a[180:183], a[80:83], v[216:219]\n"
    "ds_read2st64_b32 v[180:181], v250 offset0:8 offset1:9\n"
    "v_mfma_f32_16x16x32_bf16 v[224:227], a[180:183], a[112:115], v[224:227]\n"
    "buffer_load_dwordx4 v248, s[8:11], s41 offen offset:0 sc1 lds\n"
    "v_mfma_f32_16x16x32_bf16 v[224:227], a[176:179], a[108:111], v[224:227]\n"
    "ds_read2st64_b32 v[182:183], v250 offset0:10 offset1:11\n"
    "v_mfma_f32_16x16x32_bf16 v[232:235], a[176:179], a[140:143], v[232:235]\n"
    "ds_read_b128 a[176:179], v241 offset:22528\n"
    "v_mfma_f32_16x16x32_bf16 v[232:235], a[180:183], a[144:147], v[232:235]\n"
    "ds_read_b128 a[180:183], v242 offset:18432\n"
    "v_mfma_f32_16x16x32_bf16 v[232:235], a[184:187], a[148:151], v[232:235]\n"
    "buffer_load_dwordx4 v248, s[8:11], s41 offen offset:1024 sc1 lds\n"
    "v_mfma_f32_16x16x32_bf16 v[232:235], a[188:191], a[152:155], v[232:235]\n"
    "v_mfma_f32_16x16x32_bf16 v[224:227], a[188:191], a[120:123], v[224:227]\n"
    "ds_read2st64_b32 v[188:189], v250 offset0:12 offset1:13\n"
    "v_mfma_f32_16x16x32_bf16 v[224:227], a[184:187], a[116:119], v[224:227]\n"
    "v_mfma_f32_16x16x32_bf16 v[216:219], a[184:187], a[84:87], v[216:219]\n"
    "buffer_load_dwordx4 v248, s[8:11], s41 offen offset:2048 sc1 lds\n"
    "v_mfma_f32_16x16x32_bf16 v[216:219], a[188:191], a[88:91], v[216:219]\n"
    "ds_read2st64_b32 v[190:191], v250 offset0:14 offset1:15\n"
    "v_mfma_f32_16x16x32_bf16 v[208:211], a[188:191], a[56:59], v[208:211]\n"
    "ds_read_b64 v[254:255], v252 offset:0\n"
    "v_mfma_f32_16x16x32_bf16 v[208:211], a[184:187], a[52:55], v[208:211]\n"
    "ds_read2st64_b32 v[196:197], v250 offset0:16 offset1:17\n"
    "v_mfma_f32_16x16x32_bf16 v[200:203], a[184:187], a[20:23], v[200:203]\n"
    "ds_read_b128 a[184:187], v243 offset:18432\n"
    "buffer_load_dwordx4 v248, s[8:11], s41 offen offset:3072 sc1 lds\n"
    "v_mfma_f32_16x16x32_bf16 v[200:203], a[188:191], a[24:27], v[200:203]\n"
    "s_waitcnt lgkmcnt(15)\n"
    "s_add_u32 s41, s41, 0x8000\n"
    "ds_read_b128 a[188:191], v242 offset:22528\n"
    "v_mfma_f32_16x16x32_bf16 v[204:207], a[160:163], a[28:31], 0\n"
    "s_waitcnt lgkmcnt(3)\n"
    "v_mad_u32_u16 v253, v254, s42, v251 op_sel:[0,0,0,0]\n"
    "v_mfma_f32_16x16x32_bf16 v[204:207], a[164:167], a[0:3], v[204:207]\n"
    "buffer_load_dwordx4 a[200:203], v253, s[20:23], 0 offen offset:0 sc1\n"
    "v_mfma_f32_16x16x32_bf16 v[212:215], a[164:167], a[32:35], 0\n"
    "v_exp_f32_e32 v232, v232\n"
    "v_mfma_f32_16x16x32_bf16 v[212:215], a[160:163], a[60:63], v[212:215]\n"
    "ds_read2st64_b32 v[198:199], v250 offset0:18 offset1:19\n"
    "v_mfma_f32_16x16x32_bf16 v[220:223], a[160:163], a[92:95], 0\n"
    "v_mad_u32_u16 v253, v254, s42, v251 op_sel:[1,0,0,0]\n"
    "v_mfma_f32_16x16x32_bf16 v[220:223], a[164:167], a[64:67], v[220:223]\n"
    "buffer_load_dwordx4 a[204:207], v253, s[20:23], 0 offen offset:0 sc1\n"
    "v_mfma_f32_16x16x32_bf16 v[228:231], a[164:167], a[96:99], 0\n"
    "v_exp_f32_e32 v233, v233\n"
    "v_mfma_f32_16x16x32_bf16 v[228:231], a[160:163], a[124:127], v[228:231]\n"
    "v_exp_f32_e32 v234, v234\n"
    "v_mfma_f32_16x16x32_bf16 v[236:239], a[160:163], a[156:159], 0\n"
    "ds_read_b64_tr_b16 a[160:161], v244 offset:0\n"
    "v_mad_u32_u16 v253, v255, s42, v251 op_sel:[0,0,0,0]\n"
    "v_mfma_f32_16x16x32_bf16 v[236:239], a[164:167], a[128:131], v[236:239]\n"
    "ds_read_b64_tr_b16 a[162:163], v244 offset:2048\n"
    "buffer_load_dwordx4 a[208:211], v253, s[20:23], 0 offen offset:0 sc1\n"
    "v_mfma_f32_16x16x32_bf16 v[236:239], a[168:171], a[132:135], v[236:239]\n"
    "ds_read_b64_tr_b16 a[164:165], v244 offset:8192\n"
    "v_mfma_f32_16x16x32_bf16 v[236:239], a[172:175], a[136:139], v[236:239]\n"
    "ds_read_b64_tr_b16 a[166:167], v244 offset:10240\n"
    "v_mfma_f32_16x16x32_bf16 v[228:231], a[172:175], a[104:107], v[228:231]\n"
    "v_mad_u32_u16 v253, v255, s42, v251 op_sel:[1,0,0,0]\n"
    "v_mfma_f32_16x16x32_bf16 v[228:231], a[168:171], a[100:103], v[228:231]\n"
    "buffer_load_dwordx4 a[212:215], v253, s[20:23], 0 offen offset:0 sc1\n"
    "ds_read_b64 v[254:255], v252 offset:8\n"
    "v_mfma_f32_16x16x32_bf16 v[220:223], a[168:171], a[68:71], v[220:223]\n"
    "v_exp_f32_e32 v235, v235\n"
    "v_mfma_f32_16x16x32_bf16 v[220:223], a[172:175], a[72:75], v[220:223]\n"
    "v_cvt_pk_bf16_f32 v232, v232, v233\n"
    "v_mfma_f32_16x16x32_bf16 v[212:215], a[172:175], a[40:43], v[212:215]\n"
    "v_exp_f32_e32 v224, v224\n"
    "v_mfma_f32_16x16x32_bf16 v[212:215], a[168:171], a[36:39], v[212:215]\n"
    "v_exp_f32_e32 v225, v225\n"
    "v_mfma_f32_16x16x32_bf16 v[204:207], a[168:171], a[4:7], v[204:207]\n"
    "ds_read_b64_tr_b16 a[168:169], v245 offset:0\n"
    "v_cvt_pk_bf16_f32 v233, v234, v235\n"
    "v_mfma_f32_16x16x32_bf16 v[204:207], a[172:175], a[8:11], v[204:207]\n"
    "ds_read_b64_tr_b16 a[170:171], v245 offset:2048\n"
    "v_mfma_f32_16x16x32_bf16 v[204:207], a[176:179], a[12:15], v[204:207]\n"
    "s_waitcnt lgkmcnt(2)\n"
    "ds_read_b64_tr_b16 a[172:173], v245 offset:8192\n"
    "v_mad_u32_u16 v253, v254, s42, v251 op_sel:[0,0,0,0]\n"
    "v_mfma_f32_16x16x32_bf16 v[204:207], a[180:183], a[16:19], v[204:207]\n"
    "ds_read_b64_tr_b16 a[174:175], v245 offset:10240\n"
    "buffer_load_dwordx4 a[216:219], v253, s[20:23], 0 offen offset:0 sc1\n"
    "v_mfma_f32_16x16x32_bf16 v[212:215], a[180:183], a[48:51], v[212:215]\n"
    "v_exp_f32_e32 v226, v226\n"
    "v_mfma_f32_16x16x32_bf16 v[212:215], a[176:179], a[44:47], v[212:215]\n"
    "v_exp_f32_e32 v227, v227\n"
    "v_mfma_f32_16x16x32_bf16 v[220:223], a[176:179], a[76:79], v[220:223]\n"
    "v_mad_u32_u16 v253, v254, s42, v251 op_sel:[1,0,0,0]\n"
    "v_cvt_pk_bf16_f32 v224, v224, v225\n"
    "v_mfma_f32_16x16x32_bf16 v[220:223], a[180:183], a[80:83], v[220:223]\n"
    "buffer_load_dwordx4 a[220:223], v253, s[20:23], 0 offen offset:0 sc1\n"
    "v_mfma_f32_16x16x32_bf16 v[228:231], a[180:183], a[112:115], v[228:231]\n"
    "v_exp_f32_e32 v216, v216\n"
    "v_mfma_f32_16x16x32_bf16 v[228:231], a[176:179], a[108:111], v[228:231]\n"
    "v_exp_f32_e32 v217, v217\n"
    "v_mfma_f32_16x16x32_bf16 v[236:239], a[176:179], a[140:143], v[236:239]\n"
    "ds_read_b64_tr_b16 a[176:177], v246 offset:0\n"
    "v_cvt_pk_bf16_f32 v225, v226, v227\n"
    "v_mfma_f32_16x16x32_bf16 v[236:239], a[180:183], a[144:147], v[236:239]\n"
    "ds_read_b64_tr_b16 a[178:179], v246 offset:2048\n"
    "v_mfma_f32_16x16x32_bf16 v[236:239], a[184:187], a[148:151], v[236:239]\n"
    "ds_read_b64_tr_b16 a[180:181], v246 offset:8192\n"
    "v_mfma_f32_16x16x32_bf16 v[236:239], a[188:191], a[152:155], v[236:239]\n"
    "ds_read_b64_tr_b16 a[182:183], v246 offset:10240\n"
    "v_mfma_f32_16x16x32_bf16 v[228:231], a[188:191], a[120:123], v[228:231]\n"
    "ds_read_b64 v[254:255], v252 offset:32\n"
    "v_mfma_f32_16x16x32_bf16 v[228:231], a[184:187], a[116:119], v[228:231]\n"
    "v_exp_f32_e32 v218, v218\n"
    "v_mfma_f32_16x16x32_bf16 v[220:223], a[184:187], a[84:87], v[220:223]\n"
    "v_exp_f32_e32 v219, v219\n"
    "v_mfma_f32_16x16x32_bf16 v[220:223], a[188:191], a[88:91], v[220:223]\n"
    "v_cvt_pk_bf16_f32 v216, v216, v217\n"
    "v_mfma_f32_16x16x32_bf16 v[212:215], a[188:191], a[56:59], v[212:215]\n"
    "s_waitcnt lgkmcnt(0)\n"
    "v_mad_u32_u16 v253, v254, s42, v251 op_sel:[0,0,0,0]\n"
    "v_mfma_f32_16x16x32_bf16 v[212:215], a[184:187], a[52:55], v[212:215]\n"
    "buffer_load_dwordx4 a[224:227], v253, s[20:23], 0 offen offset:0 sc1\n"
    "v_mfma_f32_16x16x32_bf16 v[204:207], a[184:187], a[20:23], v[204:207]\n"
    "ds_read_b64_tr_b16 a[184:185], v247 offset:0\n"
    "v_mfma_f32_16x16x32_bf16 v[204:207], a[188:191], a[24:27], v[204:207]\n"
    "ds_read_b64_tr_b16 a[186:187], v247 offset:2048\n"
    "v_mfma_f32_16x16x32_bf16 a[248:251], a[160:163], v[160:163], a[248:251]\n"
    "ds_read_b64_tr_b16 a[188:189], v247 offset:8192\n"
    "v_mad_u32_u16 v253, v254, s42, v251 op_sel:[1,0,0,0]\n"
    "v_mfma_f32_16x16x32_bf16 a[248:251], a[164:167], v[164:167], a[248:251]\n"
    "ds_read_b64_tr_b16 a[190:191], v247 offset:10240\n"
    "buffer_load_dwordx4 a[228:231], v253, s[20:23], 0 offen offset:0 sc1\n"
    "v_mfma_f32_16x16x32_bf16 a[252:255], a[164:167], v[172:175], a[252:255]\n"
    "v_exp_f32_e32 v208, v208\n"
    "v_mfma_f32_16x16x32_bf16 a[252:255], a[160:163], v[168:171], a[252:255]\n"
    "v_exp_f32_e32 v209, v209\n"
    "v_mfma_f32_16x16x32_bf16 v[56:59], a[160:163], v[176:179], v[56:59]\n"
    "v_mad_u32_u16 v253, v255, s42, v251 op_sel:[0,0,0,0]\n"
    "v_cvt_pk_bf16_f32 v217, v218, v219\n"
    "v_mfma_f32_16x16x32_bf16 v[56:59], a[164:167], v[180:183], v[56:59]\n"
    "buffer_load_dwordx4 a[232:235], v253, s[20:23], 0 offen offset:0 sc1\n"
    "v_mfma_f32_16x16x32_bf16 v[60:63], a[164:167], v[188:191], v[60:63]\n"
    "v_exp_f32_e32 v210, v210\n"
    "v_mfma_f32_16x16x32_bf16 v[60:63], a[160:163], v[184:187], v[60:63]\n"
    "v_exp_f32_e32 v211, v211\n"
    "v_mfma_f32_16x16x32_bf16 v[64:67], a[160:163], v[192:195], v[64:67]\n"
    "ds_read_b64_tr_b16 a[160:161], v244 offset:4096\n"
    "v_mad_u32_u16 v253, v255, s42, v251 op_sel:[1,0,0,0]\n"
    "v_mfma_f32_16x16x32_bf16 v[64:67], a[164:167], v[196:199], v[64:67]\n"
    "ds_read_b64_tr_b16 a[162:163], v244 offset:6144\n"
    "buffer_load_dwordx4 a[236:239], v253, s[20:23], 0 offen offset:0 sc1\n"
    "v_mfma_f32_16x16x32_bf16 v[76:79], a[168:171], v[192:195], v[76:79]\n"
    "ds_read_b64_tr_b16 a[164:165], v244 offset:12288\n"
    "v_cvt_pk_bf16_f32 v208, v208, v209\n"
    "v_mfma_f32_16x16x32_bf16 v[76:79], a[172:175], v[196:199], v[76:79]\n"
    "ds_read_b64_tr_b16 a[166:167], v244 offset:14336\n"
    "v_mfma_f32_16x16x32_bf16 v[72:75], a[172:175], v[188:191], v[72:75]\n"
    "ds_read_b64 v[254:255], v252 offset:40\n"
    "v_mfma_f32_16x16x32_bf16 v[72:75], a[168:171], v[184:187], v[72:75]\n"
    "v_exp_f32_e32 v200, v200\n"
    "v_mfma_f32_16x16x32_bf16 v[68:71], a[168:171], v[176:179], v[68:71]\n"
    "v_exp_f32_e32 v201, v201\n"
    "v_mfma_f32_16x16x32_bf16 v[68:71], a[172:175], v[180:183], v[68:71]\n"
    "v_cvt_pk_bf16_f32 v209, v210, v211\n"
    "v_mfma_f32_16x16x32_bf16 v[4:7], a[172:175], v[172:175], v[4:7]\n"
    "v_exp_f32_e32 v202, v202\n"
    "v_mfma_f32_16x16x32_bf16 v[4:7], a[168:171], v[168:171], v[4:7]\n"
    "v_exp_f32_e32 v203, v203\n"
    "v_mfma_f32_16x16x32_bf16 v[0:3], a[168:171], v[160:163], v[0:3]\n"
    "s_waitcnt lgkmcnt(0)\n"
    "ds_read_b64_tr_b16 a[168:169], v245 offset:4096\n"
    "v_mad_u32_u16 v253, v254, s42, v251 op_sel:[0,0,0,0]\n"
    "v_mfma_f32_16x16x32_bf16 v[0:3], a[172:175], v[164:167], v[0:3]\n"
    "ds_read_b64_tr_b16 a[170:171], v245 offset:6144\n"
    "v_cvt_pk_bf16_f32 v200, v200, v201\n"
    "v_mfma_f32_16x16x32_bf16 v[8:11], a[176:179], v[160:163], v[8:11]\n"
    "ds_read_b64_tr_b16 a[172:173], v245 offset:12288\n"
    "buffer_load_dwordx4 a[240:243], v253, s[20:23], 0 offen offset:0 sc1\n"
    "v_mfma_f32_16x16x32_bf16 v[8:11], a[180:183], v[164:167], v[8:11]\n"
    "ds_read_b64_tr_b16 a[174:175], v245 offset:14336\n"
    "v_cvt_pk_bf16_f32 v201, v202, v203\n"
    "v_mfma_f32_16x16x32_bf16 v[12:15], a[180:183], v[172:175], v[12:15]\n"
    "v_exp_f32_e32 v236, v236\n"
    "v_mfma_f32_16x16x32_bf16 v[12:15], a[176:179], v[168:171], v[12:15]\n"
    "v_mad_u32_u16 v253, v254, s42, v251 op_sel:[1,0,0,0]\n"
    "v_mfma_f32_16x16x32_bf16 v[80:83], a[176:179], v[176:179], v[80:83]\n"
    "buffer_load_dwordx4 a[244:247], v253, s[20:23], 0 offen offset:0 sc1\n"
    "v_mfma_f32_16x16x32_bf16 v[80:83], a[180:183], v[180:183], v[80:83]\n"
    "v_exp_f32_e32 v237, v237\n"
    "v_mfma_f32_16x16x32_bf16 v[84:87], a[180:183], v[188:191], v[84:87]\n"
    "v_exp_f32_e32 v238, v238\n"
    "v_mfma_f32_16x16x32_bf16 v[84:87], a[176:179], v[184:187], v[84:87]\n"
    "v_exp_f32_e32 v239, v239\n"
    "v_mfma_f32_16x16x32_bf16 v[88:91], a[176:179], v[192:195], v[88:91]\n"
    "ds_read_b64_tr_b16 a[176:177], v246 offset:4096\n"
    "v_cvt_pk_bf16_f32 v234, v236, v237\n"
    "v_mfma_f32_16x16x32_bf16 v[88:91], a[180:183], v[196:199], v[88:91]\n"
    "ds_read_b64_tr_b16 a[178:179], v246 offset:6144\n"
    "v_exp_f32_e32 v228, v228\n"
    "v_mfma_f32_16x16x32_bf16 v[100:103], a[184:187], v[192:195], v[100:103]\n"
    "ds_read_b64_tr_b16 a[180:181], v246 offset:12288\n"
    "v_exp_f32_e32 v229, v229\n"
    "v_mfma_f32_16x16x32_bf16 v[100:103], a[188:191], v[196:199], v[100:103]\n"
    "ds_read_b64_tr_b16 a[182:183], v246 offset:14336\n"
    "v_cvt_pk_bf16_f32 v235, v238, v239\n"
    "v_mfma_f32_16x16x32_bf16 v[96:99], a[188:191], v[188:191], v[96:99]\n"
    "v_exp_f32_e32 v230, v230\n"
    "v_mfma_f32_16x16x32_bf16 v[96:99], a[184:187], v[184:187], v[96:99]\n"
    "v_exp_f32_e32 v231, v231\n"
    "v_mfma_f32_16x16x32_bf16 v[92:95], a[184:187], v[176:179], v[92:95]\n"
    "v_cvt_pk_bf16_f32 v226, v228, v229\n"
    "v_mfma_f32_16x16x32_bf16 v[92:95], a[188:191], v[180:183], v[92:95]\n"
    "v_exp_f32_e32 v220, v220\n"
    "v_mfma_f32_16x16x32_bf16 v[20:23], a[188:191], v[172:175], v[20:23]\n"
    "v_exp_f32_e32 v221, v221\n"
    "v_mfma_f32_16x16x32_bf16 v[20:23], a[184:187], v[168:171], v[20:23]\n"
    "v_cvt_pk_bf16_f32 v227, v230, v231\n"
    "v_mfma_f32_16x16x32_bf16 v[16:19], a[184:187], v[160:163], v[16:19]\n"
    "ds_read_b64_tr_b16 a[184:185], v247 offset:4096\n"
    "v_mfma_f32_16x16x32_bf16 v[16:19], a[188:191], v[164:167], v[16:19]\n"
    "s_waitcnt lgkmcnt(5)\n"
    "s_mov_b32 m0, s49\n"
    "ds_read_b64_tr_b16 a[186:187], v247 offset:6144\n"
    "v_mfma_f32_16x16x32_bf16 v[24:27], a[160:163], v[160:163], v[24:27]\n"
    "ds_read_b64_tr_b16 a[188:189], v247 offset:12288\n"
    "v_mfma_f32_16x16x32_bf16 v[24:27], a[164:167], v[164:167], v[24:27]\n"
    "ds_read_b64_tr_b16 a[190:191], v247 offset:14336\n"
    "v_mfma_f32_16x16x32_bf16 v[28:31], a[164:167], v[172:175], v[28:31]\n"
    "v_exp_f32_e32 v222, v222\n"
    "v_mfma_f32_16x16x32_bf16 v[28:31], a[160:163], v[168:171], v[28:31]\n"
    "v_exp_f32_e32 v223, v223\n"
    "v_mfma_f32_16x16x32_bf16 v[104:107], a[160:163], v[176:179], v[104:107]\n"
    "v_cvt_pk_bf16_f32 v218, v220, v221\n"
    "v_mfma_f32_16x16x32_bf16 v[104:107], a[164:167], v[180:183], v[104:107]\n"
    "v_exp_f32_e32 v212, v212\n"
    "v_mfma_f32_16x16x32_bf16 v[108:111], a[164:167], v[188:191], v[108:111]\n"
    "v_exp_f32_e32 v213, v213\n"
    "v_mfma_f32_16x16x32_bf16 v[108:111], a[160:163], v[184:187], v[108:111]\n"
    "s_waitcnt vmcnt(20)\n"
    "s_barrier\n"
    "v_mfma_f32_16x16x32_bf16 v[112:115], a[160:163], v[192:195], v[112:115]\n"
    "ds_read_b128 a[160:163], v243 offset:36864\n"
    "v_cvt_pk_bf16_f32 v219, v222, v223\n"
    "v_mfma_f32_16x16x32_bf16 v[112:115], a[164:167], v[196:199], v[112:115]\n"
    "ds_read_b128 a[164:167], v240 offset:32768\n"
    "ds_write_addtid_b32 v232 offset:4096\n"
    "v_mfma_f32_16x16x32_bf16 v[124:127], a[168:171], v[192:195], v[124:127]\n"
    "v_exp_f32_e32 v214, v214\n"
    "v_mfma_f32_16x16x32_bf16 v[124:127], a[172:175], v[196:199], v[124:127]\n"
    "v_exp_f32_e32 v215, v215\n"
    "v_mfma_f32_16x16x32_bf16 v[120:123], a[172:175], v[188:191], v[120:123]\n"
    "ds_write_addtid_b32 v233 offset:4352\n"
    "v_cvt_pk_bf16_f32 v210, v212, v213\n"
    "v_mfma_f32_16x16x32_bf16 v[120:123], a[168:171], v[184:187], v[120:123]\n"
    "v_exp_f32_e32 v204, v204\n"
    "v_mfma_f32_16x16x32_bf16 v[116:119], a[168:171], v[176:179], v[116:119]\n"
    "v_exp_f32_e32 v205, v205\n"
    "v_mfma_f32_16x16x32_bf16 v[116:119], a[172:175], v[180:183], v[116:119]\n"
    "ds_write_addtid_b32 v224 offset:3072\n"
    "v_cvt_pk_bf16_f32 v211, v214, v215\n"
    "v_mfma_f32_16x16x32_bf16 v[36:39], a[172:175], v[172:175], v[36:39]\n"
    "v_exp_f32_e32 v206, v206\n"
    "v_mfma_f32_16x16x32_bf16 v[36:39], a[168:171], v[168:171], v[36:39]\n"
    "v_exp_f32_e32 v207, v207\n"
    "v_mfma_f32_16x16x32_bf16 v[32:35], a[168:171], v[160:163], v[32:35]\n"
    "ds_read_b128 a[168:171], v241 offset:32768\n"
    "v_cvt_pk_bf16_f32 v202, v204, v205\n"
    "v_mfma_f32_16x16x32_bf16 v[32:35], a[172:175], v[164:167], v[32:35]\n"
    "s_waitcnt lgkmcnt(6)\n"
    "ds_read_b128 a[172:175], v240 offset:36864\n"
    "v_cvt_pk_bf16_f32 v203, v206, v207\n"
    "v_mfma_f32_16x16x32_bf16 v[40:43], a[176:179], v[160:163], v[40:43]\n"
    "ds_write_addtid_b32 v225 offset:3328\n"
    "ds_write_addtid_b32 v216 offset:2048\n"
    "v_mfma_f32_16x16x32_bf16 v[40:43], a[180:183], v[164:167], v[40:43]\n"
    "ds_write_addtid_b32 v217 offset:2304\n"
    "v_mfma_f32_16x16x32_bf16 v[44:47], a[180:183], v[172:175], v[44:47]\n"
    "ds_write_addtid_b32 v208 offset:1024\n"
    "v_mfma_f32_16x16x32_bf16 v[44:47], a[176:179], v[168:171], v[44:47]\n"
    "ds_write_addtid_b32 v209 offset:1280\n"
    "v_mfma_f32_16x16x32_bf16 v[128:131], a[176:179], v[176:179], v[128:131]\n"
    "ds_write_addtid_b32 v200 offset:0\n"
    "v_mfma_f32_16x16x32_bf16 v[128:131], a[180:183], v[180:183], v[128:131]\n"
    "ds_write_addtid_b32 v201 offset:256\n"
    "v_mfma_f32_16x16x32_bf16 v[132:135], a[180:183], v[188:191], v[132:135]\n"
    "ds_write_addtid_b32 v234 offset:4608\n"
    "v_mfma_f32_16x16x32_bf16 v[132:135], a[176:179], v[184:187], v[132:135]\n"
    "ds_write_addtid_b32 v235 offset:4864\n"
    "v_mfma_f32_16x16x32_bf16 v[136:139], a[176:179], v[192:195], v[136:139]\n"
    "ds_read_b128 a[176:179], v241 offset:36864\n"
    "v_mfma_f32_16x16x32_bf16 v[136:139], a[180:183], v[196:199], v[136:139]\n"
    "ds_read_b128 a[180:183], v242 offset:32768\n"
    "v_mfma_f32_16x16x32_bf16 v[148:151], a[184:187], v[192:195], v[148:151]\n"
    "ds_write_addtid_b32 v226 offset:3584\n"
    "v_mfma_f32_16x16x32_bf16 v[148:151], a[188:191], v[196:199], v[148:151]\n"
    "ds_write_addtid_b32 v227 offset:3840\n"
    "v_mfma_f32_16x16x32_bf16 v[144:147], a[188:191], v[188:191], v[144:147]\n"
    "ds_write_addtid_b32 v218 offset:2560\n"
    "v_mfma_f32_16x16x32_bf16 v[144:147], a[184:187], v[184:187], v[144:147]\n"
    "ds_write_addtid_b32 v219 offset:2816\n"
    "v_mfma_f32_16x16x32_bf16 v[140:143], a[184:187], v[176:179], v[140:143]\n"
    "ds_write_addtid_b32 v210 offset:1536\n"
    "v_mfma_f32_16x16x32_bf16 v[140:143], a[188:191], v[180:183], v[140:143]\n"
    "ds_write_addtid_b32 v211 offset:1792\n"
    "v_mfma_f32_16x16x32_bf16 v[52:55], a[188:191], v[172:175], v[52:55]\n"
    "ds_write_addtid_b32 v202 offset:512\n"
    "v_mfma_f32_16x16x32_bf16 v[52:55], a[184:187], v[168:171], v[52:55]\n"
    "ds_write_addtid_b32 v203 offset:768\n"
    "v_mfma_f32_16x16x32_bf16 v[48:51], a[184:187], v[160:163], v[48:51]\n"
    "ds_read_b128 a[184:187], v243 offset:32768\n"
    "v_mfma_f32_16x16x32_bf16 v[48:51], a[188:191], v[164:167], v[48:51]\n"
    "s_waitcnt lgkmcnt(15)\n"
    "ds_read_b128 a[188:191], v242 offset:36864\n"
    "v_mfma_f32_16x16x32_bf16 v[160:163], a[160:163], a[28:31], 0\n"
    "v_mfma_f32_16x16x32_bf16 v[160:163], a[164:167], a[0:3], v[160:163]\n"
    "v_mfma_f32_16x16x32_bf16 v[168:171], a[164:167], a[32:35], 0\n"
    "v_mfma_f32_16x16x32_bf16 v[168:171], a[160:163], a[60:63], v[168:171]\n"
    "v_mfma_f32_16x16x32_bf16 v[176:179], a[160:163], a[92:95], 0\n"
    "v_mfma_f32_16x16x32_bf16 v[176:179], a[164:167], a[64:67], v[176:179]\n"
    "v_mfma_f32_16x16x32_bf16 v[184:187], a[164:167], a[96:99], 0\n"
    "s_add_u32 m0, s43, 0\n"
    "v_mfma_f32_16x16x32_bf16 v[184:187], a[160:163], a[124:127], v[184:187]\n"
    "s_waitcnt lgkmcnt(0)\n"
    "s_barrier\n"
    "v_mfma_f32_16x16x32_bf16 v[192:195], a[160:163], a[156:159], 0\n"
    "ds_read_b128 a[160:163], v243 offset:38912\n"
    "v_mfma_f32_16x16x32_bf16 v[192:195], a[164:167], a[128:131], v[192:195]\n"
    "ds_read_b128 a[164:167], v240 offset:34816\n"
    "v_mfma_f32_16x16x32_bf16 v[192:195], a[168:171], a[132:135], v[192:195]\n"
    "buffer_load_dwordx4 v248, s[8:11], s40 offen offset:0 sc1 lds\n"
    "v_mfma_f32_16x16x32_bf16 v[192:195], a[172:175], a[136:139], v[192:195]\n"
    "ds_read2st64_b32 v[204:205], v250 offset0:0 offset1:1\n"
    "v_mfma_f32_16x16x32_bf16 v[184:187], a[172:175], a[104:107], v[184:187]\n"
    "s_waitcnt vmcnt(11)\n"
    "v_smfmac_f32_16x16x64_bf16 v[156:159], a[192:195], a[200:207], v152\n"
    "v_mfma_f32_16x16x32_bf16 v[184:187], a[168:171], a[100:103], v[184:187]\n"
    "buffer_load_dwordx4 v248, s[8:11], s40 offen offset:1024 sc1 lds\n"
    "v_mfma_f32_16x16x32_bf16 v[176:179], a[168:171], a[68:71], v[176:179]\n"
    "ds_read2st64_b32 v[206:207], v250 offset0:2 offset1:3\n"
    "v_mfma_f32_16x16x32_bf16 v[176:179], a[172:175], a[72:75], v[176:179]\n"
    "v_mfma_f32_16x16x32_bf16 v[168:171], a[172:175], a[40:43], v[168:171]\n"
    "s_waitcnt vmcnt(10)\n"
    "v_smfmac_f32_16x16x64_bf16 v[156:159], a[192:195], a[208:215], v152\n"
    "buffer_load_dwordx4 v248, s[8:11], s40 offen offset:2048 sc1 lds\n"
    "v_mfma_f32_16x16x32_bf16 v[168:171], a[168:171], a[36:39], v[168:171]\n"
    "ds_read2st64_b32 v[212:213], v250 offset0:4 offset1:5\n"
    "v_mfma_f32_16x16x32_bf16 v[160:163], a[168:171], a[4:7], v[160:163]\n"
    "ds_read_b128 a[168:171], v241 offset:34816\n"
    "v_mfma_f32_16x16x32_bf16 v[160:163], a[172:175], a[8:11], v[160:163]\n"
    "ds_read_b128 a[172:175], v240 offset:38912\n"
    "v_mfma_f32_16x16x32_bf16 v[160:163], a[176:179], a[12:15], v[160:163]\n"
    "s_waitcnt vmcnt(9)\n"
    "buffer_load_dwordx4 v248, s[8:11], s40 offen offset:3072 sc1 lds\n"
    "v_smfmac_f32_16x16x64_bf16 v[156:159], a[192:195], a[216:223], v152\n"
    "s_add_u32 m0, s43, 4096\n"
    "ds_read2st64_b32 v[214:215], v250 offset0:6 offset1:7\n"
    "v_mfma_f32_16x16x32_bf16 v[160:163], a[180:183], a[16:19], v[160:163]\n"
    "s_add_u32 s40, s40, 0x8000\n"
    "v_mfma_f32_16x16x32_bf16 v[168:171], a[180:183], a[48:51], v[168:171]\n"
    "v_mfma_f32_16x16x32_bf16 v[168:171], a[176:179], a[44:47], v[168:171]\n"
    "buffer_load_dwordx4 v248, s[8:11], s41 offen offset:0 sc1 lds\n"
    "v_mfma_f32_16x16x32_bf16 v[176:179], a[176:179], a[76:79], v[176:179]\n"
    "ds_read2st64_b32 v[220:221], v250 offset0:8 offset1:9\n"
    "v_mfma_f32_16x16x32_bf16 v[176:179], a[180:183], a[80:83], v[176:179]\n"
    "v_mfma_f32_16x16x32_bf16 v[184:187], a[180:183], a[112:115], v[184:187]\n"
    "v_mfma_f32_16x16x32_bf16 v[184:187], a[176:179], a[108:111], v[184:187]\n"
    "buffer_load_dwordx4 v248, s[8:11], s41 offen offset:1024 sc1 lds\n"
    "v_mfma_f32_16x16x32_bf16 v[192:195], a[176:179], a[140:143], v[192:195]\n"
    "ds_read_b128 a[176:179], v241 offset:38912\n"
    "v_mfma_f32_16x16x32_bf16 v[192:195], a[180:183], a[144:147], v[192:195]\n"
    "ds_read_b128 a[180:183], v242 offset:34816\n"
    "v_mfma_f32_16x16x32_bf16 v[192:195], a[184:187], a[148:151], v[192:195]\n"
    "ds_read2st64_b32 v[222:223], v250 offset0:10 offset1:11\n"
    "v_mfma_f32_16x16x32_bf16 v[192:195], a[188:191], a[152:155], v[192:195]\n"
    "buffer_load_dwordx4 v248, s[8:11], s41 offen offset:2048 sc1 lds\n"
    "v_mfma_f32_16x16x32_bf16 v[184:187], a[188:191], a[120:123], v[184:187]\n"
    "v_mfma_f32_16x16x32_bf16 v[184:187], a[184:187], a[116:119], v[184:187]\n"
    "ds_read2st64_b32 v[228:229], v250 offset0:12 offset1:13\n"
    "v_mfma_f32_16x16x32_bf16 v[176:179], a[184:187], a[84:87], v[176:179]\n"
    "s_waitcnt vmcnt(11)\n"
    "v_exp_f32_e32 v192, v192\n"
    "v_smfmac_f32_16x16x64_bf16 v[156:159], a[192:195], a[224:231], v152\n"
    "buffer_load_dwordx4 v248, s[8:11], s41 offen offset:3072 sc1 lds\n"
    "v_mfma_f32_16x16x32_bf16 v[176:179], a[188:191], a[88:91], v[176:179]\n"
    "s_add_u32 s41, s41, 0x8000\n"
    "v_exp_f32_e32 v193, v193\n"
    "v_mfma_f32_16x16x32_bf16 v[168:171], a[188:191], a[56:59], v[168:171]\n"
    "ds_read2st64_b32 v[230:231], v250 offset0:14 offset1:15\n"
    "v_mfma_f32_16x16x32_bf16 v[168:171], a[184:187], a[52:55], v[168:171]\n"
    "v_exp_f32_e32 v194, v194\n"
    "v_mfma_f32_16x16x32_bf16 v[160:163], a[184:187], a[20:23], v[160:163]\n"
    "s_waitcnt vmcnt(10)\n"
    "ds_read_b128 a[184:187], v243 offset:34816\n"
    "v_smfmac_f32_16x16x64_bf16 v[156:159], a[192:195], a[232:239], v152\n"
    "v_exp_f32_e32 v195, v195\n"
    "v_mfma_f32_16x16x32_bf16 v[160:163], a[188:191], a[24:27], v[160:163]\n"
    "s_waitcnt lgkmcnt(8)\n"
    "ds_read_b128 a[188:191], v242 offset:38912\n"
    "v_mfma_f32_16x16x32_bf16 v[164:167], a[160:163], a[28:31], 0\n"
    "ds_read2st64_b32 v[236:237], v250 offset0:16 offset1:17\n"
    "v_mfma_f32_16x16x32_bf16 v[164:167], a[164:167], a[0:3], v[164:167]\n"
    "v_cvt_pk_bf16_f32 v192, v192, v193\n"
    "v_mfma_f32_16x16x32_bf16 v[172:175], a[164:167], a[32:35], 0\n"
    "s_waitcnt vmcnt(8)\n"
    "v_exp_f32_e32 v184, v184\n"
    "v_smfmac_f32_16x16x64_bf16 v[156:159], a[192:195], a[240:247], v152\n"
    "v_exp_f32_e32 v185, v185\n"
    "v_mfma_f32_16x16x32_bf16 v[172:175], a[160:163], a[60:63], v[172:175]\n"
    "ds_read2st64_b32 v[238:239], v250 offset0:18 offset1:19\n"
    "v_mfma_f32_16x16x32_bf16 v[180:183], a[160:163], a[92:95], 0\n"
    "v_mfma_f32_16x16x32_bf16 v[180:183], a[164:167], a[64:67], v[180:183]\n"
    "v_cvt_pk_bf16_f32 v193, v194, v195\n"
    "v_mfma_f32_16x16x32_bf16 v[188:191], a[164:167], a[96:99], 0\n"
    "v_exp_f32_e32 v186, v186\n"
    "v_mfma_f32_16x16x32_bf16 v[188:191], a[160:163], a[124:127], v[188:191]\n"
    "v_exp_f32_e32 v187, v187\n"
    "v_mfma_f32_16x16x32_bf16 v[196:199], a[160:163], a[156:159], 0\n"
    "ds_read_b64_tr_b16 a[160:161], v244 offset:16384\n"
    "v_and_b32_e32 v253, 0xffff0000, v255\n"
    "v_mfma_f32_16x16x32_bf16 v[196:199], a[164:167], a[128:131], v[196:199]\n"
    "ds_read_b64_tr_b16 a[162:163], v244 offset:18432\n"
    "v_mfma_f32_16x16x32_bf16 v[196:199], a[168:171], a[132:135], v[196:199]\n"
    "ds_read_b64_tr_b16 a[164:165], v244 offset:24576\n"
    "v_mfma_f32_16x16x32_bf16 v[196:199], a[172:175], a[136:139], v[196:199]\n"
    "ds_read_b64_tr_b16 a[166:167], v244 offset:26624\n"
    "v_mfma_f32_16x16x32_bf16 v[188:191], a[172:175], a[104:107], v[188:191]\n"
    "v_cvt_pk_bf16_f32 v184, v184, v185\n"
    "v_mfma_f32_16x16x32_bf16 v[188:191], a[168:171], a[100:103], v[188:191]\n"
    "v_exp_f32_e32 v176, v176\n"
    "v_mfma_f32_16x16x32_bf16 v[180:183], a[168:171], a[68:71], v[180:183]\n"
    "v_exp_f32_e32 v177, v177\n"
    "v_mfma_f32_16x16x32_bf16 v[180:183], a[172:175], a[72:75], v[180:183]\n"
    "v_cvt_pk_bf16_f32 v185, v186, v187\n"
    "v_mul_f32_e32 v156, v253, v156\n"
    "v_mfma_f32_16x16x32_bf16 v[172:175], a[172:175], a[40:43], v[172:175]\n"
    "v_exp_f32_e32 v178, v178\n"
    "v_mfma_f32_16x16x32_bf16 v[172:175], a[168:171], a[36:39], v[172:175]\n"
    "v_exp_f32_e32 v179, v179\n"
    "v_mfma_f32_16x16x32_bf16 v[164:167], a[168:171], a[4:7], v[164:167]\n"
    "ds_read_b64_tr_b16 a[168:169], v245 offset:16384\n"
    "v_mfma_f32_16x16x32_bf16 v[164:167], a[172:175], a[8:11], v[164:167]\n"
    "s_waitcnt lgkmcnt(7)\n"
    "ds_read_b64_tr_b16 a[170:171], v245 offset:18432\n"
    "v_mfma_f32_16x16x32_bf16 v[164:167], a[176:179], a[12:15], v[164:167]\n"
    "ds_read_b64_tr_b16 a[172:173], v245 offset:24576\n"
    "v_mfma_f32_16x16x32_bf16 v[164:167], a[180:183], a[16:19], v[164:167]\n"
    "ds_read_b64_tr_b16 a[174:175], v245 offset:26624\n"
    "v_mfma_f32_16x16x32_bf16 v[172:175], a[180:183], a[48:51], v[172:175]\n"
    "v_cvt_pk_bf16_f32 v176, v176, v177\n"
    "v_mfma_f32_16x16x32_bf16 v[172:175], a[176:179], a[44:47], v[172:175]\n"
    "v_exp_f32_e32 v168, v168\n"
    "v_mfma_f32_16x16x32_bf16 v[180:183], a[176:179], a[76:79], v[180:183]\n"
    "v_mul_f32_e32 v157, v253, v157\n"
    "v_mfma_f32_16x16x32_bf16 v[180:183], a[180:183], a[80:83], v[180:183]\n"
    "v_exp_f32_e32 v169, v169\n"
    "v_mfma_f32_16x16x32_bf16 v[188:191], a[180:183], a[112:115], v[188:191]\n"
    "v_cvt_pk_bf16_f32 v177, v178, v179\n"
    "v_mfma_f32_16x16x32_bf16 v[188:191], a[176:179], a[108:111], v[188:191]\n"
    "v_exp_f32_e32 v170, v170\n"
    "v_mfma_f32_16x16x32_bf16 v[196:199], a[176:179], a[140:143], v[196:199]\n"
    "ds_read_b64_tr_b16 a[176:177], v246 offset:16384\n"
    "v_mul_f32_e32 v158, v253, v158\n"
    "v_mfma_f32_16x16x32_bf16 v[196:199], a[180:183], a[144:147], v[196:199]\n"
    "ds_read_b64_tr_b16 a[178:179], v246 offset:18432\n"
    "v_mfma_f32_16x16x32_bf16 v[196:199], a[184:187], a[148:151], v[196:199]\n"
    "ds_read_b64_tr_b16 a[180:181], v246 offset:24576\n"
    "v_mfma_f32_16x16x32_bf16 v[196:199], a[188:191], a[152:155], v[196:199]\n"
    "ds_read_b64_tr_b16 a[182:183], v246 offset:26624\n"
    "v_mfma_f32_16x16x32_bf16 v[188:191], a[188:191], a[120:123], v[188:191]\n"
    "v_exp_f32_e32 v171, v171\n"
    "v_mfma_f32_16x16x32_bf16 v[188:191], a[184:187], a[116:119], v[188:191]\n"
    "v_cvt_pk_bf16_f32 v168, v168, v169\n"
    "v_mul_f32_e32 v159, v253, v159\n"
    "v_mfma_f32_16x16x32_bf16 v[180:183], a[184:187], a[84:87], v[180:183]\n"
    "v_exp_f32_e32 v160, v160\n"
    "v_mfma_f32_16x16x32_bf16 v[180:183], a[188:191], a[88:91], v[180:183]\n"
    "v_exp_f32_e32 v161, v161\n"
    "v_mfma_f32_16x16x32_bf16 v[172:175], a[188:191], a[56:59], v[172:175]\n"
    "v_cvt_pk_bf16_f32 v169, v170, v171\n"
    "v_mfma_f32_16x16x32_bf16 v[172:175], a[184:187], a[52:55], v[172:175]\n"
    "v_exp_f32_e32 v162, v162\n"
    "v_mfma_f32_16x16x32_bf16 v[164:167], a[184:187], a[20:23], v[164:167]\n"
    "ds_read_b64_tr_b16 a[184:185], v247 offset:16384\n"
    "v_mfma_f32_16x16x32_bf16 v[164:167], a[188:191], a[24:27], v[164:167]\n"
    "s_waitcnt lgkmcnt(5)\n"
    "ds_read_b64_tr_b16 a[186:187], v247 offset:18432\n"
    "v_mfma_f32_16x16x32_bf16 a[248:251], a[160:163], v[200:203], a[248:251]\n"
    "ds_read_b64_tr_b16 a[188:189], v247 offset:24576\n"
    "v_mad_u32_u16 v253, v255, s42, v153 op_sel:[0,0,0,0]\n"
    "v_mfma_f32_16x16x32_bf16 a[248:251], a[164:167], v[204:207], a[248:251]\n"
    "ds_read_b64_tr_b16 a[190:191], v247 offset:26624\n"
    "v_mfma_f32_16x16x32_bf16 a[252:255], a[164:167], v[212:215], a[252:255]\n"
    "v_exp_f32_e32 v163, v163\n"
    "v_mfma_f32_16x16x32_bf16 a[252:255], a[160:163], v[208:211], a[252:255]\n"
    "v_cvt_pk_bf16_f32 v160, v160, v161\n"
    "v_mfma_f32_16x16x32_bf16 v[56:59], a[160:163], v[216:219], v[56:59]\n"
    "v_cvt_pk_bf16_f32 v154, v156, v157\n"
    "v_mfma_f32_16x16x32_bf16 v[56:59], a[164:167], v[220:223], v[56:59]\n"
    "v_cvt_pk_bf16_f32 v161, v162, v163\n"
    "v_mfma_f32_16x16x32_bf16 v[60:63], a[164:167], v[228:231], v[60:63]\n"
    "v_exp_f32_e32 v196, v196\n"
    "v_mfma_f32_16x16x32_bf16 v[60:63], a[160:163], v[224:227], v[60:63]\n"
    "v_exp_f32_e32 v197, v197\n"
    "v_mfma_f32_16x16x32_bf16 v[64:67], a[160:163], v[232:235], v[64:67]\n"
    "ds_read_b64_tr_b16 a[160:161], v244 offset:20480\n"
    "v_mfma_f32_16x16x32_bf16 v[64:67], a[164:167], v[236:239], v[64:67]\n"
    "ds_read_b64_tr_b16 a[162:163], v244 offset:22528\n"
    "v_mfma_f32_16x16x32_bf16 v[76:79], a[168:171], v[232:235], v[76:79]\n"
    "ds_read_b64_tr_b16 a[164:165], v244 offset:28672\n"
    "v_mfma_f32_16x16x32_bf16 v[76:79], a[172:175], v[236:239], v[76:79]\n"
    "ds_read_b64_tr_b16 a[166:167], v244 offset:30720\n"
    "v_mfma_f32_16x16x32_bf16 v[72:75], a[172:175], v[228:231], v[72:75]\n"
    "v_exp_f32_e32 v198, v198\n"
    "v_mfma_f32_16x16x32_bf16 v[72:75], a[168:171], v[224:227], v[72:75]\n"
    "v_exp_f32_e32 v199, v199\n"
    "v_mfma_f32_16x16x32_bf16 v[68:71], a[168:171], v[216:219], v[68:71]\n"
    "v_cvt_pk_bf16_f32 v194, v196, v197\n"
    "v_cvt_pk_bf16_f32 v155, v158, v159\n"
    "v_mfma_f32_16x16x32_bf16 v[68:71], a[172:175], v[220:223], v[68:71]\n"
    "v_exp_f32_e32 v188, v188\n"
    "v_mfma_f32_16x16x32_bf16 v[4:7], a[172:175], v[212:215], v[4:7]\n"
    "v_exp_f32_e32 v189, v189\n"
    "v_mfma_f32_16x16x32_bf16 v[4:7], a[168:171], v[208:211], v[4:7]\n"
    "v_cvt_pk_bf16_f32 v195, v198, v199\n"
    "v_mfma_f32_16x16x32_bf16 v[0:3], a[168:171], v[200:203], v[0:3]\n"
    "ds_read_b64_tr_b16 a[168:169], v245 offset:20480\n"
    "v_mfma_f32_16x16x32_bf16 v[0:3], a[172:175], v[204:207], v[0:3]\n"
    "s_waitcnt lgkmcnt(5)\n"
    "ds_read_b64_tr_b16 a[170:171], v245 offset:22528\n"
    "v_mfma_f32_16x16x32_bf16 v[8:11], a[176:179], v[200:203], v[8:11]\n"
    "ds_read_b64_tr_b16 a[172:173], v245 offset:28672\n"
    "v_mov_b32_e32 v156, 0\n"
    "v_mfma_f32_16x16x32_bf16 v[8:11], a[180:183], v[204:207], v[8:11]\n"
    "ds_read_b64_tr_b16 a[174:175], v245 offset:30720\n"
    "v_mfma_f32_16x16x32_bf16 v[12:15], a[180:183], v[212:215], v[12:15]\n"
    "v_exp_f32_e32 v190, v190\n"
    "v_mfma_f32_16x16x32_bf16 v[12:15], a[176:179], v[208:211], v[12:15]\n"
    "v_exp_f32_e32 v191, v191\n"
    "v_mfma_f32_16x16x32_bf16 v[80:83], a[176:179], v[216:219], v[80:83]\n"
    "v_mov_b32_e32 v157, 0\n"
    "v_mfma_f32_16x16x32_bf16 v[80:83], a[180:183], v[220:223], v[80:83]\n"
    "v_cvt_pk_bf16_f32 v186, v188, v189\n"
    "v_mfma_f32_16x16x32_bf16 v[84:87], a[180:183], v[228:231], v[84:87]\n"
    "v_exp_f32_e32 v180, v180\n"
    "v_mfma_f32_16x16x32_bf16 v[84:87], a[176:179], v[224:227], v[84:87]\n"
    "v_exp_f32_e32 v181, v181\n"
    "v_mfma_f32_16x16x32_bf16 v[88:91], a[176:179], v[232:235], v[88:91]\n"
    "ds_read_b64_tr_b16 a[176:177], v246 offset:20480\n"
    "v_mfma_f32_16x16x32_bf16 v[88:91], a[180:183], v[236:239], v[88:91]\n"
    "ds_read_b64_tr_b16 a[178:179], v246 offset:22528\n"
    "v_mfma_f32_16x16x32_bf16 v[100:103], a[184:187], v[232:235], v[100:103]\n"
    "ds_read_b64_tr_b16 a[180:181], v246 offset:28672\n"
    "v_mfma_f32_16x16x32_bf16 v[100:103], a[188:191], v[236:239], v[100:103]\n"
    "ds_read_b64_tr_b16 a[182:183], v246 offset:30720\n"
    "v_mfma_f32_16x16x32_bf16 v[96:99], a[188:191], v[228:231], v[96:99]\n"
    "v_cvt_pk_bf16_f32 v187, v190, v191\n"
    "v_mov_b32_e32 v158, 0\n"
    "v_mfma_f32_16x16x32_bf16 v[96:99], a[184:187], v[224:227], v[96:99]\n"
    "v_exp_f32_e32 v182, v182\n"
    "v_mfma_f32_16x16x32_bf16 v[92:95], a[184:187], v[216:219], v[92:95]\n"
    "v_exp_f32_e32 v183, v183\n"
    "v_mfma_f32_16x16x32_bf16 v[92:95], a[188:191], v[220:223], v[92:95]\n"
    "v_cvt_pk_bf16_f32 v178, v180, v181\n"
    "v_mfma_f32_16x16x32_bf16 v[20:23], a[188:191], v[212:215], v[20:23]\n"
    "v_exp_f32_e32 v172, v172\n"
    "v_mfma_f32_16x16x32_bf16 v[20:23], a[184:187], v[208:211], v[20:23]\n"
    "v_exp_f32_e32 v173, v173\n"
    "v_mfma_f32_16x16x32_bf16 v[16:19], a[184:187], v[200:203], v[16:19]\n"
    "ds_read_b64_tr_b16 a[184:185], v247 offset:20480\n"
    "v_mov_b32_e32 v159, 0\n"
    "v_mfma_f32_16x16x32_bf16 v[16:19], a[188:191], v[204:207], v[16:19]\n"
    "s_waitcnt lgkmcnt(5)\n"
    "s_mov_b32 m0, s49\n"
    "ds_read_b64_tr_b16 a[186:187], v247 offset:22528\n"
    "v_mfma_f32_16x16x32_bf16 v[24:27], a[160:163], v[200:203], v[24:27]\n"
    "ds_read_b64_tr_b16 a[188:189], v247 offset:28672\n"
    "v_mfma_f32_16x16x32_bf16 v[24:27], a[164:167], v[204:207], v[24:27]\n"
    "ds_read_b64_tr_b16 a[190:191], v247 offset:30720\n"
    "v_mfma_f32_16x16x32_bf16 v[28:31], a[164:167], v[212:215], v[28:31]\n"
    "buffer_store_dwordx2 v[154:155], v253, s[24:27], 0 offen offset:0 sc1\n"
    "v_cvt_pk_bf16_f32 v179, v182, v183\n"
    "v_mfma_f32_16x16x32_bf16 v[28:31], a[160:163], v[208:211], v[28:31]\n"
    "v_exp_f32_e32 v174, v174\n"
    "v_mfma_f32_16x16x32_bf16 v[104:107], a[160:163], v[216:219], v[104:107]\n"
    "v_mfma_f32_16x16x32_bf16 v[104:107], a[164:167], v[220:223], v[104:107]\n"
    "v_exp_f32_e32 v175, v175\n"
    "v_mfma_f32_16x16x32_bf16 v[108:111], a[164:167], v[228:231], v[108:111]\n"
    "v_cvt_pk_bf16_f32 v170, v172, v173\n"
    "v_mfma_f32_16x16x32_bf16 v[108:111], a[160:163], v[224:227], v[108:111]\n"
    "s_waitcnt vmcnt(9)\n"
    "s_barrier\n"
    "v_mfma_f32_16x16x32_bf16 v[112:115], a[160:163], v[232:235], v[112:115]\n"
    "ds_read_b128 a[160:163], v243 offset:53248\n"
    "v_mfma_f32_16x16x32_bf16 v[112:115], a[164:167], v[236:239], v[112:115]\n"
    "ds_read_b128 a[164:167], v240 offset:49152\n"
    "v_mfma_f32_16x16x32_bf16 v[124:127], a[168:171], v[232:235], v[124:127]\n"
    "v_exp_f32_e32 v164, v164\n"
    "v_mfma_f32_16x16x32_bf16 v[124:127], a[172:175], v[236:239], v[124:127]\n"
    "v_exp_f32_e32 v165, v165\n"
    "v_mfma_f32_16x16x32_bf16 v[120:123], a[172:175], v[228:231], v[120:123]\n"
    "ds_write_addtid_b32 v192 offset:4096\n"
    "v_cvt_pk_bf16_f32 v171, v174, v175\n"
    "v_mfma_f32_16x16x32_bf16 v[120:123], a[168:171], v[224:227], v[120:123]\n"
    "ds_write_addtid_b32 v193 offset:4352\n"
    "v_mfma_f32_16x16x32_bf16 v[116:119], a[168:171], v[216:219], v[116:119]\n"
    "v_exp_f32_e32 v166, v166\n"
    "v_mfma_f32_16x16x32_bf16 v[116:119], a[172:175], v[220:223], v[116:119]\n"
    "v_exp_f32_e32 v167, v167\n"
    "v_mfma_f32_16x16x32_bf16 v[36:39], a[172:175], v[212:215], v[36:39]\n"
    "ds_write_addtid_b32 v184 offset:3072\n"
    "v_cvt_pk_bf16_f32 v162, v164, v165\n"
    "v_mfma_f32_16x16x32_bf16 v[36:39], a[168:171], v[208:211], v[36:39]\n"
    "ds_write_addtid_b32 v185 offset:3328\n"
    "v_cvt_pk_bf16_f32 v163, v166, v167\n"
    "v_mfma_f32_16x16x32_bf16 v[32:35], a[168:171], v[200:203], v[32:35]\n"
    "ds_read_b128 a[168:171], v241 offset:49152\n"
    "v_mfma_f32_16x16x32_bf16 v[32:35], a[172:175], v[204:207], v[32:35]\n"
    "s_waitcnt lgkmcnt(7)\n"
    "ds_read_b128 a[172:175], v240 offset:53248\n"
    "v_mfma_f32_16x16x32_bf16 v[40:43], a[176:179], v[200:203], v[40:43]\n"
    "ds_write_addtid_b32 v176 offset:2048\n"
    "v_mfma_f32_16x16x32_bf16 v[40:43], a[180:183], v[204:207], v[40:43]\n"
    "ds_write_addtid_b32 v177 offset:2304\n"
    "v_mfma_f32_16x16x32_bf16 v[44:47], a[180:183], v[212:215], v[44:47]\n"
    "ds_write_addtid_b32 v168 offset:1024\n"
    "v_mfma_f32_16x16x32_bf16 v[44:47], a[176:179], v[208:211], v[44:47]\n"
    "ds_write_addtid_b32 v169 offset:1280\n"
    "v_mfma_f32_16x16x32_bf16 v[128:131], a[176:179], v[216:219], v[128:131]\n"
    "ds_write_addtid_b32 v160 offset:0\n"
    "v_mfma_f32_16x16x32_bf16 v[128:131], a[180:183], v[220:223], v[128:131]\n"
    "ds_write_addtid_b32 v161 offset:256\n"
    "v_mfma_f32_16x16x32_bf16 v[132:135], a[180:183], v[228:231], v[132:135]\n"
    "ds_write_addtid_b32 v194 offset:4608\n"
    "v_mfma_f32_16x16x32_bf16 v[132:135], a[176:179], v[224:227], v[132:135]\n"
    "ds_write_addtid_b32 v195 offset:4864\n"
    "v_mfma_f32_16x16x32_bf16 v[136:139], a[176:179], v[232:235], v[136:139]\n"
    "ds_read_b128 a[176:179], v241 offset:53248\n"
    "v_mfma_f32_16x16x32_bf16 v[136:139], a[180:183], v[236:239], v[136:139]\n"
    "ds_read_b128 a[180:183], v242 offset:49152\n"
    "v_mfma_f32_16x16x32_bf16 v[148:151], a[184:187], v[232:235], v[148:151]\n"
    "ds_write_addtid_b32 v186 offset:3584\n"
    "v_mfma_f32_16x16x32_bf16 v[148:151], a[188:191], v[236:239], v[148:151]\n"
    "ds_write_addtid_b32 v187 offset:3840\n"
    "v_mfma_f32_16x16x32_bf16 v[144:147], a[188:191], v[228:231], v[144:147]\n"
    "ds_write_addtid_b32 v178 offset:2560\n"
    "v_mfma_f32_16x16x32_bf16 v[144:147], a[184:187], v[224:227], v[144:147]\n"
    "ds_write_addtid_b32 v179 offset:2816\n"
    "v_mfma_f32_16x16x32_bf16 v[140:143], a[184:187], v[216:219], v[140:143]\n"
    "ds_write_addtid_b32 v170 offset:1536\n"
    "v_mfma_f32_16x16x32_bf16 v[140:143], a[188:191], v[220:223], v[140:143]\n"
    "ds_write_addtid_b32 v171 offset:1792\n"
    "v_mfma_f32_16x16x32_bf16 v[52:55], a[188:191], v[212:215], v[52:55]\n"
    "ds_write_addtid_b32 v162 offset:512\n"
    "v_mfma_f32_16x16x32_bf16 v[52:55], a[184:187], v[208:211], v[52:55]\n"
    "ds_write_addtid_b32 v163 offset:768\n"
    "v_mfma_f32_16x16x32_bf16 v[48:51], a[184:187], v[200:203], v[48:51]\n"
    "ds_read_b128 a[184:187], v243 offset:49152\n"
    "v_mfma_f32_16x16x32_bf16 v[48:51], a[188:191], v[204:207], v[48:51]\n"
    "s_waitcnt lgkmcnt(15)\n"
    "ds_read_b128 a[188:191], v242 offset:53248\n"
    "v_mfma_f32_16x16x32_bf16 v[200:203], a[160:163], a[28:31], 0\n"
    "v_mfma_f32_16x16x32_bf16 v[200:203], a[164:167], a[0:3], v[200:203]\n"
    "v_mfma_f32_16x16x32_bf16 v[208:211], a[164:167], a[32:35], 0\n"
    "v_mfma_f32_16x16x32_bf16 v[208:211], a[160:163], a[60:63], v[208:211]\n"
    "v_mfma_f32_16x16x32_bf16 v[216:219], a[160:163], a[92:95], 0\n"
    "v_mfma_f32_16x16x32_bf16 v[216:219], a[164:167], a[64:67], v[216:219]\n"
    "v_mfma_f32_16x16x32_bf16 v[224:227], a[164:167], a[96:99], 0\n"
    "s_add_u32 m0, s43, 16384\n"
    "v_mfma_f32_16x16x32_bf16 v[224:227], a[160:163], a[124:127], v[224:227]\n"
    "s_waitcnt lgkmcnt(0)\n"
    "s_barrier\n"
    "v_mfma_f32_16x16x32_bf16 v[232:235], a[160:163], a[156:159], 0\n"
    "ds_read_b128 a[160:163], v243 offset:55296\n"
    "v_mfma_f32_16x16x32_bf16 v[232:235], a[164:167], a[128:131], v[232:235]\n"
    "ds_read_b128 a[164:167], v240 offset:51200\n"
    "v_mfma_f32_16x16x32_bf16 v[232:235], a[168:171], a[132:135], v[232:235]\n"
    "buffer_load_dwordx4 v248, s[8:11], s40 offen offset:0 sc1 lds\n"
    "v_mfma_f32_16x16x32_bf16 v[232:235], a[172:175], a[136:139], v[232:235]\n"
    "ds_read2st64_b32 v[164:165], v250 offset0:0 offset1:1\n"
    "v_mfma_f32_16x16x32_bf16 v[224:227], a[172:175], a[104:107], v[224:227]\n"
    "v_mfma_f32_16x16x32_bf16 v[224:227], a[168:171], a[100:103], v[224:227]\n"
    "ds_read2st64_b32 v[166:167], v250 offset0:2 offset1:3\n"
    "v_mfma_f32_16x16x32_bf16 v[216:219], a[168:171], a[68:71], v[216:219]\n"
    "buffer_load_dwordx4 v248, s[8:11], s40 offen offset:1024 sc1 lds\n"
    "v_mfma_f32_16x16x32_bf16 v[216:219], a[172:175], a[72:75], v[216:219]\n"
    "v_mfma_f32_16x16x32_bf16 v[208:211], a[172:175], a[40:43], v[208:211]\n"
    "v_mfma_f32_16x16x32_bf16 v[208:211], a[168:171], a[36:39], v[208:211]\n"
    "ds_read2st64_b32 v[172:173], v250 offset0:4 offset1:5\n"
    "v_mfma_f32_16x16x32_bf16 v[200:203], a[168:171], a[4:7], v[200:203]\n"
    "ds_read_b128 a[168:171], v241 offset:51200\n"
    "buffer_load_dwordx4 v248, s[8:11], s40 offen offset:2048 sc1 lds\n"
    "v_mfma_f32_16x16x32_bf16 v[200:203], a[172:175], a[8:11], v[200:203]\n"
    "ds_read_b128 a[172:175], v240 offset:55296\n"
    "v_mfma_f32_16x16x32_bf16 v[200:203], a[176:179], a[12:15], v[200:203]\n"
    "v_mfma_f32_16x16x32_bf16 v[200:203], a[180:183], a[16:19], v[200:203]\n"
    "ds_read2st64_b32 v[174:175], v250 offset0:6 offset1:7\n"
    "v_mfma_f32_16x16x32_bf16 v[208:211], a[180:183], a[48:51], v[208:211]\n"
    "buffer_load_dwordx4 v248, s[8:11], s40 offen offset:3072 sc1 lds\n"
    "v_mfma_f32_16x16x32_bf16 v[208:211], a[176:179], a[44:47], v[208:211]\n"
    "s_add_u32 m0, s43, 20480\n"
    "v_mfma_f32_16x16x32_bf16 v[216:219], a[176:179], a[76:79], v[216:219]\n"
    "s_add_u32 s40, s40, 0x8000\n"
    "v_mfma_f32_16x16x32_bf16 v[216:219], a[180:183], a[80:83], v[216:219]\n"
    "ds_read2st64_b32 v[180:181], v250 offset0:8 offset1:9\n"
    "v_mfma_f32_16x16x32_bf16 v[224:227], a[180:183], a[112:115], v[224:227]\n"
    "buffer_load_dwordx4 v248, s[8:11], s41 offen offset:0 sc1 lds\n"
    "v_mfma_f32_16x16x32_bf16 v[224:227], a[176:179], a[108:111], v[224:227]\n"
    "ds_read2st64_b32 v[182:183], v250 offset0:10 offset1:11\n"
    "v_mfma_f32_16x16x32_bf16 v[232:235], a[176:179], a[140:143], v[232:235]\n"
    "ds_read_b128 a[176:179], v241 offset:55296\n"
    "v_mfma_f32_16x16x32_bf16 v[232:235], a[180:183], a[144:147], v[232:235]\n"
    "ds_read_b128 a[180:183], v242 offset:51200\n"
    "v_mfma_f32_16x16x32_bf16 v[232:235], a[184:187], a[148:151], v[232:235]\n"
    "buffer_load_dwordx4 v248, s[8:11], s41 offen offset:1024 sc1 lds\n"
    "v_mfma_f32_16x16x32_bf16 v[232:235], a[188:191], a[152:155], v[232:235]\n"
    "v_mfma_f32_16x16x32_bf16 v[224:227], a[188:191], a[120:123], v[224:227]\n"
    "ds_read2st64_b32 v[188:189], v250 offset0:12 offset1:13\n"
    "v_mfma_f32_16x16x32_bf16 v[224:227], a[184:187], a[116:119], v[224:227]\n"
    "v_mfma_f32_16x16x32_bf16 v[216:219], a[184:187], a[84:87], v[216:219]\n"
    "buffer_load_dwordx4 v248, s[8:11], s41 offen offset:2048 sc1 lds\n"
    "v_mfma_f32_16x16x32_bf16 v[216:219], a[188:191], a[88:91], v[216:219]\n"
    "ds_read2st64_b32 v[190:191], v250 offset0:14 offset1:15\n"
    "v_mfma_f32_16x16x32_bf16 v[208:211], a[188:191], a[56:59], v[208:211]\n"
    "ds_read_b64 v[254:255], v252 offset:64\n"
    "v_mfma_f32_16x16x32_bf16 v[208:211], a[184:187], a[52:55], v[208:211]\n"
    "ds_read2st64_b32 v[196:197], v250 offset0:16 offset1:17\n"
    "v_mfma_f32_16x16x32_bf16 v[200:203], a[184:187], a[20:23], v[200:203]\n"
    "ds_read_b128 a[184:187], v243 offset:51200\n"
    "buffer_load_dwordx4 v248, s[8:11], s41 offen offset:3072 sc1 lds\n"
    "v_mfma_f32_16x16x32_bf16 v[200:203], a[188:191], a[24:27], v[200:203]\n"
    "s_waitcnt lgkmcnt(15)\n"
    "s_add_u32 s41, s41, 0x8000\n"
    "ds_read_b128 a[188:191], v242 offset:55296\n"
    "v_mfma_f32_16x16x32_bf16 v[204:207], a[160:163], a[28:31], 0\n"
    "s_waitcnt lgkmcnt(3)\n"
    "v_mad_u32_u16 v253, v254, s42, v251 op_sel:[0,0,0,0]\n"
    "v_mfma_f32_16x16x32_bf16 v[204:207], a[164:167], a[0:3], v[204:207]\n"
    "buffer_load_dwordx4 a[200:203], v253, s[20:23], 0 offen offset:0 sc1\n"
    "v_mfma_f32_16x16x32_bf16 v[212:215], a[164:167], a[32:35], 0\n"
    "v_exp_f32_e32 v232, v232\n"
    "v_mfma_f32_16x16x32_bf16 v[212:215], a[160:163], a[60:63], v[212:215]\n"
    "ds_read2st64_b32 v[198:199], v250 offset0:18 offset1:19\n"
    "v_mfma_f32_16x16x32_bf16 v[220:223], a[160:163], a[92:95], 0\n"
    "v_mad_u32_u16 v253, v254, s42, v251 op_sel:[1,0,0,0]\n"
    "v_mfma_f32_16x16x32_bf16 v[220:223], a[164:167], a[64:67], v[220:223]\n"
    "buffer_load_dwordx4 a[204:207], v253, s[20:23], 0 offen offset:0 sc1\n"
    "v_mfma_f32_16x16x32_bf16 v[228:231], a[164:167], a[96:99], 0\n"
    "v_exp_f32_e32 v233, v233\n"
    "v_mfma_f32_16x16x32_bf16 v[228:231], a[160:163], a[124:127], v[228:231]\n"
    "v_exp_f32_e32 v234, v234\n"
    "v_mfma_f32_16x16x32_bf16 v[236:239], a[160:163], a[156:159], 0\n"
    "ds_read_b64_tr_b16 a[160:161], v244 offset:32768\n"
    "v_mad_u32_u16 v253, v255, s42, v251 op_sel:[0,0,0,0]\n"
    "v_mfma_f32_16x16x32_bf16 v[236:239], a[164:167], a[128:131], v[236:239]\n"
    "ds_read_b64_tr_b16 a[162:163], v244 offset:34816\n"
    "buffer_load_dwordx4 a[208:211], v253, s[20:23], 0 offen offset:0 sc1\n"
    "v_mfma_f32_16x16x32_bf16 v[236:239], a[168:171], a[132:135], v[236:239]\n"
    "ds_read_b64_tr_b16 a[164:165], v244 offset:40960\n"
    "v_mfma_f32_16x16x32_bf16 v[236:239], a[172:175], a[136:139], v[236:239]\n"
    "ds_read_b64_tr_b16 a[166:167], v244 offset:43008\n"
    "v_mfma_f32_16x16x32_bf16 v[228:231], a[172:175], a[104:107], v[228:231]\n"
    "v_mad_u32_u16 v253, v255, s42, v251 op_sel:[1,0,0,0]\n"
    "v_mfma_f32_16x16x32_bf16 v[228:231], a[168:171], a[100:103], v[228:231]\n"
    "buffer_load_dwordx4 a[212:215], v253, s[20:23], 0 offen offset:0 sc1\n"
    "ds_read_b64 v[254:255], v252 offset:72\n"
    "v_mfma_f32_16x16x32_bf16 v[220:223], a[168:171], a[68:71], v[220:223]\n"
    "v_exp_f32_e32 v235, v235\n"
    "v_mfma_f32_16x16x32_bf16 v[220:223], a[172:175], a[72:75], v[220:223]\n"
    "v_cvt_pk_bf16_f32 v232, v232, v233\n"
    "v_mfma_f32_16x16x32_bf16 v[212:215], a[172:175], a[40:43], v[212:215]\n"
    "v_exp_f32_e32 v224, v224\n"
    "v_mfma_f32_16x16x32_bf16 v[212:215], a[168:171], a[36:39], v[212:215]\n"
    "v_exp_f32_e32 v225, v225\n"
    "v_mfma_f32_16x16x32_bf16 v[204:207], a[168:171], a[4:7], v[204:207]\n"
    "ds_read_b64_tr_b16 a[168:169], v245 offset:32768\n"
    "v_cvt_pk_bf16_f32 v233, v234, v235\n"
    "v_mfma_f32_16x16x32_bf16 v[204:207], a[172:175], a[8:11], v[204:207]\n"
    "ds_read_b64_tr_b16 a[170:171], v245 offset:34816\n"
    "v_mfma_f32_16x16x32_bf16 v[204:207], a[176:179], a[12:15], v[204:207]\n"
    "s_waitcnt lgkmcnt(2)\n"
    "ds_read_b64_tr_b16 a[172:173], v245 offset:40960\n"
    "v_mad_u32_u16 v253, v254, s42, v251 op_sel:[0,0,0,0]\n"
    "v_mfma_f32_16x16x32_bf16 v[204:207], a[180:183], a[16:19], v[204:207]\n"
    "ds_read_b64_tr_b16 a[174:175], v245 offset:43008\n"
    "buffer_load_dwordx4 a[216:219], v253, s[20:23], 0 offen offset:0 sc1\n"
    "v_mfma_f32_16x16x32_bf16 v[212:215], a[180:183], a[48:51], v[212:215]\n"
    "v_exp_f32_e32 v226, v226\n"
    "v_mfma_f32_16x16x32_bf16 v[212:215], a[176:179], a[44:47], v[212:215]\n"
    "v_exp_f32_e32 v227, v227\n"
    "v_mfma_f32_16x16x32_bf16 v[220:223], a[176:179], a[76:79], v[220:223]\n"
    "v_mad_u32_u16 v253, v254, s42, v251 op_sel:[1,0,0,0]\n"
    "v_cvt_pk_bf16_f32 v224, v224, v225\n"
    "v_mfma_f32_16x16x32_bf16 v[220:223], a[180:183], a[80:83], v[220:223]\n"
    "buffer_load_dwordx4 a[220:223], v253, s[20:23], 0 offen offset:0 sc1\n"
    "v_mfma_f32_16x16x32_bf16 v[228:231], a[180:183], a[112:115], v[228:231]\n"
    "v_exp_f32_e32 v216, v216\n"
    "v_mfma_f32_16x16x32_bf16 v[228:231], a[176:179], a[108:111], v[228:231]\n"
    "v_exp_f32_e32 v217, v217\n"
    "v_mfma_f32_16x16x32_bf16 v[236:239], a[176:179], a[140:143], v[236:239]\n"
    "ds_read_b64_tr_b16 a[176:177], v246 offset:32768\n"
    "v_cvt_pk_bf16_f32 v225, v226, v227\n"
    "v_mfma_f32_16x16x32_bf16 v[236:239], a[180:183], a[144:147], v[236:239]\n"
    "ds_read_b64_tr_b16 a[178:179], v246 offset:34816\n"
    "v_mfma_f32_16x16x32_bf16 v[236:239], a[184:187], a[148:151], v[236:239]\n"
    "ds_read_b64_tr_b16 a[180:181], v246 offset:40960\n"
    "v_mfma_f32_16x16x32_bf16 v[236:239], a[188:191], a[152:155], v[236:239]\n"
    "ds_read_b64_tr_b16 a[182:183], v246 offset:43008\n"
    "v_mfma_f32_16x16x32_bf16 v[228:231], a[188:191], a[120:123], v[228:231]\n"
    "ds_read_b64 v[254:255], v252 offset:96\n"
    "v_mfma_f32_16x16x32_bf16 v[228:231], a[184:187], a[116:119], v[228:231]\n"
    "v_exp_f32_e32 v218, v218\n"
    "v_mfma_f32_16x16x32_bf16 v[220:223], a[184:187], a[84:87], v[220:223]\n"
    "v_exp_f32_e32 v219, v219\n"
    "v_mfma_f32_16x16x32_bf16 v[220:223], a[188:191], a[88:91], v[220:223]\n"
    "v_cvt_pk_bf16_f32 v216, v216, v217\n"
    "v_mfma_f32_16x16x32_bf16 v[212:215], a[188:191], a[56:59], v[212:215]\n"
    "s_waitcnt lgkmcnt(0)\n"
    "v_mad_u32_u16 v253, v254, s42, v251 op_sel:[0,0,0,0]\n"
    "v_mfma_f32_16x16x32_bf16 v[212:215], a[184:187], a[52:55], v[212:215]\n"
    "buffer_load_dwordx4 a[224:227], v253, s[20:23], 0 offen offset:0 sc1\n"
    "v_mfma_f32_16x16x32_bf16 v[204:207], a[184:187], a[20:23], v[204:207]\n"
    "ds_read_b64_tr_b16 a[184:185], v247 offset:32768\n"
    "v_mfma_f32_16x16x32_bf16 v[204:207], a[188:191], a[24:27], v[204:207]\n"
    "ds_read_b64_tr_b16 a[186:187], v247 offset:34816\n"
    "v_mfma_f32_16x16x32_bf16 a[248:251], a[160:163], v[160:163], a[248:251]\n"
    "ds_read_b64_tr_b16 a[188:189], v247 offset:40960\n"
    "v_mad_u32_u16 v253, v254, s42, v251 op_sel:[1,0,0,0]\n"
    "v_mfma_f32_16x16x32_bf16 a[248:251], a[164:167], v[164:167], a[248:251]\n"
    "ds_read_b64_tr_b16 a[190:191], v247 offset:43008\n"
    "buffer_load_dwordx4 a[228:231], v253, s[20:23], 0 offen offset:0 sc1\n"
    "v_mfma_f32_16x16x32_bf16 a[252:255], a[164:167], v[172:175], a[252:255]\n"
    "v_exp_f32_e32 v208, v208\n"
    "v_mfma_f32_16x16x32_bf16 a[252:255], a[160:163], v[168:171], a[252:255]\n"
    "v_exp_f32_e32 v209, v209\n"
    "v_mfma_f32_16x16x32_bf16 v[56:59], a[160:163], v[176:179], v[56:59]\n"
    "v_mad_u32_u16 v253, v255, s42, v251 op_sel:[0,0,0,0]\n"
    "v_cvt_pk_bf16_f32 v217, v218, v219\n"
    "v_mfma_f32_16x16x32_bf16 v[56:59], a[164:167], v[180:183], v[56:59]\n"
    "buffer_load_dwordx4 a[232:235], v253, s[20:23], 0 offen offset:0 sc1\n"
    "v_mfma_f32_16x16x32_bf16 v[60:63], a[164:167], v[188:191], v[60:63]\n"
    "v_exp_f32_e32 v210, v210\n"
    "v_mfma_f32_16x16x32_bf16 v[60:63], a[160:163], v[184:187], v[60:63]\n"
    "v_exp_f32_e32 v211, v211\n"
    "v_mfma_f32_16x16x32_bf16 v[64:67], a[160:163], v[192:195], v[64:67]\n"
    "ds_read_b64_tr_b16 a[160:161], v244 offset:36864\n"
    "v_mad_u32_u16 v253, v255, s42, v251 op_sel:[1,0,0,0]\n"
    "v_mfma_f32_16x16x32_bf16 v[64:67], a[164:167], v[196:199], v[64:67]\n"
    "ds_read_b64_tr_b16 a[162:163], v244 offset:38912\n"
    "buffer_load_dwordx4 a[236:239], v253, s[20:23], 0 offen offset:0 sc1\n"
    "v_mfma_f32_16x16x32_bf16 v[76:79], a[168:171], v[192:195], v[76:79]\n"
    "ds_read_b64_tr_b16 a[164:165], v244 offset:45056\n"
    "v_cvt_pk_bf16_f32 v208, v208, v209\n"
    "v_mfma_f32_16x16x32_bf16 v[76:79], a[172:175], v[196:199], v[76:79]\n"
    "ds_read_b64_tr_b16 a[166:167], v244 offset:47104\n"
    "v_mfma_f32_16x16x32_bf16 v[72:75], a[172:175], v[188:191], v[72:75]\n"
    "ds_read_b64 v[254:255], v252 offset:104\n"
    "v_mfma_f32_16x16x32_bf16 v[72:75], a[168:171], v[184:187], v[72:75]\n"
    "v_exp_f32_e32 v200, v200\n"
    "v_mfma_f32_16x16x32_bf16 v[68:71], a[168:171], v[176:179], v[68:71]\n"
    "v_exp_f32_e32 v201, v201\n"
    "v_mfma_f32_16x16x32_bf16 v[68:71], a[172:175], v[180:183], v[68:71]\n"
    "v_cvt_pk_bf16_f32 v209, v210, v211\n"
    "v_mfma_f32_16x16x32_bf16 v[4:7], a[172:175], v[172:175], v[4:7]\n"
    "v_exp_f32_e32 v202, v202\n"
    "v_mfma_f32_16x16x32_bf16 v[4:7], a[168:171], v[168:171], v[4:7]\n"
    "v_exp_f32_e32 v203, v203\n"
    "v_mfma_f32_16x16x32_bf16 v[0:3], a[168:171], v[160:163], v[0:3]\n"
    "s_waitcnt lgkmcnt(0)\n"
    "ds_read_b64_tr_b16 a[168:169], v245 offset:36864\n"
    "v_mad_u32_u16 v253, v254, s42, v251 op_sel:[0,0,0,0]\n"
    "v_mfma_f32_16x16x32_bf16 v[0:3], a[172:175], v[164:167], v[0:3]\n"
    "ds_read_b64_tr_b16 a[170:171], v245 offset:38912\n"
    "v_cvt_pk_bf16_f32 v200, v200, v201\n"
    "v_mfma_f32_16x16x32_bf16 v[8:11], a[176:179], v[160:163], v[8:11]\n"
    "ds_read_b64_tr_b16 a[172:173], v245 offset:45056\n"
    "buffer_load_dwordx4 a[240:243], v253, s[20:23], 0 offen offset:0 sc1\n"
    "v_mfma_f32_16x16x32_bf16 v[8:11], a[180:183], v[164:167], v[8:11]\n"
    "ds_read_b64_tr_b16 a[174:175], v245 offset:47104\n"
    "v_cvt_pk_bf16_f32 v201, v202, v203\n"
    "v_mfma_f32_16x16x32_bf16 v[12:15], a[180:183], v[172:175], v[12:15]\n"
    "v_exp_f32_e32 v236, v236\n"
    "v_mfma_f32_16x16x32_bf16 v[12:15], a[176:179], v[168:171], v[12:15]\n"
    "v_mad_u32_u16 v253, v254, s42, v251 op_sel:[1,0,0,0]\n"
    "v_mfma_f32_16x16x32_bf16 v[80:83], a[176:179], v[176:179], v[80:83]\n"
    "buffer_load_dwordx4 a[244:247], v253, s[20:23], 0 offen offset:0 sc1\n"
    "v_mfma_f32_16x16x32_bf16 v[80:83], a[180:183], v[180:183], v[80:83]\n"
    "v_exp_f32_e32 v237, v237\n"
    "v_mfma_f32_16x16x32_bf16 v[84:87], a[180:183], v[188:191], v[84:87]\n"
    "v_exp_f32_e32 v238, v238\n"
    "v_mfma_f32_16x16x32_bf16 v[84:87], a[176:179], v[184:187], v[84:87]\n"
    "v_exp_f32_e32 v239, v239\n"
    "v_mfma_f32_16x16x32_bf16 v[88:91], a[176:179], v[192:195], v[88:91]\n"
    "ds_read_b64_tr_b16 a[176:177], v246 offset:36864\n"
    "v_cvt_pk_bf16_f32 v234, v236, v237\n"
    "v_mfma_f32_16x16x32_bf16 v[88:91], a[180:183], v[196:199], v[88:91]\n"
    "ds_read_b64_tr_b16 a[178:179], v246 offset:38912\n"
    "v_exp_f32_e32 v228, v228\n"
    "v_mfma_f32_16x16x32_bf16 v[100:103], a[184:187], v[192:195], v[100:103]\n"
    "ds_read_b64_tr_b16 a[180:181], v246 offset:45056\n"
    "v_exp_f32_e32 v229, v229\n"
    "v_mfma_f32_16x16x32_bf16 v[100:103], a[188:191], v[196:199], v[100:103]\n"
    "ds_read_b64_tr_b16 a[182:183], v246 offset:47104\n"
    "v_cvt_pk_bf16_f32 v235, v238, v239\n"
    "v_mfma_f32_16x16x32_bf16 v[96:99], a[188:191], v[188:191], v[96:99]\n"
    "v_exp_f32_e32 v230, v230\n"
    "v_mfma_f32_16x16x32_bf16 v[96:99], a[184:187], v[184:187], v[96:99]\n"
    "v_exp_f32_e32 v231, v231\n"
    "v_mfma_f32_16x16x32_bf16 v[92:95], a[184:187], v[176:179], v[92:95]\n"
    "v_cvt_pk_bf16_f32 v226, v228, v229\n"
    "v_mfma_f32_16x16x32_bf16 v[92:95], a[188:191], v[180:183], v[92:95]\n"
    "v_exp_f32_e32 v220, v220\n"
    "v_mfma_f32_16x16x32_bf16 v[20:23], a[188:191], v[172:175], v[20:23]\n"
    "v_exp_f32_e32 v221, v221\n"
    "v_mfma_f32_16x16x32_bf16 v[20:23], a[184:187], v[168:171], v[20:23]\n"
    "v_cvt_pk_bf16_f32 v227, v230, v231\n"
    "v_mfma_f32_16x16x32_bf16 v[16:19], a[184:187], v[160:163], v[16:19]\n"
    "ds_read_b64_tr_b16 a[184:185], v247 offset:36864\n"
    "v_mfma_f32_16x16x32_bf16 v[16:19], a[188:191], v[164:167], v[16:19]\n"
    "s_waitcnt lgkmcnt(5)\n"
    "s_mov_b32 m0, s49\n"
    "ds_read_b64_tr_b16 a[186:187], v247 offset:38912\n"
    "v_mfma_f32_16x16x32_bf16 v[24:27], a[160:163], v[160:163], v[24:27]\n"
    "ds_read_b64_tr_b16 a[188:189], v247 offset:45056\n"
    "v_mfma_f32_16x16x32_bf16 v[24:27], a[164:167], v[164:167], v[24:27]\n"
    "ds_read_b64_tr_b16 a[190:191], v247 offset:47104\n"
    "v_mfma_f32_16x16x32_bf16 v[28:31], a[164:167], v[172:175], v[28:31]\n"
    "v_exp_f32_e32 v222, v222\n"
    "v_mfma_f32_16x16x32_bf16 v[28:31], a[160:163], v[168:171], v[28:31]\n"
    "v_exp_f32_e32 v223, v223\n"
    "v_mfma_f32_16x16x32_bf16 v[104:107], a[160:163], v[176:179], v[104:107]\n"
    "v_cvt_pk_bf16_f32 v218, v220, v221\n"
    "v_mfma_f32_16x16x32_bf16 v[104:107], a[164:167], v[180:183], v[104:107]\n"
    "v_exp_f32_e32 v212, v212\n"
    "v_mfma_f32_16x16x32_bf16 v[108:111], a[164:167], v[188:191], v[108:111]\n"
    "v_exp_f32_e32 v213, v213\n"
    "v_mfma_f32_16x16x32_bf16 v[108:111], a[160:163], v[184:187], v[108:111]\n"
    "s_waitcnt vmcnt(20)\n"
    "s_barrier\n"
    "v_mfma_f32_16x16x32_bf16 v[112:115], a[160:163], v[192:195], v[112:115]\n"
    "ds_read_b128 a[160:163], v243 offset:4096\n"
    "v_cvt_pk_bf16_f32 v219, v222, v223\n"
    "v_mfma_f32_16x16x32_bf16 v[112:115], a[164:167], v[196:199], v[112:115]\n"
    "ds_read_b128 a[164:167], v240 offset:0\n"
    "ds_write_addtid_b32 v232 offset:4096\n"
    "v_mfma_f32_16x16x32_bf16 v[124:127], a[168:171], v[192:195], v[124:127]\n"
    "v_exp_f32_e32 v214, v214\n"
    "v_mfma_f32_16x16x32_bf16 v[124:127], a[172:175], v[196:199], v[124:127]\n"
    "v_exp_f32_e32 v215, v215\n"
    "v_mfma_f32_16x16x32_bf16 v[120:123], a[172:175], v[188:191], v[120:123]\n"
    "ds_write_addtid_b32 v233 offset:4352\n"
    "v_cvt_pk_bf16_f32 v210, v212, v213\n"
    "v_mfma_f32_16x16x32_bf16 v[120:123], a[168:171], v[184:187], v[120:123]\n"
    "v_exp_f32_e32 v204, v204\n"
    "v_mfma_f32_16x16x32_bf16 v[116:119], a[168:171], v[176:179], v[116:119]\n"
    "v_exp_f32_e32 v205, v205\n"
    "v_mfma_f32_16x16x32_bf16 v[116:119], a[172:175], v[180:183], v[116:119]\n"
    "ds_write_addtid_b32 v224 offset:3072\n"
    "v_cvt_pk_bf16_f32 v211, v214, v215\n"
    "v_mfma_f32_16x16x32_bf16 v[36:39], a[172:175], v[172:175], v[36:39]\n"
    "v_exp_f32_e32 v206, v206\n"
    "v_mfma_f32_16x16x32_bf16 v[36:39], a[168:171], v[168:171], v[36:39]\n"
    "v_exp_f32_e32 v207, v207\n"
    "v_mfma_f32_16x16x32_bf16 v[32:35], a[168:171], v[160:163], v[32:35]\n"
    "ds_read_b128 a[168:171], v241 offset:0\n"
    "v_cvt_pk_bf16_f32 v202, v204, v205\n"
    "v_mfma_f32_16x16x32_bf16 v[32:35], a[172:175], v[164:167], v[32:35]\n"
    "s_waitcnt lgkmcnt(6)\n"
    "ds_read_b128 a[172:175], v240 offset:4096\n"
    "v_cvt_pk_bf16_f32 v203, v206, v207\n"
    "v_mfma_f32_16x16x32_bf16 v[40:43], a[176:179], v[160:163], v[40:43]\n"
    "ds_write_addtid_b32 v225 offset:3328\n"
    "ds_write_addtid_b32 v216 offset:2048\n"
    "v_mfma_f32_16x16x32_bf16 v[40:43], a[180:183], v[164:167], v[40:43]\n"
    "ds_write_addtid_b32 v217 offset:2304\n"
    "v_mfma_f32_16x16x32_bf16 v[44:47], a[180:183], v[172:175], v[44:47]\n"
    "ds_write_addtid_b32 v208 offset:1024\n"
    "v_mfma_f32_16x16x32_bf16 v[44:47], a[176:179], v[168:171], v[44:47]\n"
    "ds_write_addtid_b32 v209 offset:1280\n"
    "v_mfma_f32_16x16x32_bf16 v[128:131], a[176:179], v[176:179], v[128:131]\n"
    "ds_write_addtid_b32 v200 offset:0\n"
    "v_mfma_f32_16x16x32_bf16 v[128:131], a[180:183], v[180:183], v[128:131]\n"
    "ds_write_addtid_b32 v201 offset:256\n"
    "v_mfma_f32_16x16x32_bf16 v[132:135], a[180:183], v[188:191], v[132:135]\n"
    "ds_write_addtid_b32 v234 offset:4608\n"
    "v_mfma_f32_16x16x32_bf16 v[132:135], a[176:179], v[184:187], v[132:135]\n"
    "ds_write_addtid_b32 v235 offset:4864\n"
    "v_mfma_f32_16x16x32_bf16 v[136:139], a[176:179], v[192:195], v[136:139]\n"
    "ds_read_b128 a[176:179], v241 offset:4096\n"
    "v_mfma_f32_16x16x32_bf16 v[136:139], a[180:183], v[196:199], v[136:139]\n"
    "ds_read_b128 a[180:183], v242 offset:0\n"
    "v_mfma_f32_16x16x32_bf16 v[148:151], a[184:187], v[192:195], v[148:151]\n"
    "ds_write_addtid_b32 v226 offset:3584\n"
    "v_mfma_f32_16x16x32_bf16 v[148:151], a[188:191], v[196:199], v[148:151]\n"
    "ds_write_addtid_b32 v227 offset:3840\n"
    "v_mfma_f32_16x16x32_bf16 v[144:147], a[188:191], v[188:191], v[144:147]\n"
    "ds_write_addtid_b32 v218 offset:2560\n"
    "v_mfma_f32_16x16x32_bf16 v[144:147], a[184:187], v[184:187], v[144:147]\n"
    "ds_write_addtid_b32 v219 offset:2816\n"
    "v_mfma_f32_16x16x32_bf16 v[140:143], a[184:187], v[176:179], v[140:143]\n"
    "ds_write_addtid_b32 v210 offset:1536\n"
    "v_mfma_f32_16x16x32_bf16 v[140:143], a[188:191], v[180:183], v[140:143]\n"
    "ds_write_addtid_b32 v211 offset:1792\n"
    "v_mfma_f32_16x16x32_bf16 v[52:55], a[188:191], v[172:175], v[52:55]\n"
    "ds_write_addtid_b32 v202 offset:512\n"
    "v_mfma_f32_16x16x32_bf16 v[52:55], a[184:187], v[168:171], v[52:55]\n"
    "ds_write_addtid_b32 v203 offset:768\n"
    "v_mfma_f32_16x16x32_bf16 v[48:51], a[184:187], v[160:163], v[48:51]\n"
    "ds_read_b128 a[184:187], v243 offset:0\n"
    "v_mfma_f32_16x16x32_bf16 v[48:51], a[188:191], v[164:167], v[48:51]\n"
    "s_waitcnt lgkmcnt(15)\n"
    "ds_read_b128 a[188:191], v242 offset:4096\n"
    "v_mfma_f32_16x16x32_bf16 v[160:163], a[160:163], a[28:31], 0\n"
    "v_mfma_f32_16x16x32_bf16 v[160:163], a[164:167], a[0:3], v[160:163]\n"
    "v_mfma_f32_16x16x32_bf16 v[168:171], a[164:167], a[32:35], 0\n"
    "v_mfma_f32_16x16x32_bf16 v[168:171], a[160:163], a[60:63], v[168:171]\n"
    "v_mfma_f32_16x16x32_bf16 v[176:179], a[160:163], a[92:95], 0\n"
    "v_mfma_f32_16x16x32_bf16 v[176:179], a[164:167], a[64:67], v[176:179]\n"
    "v_mfma_f32_16x16x32_bf16 v[184:187], a[164:167], a[96:99], 0\n"
    "s_add_u32 m0, s43, 32768\n"
    "v_mfma_f32_16x16x32_bf16 v[184:187], a[160:163], a[124:127], v[184:187]\n"
    "s_waitcnt lgkmcnt(0)\n"
    "s_barrier\n"
    "v_mfma_f32_16x16x32_bf16 v[192:195], a[160:163], a[156:159], 0\n"
    "ds_read_b128 a[160:163], v243 offset:6144\n"
    "v_mfma_f32_16x16x32_bf16 v[192:195], a[164:167], a[128:131], v[192:195]\n"
    "ds_read_b128 a[164:167], v240 offset:2048\n"
    "v_mfma_f32_16x16x32_bf16 v[192:195], a[168:171], a[132:135], v[192:195]\n"
    "buffer_load_dwordx4 v248, s[8:11], s40 offen offset:0 sc1 lds\n"
    "v_mfma_f32_16x16x32_bf16 v[192:195], a[172:175], a[136:139], v[192:195]\n"
    "ds_read2st64_b32 v[204:205], v250 offset0:0 offset1:1\n"
    "v_mfma_f32_16x16x32_bf16 v[184:187], a[172:175], a[104:107], v[184:187]\n"
    "s_waitcnt vmcnt(11)\n"
    "v_smfmac_f32_16x16x64_bf16 v[156:159], a[192:195], a[200:207], v152\n"
    "v_mfma_f32_16x16x32_bf16 v[184:187], a[168:171], a[100:103], v[184:187]\n"
    "buffer_load_dwordx4 v248, s[8:11], s40 offen offset:1024 sc1 lds\n"
    "v_mfma_f32_16x16x32_bf16 v[176:179], a[168:171], a[68:71], v[176:179]\n"
    "ds_read2st64_b32 v[206:207], v250 offset0:2 offset1:3\n"
    "v_mfma_f32_16x16x32_bf16 v[176:179], a[172:175], a[72:75], v[176:179]\n"
    "v_mfma_f32_16x16x32_bf16 v[168:171], a[172:175], a[40:43], v[168:171]\n"
    "s_waitcnt vmcnt(10)\n"
    "v_smfmac_f32_16x16x64_bf16 v[156:159], a[192:195], a[208:215], v152\n"
    "buffer_load_dwordx4 v248, s[8:11], s40 offen offset:2048 sc1 lds\n"
    "v_mfma_f32_16x16x32_bf16 v[168:171], a[168:171], a[36:39], v[168:171]\n"
    "ds_read2st64_b32 v[212:213], v250 offset0:4 offset1:5\n"
    "v_mfma_f32_16x16x32_bf16 v[160:163], a[168:171], a[4:7], v[160:163]\n"
    "ds_read_b128 a[168:171], v241 offset:2048\n"
    "v_mfma_f32_16x16x32_bf16 v[160:163], a[172:175], a[8:11], v[160:163]\n"
    "ds_read_b128 a[172:175], v240 offset:6144\n"
    "v_mfma_f32_16x16x32_bf16 v[160:163], a[176:179], a[12:15], v[160:163]\n"
    "s_waitcnt vmcnt(9)\n"
    "buffer_load_dwordx4 v248, s[8:11], s40 offen offset:3072 sc1 lds\n"
    "v_smfmac_f32_16x16x64_bf16 v[156:159], a[192:195], a[216:223], v152\n"
    "s_add_u32 m0, s43, 36864\n"
    "ds_read2st64_b32 v[214:215], v250 offset0:6 offset1:7\n"
    "v_mfma_f32_16x16x32_bf16 v[160:163], a[180:183], a[16:19], v[160:163]\n"
    "s_add_u32 s40, s40, 0x8000\n"
    "v_mfma_f32_16x16x32_bf16 v[168:171], a[180:183], a[48:51], v[168:171]\n"
    "v_mfma_f32_16x16x32_bf16 v[168:171], a[176:179], a[44:47], v[168:171]\n"
    "buffer_load_dwordx4 v248, s[8:11], s41 offen offset:0 sc1 lds\n"
    "v_mfma_f32_16x16x32_bf16 v[176:179], a[176:179], a[76:79], v[176:179]\n"
    "ds_read2st64_b32 v[220:221], v250 offset0:8 offset1:9\n"
    "v_mfma_f32_16x16x32_bf16 v[176:179], a[180:183], a[80:83], v[176:179]\n"
    "v_mfma_f32_16x16x32_bf16 v[184:187], a[180:183], a[112:115], v[184:187]\n"
    "v_mfma_f32_16x16x32_bf16 v[184:187], a[176:179], a[108:111], v[184:187]\n"
    "buffer_load_dwordx4 v248, s[8:11], s41 offen offset:1024 sc1 lds\n"
    "v_mfma_f32_16x16x32_bf16 v[192:195], a[176:179], a[140:143], v[192:195]\n"
    "ds_read_b128 a[176:179], v241 offset:6144\n"
    "v_mfma_f32_16x16x32_bf16 v[192:195], a[180:183], a[144:147], v[192:195]\n"
    "ds_read_b128 a[180:183], v242 offset:2048\n"
    "v_mfma_f32_16x16x32_bf16 v[192:195], a[184:187], a[148:151], v[192:195]\n"
    "ds_read2st64_b32 v[222:223], v250 offset0:10 offset1:11\n"
    "v_mfma_f32_16x16x32_bf16 v[192:195], a[188:191], a[152:155], v[192:195]\n"
    "buffer_load_dwordx4 v248, s[8:11], s41 offen offset:2048 sc1 lds\n"
    "v_mfma_f32_16x16x32_bf16 v[184:187], a[188:191], a[120:123], v[184:187]\n"
    "v_mfma_f32_16x16x32_bf16 v[184:187], a[184:187], a[116:119], v[184:187]\n"
    "ds_read2st64_b32 v[228:229], v250 offset0:12 offset1:13\n"
    "v_mfma_f32_16x16x32_bf16 v[176:179], a[184:187], a[84:87], v[176:179]\n"
    "s_waitcnt vmcnt(11)\n"
    "v_exp_f32_e32 v192, v192\n"
    "v_smfmac_f32_16x16x64_bf16 v[156:159], a[192:195], a[224:231], v152\n"
    "buffer_load_dwordx4 v248, s[8:11], s41 offen offset:3072 sc1 lds\n"
    "v_mfma_f32_16x16x32_bf16 v[176:179], a[188:191], a[88:91], v[176:179]\n"
    "s_add_u32 s41, s41, 0x8000\n"
    "v_exp_f32_e32 v193, v193\n"
    "v_mfma_f32_16x16x32_bf16 v[168:171], a[188:191], a[56:59], v[168:171]\n"
    "ds_read2st64_b32 v[230:231], v250 offset0:14 offset1:15\n"
    "v_mfma_f32_16x16x32_bf16 v[168:171], a[184:187], a[52:55], v[168:171]\n"
    "v_exp_f32_e32 v194, v194\n"
    "v_mfma_f32_16x16x32_bf16 v[160:163], a[184:187], a[20:23], v[160:163]\n"
    "s_waitcnt vmcnt(10)\n"
    "ds_read_b128 a[184:187], v243 offset:2048\n"
    "v_smfmac_f32_16x16x64_bf16 v[156:159], a[192:195], a[232:239], v152\n"
    "v_exp_f32_e32 v195, v195\n"
    "v_mfma_f32_16x16x32_bf16 v[160:163], a[188:191], a[24:27], v[160:163]\n"
    "s_waitcnt lgkmcnt(8)\n"
    "ds_read_b128 a[188:191], v242 offset:6144\n"
    "v_mfma_f32_16x16x32_bf16 v[164:167], a[160:163], a[28:31], 0\n"
    "ds_read2st64_b32 v[236:237], v250 offset0:16 offset1:17\n"
    "v_mfma_f32_16x16x32_bf16 v[164:167], a[164:167], a[0:3], v[164:167]\n"
    "v_cvt_pk_bf16_f32 v192, v192, v193\n"
    "v_mfma_f32_16x16x32_bf16 v[172:175], a[164:167], a[32:35], 0\n"
    "s_waitcnt vmcnt(8)\n"
    "v_exp_f32_e32 v184, v184\n"
    "v_smfmac_f32_16x16x64_bf16 v[156:159], a[192:195], a[240:247], v152\n"
    "v_exp_f32_e32 v185, v185\n"
    "v_mfma_f32_16x16x32_bf16 v[172:175], a[160:163], a[60:63], v[172:175]\n"
    "ds_read2st64_b32 v[238:239], v250 offset0:18 offset1:19\n"
    "v_mfma_f32_16x16x32_bf16 v[180:183], a[160:163], a[92:95], 0\n"
    "v_mfma_f32_16x16x32_bf16 v[180:183], a[164:167], a[64:67], v[180:183]\n"
    "v_cvt_pk_bf16_f32 v193, v194, v195\n"
    "v_mfma_f32_16x16x32_bf16 v[188:191], a[164:167], a[96:99], 0\n"
    "v_exp_f32_e32 v186, v186\n"
    "v_mfma_f32_16x16x32_bf16 v[188:191], a[160:163], a[124:127], v[188:191]\n"
    "v_exp_f32_e32 v187, v187\n"
    "v_mfma_f32_16x16x32_bf16 v[196:199], a[160:163], a[156:159], 0\n"
    "ds_read_b64_tr_b16 a[160:161], v244 offset:49152\n"
    "v_and_b32_e32 v253, 0xffff0000, v255\n"
    "v_mfma_f32_16x16x32_bf16 v[196:199], a[164:167], a[128:131], v[196:199]\n"
    "ds_read_b64_tr_b16 a[162:163], v244 offset:51200\n"
    "v_mfma_f32_16x16x32_bf16 v[196:199], a[168:171], a[132:135], v[196:199]\n"
    "ds_read_b64_tr_b16 a[164:165], v244 offset:57344\n"
    "v_mfma_f32_16x16x32_bf16 v[196:199], a[172:175], a[136:139], v[196:199]\n"
    "ds_read_b64_tr_b16 a[166:167], v244 offset:59392\n"
    "v_mfma_f32_16x16x32_bf16 v[188:191], a[172:175], a[104:107], v[188:191]\n"
    "v_cvt_pk_bf16_f32 v184, v184, v185\n"
    "v_mfma_f32_16x16x32_bf16 v[188:191], a[168:171], a[100:103], v[188:191]\n"
    "v_exp_f32_e32 v176, v176\n"
    "v_mfma_f32_16x16x32_bf16 v[180:183], a[168:171], a[68:71], v[180:183]\n"
    "v_exp_f32_e32 v177, v177\n"
    "v_mfma_f32_16x16x32_bf16 v[180:183], a[172:175], a[72:75], v[180:183]\n"
    "v_cvt_pk_bf16_f32 v185, v186, v187\n"
    "v_mul_f32_e32 v156, v253, v156\n"
    "v_mfma_f32_16x16x32_bf16 v[172:175], a[172:175], a[40:43], v[172:175]\n"
    "v_exp_f32_e32 v178, v178\n"
    "v_mfma_f32_16x16x32_bf16 v[172:175], a[168:171], a[36:39], v[172:175]\n"
    "v_exp_f32_e32 v179, v179\n"
    "v_mfma_f32_16x16x32_bf16 v[164:167], a[168:171], a[4:7], v[164:167]\n"
    "ds_read_b64_tr_b16 a[168:169], v245 offset:49152\n"
    "v_mfma_f32_16x16x32_bf16 v[164:167], a[172:175], a[8:11], v[164:167]\n"
    "s_waitcnt lgkmcnt(7)\n"
    "ds_read_b64_tr_b16 a[170:171], v245 offset:51200\n"
    "v_mfma_f32_16x16x32_bf16 v[164:167], a[176:179], a[12:15], v[164:167]\n"
    "ds_read_b64_tr_b16 a[172:173], v245 offset:57344\n"
    "v_mul_f32_e32 v157, v253, v157\n"
    "v_mfma_f32_16x16x32_bf16 v[164:167], a[180:183], a[16:19], v[164:167]\n"
    "ds_read_b64_tr_b16 a[174:175], v245 offset:59392\n"
    "v_mfma_f32_16x16x32_bf16 v[172:175], a[180:183], a[48:51], v[172:175]\n"
    "v_cvt_pk_bf16_f32 v176, v176, v177\n"
    "v_mfma_f32_16x16x32_bf16 v[172:175], a[176:179], a[44:47], v[172:175]\n"
    "v_exp_f32_e32 v168, v168\n"
    "v_mfma_f32_16x16x32_bf16 v[180:183], a[176:179], a[76:79], v[180:183]\n"
    "v_mul_f32_e32 v158, v253, v158\n"
    "v_mfma_f32_16x16x32_bf16 v[180:183], a[180:183], a[80:83], v[180:183]\n"
    "v_exp_f32_e32 v169, v169\n"
    "v_mfma_f32_16x16x32_bf16 v[188:191], a[180:183], a[112:115], v[188:191]\n"
    "v_cvt_pk_bf16_f32 v177, v178, v179\n"
    "v_mfma_f32_16x16x32_bf16 v[188:191], a[176:179], a[108:111], v[188:191]\n"
    "v_exp_f32_e32 v170, v170\n"
    "v_mfma_f32_16x16x32_bf16 v[196:199], a[176:179], a[140:143], v[196:199]\n"
    "ds_read_b64_tr_b16 a[176:177], v246 offset:49152\n"
    "v_mfma_f32_16x16x32_bf16 v[196:199], a[180:183], a[144:147], v[196:199]\n"
    "ds_read_b64_tr_b16 a[178:179], v246 offset:51200\n"
    "v_mfma_f32_16x16x32_bf16 v[196:199], a[184:187], a[148:151], v[196:199]\n"
    "ds_read_b64_tr_b16 a[180:181], v246 offset:57344\n"
    "v_mfma_f32_16x16x32_bf16 v[196:199], a[188:191], a[152:155], v[196:199]\n"
    "ds_read_b64_tr_b16 a[182:183], v246 offset:59392\n"
    "v_mul_f32_e32 v159, v253, v159\n"
    "v_mfma_f32_16x16x32_bf16 v[188:191], a[188:191], a[120:123], v[188:191]\n"
    "v_exp_f32_e32 v171, v171\n"
    "v_mfma_f32_16x16x32_bf16 v[188:191], a[184:187], a[116:119], v[188:191]\n"
    "v_cvt_pk_bf16_f32 v168, v168, v169\n"
    "v_mfma_f32_16x16x32_bf16 v[180:183], a[184:187], a[84:87], v[180:183]\n"
    "v_exp_f32_e32 v160, v160\n"
    "v_mfma_f32_16x16x32_bf16 v[180:183], a[188:191], a[88:91], v[180:183]\n"
    "v_exp_f32_e32 v161, v161\n"
    "v_mfma_f32_16x16x32_bf16 v[172:175], a[188:191], a[56:59], v[172:175]\n"
    "v_cvt_pk_bf16_f32 v169, v170, v171\n"
    "v_mfma_f32_16x16x32_bf16 v[172:175], a[184:187], a[52:55], v[172:175]\n"
    "v_exp_f32_e32 v162, v162\n"
    "v_mfma_f32_16x16x32_bf16 v[164:167], a[184:187], a[20:23], v[164:167]\n"
    "ds_read_b64_tr_b16 a[184:185], v247 offset:49152\n"
    "v_mad_u32_u16 v253, v255, s42, v153 op_sel:[0,0,0,0]\n"
    "v_mfma_f32_16x16x32_bf16 v[164:167], a[188:191], a[24:27], v[164:167]\n"
    "s_waitcnt lgkmcnt(5)\n"
    "ds_read_b64_tr_b16 a[186:187], v247 offset:51200\n"
    "v_mfma_f32_16x16x32_bf16 a[248:251], a[160:163], v[200:203], a[248:251]\n"
    "ds_read_b64_tr_b16 a[188:189], v247 offset:57344\n"
    "v_mfma_f32_16x16x32_bf16 a[248:251], a[164:167], v[204:207], a[248:251]\n"
    "ds_read_b64_tr_b16 a[190:191], v247 offset:59392\n"
    "v_mfma_f32_16x16x32_bf16 a[252:255], a[164:167], v[212:215], a[252:255]\n"
    "v_exp_f32_e32 v163, v163\n"
    "v_mfma_f32_16x16x32_bf16 a[252:255], a[160:163], v[208:211], a[252:255]\n"
    "v_cvt_pk_bf16_f32 v160, v160, v161\n"
    "v_mfma_f32_16x16x32_bf16 v[56:59], a[160:163], v[216:219], v[56:59]\n"
    "v_cvt_pk_bf16_f32 v154, v156, v157\n"
    "v_mfma_f32_16x16x32_bf16 v[56:59], a[164:167], v[220:223], v[56:59]\n"
    "v_cvt_pk_bf16_f32 v161, v162, v163\n"
    "v_mfma_f32_16x16x32_bf16 v[60:63], a[164:167], v[228:231], v[60:63]\n"
    "v_exp_f32_e32 v196, v196\n"
    "v_mfma_f32_16x16x32_bf16 v[60:63], a[160:163], v[224:227], v[60:63]\n"
    "v_exp_f32_e32 v197, v197\n"
    "v_mfma_f32_16x16x32_bf16 v[64:67], a[160:163], v[232:235], v[64:67]\n"
    "ds_read_b64_tr_b16 a[160:161], v244 offset:53248\n"
    "v_mfma_f32_16x16x32_bf16 v[64:67], a[164:167], v[236:239], v[64:67]\n"
    "ds_read_b64_tr_b16 a[162:163], v244 offset:55296\n"
    "v_cvt_pk_bf16_f32 v155, v158, v159\n"
    "v_mfma_f32_16x16x32_bf16 v[76:79], a[168:171], v[232:235], v[76:79]\n"
    "ds_read_b64_tr_b16 a[164:165], v244 offset:61440\n"
    "v_mfma_f32_16x16x32_bf16 v[76:79], a[172:175], v[236:239], v[76:79]\n"
    "ds_read_b64_tr_b16 a[166:167], v244 offset:63488\n"
    "v_mfma_f32_16x16x32_bf16 v[72:75], a[172:175], v[228:231], v[72:75]\n"
    "v_exp_f32_e32 v198, v198\n"
    "v_mfma_f32_16x16x32_bf16 v[72:75], a[168:171], v[224:227], v[72:75]\n"
    "v_exp_f32_e32 v199, v199\n"
    "v_mfma_f32_16x16x32_bf16 v[68:71], a[168:171], v[216:219], v[68:71]\n"
    "v_cvt_pk_bf16_f32 v194, v196, v197\n"
    "v_mov_b32_e32 v156, 0\n"
    "v_mfma_f32_16x16x32_bf16 v[68:71], a[172:175], v[220:223], v[68:71]\n"
    "v_exp_f32_e32 v188, v188\n"
    "v_mfma_f32_16x16x32_bf16 v[4:7], a[172:175], v[212:215], v[4:7]\n"
    "v_exp_f32_e32 v189, v189\n"
    "v_mfma_f32_16x16x32_bf16 v[4:7], a[168:171], v[208:211], v[4:7]\n"
    "v_cvt_pk_bf16_f32 v195, v198, v199\n"
    "v_mfma_f32_16x16x32_bf16 v[0:3], a[168:171], v[200:203], v[0:3]\n"
    "ds_read_b64_tr_b16 a[168:169], v245 offset:53248\n"
    "v_mfma_f32_16x16x32_bf16 v[0:3], a[172:175], v[204:207], v[0:3]\n"
    "s_waitcnt lgkmcnt(5)\n"
    "ds_read_b64_tr_b16 a[170:171], v245 offset:55296\n"
    "v_mfma_f32_16x16x32_bf16 v[8:11], a[176:179], v[200:203], v[8:11]\n"
    "ds_read_b64_tr_b16 a[172:173], v245 offset:61440\n"
    "v_mfma_f32_16x16x32_bf16 v[8:11], a[180:183], v[204:207], v[8:11]\n"
    "ds_read_b64_tr_b16 a[174:175], v245 offset:63488\n"
    "v_mov_b32_e32 v157, 0\n"
    "v_mfma_f32_16x16x32_bf16 v[12:15], a[180:183], v[212:215], v[12:15]\n"
    "v_exp_f32_e32 v190, v190\n"
    "v_mfma_f32_16x16x32_bf16 v[12:15], a[176:179], v[208:211], v[12:15]\n"
    "v_exp_f32_e32 v191, v191\n"
    "v_mfma_f32_16x16x32_bf16 v[80:83], a[176:179], v[216:219], v[80:83]\n"
    "v_mov_b32_e32 v158, 0\n"
    "v_mfma_f32_16x16x32_bf16 v[80:83], a[180:183], v[220:223], v[80:83]\n"
    "v_cvt_pk_bf16_f32 v186, v188, v189\n"
    "v_mfma_f32_16x16x32_bf16 v[84:87], a[180:183], v[228:231], v[84:87]\n"
    "v_exp_f32_e32 v180, v180\n"
    "v_mfma_f32_16x16x32_bf16 v[84:87], a[176:179], v[224:227], v[84:87]\n"
    "v_exp_f32_e32 v181, v181\n"
    "v_mfma_f32_16x16x32_bf16 v[88:91], a[176:179], v[232:235], v[88:91]\n"
    "ds_read_b64_tr_b16 a[176:177], v246 offset:53248\n"
    "v_mfma_f32_16x16x32_bf16 v[88:91], a[180:183], v[236:239], v[88:91]\n"
    "ds_read_b64_tr_b16 a[178:179], v246 offset:55296\n"
    "v_mfma_f32_16x16x32_bf16 v[100:103], a[184:187], v[232:235], v[100:103]\n"
    "ds_read_b64_tr_b16 a[180:181], v246 offset:61440\n"
    "v_mfma_f32_16x16x32_bf16 v[100:103], a[188:191], v[236:239], v[100:103]\n"
    "ds_read_b64_tr_b16 a[182:183], v246 offset:63488\n"
    "v_mfma_f32_16x16x32_bf16 v[96:99], a[188:191], v[228:231], v[96:99]\n"
    "v_cvt_pk_bf16_f32 v187, v190, v191\n"
    "v_mov_b32_e32 v159, 0\n"
    "v_mfma_f32_16x16x32_bf16 v[96:99], a[184:187], v[224:227], v[96:99]\n"
    "v_exp_f32_e32 v182, v182\n"
    "v_mfma_f32_16x16x32_bf16 v[92:95], a[184:187], v[216:219], v[92:95]\n"
    "v_exp_f32_e32 v183, v183\n"
    "v_mfma_f32_16x16x32_bf16 v[92:95], a[188:191], v[220:223], v[92:95]\n"
    "v_cvt_pk_bf16_f32 v178, v180, v181\n"
    "v_mfma_f32_16x16x32_bf16 v[20:23], a[188:191], v[212:215], v[20:23]\n"
    "v_exp_f32_e32 v172, v172\n"
    "v_mfma_f32_16x16x32_bf16 v[20:23], a[184:187], v[208:211], v[20:23]\n"
    "v_exp_f32_e32 v173, v173\n"
    "v_mfma_f32_16x16x32_bf16 v[16:19], a[184:187], v[200:203], v[16:19]\n"
    "ds_read_b64_tr_b16 a[184:185], v247 offset:53248\n"
    "v_mfma_f32_16x16x32_bf16 v[16:19], a[188:191], v[204:207], v[16:19]\n"
    "s_waitcnt lgkmcnt(5)\n"
    "s_mov_b32 m0, s49\n"
    "ds_read_b64_tr_b16 a[186:187], v247 offset:55296\n"
    "v_mfma_f32_16x16x32_bf16 v[24:27], a[160:163], v[200:203], v[24:27]\n"
    "ds_read_b64_tr_b16 a[188:189], v247 offset:61440\n"
    "v_add_u32_e32 v252, 0x80, v252\n"
    "v_mfma_f32_16x16x32_bf16 v[24:27], a[164:167], v[204:207], v[24:27]\n"
    "ds_read_b64_tr_b16 a[190:191], v247 offset:63488\n"
    "v_mfma_f32_16x16x32_bf16 v[28:31], a[164:167], v[212:215], v[28:31]\n"
    "buffer_store_dwordx2 v[154:155], v253, s[24:27], 0 offen offset:0 sc1\n"
    "v_cvt_pk_bf16_f32 v179, v182, v183\n"
    "v_mfma_f32_16x16x32_bf16 v[28:31], a[160:163], v[208:211], v[28:31]\n"
    "v_exp_f32_e32 v174, v174\n"
    "v_mfma_f32_16x16x32_bf16 v[104:107], a[160:163], v[216:219], v[104:107]\n"
    "v_mfma_f32_16x16x32_bf16 v[104:107], a[164:167], v[220:223], v[104:107]\n"
    "v_exp_f32_e32 v175, v175\n"
    "v_mfma_f32_16x16x32_bf16 v[108:111], a[164:167], v[228:231], v[108:111]\n"
    "v_cvt_pk_bf16_f32 v170, v172, v173\n"
    "v_mfma_f32_16x16x32_bf16 v[108:111], a[160:163], v[224:227], v[108:111]\n"
    "s_waitcnt vmcnt(9)\n"
    "s_barrier\n"
    "v_mfma_f32_16x16x32_bf16 v[112:115], a[160:163], v[232:235], v[112:115]\n"
    "ds_read_b128 a[160:163], v243 offset:20480\n"
    "v_mfma_f32_16x16x32_bf16 v[112:115], a[164:167], v[236:239], v[112:115]\n"
    "ds_read_b128 a[164:167], v240 offset:16384\n"
    "v_mfma_f32_16x16x32_bf16 v[124:127], a[168:171], v[232:235], v[124:127]\n"
    "v_exp_f32_e32 v164, v164\n"
    "v_mfma_f32_16x16x32_bf16 v[124:127], a[172:175], v[236:239], v[124:127]\n"
    "v_exp_f32_e32 v165, v165\n"
    "v_mfma_f32_16x16x32_bf16 v[120:123], a[172:175], v[228:231], v[120:123]\n"
    "ds_write_addtid_b32 v192 offset:4096\n"
    "v_cvt_pk_bf16_f32 v171, v174, v175\n"
    "v_mfma_f32_16x16x32_bf16 v[120:123], a[168:171], v[224:227], v[120:123]\n"
    "ds_write_addtid_b32 v193 offset:4352\n"
    "v_mfma_f32_16x16x32_bf16 v[116:119], a[168:171], v[216:219], v[116:119]\n"
    "v_exp_f32_e32 v166, v166\n"
    "v_mfma_f32_16x16x32_bf16 v[116:119], a[172:175], v[220:223], v[116:119]\n"
    "v_exp_f32_e32 v167, v167\n"
    "v_mfma_f32_16x16x32_bf16 v[36:39], a[172:175], v[212:215], v[36:39]\n"
    "ds_write_addtid_b32 v184 offset:3072\n"
    "v_cvt_pk_bf16_f32 v162, v164, v165\n"
    "v_mfma_f32_16x16x32_bf16 v[36:39], a[168:171], v[208:211], v[36:39]\n"
    "ds_write_addtid_b32 v185 offset:3328\n"
    "v_cvt_pk_bf16_f32 v163, v166, v167\n"
    "v_mfma_f32_16x16x32_bf16 v[32:35], a[168:171], v[200:203], v[32:35]\n"
    "ds_read_b128 a[168:171], v241 offset:16384\n"
    "v_mfma_f32_16x16x32_bf16 v[32:35], a[172:175], v[204:207], v[32:35]\n"
    "s_waitcnt lgkmcnt(7)\n"
    "ds_read_b128 a[172:175], v240 offset:20480\n"
    "v_mfma_f32_16x16x32_bf16 v[40:43], a[176:179], v[200:203], v[40:43]\n"
    "ds_write_addtid_b32 v176 offset:2048\n"
    "v_mfma_f32_16x16x32_bf16 v[40:43], a[180:183], v[204:207], v[40:43]\n"
    "ds_write_addtid_b32 v177 offset:2304\n"
    "v_mfma_f32_16x16x32_bf16 v[44:47], a[180:183], v[212:215], v[44:47]\n"
    "ds_write_addtid_b32 v168 offset:1024\n"
    "v_mfma_f32_16x16x32_bf16 v[44:47], a[176:179], v[208:211], v[44:47]\n"
    "ds_write_addtid_b32 v169 offset:1280\n"
    "v_mfma_f32_16x16x32_bf16 v[128:131], a[176:179], v[216:219], v[128:131]\n"
    "ds_write_addtid_b32 v160 offset:0\n"
    "v_mfma_f32_16x16x32_bf16 v[128:131], a[180:183], v[220:223], v[128:131]\n"
    "ds_write_addtid_b32 v161 offset:256\n"
    "v_mfma_f32_16x16x32_bf16 v[132:135], a[180:183], v[228:231], v[132:135]\n"
    "ds_write_addtid_b32 v194 offset:4608\n"
    "v_mfma_f32_16x16x32_bf16 v[132:135], a[176:179], v[224:227], v[132:135]\n"
    "ds_write_addtid_b32 v195 offset:4864\n"
    "v_mfma_f32_16x16x32_bf16 v[136:139], a[176:179], v[232:235], v[136:139]\n"
    "ds_read_b128 a[176:179], v241 offset:20480\n"
    "v_mfma_f32_16x16x32_bf16 v[136:139], a[180:183], v[236:239], v[136:139]\n"
    "ds_read_b128 a[180:183], v242 offset:16384\n"
    "v_mfma_f32_16x16x32_bf16 v[148:151], a[184:187], v[232:235], v[148:151]\n"
    "ds_write_addtid_b32 v186 offset:3584\n"
    "v_mfma_f32_16x16x32_bf16 v[148:151], a[188:191], v[236:239], v[148:151]\n"
    "ds_write_addtid_b32 v187 offset:3840\n"
    "v_mfma_f32_16x16x32_bf16 v[144:147], a[188:191], v[228:231], v[144:147]\n"
    "ds_write_addtid_b32 v178 offset:2560\n"
    "v_mfma_f32_16x16x32_bf16 v[144:147], a[184:187], v[224:227], v[144:147]\n"
    "ds_write_addtid_b32 v179 offset:2816\n"
    "v_mfma_f32_16x16x32_bf16 v[140:143], a[184:187], v[216:219], v[140:143]\n"
    "s_sub_u32 s44, s44, 1\n"
    "ds_write_addtid_b32 v170 offset:1536\n"
    "v_mfma_f32_16x16x32_bf16 v[140:143], a[188:191], v[220:223], v[140:143]\n"
    "ds_write_addtid_b32 v171 offset:1792\n"
    "v_mfma_f32_16x16x32_bf16 v[52:55], a[188:191], v[212:215], v[52:55]\n"
    "ds_write_addtid_b32 v162 offset:512\n"
    "v_mfma_f32_16x16x32_bf16 v[52:55], a[184:187], v[208:211], v[52:55]\n"
    "ds_write_addtid_b32 v163 offset:768\n"
    "v_mfma_f32_16x16x32_bf16 v[48:51], a[184:187], v[200:203], v[48:51]\n"
    "ds_read_b128 a[184:187], v243 offset:16384\n"
    "v_mfma_f32_16x16x32_bf16 v[48:51], a[188:191], v[204:207], v[48:51]\n"
    "s_waitcnt lgkmcnt(15)\n"
    "ds_read_b128 a[188:191], v242 offset:20480\n"
    "s_cbranch_scc0 .Lat_g\n"
    "s_mov_b32 s44, 1\n"
    "s_cmp_eq_u32 s34, 0\n"
    "s_cbranch_scc1 .Lat_p\n"
    ".Lat_tail:\n"
    "v_mfma_f32_16x16x32_bf16 v[200:203], a[160:163], a[28:31], 0\n"
    "v_mfma_f32_16x16x32_bf16 v[200:203], a[164:167], a[0:3], v[200:203]\n"
    "v_mbcnt_lo_u32_b32 v253, -1, 0\n"
    "v_mfma_f32_16x16x32_bf16 v[208:211], a[164:167], a[32:35], 0\n"
    "v_mfma_f32_16x16x32_bf16 v[208:211], a[160:163], a[60:63], v[208:211]\n"
    "v_mfma_f32_16x16x32_bf16 v[216:219], a[160:163], a[92:95], 0\n"
    "s_mov_b32 s45, 5693440\n"
    "v_mbcnt_hi_u32_b32 v253, -1, v253\n"
    "v_mfma_f32_16x16x32_bf16 v[216:219], a[164:167], a[64:67], v[216:219]\n"
    "v_mfma_f32_16x16x32_bf16 v[224:227], a[164:167], a[96:99], 0\n"
    "s_add_u32 m0, s43, 49152\n"
    "v_and_b32_e32 v254, 3, v253\n"
    "v_mfma_f32_16x16x32_bf16 v[224:227], a[160:163], a[124:127], v[224:227]\n"
    "s_waitcnt lgkmcnt(0)\n"
    "s_barrier\n"
    "v_mfma_f32_16x16x32_bf16 v[232:235], a[160:163], a[156:159], 0\n"
    "ds_read_b128 a[160:163], v243 offset:22528\n"
    "v_mfma_f32_16x16x32_bf16 v[232:235], a[164:167], a[128:131], v[232:235]\n"
    "ds_read_b128 a[164:167], v240 offset:18432\n"
    "v_mfma_f32_16x16x32_bf16 v[232:235], a[168:171], a[132:135], v[232:235]\n"
    "buffer_load_dwordx4 v248, s[8:11], s40 offen offset:0 sc1 lds\n"
    "v_mfma_f32_16x16x32_bf16 v[232:235], a[172:175], a[136:139], v[232:235]\n"
    "s_lshl_b32 s56, %2, 15\n"
    "v_lshlrev_b32_e32 v254, 2, v254\n"
    "v_mfma_f32_16x16x32_bf16 v[224:227], a[172:175], a[104:107], v[224:227]\n"
    "v_bfe_u32 v252, v253, 4, 2\n"
    "v_mfma_f32_16x16x32_bf16 v[224:227], a[168:171], a[100:103], v[224:227]\n"
    "v_lshlrev_b32_e32 v252, 1, v252\n"
    "v_mfma_f32_16x16x32_bf16 v[216:219], a[168:171], a[68:71], v[216:219]\n"
    "buffer_load_dwordx4 v248, s[8:11], s40 offen offset:1024 sc1 lds\n"
    "v_mfma_f32_16x16x32_bf16 v[216:219], a[172:175], a[72:75], v[216:219]\n"
    "ds_read2st64_b32 v[164:165], v250 offset0:0 offset1:1\n"
    "v_mfma_f32_16x16x32_bf16 v[208:211], a[172:175], a[40:43], v[208:211]\n"
    "v_xor_b32_e32 v252, v252, v253\n"
    "v_mfma_f32_16x16x32_bf16 v[208:211], a[168:171], a[36:39], v[208:211]\n"
    "ds_read2st64_b32 v[166:167], v250 offset0:2 offset1:3\n"
    "v_mfma_f32_16x16x32_bf16 v[200:203], a[168:171], a[4:7], v[200:203]\n"
    "ds_read_b128 a[168:171], v241 offset:18432\n"
    "buffer_load_dwordx4 v248, s[8:11], s40 offen offset:2048 sc1 lds\n"
    "v_mfma_f32_16x16x32_bf16 v[200:203], a[172:175], a[8:11], v[200:203]\n"
    "ds_read_b128 a[172:175], v240 offset:22528\n"
    "v_mfma_f32_16x16x32_bf16 v[200:203], a[176:179], a[12:15], v[200:203]\n"
    "s_add_u32 s57, s56, 5505024\n"
    "v_and_b32_e32 v252, 7, v252\n"
    "v_mfma_f32_16x16x32_bf16 v[200:203], a[180:183], a[16:19], v[200:203]\n"
    "v_lshlrev_b32_e32 v252, 4, v252\n"
    "v_mfma_f32_16x16x32_bf16 v[208:211], a[180:183], a[48:51], v[208:211]\n"
    "buffer_load_dwordx4 v248, s[8:11], s40 offen offset:3072 sc1 lds\n"
    "v_mfma_f32_16x16x32_bf16 v[208:211], a[176:179], a[44:47], v[208:211]\n"
    "s_add_u32 m0, s43, 53248\n"
    "ds_read2st64_b32 v[172:173], v250 offset0:4 offset1:5\n"
    "v_mfma_f32_16x16x32_bf16 v[216:219], a[176:179], a[76:79], v[216:219]\n"
    "s_add_u32 s40, s40, 0x8000\n"
    "v_lshrrev_b32_e32 v255, 3, v253\n"
    "v_mfma_f32_16x16x32_bf16 v[216:219], a[180:183], a[80:83], v[216:219]\n"
    "v_lshl_add_u32 v252, v255, 9, v252\n"
    "v_mfma_f32_16x16x32_bf16 v[224:227], a[180:183], a[112:115], v[224:227]\n"
    "buffer_load_dwordx4 v248, s[8:11], s41 offen offset:0 sc1 lds\n"
    "v_mfma_f32_16x16x32_bf16 v[224:227], a[176:179], a[108:111], v[224:227]\n"
    "s_add_u32 s56, s56, 5373952\n"
    "v_lshlrev_b32_e32 v253, 4, v253\n"
    "v_mfma_f32_16x16x32_bf16 v[232:235], a[176:179], a[140:143], v[232:235]\n"
    "ds_read_b128 a[176:179], v241 offset:22528\n"
    "v_mfma_f32_16x16x32_bf16 v[232:235], a[180:183], a[144:147], v[232:235]\n"
    "ds_read_b128 a[180:183], v242 offset:18432\n"
    "v_mfma_f32_16x16x32_bf16 v[232:235], a[184:187], a[148:151], v[232:235]\n"
    "buffer_load_dwordx4 v248, s[8:11], s41 offen offset:1024 sc1 lds\n"
    "v_mfma_f32_16x16x32_bf16 v[232:235], a[188:191], a[152:155], v[232:235]\n"
    "ds_read2st64_b32 v[174:175], v250 offset0:6 offset1:7\n"
    "v_mfma_f32_16x16x32_bf16 v[224:227], a[188:191], a[120:123], v[224:227]\n"
    "ds_read2st64_b32 v[180:181], v250 offset0:8 offset1:9\n"
    "v_mfma_f32_16x16x32_bf16 v[224:227], a[184:187], a[116:119], v[224:227]\n"
    "ds_read2st64_b32 v[182:183], v250 offset0:10 offset1:11\n"
    "v_mfma_f32_16x16x32_bf16 v[216:219], a[184:187], a[84:87], v[216:219]\n"
    "buffer_load_dwordx4 v248, s[8:11], s41 offen offset:2048 sc1 lds\n"
    "v_mfma_f32_16x16x32_bf16 v[216:219], a[188:191], a[88:91], v[216:219]\n"
    "ds_read2st64_b32 v[188:189], v250 offset0:12 offset1:13\n"
    "v_mfma_f32_16x16x32_bf16 v[208:211], a[188:191], a[56:59], v[208:211]\n"
    "ds_read2st64_b32 v[190:191], v250 offset0:14 offset1:15\n"
    "v_mfma_f32_16x16x32_bf16 v[208:211], a[184:187], a[52:55], v[208:211]\n"
    "v_exp_f32_e32 v232, v232\n"
    "v_mfma_f32_16x16x32_bf16 v[200:203], a[184:187], a[20:23], v[200:203]\n"
    "ds_read_b128 a[184:187], v243 offset:18432\n"
    "buffer_load_dwordx4 v248, s[8:11], s41 offen offset:3072 sc1 lds\n"
    "v_mfma_f32_16x16x32_bf16 v[200:203], a[188:191], a[24:27], v[200:203]\n"
    "s_waitcnt lgkmcnt(9)\n"
    "s_add_u32 s41, s41, 0x8000\n"
    "ds_read_b128 a[188:191], v242 offset:22528\n"
    "v_mfma_f32_16x16x32_bf16 v[204:207], a[160:163], a[28:31], 0\n"
    "ds_read2st64_b32 v[196:197], v250 offset0:16 offset1:17\n"
    "v_mfma_f32_16x16x32_bf16 v[204:207], a[164:167], a[0:3], v[204:207]\n"
    "v_exp_f32_e32 v233, v233\n"
    "v_mfma_f32_16x16x32_bf16 v[212:215], a[164:167], a[32:35], 0\n"
    "v_exp_f32_e32 v234, v234\n"
    "v_mfma_f32_16x16x32_bf16 v[212:215], a[160:163], a[60:63], v[212:215]\n"
    "ds_read2st64_b32 v[198:199], v250 offset0:18 offset1:19\n"
    "v_mfma_f32_16x16x32_bf16 v[220:223], a[160:163], a[92:95], 0\n"
    "v_exp_f32_e32 v235, v235\n"
    "v_mfma_f32_16x16x32_bf16 v[220:223], a[164:167], a[64:67], v[220:223]\n"
    "v_cvt_pk_bf16_f32 v232, v232, v233\n"
    "v_mfma_f32_16x16x32_bf16 v[228:231], a[164:167], a[96:99], 0\n"
    "v_exp_f32_e32 v224, v224\n"
    "v_mfma_f32_16x16x32_bf16 v[228:231], a[160:163], a[124:127], v[228:231]\n"
    "v_exp_f32_e32 v225, v225\n"
    "v_mfma_f32_16x16x32_bf16 v[236:239], a[160:163], a[156:159], 0\n"
    "ds_read_b64_tr_b16 a[160:161], v244 offset:0\n"
    "v_mfma_f32_16x16x32_bf16 v[236:239], a[164:167], a[128:131], v[236:239]\n"
    "ds_read_b64_tr_b16 a[162:163], v244 offset:2048\n"
    "v_mfma_f32_16x16x32_bf16 v[236:239], a[168:171], a[132:135], v[236:239]\n"
    "ds_read_b64_tr_b16 a[164:165], v244 offset:8192\n"
    "v_mfma_f32_16x16x32_bf16 v[236:239], a[172:175], a[136:139], v[236:239]\n"
    "ds_read_b64_tr_b16 a[166:167], v244 offset:10240\n"
    "v_mfma_f32_16x16x32_bf16 v[228:231], a[172:175], a[104:107], v[228:231]\n"
    "v_cvt_pk_bf16_f32 v233, v234, v235\n"
    "v_mfma_f32_16x16x32_bf16 v[228:231], a[168:171], a[100:103], v[228:231]\n"
    "v_exp_f32_e32 v226, v226\n"
    "v_mfma_f32_16x16x32_bf16 v[220:223], a[168:171], a[68:71], v[220:223]\n"
    "v_exp_f32_e32 v227, v227\n"
    "v_mfma_f32_16x16x32_bf16 v[220:223], a[172:175], a[72:75], v[220:223]\n"
    "v_cvt_pk_bf16_f32 v224, v224, v225\n"
    "v_mfma_f32_16x16x32_bf16 v[212:215], a[172:175], a[40:43], v[212:215]\n"
    "v_exp_f32_e32 v216, v216\n"
    "v_mfma_f32_16x16x32_bf16 v[212:215], a[168:171], a[36:39], v[212:215]\n"
    "v_exp_f32_e32 v217, v217\n"
    "v_mfma_f32_16x16x32_bf16 v[204:207], a[168:171], a[4:7], v[204:207]\n"
    "ds_read_b64_tr_b16 a[168:169], v245 offset:0\n"
    "v_mfma_f32_16x16x32_bf16 v[204:207], a[172:175], a[8:11], v[204:207]\n"
    "s_waitcnt lgkmcnt(7)\n"
    "ds_read_b64_tr_b16 a[170:171], v245 offset:2048\n"
    "v_mfma_f32_16x16x32_bf16 v[204:207], a[176:179], a[12:15], v[204:207]\n"
    "ds_read_b64_tr_b16 a[172:173], v245 offset:8192\n"
    "buffer_load_dword v255, v254, s[8:11], s45 offen offset:0 sc1\n"
    "v_mfma_f32_16x16x32_bf16 v[204:207], a[180:183], a[16:19], v[204:207]\n"
    "ds_read_b64_tr_b16 a[174:175], v245 offset:10240\n"
    "v_mfma_f32_16x16x32_bf16 v[212:215], a[180:183], a[48:51], v[212:215]\n"
    "v_cvt_pk_bf16_f32 v225, v226, v227\n"
    "v_mfma_f32_16x16x32_bf16 v[212:215], a[176:179], a[44:47], v[212:215]\n"
    "v_exp_f32_e32 v218, v218\n"
    "v_mfma_f32_16x16x32_bf16 v[220:223], a[176:179], a[76:79], v[220:223]\n"
    "v_exp_f32_e32 v219, v219\n"
    "v_mfma_f32_16x16x32_bf16 v[220:223], a[180:183], a[80:83], v[220:223]\n"
    "v_cvt_pk_bf16_f32 v216, v216, v217\n"
    "v_mfma_f32_16x16x32_bf16 v[228:231], a[180:183], a[112:115], v[228:231]\n"
    "v_exp_f32_e32 v208, v208\n"
    "v_mfma_f32_16x16x32_bf16 v[228:231], a[176:179], a[108:111], v[228:231]\n"
    "v_exp_f32_e32 v209, v209\n"
    "v_mfma_f32_16x16x32_bf16 v[236:239], a[176:179], a[140:143], v[236:239]\n"
    "ds_read_b64_tr_b16 a[176:177], v246 offset:0\n"
    "v_mfma_f32_16x16x32_bf16 v[236:239], a[180:183], a[144:147], v[236:239]\n"
    "ds_read_b64_tr_b16 a[178:179], v246 offset:2048\n"
    "v_mfma_f32_16x16x32_bf16 v[236:239], a[184:187], a[148:151], v[236:239]\n"
    "ds_read_b64_tr_b16 a[180:181], v246 offset:8192\n"
    "v_mfma_f32_16x16x32_bf16 v[236:239], a[188:191], a[152:155], v[236:239]\n"
    "ds_read_b64_tr_b16 a[182:183], v246 offset:10240\n"
    "v_mfma_f32_16x16x32_bf16 v[228:231], a[188:191], a[120:123], v[228:231]\n"
    "v_cvt_pk_bf16_f32 v217, v218, v219\n"
    "v_mfma_f32_16x16x32_bf16 v[228:231], a[184:187], a[116:119], v[228:231]\n"
    "v_exp_f32_e32 v210, v210\n"
    "v_mfma_f32_16x16x32_bf16 v[220:223], a[184:187], a[84:87], v[220:223]\n"
    "v_exp_f32_e32 v211, v211\n"
    "v_mfma_f32_16x16x32_bf16 v[220:223], a[188:191], a[88:91], v[220:223]\n"
    "v_cvt_pk_bf16_f32 v208, v208, v209\n"
    "v_mfma_f32_16x16x32_bf16 v[212:215], a[188:191], a[56:59], v[212:215]\n"
    "v_exp_f32_e32 v200, v200\n"
    "v_mfma_f32_16x16x32_bf16 v[212:215], a[184:187], a[52:55], v[212:215]\n"
    "v_exp_f32_e32 v201, v201\n"
    "v_mfma_f32_16x16x32_bf16 v[204:207], a[184:187], a[20:23], v[204:207]\n"
    "ds_read_b64_tr_b16 a[184:185], v247 offset:0\n"
    "v_mfma_f32_16x16x32_bf16 v[204:207], a[188:191], a[24:27], v[204:207]\n"
    "s_waitcnt lgkmcnt(5)\n"
    "ds_read_b64_tr_b16 a[186:187], v247 offset:2048\n"
    "v_mfma_f32_16x16x32_bf16 a[248:251], a[160:163], v[160:163], a[248:251]\n"
    "ds_read_b64_tr_b16 a[188:189], v247 offset:8192\n"
    "v_mfma_f32_16x16x32_bf16 a[248:251], a[164:167], v[164:167], a[248:251]\n"
    "ds_read_b64_tr_b16 a[190:191], v247 offset:10240\n"
    "v_mfma_f32_16x16x32_bf16 a[252:255], a[164:167], v[172:175], a[252:255]\n"
    "v_cvt_pk_bf16_f32 v209, v210, v211\n"
    "v_mfma_f32_16x16x32_bf16 a[252:255], a[160:163], v[168:171], a[252:255]\n"
    "v_exp_f32_e32 v202, v202\n"
    "v_mfma_f32_16x16x32_bf16 v[56:59], a[160:163], v[176:179], v[56:59]\n"
    "v_exp_f32_e32 v203, v203\n"
    "v_mfma_f32_16x16x32_bf16 v[56:59], a[164:167], v[180:183], v[56:59]\n"
    "v_cvt_pk_bf16_f32 v200, v200, v201\n"
    "v_mfma_f32_16x16x32_bf16 v[60:63], a[164:167], v[188:191], v[60:63]\n"
    "v_cvt_pk_bf16_f32 v201, v202, v203\n"
    "v_mfma_f32_16x16x32_bf16 v[60:63], a[160:163], v[184:187], v[60:63]\n"
    "v_exp_f32_e32 v236, v236\n"
    "v_mfma_f32_16x16x32_bf16 v[64:67], a[160:163], v[192:195], v[64:67]\n"
    "ds_read_b64_tr_b16 a[160:161], v244 offset:4096\n"
    "v_mfma_f32_16x16x32_bf16 v[64:67], a[164:167], v[196:199], v[64:67]\n"
    "ds_read_b64_tr_b16 a[162:163], v244 offset:6144\n"
    "v_mfma_f32_16x16x32_bf16 v[76:79], a[168:171], v[192:195], v[76:79]\n"
    "ds_read_b64_tr_b16 a[164:165], v244 offset:12288\n"
    "v_mfma_f32_16x16x32_bf16 v[76:79], a[172:175], v[196:199], v[76:79]\n"
    "ds_read_b64_tr_b16 a[166:167], v244 offset:14336\n"
    "v_mfma_f32_16x16x32_bf16 v[72:75], a[172:175], v[188:191], v[72:75]\n"
    "v_exp_f32_e32 v237, v237\n"
    "v_mfma_f32_16x16x32_bf16 v[72:75], a[168:171], v[184:187], v[72:75]\n"
    "v_exp_f32_e32 v238, v238\n"
    "v_mfma_f32_16x16x32_bf16 v[68:71], a[168:171], v[176:179], v[68:71]\n"
    "v_exp_f32_e32 v239, v239\n"
    "v_mfma_f32_16x16x32_bf16 v[68:71], a[172:175], v[180:183], v[68:71]\n"
    "v_cvt_pk_bf16_f32 v234, v236, v237\n"
    "v_mfma_f32_16x16x32_bf16 v[4:7], a[172:175], v[172:175], v[4:7]\n"
    "v_exp_f32_e32 v228, v228\n"
    "v_mfma_f32_16x16x32_bf16 v[4:7], a[168:171], v[168:171], v[4:7]\n"
    "v_exp_f32_e32 v229, v229\n"
    "v_mfma_f32_16x16x32_bf16 v[0:3], a[168:171], v[160:163], v[0:3]\n"
    "ds_read_b64_tr_b16 a[168:169], v245 offset:4096\n"
    "v_mfma_f32_16x16x32_bf16 v[0:3], a[172:175], v[164:167], v[0:3]\n"
    "s_waitcnt lgkmcnt(5)\n"
    "ds_read_b64_tr_b16 a[170:171], v245 offset:6144\n"
    "v_mfma_f32_16x16x32_bf16 v[8:11], a[176:179], v[160:163], v[8:11]\n"
    "ds_read_b64_tr_b16 a[172:173], v245 offset:12288\n"
    "v_mfma_f32_16x16x32_bf16 v[8:11], a[180:183], v[164:167], v[8:11]\n"
    "ds_read_b64_tr_b16 a[174:175], v245 offset:14336\n"
    "v_mfma_f32_16x16x32_bf16 v[12:15], a[180:183], v[172:175], v[12:15]\n"
    "v_cvt_pk_bf16_f32 v235, v238, v239\n"
    "v_mfma_f32_16x16x32_bf16 v[12:15], a[176:179], v[168:171], v[12:15]\n"
    "v_exp_f32_e32 v230, v230\n"
    "v_mfma_f32_16x16x32_bf16 v[80:83], a[176:179], v[176:179], v[80:83]\n"
    "v_exp_f32_e32 v231, v231\n"
    "v_mfma_f32_16x16x32_bf16 v[80:83], a[180:183], v[180:183], v[80:83]\n"
    "v_cvt_pk_bf16_f32 v226, v228, v229\n"
    "v_mfma_f32_16x16x32_bf16 v[84:87], a[180:183], v[188:191], v[84:87]\n"
    "v_exp_f32_e32 v220, v220\n"
    "v_mfma_f32_16x16x32_bf16 v[84:87], a[176:179], v[184:187], v[84:87]\n"
    "v_exp_f32_e32 v221, v221\n"
    "v_mfma_f32_16x16x32_bf16 v[88:91], a[176:179], v[192:195], v[88:91]\n"
    "ds_read_b64_tr_b16 a[176:177], v246 offset:4096\n"
    "v_mfma_f32_16x16x32_bf16 v[88:91], a[180:183], v[196:199], v[88:91]\n"
    "ds_read_b64_tr_b16 a[178:179], v246 offset:6144\n"
    "v_mfma_f32_16x16x32_bf16 v[100:103], a[184:187], v[192:195], v[100:103]\n"
    "ds_read_b64_tr_b16 a[180:181], v246 offset:12288\n"
    "v_mfma_f32_16x16x32_bf16 v[100:103], a[188:191], v[196:199], v[100:103]\n"
    "ds_read_b64_tr_b16 a[182:183], v246 offset:14336\n"
    "v_mfma_f32_16x16x32_bf16 v[96:99], a[188:191], v[188:191], v[96:99]\n"
    "s_waitcnt vmcnt(0)\n"
    "v_cmp_ne_u32_e32 vcc, s39, v255\n"
    "v_mfma_f32_16x16x32_bf16 v[96:99], a[184:187], v[184:187], v[96:99]\n"
    "v_cvt_pk_bf16_f32 v227, v230, v231\n"
    "v_mfma_f32_16x16x32_bf16 v[92:95], a[184:187], v[176:179], v[92:95]\n"
    "s_cbranch_vccnz .Lat_wpoll\n"
    ".Lat_wpoll_ret:\n"
    "v_mfma_f32_16x16x32_bf16 v[92:95], a[188:191], v[180:183], v[92:95]\n"
    "v_exp_f32_e32 v222, v222\n"
    "v_mfma_f32_16x16x32_bf16 v[20:23], a[188:191], v[172:175], v[20:23]\n"
    "v_exp_f32_e32 v223, v223\n"
    "v_mfma_f32_16x16x32_bf16 v[20:23], a[184:187], v[168:171], v[20:23]\n"
    "v_cvt_pk_bf16_f32 v218, v220, v221\n"
    "v_mfma_f32_16x16x32_bf16 v[16:19], a[184:187], v[160:163], v[16:19]\n"
    "ds_read_b64_tr_b16 a[184:185], v247 offset:4096\n"
    "v_mfma_f32_16x16x32_bf16 v[16:19], a[188:191], v[164:167], v[16:19]\n"
    "s_waitcnt lgkmcnt(5)\n"
    "s_mov_b32 m0, s49\n"
    "ds_read_b64_tr_b16 a[186:187], v247 offset:6144\n"
    "v_mfma_f32_16x16x32_bf16 v[24:27], a[160:163], v[160:163], v[24:27]\n"
    "ds_read_b64_tr_b16 a[188:189], v247 offset:12288\n"
    "v_mfma_f32_16x16x32_bf16 v[24:27], a[164:167], v[164:167], v[24:27]\n"
    "ds_read_b64_tr_b16 a[190:191], v247 offset:14336\n"
    "v_mfma_f32_16x16x32_bf16 v[28:31], a[164:167], v[172:175], v[28:31]\n"
    "v_exp_f32_e32 v212, v212\n"
    "v_mfma_f32_16x16x32_bf16 v[28:31], a[160:163], v[168:171], v[28:31]\n"
    "v_exp_f32_e32 v213, v213\n"
    "v_mfma_f32_16x16x32_bf16 v[104:107], a[160:163], v[176:179], v[104:107]\n"
    "v_cvt_pk_bf16_f32 v219, v222, v223\n"
    "v_mfma_f32_16x16x32_bf16 v[104:107], a[164:167], v[180:183], v[104:107]\n"
    "v_exp_f32_e32 v214, v214\n"
    "v_mfma_f32_16x16x32_bf16 v[108:111], a[164:167], v[188:191], v[108:111]\n"
    "v_exp_f32_e32 v215, v215\n"
    "v_mfma_f32_16x16x32_bf16 v[108:111], a[160:163], v[184:187], v[108:111]\n"
    "s_waitcnt vmcnt(9)\n"
    "s_barrier\n"
    "v_mfma_f32_16x16x32_bf16 v[112:115], a[160:163], v[192:195], v[112:115]\n"
    "ds_read_b128 a[160:163], v243 offset:36864\n"
    "v_mfma_f32_16x16x32_bf16 v[112:115], a[164:167], v[196:199], v[112:115]\n"
    "ds_read_b128 a[164:167], v240 offset:32768\n"
    "v_mfma_f32_16x16x32_bf16 v[124:127], a[168:171], v[192:195], v[124:127]\n"
    "ds_write_addtid_b32 v232 offset:4096\n"
    "v_cvt_pk_bf16_f32 v210, v212, v213\n"
    "v_mfma_f32_16x16x32_bf16 v[124:127], a[172:175], v[196:199], v[124:127]\n"
    "v_exp_f32_e32 v204, v204\n"
    "v_mfma_f32_16x16x32_bf16 v[120:123], a[172:175], v[188:191], v[120:123]\n"
    "v_exp_f32_e32 v205, v205\n"
    "v_mfma_f32_16x16x32_bf16 v[120:123], a[168:171], v[184:187], v[120:123]\n"
    "ds_write_addtid_b32 v233 offset:4352\n"
    "v_cvt_pk_bf16_f32 v211, v214, v215\n"
    "v_mfma_f32_16x16x32_bf16 v[116:119], a[168:171], v[176:179], v[116:119]\n"
    "v_exp_f32_e32 v206, v206\n"
    "v_mfma_f32_16x16x32_bf16 v[116:119], a[172:175], v[180:183], v[116:119]\n"
    "v_exp_f32_e32 v207, v207\n"
    "v_mfma_f32_16x16x32_bf16 v[36:39], a[172:175], v[172:175], v[36:39]\n"
    "ds_write_addtid_b32 v224 offset:3072\n"
    "v_cvt_pk_bf16_f32 v202, v204, v205\n"
    "v_mfma_f32_16x16x32_bf16 v[36:39], a[168:171], v[168:171], v[36:39]\n"
    "ds_write_addtid_b32 v225 offset:3328\n"
    "v_cvt_pk_bf16_f32 v203, v206, v207\n"
    "v_mfma_f32_16x16x32_bf16 v[32:35], a[168:171], v[160:163], v[32:35]\n"
    "ds_read_b128 a[168:171], v241 offset:32768\n"
    "v_mfma_f32_16x16x32_bf16 v[32:35], a[172:175], v[164:167], v[32:35]\n"
    "s_waitcnt lgkmcnt(7)\n"
    "ds_read_b128 a[172:175], v240 offset:36864\n"
    "v_mfma_f32_16x16x32_bf16 v[40:43], a[176:179], v[160:163], v[40:43]\n"
    "ds_write_addtid_b32 v216 offset:2048\n"
    "v_mfma_f32_16x16x32_bf16 v[40:43], a[180:183], v[164:167], v[40:43]\n"
    "ds_write_addtid_b32 v217 offset:2304\n"
    "v_mfma_f32_16x16x32_bf16 v[44:47], a[180:183], v[172:175], v[44:47]\n"
    "ds_write_addtid_b32 v208 offset:1024\n"
    "v_mfma_f32_16x16x32_bf16 v[44:47], a[176:179], v[168:171], v[44:47]\n"
    "ds_write_addtid_b32 v209 offset:1280\n"
    "v_mfma_f32_16x16x32_bf16 v[128:131], a[176:179], v[176:179], v[128:131]\n"
    "ds_write_addtid_b32 v200 offset:0\n"
    "v_mfma_f32_16x16x32_bf16 v[128:131], a[180:183], v[180:183], v[128:131]\n"
    "ds_write_addtid_b32 v201 offset:256\n"
    "v_mfma_f32_16x16x32_bf16 v[132:135], a[180:183], v[188:191], v[132:135]\n"
    "ds_write_addtid_b32 v234 offset:4608\n"
    "v_mfma_f32_16x16x32_bf16 v[132:135], a[176:179], v[184:187], v[132:135]\n"
    "ds_write_addtid_b32 v235 offset:4864\n"
    "v_mfma_f32_16x16x32_bf16 v[136:139], a[176:179], v[192:195], v[136:139]\n"
    "ds_read_b128 a[176:179], v241 offset:36864\n"
    "v_mfma_f32_16x16x32_bf16 v[136:139], a[180:183], v[196:199], v[136:139]\n"
    "ds_read_b128 a[180:183], v242 offset:32768\n"
    "v_mfma_f32_16x16x32_bf16 v[148:151], a[184:187], v[192:195], v[148:151]\n"
    "ds_write_addtid_b32 v226 offset:3584\n"
    "v_mfma_f32_16x16x32_bf16 v[148:151], a[188:191], v[196:199], v[148:151]\n"
    "ds_write_addtid_b32 v227 offset:3840\n"
    "v_mfma_f32_16x16x32_bf16 v[144:147], a[188:191], v[188:191], v[144:147]\n"
    "ds_write_addtid_b32 v218 offset:2560\n"
    "v_mfma_f32_16x16x32_bf16 v[144:147], a[184:187], v[184:187], v[144:147]\n"
    "ds_write_addtid_b32 v219 offset:2816\n"
    "v_mfma_f32_16x16x32_bf16 v[140:143], a[184:187], v[176:179], v[140:143]\n"
    "ds_write_addtid_b32 v210 offset:1536\n"
    "v_mfma_f32_16x16x32_bf16 v[140:143], a[188:191], v[180:183], v[140:143]\n"
    "ds_write_addtid_b32 v211 offset:1792\n"
    "v_mfma_f32_16x16x32_bf16 v[52:55], a[188:191], v[172:175], v[52:55]\n"
    "ds_write_addtid_b32 v202 offset:512\n"
    "v_mfma_f32_16x16x32_bf16 v[52:55], a[184:187], v[168:171], v[52:55]\n"
    "ds_write_addtid_b32 v203 offset:768\n"
    "v_mfma_f32_16x16x32_bf16 v[48:51], a[184:187], v[160:163], v[48:51]\n"
    "ds_read_b128 a[184:187], v243 offset:32768\n"
    "v_mfma_f32_16x16x32_bf16 v[48:51], a[188:191], v[164:167], v[48:51]\n"
    "s_waitcnt lgkmcnt(15)\n"
    "ds_read_b128 a[188:191], v242 offset:36864\n"
    "v_mfma_f32_16x16x32_bf16 v[176:179], a[160:163], a[92:95], 0\n"
    "s_lshl_b32 s61, s51, 16\n"
    "v_mfma_f32_16x16x32_bf16 v[176:179], a[164:167], a[64:67], v[176:179]\n"
    "s_mul_i32 s47, s50, 6144\n"
    "v_mfma_f32_16x16x32_bf16 v[184:187], a[164:167], a[96:99], 0\n"
    "s_add_u32 s61, s61, s47\n"
    "v_mfma_f32_16x16x32_bf16 v[184:187], a[160:163], a[124:127], v[184:187]\n"
    "s_add_u32 s61, s61, %3\n"
    "v_mfma_f32_16x16x32_bf16 v[192:195], a[160:163], a[156:159], 0\n"
    "s_lshl_b32 s47, %2, 7\n"
    "ds_read_b128 a[160:163], v243 offset:38912\n"
    "v_mfma_f32_16x16x32_bf16 v[192:195], a[164:167], a[128:131], v[192:195]\n"
    "s_add_u32 m0, s61, 32768\n"
    "ds_read_b128 a[164:167], v240 offset:34816\n"
    "v_mfma_f32_16x16x32_bf16 v[192:195], a[168:171], a[132:135], v[192:195]\n"
    "s_add_u32 s47, s47, s35\n"
    "v_mfma_f32_16x16x32_bf16 v[192:195], a[172:175], a[136:139], v[192:195]\n"
    "s_waitcnt lgkmcnt(0)\n"
    "s_barrier\n"
    "v_mfma_f32_16x16x32_bf16 v[184:187], a[172:175], a[104:107], v[184:187]\n"
    "ds_read2st64_b32 v[204:205], v250 offset0:0 offset1:1\n"
    "v_mfma_f32_16x16x32_bf16 v[184:187], a[168:171], a[100:103], v[184:187]\n"
    "buffer_load_dwordx4 v252, s[12:15], s47 offen offset:0 lds\n"
    "v_mfma_f32_16x16x32_bf16 v[176:179], a[168:171], a[68:71], v[176:179]\n"
    "s_add_u32 s47, s47, 4096\n"
    "ds_read_b128 a[168:171], v241 offset:34816\n"
    "v_mfma_f32_16x16x32_bf16 v[176:179], a[172:175], a[72:75], v[176:179]\n"
    "s_add_u32 m0, s61, 33792\n"
    "ds_read_b128 a[172:175], v240 offset:38912\n"
    "v_mfma_f32_16x16x32_bf16 v[176:179], a[176:179], a[76:79], v[176:179]\n"
    "ds_read2st64_b32 v[206:207], v250 offset0:2 offset1:3\n"
    "v_mfma_f32_16x16x32_bf16 v[176:179], a[180:183], a[80:83], v[176:179]\n"
    "buffer_load_dwordx4 v252, s[12:15], s47 offen offset:0 lds\n"
    "v_mfma_f32_16x16x32_bf16 v[184:187], a[180:183], a[112:115], v[184:187]\n"
    "s_add_u32 s47, s47, 4096\n"
    "v_mfma_f32_16x16x32_bf16 v[184:187], a[176:179], a[108:111], v[184:187]\n"
    "s_add_u32 m0, s61, 34816\n"
    "ds_read2st64_b32 v[212:213], v250 offset0:4 offset1:5\n"
    "v_mfma_f32_16x16x32_bf16 v[192:195], a[176:179], a[140:143], v[192:195]\n"
    "ds_read_b128 a[176:179], v241 offset:38912\n"
    "v_mfma_f32_16x16x32_bf16 v[192:195], a[180:183], a[144:147], v[192:195]\n"
    "ds_read_b128 a[180:183], v242 offset:34816\n"
    "v_mfma_f32_16x16x32_bf16 v[192:195], a[184:187], a[148:151], v[192:195]\n"
    "buffer_load_dwordx4 v252, s[12:15], s47 offen offset:0 lds\n"
    "v_mfma_f32_16x16x32_bf16 v[192:195], a[188:191], a[152:155], v[192:195]\n"
    "s_add_u32 s47, s47, 4096\n"
    "ds_read2st64_b32 v[214:215], v250 offset0:6 offset1:7\n"
    "v_mfma_f32_16x16x32_bf16 v[184:187], a[188:191], a[120:123], v[184:187]\n"
    "s_add_u32 m0, s61, 35840\n"
    "v_mfma_f32_16x16x32_bf16 v[184:187], a[184:187], a[116:119], v[184:187]\n"
    "ds_read2st64_b32 v[220:221], v250 offset0:8 offset1:9\n"
    "v_exp_f32_e32 v192, v192\n"
    "v_mfma_f32_16x16x32_bf16 v[176:179], a[184:187], a[84:87], v[176:179]\n"
    "ds_read_b128 a[184:187], v243 offset:34816\n"
    "buffer_load_dwordx4 v252, s[12:15], s47 offen offset:0 lds\n"
    "v_mfma_f32_16x16x32_bf16 v[176:179], a[188:191], a[88:91], v[176:179]\n"
    "s_add_u32 s47, s47, 4096\n"
    "ds_read_b128 a[188:191], v242 offset:38912\n"
    "v_mfma_f32_16x16x32_bf16 v[180:183], a[160:163], a[92:95], 0\n"
    "s_add_u32 m0, s61, 36864\n"
    "v_exp_f32_e32 v193, v193\n"
    "v_mfma_f32_16x16x32_bf16 v[180:183], a[164:167], a[64:67], v[180:183]\n"
    "ds_read2st64_b32 v[222:223], v250 offset0:10 offset1:11\n"
    "v_mfma_f32_16x16x32_bf16 v[188:191], a[164:167], a[96:99], 0\n"
    "buffer_load_dwordx4 v252, s[12:15], s47 offen offset:0 lds\n"
    "v_exp_f32_e32 v194, v194\n"
    "v_mfma_f32_16x16x32_bf16 v[188:191], a[160:163], a[124:127], v[188:191]\n"
    "s_add_u32 s47, s47, 4096\n"
    "ds_read2st64_b32 v[228:229], v250 offset0:12 offset1:13\n"
    "v_mfma_f32_16x16x32_bf16 v[196:199], a[160:163], a[156:159], 0\n"
    "s_add_u32 m0, s61, 37888\n"
    "ds_read_b64_tr_b16 a[160:161], v244 offset:16384\n"
    "v_exp_f32_e32 v195, v195\n"
    "v_mfma_f32_16x16x32_bf16 v[196:199], a[164:167], a[128:131], v[196:199]\n"
    "s_waitcnt lgkmcnt(11)\n"
    "ds_read_b64_tr_b16 a[162:163], v244 offset:18432\n"
    "v_mfma_f32_16x16x32_bf16 v[196:199], a[168:171], a[132:135], v[196:199]\n"
    "ds_read_b64_tr_b16 a[164:165], v244 offset:24576\n"
    "buffer_load_dwordx4 v252, s[12:15], s47 offen offset:0 lds\n"
    "v_mfma_f32_16x16x32_bf16 v[196:199], a[172:175], a[136:139], v[196:199]\n"
    "s_mov_b32 m0, s49\n"
    "ds_read_b64_tr_b16 a[166:167], v244 offset:26624\n"
    "v_cvt_pk_bf16_f32 v192, v192, v193\n"
    "v_mfma_f32_16x16x32_bf16 v[188:191], a[172:175], a[104:107], v[188:191]\n"
    "s_add_u32 s48, s57, 30720\n"
    "ds_read2st64_b32 v[230:231], v250 offset0:14 offset1:15\n"
    "v_exp_f32_e32 v184, v184\n"
    "v_mfma_f32_16x16x32_bf16 v[188:191], a[168:171], a[100:103], v[188:191]\n"
    "ds_read2st64_b32 v[236:237], v250 offset0:16 offset1:17\n"
    "v_mfma_f32_16x16x32_bf16 v[180:183], a[168:171], a[68:71], v[180:183]\n"
    "ds_read_b64_tr_b16 a[168:169], v245 offset:16384\n"
    "buffer_load_dwordx4 a[192:195], v253, s[8:11], s48 offen offset:0 sc1\n"
    "v_mfma_f32_16x16x32_bf16 v[180:183], a[172:175], a[72:75], v[180:183]\n"
    "s_waitcnt lgkmcnt(13)\n"
    "ds_read_b64_tr_b16 a[170:171], v245 offset:18432\n"
    "v_exp_f32_e32 v185, v185\n"
    "v_mfma_f32_16x16x32_bf16 v[180:183], a[176:179], a[76:79], v[180:183]\n"
    "ds_read_b64_tr_b16 a[172:173], v245 offset:24576\n"
    "v_mfma_f32_16x16x32_bf16 v[180:183], a[180:183], a[80:83], v[180:183]\n"
    "ds_read_b64_tr_b16 a[174:175], v245 offset:26624\n"
    "v_cvt_pk_bf16_f32 v193, v194, v195\n"
    "v_mfma_f32_16x16x32_bf16 v[188:191], a[180:183], a[112:115], v[188:191]\n"
    "buffer_load_dwordx4 a[196:199], v253, s[8:11], s48 offen offset:1024 sc1\n"
    "v_mfma_f32_16x16x32_bf16 v[188:191], a[176:179], a[108:111], v[188:191]\n"
    "s_add_u32 s48, s57, 14336\n"
    "ds_read2st64_b32 v[238:239], v250 offset0:18 offset1:19\n"
    "v_exp_f32_e32 v186, v186\n"
    "v_mfma_f32_16x16x32_bf16 v[196:199], a[176:179], a[140:143], v[196:199]\n"
    "ds_read_b64_tr_b16 a[176:177], v246 offset:16384\n"
    "v_mfma_f32_16x16x32_bf16 v[196:199], a[180:183], a[144:147], v[196:199]\n"
    "s_waitcnt lgkmcnt(14)\n"
    "ds_read_b64_tr_b16 a[178:179], v246 offset:18432\n"
    "v_exp_f32_e32 v187, v187\n"
    "v_mfma_f32_16x16x32_bf16 v[196:199], a[184:187], a[148:151], v[196:199]\n"
    "ds_read_b64_tr_b16 a[180:181], v246 offset:24576\n"
    "buffer_load_dwordx4 a[200:203], v253, s[8:11], s48 offen offset:0 sc1\n"
    "v_mfma_f32_16x16x32_bf16 v[196:199], a[188:191], a[152:155], v[196:199]\n"
    "ds_read_b64_tr_b16 a[182:183], v246 offset:26624\n"
    "v_cvt_pk_bf16_f32 v184, v184, v185\n"
    "v_mfma_f32_16x16x32_bf16 v[188:191], a[188:191], a[120:123], v[188:191]\n"
    "v_exp_f32_e32 v176, v176\n"
    "v_mfma_f32_16x16x32_bf16 v[188:191], a[184:187], a[116:119], v[188:191]\n"
    "v_exp_f32_e32 v177, v177\n"
    "v_mfma_f32_16x16x32_bf16 v[180:183], a[184:187], a[84:87], v[180:183]\n"
    "ds_read_b64_tr_b16 a[184:185], v247 offset:16384\n"
    "buffer_load_dwordx4 a[204:207], v253, s[8:11], s48 offen offset:1024 sc1\n"
    "v_mfma_f32_16x16x32_bf16 v[180:183], a[188:191], a[88:91], v[180:183]\n"
    "s_waitcnt lgkmcnt(5)\n"
    "s_add_u32 s48, s57, 16384\n"
    "ds_read_b64_tr_b16 a[186:187], v247 offset:18432\n"
    "v_mfma_f32_16x16x32_bf16 v[56:59], a[160:163], v[216:219], v[56:59]\n"
    "ds_read_b64_tr_b16 a[188:189], v247 offset:24576\n"
    "v_mfma_f32_16x16x32_bf16 v[56:59], a[164:167], v[220:223], v[56:59]\n"
    "ds_read_b64_tr_b16 a[190:191], v247 offset:26624\n"
    "v_cvt_pk_bf16_f32 v185, v186, v187\n"
    "v_mfma_f32_16x16x32_bf16 v[60:63], a[164:167], v[228:231], v[60:63]\n"
    "buffer_load_dwordx4 a[208:211], v253, s[8:11], s48 offen offset:0 sc1\n"
    "v_mfma_f32_16x16x32_bf16 v[60:63], a[160:163], v[224:227], v[60:63]\n"
    "s_lshl_b32 s61, s51, 16\n"
    "v_exp_f32_e32 v178, v178\n"
    "v_mfma_f32_16x16x32_bf16 v[64:67], a[160:163], v[232:235], v[64:67]\n"
    "ds_read_b64_tr_b16 a[160:161], v244 offset:20480\n"
    "v_exp_f32_e32 v179, v179\n"
    "v_mfma_f32_16x16x32_bf16 v[64:67], a[164:167], v[236:239], v[64:67]\n"
    "ds_read_b64_tr_b16 a[162:163], v244 offset:22528\n"
    "v_cvt_pk_bf16_f32 v176, v176, v177\n"
    "v_mfma_f32_16x16x32_bf16 v[76:79], a[168:171], v[232:235], v[76:79]\n"
    "ds_read_b64_tr_b16 a[164:165], v244 offset:28672\n"
    "buffer_load_dwordx4 a[212:215], v253, s[8:11], s48 offen offset:1024 sc1\n"
    "v_mfma_f32_16x16x32_bf16 v[76:79], a[172:175], v[236:239], v[76:79]\n"
    "s_add_u32 s48, s57, 0\n"
    "ds_read_b64_tr_b16 a[166:167], v244 offset:30720\n"
    "v_cvt_pk_bf16_f32 v177, v178, v179\n"
    "v_mfma_f32_16x16x32_bf16 v[72:75], a[172:175], v[228:231], v[72:75]\n"
    "s_waitcnt vmcnt(12)\n"
    "s_barrier\n"
    "v_mfma_f32_16x16x32_bf16 v[72:75], a[168:171], v[224:227], v[72:75]\n"
    "s_mul_i32 s47, s50, 6144\n"
    "v_exp_f32_e32 v196, v196\n"
    "v_mfma_f32_16x16x32_bf16 v[68:71], a[168:171], v[216:219], v[68:71]\n"
    "ds_read_b64_tr_b16 a[168:169], v245 offset:20480\n"
    "buffer_load_dwordx4 a[216:219], v253, s[8:11], s48 offen offset:0 sc1\n"
    "v_mfma_f32_16x16x32_bf16 v[68:71], a[172:175], v[220:223], v[68:71]\n"
    "s_waitcnt lgkmcnt(9)\n"
    "ds_read_b64_tr_b16 a[170:171], v245 offset:22528\n"
    "ds_write_addtid_b32 v192 offset:4096\n"
    "v_mfma_f32_16x16x32_bf16 v[80:83], a[176:179], v[216:219], v[80:83]\n"
    "ds_read_b64_tr_b16 a[172:173], v245 offset:28672\n"
    "v_mfma_f32_16x16x32_bf16 v[80:83], a[180:183], v[220:223], v[80:83]\n"
    "s_add_u32 s61, s61, s47\n"
    "ds_read_b64_tr_b16 a[174:175], v245 offset:30720\n"
    "v_mfma_f32_16x16x32_bf16 v[84:87], a[180:183], v[228:231], v[84:87]\n"
    "buffer_load_dwordx4 a[220:223], v253, s[8:11], s48 offen offset:1024 sc1\n"
    "v_exp_f32_e32 v197, v197\n"
    "v_mfma_f32_16x16x32_bf16 v[84:87], a[176:179], v[224:227], v[84:87]\n"
    "s_lshl_b32 s45, s51, 16\n"
    "ds_write_addtid_b32 v193 offset:4352\n"
    "v_and_b32_e32 v248, 0xffffdfff, v240\n"
    "v_mfma_f32_16x16x32_bf16 v[88:91], a[176:179], v[232:235], v[88:91]\n"
    "ds_read_b64_tr_b16 a[176:177], v246 offset:20480\n"
    "v_mfma_f32_16x16x32_bf16 v[88:91], a[180:183], v[236:239], v[88:91]\n"
    "s_waitcnt lgkmcnt(11)\n"
    "ds_read_b64_tr_b16 a[178:179], v246 offset:22528\n"
    "v_mfma_f32_16x16x32_bf16 v[100:103], a[184:187], v[232:235], v[100:103]\n"
    "s_mul_i32 s46, s50, 6144\n"
    "ds_read_b64_tr_b16 a[180:181], v246 offset:28672\n"
    "ds_write_addtid_b32 v184 offset:3072\n"
    "v_mfma_f32_16x16x32_bf16 v[100:103], a[188:191], v[236:239], v[100:103]\n"
    "s_add_u32 s61, s61, %3\n"
    "ds_read_b64_tr_b16 a[182:183], v246 offset:30720\n"
    "v_mfma_f32_16x16x32_bf16 v[96:99], a[188:191], v[228:231], v[96:99]\n"
    "v_exp_f32_e32 v198, v198\n"
    "v_mfma_f32_16x16x32_bf16 v[96:99], a[184:187], v[224:227], v[96:99]\n"
    "v_exp_f32_e32 v199, v199\n"
    "v_mfma_f32_16x16x32_bf16 v[92:95], a[184:187], v[216:219], v[92:95]\n"
    "s_add_u32 s45, s45, s46\n"
    "ds_read_b64_tr_b16 a[184:185], v247 offset:20480\n"
    "v_cvt_pk_bf16_f32 v194, v196, v197\n"
    "v_mfma_f32_16x16x32_bf16 v[92:95], a[188:191], v[220:223], v[92:95]\n"
    "s_waitcnt lgkmcnt(12)\n"
    "ds_read_b64_tr_b16 a[186:187], v247 offset:22528\n"
    "v_exp_f32_e32 v188, v188\n"
    "v_mfma_f32_16x16x32_bf16 v[104:107], a[160:163], v[216:219], v[104:107]\n"
    "s_lshl_b32 s47, %2, 7\n"
    "ds_read_b64_tr_b16 a[188:189], v247 offset:28672\n"
    "ds_write_addtid_b32 v185 offset:3328\n"
    "v_mfma_f32_16x16x32_bf16 v[104:107], a[164:167], v[220:223], v[104:107]\n"
    "ds_read_b64_tr_b16 a[190:191], v247 offset:30720\n"
    "ds_write_addtid_b32 v176 offset:2048\n"
    "v_mfma_f32_16x16x32_bf16 v[108:111], a[164:167], v[228:231], v[108:111]\n"
    "v_exp_f32_e32 v189, v189\n"
    "v_mfma_f32_16x16x32_bf16 v[108:111], a[160:163], v[224:227], v[108:111]\n"
    "v_add_u32_e32 v248, s45, v248\n"
    "v_cvt_pk_bf16_f32 v195, v198, v199\n"
    "v_mfma_f32_16x16x32_bf16 v[112:115], a[160:163], v[232:235], v[112:115]\n"
    "s_add_u32 s47, s47, s35\n"
    "ds_read_b128 a[160:163], v243 offset:53248\n"
    "v_mfma_f32_16x16x32_bf16 v[112:115], a[164:167], v[236:239], v[112:115]\n"
    "s_waitcnt lgkmcnt(13)\n"
    "ds_read_b128 a[164:167], v240 offset:49152\n"
    "ds_write_addtid_b32 v177 offset:2304\n"
    "v_mfma_f32_16x16x32_bf16 v[124:127], a[168:171], v[232:235], v[124:127]\n"
    "v_exp_f32_e32 v190, v190\n"
    "v_mfma_f32_16x16x32_bf16 v[124:127], a[172:175], v[236:239], v[124:127]\n"
    "v_exp_f32_e32 v191, v191\n"
    "v_mfma_f32_16x16x32_bf16 v[120:123], a[172:175], v[228:231], v[120:123]\n"
    "v_xor_b32_e32 v255, 64, v248\n"
    "v_cvt_pk_bf16_f32 v186, v188, v189\n"
    "v_mfma_f32_16x16x32_bf16 v[120:123], a[168:171], v[224:227], v[120:123]\n"
    "s_add_u32 s47, s47, 5697536\n"
    "v_exp_f32_e32 v180, v180\n"
    "v_mfma_f32_16x16x32_bf16 v[116:119], a[168:171], v[216:219], v[116:119]\n"
    "ds_read_b128 a[168:171], v241 offset:49152\n"
    "ds_write_addtid_b32 v194 offset:4608\n"
    "v_mfma_f32_16x16x32_bf16 v[116:119], a[172:175], v[220:223], v[116:119]\n"
    "s_waitcnt lgkmcnt(11)\n"
    "s_waitcnt vmcnt(8)\n"
    "ds_read_b128 a[172:175], v240 offset:53248\n"
    "v_mfma_f32_16x16x32_bf16 v[128:131], a[176:179], v[216:219], v[128:131]\n"
    "v_exp_f32_e32 v181, v181\n"
    "v_mfma_f32_16x16x32_bf16 v[128:131], a[180:183], v[220:223], v[128:131]\n"
    "ds_read_b128 a[224:227], v248 offset:0\n"
    "v_cvt_pk_bf16_f32 v187, v190, v191\n"
    "v_mfma_f32_16x16x32_bf16 v[132:135], a[180:183], v[228:231], v[132:135]\n"
    "v_exp_f32_e32 v182, v182\n"
    "v_mfma_f32_16x16x32_bf16 v[132:135], a[176:179], v[224:227], v[132:135]\n"
    "ds_read_b128 a[228:231], v255 offset:0\n"
    "ds_write_addtid_b32 v195 offset:4864\n"
    "v_mfma_f32_16x16x32_bf16 v[136:139], a[176:179], v[232:235], v[136:139]\n"
    "ds_read_b128 a[176:179], v241 offset:53248\n"
    "v_mfma_f32_16x16x32_bf16 v[136:139], a[180:183], v[236:239], v[136:139]\n"
    "s_waitcnt lgkmcnt(11)\n"
    "ds_read_b128 a[180:183], v242 offset:49152\n"
    "v_mfma_f32_16x16x32_bf16 v[148:151], a[184:187], v[232:235], v[148:151]\n"
    "ds_read_b128 a[232:235], v248 offset:2048\n"
    "v_mfma_f32_16x16x32_bf16 v[148:151], a[188:191], v[236:239], v[148:151]\n"
    "ds_read_b128 a[236:239], v255 offset:2048\n"
    "v_exp_f32_e32 v183, v183\n"
    "v_mfma_f32_16x16x32_bf16 v[144:147], a[188:191], v[228:231], v[144:147]\n"
    "ds_read_b128 a[240:243], v248 offset:4096\n"
    "v_mfma_f32_16x16x32_bf16 v[144:147], a[184:187], v[224:227], v[144:147]\n"
    "ds_read_b128 a[244:247], v255 offset:4096\n"
    "v_cvt_pk_bf16_f32 v178, v180, v181\n"
    "v_mfma_f32_16x16x32_bf16 v[140:143], a[184:187], v[216:219], v[140:143]\n"
    "ds_read_b128 a[184:187], v243 offset:49152\n"
    "v_mfma_f32_16x16x32_bf16 v[140:143], a[188:191], v[220:223], v[140:143]\n"
    "s_waitcnt lgkmcnt(14)\n"
    "ds_read_b128 a[188:191], v242 offset:53248\n"
    "v_cvt_pk_bf16_f32 v179, v182, v183\n"
    "v_mfma_f32_16x16x32_bf16 v[216:219], a[160:163], a[92:95], 0\n"
    "ds_write_addtid_b32 v186 offset:3584\n"
    "v_mfma_f32_16x16x32_bf16 v[216:219], a[164:167], a[64:67], v[216:219]\n"
    "ds_write_addtid_b32 v187 offset:3840\n"
    "v_mfma_f32_16x16x32_bf16 v[224:227], a[164:167], a[96:99], 0\n"
    "ds_write_addtid_b32 v178 offset:2560\n"
    "v_mfma_f32_16x16x32_bf16 v[224:227], a[160:163], a[124:127], v[224:227]\n"
    "ds_write_addtid_b32 v179 offset:2816\n"
    "v_mfma_f32_16x16x32_bf16 v[232:235], a[160:163], a[156:159], 0\n"
    "ds_read_b128 a[160:163], v243 offset:55296\n"
    "v_mfma_f32_16x16x32_bf16 v[232:235], a[164:167], a[128:131], v[232:235]\n"
    "s_waitcnt lgkmcnt(15)\n"
    "ds_read_b128 a[164:167], v240 offset:51200\n"
    "v_mfma_f32_16x16x32_bf16 v[232:235], a[168:171], a[132:135], v[232:235]\n"
    "v_mfma_f32_16x16x32_bf16 v[232:235], a[172:175], a[136:139], v[232:235]\n"
    "s_add_u32 m0, s61, 32768\n"
    "v_mfma_f32_16x16x32_bf16 v[224:227], a[172:175], a[104:107], v[224:227]\n"
    "s_waitcnt lgkmcnt(2)\n"
    "s_barrier\n"
    "v_mfma_f32_16x16x32_bf16 v[224:227], a[168:171], a[100:103], v[224:227]\n"
    "ds_read2st64_b32 v[180:181], v250 offset0:8 offset1:9\n"
    "v_mfma_f32_16x16x32_bf16 v[216:219], a[168:171], a[68:71], v[216:219]\n"
    "ds_read_b128 a[168:171], v241 offset:51200\n"
    "buffer_load_dwordx4 v252, s[8:11], s47 offen offset:0 sc1 lds\n"
    "v_mfma_f32_16x16x32_bf16 v[216:219], a[172:175], a[72:75], v[216:219]\n"
    "s_add_u32 s47, s47, 4096\n"
    "ds_read_b128 a[172:175], v240 offset:55296\n"
    "v_mfma_f32_16x16x32_bf16 v[216:219], a[176:179], a[76:79], v[216:219]\n"
    "s_add_u32 m0, s61, 33792\n"
    "ds_read2st64_b32 v[182:183], v250 offset0:10 offset1:11\n"
    "v_mfma_f32_16x16x32_bf16 v[216:219], a[180:183], a[80:83], v[216:219]\n"
    "buffer_load_dwordx4 v252, s[8:11], s47 offen offset:0 sc1 lds\n"
    "v_mfma_f32_16x16x32_bf16 v[224:227], a[180:183], a[112:115], v[224:227]\n"
    "s_add_u32 s47, s47, 4096\n"
    "v_mfma_f32_16x16x32_bf16 v[224:227], a[176:179], a[108:111], v[224:227]\n"
    "s_add_u32 m0, s61, 34816\n"
    "ds_read2st64_b32 v[188:189], v250 offset0:12 offset1:13\n"
    "v_mfma_f32_16x16x32_bf16 v[232:235], a[176:179], a[140:143], v[232:235]\n"
    "ds_read_b128 a[176:179], v241 offset:55296\n"
    "buffer_load_dwordx4 v252, s[8:11], s47 offen offset:0 sc1 lds\n"
    "v_mfma_f32_16x16x32_bf16 v[232:235], a[180:183], a[144:147], v[232:235]\n"
    "s_add_u32 s47, s47, 4096\n"
    "ds_read_b128 a[180:183], v242 offset:51200\n"
    "v_mfma_f32_16x16x32_bf16 v[232:235], a[184:187], a[148:151], v[232:235]\n"
    "s_add_u32 m0, s61, 35840\n"
    "ds_read2st64_b32 v[190:191], v250 offset0:14 offset1:15\n"
    "v_mfma_f32_16x16x32_bf16 v[232:235], a[188:191], a[152:155], v[232:235]\n"
    "buffer_load_dwordx4 v252, s[8:11], s47 offen offset:0 sc1 lds\n"
    "v_mfma_f32_16x16x32_bf16 v[224:227], a[188:191], a[120:123], v[224:227]\n"
    "s_add_u32 s47, s47, 4096\n"
    "ds_read2st64_b32 v[196:197], v250 offset0:16 offset1:17\n"
    "v_mfma_f32_16x16x32_bf16 v[224:227], a[184:187], a[116:119], v[224:227]\n"
    "s_add_u32 m0, s61, 36864\n"
    "ds_read2st64_b32 v[198:199], v250 offset0:18 offset1:19\n"
    "v_exp_f32_e32 v232, v232\n"
    "v_mfma_f32_16x16x32_bf16 v[216:219], a[184:187], a[84:87], v[216:219]\n"
    "ds_read_b128 a[184:187], v243 offset:51200\n"
    "buffer_load_dwordx4 v252, s[8:11], s47 offen offset:0 sc1 lds\n"
    "v_mfma_f32_16x16x32_bf16 v[216:219], a[188:191], a[88:91], v[216:219]\n"
    "s_waitcnt lgkmcnt(11)\n"
    "s_add_u32 s47, s47, 4096\n"
    "ds_read_b128 a[188:191], v242 offset:55296\n"
    "v_mfma_f32_16x16x32_bf16 v[220:223], a[160:163], a[92:95], 0\n"
    "s_add_u32 m0, s61, 37888\n"
    "v_exp_f32_e32 v233, v233\n"
    "v_mfma_f32_16x16x32_bf16 v[220:223], a[164:167], a[64:67], v[220:223]\n"
    "buffer_load_dwordx4 v252, s[8:11], s47 offen offset:0 sc1 lds\n"
    "v_mfma_f32_16x16x32_bf16 v[228:231], a[164:167], a[96:99], 0\n"
    "s_mov_b32 m0, s49\n"
    "v_exp_f32_e32 v234, v234\n"
    "v_mfma_f32_16x16x32_bf16 v[228:231], a[160:163], a[124:127], v[228:231]\n"
    "s_add_u32 s48, s57, 18432\n"
    "v_exp_f32_e32 v235, v235\n"
    "v_mfma_f32_16x16x32_bf16 v[236:239], a[160:163], a[156:159], 0\n"
    "ds_read_b64_tr_b16 a[160:161], v244 offset:32768\n"
    "v_mfma_f32_16x16x32_bf16 v[236:239], a[164:167], a[128:131], v[236:239]\n"
    "s_waitcnt lgkmcnt(10)\n"
    "ds_read_b64_tr_b16 a[162:163], v244 offset:34816\n"
    "v_cvt_pk_bf16_f32 v232, v232, v233\n"
    "v_mfma_f32_16x16x32_bf16 v[236:239], a[168:171], a[132:135], v[236:239]\n"
    "buffer_load_dwordx4 a[92:95], v253, s[8:11], s48 offen offset:0 sc1\n"
    "s_waitcnt lgkmcnt(4)\n"
    "s_barrier\n"
    "v_mfma_f32_16x16x32_bf16 v[236:239], a[172:175], a[136:139], v[236:239]\n"
    "ds_read_b64_tr_b16 a[164:165], v244 offset:40960\n"
    "v_exp_f32_e32 v224, v224\n"
    "v_mfma_f32_16x16x32_bf16 v[228:231], a[172:175], a[104:107], v[228:231]\n"
    "ds_read_b64_tr_b16 a[166:167], v244 offset:43008\n"
    "v_exp_f32_e32 v225, v225\n"
    "v_mfma_f32_16x16x32_bf16 v[228:231], a[168:171], a[100:103], v[228:231]\n"
    "buffer_load_dwordx4 a[124:127], v253, s[8:11], s48 offen offset:1024 sc1\n"
    "v_mfma_f32_16x16x32_bf16 v[220:223], a[168:171], a[68:71], v[220:223]\n"
    "s_add_u32 s48, s57, 2048\n"
    "ds_read_b64_tr_b16 a[168:169], v245 offset:32768\n"
    "v_cvt_pk_bf16_f32 v233, v234, v235\n"
    "v_mfma_f32_16x16x32_bf16 v[220:223], a[172:175], a[72:75], v[220:223]\n"
    "ds_read_b64_tr_b16 a[170:171], v245 offset:34816\n"
    "v_exp_f32_e32 v226, v226\n"
    "v_mfma_f32_16x16x32_bf16 v[220:223], a[176:179], a[76:79], v[220:223]\n"
    "ds_read_b64_tr_b16 a[172:173], v245 offset:40960\n"
    "buffer_load_dwordx4 a[156:159], v253, s[8:11], s48 offen offset:0 sc1\n"
    "v_mfma_f32_16x16x32_bf16 v[220:223], a[180:183], a[80:83], v[220:223]\n"
    "ds_read_b64_tr_b16 a[174:175], v245 offset:43008\n"
    "v_mfma_f32_16x16x32_bf16 v[228:231], a[180:183], a[112:115], v[228:231]\n"
    "v_exp_f32_e32 v227, v227\n"
    "v_mfma_f32_16x16x32_bf16 v[228:231], a[176:179], a[108:111], v[228:231]\n"
    "buffer_load_dwordx4 a[64:67], v253, s[8:11], s48 offen offset:1024 sc1\n"
    "v_cvt_pk_bf16_f32 v224, v224, v225\n"
    "v_mfma_f32_16x16x32_bf16 v[236:239], a[176:179], a[140:143], v[236:239]\n"
    "s_add_u32 s48, s57, 20480\n"
    "ds_read_b64_tr_b16 a[176:177], v246 offset:32768\n"
    "v_exp_f32_e32 v216, v216\n"
    "v_mfma_f32_16x16x32_bf16 v[236:239], a[180:183], a[144:147], v[236:239]\n"
    "s_waitcnt lgkmcnt(9)\n"
    "ds_read_b64_tr_b16 a[178:179], v246 offset:34816\n"
    "v_mfma_f32_16x16x32_bf16 v[236:239], a[184:187], a[148:151], v[236:239]\n"
    "ds_read_b64_tr_b16 a[180:181], v246 offset:40960\n"
    "buffer_load_dwordx4 a[96:99], v253, s[8:11], s48 offen offset:0 sc1\n"
    "v_mfma_f32_16x16x32_bf16 v[236:239], a[188:191], a[152:155], v[236:239]\n"
    "ds_read_b64_tr_b16 a[182:183], v246 offset:43008\n"
    "v_mfma_f32_16x16x32_bf16 v[228:231], a[188:191], a[120:123], v[228:231]\n"
    "v_exp_f32_e32 v217, v217\n"
    "v_mfma_f32_16x16x32_bf16 v[228:231], a[184:187], a[116:119], v[228:231]\n"
    "buffer_load_dwordx4 a[128:131], v253, s[8:11], s48 offen offset:1024 sc1\n"
    "v_cvt_pk_bf16_f32 v225, v226, v227\n"
    "v_mfma_f32_16x16x32_bf16 v[220:223], a[184:187], a[84:87], v[220:223]\n"
    "s_add_u32 s48, s57, 4096\n"
    "ds_read_b64_tr_b16 a[184:185], v247 offset:32768\n"
    "v_exp_f32_e32 v218, v218\n"
    "v_mfma_f32_16x16x32_bf16 v[220:223], a[188:191], a[88:91], v[220:223]\n"
    "s_waitcnt lgkmcnt(9)\n"
    "ds_read_b64_tr_b16 a[186:187], v247 offset:34816\n"
    "v_mfma_f32_16x16x32_bf16 v[56:59], a[160:163], v[176:179], v[56:59]\n"
    "ds_read_b64_tr_b16 a[188:189], v247 offset:40960\n"
    "buffer_load_dwordx4 a[68:71], v253, s[8:11], s48 offen offset:0 sc1\n"
    "v_mfma_f32_16x16x32_bf16 v[56:59], a[164:167], v[180:183], v[56:59]\n"
    "ds_read_b64_tr_b16 a[190:191], v247 offset:43008\n"
    "v_mfma_f32_16x16x32_bf16 v[60:63], a[164:167], v[188:191], v[60:63]\n"
    "v_exp_f32_e32 v219, v219\n"
    "v_mfma_f32_16x16x32_bf16 v[60:63], a[160:163], v[184:187], v[60:63]\n"
    "buffer_load_dwordx4 a[100:103], v253, s[8:11], s48 offen offset:1024 sc1\n"
    "v_cvt_pk_bf16_f32 v216, v216, v217\n"
    "v_mfma_f32_16x16x32_bf16 v[64:67], a[160:163], v[192:195], v[64:67]\n"
    "s_add_u32 s48, s57, 22528\n"
    "ds_read_b64_tr_b16 a[160:161], v244 offset:36864\n"
    "v_cvt_pk_bf16_f32 v217, v218, v219\n"
    "v_mfma_f32_16x16x32_bf16 v[64:67], a[164:167], v[196:199], v[64:67]\n"
    "s_waitcnt lgkmcnt(9)\n"
    "ds_read_b64_tr_b16 a[162:163], v244 offset:38912\n"
    "v_exp_f32_e32 v236, v236\n"
    "v_mfma_f32_16x16x32_bf16 v[76:79], a[168:171], v[192:195], v[76:79]\n"
    "ds_read_b64_tr_b16 a[164:165], v244 offset:45056\n"
    "buffer_load_dwordx4 a[132:135], v253, s[8:11], s48 offen offset:0 sc1\n"
    "v_mfma_f32_16x16x32_bf16 v[76:79], a[172:175], v[196:199], v[76:79]\n"
    "ds_read_b64_tr_b16 a[166:167], v244 offset:47104\n"
    "v_exp_f32_e32 v237, v237\n"
    "v_mfma_f32_16x16x32_bf16 v[72:75], a[172:175], v[188:191], v[72:75]\n"
    "v_exp_f32_e32 v238, v238\n"
    "v_mfma_f32_16x16x32_bf16 v[72:75], a[168:171], v[184:187], v[72:75]\n"
    "buffer_load_dwordx4 a[72:75], v253, s[8:11], s48 offen offset:1024 sc1\n"
    "v_exp_f32_e32 v239, v239\n"
    "v_mfma_f32_16x16x32_bf16 v[68:71], a[168:171], v[176:179], v[68:71]\n"
    "s_add_u32 s48, s57, 6144\n"
    "ds_read_b64_tr_b16 a[168:169], v245 offset:36864\n"
    "ds_write_addtid_b32 v232 offset:4096\n"
    "v_mfma_f32_16x16x32_bf16 v[68:71], a[172:175], v[180:183], v[68:71]\n"
    "s_waitcnt lgkmcnt(10)\n"
    "ds_read_b64_tr_b16 a[170:171], v245 offset:38912\n"
    "v_cvt_pk_bf16_f32 v234, v236, v237\n"
    "v_mfma_f32_16x16x32_bf16 v[80:83], a[176:179], v[176:179], v[80:83]\n"
    "ds_read_b64_tr_b16 a[172:173], v245 offset:45056\n"
    "buffer_load_dwordx4 a[104:107], v253, s[8:11], s48 offen offset:0 sc1\n"
    "v_mfma_f32_16x16x32_bf16 v[80:83], a[180:183], v[180:183], v[80:83]\n"
    "ds_read_b64_tr_b16 a[174:175], v245 offset:47104\n"
    "v_exp_f32_e32 v228, v228\n"
    "v_mfma_f32_16x16x32_bf16 v[84:87], a[180:183], v[188:191], v[84:87]\n"
    "v_exp_f32_e32 v229, v229\n"
    "v_mfma_f32_16x16x32_bf16 v[84:87], a[176:179], v[184:187], v[84:87]\n"
    "buffer_load_dwordx4 a[136:139], v253, s[8:11], s48 offen offset:1024 sc1\n"
    "ds_write_addtid_b32 v233 offset:4352\n"
    "v_mfma_f32_16x16x32_bf16 v[88:91], a[176:179], v[192:195], v[88:91]\n"
    "s_add_u32 s48, s57, 24576\n"
    "ds_read_b64_tr_b16 a[176:177], v246 offset:36864\n"
    "v_cvt_pk_bf16_f32 v235, v238, v239\n"
    "v_mfma_f32_16x16x32_bf16 v[88:91], a[180:183], v[196:199], v[88:91]\n"
    "s_waitcnt lgkmcnt(11)\n"
    "ds_read_b64_tr_b16 a[178:179], v246 offset:38912\n"
    "v_exp_f32_e32 v230, v230\n"
    "v_mfma_f32_16x16x32_bf16 v[100:103], a[184:187], v[192:195], v[100:103]\n"
    "ds_read_b64_tr_b16 a[180:181], v246 offset:45056\n"
    "buffer_load_dwordx4 a[76:79], v253, s[8:11], s48 offen offset:0 sc1\n"
    "v_mfma_f32_16x16x32_bf16 v[100:103], a[188:191], v[196:199], v[100:103]\n"
    "ds_read_b64_tr_b16 a[182:183], v246 offset:47104\n"
    "ds_write_addtid_b32 v224 offset:3072\n"
    "v_mfma_f32_16x16x32_bf16 v[96:99], a[188:191], v[188:191], v[96:99]\n"
    "v_exp_f32_e32 v231, v231\n"
    "v_mfma_f32_16x16x32_bf16 v[96:99], a[184:187], v[184:187], v[96:99]\n"
    "buffer_load_dwordx4 a[108:111], v253, s[8:11], s48 offen offset:1024 sc1\n"
    "v_cvt_pk_bf16_f32 v226, v228, v229\n"
    "v_mfma_f32_16x16x32_bf16 v[92:95], a[184:187], v[176:179], v[92:95]\n"
    "s_add_u32 s48, s57, 8192\n"
    "ds_read_b64_tr_b16 a[184:185], v247 offset:36864\n"
    "ds_write_addtid_b32 v225 offset:3328\n"
    "v_mfma_f32_16x16x32_bf16 v[92:95], a[188:191], v[180:183], v[92:95]\n"
    "s_waitcnt lgkmcnt(13)\n"
    "ds_read_b64_tr_b16 a[186:187], v247 offset:38912\n"
    "v_exp_f32_e32 v220, v220\n"
    "v_mfma_f32_16x16x32_bf16 v[104:107], a[160:163], v[176:179], v[104:107]\n"
    "ds_read_b64_tr_b16 a[188:189], v247 offset:45056\n"
    "buffer_load_dwordx4 a[140:143], v253, s[8:11], s48 offen offset:0 sc1\n"
    "v_mfma_f32_16x16x32_bf16 v[104:107], a[164:167], v[180:183], v[104:107]\n"
    "ds_read_b64_tr_b16 a[190:191], v247 offset:47104\n"
    "ds_write_addtid_b32 v216 offset:2048\n"
    "v_mfma_f32_16x16x32_bf16 v[108:111], a[164:167], v[188:191], v[108:111]\n"
    "v_exp_f32_e32 v221, v221\n"
    "v_mfma_f32_16x16x32_bf16 v[108:111], a[160:163], v[184:187], v[108:111]\n"
    "buffer_load_dwordx4 a[80:83], v253, s[8:11], s48 offen offset:1024 sc1\n"
    "v_cvt_pk_bf16_f32 v227, v230, v231\n"
    "v_mfma_f32_16x16x32_bf16 v[112:115], a[160:163], v[192:195], v[112:115]\n"
    "s_add_u32 s48, s57, 26624\n"
    "ds_read_b64_tr_b16 a[160:161], v244 offset:49152\n"
    "v_exp_f32_e32 v222, v222\n"
    "v_mfma_f32_16x16x32_bf16 v[112:115], a[164:167], v[196:199], v[112:115]\n"
    "s_waitcnt lgkmcnt(13)\n"
    "ds_read_b64_tr_b16 a[162:163], v244 offset:51200\n"
    "v_exp_f32_e32 v223, v223\n"
    "v_mfma_f32_16x16x32_bf16 v[124:127], a[168:171], v[192:195], v[124:127]\n"
    "ds_read_b64_tr_b16 a[164:165], v244 offset:57344\n"
    "buffer_load_dwordx4 a[112:115], v253, s[8:11], s48 offen offset:0 sc1\n"
    "v_mfma_f32_16x16x32_bf16 v[124:127], a[172:175], v[196:199], v[124:127]\n"
    "ds_read_b64_tr_b16 a[166:167], v244 offset:59392\n"
    "ds_write_addtid_b32 v217 offset:2304\n"
    "v_mfma_f32_16x16x32_bf16 v[120:123], a[172:175], v[188:191], v[120:123]\n"
    "ds_write_addtid_b32 v234 offset:4608\n"
    "v_cvt_pk_bf16_f32 v218, v220, v221\n"
    "v_mfma_f32_16x16x32_bf16 v[120:123], a[168:171], v[184:187], v[120:123]\n"
    "buffer_load_dwordx4 a[144:147], v253, s[8:11], s48 offen offset:1024 sc1\n"
    "v_cvt_pk_bf16_f32 v219, v222, v223\n"
    "v_mfma_f32_16x16x32_bf16 v[116:119], a[168:171], v[176:179], v[116:119]\n"
    "s_add_u32 s48, s57, 10240\n"
    "ds_read_b64_tr_b16 a[168:169], v245 offset:49152\n"
    "ds_write_addtid_b32 v235 offset:4864\n"
    "v_mfma_f32_16x16x32_bf16 v[116:119], a[172:175], v[180:183], v[116:119]\n"
    "s_waitcnt lgkmcnt(15)\n"
    "ds_read_b64_tr_b16 a[170:171], v245 offset:51200\n"
    "ds_write_addtid_b32 v226 offset:3584\n"
    "v_mfma_f32_16x16x32_bf16 v[128:131], a[176:179], v[176:179], v[128:131]\n"
    "ds_read_b64_tr_b16 a[172:173], v245 offset:57344\n"
    "buffer_load_dwordx4 a[84:87], v253, s[8:11], s48 offen offset:0 sc1\n"
    "v_mfma_f32_16x16x32_bf16 v[128:131], a[180:183], v[180:183], v[128:131]\n"
    "ds_read_b64_tr_b16 a[174:175], v245 offset:59392\n"
    "ds_write_addtid_b32 v227 offset:3840\n"
    "v_mfma_f32_16x16x32_bf16 v[132:135], a[180:183], v[188:191], v[132:135]\n"
    "ds_write_addtid_b32 v218 offset:2560\n"
    "ds_write_addtid_b32 v219 offset:2816\n"
    "v_mfma_f32_16x16x32_bf16 v[132:135], a[176:179], v[184:187], v[132:135]\n"
    "buffer_load_dwordx4 a[116:119], v253, s[8:11], s48 offen offset:1024 sc1\n"
    "v_mfma_f32_16x16x32_bf16 v[136:139], a[176:179], v[192:195], v[136:139]\n"
    "s_add_u32 s48, s57, 28672\n"
    "ds_read_b64_tr_b16 a[176:177], v246 offset:49152\n"
    "v_mfma_f32_16x16x32_bf16 v[136:139], a[180:183], v[196:199], v[136:139]\n"
    "s_waitcnt lgkmcnt(15)\n"
    "ds_read_b64_tr_b16 a[178:179], v246 offset:51200\n"
    "v_mfma_f32_16x16x32_bf16 v[148:151], a[184:187], v[192:195], v[148:151]\n"
    "buffer_load_dwordx4 a[148:151], v253, s[8:11], s48 offen offset:0 sc1\n"
    "s_waitcnt lgkmcnt(2)\n"
    "s_barrier\n"
    "v_mfma_f32_16x16x32_bf16 v[148:151], a[188:191], v[196:199], v[148:151]\n"
    "ds_read_b64_tr_b16 a[180:181], v246 offset:57344\n"
    "v_mfma_f32_16x16x32_bf16 v[144:147], a[188:191], v[188:191], v[144:147]\n"
    "ds_read_b64_tr_b16 a[182:183], v246 offset:59392\n"
    "ds_read2st64_b32 v[220:221], v250 offset0:8 offset1:9\n"
    "v_mfma_f32_16x16x32_bf16 v[144:147], a[184:187], v[184:187], v[144:147]\n"
    "buffer_load_dwordx4 a[88:91], v253, s[8:11], s48 offen offset:1024 sc1\n"
    "ds_read2st64_b32 v[222:223], v250 offset0:10 offset1:11\n"
    "v_mfma_f32_16x16x32_bf16 v[140:143], a[184:187], v[176:179], v[140:143]\n"
    "s_add_u32 s48, s57, 12288\n"
    "ds_read_b64_tr_b16 a[184:185], v247 offset:49152\n"
    "ds_read2st64_b32 v[228:229], v250 offset0:12 offset1:13\n"
    "v_mfma_f32_16x16x32_bf16 v[140:143], a[188:191], v[180:183], v[140:143]\n"
    "ds_read_b64_tr_b16 a[186:187], v247 offset:51200\n"
    "ds_read2st64_b32 v[230:231], v250 offset0:14 offset1:15\n"
    "v_mfma_f32_16x16x32_bf16 v[56:59], a[160:163], v[216:219], v[56:59]\n"
    "ds_read_b64_tr_b16 a[188:189], v247 offset:57344\n"
    "buffer_load_dwordx4 a[120:123], v253, s[8:11], s48 offen offset:0 sc1\n"
    "v_mfma_f32_16x16x32_bf16 v[60:63], a[160:163], v[224:227], v[60:63]\n"
    "ds_read_b64_tr_b16 a[190:191], v247 offset:59392\n"
    "ds_read2st64_b32 v[236:237], v250 offset0:16 offset1:17\n"
    "v_mfma_f32_16x16x32_bf16 v[64:67], a[160:163], v[232:235], v[64:67]\n"
    "ds_read_b64_tr_b16 a[160:161], v244 offset:53248\n"
    "ds_read2st64_b32 v[238:239], v250 offset0:18 offset1:19\n"
    "v_mfma_f32_16x16x32_bf16 v[76:79], a[168:171], v[232:235], v[76:79]\n"
    "ds_read_b64_tr_b16 a[162:163], v244 offset:55296\n"
    "buffer_load_dwordx4 a[152:155], v253, s[8:11], s48 offen offset:1024 sc1\n"
    "v_mfma_f32_16x16x32_bf16 v[72:75], a[168:171], v[224:227], v[72:75]\n"
    "v_lshrrev_b32_e32 v158, 4, v253\n"
    "v_mfma_f32_16x16x32_bf16 v[68:71], a[168:171], v[216:219], v[68:71]\n"
    "s_waitcnt lgkmcnt(1)\n"
    "v_lshrrev_b32_e32 v159, 4, v158\n"
    "v_mfma_f32_16x16x32_bf16 v[56:59], a[164:167], v[220:223], v[56:59]\n"
    "v_bfe_u32 v255, v158, 2, 2\n"
    "v_and_b32_e32 v158, 3, v158\n"
    "v_mfma_f32_16x16x32_bf16 v[60:63], a[164:167], v[228:231], v[60:63]\n"
    "v_xor_b32_e32 v159, v159, v255\n"
    "v_lshl_or_b32 v158, v159, 2, v158\n"
    "v_mfma_f32_16x16x32_bf16 v[64:67], a[164:167], v[236:239], v[64:67]\n"
    "ds_read_b64_tr_b16 a[164:165], v244 offset:61440\n"
    "v_mov_b32_e32 v159, 0x3f80\n"
    "v_mfma_f32_16x16x32_bf16 v[76:79], a[172:175], v[236:239], v[76:79]\n"
    "ds_read_b64_tr_b16 a[166:167], v244 offset:63488\n"
    "v_mov_b32_e32 v152, 0\n"
    "v_mfma_f32_16x16x32_bf16 v[72:75], a[172:175], v[228:231], v[72:75]\n"
    "ds_read_b64_tr_b16 a[168:169], v245 offset:53248\n"
    "v_mov_b32_e32 v153, 0\n"
    "v_mfma_f32_16x16x32_bf16 v[68:71], a[172:175], v[220:223], v[68:71]\n"
    "ds_read_b64_tr_b16 a[170:171], v245 offset:55296\n"
    "v_cvt_pk_bf16_f32 v64, v64, v65\n"
    "v_mfma_f32_16x16x32_bf16 v[80:83], a[176:179], v[216:219], v[80:83]\n"
    "ds_read_b64_tr_b16 a[172:173], v245 offset:61440\n"
    "v_cvt_pk_bf16_f32 v65, v66, v67\n"
    "v_mfma_f32_16x16x32_bf16 v[80:83], a[180:183], v[220:223], v[80:83]\n"
    "ds_read_b64_tr_b16 a[174:175], v245 offset:63488\n"
    "v_mov_b32_e32 v156, 0\n"
    "v_mfma_f32_16x16x32_bf16 v[84:87], a[180:183], v[228:231], v[84:87]\n"
    "v_cvt_pk_bf16_f32 v66, v76, v77\n"
    "v_mov_b32_e32 v157, 0\n"
    "v_mfma_f32_16x16x32_bf16 v[84:87], a[176:179], v[224:227], v[84:87]\n"
    "v_cvt_pk_bf16_f32 v67, v78, v79\n"
    "v_cmp_eq_u32_e32 vcc, 0, v158\n"
    "v_mfma_f32_16x16x32_bf16 v[88:91], a[176:179], v[232:235], v[88:91]\n"
    "ds_read_b64_tr_b16 a[176:177], v246 offset:53248\n"
    "buffer_store_dwordx4 v[64:67], v253, s[8:11], s86 offen offset:0 sc1\n"
    "v_cvt_pk_bf16_f32 v60, v60, v61\n"
    "v_cndmask_b32_e32 v154, 0, v159, vcc\n"
    "v_mfma_f32_16x16x32_bf16 v[88:91], a[180:183], v[236:239], v[88:91]\n"
    "ds_read_b64_tr_b16 a[178:179], v246 offset:55296\n"
    "v_cmp_eq_u32_e32 vcc, 1, v158\n"
    "v_mfma_f32_16x16x32_bf16 v[100:103], a[184:187], v[232:235], v[100:103]\n"
    "ds_read_b64_tr_b16 a[180:181], v246 offset:61440\n"
    "v_cndmask_b32_e64 v154, v154, 1.0, vcc\n"
    "v_mfma_f32_16x16x32_bf16 v[100:103], a[188:191], v[236:239], v[100:103]\n"
    "ds_read_b64_tr_b16 a[182:183], v246 offset:63488\n"
    "v_cmp_eq_u32_e32 vcc, 2, v158\n"
    "v_mfma_f32_16x16x32_bf16 v[96:99], a[188:191], v[228:231], v[96:99]\n"
    "v_cvt_pk_bf16_f32 v61, v62, v63\n"
    "v_cndmask_b32_e32 v155, 0, v159, vcc\n"
    "v_mfma_f32_16x16x32_bf16 v[96:99], a[184:187], v[224:227], v[96:99]\n"
    "v_cvt_pk_bf16_f32 v62, v72, v73\n"
    "v_cmp_eq_u32_e32 vcc, 3, v158\n"
    "v_mfma_f32_16x16x32_bf16 v[92:95], a[184:187], v[216:219], v[92:95]\n"
    "ds_read_b64_tr_b16 a[184:185], v247 offset:53248\n"
    "v_cndmask_b32_e64 v155, v155, 1.0, vcc\n"
    "v_mfma_f32_16x16x32_bf16 v[92:95], a[188:191], v[220:223], v[92:95]\n"
    "s_waitcnt lgkmcnt(9)\n"
    "ds_read_b64_tr_b16 a[186:187], v247 offset:55296\n"
    "v_and_b32_e32 v251, 0xf0, v253\n"
    "v_mfma_f32_16x16x32_bf16 v[104:107], a[160:163], v[216:219], v[104:107]\n"
    "ds_read_b64_tr_b16 a[188:189], v247 offset:61440\n"
    "v_mov_b32_e32 v254, 2.0\n"
    "v_mfma_f32_16x16x32_bf16 v[104:107], a[164:167], v[220:223], v[104:107]\n"
    "ds_read_b64_tr_b16 a[190:191], v247 offset:63488\n"
    "v_cvt_pk_bf16_f32 v63, v74, v75\n"
    "v_mfma_f32_16x16x32_bf16 v[108:111], a[164:167], v[228:231], v[108:111]\n"
    "buffer_store_dwordx4 v[60:63], v253, s[8:11], s85 offen offset:0 sc1\n"
    "v_cmp_le_u32_e32 vcc, 0x100, v253\n"
    "v_mov_b32_e32 v255, s29\n"
    "v_mfma_f32_16x16x32_bf16 v[108:111], a[160:163], v[224:227], v[108:111]\n"
    "v_cvt_pk_bf16_f32 v56, v56, v57\n"
    "v_mfma_f32_16x16x32_bf16 v[112:115], a[160:163], v[232:235], v[112:115]\n"
    "ds_read_b128 a[160:163], v243 offset:36864\n"
    "v_mfma_f32_16x16x32_bf16 v[112:115], a[164:167], v[236:239], v[112:115]\n"
    "s_waitcnt lgkmcnt(9)\n"
    "ds_read_b128 a[164:167], v240 offset:32768\n"
    "v_mfma_f32_16x16x32_bf16 v[124:127], a[168:171], v[232:235], v[124:127]\n"
    "v_cvt_pk_bf16_f32 v57, v58, v59\n"
    "v_cndmask_b32_e32 v254, v254, v255, vcc\n"
    "v_mfma_f32_16x16x32_bf16 v[124:127], a[172:175], v[236:239], v[124:127]\n"
    "v_cmp_le_u32_e32 vcc, 0x200, v253\n"
    "v_mov_b32_e32 v255, s30\n"
    "v_mfma_f32_16x16x32_bf16 v[120:123], a[172:175], v[228:231], v[120:123]\n"
    "v_cvt_pk_bf16_f32 v58, v68, v69\n"
    "v_mfma_f32_16x16x32_bf16 v[120:123], a[168:171], v[224:227], v[120:123]\n"
    "v_cvt_pk_bf16_f32 v59, v70, v71\n"
    "v_mfma_f32_16x16x32_bf16 v[116:119], a[168:171], v[216:219], v[116:119]\n"
    "ds_read_b128 a[168:171], v241 offset:32768\n"
    "v_mfma_f32_16x16x32_bf16 v[116:119], a[172:175], v[220:223], v[116:119]\n"
    "s_waitcnt lgkmcnt(7)\n"
    "ds_read_b128 a[172:175], v240 offset:36864\n"
    "v_cndmask_b32_e32 v254, v254, v255, vcc\n"
    "v_mfma_f32_16x16x32_bf16 v[128:131], a[176:179], v[216:219], v[128:131]\n"
    "buffer_store_dwordx4 v[56:59], v253, s[8:11], s84 offen offset:0 sc1\n"
    "v_cmp_le_u32_e32 vcc, 0x300, v253\n"
    "v_mov_b32_e32 v255, s31\n"
    "v_mfma_f32_16x16x32_bf16 v[128:131], a[180:183], v[220:223], v[128:131]\n"
    "s_cmp_eq_u32 s51, 0\n"
    "s_cselect_b32 s60, 2.0, 24576\n"
    "v_cndmask_b32_e32 v254, v254, v255, vcc\n"
    "v_mfma_f32_16x16x32_bf16 v[132:135], a[180:183], v[228:231], v[132:135]\n"
    "v_cvt_pk_bf16_f32 v88, v88, v89\n"
    "v_add_u32_e32 v251, v251, v254\n"
    "v_mfma_f32_16x16x32_bf16 v[132:135], a[176:179], v[224:227], v[132:135]\n"
    "s_lshl_b32 s45, s51, 16\n"
    "s_add_u32 s45, s45, %3\n"
    "s_add_u32 s45, s45, 49152\n"
    "v_mfma_f32_16x16x32_bf16 v[136:139], a[176:179], v[232:235], v[136:139]\n"
    "ds_read_b128 a[176:179], v241 offset:36864\n"
    "v_mfma_f32_16x16x32_bf16 v[136:139], a[180:183], v[236:239], v[136:139]\n"
    "s_waitcnt lgkmcnt(5)\n"
    "ds_read_b128 a[180:183], v242 offset:32768\n"
    "v_mfma_f32_16x16x32_bf16 v[148:151], a[184:187], v[232:235], v[148:151]\n"
    "v_cvt_pk_bf16_f32 v89, v90, v91\n"
    "v_mfma_f32_16x16x32_bf16 v[148:151], a[188:191], v[236:239], v[148:151]\n"
    "s_lshl_b32 s46, s50, 12\n"
    "s_add_u32 s46, s46, s45\n"
    "v_add_u32_e32 v158, s46, v253\n"
    "v_mfma_f32_16x16x32_bf16 v[144:147], a[188:191], v[228:231], v[144:147]\n"
    "v_cvt_pk_bf16_f32 v90, v100, v101\n"
    "v_mfma_f32_16x16x32_bf16 v[144:147], a[184:187], v[224:227], v[144:147]\n"
    "v_cvt_pk_bf16_f32 v91, v102, v103\n"
    "v_mfma_f32_16x16x32_bf16 v[140:143], a[184:187], v[216:219], v[140:143]\n"
    "ds_read_b128 a[184:187], v243 offset:32768\n"
    "v_mfma_f32_16x16x32_bf16 v[140:143], a[188:191], v[220:223], v[140:143]\n"
    "s_waitcnt lgkmcnt(3)\n"
    "ds_read_b128 a[188:191], v242 offset:36864\n"
    "v_mfma_f32_16x16x32_bf16 v[160:163], a[160:163], a[28:31], 0\n"
    "buffer_store_dwordx4 v[88:91], v253, s[8:11], s86 offen offset:1024 sc1\n"
    "v_mfma_f32_16x16x32_bf16 v[160:163], a[164:167], a[0:3], v[160:163]\n"
    "v_cvt_pk_bf16_f32 v84, v84, v85\n"
    "v_mfma_f32_16x16x32_bf16 v[168:171], a[164:167], a[32:35], 0\n"
    "v_cvt_pk_bf16_f32 v85, v86, v87\n"
    "v_mfma_f32_16x16x32_bf16 v[168:171], a[160:163], a[60:63], v[168:171]\n"
    "ds_read_b128 a[160:163], v243 offset:38912\n"
    "v_mfma_f32_16x16x32_bf16 v[168:171], a[168:171], a[36:39], v[168:171]\n"
    "ds_read_b128 a[164:167], v240 offset:34816\n"
    "v_mfma_f32_16x16x32_bf16 v[168:171], a[172:175], a[40:43], v[168:171]\n"
    "v_cvt_pk_bf16_f32 v86, v96, v97\n"
    "v_mfma_f32_16x16x32_bf16 v[160:163], a[172:175], a[8:11], v[160:163]\n"
    "v_cvt_pk_bf16_f32 v87, v98, v99\n"
    "v_mfma_f32_16x16x32_bf16 v[160:163], a[168:171], a[4:7], v[160:163]\n"
    "s_waitcnt lgkmcnt(2)\n"
    "ds_read_b128 a[168:171], v241 offset:34816\n"
    "v_mfma_f32_16x16x32_bf16 v[160:163], a[176:179], a[12:15], v[160:163]\n"
    "ds_read_b128 a[172:175], v240 offset:38912\n"
    "v_mfma_f32_16x16x32_bf16 v[160:163], a[180:183], a[16:19], v[160:163]\n"
    "buffer_store_dwordx4 v[84:87], v253, s[8:11], s85 offen offset:1024 sc1\n"
    "v_cvt_pk_bf16_f32 v80, v80, v81\n"
    "v_mfma_f32_16x16x32_bf16 v[168:171], a[180:183], a[48:51], v[168:171]\n"
    "v_cvt_pk_bf16_f32 v81, v82, v83\n"
    "v_mfma_f32_16x16x32_bf16 v[168:171], a[176:179], a[44:47], v[168:171]\n"
    "ds_read_b128 a[176:179], v241 offset:38912\n"
    "v_mfma_f32_16x16x32_bf16 v[168:171], a[184:187], a[52:55], v[168:171]\n"
    "ds_read_b128 a[180:183], v242 offset:34816\n"
    "v_mfma_f32_16x16x32_bf16 v[168:171], a[188:191], a[56:59], v[168:171]\n"
    "v_cvt_pk_bf16_f32 v82, v92, v93\n"
    "v_mfma_f32_16x16x32_bf16 v[160:163], a[188:191], a[24:27], v[160:163]\n"
    "v_cvt_pk_bf16_f32 v83, v94, v95\n"
    "v_mfma_f32_16x16x32_bf16 v[160:163], a[184:187], a[20:23], v[160:163]\n"
    "s_waitcnt lgkmcnt(2)\n"
    "ds_read_b128 a[184:187], v243 offset:34816\n"
    "v_mfma_f32_16x16x32_bf16 v[164:167], a[160:163], a[28:31], 0\n"
    "ds_read_b128 a[188:191], v242 offset:38912\n"
    "v_mfma_f32_16x16x32_bf16 v[164:167], a[164:167], a[0:3], v[164:167]\n"
    "v_exp_f32_e32 v168, v168\n"
    "v_mfma_f32_16x16x32_bf16 v[172:175], a[164:167], a[32:35], 0\n"
    "v_exp_f32_e32 v169, v169\n"
    "v_mfma_f32_16x16x32_bf16 v[172:175], a[160:163], a[60:63], v[172:175]\n"
    "ds_read_b64_tr_b16 a[160:161], v244 offset:16384\n"
    "v_mfma_f32_16x16x32_bf16 v[172:175], a[168:171], a[36:39], v[172:175]\n"
    "ds_read_b64_tr_b16 a[162:163], v244 offset:18432\n"
    "buffer_store_dwordx4 v[80:83], v253, s[8:11], s84 offen offset:1024 sc1\n"
    "v_exp_f32_e32 v170, v170\n"
    "v_mfma_f32_16x16x32_bf16 v[172:175], a[172:175], a[40:43], v[172:175]\n"
    "ds_read_b64_tr_b16 a[164:165], v244 offset:24576\n"
    "v_cvt_pk_bf16_f32 v112, v112, v113\n"
    "v_mfma_f32_16x16x32_bf16 v[164:167], a[172:175], a[8:11], v[164:167]\n"
    "ds_read_b64_tr_b16 a[166:167], v244 offset:26624\n"
    "v_exp_f32_e32 v171, v171\n"
    "v_mfma_f32_16x16x32_bf16 v[164:167], a[168:171], a[4:7], v[164:167]\n"
    "s_waitcnt lgkmcnt(4)\n"
    "ds_read_b64_tr_b16 a[168:169], v245 offset:16384\n"
    "v_cvt_pk_bf16_f32 v113, v114, v115\n"
    "v_mfma_f32_16x16x32_bf16 v[164:167], a[176:179], a[12:15], v[164:167]\n"
    "ds_read_b64_tr_b16 a[170:171], v245 offset:18432\n"
    "v_mfma_f32_16x16x32_bf16 v[164:167], a[180:183], a[16:19], v[164:167]\n"
    "ds_read_b64_tr_b16 a[172:173], v245 offset:24576\n"
    "v_cvt_pk_bf16_f32 v168, v168, v169\n"
    "v_mfma_f32_16x16x32_bf16 v[172:175], a[180:183], a[48:51], v[172:175]\n"
    "ds_read_b64_tr_b16 a[174:175], v245 offset:26624\n"
    "v_cvt_pk_bf16_f32 v114, v124, v125\n"
    "v_mfma_f32_16x16x32_bf16 v[172:175], a[176:179], a[44:47], v[172:175]\n"
    "ds_read_b64_tr_b16 a[176:177], v246 offset:16384\n"
    "v_exp_f32_e32 v160, v160\n"
    "v_mfma_f32_16x16x32_bf16 v[172:175], a[184:187], a[52:55], v[172:175]\n"
    "ds_read_b64_tr_b16 a[178:179], v246 offset:18432\n"
    "v_cvt_pk_bf16_f32 v115, v126, v127\n"
    "v_mfma_f32_16x16x32_bf16 v[172:175], a[188:191], a[56:59], v[172:175]\n"
    "ds_read_b64_tr_b16 a[180:181], v246 offset:24576\n"
    "buffer_store_dwordx4 v[112:115], v253, s[8:11], s86 offen offset:2048 sc1\n"
    "v_exp_f32_e32 v161, v161\n"
    "v_mfma_f32_16x16x32_bf16 v[164:167], a[188:191], a[24:27], v[164:167]\n"
    "ds_read_b64_tr_b16 a[182:183], v246 offset:26624\n"
    "v_cvt_pk_bf16_f32 v108, v108, v109\n"
    "v_mfma_f32_16x16x32_bf16 v[164:167], a[184:187], a[20:23], v[164:167]\n"
    "s_waitcnt lgkmcnt(4)\n"
    "ds_read_b64_tr_b16 a[184:185], v247 offset:16384\n"
    "v_cvt_pk_bf16_f32 v169, v170, v171\n"
    "v_mfma_f32_16x16x32_bf16 a[248:251], a[160:163], v[200:203], a[248:251]\n"
    "ds_read_b64_tr_b16 a[186:187], v247 offset:18432\n"
    "v_cvt_pk_bf16_f32 v109, v110, v111\n"
    "v_mfma_f32_16x16x32_bf16 a[248:251], a[164:167], v[204:207], a[248:251]\n"
    "ds_read_b64_tr_b16 a[188:189], v247 offset:24576\n"
    "v_exp_f32_e32 v162, v162\n"
    "v_mfma_f32_16x16x32_bf16 a[252:255], a[164:167], v[212:215], a[252:255]\n"
    "ds_read_b64_tr_b16 a[190:191], v247 offset:26624\n"
    "v_cvt_pk_bf16_f32 v110, v120, v121\n"
    "v_mfma_f32_16x16x32_bf16 a[252:255], a[160:163], v[208:211], a[252:255]\n"
    "ds_read_b64_tr_b16 a[160:161], v244 offset:20480\n"
    "v_exp_f32_e32 v163, v163\n"
    "v_mfma_f32_16x16x32_bf16 v[4:7], a[168:171], v[208:211], v[4:7]\n"
    "ds_read_b64_tr_b16 a[162:163], v244 offset:22528\n"
    "v_cvt_pk_bf16_f32 v111, v122, v123\n"
    "v_mfma_f32_16x16x32_bf16 v[4:7], a[172:175], v[212:215], v[4:7]\n"
    "ds_read_b64_tr_b16 a[164:165], v244 offset:28672\n"
    "v_cvt_pk_bf16_f32 v160, v160, v161\n"
    "v_mfma_f32_16x16x32_bf16 v[0:3], a[172:175], v[204:207], v[0:3]\n"
    "ds_read_b64_tr_b16 a[166:167], v244 offset:30720\n"
    "ds_write_addtid_b32 v168 offset:1024\n"
    "v_mfma_f32_16x16x32_bf16 v[0:3], a[168:171], v[200:203], v[0:3]\n"
    "s_waitcnt lgkmcnt(5)\n"
    "ds_read_b64_tr_b16 a[168:169], v245 offset:20480\n"
    "v_cvt_pk_bf16_f32 v161, v162, v163\n"
    "v_mfma_f32_16x16x32_bf16 v[8:11], a[176:179], v[200:203], v[8:11]\n"
    "ds_read_b64_tr_b16 a[170:171], v245 offset:22528\n"
    "buffer_store_dwordx4 v[108:111], v253, s[8:11], s85 offen offset:2048 sc1\n"
    "v_cvt_pk_bf16_f32 v104, v104, v105\n"
    "v_mfma_f32_16x16x32_bf16 v[8:11], a[180:183], v[204:207], v[8:11]\n"
    "ds_read_b64_tr_b16 a[172:173], v245 offset:28672\n"
    "v_cvt_pk_bf16_f32 v105, v106, v107\n"
    "v_mfma_f32_16x16x32_bf16 v[12:15], a[180:183], v[212:215], v[12:15]\n"
    "ds_read_b64_tr_b16 a[174:175], v245 offset:30720\n"
    "v_exp_f32_e32 v172, v172\n"
    "v_mfma_f32_16x16x32_bf16 v[12:15], a[176:179], v[208:211], v[12:15]\n"
    "ds_read_b64_tr_b16 a[176:177], v246 offset:20480\n"
    "ds_write_addtid_b32 v169 offset:1280\n"
    "v_mfma_f32_16x16x32_bf16 v[20:23], a[184:187], v[208:211], v[20:23]\n"
    "ds_read_b64_tr_b16 a[178:179], v246 offset:22528\n"
    "v_exp_f32_e32 v173, v173\n"
    "v_mfma_f32_16x16x32_bf16 v[20:23], a[188:191], v[212:215], v[20:23]\n"
    "ds_read_b64_tr_b16 a[180:181], v246 offset:28672\n"
    "v_cvt_pk_bf16_f32 v106, v116, v117\n"
    "v_mfma_f32_16x16x32_bf16 v[16:19], a[188:191], v[204:207], v[16:19]\n"
    "ds_read_b64_tr_b16 a[182:183], v246 offset:30720\n"
    "v_cvt_pk_bf16_f32 v107, v118, v119\n"
    "v_mfma_f32_16x16x32_bf16 v[16:19], a[184:187], v[200:203], v[16:19]\n"
    "s_waitcnt lgkmcnt(5)\n"
    "ds_read_b64_tr_b16 a[184:185], v247 offset:20480\n"
    "ds_write_addtid_b32 v160 offset:0\n"
    "v_mfma_f32_16x16x32_bf16 v[24:27], a[160:163], v[200:203], v[24:27]\n"
    "ds_read_b64_tr_b16 a[186:187], v247 offset:22528\n"
    "buffer_store_dwordx4 v[104:107], v253, s[8:11], s84 offen offset:2048 sc1\n"
    "v_cvt_pk_bf16_f32 v136, v136, v137\n"
    "v_mfma_f32_16x16x32_bf16 v[24:27], a[164:167], v[204:207], v[24:27]\n"
    "ds_read_b64_tr_b16 a[188:189], v247 offset:28672\n"
    "v_mfma_f32_16x16x32_bf16 v[28:31], a[164:167], v[212:215], v[28:31]\n"
    "ds_read_b64_tr_b16 a[190:191], v247 offset:30720\n"
    "v_cvt_pk_bf16_f32 v137, v138, v139\n"
    "v_mfma_f32_16x16x32_bf16 v[28:31], a[160:163], v[208:211], v[28:31]\n"
    "ds_read_b128 a[160:163], v243 offset:53248\n"
    "ds_write_addtid_b32 v161 offset:256\n"
    "v_mfma_f32_16x16x32_bf16 v[36:39], a[168:171], v[208:211], v[36:39]\n"
    "ds_read_b128 a[164:167], v240 offset:49152\n"
    "v_cvt_pk_bf16_f32 v138, v148, v149\n"
    "v_mfma_f32_16x16x32_bf16 v[36:39], a[172:175], v[212:215], v[36:39]\n"
    "v_exp_f32_e32 v174, v174\n"
    "v_mfma_f32_16x16x32_bf16 v[32:35], a[172:175], v[204:207], v[32:35]\n"
    "v_exp_f32_e32 v175, v175\n"
    "v_mfma_f32_16x16x32_bf16 v[32:35], a[168:171], v[200:203], v[32:35]\n"
    "s_waitcnt lgkmcnt(3)\n"
    "ds_read_b128 a[168:171], v241 offset:49152\n"
    "v_cvt_pk_bf16_f32 v170, v172, v173\n"
    "v_mfma_f32_16x16x32_bf16 v[40:43], a[176:179], v[200:203], v[40:43]\n"
    "ds_read_b128 a[172:175], v240 offset:53248\n"
    "v_cvt_pk_bf16_f32 v139, v150, v151\n"
    "v_mfma_f32_16x16x32_bf16 v[40:43], a[180:183], v[204:207], v[40:43]\n"
    "v_exp_f32_e32 v164, v164\n"
    "v_mfma_f32_16x16x32_bf16 v[44:47], a[180:183], v[212:215], v[44:47]\n"
    "buffer_store_dwordx4 v[136:139], v253, s[8:11], s86 offen offset:3072 sc1\n"
    "v_exp_f32_e32 v165, v165\n"
    "v_mfma_f32_16x16x32_bf16 v[44:47], a[176:179], v[208:211], v[44:47]\n"
    "ds_read_b128 a[176:179], v241 offset:53248\n"
    "v_cvt_pk_bf16_f32 v171, v174, v175\n"
    "v_mfma_f32_16x16x32_bf16 v[52:55], a[184:187], v[208:211], v[52:55]\n"
    "ds_read_b128 a[180:183], v242 offset:49152\n"
    "v_cvt_pk_bf16_f32 v132, v132, v133\n"
    "v_mfma_f32_16x16x32_bf16 v[52:55], a[188:191], v[212:215], v[52:55]\n"
    "v_exp_f32_e32 v166, v166\n"
    "v_mfma_f32_16x16x32_bf16 v[48:51], a[188:191], v[204:207], v[48:51]\n"
    "v_exp_f32_e32 v167, v167\n"
    "v_mfma_f32_16x16x32_bf16 v[48:51], a[184:187], v[200:203], v[48:51]\n"
    "s_waitcnt lgkmcnt(2)\n"
    "ds_read_b128 a[184:187], v243 offset:49152\n"
    "v_cvt_pk_bf16_f32 v162, v164, v165\n"
    "v_mfma_f32_16x16x32_bf16 v[200:203], a[160:163], a[28:31], 0\n"
    "ds_read_b128 a[188:191], v242 offset:53248\n"
    "v_cvt_pk_bf16_f32 v133, v134, v135\n"
    "v_mfma_f32_16x16x32_bf16 v[200:203], a[164:167], a[0:3], v[200:203]\n"
    "ds_write_addtid_b32 v170 offset:1536\n"
    "v_cvt_pk_bf16_f32 v163, v166, v167\n"
    "v_mfma_f32_16x16x32_bf16 v[208:211], a[164:167], a[32:35], 0\n"
    "ds_write_addtid_b32 v171 offset:1792\n"
    "v_cvt_pk_bf16_f32 v134, v144, v145\n"
    "v_mfma_f32_16x16x32_bf16 v[208:211], a[160:163], a[60:63], v[208:211]\n"
    "ds_read_b128 a[160:163], v243 offset:55296\n"
    "v_cvt_pk_bf16_f32 v135, v146, v147\n"
    "v_mfma_f32_16x16x32_bf16 v[208:211], a[168:171], a[36:39], v[208:211]\n"
    "ds_read_b128 a[164:167], v240 offset:51200\n"
    "buffer_store_dwordx4 v[132:135], v253, s[8:11], s85 offen offset:3072 sc1\n"
    "v_cvt_pk_bf16_f32 v128, v128, v129\n"
    "v_mfma_f32_16x16x32_bf16 v[208:211], a[172:175], a[40:43], v[208:211]\n"
    "s_lshl_b32 s46, s89, 12\n"
    "s_add_u32 s46, s46, s45\n"
    "ds_write_addtid_b32 v162 offset:512\n"
    "v_add_u32_e32 v159, s46, v253\n"
    "v_mfma_f32_16x16x32_bf16 v[200:203], a[172:175], a[8:11], v[200:203]\n"
    "s_sub_u32 s45, s45, 4096\n"
    "ds_write_addtid_b32 v163 offset:768\n"
    "v_lshrrev_b32_e32 v255, 2, v253\n"
    "v_mfma_f32_16x16x32_bf16 v[200:203], a[168:171], a[4:7], v[200:203]\n"
    "s_waitcnt lgkmcnt(6)\n"
    "ds_read_b128 a[168:171], v241 offset:51200\n"
    "v_add_u32_e32 v255, s45, v255\n"
    "v_mfma_f32_16x16x32_bf16 v[200:203], a[176:179], a[12:15], v[200:203]\n"
    "ds_read_b128 a[172:175], v240 offset:55296\n"
    "v_mfma_f32_16x16x32_bf16 v[200:203], a[180:183], a[16:19], v[200:203]\n"
    "v_cvt_pk_bf16_f32 v129, v130, v131\n"
    "v_mfma_f32_16x16x32_bf16 v[208:211], a[180:183], a[48:51], v[208:211]\n"
    "s_waitcnt lgkmcnt(2)\n"
    "s_barrier\n"
    "v_mfma_f32_16x16x32_bf16 v[208:211], a[176:179], a[44:47], v[208:211]\n"
    "s_lshl_b32 s45, s89, 8\n"
    "ds_read_b128 a[176:179], v241 offset:55296\n"
    "v_add_u32_e32 v254, s45, v255\n"
    "v_mfma_f32_16x16x32_bf16 v[208:211], a[184:187], a[52:55], v[208:211]\n"
    "ds_read_b128 a[180:183], v242 offset:51200\n"
    "v_cvt_pk_bf16_f32 v130, v140, v141\n"
    "v_mfma_f32_16x16x32_bf16 v[208:211], a[188:191], a[56:59], v[208:211]\n"
    "ds_read2st64_b32 v[164:165], v250 offset0:0 offset1:1\n"
    "v_mfma_f32_16x16x32_bf16 v[200:203], a[188:191], a[24:27], v[200:203]\n"
    "s_lshl_b32 s45, s50, 8\n"
    "ds_read2st64_b32 v[166:167], v250 offset0:2 offset1:3\n"
    "v_add_u32_e32 v255, s45, v255\n"
    "v_mfma_f32_16x16x32_bf16 v[200:203], a[184:187], a[20:23], v[200:203]\n"
    "s_waitcnt lgkmcnt(4)\n"
    "ds_read_b128 a[184:187], v243 offset:51200\n"
    "v_cvt_pk_bf16_f32 v131, v142, v143\n"
    "v_mfma_f32_16x16x32_bf16 v[204:207], a[160:163], a[28:31], 0\n"
    "ds_read_b128 a[188:191], v242 offset:55296\n"
    "buffer_store_dwordx4 v[128:131], v253, s[8:11], s84 offen offset:3072 sc1\n"
    "v_exp_f32_e32 v208, v208\n"
    "v_permlane16_swap_b32_e32 v147, v151\n"
    "v_mfma_f32_16x16x32_bf16 v[204:207], a[164:167], a[0:3], v[204:207]\n"
    "ds_read2st64_b32 v[172:173], v250 offset0:4 offset1:5\n"
    "v_exp_f32_e32 v209, v209\n"
    "v_mfma_f32_16x16x32_bf16 v[212:215], a[164:167], a[32:35], 0\n"
    "ds_read2st64_b32 v[174:175], v250 offset0:6 offset1:7\n"
    "v_mov_b32_e32 v148, s39\n"
    "v_mfma_f32_16x16x32_bf16 v[212:215], a[160:163], a[60:63], v[212:215]\n"
    "ds_read_b64_tr_b16 a[160:161], v244 offset:32768\n"
    "v_exp_f32_e32 v210, v210\n"
    "v_mfma_f32_16x16x32_bf16 v[212:215], a[168:171], a[36:39], v[212:215]\n"
    "ds_read_b64_tr_b16 a[162:163], v244 offset:34816\n"
    "v_mov_b32_e32 v149, 0\n"
    "v_permlane32_swap_b32_e32 v143, v151\n"
    "v_mfma_f32_16x16x32_bf16 v[212:215], a[172:175], a[40:43], v[212:215]\n"
    "ds_read_b64_tr_b16 a[164:165], v244 offset:40960\n"
    "buffer_store_dword v151, v251, s[8:11], s60 offen offset:0 sc1\n"
    "v_exp_f32_e32 v211, v211\n"
    "v_mfma_f32_16x16x32_bf16 v[204:207], a[172:175], a[8:11], v[204:207]\n"
    "ds_read_b64_tr_b16 a[166:167], v244 offset:43008\n"
    "v_cvt_pk_bf16_f32 v208, v208, v209\n"
    "v_mfma_f32_16x16x32_bf16 v[204:207], a[168:171], a[4:7], v[204:207]\n"
    "s_waitcnt lgkmcnt(6)\n"
    "ds_read_b64_tr_b16 a[168:169], v245 offset:32768\n"
    "v_lshrrev_b32_e32 v147, 4, v253\n"
    "v_mfma_f32_16x16x32_bf16 v[204:207], a[176:179], a[12:15], v[204:207]\n"
    "ds_read_b64_tr_b16 a[170:171], v245 offset:34816\n"
    "v_exp_f32_e32 v200, v200\n"
    "v_mfma_f32_16x16x32_bf16 v[204:207], a[180:183], a[16:19], v[204:207]\n"
    "ds_read_b64_tr_b16 a[172:173], v245 offset:40960\n"
    "v_bfe_u32 v145, v147, 2, 2\n"
    "v_mfma_f32_16x16x32_bf16 v[212:215], a[180:183], a[48:51], v[212:215]\n"
    "ds_read_b64_tr_b16 a[174:175], v245 offset:43008\n"
    "v_exp_f32_e32 v201, v201\n"
    "v_mfma_f32_16x16x32_bf16 v[212:215], a[176:179], a[44:47], v[212:215]\n"
    "ds_read_b64_tr_b16 a[176:177], v246 offset:32768\n"
    "v_cvt_pk_bf16_f32 v209, v210, v211\n"
    "v_mfma_f32_16x16x32_bf16 v[212:215], a[184:187], a[52:55], v[212:215]\n"
    "ds_read_b64_tr_b16 a[178:179], v246 offset:34816\n"
    "v_min_u32_e32 v145, 2, v145\n"
    "v_mfma_f32_16x16x32_bf16 v[212:215], a[188:191], a[56:59], v[212:215]\n"
    "ds_read_b64_tr_b16 a[180:181], v246 offset:40960\n"
    "v_exp_f32_e32 v202, v202\n"
    "v_mfma_f32_16x16x32_bf16 v[204:207], a[188:191], a[24:27], v[204:207]\n"
    "s_add_u32 s46, s32, 1\n"
    "ds_read_b64_tr_b16 a[182:183], v246 offset:43008\n"
    "v_exp_f32_e32 v203, v203\n"
    "v_mfma_f32_16x16x32_bf16 v[204:207], a[184:187], a[20:23], v[204:207]\n"
    "s_waitcnt lgkmcnt(4)\n"
    "ds_read_b64_tr_b16 a[184:185], v247 offset:32768\n"
    "v_add_u32_e32 v145, s46, v145\n"
    "v_mfma_f32_16x16x32_bf16 a[248:251], a[160:163], v[160:163], a[248:251]\n"
    "ds_read_b64_tr_b16 a[186:187], v247 offset:34816\n"
    "v_cvt_pk_bf16_f32 v200, v200, v201\n"
    "v_mfma_f32_16x16x32_bf16 a[248:251], a[164:167], v[164:167], a[248:251]\n"
    "ds_read_b64_tr_b16 a[188:189], v247 offset:40960\n"
    "v_and_b32_e32 v145, 3, v145\n"
    "v_mfma_f32_16x16x32_bf16 a[252:255], a[164:167], v[172:175], a[252:255]\n"
    "ds_read_b64_tr_b16 a[190:191], v247 offset:43008\n"
    "v_cvt_pk_bf16_f32 v201, v202, v203\n"
    "v_mfma_f32_16x16x32_bf16 a[252:255], a[160:163], v[168:171], a[252:255]\n"
    "ds_read_b64_tr_b16 a[160:161], v244 offset:36864\n"
    "v_exp_f32_e32 v212, v212\n"
    "v_mfma_f32_16x16x32_bf16 v[4:7], a[168:171], v[168:171], v[4:7]\n"
    "ds_read_b64_tr_b16 a[162:163], v244 offset:38912\n"
    "v_exp_f32_e32 v213, v213\n"
    "v_mfma_f32_16x16x32_bf16 v[4:7], a[172:175], v[172:175], v[4:7]\n"
    "s_barrier\n"
    "v_mfma_f32_16x16x32_bf16 v[0:3], a[172:175], v[164:167], v[0:3]\n"
    "ds_read_b64_tr_b16 a[164:165], v244 offset:45056\n"
    "ds_write_addtid_b32 v208 offset:1024\n"
    "v_mfma_f32_16x16x32_bf16 v[0:3], a[168:171], v[160:163], v[0:3]\n"
    "s_waitcnt lgkmcnt(4)\n"
    "ds_read_b64_tr_b16 a[166:167], v244 offset:47104\n"
    "v_exp_f32_e32 v214, v214\n"
    "v_mfma_f32_16x16x32_bf16 v[8:11], a[176:179], v[160:163], v[8:11]\n"
    "ds_read_b64_tr_b16 a[168:169], v245 offset:36864\n"
    "v_exp_f32_e32 v215, v215\n"
    "v_mfma_f32_16x16x32_bf16 v[8:11], a[180:183], v[164:167], v[8:11]\n"
    "ds_read_b64_tr_b16 a[170:171], v245 offset:38912\n"
    "ds_write_addtid_b32 v209 offset:1280\n"
    "v_mfma_f32_16x16x32_bf16 v[12:15], a[180:183], v[172:175], v[12:15]\n"
    "ds_read_b64_tr_b16 a[172:173], v245 offset:45056\n"
    "v_cvt_pk_bf16_f32 v210, v212, v213\n"
    "v_mfma_f32_16x16x32_bf16 v[12:15], a[176:179], v[168:171], v[12:15]\n"
    "ds_read_b64_tr_b16 a[174:175], v245 offset:47104\n"
    "v_exp_f32_e32 v204, v204\n"
    "v_mfma_f32_16x16x32_bf16 v[20:23], a[184:187], v[168:171], v[20:23]\n"
    "ds_read_b64_tr_b16 a[176:177], v246 offset:36864\n"
    "ds_write_addtid_b32 v200 offset:0\n"
    "v_mfma_f32_16x16x32_bf16 v[20:23], a[188:191], v[172:175], v[20:23]\n"
    "ds_read_b64_tr_b16 a[178:179], v246 offset:38912\n"
    "v_exp_f32_e32 v205, v205\n"
    "v_mfma_f32_16x16x32_bf16 v[16:19], a[188:191], v[164:167], v[16:19]\n"
    "ds_read_b64_tr_b16 a[180:181], v246 offset:45056\n"
    "ds_write_addtid_b32 v201 offset:256\n"
    "v_mfma_f32_16x16x32_bf16 v[16:19], a[184:187], v[160:163], v[16:19]\n"
    "s_waitcnt lgkmcnt(5)\n"
    "ds_read_b64_tr_b16 a[182:183], v246 offset:47104\n"
    "v_cvt_pk_bf16_f32 v211, v214, v215\n"
    "v_mfma_f32_16x16x32_bf16 v[24:27], a[160:163], v[160:163], v[24:27]\n"
    "ds_read_b64_tr_b16 a[184:185], v247 offset:36864\n"
    "v_exp_f32_e32 v206, v206\n"
    "v_mfma_f32_16x16x32_bf16 v[24:27], a[164:167], v[164:167], v[24:27]\n"
    "ds_read_b64_tr_b16 a[186:187], v247 offset:38912\n"
    "ds_write_addtid_b32 v210 offset:1536\n"
    "v_mfma_f32_16x16x32_bf16 v[28:31], a[164:167], v[172:175], v[28:31]\n"
    "ds_read_b64_tr_b16 a[188:189], v247 offset:45056\n"
    "v_exp_f32_e32 v207, v207\n"
    "v_mfma_f32_16x16x32_bf16 v[28:31], a[160:163], v[168:171], v[28:31]\n"
    "ds_read_b64_tr_b16 a[190:191], v247 offset:47104\n"
    "ds_write_addtid_b32 v211 offset:1792\n"
    "v_mfma_f32_16x16x32_bf16 v[36:39], a[168:171], v[168:171], v[36:39]\n"
    "ds_read_b64_tr_b16 a[160:161], v244 offset:49152\n"
    "v_cvt_pk_bf16_f32 v202, v204, v205\n"
    "v_mfma_f32_16x16x32_bf16 v[36:39], a[172:175], v[172:175], v[36:39]\n"
    "ds_read_b64_tr_b16 a[162:163], v244 offset:51200\n"
    "v_cvt_pk_bf16_f32 v203, v206, v207\n"
    "v_mfma_f32_16x16x32_bf16 v[32:35], a[172:175], v[164:167], v[32:35]\n"
    "ds_read_b64_tr_b16 a[164:165], v244 offset:57344\n"
    "v_lshlrev_b32_e32 v145, 4, v145\n"
    "v_mfma_f32_16x16x32_bf16 v[32:35], a[168:171], v[160:163], v[32:35]\n"
    "s_waitcnt lgkmcnt(4)\n"
    "ds_read_b64_tr_b16 a[166:167], v244 offset:59392\n"
    "ds_write_addtid_b32 v202 offset:512\n"
    "v_mfma_f32_16x16x32_bf16 v[40:43], a[176:179], v[160:163], v[40:43]\n"
    "ds_read_b64_tr_b16 a[168:169], v245 offset:49152\n"
    "v_and_or_b32 v145, v147, 3, v145\n"
    "v_mfma_f32_16x16x32_bf16 v[40:43], a[180:183], v[164:167], v[40:43]\n"
    "s_lshl_b32 s47, s33, 8\n"
    "ds_read_b64_tr_b16 a[170:171], v245 offset:51200\n"
    "v_lshlrev_b32_e32 v145, 2, v145\n"
    "v_mfma_f32_16x16x32_bf16 v[44:47], a[180:183], v[172:175], v[44:47]\n"
    "ds_read_b64_tr_b16 a[172:173], v245 offset:57344\n"
    "ds_write_addtid_b32 v203 offset:768\n"
    "v_mfma_f32_16x16x32_bf16 v[44:47], a[176:179], v[168:171], v[44:47]\n"
    "ds_read_b64_tr_b16 a[174:175], v245 offset:59392\n"
    "v_and_b32_e32 v240, 0xffffdfff, v240\n"
    "v_mfma_f32_16x16x32_bf16 v[52:55], a[184:187], v[168:171], v[52:55]\n"
    "s_add_u32 s47, s47, 5636096\n"
    "ds_read_b64_tr_b16 a[176:177], v246 offset:49152\n"
    "v_and_b32_e32 v241, 0xffffdfff, v241\n"
    "v_mfma_f32_16x16x32_bf16 v[52:55], a[188:191], v[172:175], v[52:55]\n"
    "s_waitcnt lgkmcnt(2)\n"
    "s_barrier\n"
    "v_mfma_f32_16x16x32_bf16 v[48:51], a[188:191], v[164:167], v[48:51]\n"
    "ds_read_b64_tr_b16 a[178:179], v246 offset:51200\n"
    "v_cmp_gt_u32_e32 vcc, 0x300, v253\n"
    "v_mfma_f32_16x16x32_bf16 v[48:51], a[184:187], v[160:163], v[48:51]\n"
    "s_waitcnt lgkmcnt(0)\n"
    "ds_read_b64_tr_b16 a[180:181], v246 offset:57344\n"
    "ds_read2st64_b32 v[204:205], v250 offset0:0 offset1:1\n"
    "v_mfma_f32_16x16x32_bf16 a[248:251], a[160:163], v[200:203], a[248:251]\n"
    "ds_read_b64_tr_b16 a[182:183], v246 offset:59392\n"
    "ds_read2st64_b32 v[206:207], v250 offset0:2 offset1:3\n"
    "v_mfma_f32_16x16x32_bf16 a[252:255], a[160:163], v[208:211], a[252:255]\n"
    "ds_read_b64_tr_b16 a[184:185], v247 offset:49152\n"
    "ds_read2st64_b32 v[212:213], v250 offset0:4 offset1:5\n"
    "v_mfma_f32_16x16x32_bf16 v[4:7], a[168:171], v[208:211], v[4:7]\n"
    "ds_read_b64_tr_b16 a[186:187], v247 offset:51200\n"
    "ds_read2st64_b32 v[214:215], v250 offset0:6 offset1:7\n"
    "v_mfma_f32_16x16x32_bf16 v[0:3], a[168:171], v[200:203], v[0:3]\n"
    "s_add_u32 s60, s60, s28\n"
    "ds_read_b64_tr_b16 a[188:189], v247 offset:57344\n"
    "v_and_b32_e32 v242, 0xffffdfff, v242\n"
    "v_mfma_f32_16x16x32_bf16 v[8:11], a[176:179], v[200:203], v[8:11]\n"
    "s_add_u32 s48, s56, 0\n"
    "ds_read_b64_tr_b16 a[190:191], v247 offset:59392\n"
    "v_mfma_f32_16x16x32_bf16 v[12:15], a[176:179], v[208:211], v[12:15]\n"
    "s_waitcnt lgkmcnt(2)\n"
    "ds_read_b64_tr_b16 a[160:161], v244 offset:53248\n"
    "v_and_b32_e32 v243, 0xffffdfff, v243\n"
    "v_mfma_f32_16x16x32_bf16 a[248:251], a[164:167], v[204:207], a[248:251]\n"
    "ds_read_b64_tr_b16 a[162:163], v244 offset:55296\n"
    "v_mfma_f32_16x16x32_bf16 a[252:255], a[164:167], v[212:215], a[252:255]\n"
    "s_waitcnt lgkmcnt(2)\n"
    "ds_read_b64_tr_b16 a[164:165], v244 offset:61440\n"
    "v_and_b32_e32 v251, 0xf0, v253\n"
    "v_mfma_f32_16x16x32_bf16 v[4:7], a[172:175], v[212:215], v[4:7]\n"
    "ds_read_b64_tr_b16 a[166:167], v244 offset:63488\n"
    "buffer_load_dwordx4 a[28:31], v253, s[8:11], s48 offen offset:0 sc1\n"
    "v_mfma_f32_16x16x32_bf16 v[0:3], a[172:175], v[204:207], v[0:3]\n"
    "ds_read_b64_tr_b16 a[168:169], v245 offset:53248\n"
    "v_mfma_f32_16x16x32_bf16 v[8:11], a[180:183], v[204:207], v[8:11]\n"
    "s_waitcnt vmcnt(1)\n"
    "ds_read_b64_tr_b16 a[170:171], v245 offset:55296\n"
    "buffer_store_dword v148, v149, s[8:11], s87 offen offset:32 sc1\n"
    "v_cndmask_b32_e64 v251, v251, 2.0, vcc\n"
    "v_mfma_f32_16x16x32_bf16 v[12:15], a[180:183], v[212:215], v[12:15]\n"
    "ds_read_b64_tr_b16 a[172:173], v245 offset:61440\n"
    "v_accvgpr_read_b32 v220, a252\n"
    "v_mfma_f32_16x16x32_bf16 v[20:23], a[184:187], v[208:211], v[20:23]\n"
    "ds_read_b64_tr_b16 a[174:175], v245 offset:63488\n"
    "buffer_load_dwordx4 a[60:63], v253, s[8:11], s48 offen offset:1024 sc1\n"
    "v_accvgpr_read_b32 v221, a253\n"
    "v_mfma_f32_16x16x32_bf16 v[20:23], a[188:191], v[212:215], v[20:23]\n"
    "ds_read_b64_tr_b16 a[176:177], v246 offset:53248\n"
    "v_accvgpr_read_b32 v222, a254\n"
    "v_mfma_f32_16x16x32_bf16 v[16:19], a[188:191], v[204:207], v[16:19]\n"
    "ds_read_b64_tr_b16 a[178:179], v246 offset:55296\n"
    "v_accvgpr_read_b32 v223, a255\n"
    "v_mfma_f32_16x16x32_bf16 v[16:19], a[184:187], v[200:203], v[16:19]\n"
    "s_waitcnt lgkmcnt(2)\n"
    "ds_read_b64_tr_b16 a[180:181], v246 offset:61440\n"
    "v_cvt_pk_bf16_f32 v220, v220, v221\n"
    "v_mfma_f32_16x16x32_bf16 v[24:27], a[160:163], v[200:203], v[24:27]\n"
    "ds_read_b64_tr_b16 a[182:183], v246 offset:63488\n"
    "buffer_load_dwordx4 a[0:3], v253, s[8:11], s48 offen offset:2048 sc1\n"
    "v_mfma_f32_16x16x32_bf16 v[24:27], a[164:167], v[204:207], v[24:27]\n"
    "ds_read_b64_tr_b16 a[184:185], v247 offset:53248\n"
    "v_cvt_pk_bf16_f32 v221, v222, v223\n"
    "v_mfma_f32_16x16x32_bf16 v[28:31], a[164:167], v[212:215], v[28:31]\n"
    "ds_read_b64_tr_b16 a[186:187], v247 offset:55296\n"
    "v_cvt_pk_bf16_f32 v222, v4, v5\n"
    "v_mfma_f32_16x16x32_bf16 v[28:31], a[160:163], v[208:211], v[28:31]\n"
    "ds_read_b64_tr_b16 a[188:189], v247 offset:61440\n"
    "v_cvt_pk_bf16_f32 v223, v6, v7\n"
    "v_mfma_f32_16x16x32_bf16 v[36:39], a[168:171], v[208:211], v[36:39]\n"
    "ds_read_b64_tr_b16 a[190:191], v247 offset:63488\n"
    "buffer_load_dwordx4 a[32:35], v253, s[8:11], s48 offen offset:3072 sc1\n"
    "buffer_store_dwordx4 v[220:223], v253, s[8:11], s83 offen offset:0 sc1\n"
    "v_accvgpr_read_b32 v216, a248\n"
    "v_accvgpr_read_b32 v217, a249\n"
    "v_mfma_f32_16x16x32_bf16 v[36:39], a[172:175], v[212:215], v[36:39]\n"
    "v_accvgpr_read_b32 v218, a250\n"
    "v_accvgpr_read_b32 v219, a251\n"
    "v_mfma_f32_16x16x32_bf16 v[32:35], a[172:175], v[204:207], v[32:35]\n"
    "v_cvt_pk_bf16_f32 v216, v216, v217\n"
    "v_cvt_pk_bf16_f32 v217, v218, v219\n"
    "v_mfma_f32_16x16x32_bf16 v[32:35], a[168:171], v[200:203], v[32:35]\n"
    "s_waitcnt lgkmcnt(0)\n"
    "v_cvt_pk_bf16_f32 v218, v0, v1\n"
    "v_cvt_pk_bf16_f32 v219, v2, v3\n"
    "v_mfma_f32_16x16x32_bf16 v[40:43], a[176:179], v[200:203], v[40:43]\n"
    "v_cvt_pk_bf16_f32 v12, v12, v13\n"
    "v_cvt_pk_bf16_f32 v13, v14, v15\n"
    "v_mfma_f32_16x16x32_bf16 v[40:43], a[180:183], v[204:207], v[40:43]\n"
    "v_cvt_pk_bf16_f32 v14, v20, v21\n"
    "v_cvt_pk_bf16_f32 v15, v22, v23\n"
    "v_mfma_f32_16x16x32_bf16 v[44:47], a[180:183], v[212:215], v[44:47]\n"
    "buffer_store_dwordx4 v[12:15], v253, s[8:11], s83 offen offset:1024 sc1\n"
    "v_cvt_pk_bf16_f32 v8, v8, v9\n"
    "v_cvt_pk_bf16_f32 v9, v10, v11\n"
    "v_mfma_f32_16x16x32_bf16 v[44:47], a[176:179], v[208:211], v[44:47]\n"
    "v_cvt_pk_bf16_f32 v10, v16, v17\n"
    "v_cvt_pk_bf16_f32 v11, v18, v19\n"
    "v_mfma_f32_16x16x32_bf16 v[52:55], a[184:187], v[208:211], v[52:55]\n"
    "v_cvt_pk_bf16_f32 v28, v28, v29\n"
    "v_cvt_pk_bf16_f32 v29, v30, v31\n"
    "v_mfma_f32_16x16x32_bf16 v[52:55], a[188:191], v[212:215], v[52:55]\n"
    "v_cvt_pk_bf16_f32 v30, v36, v37\n"
    "v_cvt_pk_bf16_f32 v31, v38, v39\n"
    "v_mfma_f32_16x16x32_bf16 v[48:51], a[188:191], v[204:207], v[48:51]\n"
    "buffer_store_dwordx4 v[28:31], v253, s[8:11], s83 offen offset:2048 sc1\n"
    "v_cvt_pk_bf16_f32 v24, v24, v25\n"
    "v_cvt_pk_bf16_f32 v25, v26, v27\n"
    "v_mfma_f32_16x16x32_bf16 v[48:51], a[184:187], v[200:203], v[48:51]\n"
    "v_cvt_pk_bf16_f32 v26, v32, v33\n"
    "v_cvt_pk_bf16_f32 v27, v34, v35\n"
    "v_cvt_pk_bf16_f32 v44, v44, v45\n"
    "v_cvt_pk_bf16_f32 v45, v46, v47\n"
    "v_cvt_pk_bf16_f32 v46, v52, v53\n"
    "v_cvt_pk_bf16_f32 v47, v54, v55\n"
    "buffer_store_dwordx4 v[44:47], v253, s[8:11], s83 offen offset:3072 sc1\n"
    "buffer_store_dword v55, v251, s[8:11], s60 offen offset:0 sc1\n"
    "v_cvt_pk_bf16_f32 v40, v40, v41\n"
    "v_cvt_pk_bf16_f32 v41, v42, v43\n"
    "v_cvt_pk_bf16_f32 v42, v48, v49\n"
    "v_cvt_pk_bf16_f32 v43, v50, v51\n"
    "v_mfma_f32_16x16x32_bf16 v[160:163], v[154:157], a[224:227], 0\n"
    "ds_write_b128 v159, v[44:47] offset:3072\n"
    "v_mfma_f32_16x16x32_bf16 v[164:167], v[152:155], a[224:227], 0\n"
    "s_add_u32 s48, s56, 4096\n"
    "ds_write_b128 v159, v[220:223] offset:0\n"
    "v_mfma_f32_16x16x32_bf16 v[168:171], v[154:157], a[228:231], 0\n"
    "ds_write_b128 v158, v[216:219] offset:0\n"
    "buffer_load_dwordx4 a[4:7], v253, s[8:11], s48 offen offset:0 sc1\n"
    "v_mfma_f32_16x16x32_bf16 v[172:175], v[152:155], a[228:231], 0\n"
    "ds_write_b128 v159, v[12:15] offset:1024\n"
    "v_mfma_f32_16x16x32_bf16 v[176:179], v[154:157], a[232:235], 0\n"
    "ds_write_b128 v158, v[8:11] offset:1024\n"
    "v_mfma_f32_16x16x32_bf16 v[180:183], v[152:155], a[232:235], 0\n"
    "ds_write_b128 v159, v[28:31] offset:2048\n"
    "v_mfma_f32_16x16x32_bf16 v[184:187], v[154:157], a[236:239], 0\n"
    "ds_write_b128 v158, v[24:27] offset:2048\n"
    "buffer_load_dwordx4 a[36:39], v253, s[8:11], s48 offen offset:1024 sc1\n"
    "v_mfma_f32_16x16x32_bf16 v[188:191], v[152:155], a[236:239], 0\n"
    "ds_write_b128 v158, v[40:43] offset:3072\n"
    "v_mfma_f32_16x16x32_bf16 v[192:195], v[154:157], a[240:243], 0\n"
    "ds_write_b32 v254, v55 offset:0\n"
    "v_mfma_f32_16x16x32_bf16 v[196:199], v[152:155], a[240:243], 0\n"
    "ds_write_b32 v255, v51 offset:0\n"
    "v_mfma_f32_16x16x32_bf16 v[200:203], v[154:157], a[244:247], 0\n"
    "buffer_load_dwordx4 a[8:11], v253, s[8:11], s48 offen offset:2048 sc1\n"
    "v_mfma_f32_16x16x32_bf16 v[204:207], v[152:155], a[244:247], 0\n"
    "s_waitcnt vmcnt(26)\n"
    "s_waitcnt lgkmcnt(0)\n"
    "s_barrier\n"
    "ds_read_b128 v[0:3], v243 offset:6144\n"
    "ds_read_b128 v[4:7], v243 offset:8192\n"
    "ds_read_b128 v[8:11], v243 offset:10240\n"
    "ds_read_b128 v[12:15], v240 offset:0\n"
    "ds_read_b128 v[16:19], v240 offset:2048\n"
    "ds_read_b128 v[20:23], v240 offset:4096\n"
    "s_waitcnt lgkmcnt(3)\n"
    "v_mfma_f32_16x16x32_bf16 v[160:163], a[192:195], v[0:3], v[160:163]\n"
    "s_lshl_b32 s45, s51, 16\n"
    "ds_read_b128 v[24:27], v241 offset:0\n"
    "v_mfma_f32_16x16x32_bf16 v[164:167], a[196:199], v[0:3], v[164:167]\n"
    "s_lshl_b32 s46, s50, 11\n"
    "ds_read_b128 v[28:31], v241 offset:2048\n"
    "v_mfma_f32_16x16x32_bf16 v[168:171], a[200:203], v[0:3], v[168:171]\n"
    "ds_read_b128 v[32:35], v241 offset:4096\n"
    "buffer_load_dwordx4 a[40:43], v253, s[8:11], s48 offen offset:3072 sc1\n"
    "v_mfma_f32_16x16x32_bf16 v[172:175], a[204:207], v[0:3], v[172:175]\n"
    "s_add_u32 s48, s56, 8192\n"
    "ds_read_b128 v[36:39], v240 offset:6144\n"
    "v_mfma_f32_16x16x32_bf16 v[176:179], a[192:195], v[4:7], v[176:179]\n"
    "buffer_load_dword v146, v145, s[8:11], s47 offen offset:32 sc1\n"
    "v_mfma_f32_16x16x32_bf16 v[180:183], a[196:199], v[4:7], v[180:183]\n"
    "s_add_u32 s46, s46, s45\n"
    "ds_read_b128 v[40:43], v240 offset:8192\n"
    "v_mfma_f32_16x16x32_bf16 v[184:187], a[200:203], v[4:7], v[184:187]\n"
    "ds_read_b128 v[44:47], v240 offset:10240\n"
    "buffer_load_dwordx4 a[12:15], v253, s[8:11], s48 offen offset:0 sc1\n"
    "v_mfma_f32_16x16x32_bf16 v[188:191], a[204:207], v[4:7], v[188:191]\n"
    "s_add_u32 s46, s46, %3\n"
    "ds_read_b128 v[0:3], v241 offset:6144\n"
    "v_mfma_f32_16x16x32_bf16 v[192:195], a[192:195], v[8:11], v[192:195]\n"
    "s_add_u32 s46, s46, 49152\n"
    "ds_read_b128 v[4:7], v241 offset:8192\n"
    "v_mfma_f32_16x16x32_bf16 v[196:199], a[196:199], v[8:11], v[196:199]\n"
    "v_add_u32_e32 v158, s46, v253\n"
    "v_mfma_f32_16x16x32_bf16 v[200:203], a[200:203], v[8:11], v[200:203]\n"
    "buffer_load_dwordx4 a[44:47], v253, s[8:11], s48 offen offset:1024 sc1\n"
    "v_mfma_f32_16x16x32_bf16 v[204:207], a[204:207], v[8:11], v[204:207]\n"
    "s_waitcnt lgkmcnt(8)\n"
    "ds_read_b128 v[8:11], v241 offset:10240\n"
    "v_mfma_f32_16x16x32_bf16 v[160:163], a[208:211], v[12:15], v[160:163]\n"
    "s_waitcnt vmcnt(7)\n"
    "buffer_store_dword v148, v149, s[8:11], s87 offen offset:0 sc1\n"
    "v_mfma_f32_16x16x32_bf16 v[164:167], a[212:215], v[12:15], v[164:167]\n"
    "s_add_u32 s45, %3, 110784\n"
    "v_and_b32_e32 v150, 15, v147\n"
    "v_mfma_f32_16x16x32_bf16 v[168:171], a[216:219], v[12:15], v[168:171]\n"
    "buffer_load_dwordx4 a[16:19], v253, s[8:11], s48 offen offset:2048 sc1\n"
    "v_mfma_f32_16x16x32_bf16 v[172:175], a[220:223], v[12:15], v[172:175]\n"
    "ds_read_b128 v[12:15], v242 offset:0\n"
    "v_mfma_f32_16x16x32_bf16 v[176:179], a[208:211], v[16:19], v[176:179]\n"
    "v_lshlrev_b32_e32 v151, 9, v150\n"
    "v_mfma_f32_16x16x32_bf16 v[180:183], a[212:215], v[16:19], v[180:183]\n"
    "v_lshl_add_u32 v150, v150, 2, s45\n"
    "v_mfma_f32_16x16x32_bf16 v[184:187], a[216:219], v[16:19], v[184:187]\n"
    "buffer_load_dwordx4 a[48:51], v253, s[8:11], s48 offen offset:3072 sc1\n"
    "v_mfma_f32_16x16x32_bf16 v[188:191], a[220:223], v[16:19], v[188:191]\n"
    "ds_read_b128 v[16:19], v242 offset:2048\n"
    "v_mfma_f32_16x16x32_bf16 v[192:195], a[208:211], v[20:23], v[192:195]\n"
    "s_lshl_b32 s46, %2, 7\n"
    "v_and_or_b32 v151, v147, 48, v151\n"
    "v_mfma_f32_16x16x32_bf16 v[196:199], a[212:215], v[20:23], v[196:199]\n"
    "s_add_u32 s58, s35, s46\n"
    "s_mul_i32 s45, %1, 74496\n"
    "v_mfma_f32_16x16x32_bf16 v[200:203], a[216:219], v[20:23], v[200:203]\n"
    "s_add_u32 s45, s45, 16183296\n"
    "v_mfma_f32_16x16x32_bf16 v[204:207], a[220:223], v[20:23], v[204:207]\n"
    "s_waitcnt lgkmcnt(8)\n"
    "ds_read_b128 v[20:23], v242 offset:4096\n"
    "v_mfma_f32_16x16x32_bf16 v[160:163], a[92:95], v[24:27], v[160:163]\n"
    "s_lshl_b32 s46, %2, 11\n"
    "v_and_b32_e32 v147, 0xf0, v253\n"
    "v_mfma_f32_16x16x32_bf16 v[164:167], a[124:127], v[24:27], v[164:167]\n"
    "s_add_u32 s46, s46, s45\n"
    "v_add_u32_e32 v159, %3, v253\n"
    "v_mfma_f32_16x16x32_bf16 v[168:171], a[156:159], v[24:27], v[168:171]\n"
    "s_bitcmp1_b32 %1, 0\n"
    "s_cselect_b32 s54, s46, 2.0\n"
    "s_cselect_b32 s55, s58, 2.0\n"
    "v_mfma_f32_16x16x32_bf16 v[172:175], a[64:67], v[24:27], v[172:175]\n"
    "ds_read_b128 v[24:27], v243 offset:0\n"
    "v_mfma_f32_16x16x32_bf16 v[176:179], a[92:95], v[28:31], v[176:179]\n"
    "s_waitcnt vmcnt(5)\n"
    "v_cmp_ne_u32_e32 vcc, s39, v146\n"
    "v_mfma_f32_16x16x32_bf16 v[180:183], a[124:127], v[28:31], v[180:183]\n"
    "s_add_u32 s61, s54, 16384\n"
    "v_mfma_f32_16x16x32_bf16 v[184:187], a[156:159], v[28:31], v[184:187]\n"
    "s_cbranch_vccnz .Lat_xpoll\n"
    ".Lat_xpoll_ret:\n"
    "v_mfma_f32_16x16x32_bf16 v[188:191], a[64:67], v[28:31], v[188:191]\n"
    "ds_read_b128 v[28:31], v243 offset:2048\n"
    "v_mfma_f32_16x16x32_bf16 v[192:195], a[92:95], v[32:35], v[192:195]\n"
    "buffer_load_dwordx4 v[96:99], v253, s[8:11], s61 offen offset:0 sc1\n"
    "v_mfma_f32_16x16x32_bf16 v[196:199], a[124:127], v[32:35], v[196:199]\n"
    "ds_read_b32 v232, v150 offset:0\n"
    "v_mfma_f32_16x16x32_bf16 v[200:203], a[156:159], v[32:35], v[200:203]\n"
    "v_mfma_f32_16x16x32_bf16 v[204:207], a[64:67], v[32:35], v[204:207]\n"
    "s_waitcnt lgkmcnt(9)\n"
    "ds_read_b128 v[32:35], v243 offset:4096\n"
    "v_mfma_f32_16x16x32_bf16 v[160:163], a[96:99], v[36:39], v[160:163]\n"
    "buffer_load_dwordx4 v[100:103], v253, s[8:11], s61 offen offset:1024 sc1\n"
    "v_mfma_f32_16x16x32_bf16 v[164:167], a[128:131], v[36:39], v[164:167]\n"
    "s_add_u32 s61, s54, 41216\n"
    "ds_read_b32 v233, v150 offset:256\n"
    "v_mfma_f32_16x16x32_bf16 v[168:171], a[68:71], v[36:39], v[168:171]\n"
    "v_mfma_f32_16x16x32_bf16 v[172:175], a[100:103], v[36:39], v[172:175]\n"
    "ds_read_b128 v[36:39], v242 offset:6144\n"
    "buffer_load_dwordx4 v[104:107], v253, s[8:11], s61 offen offset:0 sc1\n"
    "v_mfma_f32_16x16x32_bf16 v[176:179], a[96:99], v[40:43], v[176:179]\n"
    "ds_read_b32 v234, v150 offset:512\n"
    "v_mfma_f32_16x16x32_bf16 v[180:183], a[128:131], v[40:43], v[180:183]\n"
    "v_mfma_f32_16x16x32_bf16 v[184:187], a[68:71], v[40:43], v[184:187]\n"
    "buffer_load_dwordx4 v[108:111], v253, s[8:11], s61 offen offset:1024 sc1\n"
    "v_mfma_f32_16x16x32_bf16 v[188:191], a[100:103], v[40:43], v[188:191]\n"
    "s_add_u32 s61, s54, 66048\n"
    "ds_read_b128 v[40:43], v242 offset:8192\n"
    "v_mfma_f32_16x16x32_bf16 v[192:195], a[96:99], v[44:47], v[192:195]\n"
    "ds_read_b128 a[208:211], v158 offset:8192\n"
    "v_mfma_f32_16x16x32_bf16 v[196:199], a[128:131], v[44:47], v[196:199]\n"
    "buffer_load_dwordx4 v[112:115], v253, s[8:11], s61 offen offset:0 sc1\n"
    "v_mfma_f32_16x16x32_bf16 v[200:203], a[68:71], v[44:47], v[200:203]\n"
    "ds_read_b128 a[212:215], v158 offset:9216\n"
    "v_mfma_f32_16x16x32_bf16 v[204:207], a[100:103], v[44:47], v[204:207]\n"
    "s_waitcnt lgkmcnt(13)\n"
    "ds_read_b128 v[44:47], v242 offset:10240\n"
    "v_mfma_f32_16x16x32_bf16 v[160:163], a[132:135], v[0:3], v[160:163]\n"
    "buffer_load_dwordx4 v[116:119], v253, s[8:11], s61 offen offset:1024 sc1\n"
    "v_mfma_f32_16x16x32_bf16 v[164:167], a[72:75], v[0:3], v[164:167]\n"
    "s_add_u32 s61, s46, 8192\n"
    "ds_read_b128 a[200:203], v158 offset:4096\n"
    "v_mfma_f32_16x16x32_bf16 v[168:171], a[104:107], v[0:3], v[168:171]\n"
    "v_mfma_f32_16x16x32_bf16 v[172:175], a[136:139], v[0:3], v[172:175]\n"
    "buffer_load_dwordx4 v[72:75], v253, s[8:11], s61 offen offset:0 sc1\n"
    "v_mfma_f32_16x16x32_bf16 v[176:179], a[132:135], v[4:7], v[176:179]\n"
    "v_mfma_f32_16x16x32_bf16 v[180:183], a[72:75], v[4:7], v[180:183]\n"
    "ds_read_b128 a[204:207], v158 offset:5120\n"
    "v_mfma_f32_16x16x32_bf16 v[184:187], a[104:107], v[4:7], v[184:187]\n"
    "buffer_load_dwordx4 v[76:79], v253, s[8:11], s61 offen offset:1024 sc1\n"
    "v_mfma_f32_16x16x32_bf16 v[188:191], a[136:139], v[4:7], v[188:191]\n"
    "s_add_u32 s61, s46, 33024\n"
    "v_mfma_f32_16x16x32_bf16 v[192:195], a[132:135], v[8:11], v[192:195]\n"
    "ds_read_b128 a[192:195], v158 offset:0\n"
    "v_mfma_f32_16x16x32_bf16 v[196:199], a[72:75], v[8:11], v[196:199]\n"
    "buffer_load_dwordx4 v[80:83], v253, s[8:11], s61 offen offset:0 sc1\n"
    "v_mfma_f32_16x16x32_bf16 v[200:203], a[104:107], v[8:11], v[200:203]\n"
    "v_mfma_f32_16x16x32_bf16 v[204:207], a[136:139], v[8:11], v[204:207]\n"
    "s_waitcnt lgkmcnt(14)\n"
    "ds_read_b128 a[196:199], v158 offset:1024\n"
    "v_mfma_f32_16x16x32_bf16 v[160:163], a[76:79], v[12:15], v[160:163]\n"
    "buffer_load_dwordx4 v[84:87], v253, s[8:11], s61 offen offset:1024 sc1\n"
    "v_mfma_f32_16x16x32_bf16 v[164:167], a[108:111], v[12:15], v[164:167]\n"
    "s_add_u32 s61, s46, 0\n"
    "v_mfma_f32_16x16x32_bf16 v[168:171], a[140:143], v[12:15], v[168:171]\n"
    "s_lshl_b32 s52, %2, 11\n"
    "v_mfma_f32_16x16x32_bf16 v[172:175], a[80:83], v[12:15], v[172:175]\n"
    "buffer_load_dwordx4 v[48:51], v253, s[8:11], s61 offen offset:0 sc1\n"
    "v_mfma_f32_16x16x32_bf16 v[176:179], a[76:79], v[16:19], v[176:179]\n"
    "v_mfma_f32_16x16x32_bf16 v[180:183], a[108:111], v[16:19], v[180:183]\n"
    "v_add_u32_e32 v158, s52, v159\n"
    "v_mfma_f32_16x16x32_bf16 v[184:187], a[140:143], v[16:19], v[184:187]\n"
    "buffer_load_dwordx4 v[52:55], v253, s[8:11], s61 offen offset:1024 sc1\n"
    "v_mfma_f32_16x16x32_bf16 v[188:191], a[80:83], v[16:19], v[188:191]\n"
    "s_add_u32 s61, s46, 24832\n"
    "v_mfma_f32_16x16x32_bf16 v[192:195], a[76:79], v[20:23], v[192:195]\n"
    "v_mfma_f32_16x16x32_bf16 v[196:199], a[108:111], v[20:23], v[196:199]\n"
    "buffer_load_dwordx4 v[56:59], v253, s[8:11], s61 offen offset:0 sc1\n"
    "v_mfma_f32_16x16x32_bf16 v[200:203], a[140:143], v[20:23], v[200:203]\n"
    "s_add_u32 s52, s32, 3\n"
    "v_and_b32_e32 v145, 12, v145\n"
    "v_mfma_f32_16x16x32_bf16 v[204:207], a[80:83], v[20:23], v[204:207]\n"
    "s_waitcnt lgkmcnt(11)\n"
    "v_mfma_f32_16x16x32_bf16 v[160:163], a[112:115], v[24:27], v[160:163]\n"
    "buffer_load_dwordx4 v[60:63], v253, s[8:11], s61 offen offset:1024 sc1\n"
    "v_mfma_f32_16x16x32_bf16 v[164:167], a[144:147], v[24:27], v[164:167]\n"
    "s_add_u32 s61, s45, 24576\n"
    "v_mfma_f32_16x16x32_bf16 v[168:171], a[84:87], v[24:27], v[168:171]\n"
    "s_and_b32 s52, s52, 3\n"
    "v_mfma_f32_16x16x32_bf16 v[172:175], a[116:119], v[24:27], v[172:175]\n"
    "buffer_load_dwordx3 v[236:238], v147, s[8:11], s61 offen offset:0 sc1\n"
    "v_mfma_f32_16x16x32_bf16 v[176:179], a[112:115], v[28:31], v[176:179]\n"
    "s_add_u32 s61, s45, 49408\n"
    "v_mfma_f32_16x16x32_bf16 v[180:183], a[144:147], v[28:31], v[180:183]\n"
    "s_lshl_b32 s52, s52, 6\n"
    "v_mfma_f32_16x16x32_bf16 v[184:187], a[84:87], v[28:31], v[184:187]\n"
    "buffer_load_dwordx3 v[248:250], v147, s[8:11], s61 offen offset:0 sc1\n"
    "v_mfma_f32_16x16x32_bf16 v[188:191], a[116:119], v[28:31], v[188:191]\n"
    "s_add_u32 s61, s45, 74240\n"
    "v_mfma_f32_16x16x32_bf16 v[192:195], a[112:115], v[32:35], v[192:195]\n"
    "s_add_u32 s52, s52, s47\n"
    "v_mfma_f32_16x16x32_bf16 v[196:199], a[144:147], v[32:35], v[196:199]\n"
    "buffer_load_dword v246, v147, s[8:11], s61 offen offset:8 sc1\n"
    "v_mfma_f32_16x16x32_bf16 v[200:203], a[84:87], v[32:35], v[200:203]\n"
    "s_add_u32 s48, s56, 12288\n"
    "v_mfma_f32_16x16x32_bf16 v[204:207], a[116:119], v[32:35], v[204:207]\n"
    "s_waitcnt lgkmcnt(4)\n"
    "v_mfma_f32_16x16x32_bf16 v[160:163], a[148:151], v[36:39], v[160:163]\n"
    "buffer_load_dwordx4 a[20:23], v253, s[8:11], s48 offen offset:0 sc1\n"
    "v_mfma_f32_16x16x32_bf16 v[164:167], a[88:91], v[36:39], v[164:167]\n"
    "v_mfma_f32_16x16x32_bf16 v[168:171], a[120:123], v[36:39], v[168:171]\n"
    "buffer_load_dwordx4 a[52:55], v253, s[8:11], s48 offen offset:1024 sc1\n"
    "v_mfma_f32_16x16x32_bf16 v[172:175], a[152:155], v[36:39], v[172:175]\n"
    "v_mfma_f32_16x16x32_bf16 v[176:179], a[148:151], v[40:43], v[176:179]\n"
    "buffer_load_dwordx4 a[24:27], v253, s[8:11], s48 offen offset:2048 sc1\n"
    "v_mfma_f32_16x16x32_bf16 v[180:183], a[88:91], v[40:43], v[180:183]\n"
    "v_mfma_f32_16x16x32_bf16 v[184:187], a[120:123], v[40:43], v[184:187]\n"
    "buffer_load_dwordx4 a[56:59], v253, s[8:11], s48 offen offset:3072 sc1\n"
    "v_mfma_f32_16x16x32_bf16 v[188:191], a[152:155], v[40:43], v[188:191]\n"
    "s_add_u32 s48, s56, 16384\n"
    "v_mfma_f32_16x16x32_bf16 v[192:195], a[148:151], v[44:47], v[192:195]\n"
    "buffer_load_dwordx4 a[160:163], v253, s[8:11], s48 offen offset:0 sc1\n"
    "v_mfma_f32_16x16x32_bf16 v[196:199], a[88:91], v[44:47], v[196:199]\n"
    "v_mfma_f32_16x16x32_bf16 v[200:203], a[120:123], v[44:47], v[200:203]\n"
    "buffer_load_dwordx4 a[164:167], v253, s[8:11], s48 offen offset:1024 sc1\n"
    "v_mfma_f32_16x16x32_bf16 v[204:207], a[152:155], v[44:47], v[204:207]\n"
    "s_waitcnt lgkmcnt(0)\n"
    "v_mfma_f32_16x16x32_bf16 v[128:131], v[154:157], a[208:211], 0\n"
    "buffer_load_dwordx4 a[168:171], v253, s[8:11], s48 offen offset:2048 sc1\n"
    "v_mfma_f32_16x16x32_bf16 v[132:135], v[152:155], a[208:211], 0\n"
    "v_mfma_f32_16x16x32_bf16 v[136:139], v[154:157], a[212:215], 0\n"
    "buffer_load_dwordx4 a[172:175], v253, s[8:11], s48 offen offset:3072 sc1\n"
    "v_mfma_f32_16x16x32_bf16 v[140:143], v[152:155], a[212:215], 0\n"
    "s_waitcnt vmcnt(23)\n"
    "s_add_u32 s48, s56, 20480\n"
    "v_mfma_f32_16x16x32_bf16 v[128:131], v[154:157], v[96:99], v[128:131]\n"
    "buffer_load_dwordx4 a[176:179], v253, s[8:11], s48 offen offset:0 sc1\n"
    "v_mfma_f32_16x16x32_bf16 v[132:135], v[152:155], v[96:99], v[132:135]\n"
    "v_mfma_f32_16x16x32_bf16 v[136:139], v[154:157], v[100:103], v[136:139]\n"
    "buffer_load_dwordx4 a[180:183], v253, s[8:11], s48 offen offset:1024 sc1\n"
    "v_mfma_f32_16x16x32_bf16 v[140:143], v[152:155], v[100:103], v[140:143]\n"
    "s_waitcnt vmcnt(23)\n"
    "v_mfma_f32_16x16x32_bf16 v[128:131], v[154:157], v[104:107], v[128:131]\n"
    "buffer_load_dwordx4 a[184:187], v253, s[8:11], s48 offen offset:2048 sc1\n"
    "v_mfma_f32_16x16x32_bf16 v[132:135], v[152:155], v[104:107], v[132:135]\n"
    "v_mfma_f32_16x16x32_bf16 v[136:139], v[154:157], v[108:111], v[136:139]\n"
    "buffer_load_dwordx4 a[188:191], v253, s[8:11], s48 offen offset:3072 sc1\n"
    "v_mfma_f32_16x16x32_bf16 v[140:143], v[152:155], v[108:111], v[140:143]\n"
    "s_waitcnt vmcnt(23)\n"
    "s_add_u32 s48, s56, 24576\n"
    "v_mfma_f32_16x16x32_bf16 v[128:131], v[154:157], v[112:115], v[128:131]\n"
    "buffer_load_dwordx4 a[224:227], v253, s[8:11], s48 offen offset:0 sc1\n"
    "v_mfma_f32_16x16x32_bf16 v[132:135], v[152:155], v[112:115], v[132:135]\n"
    "v_mfma_f32_16x16x32_bf16 v[136:139], v[154:157], v[116:119], v[136:139]\n"
    "buffer_load_dwordx4 a[228:231], v253, s[8:11], s48 offen offset:1024 sc1\n"
    "v_mfma_f32_16x16x32_bf16 v[140:143], v[152:155], v[116:119], v[140:143]\n"
    "v_mfma_f32_16x16x32_bf16 v[224:227], v[154:157], a[200:203], 0\n"
    "buffer_load_dwordx4 a[232:235], v253, s[8:11], s48 offen offset:2048 sc1\n"
    "v_cvt_pk_bf16_f32 v128, v128, v129\n"
    "v_mfma_f32_16x16x32_bf16 v[228:231], v[152:155], a[200:203], 0\n"
    "v_cvt_pk_bf16_f32 v129, v130, v131\n"
    "v_mfma_f32_16x16x32_bf16 v[120:123], v[154:157], a[204:207], 0\n"
    "buffer_load_dwordx4 a[236:239], v253, s[8:11], s48 offen offset:3072 sc1\n"
    "v_cvt_pk_bf16_f32 v130, v132, v133\n"
    "v_mfma_f32_16x16x32_bf16 v[124:127], v[152:155], a[204:207], 0\n"
    "s_add_u32 s48, s56, 28672\n"
    "v_cvt_pk_bf16_f32 v131, v134, v135\n"
    "v_mfma_f32_16x16x32_bf16 v[208:211], v[154:157], a[192:195], 0\n"
    "ds_write_b128 v158, v[128:131] offset:16384\n"
    "v_mfma_f32_16x16x32_bf16 v[212:215], v[152:155], a[192:195], 0\n"
    "buffer_load_dwordx4 a[240:243], v253, s[8:11], s48 offen offset:0 sc1\n"
    "v_cvt_pk_bf16_f32 v136, v136, v137\n"
    "v_mfma_f32_16x16x32_bf16 v[216:219], v[154:157], a[196:199], 0\n"
    "v_cvt_pk_bf16_f32 v137, v138, v139\n"
    "v_mfma_f32_16x16x32_bf16 v[220:223], v[152:155], a[196:199], 0\n"
    "s_waitcnt vmcnt(26)\n"
    "buffer_load_dwordx4 a[244:247], v253, s[8:11], s48 offen offset:1024 sc1\n"
    "v_cvt_pk_bf16_f32 v138, v140, v141\n"
    "v_cvt_pk_bf16_f32 v139, v142, v143\n"
    "v_mfma_f32_16x16x32_bf16 v[224:227], v[154:157], v[72:75], v[224:227]\n"
    "ds_write_b128 v158, v[136:139] offset:17408\n"
    "buffer_load_dword v144, v145, s[8:11], s52 offen offset:0 sc1\n"
    "v_mfma_f32_16x16x32_bf16 v[228:231], v[152:155], v[72:75], v[228:231]\n"
    "buffer_load_dwordx4 a[248:251], v253, s[8:11], s48 offen offset:2048 sc1\n"
    "v_mfma_f32_16x16x32_bf16 v[120:123], v[154:157], v[76:79], v[120:123]\n"
    "s_waitcnt lgkmcnt(0)\n"
    "s_barrier\n"
    "ds_read_b128 a[128:131], v159 offset:16384\n"
    "ds_read_b128 a[132:135], v159 offset:17408\n"
    "ds_read_b128 a[136:139], v159 offset:18432\n"
    "ds_read_b128 a[140:143], v159 offset:19456\n"
    "v_mfma_f32_16x16x32_bf16 v[124:127], v[152:155], v[76:79], v[124:127]\n"
    "s_waitcnt vmcnt(27)\n"
    "buffer_load_dwordx4 a[252:255], v253, s[8:11], s48 offen offset:3072 sc1\n"
    "v_mfma_f32_16x16x32_bf16 v[224:227], v[154:157], v[80:83], v[224:227]\n"
    "ds_read_b128 a[144:147], v159 offset:20480\n"
    "v_mfma_f32_16x16x32_bf16 v[228:231], v[152:155], v[80:83], v[228:231]\n"
    "v_mfma_f32_16x16x32_bf16 v[120:123], v[154:157], v[84:87], v[120:123]\n"
    "ds_read_b128 a[148:151], v159 offset:21504\n"
    "v_mfma_f32_16x16x32_bf16 v[124:127], v[152:155], v[84:87], v[124:127]\n"
    "s_waitcnt lgkmcnt(4)\n"
    "ds_read_b128 a[152:155], v159 offset:22528\n"
    "v_mfma_f32_16x16x32_bf16 v[32:35], a[28:31], a[128:131], 0\n"
    "v_mfma_f32_16x16x32_bf16 v[36:39], a[60:63], a[128:131], 0\n"
    "ds_read_b128 a[156:159], v159 offset:23552\n"
    "v_mfma_f32_16x16x32_bf16 v[40:43], a[0:3], a[128:131], 0\n"
    "v_mfma_f32_16x16x32_bf16 v[44:47], a[32:35], a[128:131], 0\n"
    "s_waitcnt vmcnt(44)\n"
    "v_mfma_f32_16x16x32_bf16 v[32:35], a[4:7], a[132:135], v[32:35]\n"
    "v_mfma_f32_16x16x32_bf16 v[36:39], a[36:39], a[132:135], v[36:39]\n"
    "v_mfma_f32_16x16x32_bf16 v[40:43], a[8:11], a[132:135], v[40:43]\n"
    "v_mfma_f32_16x16x32_bf16 v[44:47], a[40:43], a[132:135], v[44:47]\n"
    "s_waitcnt lgkmcnt(4)\n"
    "s_waitcnt vmcnt(38)\n"
    "v_mfma_f32_16x16x32_bf16 v[32:35], a[12:15], a[136:139], v[32:35]\n"
    "v_mfma_f32_16x16x32_bf16 v[36:39], a[44:47], a[136:139], v[36:39]\n"
    "v_mfma_f32_16x16x32_bf16 v[40:43], a[16:19], a[136:139], v[40:43]\n"
    "v_mfma_f32_16x16x32_bf16 v[44:47], a[48:51], a[136:139], v[44:47]\n"
    "s_waitcnt vmcnt(17)\n"
    "v_mfma_f32_16x16x32_bf16 v[32:35], a[20:23], a[140:143], v[32:35]\n"
    "v_mfma_f32_16x16x32_bf16 v[36:39], a[52:55], a[140:143], v[36:39]\n"
    "v_mfma_f32_16x16x32_bf16 v[40:43], a[24:27], a[140:143], v[40:43]\n"
    "v_mfma_f32_16x16x32_bf16 v[44:47], a[56:59], a[140:143], v[44:47]\n"
    "s_waitcnt vmcnt(26)\n"
    "v_mfma_f32_16x16x32_bf16 v[208:211], v[154:157], v[48:51], v[208:211]\n"
    "v_mfma_f32_16x16x32_bf16 v[212:215], v[152:155], v[48:51], v[212:215]\n"
    "v_mfma_f32_16x16x32_bf16 v[216:219], v[154:157], v[52:55], v[216:219]\n"
    "v_mfma_f32_16x16x32_bf16 v[220:223], v[152:155], v[52:55], v[220:223]\n"
    "s_waitcnt vmcnt(24)\n"
    "v_mfma_f32_16x16x32_bf16 v[208:211], v[154:157], v[56:59], v[208:211]\n"
    "v_mfma_f32_16x16x32_bf16 v[212:215], v[152:155], v[56:59], v[212:215]\n"
    "v_mfma_f32_16x16x32_bf16 v[216:219], v[154:157], v[60:63], v[216:219]\n"
    "v_mfma_f32_16x16x32_bf16 v[220:223], v[152:155], v[60:63], v[220:223]\n"
    "s_waitcnt lgkmcnt(2)\n"
    "s_waitcnt vmcnt(13)\n"
    "s_waitcnt vmcnt(2)\n"
    "v_cmp_ne_u32_e32 vcc, s39, v144\n"
    "s_nop 3\n"
    "s_cbranch_vccnz .Lat_ypoll\n"
    ".Lat_ypoll_ret:\n"
    "s_add_u32 s61, s46, 57856\n"
    "buffer_load_dwordx4 v[88:91], v253, s[8:11], s61 offen offset:0 sc1\n"
    "buffer_load_dwordx4 v[92:95], v253, s[8:11], s61 offen offset:1024 sc1\n"
    "s_add_u32 s61, s46, 49664\n"
    "buffer_load_dwordx4 v[64:67], v253, s[8:11], s61 offen offset:0 sc1\n"
    "buffer_load_dwordx4 v[68:71], v253, s[8:11], s61 offen offset:1024 sc1\n"
    "s_add_u32 s61, s45, 74240\n"
    "buffer_load_dwordx2 v[244:245], v147, s[8:11], s61 offen offset:0 sc1\n"
    "v_mfma_f32_16x16x32_bf16 v[32:35], a[160:163], a[144:147], v[32:35]\n"
    "v_mfma_f32_16x16x32_bf16 v[36:39], a[164:167], a[144:147], v[36:39]\n"
    "s_waitcnt vmcnt(26)\n"
    "v_mfma_f32_16x16x32_bf16 v[40:43], a[168:171], a[144:147], v[40:43]\n"
    "v_mfma_f32_16x16x32_bf16 v[44:47], a[172:175], a[144:147], v[44:47]\n"
    "s_waitcnt vmcnt(14)\n"
    "v_add_f32_e32 v232, v232, v236\n"
    "v_mfma_f32_16x16x32_bf16 v[32:35], a[176:179], a[148:151], v[32:35]\n"
    "v_add_f32_e32 v233, v233, v237\n"
    "v_mfma_f32_16x16x32_bf16 v[36:39], a[180:183], a[148:151], v[36:39]\n"
    "v_mfma_f32_16x16x32_bf16 v[40:43], a[184:187], a[148:151], v[40:43]\n"
    "v_add_f32_e32 v234, v234, v238\n"
    "v_mfma_f32_16x16x32_bf16 v[44:47], a[188:191], a[148:151], v[44:47]\n"
    "s_waitcnt lgkmcnt(0)\n"
    "s_waitcnt vmcnt(10)\n"
    "v_mfma_f32_16x16x32_bf16 v[32:35], a[224:227], a[152:155], v[32:35]\n"
    "v_add_f32_e32 v232, v232, v248\n"
    "v_mfma_f32_16x16x32_bf16 v[36:39], a[228:231], a[152:155], v[36:39]\n"
    "v_add_f32_e32 v233, v233, v249\n"
    "v_mfma_f32_16x16x32_bf16 v[40:43], a[232:235], a[152:155], v[40:43]\n"
    "v_mfma_f32_16x16x32_bf16 v[44:47], a[236:239], a[152:155], v[44:47]\n"
    "s_waitcnt vmcnt(5)\n"
    "v_add_f32_e32 v234, v234, v250\n"
    "v_mfma_f32_16x16x32_bf16 v[32:35], a[240:243], a[156:159], v[32:35]\n"
    "v_mfma_f32_16x16x32_bf16 v[36:39], a[244:247], a[156:159], v[36:39]\n"
    "v_add_f32_e32 v234, v234, v246\n"
    "v_mfma_f32_16x16x32_bf16 v[40:43], a[248:251], a[156:159], v[40:43]\n"
    "v_rcp_f32_e32 v242, v234\n"
    "v_mfma_f32_16x16x32_bf16 v[44:47], a[252:255], a[156:159], v[44:47]\n"
    "s_add_u32 s48, s55, 16384\n"
    "v_fma_f32 v32, v32, v242, v192\n"
    "v_fma_f32 v33, v33, v242, v193\n"
    "v_fma_f32 v34, v34, v242, v194\n"
    "v_fma_f32 v35, v35, v242, v195\n"
    "v_fma_f32 v36, v36, v242, v196\n"
    "v_fma_f32 v37, v37, v242, v197\n"
    "v_fma_f32 v38, v38, v242, v198\n"
    "v_fma_f32 v39, v39, v242, v199\n"
    "v_cvt_pk_bf16_f32 v32, v32, v33\n"
    "v_cvt_pk_bf16_f32 v33, v34, v35\n"
    "v_cvt_pk_bf16_f32 v34, v36, v37\n"
    "v_cvt_pk_bf16_f32 v35, v38, v39\n"
    "s_waitcnt vmcnt(3)\n"
    "v_mfma_f32_16x16x32_bf16 v[224:227], v[154:157], v[88:91], v[224:227]\n"
    "v_mfma_f32_16x16x32_bf16 v[228:231], v[152:155], v[88:91], v[228:231]\n"
    "v_mfma_f32_16x16x32_bf16 v[120:123], v[154:157], v[92:95], v[120:123]\n"
    "v_mfma_f32_16x16x32_bf16 v[124:127], v[152:155], v[92:95], v[124:127]\n"
    "s_waitcnt vmcnt(1)\n"
    "v_mfma_f32_16x16x32_bf16 v[208:211], v[154:157], v[64:67], v[208:211]\n"
    "v_mfma_f32_16x16x32_bf16 v[212:215], v[152:155], v[64:67], v[212:215]\n"
    "v_mfma_f32_16x16x32_bf16 v[216:219], v[154:157], v[68:71], v[216:219]\n"
    "v_mfma_f32_16x16x32_bf16 v[220:223], v[152:155], v[68:71], v[220:223]\n"
    "v_cvt_pk_bf16_f32 v224, v224, v225\n"
    "v_cvt_pk_bf16_f32 v225, v226, v227\n"
    "v_cvt_pk_bf16_f32 v226, v228, v229\n"
    "v_cvt_pk_bf16_f32 v227, v230, v231\n"
    "ds_write_b128 v158, v[224:227] offset:8192\n"
    "v_cvt_pk_bf16_f32 v120, v120, v121\n"
    "v_cvt_pk_bf16_f32 v121, v122, v123\n"
    "v_cvt_pk_bf16_f32 v122, v124, v125\n"
    "v_cvt_pk_bf16_f32 v123, v126, v127\n"
    "ds_write_b128 v158, v[120:123] offset:9216\n"
    "v_cvt_pk_bf16_f32 v208, v208, v209\n"
    "v_cvt_pk_bf16_f32 v209, v210, v211\n"
    "v_cvt_pk_bf16_f32 v210, v212, v213\n"
    "v_cvt_pk_bf16_f32 v211, v214, v215\n"
    "ds_write_b128 v158, v[208:211] offset:0\n"
    "v_cvt_pk_bf16_f32 v216, v216, v217\n"
    "v_cvt_pk_bf16_f32 v217, v218, v219\n"
    "v_cvt_pk_bf16_f32 v218, v220, v221\n"
    "v_cvt_pk_bf16_f32 v219, v222, v223\n"
    "ds_write_b128 v158, v[216:219] offset:1024\n"
    "s_waitcnt lgkmcnt(0)\n"
    "s_barrier\n"
    "ds_read_b128 a[96:99], v159 offset:8192\n"
    "ds_read_b128 a[100:103], v159 offset:9216\n"
    "ds_read_b128 a[104:107], v159 offset:10240\n"
    "ds_read_b128 a[108:111], v159 offset:11264\n"
    "ds_read_b128 a[112:115], v159 offset:12288\n"
    "ds_read_b128 a[116:119], v159 offset:13312\n"
    "ds_read_b128 a[120:123], v159 offset:14336\n"
    "ds_read_b128 a[124:127], v159 offset:15360\n"
    "s_waitcnt lgkmcnt(6)\n"
    "v_mfma_f32_16x16x32_bf16 v[16:19], a[28:31], a[96:99], 0\n"
    "s_waitcnt vmcnt(0)\n"
    "v_add_f32_e32 v232, v232, v244\n"
    "v_mfma_f32_16x16x32_bf16 v[20:23], a[60:63], a[96:99], 0\n"
    "ds_read_b128 a[64:67], v159 offset:0\n"
    "v_mfma_f32_16x16x32_bf16 v[24:27], a[0:3], a[96:99], 0\n"
    "v_add_f32_e32 v233, v233, v245\n"
    "v_mfma_f32_16x16x32_bf16 v[28:31], a[32:35], a[96:99], 0\n"
    "ds_read_b128 a[68:71], v159 offset:1024\n"
    "v_mfma_f32_16x16x32_bf16 v[16:19], a[4:7], a[100:103], v[16:19]\n"
    "s_bitcmp1_b32 %1, 0\n"
    "s_cselect_b64 vcc, -1, 0\n"
    "v_mfma_f32_16x16x32_bf16 v[20:23], a[36:39], a[100:103], v[20:23]\n"
    "ds_read_b128 a[72:75], v159 offset:2048\n"
    "v_cndmask_b32_e32 v234, v233, v234, vcc\n"
    "v_mfma_f32_16x16x32_bf16 v[24:27], a[8:11], a[100:103], v[24:27]\n"
    "s_mov_b32 s61, 0x12800000\n"
    "v_rcp_f32_e32 v241, v233\n"
    "v_mfma_f32_16x16x32_bf16 v[28:31], a[40:43], a[100:103], v[28:31]\n"
    "s_waitcnt lgkmcnt(7)\n"
    "ds_read_b128 a[76:79], v159 offset:3072\n"
    "v_mfma_f32_16x16x32_bf16 v[16:19], a[12:15], a[104:107], v[16:19]\n"
    "v_rcp_f32_e32 v240, v232\n"
    "v_mfma_f32_16x16x32_bf16 v[20:23], a[44:47], a[104:107], v[20:23]\n"
    "ds_read_b128 a[80:83], v159 offset:4096\n"
    "v_subrev_u32_e32 v236, s61, v232\n"
    "v_mfma_f32_16x16x32_bf16 v[24:27], a[16:19], a[104:107], v[24:27]\n"
    "v_subrev_u32_e32 v237, s61, v233\n"
    "v_mfma_f32_16x16x32_bf16 v[28:31], a[48:51], a[104:107], v[28:31]\n"
    "ds_read_b128 a[84:87], v159 offset:5120\n"
    "v_mfma_f32_16x16x32_bf16 v[16:19], a[20:23], a[108:111], v[16:19]\n"
    "v_subrev_u32_e32 v238, s61, v234\n"
    "v_mfma_f32_16x16x32_bf16 v[20:23], a[52:55], a[108:111], v[20:23]\n"
    "ds_read_b128 a[88:91], v159 offset:6144\n"
    "v_mfma_f32_16x16x32_bf16 v[24:27], a[24:27], a[108:111], v[24:27]\n"
    "v_max3_u32 v236, v236, v237, v238\n"
    "v_mfma_f32_16x16x32_bf16 v[28:31], a[56:59], a[108:111], v[28:31]\n"
    "s_waitcnt lgkmcnt(9)\n"
    "ds_read_b128 a[92:95], v159 offset:7168\n"
    "v_mfma_f32_16x16x32_bf16 v[16:19], a[160:163], a[112:115], v[16:19]\n"
    "buffer_store_dwordx4 v[32:35], v151, s[16:19], s48 offen offset:0 sc0 sc1\n"
    "v_cmp_le_u32_e32 vcc, 0x5f000000, v236\n"
    "v_mfma_f32_16x16x32_bf16 v[20:23], a[164:167], a[112:115], v[20:23]\n"
    "v_fma_f32 v40, v40, v242, v200\n"
    "v_mfma_f32_16x16x32_bf16 v[24:27], a[168:171], a[112:115], v[24:27]\n"
    "v_fma_f32 v41, v41, v242, v201\n"
    "v_mfma_f32_16x16x32_bf16 v[28:31], a[172:175], a[112:115], v[28:31]\n"
    "v_fma_f32 v42, v42, v242, v202\n"
    "v_mfma_f32_16x16x32_bf16 v[16:19], a[176:179], a[116:119], v[16:19]\n"
    "v_fma_f32 v43, v43, v242, v203\n"
    "v_mfma_f32_16x16x32_bf16 v[20:23], a[180:183], a[116:119], v[20:23]\n"
    "v_fma_f32 v44, v44, v242, v204\n"
    "v_mfma_f32_16x16x32_bf16 v[24:27], a[184:187], a[116:119], v[24:27]\n"
    "v_fma_f32 v45, v45, v242, v205\n"
    "v_mfma_f32_16x16x32_bf16 v[28:31], a[188:191], a[116:119], v[28:31]\n"
    "s_waitcnt lgkmcnt(8)\n"
    "v_fma_f32 v46, v46, v242, v206\n"
    "v_mfma_f32_16x16x32_bf16 v[16:19], a[224:227], a[120:123], v[16:19]\n"
    "v_fma_f32 v47, v47, v242, v207\n"
    "v_mfma_f32_16x16x32_bf16 v[20:23], a[228:231], a[120:123], v[20:23]\n"
    "v_cvt_pk_bf16_f32 v40, v40, v41\n"
    "v_mfma_f32_16x16x32_bf16 v[24:27], a[232:235], a[120:123], v[24:27]\n"
    "v_cvt_pk_bf16_f32 v41, v42, v43\n"
    "v_mfma_f32_16x16x32_bf16 v[28:31], a[236:239], a[120:123], v[28:31]\n"
    "v_cvt_pk_bf16_f32 v42, v44, v45\n"
    "v_mfma_f32_16x16x32_bf16 v[16:19], a[240:243], a[124:127], v[16:19]\n"
    "v_cvt_pk_bf16_f32 v43, v46, v47\n"
    "v_mfma_f32_16x16x32_bf16 v[20:23], a[244:247], a[124:127], v[20:23]\n"
    "buffer_store_dwordx4 v[40:43], v151, s[16:19], s48 offen offset:64 sc0 sc1\n"
    "v_mfma_f32_16x16x32_bf16 v[24:27], a[248:251], a[124:127], v[24:27]\n"
    "buffer_store_dword v148, v149, s[8:11], s88 offen offset:0 sc1\n"
    "v_mfma_f32_16x16x32_bf16 v[28:31], a[252:255], a[124:127], v[28:31]\n"
    "s_waitcnt lgkmcnt(6)\n"
    "s_cbranch_vccnz .Lat_exit\n"
    "v_mfma_f32_16x16x32_bf16 v[0:3], a[28:31], a[64:67], 0\n"
    "s_add_u32 s48, s58, 8192\n"
    "v_fma_f32 v16, v16, v241, v176\n"
    "v_mfma_f32_16x16x32_bf16 v[4:7], a[60:63], a[64:67], 0\n"
    "v_fma_f32 v17, v17, v241, v177\n"
    "v_mfma_f32_16x16x32_bf16 v[8:11], a[0:3], a[64:67], 0\n"
    "v_fma_f32 v18, v18, v241, v178\n"
    "v_mfma_f32_16x16x32_bf16 v[12:15], a[32:35], a[64:67], 0\n"
    "v_fma_f32 v19, v19, v241, v179\n"
    "v_mfma_f32_16x16x32_bf16 v[0:3], a[4:7], a[68:71], v[0:3]\n"
    "v_fma_f32 v20, v20, v241, v180\n"
    "v_mfma_f32_16x16x32_bf16 v[4:7], a[36:39], a[68:71], v[4:7]\n"
    "v_fma_f32 v21, v21, v241, v181\n"
    "v_mfma_f32_16x16x32_bf16 v[8:11], a[8:11], a[68:71], v[8:11]\n"
    "v_fma_f32 v22, v22, v241, v182\n"
    "v_mfma_f32_16x16x32_bf16 v[12:15], a[40:43], a[68:71], v[12:15]\n"
    "s_waitcnt lgkmcnt(4)\n"
    "v_fma_f32 v23, v23, v241, v183\n"
    "v_mfma_f32_16x16x32_bf16 v[0:3], a[12:15], a[72:75], v[0:3]\n"
    "v_cvt_pk_bf16_f32 v16, v16, v17\n"
    "v_mfma_f32_16x16x32_bf16 v[4:7], a[44:47], a[72:75], v[4:7]\n"
    "v_cvt_pk_bf16_f32 v17, v18, v19\n"
    "v_mfma_f32_16x16x32_bf16 v[8:11], a[16:19], a[72:75], v[8:11]\n"
    "v_cvt_pk_bf16_f32 v18, v20, v21\n"
    "v_mfma_f32_16x16x32_bf16 v[12:15], a[48:51], a[72:75], v[12:15]\n"
    "v_cvt_pk_bf16_f32 v19, v22, v23\n"
    "v_mfma_f32_16x16x32_bf16 v[0:3], a[20:23], a[76:79], v[0:3]\n"
    "buffer_store_dwordx4 v[16:19], v151, s[16:19], s48 offen offset:0 sc0 sc1\n"
    "v_fma_f32 v24, v24, v241, v184\n"
    "v_mfma_f32_16x16x32_bf16 v[4:7], a[52:55], a[76:79], v[4:7]\n"
    "v_fma_f32 v25, v25, v241, v185\n"
    "v_mfma_f32_16x16x32_bf16 v[8:11], a[24:27], a[76:79], v[8:11]\n"
    "v_fma_f32 v26, v26, v241, v186\n"
    "v_mfma_f32_16x16x32_bf16 v[12:15], a[56:59], a[76:79], v[12:15]\n"
    "s_waitcnt lgkmcnt(2)\n"
    "v_fma_f32 v27, v27, v241, v187\n"
    "v_mfma_f32_16x16x32_bf16 v[0:3], a[160:163], a[80:83], v[0:3]\n"
    "v_fma_f32 v28, v28, v241, v188\n"
    "v_mfma_f32_16x16x32_bf16 v[4:7], a[164:167], a[80:83], v[4:7]\n"
    "v_fma_f32 v29, v29, v241, v189\n"
    "v_mfma_f32_16x16x32_bf16 v[8:11], a[168:171], a[80:83], v[8:11]\n"
    "v_fma_f32 v30, v30, v241, v190\n"
    "v_mfma_f32_16x16x32_bf16 v[12:15], a[172:175], a[80:83], v[12:15]\n"
    "v_fma_f32 v31, v31, v241, v191\n"
    "v_mfma_f32_16x16x32_bf16 v[0:3], a[176:179], a[84:87], v[0:3]\n"
    "v_cvt_pk_bf16_f32 v24, v24, v25\n"
    "v_cvt_pk_bf16_f32 v25, v26, v27\n"
    "v_mfma_f32_16x16x32_bf16 v[4:7], a[180:183], a[84:87], v[4:7]\n"
    "v_cvt_pk_bf16_f32 v26, v28, v29\n"
    "v_cvt_pk_bf16_f32 v27, v30, v31\n"
    "v_mfma_f32_16x16x32_bf16 v[8:11], a[184:187], a[84:87], v[8:11]\n"
    "buffer_store_dwordx4 v[24:27], v151, s[16:19], s48 offen offset:64 sc0 sc1\n"
    "v_mfma_f32_16x16x32_bf16 v[12:15], a[188:191], a[84:87], v[12:15]\n"
    "s_waitcnt lgkmcnt(0)\n"
    "v_mfma_f32_16x16x32_bf16 v[0:3], a[224:227], a[88:91], v[0:3]\n"
    "v_mfma_f32_16x16x32_bf16 v[4:7], a[228:231], a[88:91], v[4:7]\n"
    "v_mfma_f32_16x16x32_bf16 v[8:11], a[232:235], a[88:91], v[8:11]\n"
    "v_mfma_f32_16x16x32_bf16 v[12:15], a[236:239], a[88:91], v[12:15]\n"
    "v_mfma_f32_16x16x32_bf16 v[0:3], a[240:243], a[92:95], v[0:3]\n"
    "v_mfma_f32_16x16x32_bf16 v[4:7], a[244:247], a[92:95], v[4:7]\n"
    "v_mfma_f32_16x16x32_bf16 v[8:11], a[248:251], a[92:95], v[8:11]\n"
    "v_mfma_f32_16x16x32_bf16 v[12:15], a[252:255], a[92:95], v[12:15]\n"
    "s_add_u32 s48, s58, 0\n"
    "v_fma_f32 v0, v0, v240, v160\n"
    "v_fma_f32 v1, v1, v240, v161\n"
    "v_fma_f32 v2, v2, v240, v162\n"
    "v_fma_f32 v3, v3, v240, v163\n"
    "v_fma_f32 v4, v4, v240, v164\n"
    "v_fma_f32 v5, v5, v240, v165\n"
    "v_fma_f32 v6, v6, v240, v166\n"
    "v_fma_f32 v7, v7, v240, v167\n"
    "v_cvt_pk_bf16_f32 v0, v0, v1\n"
    "v_cvt_pk_bf16_f32 v1, v2, v3\n"
    "v_cvt_pk_bf16_f32 v2, v4, v5\n"
    "v_cvt_pk_bf16_f32 v3, v6, v7\n"
    "buffer_store_dwordx4 v[0:3], v151, s[16:19], s48 offen offset:0 sc0 sc1\n"
    "v_fma_f32 v8, v8, v240, v168\n"
    "v_fma_f32 v9, v9, v240, v169\n"
    "v_fma_f32 v10, v10, v240, v170\n"
    "v_fma_f32 v11, v11, v240, v171\n"
    "v_fma_f32 v12, v12, v240, v172\n"
    "v_fma_f32 v13, v13, v240, v173\n"
    "v_fma_f32 v14, v14, v240, v174\n"
    "v_fma_f32 v15, v15, v240, v175\n"
    "v_cvt_pk_bf16_f32 v8, v8, v9\n"
    "v_cvt_pk_bf16_f32 v9, v10, v11\n"
    "v_cvt_pk_bf16_f32 v10, v12, v13\n"
    "v_cvt_pk_bf16_f32 v11, v14, v15\n"
    "buffer_store_dwordx4 v[8:11], v151, s[16:19], s48 offen offset:64 sc0 sc1\n"
    "s_endpgm\n"
    ".Lat_wpoll:\n"
    "s_mov_b32 s53, 0x400000\n"
    ".Lat_wpoll_l:\n"
    "s_sleep 1\n"
    "buffer_load_dword v255, v254, s[8:11], s45 offen offset:0 sc1\n"
    "s_waitcnt vmcnt(0)\n"
    "v_cmp_ne_u32_e32 vcc, s39, v255\n"
    "s_sub_u32 s53, s53, 1\n"
    "s_cbranch_scc1 .Lat_wpoll_ret\n"
    "s_nop 2\n"
    "s_cbranch_vccnz .Lat_wpoll_l\n"
    "s_branch .Lat_wpoll_ret\n"
    ".Lat_xpoll:\n"
    "s_mov_b32 s53, 0x400000\n"
    ".Lat_xpoll_l:\n"
    "s_sleep 0\n"
    "buffer_load_dword v146, v145, s[8:11], s47 offen offset:32 sc1\n"
    "s_waitcnt vmcnt(0)\n"
    "v_cmp_ne_u32_e32 vcc, s39, v146\n"
    "s_sub_u32 s53, s53, 1\n"
    "s_cbranch_scc1 .Lat_xpoll_ret\n"
    "s_nop 2\n"
    "s_cbranch_vccnz .Lat_xpoll_l\n"
    "s_branch .Lat_xpoll_ret\n"
    ".Lat_ypoll:\n"
    "s_mov_b32 s53, 0x400000\n"
    ".Lat_ypoll_l:\n"
    "s_sleep 0\n"
    "buffer_load_dword v144, v145, s[8:11], s52 offen offset:0 sc1\n"
    "s_waitcnt vmcnt(0)\n"
    "v_cmp_ne_u32_e32 vcc, s39, v144\n"
    "s_sub_u32 s53, s53, 1\n"
    "s_cbranch_scc1 .Lat_ypoll_ret\n"
    "s_nop 2\n"
    "s_cbranch_vccnz .Lat_ypoll_l\n"
    "s_branch .Lat_ypoll_ret\n"
    ".Lat_apoll:\n"
    "s_mov_b32 s53, 0x400000\n"
    ".Lat_apoll_l:\n"
    "s_sleep 1\n"
    "buffer_load_dword v255, v252, s[8:11], 0 offen offset:0 sc1\n"
    "s_waitcnt vmcnt(0)\n"
    "v_cmp_ne_u32_e32 vcc, s39, v255\n"
    "s_sub_u32 s53, s53, 1\n"
    "s_cbranch_scc1 .Lat_apoll_ret\n"
    "s_nop 2\n"
    "s_cbranch_vccnz .Lat_apoll_l\n"
    "s_branch .Lat_apoll_ret\n"
    ".Lat_h1poll:\n"
    "s_mov_b32 s53, 0x400000\n"
    ".Lat_h1poll_l:\n"
    "s_sleep 1\n"
    "buffer_load_dword v0, v3, s[8:11], s59 offen offset:0 sc1\n"
    "s_waitcnt vmcnt(0)\n"
    "v_cmp_ne_u32_e32 vcc, s39, v0\n"
    "s_sub_u32 s53, s53, 1\n"
    "s_cbranch_scc1 .Lat_h1poll_ret\n"
    "s_nop 2\n"
    "s_cbranch_vccnz .Lat_h1poll_l\n"
    "s_branch .Lat_h1poll_ret\n"
    ".Lat_hpoll:\n"
    "s_mov_b32 s53, 0x400000\n"
    ".Lat_hpoll_l:\n"
    "s_sleep 1\n"
    "buffer_load_dword v0, v253, s[8:11], s59 offen offset:0 sc1\n"
    "buffer_load_dword v1, v2, s[8:11], s59 offen offset:0 sc1\n"
    "s_waitcnt vmcnt(0)\n"
    "v_xor_b32_e32 v1, s39, v1\n"
    "v_xor_b32_e32 v0, s39, v0\n"
    "v_or_b32_e32 v0, v0, v1\n"
    "v_cmp_ne_u32_e32 vcc, 0, v0\n"
    "s_sub_u32 s53, s53, 1\n"
    "s_cbranch_scc1 .Lat_hpoll_ret\n"
    "s_nop 2\n"
    "s_cbranch_vccnz .Lat_hpoll_l\n"
    "s_branch .Lat_hpoll_ret\n"
    ".Lat_qpoll:\n"
    "s_mov_b32 s53, 0x400000\n"
    ".Lat_qpoll_l:\n"
    "s_sleep 0\n"
    "buffer_load_dword v11, v5, s[8:11], s47 offen offset:0 sc1\n"
    "s_waitcnt vmcnt(0)\n"
    "v_cmp_ne_u32_e32 vcc, s39, v11\n"
    "s_sub_u32 s53, s53, 1\n"
    "s_cbranch_scc1 .Lat_qpoll_ret\n"
    "s_nop 2\n"
    "s_cbranch_vccnz .Lat_qpoll_l\n"
    "s_branch .Lat_qpoll_ret\n"
    ".Lat_lpoll:\n"
    "s_mov_b32 s53, 0x400000\n"
    ".Lat_lpoll_l:\n"
    "s_sleep 1\n"
    "buffer_load_dword v154, off, s[8:11], s80 sc1\n"
    "s_waitcnt vmcnt(0)\n"
    "v_cmp_ne_u32_e32 vcc, s39, v154\n"
    "s_sub_u32 s53, s53, 1\n"
    "s_cbranch_scc1 .Lat_lpoll_ret\n"
    "s_nop 2\n"
    "s_cbranch_vccnz .Lat_lpoll_l\n"
    ".Lat_lpoll_ret:\n"
    "v_lshlrev_b32_e32 v253, 2, v249\n"
    "s_mov_b32 m0, s82\n"
    "s_nop 0\n"
    "buffer_load_dwordx4 v253, s[8:11], s81 offen offset:0 sc1 lds\n"
    "s_waitcnt vmcnt(0)\n"
    "s_branch .Lat_g\n"
    ".Lat_exit:\n"
    "s_waitcnt vmcnt(0)\n"
    "s_nop 15\n"
    ".Lat_end:\n"
    "s_waitcnt lgkmcnt(0)\n"
    : "+s"(ka), "+s"(b), "+s"(wid) : "s"((unsigned)(size_t)lds)
    : "v0", "v1", "v2", "v3", "v4", "v5", "v6", "v7", "v8", "v9", "v10", "v11", "v12", "v13", "v14", "v15", "v16", "v17", "v18", "v19", "v20", "v21", "v22", "v23", "v24", "v25", "v26", "v27", "v28", "v29", "v30", "v31", "v32", "v33", "v34", "v35", "v36", "v37", "v38", "v39", "v40", "v41", "v42", "v43", "v44", "v45", "v46", "v47", "v48", "v49", "v50", "v51", "v52", "v53", "v54", "v55", "v56", "v57", "v58", "v59", "v60", "v61", "v62", "v63", "v64", "v65", "v66", "v67", "v68", "v69", "v70", "v71", "v72", "v73", "v74", "v75", "v76", "v77", "v78", "v79", "v80", "v81", "v82", "v83", "v84", "v85", "v86", "v87", "v88", "v89", "v90", "v91", "v92", "v93", "v94", "v95", "v96", "v97", "v98", "v99", "v100", "v101", "v102", "v103", "v104", "v105", "v106", "v107", "v108", "v109", "v110", "v111", "v112", "v113", "v114", "v115", "v116", "v117", "v118", "v119", "v120", "v121", "v122", "v123", "v124", "v125", "v126", "v127", "v128", "v129", "v130", "v131", "v132", "v133", "v134", "v135", "v136", "v137", "v138", "v139", "v140", "v141", "v142", "v143", "v144", "v145", "v146", "v147", "v148", "v149", "v150", "v151", "v152", "v153", "v154", "v155", "v156", "v157", "v158", "v159", "v160", "v161", "v162", "v163", "v164", "v165", "v166", "v167", "v168", "v169", "v170", "v171", "v172", "v173", "v174", "v175", "v176", "v177", "v178", "v179", "v180", "v181", "v182", "v183", "v184", "v185", "v186", "v187", "v188", "v189", "v190", "v191", "v192", "v193", "v194", "v195", "v196", "v197", "v198", "v199", "v200", "v201", "v202", "v203", "v204", "v205", "v206", "v207", "v208", "v209", "v210", "v211", "v212", "v213", "v214", "v215", "v216", "v217", "v218", "v219", "v220", "v221", "v222", "v223", "v224", "v225", "v226", "v227", "v228", "v229", "v230", "v231", "v232", "v233", "v234", "v235", "v236", "v237", "v238", "v239", "v240", "v241", "v242", "v243", "v244", "v245", "v246", "v247", "v248", "v249", "v250", "v251", "v252", "v253", "v254", "v255", "a0", "a1", "a2", "a3", "a4", "a5", "a6", "a7", "a8", "a9", "a10", "a11", "a12", "a13", "a14", "a15", "a16", "a17", "a18", "a19", "a20", "a21", "a22", "a23", "a24", "a25", "a26", "a27", "a28", "a29", "a30", "a31", "a32", "a33", "a34", "a35", "a36", "a37", "a38", "a39", "a40", "a41", "a42", "a43", "a44", "a45", "a46", "a47", "a48", "a49", "a50", "a51", "a52", "a53", "a54", "a55", "a56", "a57", "a58", "a59", "a60", "a61", "a62", "a63", "a64", "a65", "a66", "a67", "a68", "a69", "a70", "a71", "a72", "a73", "a74", "a75", "a76", "a77", "a78", "a79", "a80", "a81", "a82", "a83", "a84", "a85", "a86", "a87", "a88", "a89", "a90", "a91", "a92", "a93", "a94", "a95", "a96", "a97", "a98", "a99", "a100", "a101", "a102", "a103", "a104", "a105", "a106", "a107", "a108", "a109", "a110", "a111", "a112", "a113", "a114", "a115", "a116", "a117", "a118", "a119", "a120", "a121", "a122", "a123", "a124", "a125", "a126", "a127", "a128", "a129", "a130", "a131", "a132", "a133", "a134", "a135", "a136", "a137", "a138", "a139", "a140", "a141", "a142", "a143", "a144", "a145", "a146", "a147", "a148", "a149", "a150", "a151", "a152", "a153", "a154", "a155", "a156", "a157", "a158", "a159", "a160", "a161", "a162", "a163", "a164", "a165", "a166", "a167", "a168", "a169", "a170", "a171", "a172", "a173", "a174", "a175", "a176", "a177", "a178", "a179", "a180", "a181", "a182", "a183", "a184", "a185", "a186", "a187", "a188", "a189", "a190", "a191", "a192", "a193", "a194", "a195", "a196", "a197", "a198", "a199", "a200", "a201", "a202", "a203", "a204", "a205", "a206", "a207", "a208", "a209", "a210", "a211", "a212", "a213", "a214", "a215", "a216", "a217", "a218", "a219", "a220", "a221", "a222", "a223", "a224", "a225", "a226", "a227", "a228", "a229", "a230", "a231", "a232", "a233", "a234", "a235", "a236", "a237", "a238", "a239", "a240", "a241", "a242", "a243", "a244", "a245", "a246", "a247", "a248", "a249", "a250", "a251", "a252", "a253", "a254", "a255", "s8", "s9", "s10", "s11", "s12", "s13", "s14", "s15", "s16", "s17", "s18", "s19", "s20", "s21", "s22", "s23", "s24", "s25", "s26", "s27", "s28", "s29", "s30", "s31", "s32", "s33", "s34", "s35", "s36", "s37", "s38", "s39", "s40", "s41", "s42", "s43", "s44", "s45", "s46", "s47", "s48", "s49", "s50", "s51", "s52", "s53", "s54", "s55", "s56", "s57", "s58", "s59", "s60", "s61", "s62", "s63", "s80", "s81", "s82", "s83", "s84", "s85", "s86", "s87", "s88", "s89", "s90", "vcc", "m0", "scc", "memory");
  typedef __attribute__((address_space(1))) char* __attribute__((address_space(4))) const* arg_t;
  const arg_t ap = (arg_t)ka;
  typedef const u16* cu16;
  if (b >= NATT) list_branch(lds, b - NATT, wid, (const int*)ap[1], (const int*)ap[2], (cu16)ap[3], (cu16)ap[8], (cu16)ap[9], (cu16)ap[10], (char*)ap[0]);
  else exact_rows(lds, b, wid, (char*)ap[0], (cu16)ap[4], (u16*)ap[5]);
}

extern "C" void kernel_launch(void* const* d_in, const int* in_sizes, int n_in, void* d_out, int out_size, void* d_ws,
                              size_t ws_size, hipStream_t stream) {
  const u16* x = (const u16*)d_in[0];
  const int* src = (const int*)d_in[1];
  const int* dst = (const int*)d_in[2];
  const u16* deg_inv = (const u16*)d_in[3];
  const u16* wmsg = (const u16*)d_in[4];
  const u16* wq = (const u16*)d_in[5];
  const u16* wk = (const u16*)d_in[6];
  const u16* wv = (const u16*)d_in[7];
  const u16* wout = (const u16*)d_in[8];
  char* ws = (char*)d_ws;
  (void)hipFuncSetAttribute((const void*)attn_kernel, hipFuncAttributeMaxDynamicSharedMemorySize, 163840);
  attn_kernel<<<256, 256, 163840, stream>>>(ws, src, dst, deg_inv, x, (u16*)d_out, wq, wk, wv, wmsg, wout);
}
```

```cpp
#include <hip/hip_runtime.h>
#include <hip/hip_bf16.h>
#include <stdint.h>
#pragma clang diagnostic ignored "-Winline-asm"

typedef unsigned short u16;
typedef __attribute__((ext_vector_type(8))) short bf16x8;
typedef __attribute__((ext_vector_type(4))) short bf16x4;
typedef __attribute__((ext_vector_type(4))) float f32x4;
typedef __attribute__((ext_vector_type(2))) float f32x2;
typedef __attribute__((ext_vector_type(4))) unsigned u32x4;
typedef __attribute__((ext_vector_type(2))) unsigned u32x2;
#define DI __device__ __forceinline__

constexpr int NN = 10000, DD = 256, EE = 160000;
constexpr int NATT = 252, NSP = 4;

constexpr unsigned WS_H = 0;
constexpr unsigned WS_AF = WS_H + 10240 * 512;
constexpr unsigned WS_WV = WS_AF + 131072;
constexpr unsigned WS_WM = WS_WV + 131072;
constexpr unsigned WS_XF = WS_WM + 131072;
constexpr unsigned WS_HF = WS_XF + 256 * 64;
constexpr unsigned WS_BF = WS_HF + 4096;
constexpr unsigned WS_PF = WS_BF + 32768;
constexpr unsigned WS_WF = WS_PF + 4096;
constexpr unsigned WS_LF = WS_WF + 256;
constexpr unsigned WS_EP = WS_LF + 2048;
constexpr unsigned WS_G = WS_EP + 1792;
constexpr unsigned WS_QP = WS_G + 10240 * 512;
constexpr unsigned WS_XB = WS_QP + 10240 * 512;
constexpr unsigned WS_BL = WS_XB + NATT * 3 * 24832;
constexpr unsigned WS_GL = WS_BL + 16 * 5 * 260 * 64;
constexpr unsigned WS_DBG = WS_GL + 256 * 4096;
constexpr unsigned WS_END = WS_DBG + 1024 * 128 + 8192;

DI float bf2f(unsigned x) { return __uint_as_float(x << 16); }
DI float bfhi(unsigned x) { return __uint_as_float(x & 0xffff0000u); }
DI unsigned pk2(float lo, float hi) {
  typedef __attribute__((ext_vector_type(2))) __bf16 bf2_t;
  const bf2_t r = __builtin_convertvector((f32x2){lo, hi}, bf2_t);
  return __builtin_bit_cast(unsigned, r);
}
typedef __attribute__((address_space(3))) bf16x4* lds_bf16x4_p;
DI bf16x4 tr_read(unsigned p) { return __builtin_amdgcn_ds_read_tr16_b64_v4i16((lds_bf16x4_p)p); }
DI bf16x8 as_bf(u32x4 v) { return __builtin_bit_cast(bf16x8, v); }
#define SB() __builtin_amdgcn_sched_barrier(0)
#define MFMA16(a, b, c) __builtin_amdgcn_mfma_f32_16x16x32_bf16(a, b, c, 0, 0, 0)

typedef __amdgpu_buffer_rsrc_t srd_t;
DI void drain() { asm volatile("s_waitcnt vmcnt(0)" ::: "memory"); }
DI void st_flag(srd_t w, unsigned off, unsigned tag) { __builtin_amdgcn_raw_buffer_store_b32(tag, w, off, 0, 16); }
DI void wait_flag(srd_t w, unsigned off, unsigned tag) {
#pragma unroll 1
  for (int it = 0; it < (1 << 22); ++it) {
    unsigned v;
    asm volatile("buffer_load_dword %0, %1, %2, 0 offen sc1\n s_waitcnt vmcnt(0)" : "=v"(v) : "v"(off), "s"(w) : "memory");
    if (__ballot(v != tag) == 0) break;
    __builtin_amdgcn_s_sleep(2);
    asm volatile("" ::: "memory");
  }
  asm volatile("" ::: "memory");
}

DI void prod_store(const f32x4 (&acc)[2][2], int mat, int ks, int J, int lane, srd_t wsrd) {
#pragma unroll
  for (int s = 0; s < 2; ++s) {
    u32x4 o = {pk2(acc[0][s][0], acc[0][s][1]), pk2(acc[0][s][2], acc[0][s][3]), pk2(acc[1][s][0], acc[1][s][1]), pk2(acc[1][s][2], acc[1][s][3])};
    if (ks == 7 && lane >= 48) o[3] &= 0xffffu;
    const int T = 2 * J + s;
    const int at = mat ? 131072 + ((J & 1) ^ 1) * 16384 + ks * 2048 + s * 1024 : ks * 4096 + (T & 3) * 1024;
    __builtin_amdgcn_raw_buffer_store_b128(o, wsrd, WS_WV + (T >> 2) * 32768 + at + lane * 16, 0, 16);
  }
}
DI void corr(unsigned* dl, const unsigned (&lr)[16]) {
  float a0 = 0.f, a1 = 0.f;
#pragma unroll
  for (int r = 0; r < 16; ++r) { a0 += bf2f(lr[r] & 0xffffu); a1 += bfhi(lr[r]); }
  dl[0] = pk2(a0 * -0.0625f, a1 * -0.0625f);
  dl[128] = pk2((bf2f(lr[15] & 0xffffu) - 0.25f * a0) * (1.f / 3.f), (bfhi(lr[15]) - 0.25f * a1) * (1.f / 3.f));
}
DI void product_load(char* lds, int wid, int lane, int job, const u16* __restrict__ wv, const u16* __restrict__ wm, const u16* __restrict__ wo, bool left, bool right) {
  const unsigned ldsb = (unsigned)(size_t)lds;
  const int r = lane >> 3, sw = ((lane & 7) ^ (r & 6)) * 16 + wid * 128;
  const unsigned vp = (8 * (r >> 2) + (r & 3)) * 512 + sw, vn = r * 512 + sw;
  auto image = [&](int n, const u16* w, int row0, bool nat) {
#pragma unroll
    for (int i = 0; i < 8; ++i) {
      const int a = i & 3;
      asm volatile("s_mov_b32 m0, %2\n s_nop 0\n global_load_lds_dwordx4 %0, %1"
                   :: "v"(nat ? vn : vp), "s"(w + (row0 + (i >> 2) * 32 + (nat ? 8 * a : 16 * (a & 1) + 4 * (a >> 1))) * DD),
                      "s"(ldsb + n * 32768 + (i >> 2) * 16384 + wid * 4096 + a * 1024) : "m0", "memory");
    }
  };
  if (left) {
    image(2, wv, (job >> 2) * 64, true);
    image(3, wm, (job >> 2) * 64, false);
  }
  if (right)
#pragma unroll
  for (int k = 0; k < 8; ++k)
    asm volatile("s_mov_b32 m0, %2\n s_nop 0\n global_load_lds_dwordx4 %0, %1"
                 :: "v"((lane >> 3) * 512 + (lane & 7) * 16), "s"(wo + (k * 32 + wid * 8) * DD + (job & 3) * 64), "s"(ldsb + k * 4096 + wid * 1024) : "m0", "memory");
}
DI void product_w(char* lds, int wid, int lane, int job, srd_t wsrd) {
  const int m = lane & 15, g = lane >> 4;
  const int ks = 2 * (job >> 2) + (wid >> 1), J = 2 * (job & 3) + (wid & 1);
  const unsigned ldsb = (unsigned)(size_t)lds;
  unsigned* dl = reinterpret_cast<unsigned*>(lds + 0x26c00);
  const bool hi = (m >> 3) != 0;
  const unsigned ra = (m >> 3) * 1024 + (m & 7) * 128 + (g ^ (m & 6)) * 16;
  auto frag = [&](int n, int tl, int t, int k) {
    return *reinterpret_cast<const bf16x8*>(lds + n * 32768 + tl * 16384 + (k >> 1) * 4096 + t * 2048 + (ra ^ ((k & 1) * 64)));
  };
  f32x4 av[2][2] = {}, am[2][2] = {}, ad[2] = {};
  const unsigned tb = ldsb + (job & 1) * 32768 + (8 * g + ((lane >> 2) & 3)) * 128 + (wid & 1) * 64 + (lane & 3) * 16;
  bf16x8 d0[8], d1[8], d2[8], rr[8][2], vf[8][2], mf[8][2];
#pragma unroll
  for (int kk = 0; kk < 9; ++kk) {
    if (kk < 8) {
      d0[kk] = *reinterpret_cast<const bf16x8*>(dl + 512 + 16 * kk + 4 * g); d1[kk] = *reinterpret_cast<const bf16x8*>(dl + 512 + (ks == 7 ? 128 : 0) + 16 * kk + 4 * g);
      d2[kk] = *reinterpret_cast<const bf16x8*>(dl + 768 + (ks == 7 && hi ? 128 : 0) + 16 * kk + 4 * g);
#pragma unroll
      for (int s = 0; s < 2; ++s) {
        rr[kk][s] = __builtin_shufflevector(tr_read(tb + kk * 4096 + s * 8), tr_read(tb + kk * 4096 + s * 8 + 512), 0, 1, 2, 3, 4, 5, 6, 7);
        vf[kk][s] = frag(2, wid >> 1, s, kk); mf[kk][s] = frag(3, wid >> 1, s, kk);
      }
    }
    SB();
    if (kk) {
      const int k = kk - 1;
#pragma unroll
      for (int s = 0; s < 2; ++s) {
        av[0][s] = MFMA16(d0[k], rr[k][s], av[0][s]); av[1][s] = MFMA16(d1[k], rr[k][s], av[1][s]); ad[s] = MFMA16(d2[k], rr[k][s], ad[s]);
#pragma unroll
        for (int t = 0; t < 2; ++t) { av[t][s] = MFMA16(vf[k][t], rr[k][s], av[t][s]); am[t][s] = MFMA16(mf[k][t], rr[k][s], am[t][s]); }
      }
    }
    SB();
  }
#pragma unroll
  for (int t = 0; t < 2; ++t)
#pragma unroll
    for (int s = 0; s < 2; ++s) am[t][s] += ad[s];
  prod_store(av, 0, ks, J, lane, wsrd);
  prod_store(am, 1, ks, J, lane, wsrd);
}

constexpr unsigned CODE_BYTES = 84480;
DI void code_touch(unsigned i, unsigned ldsdst) {
  unsigned long long a; int o_;
  asm volatile("s_getpc_b64 %0\n s_mov_b32 %1, _Z11attn_kernelPcPKiS1_PKtS3_PtS3_S3_S3_S3_S3_@rel32@lo+4" : "=&s"(a), "=&s"(o_));
  a += (long long)o_;
  asm volatile("s_mov_b32 m0, %2\n s_nop 0\n global_load_lds_dword %0, %1" :: "v"(min(i * 128u, CODE_BYTES - 4u)), "s"(a), "s"(ldsdst) : "m0", "memory");
}

constexpr int LONGS = 1056;
typedef __attribute__((address_space(3))) unsigned* lds_u32_p;
typedef __attribute__((address_space(3))) u16* lds_u16_p;
DI void local_store(srd_t srd, int node, int lane, f32x4 acc, float di) {
  const int n = lane & 15, g = lane >> 4;
  const u32x2 o = {pk2(acc[0] * di, acc[1] * di), pk2(acc[2] * di, acc[3] * di)};
  __builtin_amdgcn_raw_buffer_store_b64(o, srd, WS_G + node * 512 + (g >> 1) * 256 + n * 16 + (g & 1) * 8, 0, 16);
}
DI void list_branch(char* lds, int j, int wid, const int* __restrict__ src, const int* __restrict__ dst, const u16* __restrict__ deg_inv,
                    const u16* wv, const u16* wm, const u16* wo, char* ws) {
  const unsigned rec = (unsigned)(size_t)lds + wid * 33280, ext = (unsigned)(size_t)lds + 0x20800 + wid * 6912;
  int lane; asm volatile("v_mbcnt_lo_u32_b32 %0, -1, 0\n\tv_mbcnt_hi_u32_b32 %0, -1, %0" : "=v"(lane));
  const int tid = wid * 64 + lane, bin = j * 4 + wid, lo = bin * 640;
  code_touch(CODE_BYTES / 128 - tid, (unsigned)(size_t)lds + 0x27f00u);
  const srd_t msrd = __builtin_amdgcn_make_buffer_rsrc(ws + WS_H, 0, 10240 * 512, 0x00020000);
  const srd_t wsrd = __builtin_amdgcn_make_buffer_rsrc(ws, 0, WS_END, 0x00020000);
  const unsigned tag = __builtin_amdgcn_raw_buffer_load_b32(wsrd, WS_EP, 0, 0) + 2u;
  unsigned lr[16];
#pragma unroll
  for (int r = 0; r < 16; ++r) lr[r] = reinterpret_cast<const unsigned*>((wid >= 2 ? wm : wv) + (240 + r) * DD)[tid & 127];
  unsigned dv[10];
#pragma unroll
  for (int i = 0; i < 10; ++i) dv[i] = (unsigned)deg_inv[min(lo + 64 * i + lane, NN - 1)];
#pragma unroll
  for (int i = 0; i < 10; ++i) ((lds_u32_p)ext)[64 * i + lane] = dv[i] << 16 | (lo + 64 * i + lane < NN ? lo + 64 * i + lane : 0xffff);
  corr(reinterpret_cast<unsigned*>(lds + 0x26c00) + 512 + (wid >> 1) * 256 + (tid & 127), lr);
  const u32x4 none = {~0u, ~0u, ~0u, ~0u};
  auto clear = [&]() {
    unsigned a = rec + lane * 16, r = rec + lane * 52;
    asm volatile("" : "+v"(a), "+v"(r));
#pragma unroll
    for (int i = 0; i < 33; ++i) *(__attribute__((address_space(3))) u32x4*)(a + (i < 32 ? i * 1024 : 32256)) = none;
#pragma unroll
    for (int i = 0; i < 10; ++i) ((lds_u32_p)(r + 64 * 52 * i))[12] = 46;
  };
  clear();
  wait_flag(wsrd, WS_BF + min(tid, NATT - 1) * 128, tag);
  __syncthreads();
  auto take = [&](const u32x4& e0, const u32x4& e1, unsigned (&a)[8], unsigned (&p)[8]) {
#pragma unroll
    for (int r = 0; r < 8; ++r) asm("v_mad_u32_u16 %0, %1, 52, %2 op_sel:[1,0,0,0]" : "=v"(a[r]) : "v"(r < 4 ? e0[r & 3] : e1[r & 3]), "s"(rec));
    SB();
#pragma unroll
    for (int r = 0; r < 8; ++r) p[r] = __atomic_fetch_add((lds_u32_p)a[r] + 12, -2u, __ATOMIC_RELAXED);
    SB();
  };
  auto fill = [&](const u32x4& e0, const u32x4& e1, unsigned (&a)[8], const unsigned (&p)[8]) {
#pragma unroll
    for (int r = 7; r >= 0; --r) a[r] = __builtin_elementwise_add_sat(a[r], p[r]);
    SB();
#pragma unroll
    for (int r = 7; r >= 0; --r) *(lds_u16_p)a[r] = r < 4 ? e0[r & 3] : e1[r & 3];
    SB();
  };
  auto add8 = [&](u32x4 e0, u32x4 e1) { unsigned a[8], p[8]; take(e0, e1, a, p); fill(e0, e1, a, p); };
  const unsigned bo = WS_BL + bin * 5 * 260 * 64 + lane * 16;
  auto ld = [&](int g, int k) { return __builtin_amdgcn_raw_buffer_load_b128(wsrd, bo + (g * 260 + k * 16) * 64, 0, 16); };
  auto fix = [&](u32x4 e, unsigned last) { return e | (unsigned)(__builtin_amdgcn_mov_dpp(last, 0xff, 15, 15, 0) >> 31); };
  bool ovf = false;
  unsigned big = 0; int off[10], tot = 0;
#pragma unroll 1
  for (int pass = 0; pass < 2; ++pass) {
    if (!ovf) {
      u32x4 c[64];
#pragma unroll
      for (int k = 0; k < 16; ++k) c[k] = ld(0, k);
      unsigned m = ~0u, a[2][8], p[2][8];
#pragma unroll
      for (int k = 0; k < 66; k += 2) {
        SB();
        if (k < 64) {
          if (k >= 48) { c[k] = fix(c[k], c[k - 16][3]); c[k + 1] = fix(c[k + 1], c[k - 15][3]); }
          if ((k & 15) == 14 && lane >= 48) c[k + 1] = none;
          if (k >= 48) m &= c[k][3] & c[k + 1][3];
          take(c[k], c[k + 1], a[k >> 1 & 1], p[k >> 1 & 1]);
        }
        if (k < 16) {
#pragma unroll
          for (int i = 16 + 2 * k; i < 20 + 2 * k; ++i) c[i] = ld(i >> 4, i & 15);
        }
        if (k >= 24 && k < 40) {
#pragma unroll
          for (int i = k - 24; i < k - 22; ++i)
            c[48 + i] = __builtin_amdgcn_raw_buffer_load_b128(wsrd, bo + (780 + i * 16) * 64 + (__builtin_amdgcn_mov_dpp(c[32 + i][3], 0xff, 15, 15, 0) & 0x40000000), 0, 16);
        }
        SB();
        if (k) fill(c[k - 2], c[k - 1], a[~k >> 1 & 1], p[~k >> 1 & 1]);
      }
      SB();
      if (__ballot((lane & 3) == 3 && m != ~0u)) {
        unsigned more = 0;
#pragma unroll
        for (int k = 0; k < 16; ++k) more |= __ballot((lane & 3) == 3 && c[48 + k][3] != ~0u) ? 1u << k : 0u;
#pragma unroll 1
        for (; more; more &= more - 1) {
          const int k = __builtin_ctz(more);
          u32x4 e = ld(4, k);
          if (k == 15 && lane >= 48) e = none;
          ovf |= __ballot((lane & 3) == 3 && e[3] != ~0u) != 0;
          add8(e, none);
        }
      }
      if (ovf) clear();
    }
    if (ovf) {
      for (int q = lane; q < EE / 4; q += 64) {
        const u32x4 d = reinterpret_cast<const u32x4*>(dst)[q], s = reinterpret_cast<const u32x4*>(src)[q];
        u32x4 e;
#pragma unroll
        for (int r = 0; r < 4; ++r) e[r] = d[r] - lo < 640u ? (d[r] - lo) << 16 | s[r] : ~0u;
        add8(e, none);
      }
    }
    if (pass) break;
    asm volatile("" : "+v"(lane));
    const int qd = lane & 3, nl = lane >> 2;
    int inc = 0;
#pragma unroll
    for (int i = 0; i < 10; ++i) off[i] = 46 - (int)((lds_u32_p)(rec + (64 * i + lane) * 52))[12];
#pragma unroll
    for (int i = 0; i < 10; ++i) {
      const int cn = off[i];
      off[i] = cn > 48 ? cn : 0; inc += off[i];
      if (cn > 48) { big |= 1u << i; ((lds_u16_p)ext)[2 * (64 * i + lane)] = 0xffff; }
    }
    __builtin_amdgcn_wave_barrier();
    auto put = [&](int odd) {
#pragma unroll 2
      for (int bl = 0; bl < 4; ++bl) {
        u32x4 o[10];
#pragma unroll
        for (int T = 0; T < 10; ++T) if (((2 * T + 1) / 5 & 1) == odd) {
          const lds_u32_p r = (lds_u32_p)(rec + (bl * 160 + T * 16 + nl) * 52);
          o[T] = (u32x4){r[3 * qd], r[1 + 3 * qd], r[2 + 3 * qd], ((lds_u32_p)ext)[bl * 160 + T * 16 + nl]};
        }
#pragma unroll
        for (int T = 0; T < 10; ++T) if (((2 * T + 1) / 5 & 1) == odd) {
          const int c = (2 * T + 1) / 5, ro = 16 * (T - (5 * c >> 1)) + nl, w = c & 1 ? ro * 43 >> 9 : ro >> 3;
          __builtin_amdgcn_raw_buffer_store_b128(o[T], wsrd, WS_GL + c * 4096 + w * (c & 1 ? 256 : 512) + ro * 64 + qd * 16, (bin * 4 + bl) * 16384, 16);
        }
      }
    };
    put(1);
    asm volatile("" ::: "memory");
    put(0);
    asm volatile("s_waitcnt vmcnt(16)" ::: "memory");
    if (lane == 0) st_flag(wsrd, WS_LF + bin * 128 + 64, tag);
#pragma unroll
    for (int o = 1; o < 64; o <<= 1) { const int y = __shfl_up(inc, o); if (lane >= o) inc += y; }
    tot = __builtin_amdgcn_readlane(inc, 63);
    drain();
    if (lane == 0) st_flag(wsrd, WS_LF + bin * 128, tag);
    __syncthreads();
    int j2 = j * 4, w2 = wid, l2 = lane;
    asm volatile("" : "+s"(j2), "+s"(w2), "+v"(l2));
    product_load(lds, w2, l2, j2, wv, wm, wo, true, true);
#pragma unroll 1
    for (int job = 0; job < 4; ++job) {
      if (job) asm volatile("s_waitcnt vmcnt(4)" ::: "memory"); else drain();
      __syncthreads();
      asm volatile("" : "+s"(w2));
      if (job < 3) product_load(lds + (~job & 1) * 32768, w2, l2, j2 + job + 1, wv, wm, wo, false, true);
      product_w(lds, w2, l2, j2 + job, wsrd);
    }
    if (w2 == 0) *(lds_u32_p)(ext - 8) = 0;
    __syncthreads();
    if (tot == 0 || tot > 2 * LONGS) break;
#pragma unroll
    for (int i = 0; i < 10; ++i) {
      const unsigned r = rec + (64 * i + lane) * 52;
      ((lds_u32_p)r)[12] = off[i] ? ext + 2558 + inc - r : -2u;
      inc -= off[i]; off[i] = off[i] << 15 | inc;
    }
  }
  __syncthreads();
  const unsigned ex0 = (unsigned)(size_t)lds + 0x20800;
  const lds_u32_p lng = (lds_u32_p)(size_t)(unsigned)(size_t)lds;
#pragma unroll
  for (int i = 0; i < 10; ++i)
    if (big >> i & 1) {
      const unsigned q = __atomic_fetch_add((lds_u32_p)(ex0 - 8), 1u, __ATOMIC_RELAXED);
      lng[2 * q] = tot > 2 * LONGS ? ~0u : off[i] + wid * 6912 + 2560; lng[2 * q + 1] = (((lds_u32_p)ext)[64 * i + lane] & 0xffff0000u) | (lo + 64 * i + lane);
    }
  __syncthreads();
  const int nl_ = *(lds_u32_p)(ex0 - 8);
  bf16x8 b1;
#pragma unroll
  for (int e = 0; e < 8; ++e) b1[e] = (lane & 15) == e + ((lane >> 1) & 8) ? 0x3f80 : 0;
  const unsigned lo16 = (lane & 31) * 16;
  {
    u32x4 d[3][16]; unsigned nn[3]; int mode[3], q = wid, hf = 0;
    auto issue = [&](u32x4 (&v)[16], unsigned& n, int& md) {
      const unsigned w = __builtin_amdgcn_readfirstlane(lng[2 * q]);
      n = lng[2 * q + 1];
      const int dg = q < nl_ && w >> 16 <= 64 ? w >> 16 : 0, g = dg - hf - (lane >> 5);
      const lds_u16_p f = (lds_u16_p)(ex0 + (w & 0xffffu)) + hf + (lane >> 5);
      md = q >= nl_ ? -1 : dg == 0 ? 0 : dg - hf > 32 ? 1 : 2;
      hf = md == 1 ? 32 : 0; q += md == 1 ? 0 : 4;
      unsigned id[16];
#pragma unroll
      for (int t = 0; t < 16; ++t) id[t] = f[2 * t];
#pragma unroll
      for (int t = 0; t < 16; ++t) { asm volatile("" : "+v"(id[t])); v[t] = __builtin_amdgcn_raw_buffer_load_b128(msrd, (2 * t < g ? id[t] : 0xffffu) * 512u + lo16, 0, 16); }
    };
    issue(d[0], nn[0], mode[0]); issue(d[1], nn[1], mode[1]);
    f32x4 keep = {};
#pragma unroll 1
    for (bool go = true; go;) {
#pragma unroll
      for (int s = 0; s < 3; ++s) {
        if (mode[s] < 0) { go = false; break; }
        issue(d[(s + 2) % 3], nn[(s + 2) % 3], mode[(s + 2) % 3]); SB();
        f32x4 acc = {};
#pragma unroll
        for (int t = 0; t < 16; ++t) acc = MFMA16(b1, as_bf(d[s][t]), acc);
        local_store(wsrd, mode[s] == 2 ? nn[s] & 0xffffu : 0x200000u, lane, acc + keep, bfhi(nn[s]));
        keep = mode[s] == 1 ? acc : (f32x4){0.f, 0.f, 0.f, 0.f}; SB();
      }
    }
  }
#pragma unroll 1
  for (int q0 = 0; q0 < nl_; q0 += 256)
#pragma unroll 1
  for (unsigned long long m = __ballot(q0 + 4 * lane + 3 - wid < nl_ && lng[2 * (q0 + 4 * lane + 3 - wid)] >> 16 > 64); m; m &= m - 1) {
    const int q = q0 + 4 * __builtin_ctzll(m) + 3 - wid;
    const unsigned w = __builtin_amdgcn_readfirstlane(lng[2 * q]), n = __builtin_amdgcn_readfirstlane(lng[2 * q + 1]); const int dg = w >> 16;
    f32x4 acc = {};
    if (w == ~0u) {
      for (int e0 = 0; e0 < EE; e0 += 64) {
        unsigned long long h = __ballot(dst[e0 + lane] == (n & 0xffffu));
        while (h) {
          const int e = e0 + __builtin_ctzll(h); h &= h - 1;
          const unsigned vo = lane < 32 ? (unsigned)src[e] * 512 + lo16 : 0x7fffff00u;
          acc = MFMA16(b1, as_bf(__builtin_amdgcn_raw_buffer_load_b128(msrd, vo, 0, 16)), acc);
        }
      }
    } else {
      const lds_u16_p f = (lds_u16_p)(ex0 + (w & 0xffffu)), u = (lds_u16_p)(ext + 2624 + 2 * LONGS);
      for (int x = lane; x < dg; x += 64) {
        const int key = f[x]; int p = 0;
#pragma unroll 8
        for (int t = 0; t < dg; ++t) { const int v = f[t]; p += v < key || (v == key && t < x); }
        u[p] = key;
      }
      __builtin_amdgcn_wave_barrier();
      for (int x = lane >> 5; x < dg + (lane >> 5); x += 32) {
        u32x4 d[16]; unsigned id[16]; f32x4 a2 = {};
#pragma unroll
        for (int t = 0; t < 16; ++t) id[t] = u[min(x + 2 * t, dg)];
#pragma unroll
        for (int t = 0; t < 16; ++t) { asm volatile("" : "+v"(id[t])); d[t] = __builtin_amdgcn_raw_buffer_load_b128(msrd, (x + 2 * t < dg ? id[t] : 0xffffu) * 512u + lo16, 0, 16); }
#pragma unroll
        for (int t = 0; t < 16; ++t) a2 = MFMA16(b1, as_bf(d[t]), a2);
        acc += a2;
      }
      __builtin_amdgcn_wave_barrier();
    }
    local_store(wsrd, n & 0xffffu, lane, acc, bfhi(n));
  }
  drain();
  __syncthreads();
  if (wid == 0 && lane == 0) st_flag(wsrd, WS_WF + j * 4, tag);
}

DI void exact_rows(char* lds, int b, int wid, char* ws, const u16* x, u16* out) {
  int lane; asm volatile("v_mbcnt_lo_u32_b32 %0, -1, 0\n\tv_mbcnt_hi_u32_b32 %0, -1, %0" : "=v"(lane));
  const srd_t w = __builtin_amdgcn_make_buffer_rsrc(ws, 0, WS_END, 0x00020000);
  const srd_t xs = __builtin_amdgcn_make_buffer_rsrc(const_cast<u16*>(x), 0, NN * 512, 0x00020000);
  const srd_t os = __builtin_amdgcn_make_buffer_rsrc(out, 0, NN * 512, 0x00020000);
  const int n = lane & 15, g = lane >> 4, c = b & 3;
  f32x4* red = reinterpret_cast<f32x4*>(lds);
  float* mx = reinterpret_cast<float*>(lds + 65536);
  auto ld = [&](unsigned off) { return as_bf(__builtin_amdgcn_raw_buffer_load_b128(w, off, 0, 16)); };
#pragma unroll 1
  for (int i = 0; i < 2 + (c & 1); ++i) {
    const int tile = (b >> 2) * 10 + (5 * c >> 1) + i;
    bf16x8 q[8];
#pragma unroll
    for (int ks = 0; ks < 8; ++ks) q[ks] = ld(WS_QP + tile * 8192 + ks * 1024 + lane * 16);
    auto score = [&](int kt) {
      f32x4 s = {};
#pragma unroll
      for (int ks = 0; ks < 8; ++ks) s = MFMA16(ld(WS_H + (kt * 16 + n) * 512 + ks * 64 + g * 16), q[ks], s);
      return s;
    };
    float m = -__builtin_inff();
#pragma unroll 1
    for (int kt = wid; kt < NN / 16; kt += 4) { const f32x4 s = score(kt); m = fmaxf(fmaxf(m, fmaxf(s[0], s[1])), fmaxf(s[2], s[3])); }
    __syncthreads();
    mx[wid * 64 + lane] = m;
    __syncthreads();
#pragma unroll
    for (int r = 0; r < 16; ++r) m = fmaxf(m, mx[r * 16 + n]);
    f32x4 acc[16] = {};
#pragma unroll 1
    for (int kt = wid; kt < NN / 16; kt += 4) {
      const f32x4 s = score(kt);
      const u32x2 p = {pk2(__builtin_amdgcn_exp2f(s[0] - m), __builtin_amdgcn_exp2f(s[1] - m)), pk2(__builtin_amdgcn_exp2f(s[2] - m), __builtin_amdgcn_exp2f(s[3] - m))};
#pragma unroll
      for (int dt = 0; dt < 16; ++dt) {
        bf16x4 v;
#pragma unroll
        for (int e = 0; e < 4; ++e) v[e] = __builtin_amdgcn_raw_buffer_load_b16(w, WS_H + (kt * 16 + 4 * g + e) * 512 + n * 2, dt * 32, 16);
        acc[dt] = __builtin_amdgcn_mfma_f32_16x16x16bf16_1k(v, __builtin_bit_cast(bf16x4, p), acc[dt], 0, 0, 0);
      }
    }
#pragma unroll
    for (int dt = 0; dt < 16; ++dt) red[(wid * 16 + dt) * 64 + lane] = acc[dt];
    __syncthreads();
    float l = 0.f;
#pragma unroll
    for (int r = 0; r < 4; ++r) l += red[(r * 16 + 15) * 64 + 48 + n][3];
    l = 1.f / l;
    f32x4 y[4] = {};
#pragma unroll 1
    for (int ks = 0; ks < 8; ++ks) {
      f32x4 o[2];
#pragma unroll
      for (int h = 0; h < 2; ++h) o[h] = (red[(2 * ks + h) * 64 + lane] + red[(16 + 2 * ks + h) * 64 + lane] + red[(32 + 2 * ks + h) * 64 + lane] + red[(48 + 2 * ks + h) * 64 + lane]) * l;
      const bf16x8 of = as_bf((u32x4){pk2(o[0][0], o[0][1]), pk2(o[0][2], o[0][3]), pk2(o[1][0], o[1][1]), pk2(o[1][2], o[1][3])});
      const bf16x8 gf = ld(WS_G + (tile * 16 + n) * 512 + ks * 64 + g * 16);
#pragma unroll
      for (int et = 0; et < 4; ++et) {
        y[et] = MFMA16(ld(WS_WV + wid * 32768 + ks * 4096 + et * 1024 + lane * 16), of, y[et]);
        y[et] = MFMA16(ld(WS_WM + wid * 32768 + (et < 2 ? 16384 : 0) + ks * 2048 + (et & 1) * 1024 + lane * 16), gf, y[et]);
      }
    }
#pragma unroll
    for (int jj = 0; jj < 2; ++jj) {
      const unsigned at = (tile * 16 + n) * 512 + wid * 128 + jj * 64 + g * 16;
      const u32x4 xv = __builtin_amdgcn_raw_buffer_load_b128(xs, at, 0, 0);
      u32x4 o;
#pragma unroll
      for (int r = 0; r < 4; ++r) o[r] = pk2(y[2 * jj + (r >> 1)][2 * (r & 1)] + bf2f(xv[r] & 0xffffu), y[2 * jj + (r >> 1)][2 * (r & 1) + 1] + bfhi(xv[r]));
      __builtin_amdgcn_raw_buffer_store_b128(o, os, at, 0, 0);
    }
  }
}

__global__ __launch_bounds__(256, 1) void attn_kernel(char* ws, const int* __restrict__ src, const int* __restrict__ dst,
                                                      const u16* __restrict__ deg_inv, const u16* __restrict__ x, u16* out,
                                                      const u16* __restrict__ wq, const u16* __restrict__ wk, const u16* __restrict__ wv,
                                                      const u16* __restrict__ wm, const u16* __restrict__ wo) {
  extern __shared__ __attribute__((aligned(16))) char lds[];
  asm volatile("s_load_dword s99, %0, 0" :: "s"(__builtin_amdgcn_kernarg_segment_ptr()) : "s99");
  int b = (blockIdx.x & 6) << 5 | blockIdx.x >> 3 << 1 | (blockIdx.x & 1);
  code_touch((blockIdx.x >> 3 & 1) * 256 + threadIdx.x, (unsigned)(size_t)lds + 0x27f00u);
  int wid = __builtin_amdgcn_readfirstlane(threadIdx.x >> 6);
  static_assert(WS_END == 37475328 && WS_EP == 5695744, "workspace layout of the generated code");
  auto ka = __builtin_amdgcn_kernarg_segment_ptr();
  asm volatile(
    "s_cmpk_ge_u32 %1, 252\n"
    "s_cbranch_scc1 .Lat_end\n"
    "s_load_dwordx2 s[20:21], %0, 0x30\n"
    "s_load_dwordx2 s[24:25], %0, 0x38\n"
    "s_load_dwordx2 s[12:13], %0, 0x20\n"
    "s_load_dwordx2 s[8:9], %0, 0x0\n"
    "s_load_dwordx2 s[16:17], %0, 0x28\n"
    "s_load_dwordx4 s[28:31], %0, 0x8\n"
    "v_mbcnt_lo_u32_b32 v24, -1, 0\n"
    "v_mbcnt_hi_u32_b32 v24, -1, v24\n"
    "s_and_b32 s50, %2, 1\n"
    "s_lshr_b32 s51, %2, 1\n"
    "v_lshrrev_b32_e32 v25, 3, v24\n"
    "v_and_b32_e32 v26, 6, v25\n"
    "v_and_b32_e32 v27, 7, v24\n"
    "v_xor_b32_e32 v26, v26, v27\n"
    "v_lshlrev_b32_e32 v26, 4, v26\n"
    "v_and_b32_e32 v27, 4, v25\n"
    "v_add_u32_e32 v27, v27, v25\n"
    "v_lshl_add_u32 v254, v27, 9, v26\n"
    "v_add_u32_e32 v255, 0x1c00, v254\n"
    "s_mov_b32 s22, 0x20000\n"
    "s_mov_b32 s23, 0x20000\n"
    "s_mov_b32 s26, 0x20000\n"
    "s_mov_b32 s27, 0x20000\n"
    "s_lshr_b32 s36, %1, 7\n"
    "s_mul_i32 s36, s36, 3\n"
    "s_add_u32 s36, s36, %1\n"
    "s_and_b32 s36, s36, 127\n"
    "s_lshl_b32 s45, %2, 7\n"
    "s_lshl_b32 s46, %2, 12\n"
    "s_add_u32 s46, s46, %3\n"
    "s_and_b32 s47, s36, 7\n"
    "s_lshl_b32 s47, s47, 14\n"
    "s_add_u32 m0, s46, 32768\n"
    "s_add_u32 s47, s47, s45\n"
    "s_and_b32 s32, %1, 3\n"
    "s_lshr_b32 s33, %1, 2\n"
    "s_mul_i32 s38, s32, 5\n"
    "s_lshr_b32 s38, s38, 1\n"
    "s_mul_i32 s35, s33, 10\n"
    "s_add_u32 s35, s35, s38\n"
    "s_lshl_b32 s35, s35, 13\n"
    "s_mov_b32 s14, 5120000\n"
    "s_mov_b32 s15, 0x20000\n"
    "s_waitcnt lgkmcnt(0)\n"
    "buffer_load_dwordx4 v254, s[20:23], s47 offen offset:0 lds\n"
    "s_add_u32 s48, s47, 1024\n"
    "buffer_load_dwordx4 v255, s[20:23], s47 offen offset:3072 lds\n"
    "s_add_u32 s47, s47, 0x1800\n"
    "buffer_load_dwordx4 v254, s[20:23], s48 offen offset:1024 lds\n"
    "buffer_load_dwordx4 v254, s[20:23], s47 offen offset:2048 lds\n"
    "s_lshr_b32 s47, s36, 4\n"
    "s_lshl_b32 s47, s47, 14\n"
    "s_and_b32 s48, s36, 8\n"
    "s_lshl_b32 s48, s48, 8\n"
    "s_add_u32 s47, s47, s48\n"
    "s_add_u32 m0, s46, 49152\n"
    "s_add_u32 s47, s47, s45\n"
    "buffer_load_dwordx4 v254, s[24:27], s47 offen offset:0 lds\n"
    "buffer_load_dwordx4 v255, s[24:27], s47 offen offset:1024 lds\n"
    "s_add_u32 s47, s45, 120832\n"
    "buffer_load_dwordx4 v254, s[24:27], s47 offen offset:2048 lds\n"
    "s_add_u32 s47, s47, 1024\n"
    "buffer_load_dwordx4 v254, s[24:27], s47 offen offset:3072 lds\n"
    "s_mul_i32 s46, %1, 159\n"
    "s_lshl_b32 s45, s50, 6\n"
    "v_add_u32_e32 v111, s45, v24\n"
    "v_add_u32_e32 v108, s46, v111\n"
    "v_min_u32_e32 v108, 39999, v108\n"
    "v_lshlrev_b32_e32 v108, 4, v108\n"
    "s_lshl1_add_u32 s47, s46, 256\n"
    "v_add_u32_e32 v109, s47, v111\n"
    "v_min_u32_e32 v109, 79999, v109\n"
    "v_lshlrev_b32_e32 v109, 3, v109\n"
    "s_cmp_lg_u32 s51, 0\n"
    "s_cselect_b64 exec, -1, 1\n"
    "global_load_dwordx4 v[96:99], v108, s[30:31]\n"
    "global_load_dwordx2 v[92:93], v109, s[30:31]\n"
    "global_load_dwordx4 v[100:103], v108, s[28:29]\n"
    "global_load_dwordx2 v[94:95], v109, s[28:29]\n"
    "s_mov_b64 exec, -1\n"
    "s_lshl_b32 s45, %2, 6\n"
    "v_add_u32_e32 v108, s45, v24\n"
    "s_sub_u32 s59, 40000, s46\n"
    "s_lshl1_add_u32 s61, s59, -256\n"
    "s_min_i32 s61, s61, 62\n"
    "s_lshl_b32 s45, %2, 2\n"
    "s_add_u32 s54, s35, 0x2000\n"
    "s_add_u32 s55, s35, 0x4000\n"
    "v_lshlrev_b32_e32 v253, 4, v24\n"
    "v_lshrrev_b32_e32 v28, 4, v24\n"
    "v_add_u32_e32 v28, s45, v28\n"
    "v_and_b32_e32 v104, 0xf0, v253\n"
    "v_lshl_add_u32 v104, v28, 9, v104\n"
    "buffer_load_dwordx4 v[0:3], v104, s[12:15], s35 offen offset:0\n"
    "buffer_load_dwordx4 v[4:7], v104, s[12:15], s35 offen offset:256\n"
    "s_bitcmp1_b32 %1, 0\n"
    "s_cselect_b32 s60, s55, 2.0\n"
    "buffer_load_dwordx4 v[8:11], v104, s[12:15], s54 offen offset:0\n"
    "buffer_load_dwordx4 v[12:15], v104, s[12:15], s54 offen offset:256\n"
    "buffer_load_dwordx4 v[16:19], v104, s[12:15], s55 offen offset:0\n"
    "buffer_load_dwordx4 v[20:23], v104, s[12:15], s55 offen offset:256\n"
    "s_mov_b32 s10, 37475328\n"
    "s_mov_b32 s11, 0x20000\n"
    "s_mov_b32 s37, 5695744\n"
    "s_buffer_load_dword s39, s[8:11], s37\n"
    "s_bfe_u32 s37, %1, 0x20001\n"
    "v_and_b32_e32 v27, 1, v25\n"
    "v_lshl_add_u32 v248, v27, 9, v26\n"
    "v_lshrrev_b32_e32 v27, 1, v25\n"
    "v_lshl_add_u32 v248, v27, 12, v248\n"
    "s_mul_i32 s40, s32, 39\n"
    "s_and_b32 s34, s32, 1\n"
    "s_add_u32 s40, s40, s34\n"
    "s_lshl1_add_u32 s40, s40, s51\n"
    "s_lshl_b32 s40, s40, 14\n"
    "s_lshl_b32 s47, s50, 8\n"
    "s_add_u32 s40, s40, s47\n"
    "s_add_u32 s41, s40, 128\n"
    "s_lshl_b32 s34, s34, 15\n"
    "s_xor_b32 s43, s50, s51\n"
    "s_lshl3_add_u32 s43, s50, s43\n"
    "s_lshl_b32 s43, s43, 13\n"
    "s_add_u32 s47, %3, 32768\n"
    "s_add_u32 s43, s43, s47\n"
    "v_bfe_u32 v25, v24, 2, 3\n"
    "v_lshlrev_b32_e32 v244, 7, v25\n"
    "v_lshrrev_b32_e32 v26, 5, v24\n"
    "v_lshl_add_u32 v244, v26, 10, v244\n"
    "v_and_b32_e32 v25, 6, v25\n"
    "v_bfe_u32 v26, v24, 1, 1\n"
    "v_xor_b32_e32 v25, v25, v26\n"
    "v_lshl_add_u32 v244, v25, 4, v244\n"
    "v_and_b32_e32 v26, 1, v24\n"
    "v_lshl_add_u32 v244, v26, 3, v244\n"
    "s_lshl_b32 s48, s51, 16\n"
    "s_add_u32 s48, s48, s47\n"
    "v_add_u32_e32 v244, s48, v244\n"
    "v_xor_b32_e32 v245, 32, v244\n"
    "v_xor_b32_e32 v246, 64, v244\n"
    "v_xor_b32_e32 v247, 0x60, v244\n"
    "v_and_b32_e32 v25, 7, v24\n"
    "v_lshlrev_b32_e32 v27, 7, v25\n"
    "v_bfe_u32 v26, v24, 3, 1\n"
    "v_lshl_add_u32 v27, v26, 10, v27\n"
    "v_and_b32_e32 v25, 6, v24\n"
    "v_lshrrev_b32_e32 v26, 4, v24\n"
    "v_xor_b32_e32 v25, v25, v26\n"
    "v_lshl_add_u32 v27, v25, 4, v27\n"
    "s_lshl_b32 s48, s51, 13\n"
    "s_add_u32 s48, s48, s47\n"
    "v_add_u32_e32 v240, s48, v27\n"
    "v_xor_b32_e32 v241, 64, v240\n"
    "v_and_b32_e32 v29, 15, v24\n"
    "s_cmp_ge_u32 s36, 112\n"
    "s_cselect_b32 s47, 8, 16\n"
    "v_cmp_le_u32_e32 vcc, s47, v29\n"
    "v_mov_b32_e32 v64, 0xbd80bd80\n"
    "v_mov_b32_e32 v65, 0xbdabbdab\n"
    "v_mov_b32_e32 v66, 0x392b392b\n"
    "v_cndmask_b32_e32 v64, v64, v65, vcc\n"
    "v_cndmask_b32_e32 v66, 0, v66, vcc\n"
    "s_cselect_b32 s47, 56, 64\n"
    "v_cmp_le_u32_e32 vcc, s47, v24\n"
    "v_mov_b32_e32 v65, 0x3e80bdab\n"
    "v_mov_b32_e32 v67, 0x392b\n"
    "v_cndmask_b32_e32 v65, v64, v65, vcc\n"
    "v_cndmask_b32_e32 v67, v66, v67, vcc\n"
    "s_cmp_ge_u32 s36, 120\n"
    "s_cselect_b32 s47, 15, 16\n"
    "v_cmp_ne_u32_e32 vcc, s47, v29\n"
    "v_mov_b32_e32 v68, 0x3db8aa3b\n"
    "s_lshr_b32 s47, s36, 5\n"
    "v_cndmask_b32_e32 v68, 0, v68, vcc\n"
    "s_lshl_b32 s47, s47, 15\n"
    "s_and_b32 s48, s36, 7\n"
    "s_lshl_b32 s48, s48, 11\n"
    "s_add_u32 s47, s47, s48\n"
    "s_and_b32 s48, s36, 8\n"
    "s_lshl_b32 s48, s48, 7\n"
    "s_add_u32 s47, s47, s48\n"
    "s_and_b32 s48, s36, 16\n"
    "s_lshl_b32 s48, s48, 10\n"
    "s_add_u32 s47, s47, s48\n"
    "s_add_u32 s52, s47, 5242880\n"
    "s_lshl_b32 s48, s50, 9\n"
    "s_add_u32 s52, s52, s48\n"
    "s_lshl_b32 s48, s50, 11\n"
    "v_add_u32_e32 v72, s48, v240\n"
    "v_add_u32_e32 v73, s48, v241\n"
    "v_and_b32_e32 v74, 0x1f0, v253\n"
    "v_lshrrev_b32_e32 v29, 5, v24\n"
    "v_lshl_add_u32 v74, v29, 3, v74\n"
    "s_lshl_b32 s53, s36, 1\n"
    "s_add_u32 s53, s53, s50\n"
    "s_lshl_b32 s53, s53, 4\n"
    "s_add_u32 s53, s53, 5689344\n"
    "s_cmp_eq_u32 s51, 0\n"
    "s_cselect_b32 s52, s52, 2.0\n"
    "s_cselect_b32 s53, s53, 2.0\n"
    "v_lshrrev_b32_e32 v109, 2, v108\n"
    "v_mul_u32_u24_e32 v109, 0x4100, v109\n"
    "v_and_b32_e32 v110, 48, v253\n"
    "v_or_b32_e32 v109, v109, v110\n"
    "v_add_u32_e32 v110, 0x104000, v109\n"
    "v_cmp_gt_u32_e32 vcc, 64, v108\n"
    "s_add_u32 s44, %3, 24576\n"
    "v_lshl_add_u32 v108, v108, 4, s44\n"
    "v_cndmask_b32_e32 v110, 2.0, v110, vcc\n"
    "v_mov_b32_e32 v32, -1\n"
    "v_mov_b32_e32 v33, -1\n"
    "v_mov_b32_e32 v34, -1\n"
    "v_mov_b32_e32 v35, -1\n"
    "ds_write_b128 v108, v[32:35] offset:0\n"
    "ds_write_b128 v108, v[32:35] offset:1280\n"
    "v_and_b32_e32 v29, 6, v28\n"
    "v_xor_b32_e32 v29, v29, v24\n"
    "v_and_b32_e32 v29, 7, v29\n"
    "v_lshlrev_b32_e32 v105, 7, v28\n"
    "v_lshl_add_u32 v105, v29, 4, v105\n"
    "v_bfe_u32 v29, v24, 3, 1\n"
    "v_subrev_u32_e32 v29, s37, v29\n"
    "v_and_b32_e32 v29, 3, v29\n"
    "v_lshl_add_u32 v105, v29, 11, v105\n"
    "s_add_u32 s45, %3, 65536\n"
    "v_add_u32_e32 v105, s45, v105\n"
    "v_xor_b32_e32 v106, 0x1000, v105\n"
    "v_and_b32_e32 v29, 15, v24\n"
    "v_cmp_eq_u32_e32 vcc, 15, v29\n"
    "v_mov_b32_e32 v107, 0xffff\n"
    "s_mov_b32 s58, 0x3f803f80\n"
    "s_mov_b32 s57, 0x43c00000\n"
    "v_cndmask_b32_e32 v107, -1, v107, vcc\n"
    "s_mov_b32 s56, 0xc3c00000\n"
    "s_mov_b32 s62, 0xc000c000\n"
    "s_mov_b32 s63, 0xc000c000\n"
    "v_add_u32_e32 v250, s45, v27\n"
    "v_xor_b32_e32 v251, 64, v250\n"
    "s_lshl_b32 s47, %2, 10\n"
    "s_add_u32 s47, s47, 5689344\n"
    "v_lshl_add_u32 v252, v24, 4, s47\n"
    "s_waitcnt vmcnt(10)\n"
    "s_waitcnt lgkmcnt(0)\n"
    "s_barrier\n"
    "s_add_u32 s39, s39, 2\n"
    "s_cmp_lg_u32 s51, 0\n"
    "s_cbranch_scc1 .Lat_bin\n"
    "ds_read_b128 v[112:115], v72 offset:0\n"
    "ds_read_b128 v[208:211], v240 offset:18432\n"
    "ds_read_b128 v[176:179], v240 offset:16384\n"
    "ds_read_b128 v[116:119], v73 offset:0\n"
    "ds_read_b128 v[212:215], v241 offset:18432\n"
    "ds_read_b128 v[180:183], v241 offset:16384\n"
    "ds_read_b128 v[120:123], v72 offset:4096\n"
    "ds_read_b128 v[216:219], v240 offset:22528\n"
    "ds_read_b128 v[184:187], v240 offset:20480\n"
    "ds_read_b128 v[124:127], v73 offset:4096\n"
    "ds_read_b128 v[220:223], v241 offset:22528\n"
    "ds_read_b128 v[188:191], v241 offset:20480\n"
    "ds_read_b128 v[128:131], v72 offset:8192\n"
    "ds_read_b128 v[224:227], v240 offset:26624\n"
    "ds_read_b128 v[192:195], v240 offset:24576\n"
    "s_waitcnt lgkmcnt(12)\n"
    "v_mfma_f32_16x16x32_bf16 v[56:59], v[208:211], v[112:115], 0\n"
    "ds_read_b128 v[132:135], v73 offset:8192\n"
    "ds_read_b128 v[228:231], v241 offset:26624\n"
    "v_mfma_f32_16x16x32_bf16 v[48:51], v[112:115], v[176:179], 0\n"
    "ds_read_b128 v[196:199], v241 offset:24576\n"
    "s_waitcnt lgkmcnt(12)\n"
    "v_mfma_f32_16x16x32_bf16 v[56:59], v[212:215], v[116:119], v[56:59]\n"
    "ds_read_b128 v[136:139], v72 offset:12288\n"
    "ds_read_b128 v[232:235], v240 offset:30720\n"
    "v_mfma_f32_16x16x32_bf16 v[48:51], v[116:119], v[180:183], v[48:51]\n"
    "ds_read_b128 v[200:203], v240 offset:28672\n"
    "s_waitcnt lgkmcnt(12)\n"
    "v_mfma_f32_16x16x32_bf16 v[56:59], v[216:219], v[120:123], v[56:59]\n"
    "ds_read_b128 v[140:143], v73 offset:12288\n"
    "ds_read_b128 v[236:239], v241 offset:30720\n"
    "v_mfma_f32_16x16x32_bf16 v[48:51], v[120:123], v[184:187], v[48:51]\n"
    "ds_read_b128 v[204:207], v241 offset:28672\n"
    "s_waitcnt lgkmcnt(12)\n"
    "v_mfma_f32_16x16x32_bf16 v[56:59], v[220:223], v[124:127], v[56:59]\n"
    "v_mfma_f32_16x16x32_bf16 v[48:51], v[124:127], v[188:191], v[48:51]\n"
    "s_waitcnt lgkmcnt(9)\n"
    "v_mfma_f32_16x16x32_bf16 v[56:59], v[224:227], v[128:131], v[56:59]\n"
    "v_mfma_f32_16x16x32_bf16 v[48:51], v[128:131], v[192:195], v[48:51]\n"
    "s_waitcnt lgkmcnt(6)\n"
    "v_mfma_f32_16x16x32_bf16 v[56:59], v[228:231], v[132:135], v[56:59]\n"
    "v_mfma_f32_16x16x32_bf16 v[48:51], v[132:135], v[196:199], v[48:51]\n"
    "s_waitcnt lgkmcnt(3)\n"
    "v_mfma_f32_16x16x32_bf16 v[56:59], v[232:235], v[136:139], v[56:59]\n"
    "v_mfma_f32_16x16x32_bf16 v[48:51], v[136:139], v[200:203], v[48:51]\n"
    "s_waitcnt lgkmcnt(0)\n"
    "v_mfma_f32_16x16x32_bf16 v[56:59], v[236:239], v[140:143], v[56:59]\n"
    "v_mfma_f32_16x16x32_bf16 v[48:51], v[140:143], v[204:207], v[48:51]\n"
    "s_nop 3\n"
    "v_cvt_pk_bf16_f32 v56, v56, v57\n"
    "v_cvt_pk_bf16_f32 v57, v58, v59\n"
    "s_nop 1\n"
    "v_mfma_f32_16x16x16_bf16 v[48:51], v[56:57], v[64:65], v[48:51]\n"
    "v_mfma_f32_16x16x16_bf16 v[48:51], v[56:57], v[66:67], v[48:51]\n"
    "s_nop 7\n"
    "v_mul_f32_e32 v48, v48, v68\n"
    "v_mul_f32_e32 v49, v49, v68\n"
    "v_mul_f32_e32 v50, v50, v68\n"
    "v_mul_f32_e32 v51, v51, v68\n"
    "v_cvt_pk_bf16_f32 v48, v48, v49\n"
    "v_cvt_pk_bf16_f32 v49, v50, v51\n"
    "s_branch .Lat_noj2\n"
    ".Lat_bin:\n"
    "s_movk_i32 s45, -640\n"
    "s_movk_i32 s48, 80\n"
    "v_mov_b32_e32 v60, 1\n"
    "v_cmp_gt_u32_e32 vcc, s59, v111\n"
    "v_cmp_gt_i32_e64 s[46:47], s61, v111\n"
    "s_waitcnt vmcnt(8)\n"
    "v_mul_u32_u24_e32 v48, 0x6667, v96\n"
    "v_mul_u32_u24_e32 v49, 0x6667, v97\n"
    "v_mul_u32_u24_e32 v50, 0x6667, v98\n"
    "v_mul_u32_u24_e32 v51, 0x6667, v99\n"
    "v_mul_u32_u24_e32 v52, 0x6667, v92\n"
    "v_mul_u32_u24_e32 v53, 0x6667, v93\n"
    "v_lshrrev_b32_e32 v48, 24, v48\n"
    "v_lshrrev_b32_e32 v49, 24, v49\n"
    "v_lshrrev_b32_e32 v50, 24, v50\n"
    "v_lshrrev_b32_e32 v51, 24, v51\n"
    "v_lshrrev_b32_e32 v52, 24, v52\n"
    "v_lshrrev_b32_e32 v53, 24, v53\n"
    "v_min_u32_e32 v48, 15, v48\n"
    "v_min_u32_e32 v49, 15, v49\n"
    "v_min_u32_e32 v50, 15, v50\n"
    "v_min_u32_e32 v51, 15, v51\n"
    "v_min_u32_e32 v52, 15, v52\n"
    "v_min_u32_e32 v53, 15, v53\n"
    "v_lshl_add_u32 v54, v48, 2, s44\n"
    "v_lshl_add_u32 v55, v49, 2, s44\n"
    "v_lshl_add_u32 v56, v50, 2, s44\n"
    "v_lshl_add_u32 v57, v51, 2, s44\n"
    "v_lshl_add_u32 v58, v52, 2, s44\n"
    "v_lshl_add_u32 v59, v53, 2, s44\n"
    "s_mov_b64 exec, vcc\n"
    "ds_add_rtn_u32 v54, v54, v60 offset:5248\n"
    "ds_add_rtn_u32 v55, v55, v60 offset:5248\n"
    "ds_add_rtn_u32 v56, v56, v60 offset:5248\n"
    "ds_add_rtn_u32 v57, v57, v60 offset:5248\n"
    "s_mov_b64 exec, s[46:47]\n"
    "ds_add_rtn_u32 v58, v58, v60 offset:5248\n"
    "ds_add_rtn_u32 v59, v59, v60 offset:5248\n"
    "s_mov_b64 exec, -1\n"
    "s_waitcnt vmcnt(6)\n"
    "v_mad_i32_i24 v96, v48, s45, v96\n"
    "v_mad_i32_i24 v97, v49, s45, v97\n"
    "v_mad_i32_i24 v98, v50, s45, v98\n"
    "v_mad_i32_i24 v99, v51, s45, v99\n"
    "v_mad_i32_i24 v92, v52, s45, v92\n"
    "v_mad_i32_i24 v93, v53, s45, v93\n"
    "v_lshl_or_b32 v96, v96, 16, v100\n"
    "v_lshl_or_b32 v97, v97, 16, v101\n"
    "v_lshl_or_b32 v98, v98, 16, v102\n"
    "v_lshl_or_b32 v99, v99, 16, v103\n"
    "v_lshl_or_b32 v92, v92, 16, v94\n"
    "v_lshl_or_b32 v93, v93, 16, v95\n"
    "s_mov_b64 exec, vcc\n"
    "s_waitcnt lgkmcnt(0)\n"
    "v_min_i32_e32 v54, 78, v54\n"
    "v_min_i32_e32 v55, 78, v55\n"
    "v_min_i32_e32 v56, 78, v56\n"
    "v_min_i32_e32 v57, 78, v57\n"
    "v_mad_u32_u24 v48, v48, s48, v54\n"
    "v_mad_u32_u24 v49, v49, s48, v55\n"
    "v_mad_u32_u24 v50, v50, s48, v56\n"
    "v_mad_u32_u24 v51, v51, s48, v57\n"
    "v_lshl_add_u32 v48, v48, 2, s44\n"
    "v_lshl_add_u32 v49, v49, 2, s44\n"
    "v_lshl_add_u32 v50, v50, 2, s44\n"
    "v_lshl_add_u32 v51, v51, 2, s44\n"
    "ds_write_b32 v48, v96 offset:4\n"
    "ds_write_b32 v49, v97 offset:4\n"
    "ds_write_b32 v50, v98 offset:4\n"
    "ds_write_b32 v51, v99 offset:4\n"
    "s_mov_b64 exec, s[46:47]\n"
    "v_min_i32_e32 v58, 78, v58\n"
    "v_min_i32_e32 v59, 78, v59\n"
    "v_mad_u32_u24 v52, v52, s48, v58\n"
    "v_mad_u32_u24 v53, v53, s48, v59\n"
    "v_lshl_add_u32 v52, v52, 2, s44\n"
    "v_lshl_add_u32 v53, v53, 2, s44\n"
    "ds_write_b32 v52, v92 offset:4\n"
    "ds_write_b32 v53, v93 offset:4\n"
    "s_mov_b64 exec, -1\n"
    ".Lat_noj2:\n"
    "buffer_store_dwordx2 v[48:49], v74, s[8:11], s52 offen offset:0 sc1\n"
    "v_mov_b32_e32 v112, s39\n"
    "v_mov_b32_e32 v113, 0\n"
    "s_waitcnt vmcnt(1)\n"
    "v_dot2_f32_bf16 v24, v0, s58, 0\n"
    "v_dot2_f32_bf16 v32, v8, s58, 0\n"
    "v_dot2_f32_bf16 v40, v16, s58, 0\n"
    "v_dot2_f32_bf16 v25, v4, s58, 0\n"
    "v_dot2_f32_bf16 v33, v12, s58, 0\n"
    "v_dot2_f32_bf16 v41, v20, s58, 0\n"
    "v_dot2_f32_bf16 v26, v0, v0, 0\n"
    "v_dot2_f32_bf16 v34, v8, v8, 0\n"
    "v_dot2_f32_bf16 v42, v16, v16, 0\n"
    "v_dot2_f32_bf16 v27, v4, v4, 0\n"
    "v_dot2_f32_bf16 v35, v12, v12, 0\n"
    "v_dot2_f32_bf16 v43, v20, v20, 0\n"
    "v_dot2_f32_bf16 v24, v1, s58, v24\n"
    "v_dot2_f32_bf16 v32, v9, s58, v32\n"
    "v_dot2_f32_bf16 v40, v17, s58, v40\n"
    "v_dot2_f32_bf16 v25, v5, s58, v25\n"
    "v_dot2_f32_bf16 v33, v13, s58, v33\n"
    "v_dot2_f32_bf16 v41, v21, s58, v41\n"
    "v_dot2_f32_bf16 v26, v1, v1, v26\n"
    "v_dot2_f32_bf16 v34, v9, v9, v34\n"
    "v_dot2_f32_bf16 v42, v17, v17, v42\n"
    "v_dot2_f32_bf16 v27, v5, v5, v27\n"
    "v_dot2_f32_bf16 v35, v13, v13, v35\n"
    "v_dot2_f32_bf16 v43, v21, v21, v43\n"
    "v_dot2_f32_bf16 v24, v2, s58, v24\n"
    "v_dot2_f32_bf16 v32, v10, s58, v32\n"
    "v_dot2_f32_bf16 v40, v18, s58, v40\n"
    "v_dot2_f32_bf16 v25, v6, s58, v25\n"
    "v_dot2_f32_bf16 v33, v14, s58, v33\n"
    "v_dot2_f32_bf16 v41, v22, s58, v41\n"
    "v_dot2_f32_bf16 v26, v2, v2, v26\n"
    "v_dot2_f32_bf16 v34, v10, v10, v34\n"
    "v_dot2_f32_bf16 v42, v18, v18, v42\n"
    "v_dot2_f32_bf16 v27, v6, v6, v27\n"
    "v_dot2_f32_bf16 v35, v14, v14, v35\n"
    "v_dot2_f32_bf16 v43, v22, v22, v43\n"
    "v_dot2_f32_bf16 v24, v3, s58, v24\n"
    "v_dot2_f32_bf16 v32, v11, s58, v32\n"
    "v_dot2_f32_bf16 v40, v19, s58, v40\n"
    "v_dot2_f32_bf16 v25, v7, s58, v25\n"
    "v_dot2_f32_bf16 v33, v15, s58, v33\n"
    "v_dot2_f32_bf16 v41, v23, s58, v41\n"
    "v_dot2_f32_bf16 v26, v3, v3, v26\n"
    "v_dot2_f32_bf16 v34, v11, v11, v34\n"
    "v_dot2_f32_bf16 v42, v19, v19, v42\n"
    "v_dot2_f32_bf16 v27, v7, v7, v27\n"
    "v_dot2_f32_bf16 v35, v15, v15, v35\n"
    "v_dot2_f32_bf16 v43, v23, v23, v43\n"
    "v_and_b32_e32 v30, 0xffff0000, v7\n"
    "v_and_b32_e32 v38, 0xffff0000, v15\n"
    "v_and_b32_e32 v46, 0xffff0000, v23\n"
    "v_add_f32_e32 v24, v24, v25\n"
    "v_add_f32_e32 v32, v32, v33\n"
    "v_add_f32_e32 v40, v40, v41\n"
    "v_add_f32_e32 v26, v26, v27\n"
    "v_add_f32_e32 v34, v34, v35\n"
    "v_add_f32_e32 v42, v42, v43\n"
    "v_add_f32_dpp v25, v25, v25 quad_perm:[1,0,3,2] row_mask:0xf bank_mask:0xf bound_ctrl:1\n"
    "v_lshlrev_b32_e32 v48, 16, v0\n"
    "v_add_f32_dpp v33, v33, v33 quad_perm:[1,0,3,2] row_mask:0xf bank_mask:0xf bound_ctrl:1\n"
    "v_and_b32_e32 v49, 0xffff0000, v0\n"
    "v_add_f32_dpp v41, v41, v41 quad_perm:[1,0,3,2] row_mask:0xf bank_mask:0xf bound_ctrl:1\n"
    "v_lshlrev_b32_e32 v50, 16, v1\n"
    "v_mov_b32_dpp v30, v30 quad_perm:[3,3,3,3] row_mask:0xf bank_mask:0xf bound_ctrl:1\n"
    "v_and_b32_e32 v51, 0xffff0000, v1\n"
    "v_mov_b32_dpp v38, v38 quad_perm:[3,3,3,3] row_mask:0xf bank_mask:0xf bound_ctrl:1\n"
    "v_lshlrev_b32_e32 v52, 16, v2\n"
    "v_mov_b32_dpp v46, v46 quad_perm:[3,3,3,3] row_mask:0xf bank_mask:0xf bound_ctrl:1\n"
    "v_and_b32_e32 v53, 0xffff0000, v2\n"
    "v_add_f32_dpp v24, v24, v24 quad_perm:[1,0,3,2] row_mask:0xf bank_mask:0xf bound_ctrl:1\n"
    "v_lshlrev_b32_e32 v54, 16, v3\n"
    "v_add_f32_dpp v32, v32, v32 quad_perm:[1,0,3,2] row_mask:0xf bank_mask:0xf bound_ctrl:1\n"
    "v_and_b32_e32 v55, 0xffff0000, v3\n"
    "v_add_f32_dpp v40, v40, v40 quad_perm:[1,0,3,2] row_mask:0xf bank_mask:0xf bound_ctrl:1\n"
    "v_lshlrev_b32_e32 v56, 16, v4\n"
    "v_add_f32_dpp v26, v26, v26 quad_perm:[1,0,3,2] row_mask:0xf bank_mask:0xf bound_ctrl:1\n"
    "v_and_b32_e32 v57, 0xffff0000, v4\n"
    "v_add_f32_dpp v34, v34, v34 quad_perm:[1,0,3,2] row_mask:0xf bank_mask:0xf bound_ctrl:1\n"
    "v_lshlrev_b32_e32 v58, 16, v5\n"
    "s_waitcnt vmcnt(0)\n"
    "buffer_store_dword v112, v113, s[8:11], s53 offen offset:0 sc1\n"
    "v_add_f32_dpp v42, v42, v42 quad_perm:[1,0,3,2] row_mask:0xf bank_mask:0xf bound_ctrl:1\n"
    "v_and_b32_e32 v59, 0xffff0000, v5\n"
    "v_add_f32_dpp v24, v24, v24 quad_perm:[2,3,0,1] row_mask:0xf bank_mask:0xf bound_ctrl:1\n"
    "v_lshlrev_b32_e32 v60, 16, v6\n"
    "v_add_f32_dpp v32, v32, v32 quad_perm:[2,3,0,1] row_mask:0xf bank_mask:0xf bound_ctrl:1\n"
    "v_and_b32_e32 v61, 0xffff0000, v6\n"
    "s_waitcnt lgkmcnt(0)\n"
    "s_barrier\n"
    "ds_read_b128 v[96:99], v108 offset:0\n"
    "ds_read_b128 v[100:103], v108 offset:4096\n"
    "s_lshl_b32 s45, %1, 6\n"
    "s_add_u32 s45, s45, 34956288\n"
    "v_add_f32_dpp v40, v40, v40 quad_perm:[2,3,0,1] row_mask:0xf bank_mask:0xf bound_ctrl:1\n"
    "v_lshlrev_b32_e32 v62, 16, v7\n"
    "v_add_f32_dpp v26, v26, v26 quad_perm:[2,3,0,1] row_mask:0xf bank_mask:0xf bound_ctrl:1\n"
    "v_and_b32_e32 v63, 0xffff0000, v7\n"
    "v_add_f32_dpp v34, v34, v34 quad_perm:[2,3,0,1] row_mask:0xf bank_mask:0xf bound_ctrl:1\n"
    "v_lshlrev_b32_e32 v64, 16, v8\n"
    "v_add_f32_dpp v42, v42, v42 quad_perm:[2,3,0,1] row_mask:0xf bank_mask:0xf bound_ctrl:1\n"
    "v_and_b32_e32 v65, 0xffff0000, v8\n"
    "v_add_f32_dpp v24, v24, v24 row_half_mirror row_mask:0xf bank_mask:0xf bound_ctrl:1\n"
    "v_lshlrev_b32_e32 v66, 16, v9\n"
    "v_add_f32_dpp v32, v32, v32 row_half_mirror row_mask:0xf bank_mask:0xf bound_ctrl:1\n"
    "v_and_b32_e32 v67, 0xffff0000, v9\n"
    "v_add_f32_dpp v40, v40, v40 row_half_mirror row_mask:0xf bank_mask:0xf bound_ctrl:1\n"
    "v_lshlrev_b32_e32 v68, 16, v10\n"
    "v_add_f32_dpp v26, v26, v26 row_half_mirror row_mask:0xf bank_mask:0xf bound_ctrl:1\n"
    "v_and_b32_e32 v69, 0xffff0000, v10\n"
    "v_add_f32_dpp v34, v34, v34 row_half_mirror row_mask:0xf bank_mask:0xf bound_ctrl:1\n"
    "v_lshlrev_b32_e32 v70, 16, v11\n"
    "v_add_f32_dpp v42, v42, v42 row_half_mirror row_mask:0xf bank_mask:0xf bound_ctrl:1\n"
    "v_and_b32_e32 v71, 0xffff0000, v11\n"
    "v_add_f32_dpp v24, v24, v24 row_mirror row_mask:0xf bank_mask:0xf bound_ctrl:1\n"
    "v_lshlrev_b32_e32 v72, 16, v12\n"
    "v_add_f32_dpp v32, v32, v32 row_mirror row_mask:0xf bank_mask:0xf bound_ctrl:1\n"
    "v_and_b32_e32 v73, 0xffff0000, v12\n"
    "v_add_f32_dpp v40, v40, v40 row_mirror row_mask:0xf bank_mask:0xf bound_ctrl:1\n"
    "v_lshlrev_b32_e32 v74, 16, v13\n"
    "v_add_f32_dpp v26, v26, v26 row_mirror row_mask:0xf bank_mask:0xf bound_ctrl:1\n"
    "v_and_b32_e32 v75, 0xffff0000, v13\n"
    "v_add_f32_dpp v34, v34, v34 row_mirror row_mask:0xf bank_mask:0xf bound_ctrl:1\n"
    "v_lshlrev_b32_e32 v76, 16, v14\n"
    "s_waitcnt lgkmcnt(0)\n"
    "buffer_store_dwordx4 v[96:99], v109, s[8:11], s45 offen offset:0 sc1\n"
    "buffer_store_dwordx4 v[100:103], v110, s[8:11], s45 offen offset:0 sc1\n"
    "v_add_f32_dpp v42, v42, v42 row_mirror row_mask:0xf bank_mask:0xf bound_ctrl:1\n"
    "v_and_b32_e32 v77, 0xffff0000, v14\n"
    "v_mul_f32_e32 v24, 0x3b800000, v24\n"
    "v_lshlrev_b32_e32 v78, 16, v15\n"
    "v_mul_f32_e32 v32, 0x3b800000, v32\n"
    "v_and_b32_e32 v79, 0xffff0000, v15\n"
    "v_mul_f32_e32 v40, 0x3b800000, v40\n"
    "v_lshlrev_b32_e32 v80, 16, v16\n"
    "v_mul_f32_e32 v25, 0.25, v25\n"
    "v_and_b32_e32 v81, 0xffff0000, v16\n"
    "v_mul_f32_e32 v33, 0.25, v33\n"
    "v_lshlrev_b32_e32 v82, 16, v17\n"
    "v_mul_f32_e32 v41, 0.25, v41\n"
    "v_and_b32_e32 v83, 0xffff0000, v17\n"
    "v_mul_f32_e64 v27, v24, -v24\n"
    "v_lshlrev_b32_e32 v84, 16, v18\n"
    "v_mul_f32_e64 v35, v32, -v32\n"
    "v_and_b32_e32 v85, 0xffff0000, v18\n"
    "v_mul_f32_e64 v43, v40, -v40\n"
    "v_lshlrev_b32_e32 v86, 16, v19\n"
    "v_sub_f32_e32 v25, v25, v30\n"
    "v_and_b32_e32 v87, 0xffff0000, v19\n"
    "v_sub_f32_e32 v33, v33, v38\n"
    "v_lshlrev_b32_e32 v88, 16, v20\n"
    "v_sub_f32_e32 v41, v41, v46\n"
    "v_and_b32_e32 v89, 0xffff0000, v20\n"
    "v_fmac_f32_e32 v27, 0x3b800000, v26\n"
    "v_lshlrev_b32_e32 v90, 16, v21\n"
    "v_fmac_f32_e32 v35, 0x3b800000, v34\n"
    "v_and_b32_e32 v91, 0xffff0000, v21\n"
    "v_fmac_f32_e32 v43, 0x3b800000, v42\n"
    "v_lshlrev_b32_e32 v92, 16, v22\n"
    "v_mul_f32_e32 v25, 0x3eaaaaab, v25\n"
    "v_and_b32_e32 v93, 0xffff0000, v22\n"
    "v_mul_f32_e32 v33, 0x3eaaaaab, v33\n"
    "v_lshlrev_b32_e32 v94, 16, v23\n"
    "v_mul_f32_e32 v41, 0x3eaaaaab, v41\n"
    "v_and_b32_e32 v95, 0xffff0000, v23\n"
    "v_max_f32_e32 v27, 0, v27\n"
    "v_max_f32_e32 v35, 0, v35\n"
    "v_max_f32_e32 v43, 0, v43\n"
    "v_cndmask_b32_e64 v25, v24, v25, s[62:63]\n"
    "v_cndmask_b32_e64 v33, v32, v33, s[62:63]\n"
    "v_cndmask_b32_e64 v41, v40, v41, s[62:63]\n"
    "v_add_f32_e32 v27, 0x3727c5ac, v27\n"
    "v_add_f32_e32 v35, 0x3727c5ac, v35\n"
    "v_add_f32_e32 v43, 0x3727c5ac, v43\n"
    "v_rsq_f32_e32 v28, v27\n"
    "v_rsq_f32_e32 v36, v35\n"
    "v_rsq_f32_e32 v44, v43\n"
    "v_fma_f32 v29, -v24, v28, s57\n"
    "v_fma_f32 v37, -v32, v36, s57\n"
    "v_fma_f32 v45, -v40, v44, s57\n"
    "v_fma_f32 v31, -v25, v28, s57\n"
    "v_fma_f32 v39, -v33, v36, s57\n"
    "v_fma_f32 v47, -v41, v44, s57\n"
    "v_sub_f32_e32 v24, v29, v31\n"
    "v_sub_f32_e32 v32, v37, v39\n"
    "v_sub_f32_e32 v40, v45, v47\n"
    "v_pk_fma_f32 v[48:49], v[48:49], v[28:29], v[28:29] op_sel:[0,0,1] op_sel_hi:[1,0,1]\n"
    "v_pk_fma_f32 v[50:51], v[50:51], v[28:29], v[28:29] op_sel:[0,0,1] op_sel_hi:[1,0,1]\n"
    "v_pk_fma_f32 v[52:53], v[52:53], v[28:29], v[28:29] op_sel:[0,0,1] op_sel_hi:[1,0,1]\n"
    "v_pk_fma_f32 v[54:55], v[54:55], v[28:29], v[28:29] op_sel:[0,0,1] op_sel_hi:[1,0,1]\n"
    "v_pk_fma_f32 v[56:57], v[56:57], v[28:29], v[30:31] op_sel:[0,0,1] op_sel_hi:[1,0,1]\n"
    "v_pk_fma_f32 v[58:59], v[58:59], v[28:29], v[30:31] op_sel:[0,0,1] op_sel_hi:[1,0,1]\n"
    "v_pk_fma_f32 v[60:61], v[60:61], v[28:29], v[30:31] op_sel:[0,0,1] op_sel_hi:[1,0,1]\n"
    "v_pk_fma_f32 v[62:63], v[62:63], v[28:29], v[30:31] op_sel:[0,0,1] op_sel_hi:[1,0,1]\n"
    "v_pk_add_f32 v[48:49], v[48:49], s[56:57] op_sel_hi:[1,0]\n"
    "v_pk_add_f32 v[50:51], v[50:51], s[56:57] op_sel_hi:[1,0]\n"
    "v_pk_add_f32 v[52:53], v[52:53], s[56:57] op_sel_hi:[1,0]\n"
    "v_pk_add_f32 v[54:55], v[54:55], s[56:57] op_sel_hi:[1,0]\n"
    "v_pk_add_f32 v[56:57], v[56:57], s[56:57] op_sel_hi:[1,0]\n"
    "v_pk_add_f32 v[58:59], v[58:59], s[56:57] op_sel_hi:[1,0]\n"
    "v_pk_add_f32 v[60:61], v[60:61], s[56:57] op_sel_hi:[1,0]\n"
    "v_pk_add_f32 v[62:63], v[62:63], s[56:57] op_sel_hi:[1,0]\n"
    "v_cvt_pk_bf16_f32 v0, v48, v49\n"
    "v_cvt_pk_bf16_f32 v1, v50, v51\n"
    "v_cvt_pk_bf16_f32 v2, v52, v53\n"
    "v_cvt_pk_bf16_f32 v3, v54, v55\n"
    "v_cvt_pk_bf16_f32 v4, v56, v57\n"
    "v_cvt_pk_bf16_f32 v5, v58, v59\n"
    "v_cvt_pk_bf16_f32 v6, v60, v61\n"
    "v_cvt_pk_bf16_f32 v7, v62, v63\n"
    "buffer_store_dwordx4 v[0:3], v104, s[8:11], s35 offen offset:0 sc1\n"
    "v_bfi_b32 v7, v107, v7, 1.0\n"
    "ds_write_b128 v105, v[0:3] offset:0\n"
    "buffer_store_dwordx4 v[4:7], v104, s[8:11], s35 offen offset:256 sc1\n"
    "v_pk_fma_f32 v[64:65], v[64:65], v[36:37], v[36:37] op_sel:[0,0,1] op_sel_hi:[1,0,1]\n"
    "v_pk_fma_f32 v[66:67], v[66:67], v[36:37], v[36:37] op_sel:[0,0,1] op_sel_hi:[1,0,1]\n"
    "v_pk_fma_f32 v[68:69], v[68:69], v[36:37], v[36:37] op_sel:[0,0,1] op_sel_hi:[1,0,1]\n"
    "v_pk_fma_f32 v[70:71], v[70:71], v[36:37], v[36:37] op_sel:[0,0,1] op_sel_hi:[1,0,1]\n"
    "v_pk_fma_f32 v[72:73], v[72:73], v[36:37], v[38:39] op_sel:[0,0,1] op_sel_hi:[1,0,1]\n"
    "v_pk_fma_f32 v[74:75], v[74:75], v[36:37], v[38:39] op_sel:[0,0,1] op_sel_hi:[1,0,1]\n"
    "v_pk_fma_f32 v[76:77], v[76:77], v[36:37], v[38:39] op_sel:[0,0,1] op_sel_hi:[1,0,1]\n"
    "v_pk_fma_f32 v[78:79], v[78:79], v[36:37], v[38:39] op_sel:[0,0,1] op_sel_hi:[1,0,1]\n"
    "v_pk_add_f32 v[64:65], v[64:65], s[56:57] op_sel_hi:[1,0]\n"
    "v_pk_add_f32 v[66:67], v[66:67], s[56:57] op_sel_hi:[1,0]\n"
    "v_pk_add_f32 v[68:69], v[68:69], s[56:57] op_sel_hi:[1,0]\n"
    "v_pk_add_f32 v[70:71], v[70:71], s[56:57] op_sel_hi:[1,0]\n"
    "v_pk_add_f32 v[72:73], v[72:73], s[56:57] op_sel_hi:[1,0]\n"
    "v_pk_add_f32 v[74:75], v[74:75], s[56:57] op_sel_hi:[1,0]\n"
    "v_pk_add_f32 v[76:77], v[76:77], s[56:57] op_sel_hi:[1,0]\n"
    "v_pk_add_f32 v[78:79], v[78:79], s[56:57] op_sel_hi:[1,0]\n"
    "v_cvt_pk_bf16_f32 v8, v64, v65\n"
    "v_cvt_pk_bf16_f32 v9, v66, v67\n"
    "v_cvt_pk_bf16_f32 v10, v68, v69\n"
    "v_cvt_pk_bf16_f32 v11, v70, v71\n"
    "v_cvt_pk_bf16_f32 v12, v72, v73\n"
    "v_cvt_pk_bf16_f32 v13, v74, v75\n"
    "v_cvt_pk_bf16_f32 v14, v76, v77\n"
    "v_cvt_pk_bf16_f32 v15, v78, v79\n"
    "buffer_store_dwordx4 v[8:11], v104, s[8:11], s54 offen offset:0 sc1\n"
    "v_bfi_b32 v15, v107, v15, 1.0\n"
    "ds_write_b128 v105, v[8:11] offset:8192\n"
    "buffer_store_dwordx4 v[12:15], v104, s[8:11], s54 offen offset:256 sc1\n"
    "v_pk_fma_f32 v[80:81], v[80:81], v[44:45], v[44:45] op_sel:[0,0,1] op_sel_hi:[1,0,1]\n"
    "v_pk_fma_f32 v[82:83], v[82:83], v[44:45], v[44:45] op_sel:[0,0,1] op_sel_hi:[1,0,1]\n"
    "v_pk_fma_f32 v[84:85], v[84:85], v[44:45], v[44:45] op_sel:[0,0,1] op_sel_hi:[1,0,1]\n"
    "v_pk_fma_f32 v[86:87], v[86:87], v[44:45], v[44:45] op_sel:[0,0,1] op_sel_hi:[1,0,1]\n"
    "v_pk_fma_f32 v[88:89], v[88:89], v[44:45], v[46:47] op_sel:[0,0,1] op_sel_hi:[1,0,1]\n"
    "v_pk_fma_f32 v[90:91], v[90:91], v[44:45], v[46:47] op_sel:[0,0,1] op_sel_hi:[1,0,1]\n"
    "v_pk_fma_f32 v[92:93], v[92:93], v[44:45], v[46:47] op_sel:[0,0,1] op_sel_hi:[1,0,1]\n"
    "v_pk_fma_f32 v[94:95], v[94:95], v[44:45], v[46:47] op_sel:[0,0,1] op_sel_hi:[1,0,1]\n"
    "v_pk_add_f32 v[80:81], v[80:81], s[56:57] op_sel_hi:[1,0]\n"
    "v_pk_add_f32 v[82:83], v[82:83], s[56:57] op_sel_hi:[1,0]\n"
    "v_pk_add_f32 v[84:85], v[84:85], s[56:57] op_sel_hi:[1,0]\n"
    "v_pk_add_f32 v[86:87], v[86:87], s[56:57] op_sel_hi:[1,0]\n"
    "v_pk_add_f32 v[88:89], v[88:89], s[56:57] op_sel_hi:[1,0]\n"
    "v_pk_add_f32 v[90:91], v[90:91], s[56:57] op_sel_hi:[1,0]\n"
    "v_pk_add_f32 v[92:93], v[92:93], s[56:57] op_sel_hi:[1,0]\n"
    "v_pk_add_f32 v[94:95], v[94:95], s[56:57] op_sel_hi:[1,0]\n"
    "v_cvt_pk_bf16_f32 v16, v80, v81\n"
    "v_cvt_pk_bf16_f32 v17, v82, v83\n"
    "v_cvt_pk_bf16_f32 v18, v84, v85\n"
    "v_cvt_pk_bf16_f32 v19, v86, v87\n"
    "v_cvt_pk_bf16_f32 v20, v88, v89\n"
    "v_cvt_pk_bf16_f32 v21, v90, v91\n"
    "v_cvt_pk_bf16_f32 v22, v92, v93\n"
    "v_cvt_pk_bf16_f32 v23, v94, v95\n"
    "buffer_store_dwordx4 v[16:19], v104, s[8:11], s60 offen offset:0 sc1\n"
    "v_bfi_b32 v23, v107, v23, 1.0\n"
    "ds_write_b128 v105, v[16:19] offset:16384\n"
    "buffer_store_dwordx4 v[20:23], v104, s[8:11], s60 offen offset:256 sc1\n"
    "v_pk_add_f32 v[56:57], v[56:57], v[24:25] op_sel_hi:[1,0]\n"
    "v_pk_add_f32 v[58:59], v[58:59], v[24:25] op_sel_hi:[1,0]\n"
    "v_pk_add_f32 v[60:61], v[60:61], v[24:25] op_sel_hi:[1,0]\n"
    "v_pk_add_f32 v[62:63], v[62:63], v[24:25] op_sel_hi:[1,0]\n"
    "v_cvt_pk_bf16_f32 v56, v56, v57\n"
    "v_cvt_pk_bf16_f32 v57, v58, v59\n"
    "v_cvt_pk_bf16_f32 v58, v60, v61\n"
    "v_cvt_pk_bf16_f32 v59, v62, v63\n"
    "ds_write_b128 v106, v[56:59] offset:0\n"
    "v_pk_add_f32 v[72:73], v[72:73], v[32:33] op_sel_hi:[1,0]\n"
    "v_pk_add_f32 v[74:75], v[74:75], v[32:33] op_sel_hi:[1,0]\n"
    "v_pk_add_f32 v[76:77], v[76:77], v[32:33] op_sel_hi:[1,0]\n"
    "v_pk_add_f32 v[78:79], v[78:79], v[32:33] op_sel_hi:[1,0]\n"
    "v_cvt_pk_bf16_f32 v72, v72, v73\n"
    "v_cvt_pk_bf16_f32 v73, v74, v75\n"
    "v_cvt_pk_bf16_f32 v74, v76, v77\n"
    "v_cvt_pk_bf16_f32 v75, v78, v79\n"
    "ds_write_b128 v106, v[72:75] offset:8192\n"
    "v_pk_add_f32 v[88:89], v[88:89], v[40:41] op_sel_hi:[1,0]\n"
    "v_pk_add_f32 v[90:91], v[90:91], v[40:41] op_sel_hi:[1,0]\n"
    "v_pk_add_f32 v[92:93], v[92:93], v[40:41] op_sel_hi:[1,0]\n"
    "v_pk_add_f32 v[94:95], v[94:95], v[40:41] op_sel_hi:[1,0]\n"
    "v_cvt_pk_bf16_f32 v88, v88, v89\n"
    "v_cvt_pk_bf16_f32 v89, v90, v91\n"
    "v_cvt_pk_bf16_f32 v90, v92, v93\n"
    "v_cvt_pk_bf16_f32 v91, v94, v95\n"
    "ds_write_b128 v106, v[88:91] offset:16384\n"
    "s_waitcnt lgkmcnt(0)\n"
    "s_barrier\n"
    "ds_read_b128 a[0:3], v250 offset:0\n"
    "ds_read_b128 a[32:35], v250 offset:8192\n"
    "ds_read_b128 a[64:67], v250 offset:16384\n"
    "ds_read_b128 a[4:7], v251 offset:0\n"
    "ds_read_b128 a[36:39], v251 offset:8192\n"
    "ds_read_b128 a[68:71], v251 offset:16384\n"
    "ds_read_b128 a[8:11], v250 offset:2048\n"
    "ds_read_b128 a[40:43], v250 offset:10240\n"
    "ds_read_b128 a[72:75], v250 offset:18432\n"
    "ds_read_b128 a[12:15], v251 offset:2048\n"
    "ds_read_b128 a[44:47], v251 offset:10240\n"
    "ds_read_b128 a[76:79], v251 offset:18432\n"
    "buffer_load_dword v255, v252, s[8:11], 0 offen offset:0 sc1\n"
    "ds_read_b128 a[16:19], v250 offset:4096\n"
    "ds_read_b128 a[48:51], v250 offset:12288\n"
    "ds_read_b128 a[80:83], v250 offset:20480\n"
    "ds_read_b128 a[20:23], v251 offset:4096\n"
    "ds_read_b128 a[52:55], v251 offset:12288\n"
    "ds_read_b128 a[84:87], v251 offset:20480\n"
    "ds_read_b128 a[24:27], v250 offset:6144\n"
    "ds_read_b128 a[56:59], v250 offset:14336\n"
    "ds_read_b128 a[88:91], v250 offset:22528\n"
    "ds_read_b128 a[28:31], v251 offset:6144\n"
    "ds_read_b128 a[60:63], v251 offset:14336\n"
    "ds_read_b128 a[92:95], v251 offset:22528\n"
    "s_lshl_b32 s52, %2, 15\n"
    "s_add_u32 s52, s52, 5242880\n"
    "s_lshl_b32 s61, s37, 12\n"
    "s_lshl_b32 s46, %1, 4\n"
    "s_lshl_b32 s47, %1, 7\n"
    "s_add_u32 s47, s47, 4096\n"
    "s_cmp_eq_u32 s50, 0\n"
    "s_cselect_b32 s46, s46, s47\n"
    "s_add_u32 s46, s46, 5652480\n"
    "s_cmp_eq_u32 s51, 0\n"
    "s_cselect_b32 s46, s46, 2.0\n"
    "v_mov_b32_e32 v40, s39\n"
    "v_mov_b32_e32 v41, 0\n"
    "s_waitcnt vmcnt(1)\n"
    "s_waitcnt lgkmcnt(0)\n"
    "s_barrier\n"
    "buffer_store_dword v40, v41, s[8:11], s46 offen offset:0 sc1\n"
    "s_waitcnt vmcnt(1)\n"
    "v_cmp_ne_u32_e32 vcc, s39, v255\n"
    "s_nop 4\n"
    "s_cbranch_vccnz .Lat_apoll\n"
    ".Lat_apoll_ret:\n"
    "s_add_u32 s47, s52, s61\n"
    "buffer_load_dwordx4 v[112:115], v253, s[8:11], s47 offen offset:0 sc1\n"
    "buffer_load_dwordx4 v[116:119], v253, s[8:11], s47 offen offset:1024 sc1\n"
    "buffer_load_dwordx4 v[128:131], v253, s[8:11], s47 offen offset:2048 sc1\n"
    "s_add_u32 s61, s61, 0x1000\n"
    "buffer_load_dwordx4 v[132:135], v253, s[8:11], s47 offen offset:3072 sc1\n"
    "s_bitset0_b32 s61, 14\n"
    "s_add_u32 s47, s52, s61\n"
    "buffer_load_dwordx4 v[144:147], v253, s[8:11], s47 offen offset:0 sc1\n"
    "buffer_load_dwordx4 v[148:151], v253, s[8:11], s47 offen offset:1024 sc1\n"
    "buffer_load_dwordx4 v[160:163], v253, s[8:11], s47 offen offset:2048 sc1\n"
    "s_add_u32 s61, s61, 0x1000\n"
    "buffer_load_dwordx4 v[164:167], v253, s[8:11], s47 offen offset:3072 sc1\n"
    "s_bitset0_b32 s61, 14\n"
    "s_add_u32 s47, s52, s61\n"
    "buffer_load_dwordx4 v[176:179], v253, s[8:11], s47 offen offset:0 sc1\n"
    "buffer_load_dwordx4 v[180:183], v253, s[8:11], s47 offen offset:1024 sc1\n"
    "buffer_load_dwordx4 v[192:195], v253, s[8:11], s47 offen offset:2048 sc1\n"
    "s_add_u32 s61, s61, 0x1000\n"
    "s_lshl_b32 s59, s32, 4\n"
    "s_lshr_b32 s48, s32, 1\n"
    "s_sub_u32 s59, s59, s48\n"
    "s_lshl_b32 s59, s59, 6\n"
    "s_sub_u32 s48, 3984, s59\n"
    "v_cmp_le_u32_e32 vcc, 0x40, v253\n"
    "v_mov_b32_e32 v0, s48\n"
    "s_cmp_eq_u32 s32, 2\n"
    "s_cselect_b32 s48, 1024, 0\n"
    "v_mov_b32_e32 v2, s48\n"
    "s_add_u32 s59, s59, 5652480\n"
    "v_cndmask_b32_e32 v3, v253, v0, vcc\n"
    "s_lshl_b32 s45, %2, 11\n"
    "s_add_u32 s48, s45, %3\n"
    "v_add_u32_e32 v9, s48, v253\n"
    "s_add_u32 s45, s45, 10940416\n"
    "v_add_u32_e32 v8, s45, v253\n"
    "v_lshrrev_b32_e32 v13, 7, v253\n"
    "v_and_b32_e32 v12, 0x70, v254\n"
    "v_lshl_add_u32 v12, v13, 9, v12\n"
    "v_add_u32_e32 v13, 0xc00, v12\n"
    "v_add_u32_e32 v14, 0x1800, v12\n"
    "v_add_u32_e32 v15, 0x2400, v12\n"
    "s_lshl_b32 s57, s50, 8\n"
    "s_add_u32 s57, s57, 5111808\n"
    "s_add_u32 s62, s43, s34\n"
    "s_xor_b32 s63, s34, 0xc000\n"
    "s_add_u32 s63, s63, s43\n"
    "s_waitcnt vmcnt(9)\n"
    "v_mfma_f32_16x16x32_bf16 v[24:27], v[112:115], a[0:3], 0\n"
    "buffer_load_dwordx4 v[196:199], v253, s[8:11], s47 offen offset:3072 sc1\n"
    "v_mfma_f32_16x16x32_bf16 v[28:31], v[116:119], a[0:3], 0\n"
    "s_bitset0_b32 s61, 14\n"
    "s_mov_b32 s18, 5120000\n"
    "s_mov_b32 s19, 0x20000\n"
    "v_mfma_f32_16x16x32_bf16 v[48:51], v[112:115], a[32:35], 0\n"
    "s_add_u32 s47, s52, s61\n"
    "v_xor_b32_e32 v242, 0x12000, v240\n"
    "v_xor_b32_e32 v243, 0x12000, v241\n"
    "v_mfma_f32_16x16x32_bf16 v[52:55], v[116:119], a[32:35], 0\n"
    "buffer_load_dwordx4 v[208:211], v253, s[8:11], s47 offen offset:0 sc1\n"
    "v_mfma_f32_16x16x32_bf16 v[64:67], v[112:115], a[64:67], 0\n"
    "s_cmp_eq_u32 %2, 3\n"
    "s_cselect_b32 s88, 0x2ff, -1\n"
    "v_mov_b32_e32 v10, 0xc2980000\n"
    "v_mfma_f32_16x16x32_bf16 v[68:71], v[116:119], a[64:67], 0\n"
    "s_waitcnt vmcnt(9)\n"
    "v_cmp_lt_u32_e32 vcc, s88, v253\n"
    "s_nop 1\n"
    "v_cndmask_b32_e32 v10, 0, v10, vcc\n"
    "v_mfma_f32_16x16x32_bf16 v[24:27], v[128:131], a[4:7], v[24:27]\n"
    "buffer_load_dwordx4 v[212:215], v253, s[8:11], s47 offen offset:1024 sc1\n"
    "v_mfma_f32_16x16x32_bf16 v[28:31], v[132:135], a[4:7], v[28:31]\n"
    "s_add_u32 s52, s52, 0x4000\n"
    "v_lshrrev_b32_e32 v5, 4, v253\n"
    "v_min_u32_e32 v5, 2, v5\n"
    "v_mfma_f32_16x16x32_bf16 v[48:51], v[128:131], a[36:39], v[48:51]\n"
    "s_add_u32 s88, s32, 1\n"
    "v_add_u32_e32 v5, s88, v5\n"
    "v_mov_b32_e32 v80, 0\n"
    "v_mfma_f32_16x16x32_bf16 v[52:55], v[132:135], a[36:39], v[52:55]\n"
    "buffer_load_dwordx4 v[224:227], v253, s[8:11], s47 offen offset:2048 sc1\n"
    "v_mfma_f32_16x16x32_bf16 v[64:67], v[128:131], a[68:71], v[64:67]\n"
    "s_add_u32 s61, s61, 0x1000\n"
    "v_and_b32_e32 v5, 3, v5\n"
    "v_lshlrev_b32_e32 v5, 7, v5\n"
    "v_mfma_f32_16x16x32_bf16 v[68:71], v[132:135], a[68:71], v[68:71]\n"
    "s_waitcnt vmcnt(9)\n"
    "s_lshl_b32 s58, %1, 7\n"
    "s_add_u32 s58, s58, 5656576\n"
    "s_cmp_eq_u32 %2, 2\n"
    "s_cselect_b32 s58, s58, 2.0\n"
    "v_mfma_f32_16x16x32_bf16 v[24:27], v[144:147], a[8:11], v[24:27]\n"
    "buffer_load_dwordx4 v[228:231], v253, s[8:11], s47 offen offset:3072 sc1\n"
    "v_mfma_f32_16x16x32_bf16 v[28:31], v[148:151], a[8:11], v[28:31]\n"
    "s_bitset0_b32 s61, 14\n"
    "s_lshl_b32 s88, s50, 13\n"
    "s_add_u32 s88, s88, %3\n"
    "v_mfma_f32_16x16x32_bf16 v[48:51], v[144:147], a[40:43], v[48:51]\n"
    "s_add_u32 s47, s52, s61\n"
    "v_add_u32_e32 v6, s88, v253\n"
    "v_add_u32_e32 v7, %3, v253\n"
    "v_mfma_f32_16x16x32_bf16 v[52:55], v[148:151], a[40:43], v[52:55]\n"
    "buffer_load_dwordx4 v[120:123], v253, s[8:11], s47 offen offset:0 sc1\n"
    "v_mfma_f32_16x16x32_bf16 v[64:67], v[144:147], a[72:75], v[64:67]\n"
    "s_mul_i32 s88, %2, 1024\n"
    "s_add_u32 s88, s88, %3\n"
    "v_lshrrev_b32_e32 v250, 2, v253\n"
    "v_mfma_f32_16x16x32_bf16 v[68:71], v[148:151], a[72:75], v[68:71]\n"
    "s_waitcnt vmcnt(9)\n"
    "v_add_u32_e32 v249, s88, v250\n"
    "s_mov_b32 s49, s88\n"
    "v_mfma_f32_16x16x32_bf16 v[24:27], v[160:163], a[12:15], v[24:27]\n"
    "buffer_load_dwordx4 v[124:127], v253, s[8:11], s47 offen offset:1024 sc1\n"
    "v_mfma_f32_16x16x32_bf16 v[28:31], v[164:167], a[12:15], v[28:31]\n"
    "s_lshl_b32 s81, s88, 2\n"
    "s_lshl2_add_u32 s90, %1, %2\n"
    "s_lshl_b32 s90, s90, 10\n"
    "v_mfma_f32_16x16x32_bf16 v[48:51], v[160:163], a[44:47], v[48:51]\n"
    "s_sub_u32 s81, s90, s81\n"
    "s_add_u32 s81, s81, 36287488\n"
    "s_xor_b32 s88, %2, 2\n"
    "v_mfma_f32_16x16x32_bf16 v[52:55], v[164:167], a[44:47], v[52:55]\n"
    "buffer_load_dwordx4 v[136:139], v253, s[8:11], s47 offen offset:2048 sc1\n"
    "v_mfma_f32_16x16x32_bf16 v[64:67], v[160:163], a[76:79], v[64:67]\n"
    "s_add_u32 s61, s61, 0x1000\n"
    "s_mul_i32 s88, s88, 1024\n"
    "s_add_u32 s88, s88, %3\n"
    "v_mfma_f32_16x16x32_bf16 v[68:71], v[164:167], a[76:79], v[68:71]\n"
    "s_waitcnt vmcnt(9)\n"
    "v_add_u32_e32 v250, s88, v250\n"
    "v_and_b32_e32 v251, 0x1f0, v253\n"
    "v_mfma_f32_16x16x32_bf16 v[24:27], v[176:179], a[16:19], v[24:27]\n"
    "buffer_load_dwordx4 v[140:143], v253, s[8:11], s47 offen offset:3072 sc1\n"
    "v_mfma_f32_16x16x32_bf16 v[28:31], v[180:183], a[16:19], v[28:31]\n"
    "s_bitset0_b32 s61, 14\n"
    "s_lshl_b32 s82, %2, 10\n"
    "s_add_u32 s82, s82, %3\n"
    "v_mfma_f32_16x16x32_bf16 v[48:51], v[176:179], a[48:51], v[48:51]\n"
    "s_add_u32 s47, s52, s61\n"
    "s_add_u32 s82, s82, 20480\n"
    "v_lshrrev_b32_e32 v252, 9, v253\n"
    "v_mfma_f32_16x16x32_bf16 v[52:55], v[180:183], a[48:51], v[52:55]\n"
    "buffer_load_dwordx4 v[152:155], v253, s[8:11], s47 offen offset:0 sc1\n"
    "v_mfma_f32_16x16x32_bf16 v[64:67], v[176:179], a[80:83], v[64:67]\n"
    "buffer_load_dword v0, v3, s[8:11], s59 offen offset:0 sc1\n"
    "v_mfma_f32_16x16x32_bf16 v[68:71], v[180:183], a[80:83], v[68:71]\n"
    "s_waitcnt vmcnt(10)\n"
    "s_lshr_b32 s80, %1, 4\n"
    "v_lshl_add_u32 v252, v252, 4, s82\n"
    "v_mfma_f32_16x16x32_bf16 v[24:27], v[192:195], a[20:23], v[24:27]\n"
    "buffer_load_dwordx4 v[156:159], v253, s[8:11], s47 offen offset:1024 sc1\n"
    "v_mfma_f32_16x16x32_bf16 v[28:31], v[196:199], a[20:23], v[28:31]\n"
    "s_lshl_b32 s80, s80, 7\n"
    "s_add_u32 s80, s80, 5693696\n"
    "s_lshr_b32 s88, s34, 9\n"
    "v_mfma_f32_16x16x32_bf16 v[48:51], v[192:195], a[52:55], v[48:51]\n"
    "s_add_u32 s80, s80, s88\n"
    "s_mov_b64 s[20:21], s[8:9]\n"
    "s_mov_b32 s22, 5242880\n"
    "v_mfma_f32_16x16x32_bf16 v[52:55], v[196:199], a[52:55], v[52:55]\n"
    "buffer_load_dwordx4 v[168:171], v253, s[8:11], s47 offen offset:2048 sc1\n"
    "v_mfma_f32_16x16x32_bf16 v[64:67], v[192:195], a[84:87], v[64:67]\n"
    "s_add_u32 s61, s61, 0x1000\n"
    "s_add_u32 s24, s8, 5697536\n"
    "s_addc_u32 s25, s9, 0\n"
    "v_mfma_f32_16x16x32_bf16 v[68:71], v[196:199], a[84:87], v[68:71]\n"
    "s_waitcnt vmcnt(10)\n"
    "s_mov_b32 s26, 5242880\n"
    "v_mov_b32_e32 v81, 0\n"
    "v_mfma_f32_16x16x32_bf16 v[24:27], v[208:211], a[24:27], v[24:27]\n"
    "buffer_load_dwordx4 v[172:175], v253, s[8:11], s47 offen offset:3072 sc1\n"
    "v_mfma_f32_16x16x32_bf16 v[28:31], v[212:215], a[24:27], v[28:31]\n"
    "s_bitset0_b32 s61, 14\n"
    "s_movk_i32 s42, 512\n"
    "s_mov_b32 s37, 0\n"
    "v_mfma_f32_16x16x32_bf16 v[48:51], v[208:211], a[56:59], v[48:51]\n"
    "s_add_u32 s47, s52, s61\n"
    "s_add_u32 s38, s38, s50\n"
    "v_bfe_u32 v16, v253, 4, 2\n"
    "v_mfma_f32_16x16x32_bf16 v[52:55], v[212:215], a[56:59], v[52:55]\n"
    "buffer_load_dwordx4 v[184:187], v253, s[8:11], s47 offen offset:0 sc1\n"
    "v_mfma_f32_16x16x32_bf16 v[64:67], v[208:211], a[88:91], v[64:67]\n"
    "v_bfe_u32 v17, v253, 6, 2\n"
    "v_mov_b32_e32 v18, 0x3f80\n"
    "v_mfma_f32_16x16x32_bf16 v[68:71], v[212:215], a[88:91], v[68:71]\n"
    "s_waitcnt vmcnt(10)\n"
    "v_cmp_eq_u32_e32 vcc, 3, v16\n"
    "s_nop 1\n"
    "v_cndmask_b32_e64 v18, v18, 1.0, vcc\n"
    "v_cndmask_b32_e64 v16, v16, 0, vcc\n"
    "v_mfma_f32_16x16x32_bf16 v[24:27], v[224:227], a[28:31], v[24:27]\n"
    "buffer_load_dwordx4 v[188:191], v253, s[8:11], s47 offen offset:1024 sc1\n"
    "v_mfma_f32_16x16x32_bf16 v[28:31], v[228:231], a[28:31], v[28:31]\n"
    "v_or_b32_e32 v16, 12, v16\n"
    "v_mul_u32_u24_e32 v16, 0x1111, v16\n"
    "v_mfma_f32_16x16x32_bf16 v[48:51], v[224:227], a[60:63], v[48:51]\n"
    "v_cmp_eq_u32_e32 vcc, 0, v17\n"
    "s_nop 1\n"
    "v_cndmask_b32_e32 v19, 0, v18, vcc\n"
    "v_mfma_f32_16x16x32_bf16 v[52:55], v[228:231], a[60:63], v[52:55]\n"
    "buffer_load_dwordx4 v[200:203], v253, s[8:11], s47 offen offset:2048 sc1\n"
    "v_mfma_f32_16x16x32_bf16 v[64:67], v[224:227], a[92:95], v[64:67]\n"
    "s_add_u32 s61, s61, 0x1000\n"
    "v_cvt_pk_bf16_f32 v24, v24, v25\n"
    "v_accvgpr_write_b32 a192, v19\n"
    "v_mfma_f32_16x16x32_bf16 v[68:71], v[228:231], a[92:95], v[68:71]\n"
    "s_waitcnt vmcnt(10)\n"
    "v_cvt_pk_bf16_f32 v25, v26, v27\n"
    "v_cmp_eq_u32_e32 vcc, 1, v17\n"
    "s_nop 1\n"
    "v_cndmask_b32_e32 v19, 0, v18, vcc\n"
    "v_mfma_f32_16x16x32_bf16 v[32:35], v[120:123], a[0:3], 0\n"
    "buffer_load_dwordx4 v[204:207], v253, s[8:11], s47 offen offset:3072 sc1\n"
    "v_mfma_f32_16x16x32_bf16 v[36:39], v[124:127], a[0:3], 0\n"
    "s_bitset0_b32 s61, 14\n"
    "v_cvt_pk_bf16_f32 v26, v28, v29\n"
    "v_accvgpr_write_b32 a193, v19\n"
    "v_mfma_f32_16x16x32_bf16 v[56:59], v[120:123], a[32:35], 0\n"
    "s_add_u32 s47, s52, s61\n"
    "v_cvt_pk_bf16_f32 v27, v30, v31\n"
    "v_cmp_eq_u32_e32 vcc, 2, v17\n"
    "s_nop 1\n"
    "v_cndmask_b32_e32 v19, 0, v18, vcc\n"
    "v_mfma_f32_16x16x32_bf16 v[60:63], v[124:127], a[32:35], 0\n"
    "buffer_load_dwordx4 v[216:219], v253, s[8:11], s47 offen offset:0 sc1\n"
    "v_mfma_f32_16x16x32_bf16 v[72:75], v[120:123], a[64:67], 0\n"
    "buffer_store_dwordx4 v[24:27], v8, s[8:11], s35 offen offset:0 sc1\n"
    "v_cvt_pk_bf16_f32 v48, v48, v49\n"
    "v_accvgpr_write_b32 a194, v19\n"
    "v_mfma_f32_16x16x32_bf16 v[76:79], v[124:127], a[64:67], 0\n"
    "s_waitcnt vmcnt(11)\n"
    "v_cvt_pk_bf16_f32 v49, v50, v51\n"
    "v_cmp_eq_u32_e32 vcc, 3, v17\n"
    "s_nop 1\n"
    "v_cndmask_b32_e32 v19, 0, v18, vcc\n"
    "v_mfma_f32_16x16x32_bf16 v[32:35], v[136:139], a[4:7], v[32:35]\n"
    "buffer_load_dwordx4 v[220:223], v253, s[8:11], s47 offen offset:1024 sc1\n"
    "v_mfma_f32_16x16x32_bf16 v[36:39], v[140:143], a[4:7], v[36:39]\n"
    "s_add_u32 s52, s52, 0x4000\n"
    "v_cvt_pk_bf16_f32 v50, v52, v53\n"
    "v_accvgpr_write_b32 a195, v19\n"
    "v_mfma_f32_16x16x32_bf16 v[56:59], v[136:139], a[36:39], v[56:59]\n"
    "s_add_u32 s28, s38, 2\n"
    "v_cvt_pk_bf16_f32 v51, v54, v55\n"
    "v_mov_b32_e32 v82, 0\n"
    "v_mfma_f32_16x16x32_bf16 v[60:63], v[140:143], a[36:39], v[60:63]\n"
    "s_waitcnt vmcnt(10)\n"
    "buffer_load_dwordx4 v[232:235], v253, s[8:11], s47 offen offset:2048 sc1\n"
    "buffer_store_dwordx4 v[48:51], v8, s[8:11], s54 offen offset:0 sc1\n"
    "v_cmp_ne_u32_e32 vcc, s39, v0\n"
    "v_mfma_f32_16x16x32_bf16 v[72:75], v[136:139], a[68:71], v[72:75]\n"
    "s_add_u32 s61, s61, 0x1000\n"
    "v_cvt_pk_bf16_f32 v64, v64, v65\n"
    "v_mov_b32_e32 v83, 0\n"
    "v_mfma_f32_16x16x32_bf16 v[76:79], v[140:143], a[68:71], v[76:79]\n"
    "s_waitcnt vmcnt(11)\n"
    "v_cvt_pk_bf16_f32 v65, v66, v67\n"
    "v_mov_b32_e32 v84, 0\n"
    "v_mfma_f32_16x16x32_bf16 v[32:35], v[152:155], a[8:11], v[32:35]\n"
    "buffer_load_dwordx4 v[236:239], v253, s[8:11], s47 offen offset:3072 sc1\n"
    "v_mfma_f32_16x16x32_bf16 v[36:39], v[156:159], a[8:11], v[36:39]\n"
    "s_bitset0_b32 s61, 14\n"
    "s_cbranch_vccnz .Lat_h1poll\n"
    ".Lat_h1poll_ret:\n"
    "v_mfma_f32_16x16x32_bf16 v[56:59], v[152:155], a[40:43], v[56:59]\n"
    "s_sub_u32 s88, s28, 10\n"
    "v_cvt_pk_bf16_f32 v66, v68, v69\n"
    "v_cvt_pk_bf16_f32 v67, v70, v71\n"
    "v_mfma_f32_16x16x32_bf16 v[60:63], v[156:159], a[40:43], v[60:63]\n"
    "buffer_store_dwordx4 v[64:67], v8, s[8:11], s60 offen offset:0 sc1\n"
    "v_mov_b32_e32 v85, 0\n"
    "v_mov_b32_e32 v86, 0\n"
    "v_mfma_f32_16x16x32_bf16 v[72:75], v[152:155], a[72:75], v[72:75]\n"
    "s_min_u32 s28, s28, s88\n"
    "s_lshl_b32 s88, s28, 0\n"
    "v_mov_b32_e32 v87, 0\n"
    "v_mfma_f32_16x16x32_bf16 v[76:79], v[156:159], a[72:75], v[76:79]\n"
    "s_waitcnt vmcnt(11)\n"
    "s_or_b32 s36, s88, s88\n"
    "v_mov_b32_e32 v88, 0\n"
    "v_mfma_f32_16x16x32_bf16 v[32:35], v[168:171], a[12:15], v[32:35]\n"
    "s_lshl1_add_u32 s88, s28, 1\n"
    "s_mul_i32 s88, s88, 52\n"
    "v_mov_b32_e32 v89, 0\n"
    "v_mfma_f32_16x16x32_bf16 v[36:39], v[172:175], a[12:15], v[36:39]\n"
    "s_lshr_b32 s88, s88, 8\n"
    "s_mul_i32 s90, s88, 5\n"
    "v_mov_b32_e32 v90, 0\n"
    "v_mfma_f32_16x16x32_bf16 v[56:59], v[168:171], a[44:47], v[56:59]\n"
    "s_lshr_b32 s90, s90, 1\n"
    "ds_write_b128 v9, v[24:27] offset:0\n"
    "v_mfma_f32_16x16x32_bf16 v[60:63], v[172:175], a[44:47], v[60:63]\n"
    "s_sub_u32 s28, s28, s90\n"
    "s_sub_u32 s90, s32, s88\n"
    "v_mov_b32_e32 v91, 0\n"
    "v_mfma_f32_16x16x32_bf16 v[72:75], v[168:171], a[76:79], v[72:75]\n"
    "s_sub_u32 s90, s90, 1\n"
    "s_and_b32 s90, s90, 3\n"
    "v_mov_b32_e32 v92, 0\n"
    "v_mfma_f32_16x16x32_bf16 v[76:79], v[172:175], a[76:79], v[76:79]\n"
    "s_waitcnt vmcnt(9)\n"
    "s_lshl2_add_u32 s88, s33, s88\n"
    "v_mov_b32_e32 v93, 0\n"
    "v_mfma_f32_16x16x32_bf16 v[32:35], v[184:187], a[16:19], v[32:35]\n"
    "s_mul_i32 s88, s88, 3\n"
    "v_mov_b32_e32 v94, 0\n"
    "v_mov_b32_e32 v95, 0\n"
    "v_mfma_f32_16x16x32_bf16 v[36:39], v[188:191], a[16:19], v[36:39]\n"
    "s_add_u32 s88, s88, s90\n"
    "v_mov_b32_e32 v96, 0\n"
    "v_mfma_f32_16x16x32_bf16 v[56:59], v[184:187], a[48:51], v[56:59]\n"
    "s_mul_i32 s88, s88, 24832\n"
    "s_add_u32 s88, s88, 16183296\n"
    "v_mfma_f32_16x16x32_bf16 v[60:63], v[188:191], a[48:51], v[60:63]\n"
    "s_cmp_eq_u32 s90, 3\n"
    "s_cselect_b32 s88, 2.0, s88\n"
    "s_cselect_b32 s89, 2, 3\n"
    "v_mfma_f32_16x16x32_bf16 v[72:75], v[184:187], a[80:83], v[72:75]\n"
    "s_lshl1_add_u32 s83, s28, s51\n"
    "v_mov_b32_e32 v97, 0\n"
    "v_mfma_f32_16x16x32_bf16 v[76:79], v[188:191], a[80:83], v[76:79]\n"
    "s_waitcnt vmcnt(7)\n"
    "ds_write_b128 v9, v[48:51] offset:8192\n"
    "v_mfma_f32_16x16x32_bf16 v[32:35], v[200:203], a[20:23], v[32:35]\n"
    "s_lshl_b32 s83, s83, 12\n"
    "v_mov_b32_e32 v98, 0\n"
    "v_mfma_f32_16x16x32_bf16 v[36:39], v[204:207], a[20:23], v[36:39]\n"
    "s_add_u32 s83, s83, s88\n"
    "v_mov_b32_e32 v99, 0\n"
    "v_mfma_f32_16x16x32_bf16 v[56:59], v[200:203], a[52:55], v[56:59]\n"
    "s_lshl2_add_u32 s28, s28, s88\n"
    "v_mov_b32_e32 v100, 0\n"
    "v_mfma_f32_16x16x32_bf16 v[60:63], v[204:207], a[52:55], v[60:63]\n"
    "s_add_u32 s29, s38, 4\n"
    "v_mov_b32_e32 v101, 0\n"
    "v_mfma_f32_16x16x32_bf16 v[72:75], v[200:203], a[84:87], v[72:75]\n"
    "s_sub_u32 s88, s29, 10\n"
    "v_mov_b32_e32 v102, 0\n"
    "v_mfma_f32_16x16x32_bf16 v[76:79], v[204:207], a[84:87], v[76:79]\n"
    "s_waitcnt vmcnt(4)\n"
    "s_min_u32 s29, s29, s88\n"
    "v_mfma_f32_16x16x32_bf16 v[32:35], v[216:219], a[24:27], v[32:35]\n"
    "s_lshl_b32 s88, s29, 4\n"
    "v_mov_b32_e32 v103, 0\n"
    "v_mfma_f32_16x16x32_bf16 v[36:39], v[220:223], a[24:27], v[36:39]\n"
    "s_or_b32 s36, s88, s36\n"
    "v_mov_b32_e32 v104, 0\n"
    "v_mfma_f32_16x16x32_bf16 v[56:59], v[216:219], a[56:59], v[56:59]\n"
    "s_lshl1_add_u32 s88, s29, 1\n"
    "ds_write_b128 v9, v[64:67] offset:16384\n"
    "v_mfma_f32_16x16x32_bf16 v[60:63], v[220:223], a[56:59], v[60:63]\n"
    "s_mul_i32 s88, s88, 52\n"
    "v_mov_b32_e32 v105, 0\n"
    "v_mfma_f32_16x16x32_bf16 v[72:75], v[216:219], a[88:91], v[72:75]\n"
    "s_lshr_b32 s88, s88, 8\n"
    "v_mov_b32_e32 v106, 0\n"
    "v_mfma_f32_16x16x32_bf16 v[76:79], v[220:223], a[88:91], v[76:79]\n"
    "s_waitcnt vmcnt(1)\n"
    "s_mul_i32 s90, s88, 5\n"
    "v_mfma_f32_16x16x32_bf16 v[32:35], v[232:235], a[28:31], v[32:35]\n"
    "s_lshr_b32 s90, s90, 1\n"
    "v_mov_b32_e32 v107, 0\n"
    "v_mfma_f32_16x16x32_bf16 v[36:39], v[236:239], a[28:31], v[36:39]\n"
    "s_sub_u32 s29, s29, s90\n"
    "v_mov_b32_e32 v108, 0\n"
    "v_mfma_f32_16x16x32_bf16 v[56:59], v[232:235], a[60:63], v[56:59]\n"
    "s_sub_u32 s90, s32, s88\n"
    "v_mov_b32_e32 v109, 0\n"
    "v_mfma_f32_16x16x32_bf16 v[60:63], v[236:239], a[60:63], v[60:63]\n"
    "s_sub_u32 s90, s90, 1\n"
    "v_mov_b32_e32 v110, 0\n"
    "v_mfma_f32_16x16x32_bf16 v[72:75], v[232:235], a[92:95], v[72:75]\n"
    "s_and_b32 s90, s90, 3\n"
    "v_mov_b32_e32 v111, 0\n"
    "v_mfma_f32_16x16x32_bf16 v[76:79], v[236:239], a[92:95], v[76:79]\n"
    "s_lshl2_add_u32 s88, s33, s88\n"
    "s_mul_i32 s88, s88, 3\n"
    "v_cvt_pk_bf16_f32 v32, v32, v33\n"
    "v_cvt_pk_bf16_f32 v33, v34, v35\n"
    "v_cvt_pk_bf16_f32 v34, v36, v37\n"
    "v_cvt_pk_bf16_f32 v35, v38, v39\n"
    "v_or_b32_e32 v35, v10, v35\n"
    "ds_write_b128 v9, v[32:35] offset:1024\n"
    "buffer_store_dwordx4 v[32:35], v8, s[8:11], s35 offen offset:1024 sc1\n"
    "v_cvt_pk_bf16_f32 v56, v56, v57\n"
    "v_cvt_pk_bf16_f32 v57, v58, v59\n"
    "v_cvt_pk_bf16_f32 v58, v60, v61\n"
    "v_cvt_pk_bf16_f32 v59, v62, v63\n"
    "v_or_b32_e32 v59, v10, v59\n"
    "ds_write_b128 v9, v[56:59] offset:9216\n"
    "buffer_store_dwordx4 v[56:59], v8, s[8:11], s54 offen offset:1024 sc1\n"
    "v_cvt_pk_bf16_f32 v72, v72, v73\n"
    "v_cvt_pk_bf16_f32 v73, v74, v75\n"
    "v_cvt_pk_bf16_f32 v74, v76, v77\n"
    "v_cvt_pk_bf16_f32 v75, v78, v79\n"
    "v_or_b32_e32 v75, v10, v75\n"
    "ds_write_b128 v9, v[72:75] offset:17408\n"
    "buffer_store_dwordx4 v[72:75], v8, s[8:11], s60 offen offset:1024 sc1\n"
    "v_mov_b32_e32 v112, 0\n"
    "v_mov_b32_e32 v113, 0\n"
    "v_mov_b32_e32 v114, 0\n"
    "s_mov_b32 m0, s63\n"
    "v_mov_b32_e32 v115, 0\n"
    "v_mov_b32_e32 v116, 0\n"
    "v_mov_b32_e32 v117, 0\n"
    "buffer_load_dwordx4 v254, s[8:11], s57 offen offset:0 sc1 lds\n"
    "v_mov_b32_e32 v118, 0\n"
    "v_mov_b32_e32 v119, 0\n"
    "v_mov_b32_e32 v120, 0\n"
    "s_sub_u32 m0, s63, 1024\n"
    "v_mov_b32_e32 v121, 0\n"
    "v_mov_b32_e32 v122, 0\n"
    "v_mov_b32_e32 v123, 0\n"
    "buffer_load_dwordx4 v254, s[8:11], s57 offen offset:2048 sc1 lds\n"
    "v_mov_b32_e32 v124, 0\n"
    "v_mov_b32_e32 v125, 0\n"
    "v_mov_b32_e32 v126, 0\n"
    "s_add_u32 m0, s63, 3968\n"
    "v_mov_b32_e32 v127, 0\n"
    "v_mov_b32_e32 v128, 0\n"
    "v_mov_b32_e32 v129, 0\n"
    "buffer_load_dwordx4 v254, s[8:11], s57 offen offset:128 sc1 lds\n"
    "v_mov_b32_e32 v130, 0\n"
    "v_mov_b32_e32 v131, 0\n"
    "v_mov_b32_e32 v132, 0\n"
    "s_add_u32 m0, s63, 2944\n"
    "v_mov_b32_e32 v133, 0\n"
    "v_mov_b32_e32 v134, 0\n"
    "v_mov_b32_e32 v135, 0\n"
    "buffer_load_dwordx4 v254, s[8:11], s57 offen offset:2176 sc1 lds\n"
    "v_mov_b32_e32 v136, 0\n"
    "v_mov_b32_e32 v137, 0\n"
    "v_mov_b32_e32 v138, 0\n"
    "s_add_u32 m0, s62, 0\n"
    "v_mov_b32_e32 v139, 0\n"
    "v_mov_b32_e32 v140, 0\n"
    "v_mov_b32_e32 v141, 0\n"
    "buffer_load_dwordx4 v12, s[8:11], s40 offen offset:0 sc1 lds\n"
    "v_mov_b32_e32 v142, 0\n"
    "v_mov_b32_e32 v143, 0\n"
    "v_mov_b32_e32 v144, 0\n"
    "buffer_load_dwordx4 v13, s[8:11], s40 offen offset:1024 sc1 lds\n"
    "v_mov_b32_e32 v145, 0\n"
    "v_mov_b32_e32 v146, 0\n"
    "v_mov_b32_e32 v147, 0\n"
    "buffer_load_dwordx4 v14, s[8:11], s40 offen offset:2048 sc1 lds\n"
    "v_mov_b32_e32 v148, 0\n"
    "v_mov_b32_e32 v149, 0\n"
    "v_mov_b32_e32 v150, 0\n"
    "buffer_load_dwordx4 v15, s[8:11], s40 offen offset:3072 sc1 lds\n"
    "v_mov_b32_e32 v151, 0\n"
    "s_xor_b32 s45, s34, 0xc000\n"
    "v_add_u32_e32 v204, s45, v240\n"
    "s_add_u32 s40, s40, 0x8000\n"
    "v_add_u32_e32 v205, s45, v241\n"
    "v_add_u32_e32 v206, s45, v242\n"
    "v_add_u32_e32 v207, s45, v243\n"
    "s_add_u32 m0, s62, 4096\n"
    "v_add_u32_e32 v208, s45, v244\n"
    "v_add_u32_e32 v209, s45, v245\n"
    "v_add_u32_e32 v210, s45, v246\n"
    "buffer_load_dwordx4 v12, s[8:11], s41 offen offset:0 sc1 lds\n"
    "buffer_load_dwordx4 v13, s[8:11], s41 offen offset:1024 sc1 lds\n"
    "buffer_load_dwordx4 v14, s[8:11], s41 offen offset:2048 sc1 lds\n"
    "buffer_load_dwordx4 v15, s[8:11], s41 offen offset:3072 sc1 lds\n"
    "s_add_u32 s41, s41, 0x8000\n"
    "buffer_load_dword v0, v253, s[8:11], s59 offen offset:0 sc1\n"
    "buffer_load_dword v1, v2, s[8:11], s59 offen offset:0 sc1\n"
    "v_add_u32_e32 v211, s45, v247\n"
    "v_add_u32_e32 v200, s34, v240\n"
    "v_add_u32_e32 v201, s34, v241\n"
    "v_add_u32_e32 v202, s34, v242\n"
    "v_add_u32_e32 v203, s34, v243\n"
    "v_mov_b32_e32 v152, v16\n"
    "v_and_b32_e32 v153, 0xf0, v253\n"
    "v_lshrrev_b32_e32 v154, 9, v253\n"
    "v_lshl_add_u32 v153, v154, 8, v153\n"
    "v_bfe_u32 v154, v253, 8, 1\n"
    "v_lshl_add_u32 v153, v154, 3, v153\n"
    "v_mov_b32_e32 v156, 0\n"
    "v_mov_b32_e32 v157, 0\n"
    "v_mov_b32_e32 v158, 0\n"
    "v_mov_b32_e32 v159, 0\n"
    "v_mov_b32_e32 v56, 0\n"
    "v_mov_b32_e32 v57, 0\n"
    "v_mov_b32_e32 v58, 0\n"
    "v_mov_b32_e32 v59, 0\n"
    "v_mov_b32_e32 v60, 0\n"
    "v_mov_b32_e32 v61, 0\n"
    "v_mov_b32_e32 v62, 0\n"
    "v_mov_b32_e32 v63, 0\n"
    "v_mov_b32_e32 v64, 0\n"
    "v_mov_b32_e32 v65, 0\n"
    "v_mov_b32_e32 v66, 0\n"
    "v_mov_b32_e32 v67, 0\n"
    "v_mov_b32_e32 v68, 0\n"
    "v_mov_b32_e32 v69, 0\n"
    "v_mov_b32_e32 v70, 0\n"
    "v_mov_b32_e32 v71, 0\n"
    "v_mov_b32_e32 v72, 0\n"
    "v_mov_b32_e32 v73, 0\n"
    "v_mov_b32_e32 v74, 0\n"
    "v_mov_b32_e32 v75, 0\n"
    "v_mov_b32_e32 v76, 0\n"
    "v_mov_b32_e32 v77, 0\n"
    "v_mov_b32_e32 v78, 0\n"
    "v_mov_b32_e32 v79, 0\n"
    "s_add_u32 s88, s88, s90\n"
    "s_mul_i32 s88, s88, 24832\n"
    "s_add_u32 s88, s88, 16183296\n"
    "s_lshl1_add_u32 s84, s29, s51\n"
    "s_lshl_b32 s84, s84, 12\n"
    "s_add_u32 s84, s84, s88\n"
    "s_lshl2_add_u32 s29, s29, s88\n"
    "s_add_u32 s30, s38, 6\n"
    "s_sub_u32 s88, s30, 10\n"
    "s_min_u32 s30, s30, s88\n"
    "s_lshl_b32 s88, s30, 8\n"
    "s_or_b32 s36, s88, s36\n"
    "s_lshl1_add_u32 s88, s30, 1\n"
    "s_mul_i32 s88, s88, 52\n"
    "s_lshr_b32 s88, s88, 8\n"
    "s_mul_i32 s90, s88, 5\n"
    "s_lshr_b32 s90, s90, 1\n"
    "s_sub_u32 s30, s30, s90\n"
    "s_sub_u32 s90, s32, s88\n"
    "s_sub_u32 s90, s90, 1\n"
    "s_and_b32 s90, s90, 3\n"
    "s_lshl2_add_u32 s88, s33, s88\n"
    "s_mul_i32 s88, s88, 3\n"
    "s_add_u32 s88, s88, s90\n"
    "s_mul_i32 s88, s88, 24832\n"
    "s_add_u32 s88, s88, 16183296\n"
    "s_lshl1_add_u32 s85, s30, s51\n"
    "s_lshl_b32 s85, s85, 12\n"
    "s_add_u32 s85, s85, s88\n"
    "s_lshl2_add_u32 s30, s30, s88\n"
    "s_add_u32 s31, s38, 8\n"
    "s_sub_u32 s88, s31, 10\n"
    "s_min_u32 s31, s31, s88\n"
    "s_lshl_b32 s88, s31, 12\n"
    "s_or_b32 s36, s88, s36\n"
    "s_lshl1_add_u32 s88, s31, 1\n"
    "s_mul_i32 s88, s88, 52\n"
    "s_lshr_b32 s88, s88, 8\n"
    "s_mul_i32 s90, s88, 5\n"
    "s_lshr_b32 s90, s90, 1\n"
    "s_sub_u32 s31, s31, s90\n"
    "s_sub_u32 s90, s32, s88\n"
    "s_sub_u32 s90, s90, 1\n"
    "s_and_b32 s90, s90, 3\n"
    "s_lshl2_add_u32 s88, s33, s88\n"
    "s_mul_i32 s88, s88, 3\n"
    "s_add_u32 s88, s88, s90\n"
    "s_mul_i32 s88, s88, 24832\n"
    "s_add_u32 s88, s88, 16183296\n"
    "s_lshl1_add_u32 s86, s31, s51\n"
    "s_lshl_b32 s86, s86, 12\n"
    "s_add_u32 s86, s86, s88\n"
    "s_lshl2_add_u32 s31, s31, s88\n"
    "s_lshl4_add_u32 s87, %1, %2\n"
    "s_lshl2_add_u32 s87, s87, 5636096\n"
    "s_or_b32 s88, %1, %2\n"
    "s_cselect_b32 s88, 2.0, 5695744\n"
    "s_lshl_b32 s47, s33, 9\n"
    "s_add_u32 s47, s47, 5656640\n"
    "v_lshlrev_b32_e32 v9, 3, v253\n"
    "s_cmp_eq_u32 s50, 0\n"
    "s_cselect_b32 s46, s35, s54\n"
    "s_cselect_b32 s45, s60, 2.0\n"
    "s_cmp_eq_u32 s51, 0\n"
    "s_cselect_b32 s46, s46, s45\n"
    "s_add_u32 s46, s46, 10940416\n"
    "s_waitcnt vmcnt(14)\n"
    "s_waitcnt lgkmcnt(0)\n"
    "s_barrier\n"
    "buffer_store_dword v40, v41, s[8:11], s58 offen offset:64 sc1\n"
    "buffer_load_dword v228, v9, s[8:11], s46 offen offset:0 sc1\n"
    "ds_read_b128 a[0:3], v6 offset:0\n"
    "ds_read_b128 a[4:7], v6 offset:1024\n"
    "ds_read_b128 a[8:11], v6 offset:2048\n"
    "ds_read_b128 a[12:15], v6 offset:3072\n"
    "ds_read_b128 a[16:19], v6 offset:4096\n"
    "ds_read_b128 a[20:23], v6 offset:5120\n"
    "ds_read_b128 a[24:27], v6 offset:6144\n"
    "ds_read_b128 a[28:31], v6 offset:7168\n"
    "s_waitcnt vmcnt(4)\n"
    "s_barrier\n"
    "ds_read_b128 a[160:163], v204 offset:0\n"
    "ds_read_b128 a[164:167], v205 offset:0\n"
    "ds_read_b128 a[168:171], v204 offset:4096\n"
    "ds_read_b128 a[172:175], v205 offset:4096\n"
    "ds_read_b128 a[176:179], v206 offset:0\n"
    "ds_read_b128 a[180:183], v207 offset:0\n"
    "ds_read_b128 a[184:187], v206 offset:4096\n"
    "ds_read_b128 a[188:191], v207 offset:4096\n"
    "s_add_u32 m0, s62, 16384\n"
    "s_nop 0\n"
    "buffer_load_dwordx4 v12, s[8:11], s40 offen offset:0 sc1 lds\n"
    "buffer_load_dwordx4 v13, s[8:11], s40 offen offset:1024 sc1 lds\n"
    "buffer_load_dwordx4 v14, s[8:11], s40 offen offset:2048 sc1 lds\n"
    "buffer_load_dwordx4 v15, s[8:11], s40 offen offset:3072 sc1 lds\n"
    "s_add_u32 s40, s40, 0x8000\n"
    "s_add_u32 m0, s62, 20480\n"
    "s_nop 0\n"
    "buffer_load_dwordx4 v12, s[8:11], s41 offen offset:0 sc1 lds\n"
    "buffer_load_dwordx4 v13, s[8:11], s41 offen offset:1024 sc1 lds\n"
    "buffer_load_dwordx4 v14, s[8:11], s41 offen offset:2048 sc1 lds\n"
    "buffer_load_dwordx4 v15, s[8:11], s41 offen offset:3072 sc1 lds\n"
    "s_add_u32 s41, s41, 0x8000\n"
    "s_waitcnt lgkmcnt(4)\n"
    "v_mfma_f32_16x16x32_bf16 v[220:223], a[160:163], a[0:3], 0\n"
    "ds_read_b128 a[32:35], v7 offset:16384\n"
    "ds_read_b128 a[36:39], v7 offset:17408\n"
    "v_mfma_f32_16x16x32_bf16 v[220:223], a[164:167], a[4:7], v[220:223]\n"
    "ds_read_b128 a[40:43], v7 offset:18432\n"
    "ds_read_b128 a[44:47], v7 offset:19456\n"
    "v_mfma_f32_16x16x32_bf16 v[220:223], a[168:171], a[8:11], v[220:223]\n"
    "ds_read_b128 a[48:51], v7 offset:20480\n"
    "ds_read_b128 a[52:55], v7 offset:21504\n"
    "v_mfma_f32_16x16x32_bf16 v[220:223], a[172:175], a[12:15], v[220:223]\n"
    "ds_read_b128 a[56:59], v7 offset:22528\n"
    "ds_read_b128 a[60:63], v7 offset:23552\n"
    "s_waitcnt lgkmcnt(8)\n"
    "v_mfma_f32_16x16x32_bf16 v[220:223], a[176:179], a[16:19], v[220:223]\n"
    "ds_read_b64_tr_b16 a[232:233], v208 offset:0\n"
    "ds_read_b64_tr_b16 a[234:235], v209 offset:0\n"
    "v_mfma_f32_16x16x32_bf16 v[220:223], a[180:183], a[20:23], v[220:223]\n"
    "ds_read_b64_tr_b16 a[236:237], v210 offset:0\n"
    "ds_read_b64_tr_b16 a[238:239], v211 offset:0\n"
    "v_mfma_f32_16x16x32_bf16 v[220:223], a[184:187], a[24:27], v[220:223]\n"
    "ds_read_b64_tr_b16 a[240:241], v208 offset:4096\n"
    "ds_read_b64_tr_b16 a[242:243], v209 offset:4096\n"
    "v_mfma_f32_16x16x32_bf16 v[220:223], a[188:191], a[28:31], v[220:223]\n"
    "buffer_load_dword v11, v5, s[8:11], s47 offen offset:0 sc1\n"
    "ds_read_b64_tr_b16 a[244:245], v210 offset:4096\n"
    "s_waitcnt lgkmcnt(11)\n"
    "v_mfma_f32_16x16x32_bf16 v[224:227], a[160:163], a[32:35], 0\n"
    "ds_read_b64_tr_b16 a[246:247], v211 offset:4096\n"
    "ds_read_b128 a[200:203], v203 offset:6144\n"
    "v_mfma_f32_16x16x32_bf16 v[224:227], a[164:167], a[36:39], v[224:227]\n"
    "ds_read_b128 a[204:207], v200 offset:2048\n"
    "ds_read_b128 a[208:211], v201 offset:2048\n"
    "v_mfma_f32_16x16x32_bf16 v[224:227], a[168:171], a[40:43], v[224:227]\n"
    "ds_read_b128 a[212:215], v200 offset:6144\n"
    "ds_read_b128 a[216:219], v201 offset:6144\n"
    "v_mfma_f32_16x16x32_bf16 v[224:227], a[172:175], a[44:47], v[224:227]\n"
    "ds_read_b128 a[220:223], v202 offset:2048\n"
    "ds_read_b128 a[224:227], v203 offset:2048\n"
    "s_waitcnt lgkmcnt(15)\n"
    "v_mfma_f32_16x16x32_bf16 v[224:227], a[176:179], a[48:51], v[224:227]\n"
    "ds_read_b128 a[228:231], v202 offset:6144\n"
    "v_mfma_f32_16x16x32_bf16 v[224:227], a[180:183], a[52:55], v[224:227]\n"
    "v_mfma_f32_16x16x32_bf16 v[224:227], a[184:187], a[56:59], v[224:227]\n"
    "v_mfma_f32_16x16x32_bf16 v[224:227], a[188:191], a[60:63], v[224:227]\n"
    "s_andn2_b32 s61, s32, s50\n"
    "s_bfe_i32 s61, s61, 0x10000\n"
    "ds_read_b128 a[160:163], v203 offset:4096\n"
    "ds_read_b128 a[164:167], v200 offset:0\n"
    "ds_read_b128 a[168:171], v201 offset:0\n"
    "ds_read_b128 a[172:175], v200 offset:4096\n"
    "ds_read_b128 a[176:179], v201 offset:4096\n"
    "ds_read_b128 a[180:183], v202 offset:0\n"
    "ds_read_b128 a[184:187], v203 offset:0\n"
    "ds_read_b128 a[188:191], v202 offset:4096\n"
    "s_and_b32 s57, s63, 0xffffdfff\n"
    "s_sub_u32 s57, s57, 16384\n"
    "v_add_u32_e32 v216, s57, v253\n"
    "s_lshl_b32 s45, s51, 12\n"
    "s_add_u32 s57, s57, s45\n"
    "s_mul_i32 s48, s33, 81920\n"
    "s_add_u32 s48, s48, s45\n"
    "s_add_u32 s48, s48, 10940416\n"
    "v_exp_f32_e32 v220, v220\n"
    "v_exp_f32_e32 v221, v221\n"
    "v_exp_f32_e32 v222, v222\n"
    "v_exp_f32_e32 v223, v223\n"
    "v_exp_f32_e32 v224, v224\n"
    "v_exp_f32_e32 v225, v225\n"
    "v_exp_f32_e32 v226, v226\n"
    "v_exp_f32_e32 v227, v227\n"
    "v_cvt_pk_bf16_f32 v220, v220, v221\n"
    "v_cvt_pk_bf16_f32 v221, v222, v223\n"
    "v_cvt_pk_bf16_f32 v222, v224, v225\n"
    "v_cvt_pk_bf16_f32 v223, v226, v227\n"
    "v_and_b32_e32 v222, s61, v222\n"
    "v_and_b32_e32 v223, s61, v223\n"
    "s_waitcnt vmcnt(11)\n"
    "s_waitcnt lgkmcnt(0)\n"
    "s_barrier\n"
    "v_xor_b32_e32 v1, s39, v1\n"
    "v_xor_b32_e32 v0, s39, v0\n"
    "v_or_b32_e32 v0, v0, v1\n"
    "v_cmp_ne_u32_e32 vcc, 0, v0\n"
    "s_nop 3\n"
    "s_cbranch_vccnz .Lat_hpoll\n"
    ".Lat_hpoll_ret:\n"
    "s_waitcnt vmcnt(0)\n"
    "v_cmp_ne_u32_e32 vcc, s39, v11\n"
    "s_nop 3\n"
    "s_cbranch_vccnz .Lat_qpoll\n"
    ".Lat_qpoll_ret:\n"
    "s_cmp_eq_u32 s50, 0\n"
    "s_cbranch_scc1 .Lat_qx\n"
    "s_bfe_u32 s45, s36, 0x40000\n"
    "s_add_u32 m0, s57, 0\n"
    "s_lshl_b32 s45, s45, 13\n"
    "s_add_u32 s45, s45, s48\n"
    "buffer_load_dwordx4 v253, s[8:11], s45 offen offset:0 sc1 lds\n"
    "buffer_load_dwordx4 v253, s[8:11], s45 offen offset:1024 sc1 lds\n"
    "buffer_load_dwordx4 v253, s[8:11], s45 offen offset:2048 sc1 lds\n"
    "buffer_load_dwordx4 v253, s[8:11], s45 offen offset:3072 sc1 lds\n"
    ".Lat_qx:\n"
    "s_bfe_u32 s45, s36, 0x40004\n"
    "s_add_u32 m0, s57, 8192\n"
    "s_lshl_b32 s45, s45, 13\n"
    "s_add_u32 s45, s45, s48\n"
    "buffer_load_dwordx4 v253, s[8:11], s45 offen offset:0 sc1 lds\n"
    "buffer_load_dwordx4 v253, s[8:11], s45 offen offset:1024 sc1 lds\n"
    "buffer_load_dwordx4 v253, s[8:11], s45 offen offset:2048 sc1 lds\n"
    "buffer_load_dwordx4 v253, s[8:11], s45 offen offset:3072 sc1 lds\n"
    "v_mfma_f32_16x16x16_bf16 v[52:55], a[246:247], v[222:223], 0\n"
    "v_mfma_f32_16x16x16_bf16 v[48:51], a[244:245], v[222:223], 0\n"
    "s_bfe_u32 s45, s36, 0x40008\n"
    "s_add_u32 m0, s57, 16384\n"
    "s_lshl_b32 s45, s45, 13\n"
    "s_add_u32 s45, s45, s48\n"
    "v_mfma_f32_16x16x16_bf16 v[44:47], a[242:243], v[222:223], 0\n"
    "buffer_load_dwordx4 v253, s[8:11], s45 offen offset:0 sc1 lds\n"
    "v_mfma_f32_16x16x16_bf16 v[40:43], a[240:241], v[222:223], 0\n"
    "v_mfma_f32_16x16x16_bf16 v[36:39], a[238:239], v[222:223], 0\n"
    "buffer_load_dwordx4 v253, s[8:11], s45 offen offset:1024 sc1 lds\n"
    "v_mfma_f32_16x16x16_bf16 v[32:35], a[236:237], v[222:223], 0\n"
    "v_mfma_f32_16x16x16_bf16 a[252:255], a[234:235], v[222:223], 0\n"
    "buffer_load_dwordx4 v253, s[8:11], s45 offen offset:2048 sc1 lds\n"
    "v_mfma_f32_16x16x16_bf16 a[248:251], a[232:233], v[222:223], 0\n"
    "v_mfma_f32_16x16x16_bf16 v[28:31], a[246:247], v[220:221], 0\n"
    "buffer_load_dwordx4 v253, s[8:11], s45 offen offset:3072 sc1 lds\n"
    "v_mfma_f32_16x16x16_bf16 v[24:27], a[244:245], v[220:221], 0\n"
    "s_bfe_u32 s45, s36, 0x4000c\n"
    "s_add_u32 m0, s57, 24576\n"
    "s_lshl_b32 s45, s45, 13\n"
    "s_add_u32 s45, s45, s48\n"
    "v_mfma_f32_16x16x16_bf16 v[20:23], a[242:243], v[220:221], 0\n"
    "buffer_load_dwordx4 v253, s[8:11], s45 offen offset:0 sc1 lds\n"
    "v_mfma_f32_16x16x16_bf16 v[16:19], a[240:241], v[220:221], 0\n"
    "v_mfma_f32_16x16x16_bf16 v[12:15], a[238:239], v[220:221], 0\n"
    "buffer_load_dwordx4 v253, s[8:11], s45 offen offset:1024 sc1 lds\n"
    "v_mfma_f32_16x16x16_bf16 v[8:11], a[236:237], v[220:221], 0\n"
    "v_mfma_f32_16x16x16_bf16 v[4:7], a[234:235], v[220:221], 0\n"
    "buffer_load_dwordx4 v253, s[8:11], s45 offen offset:2048 sc1 lds\n"
    "v_mfma_f32_16x16x16_bf16 v[0:3], a[232:233], v[220:221], 0\n"
    "v_mfma_f32_16x16x32_bf16 v[160:163], a[160:163], a[28:31], 0\n"
    "buffer_load_dwordx4 v253, s[8:11], s45 offen offset:3072 sc1 lds\n"
    "v_mfma_f32_16x16x32_bf16 v[160:163], a[164:167], a[0:3], v[160:163]\n"
    "s_mov_b32 m0, s49\n"
    "v_mfma_f32_16x16x32_bf16 v[160:163], a[168:171], a[4:7], v[160:163]\n"
    "v_mfma_f32_16x16x32_bf16 v[160:163], a[172:175], a[8:11], v[160:163]\n"
    "v_mfma_f32_16x16x32_bf16 v[160:163], a[176:179], a[12:15], v[160:163]\n"
    "v_mfma_f32_16x16x32_bf16 v[160:163], a[180:183], a[16:19], v[160:163]\n"
    "v_mfma_f32_16x16x32_bf16 v[160:163], a[184:187], a[20:23], v[160:163]\n"
    "v_mfma_f32_16x16x32_bf16 v[160:163], a[188:191], a[24:27], v[160:163]\n"
    "v_mfma_f32_16x16x32_bf16 v[164:167], a[200:203], a[28:31], 0\n"
    "v_mfma_f32_16x16x32_bf16 v[164:167], a[204:207], a[0:3], v[164:167]\n"
    "v_mfma_f32_16x16x32_bf16 v[164:167], a[208:211], a[4:7], v[164:167]\n"
    "v_exp_f32_e32 v160, v160\n"
    "v_mfma_f32_16x16x32_bf16 v[164:167], a[212:215], a[8:11], v[164:167]\n"
    "v_exp_f32_e32 v161, v161\n"
    "v_mfma_f32_16x16x32_bf16 v[164:167], a[216:219], a[12:15], v[164:167]\n"
    "v_exp_f32_e32 v162, v162\n"
    "v_mfma_f32_16x16x32_bf16 v[164:167], a[220:223], a[16:19], v[164:167]\n"
    "v_exp_f32_e32 v163, v163\n"
    "v_mfma_f32_16x16x32_bf16 v[164:167], a[224:227], a[20:23], v[164:167]\n"
    "v_cvt_pk_bf16_f32 v160, v160, v161\n"
    "v_mfma_f32_16x16x32_bf16 v[164:167], a[228:231], a[24:27], v[164:167]\n"
    "s_waitcnt vmcnt(8)\n"
    "s_barrier\n"
    "s_cmp_eq_u32 s50, 0\n"
    "s_cbranch_scc1 .Lat_qy\n"
    "ds_read_b128 a[60:63], v216 offset:7168\n"
    "ds_read_b128 a[32:35], v216 offset:0\n"
    "ds_read_b128 a[36:39], v216 offset:1024\n"
    "ds_read_b128 a[40:43], v216 offset:2048\n"
    "ds_read_b128 a[44:47], v216 offset:3072\n"
    "ds_read_b128 a[48:51], v216 offset:4096\n"
    "ds_read_b128 a[52:55], v216 offset:5120\n"
    "ds_read_b128 a[56:59], v216 offset:6144\n"
    ".Lat_qy:\n"
    "ds_read_b128 a[92:95], v216 offset:15360\n"
    "ds_read_b128 a[64:67], v216 offset:8192\n"
    "s_waitcnt lgkmcnt(8)\n"
    "v_mfma_f32_16x16x32_bf16 v[168:171], a[160:163], a[60:63], 0\n"
    "v_cvt_pk_bf16_f32 v161, v162, v163\n"
    "v_mfma_f32_16x16x32_bf16 v[168:171], a[164:167], a[32:35], v[168:171]\n"
    "s_waitcnt lgkmcnt(6)\n"
    "ds_read_b128 a[68:71], v216 offset:9216\n"
    "v_mfma_f32_16x16x32_bf16 v[168:171], a[168:171], a[36:39], v[168:171]\n"
    "ds_read_b128 a[72:75], v216 offset:10240\n"
    "v_mfma_f32_16x16x32_bf16 v[168:171], a[172:175], a[40:43], v[168:171]\n"
    "s_waitcnt lgkmcnt(6)\n"
    "ds_read_b128 a[76:79], v216 offset:11264\n"
    "ds_write_addtid_b32 v160 offset:0\n"
    "v_mfma_f32_16x16x32_bf16 v[168:171], a[176:179], a[44:47], v[168:171]\n"
    "v_exp_f32_e32 v164, v164\n"
    "v_mfma_f32_16x16x32_bf16 v[168:171], a[180:183], a[48:51], v[168:171]\n"
    "s_waitcnt lgkmcnt(6)\n"
    "ds_read_b128 a[80:83], v216 offset:12288\n"
    "v_mfma_f32_16x16x32_bf16 v[168:171], a[184:187], a[52:55], v[168:171]\n"
    "ds_read_b128 a[84:87], v216 offset:13312\n"
    "v_mfma_f32_16x16x32_bf16 v[168:171], a[188:191], a[56:59], v[168:171]\n"
    "ds_read_b128 a[88:91], v216 offset:14336\n"
    "ds_write_addtid_b32 v161 offset:256\n"
    "v_mfma_f32_16x16x32_bf16 v[172:175], a[200:203], a[60:63], 0\n"
    "v_exp_f32_e32 v165, v165\n"
    "v_mfma_f32_16x16x32_bf16 v[172:175], a[204:207], a[32:35], v[172:175]\n"
    "v_exp_f32_e32 v166, v166\n"
    "v_mfma_f32_16x16x32_bf16 v[172:175], a[208:211], a[36:39], v[172:175]\n"
    "v_exp_f32_e32 v167, v167\n"
    "v_mfma_f32_16x16x32_bf16 v[172:175], a[212:215], a[40:43], v[172:175]\n"
    "v_cvt_pk_bf16_f32 v162, v164, v165\n"
    "v_cvt_pk_bf16_f32 v163, v166, v167\n"
    "v_mfma_f32_16x16x32_bf16 v[172:175], a[216:219], a[44:47], v[172:175]\n"
    "ds_write_addtid_b32 v162 offset:512\n"
    "v_exp_f32_e32 v168, v168\n"
    "v_mfma_f32_16x16x32_bf16 v[172:175], a[220:223], a[48:51], v[172:175]\n"
    "v_exp_f32_e32 v169, v169\n"
    "v_mfma_f32_16x16x32_bf16 v[172:175], a[224:227], a[52:55], v[172:175]\n"
    "v_exp_f32_e32 v170, v170\n"
    "v_mfma_f32_16x16x32_bf16 v[172:175], a[228:231], a[56:59], v[172:175]\n"
    "s_waitcnt lgkmcnt(9)\n"
    "v_exp_f32_e32 v171, v171\n"
    "v_mfma_f32_16x16x32_bf16 v[176:179], a[160:163], a[92:95], 0\n"
    "ds_write_addtid_b32 v163 offset:768\n"
    "v_cvt_pk_bf16_f32 v168, v168, v169\n"
    "v_mfma_f32_16x16x32_bf16 v[176:179], a[164:167], a[64:67], v[176:179]\n"
    "s_waitcnt lgkmcnt(8)\n"
    "ds_write_addtid_b32 v168 offset:4096\n"
    "v_cvt_pk_bf16_f32 v169, v170, v171\n"
    "v_mfma_f32_16x16x32_bf16 v[176:179], a[168:171], a[68:71], v[176:179]\n"
    "v_exp_f32_e32 v172, v172\n"
    "v_mfma_f32_16x16x32_bf16 v[176:179], a[172:175], a[72:75], v[176:179]\n"
    "s_waitcnt lgkmcnt(6)\n"
    "v_exp_f32_e32 v173, v173\n"
    "v_mfma_f32_16x16x32_bf16 v[176:179], a[176:179], a[76:79], v[176:179]\n"
    "v_exp_f32_e32 v174, v174\n"
    "v_mfma_f32_16x16x32_bf16 v[176:179], a[180:183], a[80:83], v[176:179]\n"
    "s_waitcnt vmcnt(0)\n"
    "s_barrier\n"
    "s_waitcnt lgkmcnt(4)\n"
    "v_mfma_f32_16x16x32_bf16 v[176:179], a[184:187], a[84:87], v[176:179]\n"
    "ds_write_addtid_b32 v169 offset:4352\n"
    "v_exp_f32_e32 v175, v175\n"
    "v_mfma_f32_16x16x32_bf16 v[176:179], a[188:191], a[88:91], v[176:179]\n"
    "ds_read_b128 a[124:127], v216 offset:23552\n"
    "v_cvt_pk_bf16_f32 v170, v172, v173\n"
    "v_mfma_f32_16x16x32_bf16 v[180:183], a[200:203], a[92:95], 0\n"
    "ds_read_b128 a[96:99], v216 offset:16384\n"
    "v_cvt_pk_bf16_f32 v171, v174, v175\n"
    "v_mfma_f32_16x16x32_bf16 v[180:183], a[204:207], a[64:67], v[180:183]\n"
    "ds_read_b128 a[100:103], v216 offset:17408\n"
    "v_exp_f32_e32 v176, v176\n"
    "v_mfma_f32_16x16x32_bf16 v[180:183], a[208:211], a[68:71], v[180:183]\n"
    "ds_read_b128 a[104:107], v216 offset:18432\n"
    "ds_write_addtid_b32 v170 offset:4608\n"
    "v_mfma_f32_16x16x32_bf16 v[180:183], a[212:215], a[72:75], v[180:183]\n"
    "ds_read_b128 a[108:111], v216 offset:19456\n"
    "v_exp_f32_e32 v177, v177\n"
    "v_mfma_f32_16x16x32_bf16 v[180:183], a[216:219], a[76:79], v[180:183]\n"
    "ds_read_b128 a[112:115], v216 offset:20480\n"
    "v_exp_f32_e32 v178, v178\n"
    "v_mfma_f32_16x16x32_bf16 v[180:183], a[220:223], a[80:83], v[180:183]\n"
    "ds_read_b128 a[116:119], v216 offset:21504\n"
    "ds_write_addtid_b32 v171 offset:4864\n"
    "v_mfma_f32_16x16x32_bf16 v[180:183], a[224:227], a[84:87], v[180:183]\n"
    "ds_read_b128 a[120:123], v216 offset:22528\n"
    "v_mfma_f32_16x16x32_bf16 v[180:183], a[228:231], a[88:91], v[180:183]\n"
    "s_waitcnt lgkmcnt(8)\n"
    "s_sub_u32 m0, s63, 16384\n"
    "v_exp_f32_e32 v179, v179\n"
    "v_mfma_f32_16x16x32_bf16 v[184:187], a[160:163], a[124:127], 0\n"
    "ds_read_b128 a[156:159], v216 offset:31744\n"
    "buffer_load_dwordx4 v248, s[8:11], s40 offen offset:0 sc1 lds\n"
    "v_cvt_pk_bf16_f32 v176, v176, v177\n"
    "v_mfma_f32_16x16x32_bf16 v[184:187], a[164:167], a[96:99], v[184:187]\n"
    "s_waitcnt lgkmcnt(7)\n"
    "ds_read_b128 a[128:131], v216 offset:24576\n"
    "v_cvt_pk_bf16_f32 v177, v178, v179\n"
    "v_mfma_f32_16x16x32_bf16 v[184:187], a[168:171], a[100:103], v[184:187]\n"
    "ds_read_b128 a[132:135], v216 offset:25600\n"
    "v_mfma_f32_16x16x32_bf16 v[184:187], a[172:175], a[104:107], v[184:187]\n"
    "s_waitcnt lgkmcnt(6)\n"
    "ds_read_b128 a[136:139], v216 offset:26624\n"
    "v_mfma_f32_16x16x32_bf16 v[184:187], a[176:179], a[108:111], v[184:187]\n"
    "ds_read_b128 a[140:143], v216 offset:27648\n"
    "buffer_load_dwordx4 v248, s[8:11], s40 offen offset:1024 sc1 lds\n"
    "v_mfma_f32_16x16x32_bf16 v[184:187], a[180:183], a[112:115], v[184:187]\n"
    "s_waitcnt lgkmcnt(5)\n"
    "ds_read_b128 a[144:147], v216 offset:28672\n"
    "v_mfma_f32_16x16x32_bf16 v[184:187], a[184:187], a[116:119], v[184:187]\n"
    "ds_read_b128 a[148:151], v216 offset:29696\n"
    "v_mfma_f32_16x16x32_bf16 v[184:187], a[188:191], a[120:123], v[184:187]\n"
    "ds_read_b128 a[152:155], v216 offset:30720\n"
    "v_mfma_f32_16x16x32_bf16 v[188:191], a[200:203], a[124:127], 0\n"
    "buffer_load_dwordx4 v248, s[8:11], s40 offen offset:2048 sc1 lds\n"
    "v_exp_f32_e32 v180, v180\n"
    "v_mfma_f32_16x16x32_bf16 v[188:191], a[204:207], a[96:99], v[188:191]\n"
    "ds_write_b32 v249, v176 offset:8192\n"
    "v_exp_f32_e32 v181, v181\n"
    "v_mfma_f32_16x16x32_bf16 v[188:191], a[208:211], a[100:103], v[188:191]\n"
    "ds_write_b32 v249, v177 offset:8448\n"
    "v_exp_f32_e32 v182, v182\n"
    "v_mfma_f32_16x16x32_bf16 v[188:191], a[212:215], a[104:107], v[188:191]\n"
    "v_exp_f32_e32 v183, v183\n"
    "v_mfma_f32_16x16x32_bf16 v[188:191], a[216:219], a[108:111], v[188:191]\n"
    "buffer_load_dwordx4 v248, s[8:11], s40 offen offset:3072 sc1 lds\n"
    "v_cvt_pk_bf16_f32 v178, v180, v181\n"
    "v_cvt_pk_bf16_f32 v179, v182, v183\n"
    "v_mfma_f32_16x16x32_bf16 v[188:191], a[220:223], a[112:115], v[188:191]\n"
    "s_add_u32 s40, s40, 0x8000\n"
    "ds_write_b32 v249, v178 offset:8704\n"
    "v_exp_f32_e32 v184, v184\n"
    "v_mfma_f32_16x16x32_bf16 v[188:191], a[224:227], a[116:119], v[188:191]\n"
    "s_sub_u32 m0, s63, 12288\n"
    "ds_write_b32 v249, v179 offset:8960\n"
    "v_exp_f32_e32 v185, v185\n"
    "v_mfma_f32_16x16x32_bf16 v[188:191], a[228:231], a[120:123], v[188:191]\n"
    "s_waitcnt lgkmcnt(10)\n"
    "v_exp_f32_e32 v186, v186\n"
    "v_mfma_f32_16x16x32_bf16 v[192:195], a[160:163], a[156:159], 0\n"
    "buffer_load_dwordx4 v248, s[8:11], s41 offen offset:0 sc1 lds\n"
    "v_exp_f32_e32 v187, v187\n"
    "v_mfma_f32_16x16x32_bf16 v[192:195], a[164:167], a[128:131], v[192:195]\n"
    "s_waitcnt lgkmcnt(8)\n"
    "ds_read_b128 a[160:163], v203 offset:20480\n"
    "v_cvt_pk_bf16_f32 v184, v184, v185\n"
    "v_mfma_f32_16x16x32_bf16 v[192:195], a[168:171], a[132:135], v[192:195]\n"
    "ds_read_b128 a[164:167], v200 offset:16384\n"
    "v_cvt_pk_bf16_f32 v185, v186, v187\n"
    "v_mfma_f32_16x16x32_bf16 v[192:195], a[172:175], a[136:139], v[192:195]\n"
    "s_waitcnt lgkmcnt(8)\n"
    "ds_read_b128 a[168:171], v201 offset:16384\n"
    "v_exp_f32_e32 v188, v188\n"
    "v_mfma_f32_16x16x32_bf16 v[192:195], a[176:179], a[140:143], v[192:195]\n"
    "ds_read_b128 a[172:175], v200 offset:20480\n"
    "buffer_load_dwordx4 v248, s[8:11], s41 offen offset:1024 sc1 lds\n"
    "v_exp_f32_e32 v189, v189\n"
    "v_mfma_f32_16x16x32_bf16 v[192:195], a[180:183], a[144:147], v[192:195]\n"
    "s_waitcnt lgkmcnt(8)\n"
    "ds_read_b128 a[176:179], v201 offset:20480\n"
    "v_exp_f32_e32 v190, v190\n"
    "v_mfma_f32_16x16x32_bf16 v[192:195], a[184:187], a[148:151], v[192:195]\n"
    "ds_read_b128 a[180:183], v202 offset:16384\n"
    "v_mfma_f32_16x16x32_bf16 v[192:195], a[188:191], a[152:155], v[192:195]\n"
    "ds_read_b128 a[184:187], v203 offset:16384\n"
    "v_mfma_f32_16x16x32_bf16 v[196:199], a[200:203], a[156:159], 0\n"
    "ds_read_b128 a[188:191], v202 offset:20480\n"
    "buffer_load_dwordx4 v248, s[8:11], s41 offen offset:2048 sc1 lds\n"
    "v_mfma_f32_16x16x32_bf16 v[196:199], a[204:207], a[128:131], v[196:199]\n"
    "ds_write_b32 v249, v184 offset:12288\n"
    "v_exp_f32_e32 v191, v191\n"
    "v_mfma_f32_16x16x32_bf16 v[196:199], a[208:211], a[132:135], v[196:199]\n"
    "ds_write_b32 v249, v185 offset:12544\n"
    "v_cvt_pk_bf16_f32 v186, v188, v189\n"
    "v_cvt_pk_bf16_f32 v187, v190, v191\n"
    "v_mfma_f32_16x16x32_bf16 v[196:199], a[212:215], a[136:139], v[196:199]\n"
    "ds_write_b32 v249, v186 offset:12800\n"
    "v_exp_f32_e32 v192, v192\n"
    "v_mfma_f32_16x16x32_bf16 v[196:199], a[216:219], a[140:143], v[196:199]\n"
    "buffer_load_dwordx4 v248, s[8:11], s41 offen offset:3072 sc1 lds\n"
    "v_exp_f32_e32 v193, v193\n"
    "v_mfma_f32_16x16x32_bf16 v[196:199], a[220:223], a[144:147], v[196:199]\n"
    "s_add_u32 s41, s41, 0x8000\n"
    "ds_write_b32 v249, v187 offset:13056\n"
    "v_exp_f32_e32 v194, v194\n"
    "v_mfma_f32_16x16x32_bf16 v[196:199], a[224:227], a[148:151], v[196:199]\n"
    "v_exp_f32_e32 v195, v195\n"
    "v_mfma_f32_16x16x32_bf16 v[196:199], a[228:231], a[152:155], v[196:199]\n"
    "v_cvt_pk_bf16_f32 v192, v192, v193\n"
    "ds_write_b32 v249, v192 offset:16384\n"
    "v_cvt_pk_bf16_f32 v193, v194, v195\n"
    "ds_write_b32 v249, v193 offset:16640\n"
    "s_mov_b32 s44, 2\n"
    "s_cmp_lg_u32 s34, 0\n"
    "s_waitcnt lgkmcnt(4)\n"
    "s_nop 0\n"
    "v_exp_f32_e32 v196, v196\n"
    "v_exp_f32_e32 v197, v197\n"
    "v_exp_f32_e32 v198, v198\n"
    "v_exp_f32_e32 v199, v199\n"
    "v_cvt_pk_bf16_f32 v194, v196, v197\n"
    "v_cvt_pk_bf16_f32 v195, v198, v199\n"
    "ds_write_b32 v249, v194 offset:16896\n"
    "ds_write_b32 v249, v195 offset:17152\n"
    "s_cbranch_scc1 .Lat_p2e\n"
    ".p2align 6\n"
    ".Lat_p:\n"
    "v_mfma_f32_16x16x32_bf16 v[200:203], a[160:163], a[28:31], 0\n"
    "v_mfma_f32_16x16x32_bf16 v[200:203], a[164:167], a[0:3], v[200:203]\n"
    "v_mfma_f32_16x16x32_bf16 v[208:211], a[164:167], a[32:35], 0\n"
    "v_mfma_f32_16x16x32_bf16 v[208:211], a[160:163], a[60:63], v[208:211]\n"
    "v_mfma_f32_16x16x32_bf16 v[216:219], a[160:163], a[92:95], 0\n"
    "v_mfma_f32_16x16x32_bf16 v[216:219], a[164:167], a[64:67], v[216:219]\n"
    "s_waitcnt lgkmcnt(0)\n"
    "v_mfma_f32_16x16x32_bf16 v[224:227], a[164:167], a[96:99], 0\n"
    "s_barrier\n"
    "v_mfma_f32_16x16x32_bf16 v[224:227], a[160:163], a[124:127], v[224:227]\n"
    "s_add_u32 m0, s43, 49152\n"
    "ds_read2st64_b32 v[164:165], v250 offset0:0 offset1:1\n"
    "v_mfma_f32_16x16x32_bf16 v[232:235], a[160:163], a[156:159], 0\n"
    "ds_read_b128 a[160:163], v243 offset:22528\n"
    "v_mfma_f32_16x16x32_bf16 v[232:235], a[164:167], a[128:131], v[232:235]\n"
    "ds_read_b128 a[164:167], v240 offset:18432\n"
    "v_mfma_f32_16x16x32_bf16 v[232:235], a[168:171], a[132:135], v[232:235]\n"
    "buffer_load_dwordx4 v248, s[8:11], s40 offen offset:0 sc1 lds\n"
    "v_mfma_f32_16x16x32_bf16 v[232:235], a[172:175], a[136:139], v[232:235]\n"
    "v_mfma_f32_16x16x32_bf16 v[224:227], a[172:175], a[104:107], v[224:227]\n"
    "v_mfma_f32_16x16x32_bf16 v[224:227], a[168:171], a[100:103], v[224:227]\n"
    "ds_read2st64_b32 v[166:167], v250 offset0:2 offset1:3\n"
    "v_mfma_f32_16x16x32_bf16 v[216:219], a[168:171], a[68:71], v[216:219]\n"
    "v_mfma_f32_16x16x32_bf16 v[216:219], a[172:175], a[72:75], v[216:219]\n"
    "buffer_load_dwordx4 v248, s[8:11], s40 offen offset:1024 sc1 lds\n"
    "v_mfma_f32_16x16x32_bf16 v[208:211], a[172:175], a[40:43], v[208:211]\n"
    "v_mfma_f32_16x16x32_bf16 v[208:211], a[168:171], a[36:39], v[208:211]\n"
    "ds_read2st64_b32 v[172:173], v250 offset0:16 offset1:17\n"
    "v_mfma_f32_16x16x32_bf16 v[200:203], a[168:171], a[4:7], v[200:203]\n"
    "ds_read_b128 a[168:171], v241 offset:18432\n"
    "v_mfma_f32_16x16x32_bf16 v[200:203], a[172:175], a[8:11], v[200:203]\n"
    "ds_read_b128 a[172:175], v240 offset:22528\n"
    "v_mfma_f32_16x16x32_bf16 v[200:203], a[176:179], a[12:15], v[200:203]\n"
    "buffer_load_dwordx4 v248, s[8:11], s40 offen offset:2048 sc1 lds\n"
    "v_mfma_f32_16x16x32_bf16 v[200:203], a[180:183], a[16:19], v[200:203]\n"
    "ds_read2st64_b32 v[174:175], v250 offset0:18 offset1:19\n"
    "v_mfma_f32_16x16x32_bf16 v[208:211], a[180:183], a[48:51], v[208:211]\n"
    "v_mfma_f32_16x16x32_bf16 v[208:211], a[176:179], a[44:47], v[208:211]\n"
    "v_mfma_f32_16x16x32_bf16 v[216:219], a[176:179], a[76:79], v[216:219]\n"
    "ds_read2st64_b32 v[180:181], v250 offset0:32 offset1:33\n"
    "v_mfma_f32_16x16x32_bf16 v[216:219], a[180:183], a[80:83], v[216:219]\n"
    "buffer_load_dwordx4 v248, s[8:11], s40 offen offset:3072 sc1 lds\n"
    "v_mfma_f32_16x16x32_bf16 v[224:227], a[180:183], a[112:115], v[224:227]\n"
    "s_add_u32 m0, s43, 53248\n"
    "v_mfma_f32_16x16x32_bf16 v[224:227], a[176:179], a[108:111], v[224:227]\n"
    "s_add_u32 s40, s40, 0x8000\n"
    "ds_read2st64_b32 v[182:183], v250 offset0:34 offset1:35\n"
    "v_mfma_f32_16x16x32_bf16 v[232:235], a[176:179], a[140:143], v[232:235]\n"
    "ds_read_b128 a[176:179], v241 offset:22528\n"
    "v_mfma_f32_16x16x32_bf16 v[232:235], a[180:183], a[144:147], v[232:235]\n"
    "ds_read_b128 a[180:183], v242 offset:18432\n"
    "v_mfma_f32_16x16x32_bf16 v[232:235], a[184:187], a[148:151], v[232:235]\n"
    "buffer_load_dwordx4 v248, s[8:11], s41 offen offset:0 sc1 lds\n"
    "v_mfma_f32_16x16x32_bf16 v[232:235], a[188:191], a[152:155], v[232:235]\n"
    "v_mfma_f32_16x16x32_bf16 v[224:227], a[188:191], a[120:123], v[224:227]\n"
    "ds_read2st64_b32 v[188:189], v250 offset0:48 offset1:49\n"
    "v_mfma_f32_16x16x32_bf16 v[224:227], a[184:187], a[116:119], v[224:227]\n"
    "v_mfma_f32_16x16x32_bf16 v[216:219], a[184:187], a[84:87], v[216:219]\n"
    "v_exp_f32_e32 v232, v232\n"
    "v_mfma_f32_16x16x32_bf16 v[216:219], a[188:191], a[88:91], v[216:219]\n"
    "buffer_load_dwordx4 v248, s[8:11], s41 offen offset:1024 sc1 lds\n"
    "v_mfma_f32_16x16x32_bf16 v[208:211], a[188:191], a[56:59], v[208:211]\n"
    "ds_read2st64_b32 v[190:191], v250 offset0:50 offset1:51\n"
    "v_mfma_f32_16x16x32_bf16 v[208:211], a[184:187], a[52:55], v[208:211]\n"
    "v_exp_f32_e32 v233, v233\n"
    "v_mfma_f32_16x16x32_bf16 v[200:203], a[184:187], a[20:23], v[200:203]\n"
    "ds_read_b128 a[184:187], v243 offset:18432\n"
    "v_mfma_f32_16x16x32_bf16 v[200:203], a[188:191], a[24:27], v[200:203]\n"
    "s_waitcnt lgkmcnt(8)\n"
    "ds_read_b128 a[188:191], v242 offset:22528\n"
    "v_mfma_f32_16x16x32_bf16 v[204:207], a[160:163], a[28:31], 0\n"
    "buffer_load_dwordx4 v248, s[8:11], s41 offen offset:2048 sc1 lds\n"
    "v_mfma_f32_16x16x32_bf16 v[204:207], a[164:167], a[0:3], v[204:207]\n"
    "ds_read2st64_b32 v[196:197], v250 offset0:64 offset1:65\n"
    "v_mfma_f32_16x16x32_bf16 v[212:215], a[164:167], a[32:35], 0\n"
    "v_exp_f32_e32 v234, v234\n"
    "v_mfma_f32_16x16x32_bf16 v[212:215], a[160:163], a[60:63], v[212:215]\n"
    "ds_read2st64_b32 v[198:199], v250 offset0:66 offset1:67\n"
    "v_mfma_f32_16x16x32_bf16 v[220:223], a[160:163], a[92:95], 0\n"
    "v_exp_f32_e32 v235, v235\n"
    "v_mfma_f32_16x16x32_bf16 v[220:223], a[164:167], a[64:67], v[220:223]\n"
    "buffer_load_dwordx4 v248, s[8:11], s41 offen offset:3072 sc1 lds\n"
    "v_mfma_f32_16x16x32_bf16 v[228:231], a[164:167], a[96:99], 0\n"
    "s_add_u32 s41, s41, 0x8000\n"
    "v_cvt_pk_bf16_f32 v232, v232, v233\n"
    "v_mfma_f32_16x16x32_bf16 v[228:231], a[160:163], a[124:127], v[228:231]\n"
    "v_exp_f32_e32 v224, v224\n"
    "v_mfma_f32_16x16x32_bf16 v[236:239], a[160:163], a[156:159], 0\n"
    "ds_read_b64_tr_b16 a[160:161], v244 offset:0\n"
    "v_mfma_f32_16x16x32_bf16 v[236:239], a[164:167], a[128:131], v[236:239]\n"
    "ds_read_b64_tr_b16 a[162:163], v244 offset:2048\n"
    "v_mfma_f32_16x16x32_bf16 v[236:239], a[168:171], a[132:135], v[236:239]\n"
    "ds_read_b64_tr_b16 a[164:165], v244 offset:8192\n"
    "v_mfma_f32_16x16x32_bf16 v[236:239], a[172:175], a[136:139], v[236:239]\n"
    "ds_read_b64_tr_b16 a[166:167], v244 offset:10240\n"
    "v_mfma_f32_16x16x32_bf16 v[228:231], a[172:175], a[104:107], v[228:231]\n"
    "v_exp_f32_e32 v225, v225\n"
    "v_mfma_f32_16x16x32_bf16 v[228:231], a[168:171], a[100:103], v[228:231]\n"
    "v_cvt_pk_bf16_f32 v233, v234, v235\n"
    "v_mfma_f32_16x16x32_bf16 v[220:223], a[168:171], a[68:71], v[220:223]\n"
    "v_exp_f32_e32 v226, v226\n"
    "v_mfma_f32_16x16x32_bf16 v[220:223], a[172:175], a[72:75], v[220:223]\n"
    "v_exp_f32_e32 v227, v227\n"
    "v_mfma_f32_16x16x32_bf16 v[212:215], a[172:175], a[40:43], v[212:215]\n"
    "v_cvt_pk_bf16_f32 v224, v224, v225\n"
    "v_mfma_f32_16x16x32_bf16 v[212:215], a[168:171], a[36:39], v[212:215]\n"
    "v_exp_f32_e32 v216, v216\n"
    "v_mfma_f32_16x16x32_bf16 v[204:207], a[168:171], a[4:7], v[204:207]\n"
    "ds_read_b64_tr_b16 a[168:169], v245 offset:0\n"
    "v_mfma_f32_16x16x32_bf16 v[204:207], a[172:175], a[8:11], v[204:207]\n"
    "s_waitcnt lgkmcnt(7)\n"
    "ds_read_b64_tr_b16 a[170:171], v245 offset:2048\n"
    "v_mfma_f32_16x16x32_bf16 v[204:207], a[176:179], a[12:15], v[204:207]\n"
    "ds_read_b64_tr_b16 a[172:173], v245 offset:8192\n"
    "v_mfma_f32_16x16x32_bf16 v[204:207], a[180:183], a[16:19], v[204:207]\n"
    "ds_read_b64_tr_b16 a[174:175], v245 offset:10240\n"
    "v_mfma_f32_16x16x32_bf16 v[212:215], a[180:183], a[48:51], v[212:215]\n"
    "v_exp_f32_e32 v217, v217\n"
    "v_mfma_f32_16x16x32_bf16 v[212:215], a[176:179], a[44:47], v[212:215]\n"
    "v_cvt_pk_bf16_f32 v225, v226, v227\n"
    "v_mfma_f32_16x16x32_bf16 v[220:223], a[176:179], a[76:79], v[220:223]\n"
    "v_exp_f32_e32 v218, v218\n"
    "v_mfma_f32_16x16x32_bf16 v[220:223], a[180:183], a[80:83], v[220:223]\n"
    "v_exp_f32_e32 v219, v219\n"
    "v_mfma_f32_16x16x32_bf16 v[228:231], a[180:183], a[112:115], v[228:231]\n"
    "v_cvt_pk_bf16_f32 v216, v216, v217\n"
    "v_mfma_f32_16x16x32_bf16 v[228:231], a[176:179], a[108:111], v[228:231]\n"
    "v_exp_f32_e32 v208, v208\n"
    "v_mfma_f32_16x16x32_bf16 v[236:239], a[176:179], a[140:143], v[236:239]\n"
    "ds_read_b64_tr_b16 a[176:177], v246 offset:0\n"
    "v_mfma_f32_16x16x32_bf16 v[236:239], a[180:183], a[144:147], v[236:239]\n"
    "ds_read_b64_tr_b16 a[178:179], v246 offset:2048\n"
    "v_mfma_f32_16x16x32_bf16 v[236:239], a[184:187], a[148:151], v[236:239]\n"
    "ds_read_b64_tr_b16 a[180:181], v246 offset:8192\n"
    "v_mfma_f32_16x16x32_bf16 v[236:239], a[188:191], a[152:155], v[236:239]\n"
    "ds_read_b64_tr_b16 a[182:183], v246 offset:10240\n"
    "v_mfma_f32_16x16x32_bf16 v[228:231], a[188:191], a[120:123], v[228:231]\n"
    "v_exp_f32_e32 v209, v209\n"
    "v_mfma_f32_16x16x32_bf16 v[228:231], a[184:187], a[116:119], v[228:231]\n"
    "v_cvt_pk_bf16_f32 v217, v218, v219\n"
    "v_mfma_f32_16x16x32_bf16 v[220:223], a[184:187], a[84:87], v[220:223]\n"
    "v_exp_f32_e32 v210, v210\n"
    "v_mfma_f32_16x16x32_bf16 v[220:223], a[188:191], a[88:91], v[220:223]\n"
    "v_exp_f32_e32 v211, v211\n"
    "v_mfma_f32_16x16x32_bf16 v[212:215], a[188:191], a[56:59], v[212:215]\n"
    "v_cvt_pk_bf16_f32 v208, v208, v209\n"
    "v_mfma_f32_16x16x32_bf16 v[212:215], a[184:187], a[52:55], v[212:215]\n"
    "v_exp_f32_e32 v200, v200\n"
    "v_mfma_f32_16x16x32_bf16 v[204:207], a[184:187], a[20:23], v[204:207]\n"
    "ds_read_b64_tr_b16 a[184:185], v247 offset:0\n"
    "v_mfma_f32_16x16x32_bf16 v[204:207], a[188:191], a[24:27], v[204:207]\n"
    "s_waitcnt lgkmcnt(5)\n"
    "ds_read_b64_tr_b16 a[186:187], v247 offset:2048\n"
    "v_mfma_f32_16x16x32_bf16 v[0:3], a[160:163], v[160:163], v[0:3]\n"
    "ds_read_b64_tr_b16 a[188:189], v247 offset:8192\n"
    "v_mfma_f32_16x16x32_bf16 v[0:3], a[164:167], v[164:167], v[0:3]\n"
    "ds_read_b64_tr_b16 a[190:191], v247 offset:10240\n"
    "v_mfma_f32_16x16x32_bf16 a[248:251], a[164:167], v[172:175], a[248:251]\n"
    "v_exp_f32_e32 v201, v201\n"
    "v_mfma_f32_16x16x32_bf16 a[248:251], a[160:163], v[168:171], a[248:251]\n"
    "v_cvt_pk_bf16_f32 v209, v210, v211\n"
    "v_mfma_f32_16x16x32_bf16 v[56:59], a[160:163], v[176:179], v[56:59]\n"
    "v_exp_f32_e32 v202, v202\n"
    "v_mfma_f32_16x16x32_bf16 v[56:59], a[164:167], v[180:183], v[56:59]\n"
    "v_exp_f32_e32 v203, v203\n"
    "v_mfma_f32_16x16x32_bf16 v[60:63], a[164:167], v[188:191], v[60:63]\n"
    "v_cvt_pk_bf16_f32 v200, v200, v201\n"
    "v_mfma_f32_16x16x32_bf16 v[60:63], a[160:163], v[184:187], v[60:63]\n"
    "v_cvt_pk_bf16_f32 v201, v202, v203\n"
    "v_mfma_f32_16x16x32_bf16 v[64:67], a[160:163], v[192:195], v[64:67]\n"
    "ds_read_b64_tr_b16 a[160:161], v244 offset:4096\n"
    "v_mfma_f32_16x16x32_bf16 v[64:67], a[164:167], v[196:199], v[64:67]\n"
    "ds_read_b64_tr_b16 a[162:163], v244 offset:6144\n"
    "v_mfma_f32_16x16x32_bf16 v[76:79], a[168:171], v[192:195], v[76:79]\n"
    "ds_read_b64_tr_b16 a[164:165], v244 offset:12288\n"
    "v_mfma_f32_16x16x32_bf16 v[76:79], a[172:175], v[196:199], v[76:79]\n"
    "ds_read_b64_tr_b16 a[166:167], v244 offset:14336\n"
    "v_mfma_f32_16x16x32_bf16 v[72:75], a[172:175], v[188:191], v[72:75]\n"
    "v_exp_f32_e32 v236, v236\n"
    "v_mfma_f32_16x16x32_bf16 v[72:75], a[168:171], v[184:187], v[72:75]\n"
    "v_exp_f32_e32 v237, v237\n"
    "v_mfma_f32_16x16x32_bf16 v[68:71], a[168:171], v[176:179], v[68:71]\n"
    "v_exp_f32_e32 v238, v238\n"
    "v_mfma_f32_16x16x32_bf16 v[68:71], a[172:175], v[180:183], v[68:71]\n"
    "v_exp_f32_e32 v239, v239\n"
    "v_mfma_f32_16x16x32_bf16 a[252:255], a[172:175], v[172:175], a[252:255]\n"
    "v_cvt_pk_bf16_f32 v234, v236, v237\n"
    "v_mfma_f32_16x16x32_bf16 a[252:255], a[168:171], v[168:171], a[252:255]\n"
    "v_exp_f32_e32 v228, v228\n"
    "v_mfma_f32_16x16x32_bf16 v[4:7], a[168:171], v[160:163], v[4:7]\n"
    "ds_read_b64_tr_b16 a[168:169], v245 offset:4096\n"
    "v_mfma_f32_16x16x32_bf16 v[4:7], a[172:175], v[164:167], v[4:7]\n"
    "s_waitcnt lgkmcnt(5)\n"
    "ds_read_b64_tr_b16 a[170:171], v245 offset:6144\n"
    "v_mfma_f32_16x16x32_bf16 v[8:11], a[176:179], v[160:163], v[8:11]\n"
    "ds_read_b64_tr_b16 a[172:173], v245 offset:12288\n"
    "v_mfma_f32_16x16x32_bf16 v[8:11], a[180:183], v[164:167], v[8:11]\n"
    "ds_read_b64_tr_b16 a[174:175], v245 offset:14336\n"
    "v_mfma_f32_16x16x32_bf16 v[32:35], a[180:183], v[172:175], v[32:35]\n"
    "v_exp_f32_e32 v229, v229\n"
    "v_mfma_f32_16x16x32_bf16 v[32:35], a[176:179], v[168:171], v[32:35]\n"
    "v_cvt_pk_bf16_f32 v235, v238, v239\n"
    "v_mfma_f32_16x16x32_bf16 v[80:83], a[176:179], v[176:179], v[80:83]\n"
    "v_exp_f32_e32 v230, v230\n"
    "v_mfma_f32_16x16x32_bf16 v[80:83], a[180:183], v[180:183], v[80:83]\n"
    "v_exp_f32_e32 v231, v231\n"
    "v_mfma_f32_16x16x32_bf16 v[84:87], a[180:183], v[188:191], v[84:87]\n"
    "v_cvt_pk_bf16_f32 v226, v228, v229\n"
    "v_mfma_f32_16x16x32_bf16 v[84:87], a[176:179], v[184:187], v[84:87]\n"
    "v_exp_f32_e32 v220, v220\n"
    "v_mfma_f32_16x16x32_bf16 v[88:91], a[176:179], v[192:195], v[88:91]\n"
    "ds_read_b64_tr_b16 a[176:177], v246 offset:4096\n"
    "v_mfma_f32_16x16x32_bf16 v[88:91], a[180:183], v[196:199], v[88:91]\n"
    "ds_read_b64_tr_b16 a[178:179], v246 offset:6144\n"
    "v_mfma_f32_16x16x32_bf16 v[100:103], a[184:187], v[192:195], v[100:103]\n"
    "ds_read_b64_tr_b16 a[180:181], v246 offset:12288\n"
    "v_mfma_f32_16x16x32_bf16 v[100:103], a[188:191], v[196:199], v[100:103]\n"
    "ds_read_b64_tr_b16 a[182:183], v246 offset:14336\n"
    "v_mfma_f32_16x16x32_bf16 v[96:99], a[188:191], v[188:191], v[96:99]\n"
    "v_exp_f32_e32 v221, v221\n"
    "v_mfma_f32_16x16x32_bf16 v[96:99], a[184:187], v[184:187], v[96:99]\n"
    "v_cvt_pk_bf16_f32 v227, v230, v231\n"
    "v_mfma_f32_16x16x32_bf16 v[92:95], a[184:187], v[176:179], v[92:95]\n"
    "v_exp_f32_e32 v222, v222\n"
    "v_mfma_f32_16x16x32_bf16 v[92:95], a[188:191], v[180:183], v[92:95]\n"
    "v_exp_f32_e32 v223, v223\n"
    "v_mfma_f32_16x16x32_bf16 v[36:39], a[188:191], v[172:175], v[36:39]\n"
    "v_cvt_pk_bf16_f32 v218, v220, v221\n"
    "v_mfma_f32_16x16x32_bf16 v[36:39], a[184:187], v[168:171], v[36:39]\n"
    "v_exp_f32_e32 v212, v212\n"
    "v_mfma_f32_16x16x32_bf16 v[12:15], a[184:187], v[160:163], v[12:15]\n"
    "s_mov_b32 m0, s49\n"
    "ds_read_b64_tr_b16 a[184:185], v247 offset:4096\n"
    "v_mfma_f32_16x16x32_bf16 v[12:15], a[188:191], v[164:167], v[12:15]\n"
    "s_waitcnt lgkmcnt(5)\n"
    "ds_read_b64_tr_b16 a[186:187], v247 offset:6144\n"
    "v_mfma_f32_16x16x32_bf16 v[16:19], a[160:163], v[160:163], v[16:19]\n"
    "ds_read_b64_tr_b16 a[188:189], v247 offset:12288\n"
    "v_mfma_f32_16x16x32_bf16 v[16:19], a[164:167], v[164:167], v[16:19]\n"
    "ds_read_b64_tr_b16 a[190:191], v247 offset:14336\n"
    "v_mfma_f32_16x16x32_bf16 v[40:43], a[164:167], v[172:175], v[40:43]\n"
    "v_exp_f32_e32 v213, v213\n"
    "v_mfma_f32_16x16x32_bf16 v[40:43], a[160:163], v[168:171], v[40:43]\n"
    "v_cvt_pk_bf16_f32 v219, v222, v223\n"
    "v_mfma_f32_16x16x32_bf16 v[104:107], a[160:163], v[176:179], v[104:107]\n"
    "v_exp_f32_e32 v214, v214\n"
    "v_mfma_f32_16x16x32_bf16 v[104:107], a[164:167], v[180:183], v[104:107]\n"
    "s_waitcnt vmcnt(8)\n"
    "v_mfma_f32_16x16x32_bf16 v[108:111], a[164:167], v[188:191], v[108:111]\n"
    "s_barrier\n"
    "v_mfma_f32_16x16x32_bf16 v[108:111], a[160:163], v[184:187], v[108:111]\n"
    "v_exp_f32_e32 v215, v215\n"
    "v_mfma_f32_16x16x32_bf16 v[112:115], a[160:163], v[192:195], v[112:115]\n"
    "ds_read_b128 a[160:163], v243 offset:36864\n"
    "v_mfma_f32_16x16x32_bf16 v[112:115], a[164:167], v[196:199], v[112:115]\n"
    "ds_read_b128 a[164:167], v240 offset:32768\n"
    "v_mfma_f32_16x16x32_bf16 v[124:127], a[168:171], v[192:195], v[124:127]\n"
    "ds_write_addtid_b32 v232 offset:16384\n"
    "v_cvt_pk_bf16_f32 v210, v212, v213\n"
    "v_mfma_f32_16x16x32_bf16 v[124:127], a[172:175], v[196:199], v[124:127]\n"
    "v_exp_f32_e32 v204, v204\n"
    "v_mfma_f32_16x16x32_bf16 v[120:123], a[172:175], v[188:191], v[120:123]\n"
    "v_exp_f32_e32 v205, v205\n"
    "v_mfma_f32_16x16x32_bf16 v[120:123], a[168:171], v[184:187], v[120:123]\n"
    "ds_write_addtid_b32 v233 offset:16640\n"
    "v_cvt_pk_bf16_f32 v211, v214, v215\n"
    "v_mfma_f32_16x16x32_bf16 v[116:119], a[168:171], v[176:179], v[116:119]\n"
    "v_exp_f32_e32 v206, v206\n"
    "v_mfma_f32_16x16x32_bf16 v[116:119], a[172:175], v[180:183], v[116:119]\n"
    "v_exp_f32_e32 v207, v207\n"
    "v_mfma_f32_16x16x32_bf16 v[44:47], a[172:175], v[172:175], v[44:47]\n"
    "ds_write_addtid_b32 v224 offset:12288\n"
    "v_cvt_pk_bf16_f32 v202, v204, v205\n"
    "v_mfma_f32_16x16x32_bf16 v[44:47], a[168:171], v[168:171], v[44:47]\n"
    "ds_write_addtid_b32 v225 offset:12544\n"
    "v_cvt_pk_bf16_f32 v203, v206, v207\n"
    "v_mfma_f32_16x16x32_bf16 v[20:23], a[168:171], v[160:163], v[20:23]\n"
    "ds_read_b128 a[168:171], v241 offset:32768\n"
    "v_mfma_f32_16x16x32_bf16 v[20:23], a[172:175], v[164:167], v[20:23]\n"
    "s_waitcnt lgkmcnt(7)\n"
    "ds_read_b128 a[172:175], v240 offset:36864\n"
    "v_mfma_f32_16x16x32_bf16 v[24:27], a[176:179], v[160:163], v[24:27]\n"
    "ds_write_addtid_b32 v216 offset:8192\n"
    "v_mfma_f32_16x16x32_bf16 v[24:27], a[180:183], v[164:167], v[24:27]\n"
    "ds_write_addtid_b32 v217 offset:8448\n"
    "v_mfma_f32_16x16x32_bf16 v[48:51], a[180:183], v[172:175], v[48:51]\n"
    "ds_write_addtid_b32 v208 offset:4096\n"
    "v_mfma_f32_16x16x32_bf16 v[48:51], a[176:179], v[168:171], v[48:51]\n"
    "ds_write_addtid_b32 v209 offset:4352\n"
    "v_mfma_f32_16x16x32_bf16 v[128:131], a[176:179], v[176:179], v[128:131]\n"
    "ds_write_addtid_b32 v200 offset:0\n"
    "v_mfma_f32_16x16x32_bf16 v[128:131], a[180:183], v[180:183], v[128:131]\n"
    "ds_write_addtid_b32 v201 offset:256\n"
    "v_mfma_f32_16x16x32_bf16 v[132:135], a[180:183], v[188:191], v[132:135]\n"
    "ds_write_addtid_b32 v234 offset:16896\n"
    "v_mfma_f32_16x16x32_bf16 v[132:135], a[176:179], v[184:187], v[132:135]\n"
    "ds_write_addtid_b32 v235 offset:17152\n"
    "v_mfma_f32_16x16x32_bf16 v[136:139], a[176:179], v[192:195], v[136:139]\n"
    "ds_read_b128 a[176:179], v241 offset:36864\n"
    "v_mfma_f32_16x16x32_bf16 v[136:139], a[180:183], v[196:199], v[136:139]\n"
    "ds_read_b128 a[180:183], v242 offset:32768\n"
    "v_mfma_f32_16x16x32_bf16 v[148:151], a[184:187], v[192:195], v[148:151]\n"
    "ds_write_addtid_b32 v226 offset:12800\n"
    "v_mfma_f32_16x16x32_bf16 v[148:151], a[188:191], v[196:199], v[148:151]\n"
    "ds_write_addtid_b32 v227 offset:13056\n"
    "v_mfma_f32_16x16x32_bf16 v[144:147], a[188:191], v[188:191], v[144:147]\n"
    "ds_write_addtid_b32 v218 offset:8704\n"
    "v_mfma_f32_16x16x32_bf16 v[144:147], a[184:187], v[184:187], v[144:147]\n"
    "ds_write_addtid_b32 v219 offset:8960\n"
    "v_mfma_f32_16x16x32_bf16 v[140:143], a[184:187], v[176:179], v[140:143]\n"
    "ds_write_addtid_b32 v210 offset:4608\n"
    "v_mfma_f32_16x16x32_bf16 v[140:143], a[188:191], v[180:183], v[140:143]\n"
    "ds_write_addtid_b32 v211 offset:4864\n"
    "v_mfma_f32_16x16x32_bf16 v[52:55], a[188:191], v[172:175], v[52:55]\n"
    "ds_write_addtid_b32 v202 offset:512\n"
    "v_mfma_f32_16x16x32_bf16 v[52:55], a[184:187], v[168:171], v[52:55]\n"
    "ds_write_addtid_b32 v203 offset:768\n"
    "v_mfma_f32_16x16x32_bf16 v[28:31], a[184:187], v[160:163], v[28:31]\n"
    "ds_read_b128 a[184:187], v243 offset:32768\n"
    "v_mfma_f32_16x16x32_bf16 v[28:31], a[188:191], v[164:167], v[28:31]\n"
    "s_waitcnt lgkmcnt(15)\n"
    "ds_read_b128 a[188:191], v242 offset:36864\n"
    "v_mfma_f32_16x16x32_bf16 v[160:163], a[160:163], a[28:31], 0\n"
    "v_mfma_f32_16x16x32_bf16 v[160:163], a[164:167], a[0:3], v[160:163]\n"
    "v_mfma_f32_16x16x32_bf16 v[168:171], a[164:167], a[32:35], 0\n"
    "v_mfma_f32_16x16x32_bf16 v[168:171], a[160:163], a[60:63], v[168:171]\n"
    "v_mfma_f32_16x16x32_bf16 v[176:179], a[160:163], a[92:95], 0\n"
    "v_mfma_f32_16x16x32_bf16 v[176:179], a[164:167], a[64:67], v[176:179]\n"
    "s_waitcnt lgkmcnt(0)\n"
    "v_mfma_f32_16x16x32_bf16 v[184:187], a[164:167], a[96:99], 0\n"
    "s_barrier\n"
    "v_mfma_f32_16x16x32_bf16 v[184:187], a[160:163], a[124:127], v[184:187]\n"
    "s_add_u32 m0, s43, 0\n"
    "ds_read2st64_b32 v[204:205], v250 offset0:0 offset1:1\n"
    "v_mfma_f32_16x16x32_bf16 v[192:195], a[160:163], a[156:159], 0\n"
    "ds_read_b128 a[160:163], v243 offset:38912\n"
    "v_mfma_f32_16x16x32_bf16 v[192:195], a[164:167], a[128:131], v[192:195]\n"
    "ds_read_b128 a[164:167], v240 offset:34816\n"
    "v_mfma_f32_16x16x32_bf16 v[192:195], a[168:171], a[132:135], v[192:195]\n"
    "buffer_load_dwordx4 v248, s[8:11], s40 offen offset:0 sc1 lds\n"
    "v_mfma_f32_16x16x32_bf16 v[192:195], a[172:175], a[136:139], v[192:195]\n"
    "v_mfma_f32_16x16x32_bf16 v[184:187], a[172:175], a[104:107], v[184:187]\n"
    "v_mfma_f32_16x16x32_bf16 v[184:187], a[168:171], a[100:103], v[184:187]\n"
    "ds_read2st64_b32 v[206:207], v250 offset0:2 offset1:3\n"
    "v_mfma_f32_16x16x32_bf16 v[176:179], a[168:171], a[68:71], v[176:179]\n"
    "v_mfma_f32_16x16x32_bf16 v[176:179], a[172:175], a[72:75], v[176:179]\n"
    "buffer_load_dwordx4 v248, s[8:11], s40 offen offset:1024 sc1 lds\n"
    "v_mfma_f32_16x16x32_bf16 v[168:171], a[172:175], a[40:43], v[168:171]\n"
    "v_mfma_f32_16x16x32_bf16 v[168:171], a[168:171], a[36:39], v[168:171]\n"
    "ds_read2st64_b32 v[212:213], v250 offset0:16 offset1:17\n"
    "v_mfma_f32_16x16x32_bf16 v[160:163], a[168:171], a[4:7], v[160:163]\n"
    "ds_read_b128 a[168:171], v241 offset:34816\n"
    "v_mfma_f32_16x16x32_bf16 v[160:163], a[172:175], a[8:11], v[160:163]\n"
    "ds_read_b128 a[172:175], v240 offset:38912\n"
    "v_mfma_f32_16x16x32_bf16 v[160:163], a[176:179], a[12:15], v[160:163]\n"
    "buffer_load_dwordx4 v248, s[8:11], s40 offen offset:2048 sc1 lds\n"
    "v_mfma_f32_16x16x32_bf16 v[160:163], a[180:183], a[16:19], v[160:163]\n"
    "ds_read2st64_b32 v[214:215], v250 offset0:18 offset1:19\n"
    "v_mfma_f32_16x16x32_bf16 v[168:171], a[180:183], a[48:51], v[168:171]\n"
    "v_mfma_f32_16x16x32_bf16 v[168:171], a[176:179], a[44:47], v[168:171]\n"
    "v_mfma_f32_16x16x32_bf16 v[176:179], a[176:179], a[76:79], v[176:179]\n"
    "ds_read2st64_b32 v[220:221], v250 offset0:32 offset1:33\n"
    "v_mfma_f32_16x16x32_bf16 v[176:179], a[180:183], a[80:83], v[176:179]\n"
    "buffer_load_dwordx4 v248, s[8:11], s40 offen offset:3072 sc1 lds\n"
    "v_mfma_f32_16x16x32_bf16 v[184:187], a[180:183], a[112:115], v[184:187]\n"
    "s_add_u32 m0, s43, 4096\n"
    "v_mfma_f32_16x16x32_bf16 v[184:187], a[176:179], a[108:111], v[184:187]\n"
    "s_add_u32 s40, s40, 0x8000\n"
    "ds_read2st64_b32 v[222:223], v250 offset0:34 offset1:35\n"
    "v_mfma_f32_16x16x32_bf16 v[192:195], a[176:179], a[140:143], v[192:195]\n"
    "ds_read_b128 a[176:179], v241 offset:38912\n"
    "v_mfma_f32_16x16x32_bf16 v[192:195], a[180:183], a[144:147], v[192:195]\n"
    "ds_read_b128 a[180:183], v242 offset:34816\n"
    "v_mfma_f32_16x16x32_bf16 v[192:195], a[184:187], a[148:151], v[192:195]\n"
    "buffer_load_dwordx4 v248, s[8:11], s41 offen offset:0 sc1 lds\n"
    "v_mfma_f32_16x16x32_bf16 v[192:195], a[188:191], a[152:155], v[192:195]\n"
    "v_mfma_f32_16x16x32_bf16 v[184:187], a[188:191], a[120:123], v[184:187]\n"
    "ds_read2st64_b32 v[228:229], v250 offset0:48 offset1:49\n"
    "v_mfma_f32_16x16x32_bf16 v[184:187], a[184:187], a[116:119], v[184:187]\n"
    "v_mfma_f32_16x16x32_bf16 v[176:179], a[184:187], a[84:87], v[176:179]\n"
    "v_exp_f32_e32 v192, v192\n"
    "v_mfma_f32_16x16x32_bf16 v[176:179], a[188:191], a[88:91], v[176:179]\n"
    "buffer_load_dwordx4 v248, s[8:11], s41 offen offset:1024 sc1 lds\n"
    "v_mfma_f32_16x16x32_bf16 v[168:171], a[188:191], a[56:59], v[168:171]\n"
    "ds_read2st64_b32 v[230:231], v250 offset0:50 offset1:51\n"
    "v_mfma_f32_16x16x32_bf16 v[168:171], a[184:187], a[52:55], v[168:171]\n"
    "v_exp_f32_e32 v193, v193\n"
    "v_mfma_f32_16x16x32_bf16 v[160:163], a[184:187], a[20:23], v[160:163]\n"
    "ds_read_b128 a[184:187], v243 offset:34816\n"
    "v_mfma_f32_16x16x32_bf16 v[160:163], a[188:191], a[24:27], v[160:163]\n"
    "s_waitcnt lgkmcnt(8)\n"
    "ds_read_b128 a[188:191], v242 offset:38912\n"
    "v_mfma_f32_16x16x32_bf16 v[164:167], a[160:163], a[28:31], 0\n"
    "buffer_load_dwordx4 v248, s[8:11], s41 offen offset:2048 sc1 lds\n"
    "v_mfma_f32_16x16x32_bf16 v[164:167], a[164:167], a[0:3], v[164:167]\n"
    "ds_read2st64_b32 v[236:237], v250 offset0:64 offset1:65\n"
    "v_mfma_f32_16x16x32_bf16 v[172:175], a[164:167], a[32:35], 0\n"
    "v_exp_f32_e32 v194, v194\n"
    "v_mfma_f32_16x16x32_bf16 v[172:175], a[160:163], a[60:63], v[172:175]\n"
    "ds_read2st64_b32 v[238:239], v250 offset0:66 offset1:67\n"
    "v_mfma_f32_16x16x32_bf16 v[180:183], a[160:163], a[92:95], 0\n"
    "v_exp_f32_e32 v195, v195\n"
    "v_mfma_f32_16x16x32_bf16 v[180:183], a[164:167], a[64:67], v[180:183]\n"
    "buffer_load_dwordx4 v248, s[8:11], s41 offen offset:3072 sc1 lds\n"
    "v_mfma_f32_16x16x32_bf16 v[188:191], a[164:167], a[96:99], 0\n"
    "s_add_u32 s41, s41, 0x8000\n"
    "v_cvt_pk_bf16_f32 v192, v192, v193\n"
    "v_mfma_f32_16x16x32_bf16 v[188:191], a[160:163], a[124:127], v[188:191]\n"
    "v_exp_f32_e32 v184, v184\n"
    "v_mfma_f32_16x16x32_bf16 v[196:199], a[160:163], a[156:159], 0\n"
    "ds_read_b64_tr_b16 a[160:161], v244 offset:16384\n"
    "v_mfma_f32_16x16x32_bf16 v[196:199], a[164:167], a[128:131], v[196:199]\n"
    "ds_read_b64_tr_b16 a[162:163], v244 offset:18432\n"
    "v_mfma_f32_16x16x32_bf16 v[196:199], a[168:171], a[132:135], v[196:199]\n"
    "ds_read_b64_tr_b16 a[164:165], v244 offset:24576\n"
    "v_mfma_f32_16x16x32_bf16 v[196:199], a[172:175], a[136:139], v[196:199]\n"
    "ds_read_b64_tr_b16 a[166:167], v244 offset:26624\n"
    "v_mfma_f32_16x16x32_bf16 v[188:191], a[172:175], a[104:107], v[188:191]\n"
    "v_exp_f32_e32 v185, v185\n"
    "v_mfma_f32_16x16x32_bf16 v[188:191], a[168:171], a[100:103], v[188:191]\n"
    "v_cvt_pk_bf16_f32 v193, v194, v195\n"
    "v_mfma_f32_16x16x32_bf16 v[180:183], a[168:171], a[68:71], v[180:183]\n"
    "v_exp_f32_e32 v186, v186\n"
    "v_mfma_f32_16x16x32_bf16 v[180:183], a[172:175], a[72:75], v[180:183]\n"
    "v_exp_f32_e32 v187, v187\n"
    "v_mfma_f32_16x16x32_bf16 v[172:175], a[172:175], a[40:43], v[172:175]\n"
    "v_cvt_pk_bf16_f32 v184, v184, v185\n"
    "v_mfma_f32_16x16x32_bf16 v[172:175], a[168:171], a[36:39], v[172:175]\n"
    "v_exp_f32_e32 v176, v176\n"
    "v_mfma_f32_16x16x32_bf16 v[164:167], a[168:171], a[4:7], v[164:167]\n"
    "ds_read_b64_tr_b16 a[168:169], v245 offset:16384\n"
    "v_mfma_f32_16x16x32_bf16 v[164:167], a[172:175], a[8:11], v[164:167]\n"
    "s_waitcnt lgkmcnt(7)\n"
    "ds_read_b64_tr_b16 a[170:171], v245 offset:18432\n"
    "v_mfma_f32_16x16x32_bf16 v[164:167], a[176:179], a[12:15], v[164:167]\n"
    "ds_read_b64_tr_b16 a[172:173], v245 offset:24576\n"
    "v_mfma_f32_16x16x32_bf16 v[164:167], a[180:183], a[16:19], v[164:167]\n"
    "ds_read_b64_tr_b16 a[174:175], v245 offset:26624\n"
    "v_mfma_f32_16x16x32_bf16 v[172:175], a[180:183], a[48:51], v[172:175]\n"
    "v_exp_f32_e32 v177, v177\n"
    "v_mfma_f32_16x16x32_bf16 v[172:175], a[176:179], a[44:47], v[172:175]\n"
    "v_cvt_pk_bf16_f32 v185, v186, v187\n"
    "v_mfma_f32_16x16x32_bf16 v[180:183], a[176:179], a[76:79], v[180:183]\n"
    "v_exp_f32_e32 v178, v178\n"
    "v_mfma_f32_16x16x32_bf16 v[180:183], a[180:183], a[80:83], v[180:183]\n"
    "v_exp_f32_e32 v179, v179\n"
    "v_mfma_f32_16x16x32_bf16 v[188:191], a[180:183], a[112:115], v[188:191]\n"
    "v_cvt_pk_bf16_f32 v176, v176, v177\n"
    "v_mfma_f32_16x16x32_bf16 v[188:191], a[176:179], a[108:111], v[188:191]\n"
    "v_exp_f32_e32 v168, v168\n"
    "v_mfma_f32_16x16x32_bf16 v[196:199], a[176:179], a[140:143], v[196:199]\n"
    "ds_read_b64_tr_b16 a[176:177], v246 offset:16384\n"
    "v_mfma_f32_16x16x32_bf16 v[196:199], a[180:183], a[144:147], v[196:199]\n"
    "ds_read_b64_tr_b16 a[178:179], v246 offset:18432\n"
    "v_mfma_f32_16x16x32_bf16 v[196:199], a[184:187], a[148:151], v[196:199]\n"
    "ds_read_b64_tr_b16 a[180:181], v246 offset:24576\n"
    "v_mfma_f32_16x16x32_bf16 v[196:199], a[188:191], a[152:155], v[196:199]\n"
    "ds_read_b64_tr_b16 a[182:183], v246 offset:26624\n"
    "v_mfma_f32_16x16x32_bf16 v[188:191], a[188:191], a[120:123], v[188:191]\n"
    "v_exp_f32_e32 v169, v169\n"
    "v_mfma_f32_16x16x32_bf16 v[188:191], a[184:187], a[116:119], v[188:191]\n"
    "v_cvt_pk_bf16_f32 v177, v178, v179\n"
    "v_mfma_f32_16x16x32_bf16 v[180:183], a[184:187], a[84:87], v[180:183]\n"
    "v_exp_f32_e32 v170, v170\n"
    "v_mfma_f32_16x16x32_bf16 v[180:183], a[188:191], a[88:91], v[180:183]\n"
    "v_exp_f32_e32 v171, v171\n"
    "v_mfma_f32_16x16x32_bf16 v[172:175], a[188:191], a[56:59], v[172:175]\n"
    "v_cvt_pk_bf16_f32 v168, v168, v169\n"
    "v_mfma_f32_16x16x32_bf16 v[172:175], a[184:187], a[52:55], v[172:175]\n"
    "v_exp_f32_e32 v160, v160\n"
    "v_mfma_f32_16x16x32_bf16 v[164:167], a[184:187], a[20:23], v[164:167]\n"
    "ds_read_b64_tr_b16 a[184:185], v247 offset:16384\n"
    "v_mfma_f32_16x16x32_bf16 v[164:167], a[188:191], a[24:27], v[164:167]\n"
    "s_waitcnt lgkmcnt(5)\n"
    "ds_read_b64_tr_b16 a[186:187], v247 offset:18432\n"
    "v_mfma_f32_16x16x32_bf16 v[0:3], a[160:163], v[200:203], v[0:3]\n"
    "ds_read_b64_tr_b16 a[188:189], v247 offset:24576\n"
    "v_mfma_f32_16x16x32_bf16 v[0:3], a[164:167], v[204:207], v[0:3]\n"
    "ds_read_b64_tr_b16 a[190:191], v247 offset:26624\n"
    "v_mfma_f32_16x16x32_bf16 a[248:251], a[164:167], v[212:215], a[248:251]\n"
    "v_exp_f32_e32 v161, v161\n"
    "v_mfma_f32_16x16x32_bf16 a[248:251], a[160:163], v[208:211], a[248:251]\n"
    "v_cvt_pk_bf16_f32 v169, v170, v171\n"
    "v_mfma_f32_16x16x32_bf16 v[56:59], a[160:163], v[216:219], v[56:59]\n"
    "v_exp_f32_e32 v162, v162\n"
    "v_mfma_f32_16x16x32_bf16 v[56:59], a[164:167], v[220:223], v[56:59]\n"
    "v_exp_f32_e32 v163, v163\n"
    "v_mfma_f32_16x16x32_bf16 v[60:63], a[164:167], v[228:231], v[60:63]\n"
    "v_cvt_pk_bf16_f32 v160, v160, v161\n"
    "v_mfma_f32_16x16x32_bf16 v[60:63], a[160:163], v[224:227], v[60:63]\n"
    "v_cvt_pk_bf16_f32 v161, v162, v163\n"
    "v_mfma_f32_16x16x32_bf16 v[64:67], a[160:163], v[232:235], v[64:67]\n"
    "ds_read_b64_tr_b16 a[160:161], v244 offset:20480\n"
    "v_mfma_f32_16x16x32_bf16 v[64:67], a[164:167], v[236:239], v[64:67]\n"
    "ds_read_b64_tr_b16 a[162:163], v244 offset:22528\n"
    "v_mfma_f32_16x16x32_bf16 v[76:79], a[168:171], v[232:235], v[76:79]\n"
    "ds_read_b64_tr_b16 a[164:165], v244 offset:28672\n"
    "v_mfma_f32_16x16x32_bf16 v[76:79], a[172:175], v[236:239], v[76:79]\n"
    "ds_read_b64_tr_b16 a[166:167], v244 offset:30720\n"
    "v_mfma_f32_16x16x32_bf16 v[72:75], a[172:175], v[228:231], v[72:75]\n"
    "v_exp_f32_e32 v196, v196\n"
    "v_mfma_f32_16x16x32_bf16 v[72:75], a[168:171], v[224:227], v[72:75]\n"
    "v_exp_f32_e32 v197, v197\n"
    "v_mfma_f32_16x16x32_bf16 v[68:71], a[168:171], v[216:219], v[68:71]\n"
    "v_exp_f32_e32 v198, v198\n"
    "v_mfma_f32_16x16x32_bf16 v[68:71], a[172:175], v[220:223], v[68:71]\n"
    "v_exp_f32_e32 v199, v199\n"
    "v_mfma_f32_16x16x32_bf16 a[252:255], a[172:175], v[212:215], a[252:255]\n"
    "v_cvt_pk_bf16_f32 v194, v196, v197\n"
    "v_mfma_f32_16x16x32_bf16 a[252:255], a[168:171], v[208:211], a[252:255]\n"
    "v_exp_f32_e32 v188, v188\n"
    "v_mfma_f32_16x16x32_bf16 v[4:7], a[168:171], v[200:203], v[4:7]\n"
    "ds_read_b64_tr_b16 a[168:169], v245 offset:20480\n"
    "v_mfma_f32_16x16x32_bf16 v[4:7], a[172:175], v[204:207], v[4:7]\n"
    "s_waitcnt lgkmcnt(5)\n"
    "ds_read_b64_tr_b16 a[170:171], v245 offset:22528\n"
    "v_mfma_f32_16x16x32_bf16 v[8:11], a[176:179], v[200:203], v[8:11]\n"
    "ds_read_b64_tr_b16 a[172:173], v245 offset:28672\n"
    "v_mfma_f32_16x16x32_bf16 v[8:11], a[180:183], v[204:207], v[8:11]\n"
    "ds_read_b64_tr_b16 a[174:175], v245 offset:30720\n"
    "v_mfma_f32_16x16x32_bf16 v[32:35], a[180:183], v[212:215], v[32:35]\n"
    "v_exp_f32_e32 v189, v189\n"
    "v_mfma_f32_16x16x32_bf16 v[32:35], a[176:179], v[208:211], v[32:35]\n"
    "v_cvt_pk_bf16_f32 v195, v198, v199\n"
    "v_mfma_f32_16x16x32_bf16 v[80:83], a[176:179], v[216:219], v[80:83]\n"
    "v_exp_f32_e32 v190, v190\n"
    "v_mfma_f32_16x16x32_bf16 v[80:83], a[180:183], v[220:223], v[80:83]\n"
    "v_exp_f32_e32 v191, v191\n"
    "v_mfma_f32_16x16x32_bf16 v[84:87], a[180:183], v[228:231], v[84:87]\n"
    "v_cvt_pk_bf16_f32 v186, v188, v189\n"
    "v_mfma_f32_16x16x32_bf16 v[84:87], a[176:179], v[224:227], v[84:87]\n"
    "v_exp_f32_e32 v180, v180\n"
    "v_mfma_f32_16x16x32_bf16 v[88:91], a[176:179], v[232:235], v[88:91]\n"
    "ds_read_b64_tr_b16 a[176:177], v246 offset:20480\n"
    "v_mfma_f32_16x16x32_bf16 v[88:91], a[180:183], v[236:239], v[88:91]\n"
    "ds_read_b64_tr_b16 a[178:179], v246 offset:22528\n"
    "v_mfma_f32_16x16x32_bf16 v[100:103], a[184:187], v[232:235], v[100:103]\n"
    "ds_read_b64_tr_b16 a[180:181], v246 offset:28672\n"
    "v_mfma_f32_16x16x32_bf16 v[100:103], a[188:191], v[236:239], v[100:103]\n"
    "ds_read_b64_tr_b16 a[182:183], v246 offset:30720\n"
    "v_mfma_f32_16x16x32_bf16 v[96:99], a[188:191], v[228:231], v[96:99]\n"
    "v_exp_f32_e32 v181, v181\n"
    "v_mfma_f32_16x16x32_bf16 v[96:99], a[184:187], v[224:227], v[96:99]\n"
    "v_cvt_pk_bf16_f32 v187, v190, v191\n"
    "v_mfma_f32_16x16x32_bf16 v[92:95], a[184:187], v[216:219], v[92:95]\n"
    "v_exp_f32_e32 v182, v182\n"
    "v_mfma_f32_16x16x32_bf16 v[92:95], a[188:191], v[220:223], v[92:95]\n"
    "v_exp_f32_e32 v183, v183\n"
    "v_mfma_f32_16x16x32_bf16 v[36:39], a[188:191], v[212:215], v[36:39]\n"
    "v_cvt_pk_bf16_f32 v178, v180, v181\n"
    "v_mfma_f32_16x16x32_bf16 v[36:39], a[184:187], v[208:211], v[36:39]\n"
    "v_exp_f32_e32 v172, v172\n"
    "v_mfma_f32_16x16x32_bf16 v[12:15], a[184:187], v[200:203], v[12:15]\n"
    "s_mov_b32 m0, s49\n"
    "ds_read_b64_tr_b16 a[184:185], v247 offset:20480\n"
    "v_mfma_f32_16x16x32_bf16 v[12:15], a[188:191], v[204:207], v[12:15]\n"
    "s_waitcnt lgkmcnt(5)\n"
    "ds_read_b64_tr_b16 a[186:187], v247 offset:22528\n"
    "v_mfma_f32_16x16x32_bf16 v[16:19], a[160:163], v[200:203], v[16:19]\n"
    "ds_read_b64_tr_b16 a[188:189], v247 offset:28672\n"
    "v_mfma_f32_16x16x32_bf16 v[16:19], a[164:167], v[204:207], v[16:19]\n"
    "ds_read_b64_tr_b16 a[190:191], v247 offset:30720\n"
    "v_mfma_f32_16x16x32_bf16 v[40:43], a[164:167], v[212:215], v[40:43]\n"
    "v_exp_f32_e32 v173, v173\n"
    "v_mfma_f32_16x16x32_bf16 v[40:43], a[160:163], v[208:211], v[40:43]\n"
    "v_cvt_pk_bf16_f32 v179, v182, v183\n"
    "v_mfma_f32_16x16x32_bf16 v[104:107], a[160:163], v[216:219], v[104:107]\n"
    "v_exp_f32_e32 v174, v174\n"
    "v_mfma_f32_16x16x32_bf16 v[104:107], a[164:167], v[220:223], v[104:107]\n"
    "s_waitcnt vmcnt(8)\n"
    "v_mfma_f32_16x16x32_bf16 v[108:111], a[164:167], v[228:231], v[108:111]\n"
    "s_barrier\n"
    "v_mfma_f32_16x16x32_bf16 v[108:111], a[160:163], v[224:227], v[108:111]\n"
    "v_exp_f32_e32 v175, v175\n"
    "v_mfma_f32_16x16x32_bf16 v[112:115], a[160:163], v[232:235], v[112:115]\n"
    "ds_read_b128 a[160:163], v243 offset:53248\n"
    "v_mfma_f32_16x16x32_bf16 v[112:115], a[164:167], v[236:239], v[112:115]\n"
    "ds_read_b128 a[164:167], v240 offset:49152\n"
    "v_mfma_f32_16x16x32_bf16 v[124:127], a[168:171], v[232:235], v[124:127]\n"
    "ds_write_addtid_b32 v192 offset:16384\n"
    "v_cvt_pk_bf16_f32 v170, v172, v173\n"
    "v_mfma_f32_16x16x32_bf16 v[124:127], a[172:175], v[236:239], v[124:127]\n"
    "v_exp_f32_e32 v164, v164\n"
    "v_mfma_f32_16x16x32_bf16 v[120:123], a[172:175], v[228:231], v[120:123]\n"
    "v_exp_f32_e32 v165, v165\n"
    "v_mfma_f32_16x16x32_bf16 v[120:123], a[168:171], v[224:227], v[120:123]\n"
    "ds_write_addtid_b32 v193 offset:16640\n"
    "v_cvt_pk_bf16_f32 v171, v174, v175\n"
    "v_mfma_f32_16x16x32_bf16 v[116:119], a[168:171], v[216:219], v[116:119]\n"
    "v_exp_f32_e32 v166, v166\n"
    "v_mfma_f32_16x16x32_bf16 v[116:119], a[172:175], v[220:223], v[116:119]\n"
    "v_exp_f32_e32 v167, v167\n"
    "v_mfma_f32_16x16x32_bf16 v[44:47], a[172:175], v[212:215], v[44:47]\n"
    "ds_write_addtid_b32 v184 offset:12288\n"
    "v_cvt_pk_bf16_f32 v162, v164, v165\n"
    "v_mfma_f32_16x16x32_bf16 v[44:47], a[168:171], v[208:211], v[44:47]\n"
    "ds_write_addtid_b32 v185 offset:12544\n"
    "v_cvt_pk_bf16_f32 v163, v166, v167\n"
    "v_mfma_f32_16x16x32_bf16 v[20:23], a[168:171], v[200:203], v[20:23]\n"
    "ds_read_b128 a[168:171], v241 offset:49152\n"
    "v_mfma_f32_16x16x32_bf16 v[20:23], a[172:175], v[204:207], v[20:23]\n"
    "s_waitcnt lgkmcnt(7)\n"
    "ds_read_b128 a[172:175], v240 offset:53248\n"
    "v_mfma_f32_16x16x32_bf16 v[24:27], a[176:179], v[200:203], v[24:27]\n"
    "ds_write_addtid_b32 v176 offset:8192\n"
    "v_mfma_f32_16x16x32_bf16 v[24:27], a[180:183], v[204:207], v[24:27]\n"
    "ds_write_addtid_b32 v177 offset:8448\n"
    "v_mfma_f32_16x16x32_bf16 v[48:51], a[180:183], v[212:215], v[48:51]\n"
    "ds_write_addtid_b32 v168 offset:4096\n"
    "v_mfma_f32_16x16x32_bf16 v[48:51], a[176:179], v[208:211], v[48:51]\n"
    "ds_write_addtid_b32 v169 offset:4352\n"
    "v_mfma_f32_16x16x32_bf16 v[128:131], a[176:179], v[216:219], v[128:131]\n"
    "ds_write_addtid_b32 v160 offset:0\n"
    "v_mfma_f32_16x16x32_bf16 v[128:131], a[180:183], v[220:223], v[128:131]\n"
    "ds_write_addtid_b32 v161 offset:256\n"
    "v_mfma_f32_16x16x32_bf16 v[132:135], a[180:183], v[228:231], v[132:135]\n"
    "ds_write_addtid_b32 v194 offset:16896\n"
    "v_mfma_f32_16x16x32_bf16 v[132:135], a[176:179], v[224:227], v[132:135]\n"
    "ds_write_addtid_b32 v195 offset:17152\n"
    "v_mfma_f32_16x16x32_bf16 v[136:139], a[176:179], v[232:235], v[136:139]\n"
    "ds_read_b128 a[176:179], v241 offset:53248\n"
    "v_mfma_f32_16x16x32_bf16 v[136:139], a[180:183], v[236:239], v[136:139]\n"
    "ds_read_b128 a[180:183], v242 offset:49152\n"
    "v_mfma_f32_16x16x32_bf16 v[148:151], a[184:187], v[232:235], v[148:151]\n"
    "ds_write_addtid_b32 v186 offset:12800\n"
    "v_mfma_f32_16x16x32_bf16 v[148:151], a[188:191], v[236:239], v[148:151]\n"
    "ds_write_addtid_b32 v187 offset:13056\n"
    "v_mfma_f32_16x16x32_bf16 v[144:147], a[188:191], v[228:231], v[144:147]\n"
    "ds_write_addtid_b32 v178 offset:8704\n"
    "v_mfma_f32_16x16x32_bf16 v[144:147], a[184:187], v[224:227], v[144:147]\n"
    "ds_write_addtid_b32 v179 offset:8960\n"
    "v_mfma_f32_16x16x32_bf16 v[140:143], a[184:187], v[216:219], v[140:143]\n"
    "ds_write_addtid_b32 v170 offset:4608\n"
    "v_mfma_f32_16x16x32_bf16 v[140:143], a[188:191], v[220:223], v[140:143]\n"
    "ds_write_addtid_b32 v171 offset:4864\n"
    "v_mfma_f32_16x16x32_bf16 v[52:55], a[188:191], v[212:215], v[52:55]\n"
    "ds_write_addtid_b32 v162 offset:512\n"
    "v_mfma_f32_16x16x32_bf16 v[52:55], a[184:187], v[208:211], v[52:55]\n"
    "ds_write_addtid_b32 v163 offset:768\n"
    "v_mfma_f32_16x16x32_bf16 v[28:31], a[184:187], v[200:203], v[28:31]\n"
    "ds_read_b128 a[184:187], v243 offset:49152\n"
    "v_mfma_f32_16x16x32_bf16 v[28:31], a[188:191], v[204:207], v[28:31]\n"
    "s_waitcnt lgkmcnt(15)\n"
    "ds_read_b128 a[188:191], v242 offset:53248\n"
    ".Lat_p2e:\n"
    "v_mfma_f32_16x16x32_bf16 v[200:203], a[160:163], a[28:31], 0\n"
    "v_mfma_f32_16x16x32_bf16 v[200:203], a[164:167], a[0:3], v[200:203]\n"
    "v_mfma_f32_16x16x32_bf16 v[208:211], a[164:167], a[32:35], 0\n"
    "v_mfma_f32_16x16x32_bf16 v[208:211], a[160:163], a[60:63], v[208:211]\n"
    "v_mfma_f32_16x16x32_bf16 v[216:219], a[160:163], a[92:95], 0\n"
    "v_mfma_f32_16x16x32_bf16 v[216:219], a[164:167], a[64:67], v[216:219]\n"
    "s_waitcnt lgkmcnt(0)\n"
    "v_mfma_f32_16x16x32_bf16 v[224:227], a[164:167], a[96:99], 0\n"
    "s_barrier\n"
    "v_mfma_f32_16x16x32_bf16 v[224:227], a[160:163], a[124:127], v[224:227]\n"
    "s_add_u32 m0, s43, 16384\n"
    "ds_read2st64_b32 v[164:165], v250 offset0:0 offset1:1\n"
    "v_mfma_f32_16x16x32_bf16 v[232:235], a[160:163], a[156:159], 0\n"
    "ds_read_b128 a[160:163], v243 offset:55296\n"
    "v_mfma_f32_16x16x32_bf16 v[232:235], a[164:167], a[128:131], v[232:235]\n"
    "ds_read_b128 a[164:167], v240 offset:51200\n"
    "v_mfma_f32_16x16x32_bf16 v[232:235], a[168:171], a[132:135], v[232:235]\n"
    "buffer_load_dwordx4 v248, s[8:11], s40 offen offset:0 sc1 lds\n"
    "v_mfma_f32_16x16x32_bf16 v[232:235], a[172:175], a[136:139], v[232:235]\n"
    "v_mfma_f32_16x16x32_bf16 v[224:227], a[172:175], a[104:107], v[224:227]\n"
    "v_mfma_f32_16x16x32_bf16 v[224:227], a[168:171], a[100:103], v[224:227]\n"
    "ds_read2st64_b32 v[166:167], v250 offset0:2 offset1:3\n"
    "v_mfma_f32_16x16x32_bf16 v[216:219], a[168:171], a[68:71], v[216:219]\n"
    "v_mfma_f32_16x16x32_bf16 v[216:219], a[172:175], a[72:75], v[216:219]\n"
    "buffer_load_dwordx4 v248, s[8:11], s40 offen offset:1024 sc1 lds\n"
    "v_mfma_f32_16x16x32_bf16 v[208:211], a[172:175], a[40:43], v[208:211]\n"
    "v_mfma_f32_16x16x32_bf16 v[208:211], a[168:171], a[36:39], v[208:211]\n"
    "ds_read2st64_b32 v[172:173], v250 offset0:16 offset1:17\n"
    "v_mfma_f32_16x16x32_bf16 v[200:203], a[168:171], a[4:7], v[200:203]\n"
    "ds_read_b128 a[168:171], v241 offset:51200\n"
    "v_mfma_f32_16x16x32_bf16 v[200:203], a[172:175], a[8:11], v[200:203]\n"
    "ds_read_b128 a[172:175], v240 offset:55296\n"
    "v_mfma_f32_16x16x32_bf16 v[200:203], a[176:179], a[12:15], v[200:203]\n"
    "buffer_load_dwordx4 v248, s[8:11], s40 offen offset:2048 sc1 lds\n"
    "v_mfma_f32_16x16x32_bf16 v[200:203], a[180:183], a[16:19], v[200:203]\n"
    "ds_read2st64_b32 v[174:175], v250 offset0:18 offset1:19\n"
    "v_mfma_f32_16x16x32_bf16 v[208:211], a[180:183], a[48:51], v[208:211]\n"
    "v_mfma_f32_16x16x32_bf16 v[208:211], a[176:179], a[44:47], v[208:211]\n"
    "v_mfma_f32_16x16x32_bf16 v[216:219], a[176:179], a[76:79], v[216:219]\n"
    "ds_read2st64_b32 v[180:181], v250 offset0:32 offset1:33\n"
    "v_mfma_f32_16x16x32_bf16 v[216:219], a[180:183], a[80:83], v[216:219]\n"
    "buffer_load_dwordx4 v248, s[8:11], s40 offen offset:3072 sc1 lds\n"
    "v_mfma_f32_16x16x32_bf16 v[224:227], a[180:183], a[112:115], v[224:227]\n"
    "s_add_u32 m0, s43, 20480\n"
    "v_mfma_f32_16x16x32_bf16 v[224:227], a[176:179], a[108:111], v[224:227]\n"
    "s_add_u32 s40, s40, 0x8000\n"
    "ds_read2st64_b32 v[182:183], v250 offset0:34 offset1:35\n"
    "v_mfma_f32_16x16x32_bf16 v[232:235], a[176:179], a[140:143], v[232:235]\n"
    "ds_read_b128 a[176:179], v241 offset:55296\n"
    "v_mfma_f32_16x16x32_bf16 v[232:235], a[180:183], a[144:147], v[232:235]\n"
    "ds_read_b128 a[180:183], v242 offset:51200\n"
    "v_mfma_f32_16x16x32_bf16 v[232:235], a[184:187], a[148:151], v[232:235]\n"
    "buffer_load_dwordx4 v248, s[8:11], s41 offen offset:0 sc1 lds\n"
    "v_mfma_f32_16x16x32_bf16 v[232:235], a[188:191], a[152:155], v[232:235]\n"
    "v_mfma_f32_16x16x32_bf16 v[224:227], a[188:191], a[120:123], v[224:227]\n"
    "ds_read2st64_b32 v[188:189], v250 offset0:48 offset1:49\n"
    "v_mfma_f32_16x16x32_bf16 v[224:227], a[184:187], a[116:119], v[224:227]\n"
    "v_mfma_f32_16x16x32_bf16 v[216:219], a[184:187], a[84:87], v[216:219]\n"
    "v_exp_f32_e32 v232, v232\n"
    "v_mfma_f32_16x16x32_bf16 v[216:219], a[188:191], a[88:91], v[216:219]\n"
    "buffer_load_dwordx4 v248, s[8:11], s41 offen offset:1024 sc1 lds\n"
    "v_mfma_f32_16x16x32_bf16 v[208:211], a[188:191], a[56:59], v[208:211]\n"
    "ds_read2st64_b32 v[190:191], v250 offset0:50 offset1:51\n"
    "v_mfma_f32_16x16x32_bf16 v[208:211], a[184:187], a[52:55], v[208:211]\n"
    "v_exp_f32_e32 v233, v233\n"
    "v_mfma_f32_16x16x32_bf16 v[200:203], a[184:187], a[20:23], v[200:203]\n"
    "ds_read_b128 a[184:187], v243 offset:51200\n"
    "v_mfma_f32_16x16x32_bf16 v[200:203], a[188:191], a[24:27], v[200:203]\n"
    "s_waitcnt lgkmcnt(8)\n"
    "ds_read_b128 a[188:191], v242 offset:55296\n"
    "v_mfma_f32_16x16x32_bf16 v[204:207], a[160:163], a[28:31], 0\n"
    "buffer_load_dwordx4 v248, s[8:11], s41 offen offset:2048 sc1 lds\n"
    "v_mfma_f32_16x16x32_bf16 v[204:207], a[164:167], a[0:3], v[204:207]\n"
    "ds_read2st64_b32 v[196:197], v250 offset0:64 offset1:65\n"
    "v_mfma_f32_16x16x32_bf16 v[212:215], a[164:167], a[32:35], 0\n"
    "v_exp_f32_e32 v234, v234\n"
    "v_mfma_f32_16x16x32_bf16 v[212:215], a[160:163], a[60:63], v[212:215]\n"
    "ds_read2st64_b32 v[198:199], v250 offset0:66 offset1:67\n"
    "v_mfma_f32_16x16x32_bf16 v[220:223], a[160:163], a[92:95], 0\n"
    "v_exp_f32_e32 v235, v235\n"
    "v_mfma_f32_16x16x32_bf16 v[220:223], a[164:167], a[64:67], v[220:223]\n"
    "buffer_load_dwordx4 v248, s[8:11], s41 offen offset:3072 sc1 lds\n"
    "v_cvt_pk_bf16_f32 v232, v232, v233\n"
    "v_mfma_f32_16x16x32_bf16 v[228:231], a[164:167], a[96:99], 0\n"
    "s_add_u32 s41, s41, 0x8000\n"
    "v_exp_f32_e32 v224, v224\n"
    "v_mfma_f32_16x16x32_bf16 v[228:231], a[160:163], a[124:127], v[228:231]\n"
    "v_exp_f32_e32 v225, v225\n"
    "v_mfma_f32_16x16x32_bf16 v[236:239], a[160:163], a[156:159], 0\n"
    "ds_read_b64_tr_b16 a[160:161], v244 offset:32768\n"
    "v_cvt_pk_bf16_f32 v233, v234, v235\n"
    "v_mfma_f32_16x16x32_bf16 v[236:239], a[164:167], a[128:131], v[236:239]\n"
    "ds_read_b64_tr_b16 a[162:163], v244 offset:34816\n"
    "v_mfma_f32_16x16x32_bf16 v[236:239], a[168:171], a[132:135], v[236:239]\n"
    "ds_read_b64_tr_b16 a[164:165], v244 offset:40960\n"
    "v_mfma_f32_16x16x32_bf16 v[236:239], a[172:175], a[136:139], v[236:239]\n"
    "ds_read_b64_tr_b16 a[166:167], v244 offset:43008\n"
    "v_mfma_f32_16x16x32_bf16 v[228:231], a[172:175], a[104:107], v[228:231]\n"
    "v_exp_f32_e32 v226, v226\n"
    "v_mfma_f32_16x16x32_bf16 v[228:231], a[168:171], a[100:103], v[228:231]\n"
    "v_exp_f32_e32 v227, v227\n"
    "v_mfma_f32_16x16x32_bf16 v[220:223], a[168:171], a[68:71], v[220:223]\n"
    "v_cvt_pk_bf16_f32 v224, v224, v225\n"
    "v_mfma_f32_16x16x32_bf16 v[220:223], a[172:175], a[72:75], v[220:223]\n"
    "v_exp_f32_e32 v216, v216\n"
    "v_mfma_f32_16x16x32_bf16 v[212:215], a[172:175], a[40:43], v[212:215]\n"
    "v_exp_f32_e32 v217, v217\n"
    "v_mfma_f32_16x16x32_bf16 v[212:215], a[168:171], a[36:39], v[212:215]\n"
    "v_cvt_pk_bf16_f32 v225, v226, v227\n"
    "v_mfma_f32_16x16x32_bf16 v[204:207], a[168:171], a[4:7], v[204:207]\n"
    "ds_read_b64_tr_b16 a[168:169], v245 offset:32768\n"
    "v_mfma_f32_16x16x32_bf16 v[204:207], a[172:175], a[8:11], v[204:207]\n"
    "s_waitcnt lgkmcnt(7)\n"
    "ds_read_b64_tr_b16 a[170:171], v245 offset:34816\n"
    "v_mfma_f32_16x16x32_bf16 v[204:207], a[176:179], a[12:15], v[204:207]\n"
    "ds_read_b64_tr_b16 a[172:173], v245 offset:40960\n"
    "v_mfma_f32_16x16x32_bf16 v[204:207], a[180:183], a[16:19], v[204:207]\n"
    "ds_read_b64_tr_b16 a[174:175], v245 offset:43008\n"
    "v_mfma_f32_16x16x32_bf16 v[212:215], a[180:183], a[48:51], v[212:215]\n"
    "v_exp_f32_e32 v218, v218\n"
    "v_mfma_f32_16x16x32_bf16 v[212:215], a[176:179], a[44:47], v[212:215]\n"
    "v_exp_f32_e32 v219, v219\n"
    "v_mfma_f32_16x16x32_bf16 v[220:223], a[176:179], a[76:79], v[220:223]\n"
    "v_cvt_pk_bf16_f32 v216, v216, v217\n"
    "v_mfma_f32_16x16x32_bf16 v[220:223], a[180:183], a[80:83], v[220:223]\n"
    "v_exp_f32_e32 v208, v208\n"
    "v_mfma_f32_16x16x32_bf16 v[228:231], a[180:183], a[112:115], v[228:231]\n"
    "v_exp_f32_e32 v209, v209\n"
    "v_mfma_f32_16x16x32_bf16 v[228:231], a[176:179], a[108:111], v[228:231]\n"
    "v_cvt_pk_bf16_f32 v217, v218, v219\n"
    "v_mfma_f32_16x16x32_bf16 v[236:239], a[176:179], a[140:143], v[236:239]\n"
    "ds_read_b64_tr_b16 a[176:177], v246 offset:32768\n"
    "v_mfma_f32_16x16x32_bf16 v[236:239], a[180:183], a[144:147], v[236:239]\n"
    "ds_read_b64_tr_b16 a[178:179], v246 offset:34816\n"
    "v_mfma_f32_16x16x32_bf16 v[236:239], a[184:187], a[148:151], v[236:239]\n"
    "ds_read_b64_tr_b16 a[180:181], v246 offset:40960\n"
    "v_mfma_f32_16x16x32_bf16 v[236:239], a[188:191], a[152:155], v[236:239]\n"
    "ds_read_b64_tr_b16 a[182:183], v246 offset:43008\n"
    "v_mfma_f32_16x16x32_bf16 v[228:231], a[188:191], a[120:123], v[228:231]\n"
    "v_exp_f32_e32 v210, v210\n"
    "v_mfma_f32_16x16x32_bf16 v[228:231], a[184:187], a[116:119], v[228:231]\n"
    "v_exp_f32_e32 v211, v211\n"
    "v_mfma_f32_16x16x32_bf16 v[220:223], a[184:187], a[84:87], v[220:223]\n"
    "v_cvt_pk_bf16_f32 v208, v208, v209\n"
    "v_mfma_f32_16x16x32_bf16 v[220:223], a[188:191], a[88:91], v[220:223]\n"
    "v_exp_f32_e32 v200, v200\n"
    "v_mfma_f32_16x16x32_bf16 v[212:215], a[188:191], a[56:59], v[212:215]\n"
    "v_exp_f32_e32 v201, v201\n"
    "v_mfma_f32_16x16x32_bf16 v[212:215], a[184:187], a[52:55], v[212:215]\n"
    "v_cvt_pk_bf16_f32 v209, v210, v211\n"
    "v_mfma_f32_16x16x32_bf16 v[204:207], a[184:187], a[20:23], v[204:207]\n"
    "ds_read_b64_tr_b16 a[184:185], v247 offset:32768\n"
    "v_mfma_f32_16x16x32_bf16 v[204:207], a[188:191], a[24:27], v[204:207]\n"
    "s_waitcnt lgkmcnt(5)\n"
    "ds_read_b64_tr_b16 a[186:187], v247 offset:34816\n"
    "v_mfma_f32_16x16x32_bf16 v[0:3], a[160:163], v[160:163], v[0:3]\n"
    "ds_read_b64_tr_b16 a[188:189], v247 offset:40960\n"
    "v_mfma_f32_16x16x32_bf16 v[0:3], a[164:167], v[164:167], v[0:3]\n"
    "ds_read_b64_tr_b16 a[190:191], v247 offset:43008\n"
    "v_mfma_f32_16x16x32_bf16 a[248:251], a[164:167], v[172:175], a[248:251]\n"
    "v_exp_f32_e32 v202, v202\n"
    "v_mfma_f32_16x16x32_bf16 a[248:251], a[160:163], v[168:171], a[248:251]\n"
    "v_exp_f32_e32 v203, v203\n"
    "v_mfma_f32_16x16x32_bf16 v[56:59], a[160:163], v[176:179], v[56:59]\n"
    "v_cvt_pk_bf16_f32 v200, v200, v201\n"
    "v_mfma_f32_16x16x32_bf16 v[56:59], a[164:167], v[180:183], v[56:59]\n"
    "v_cvt_pk_bf16_f32 v201, v202, v203\n"
    "v_mfma_f32_16x16x32_bf16 v[60:63], a[164:167], v[188:191], v[60:63]\n"
    "v_exp_f32_e32 v236, v236\n"
    "v_mfma_f32_16x16x32_bf16 v[60:63], a[160:163], v[184:187], v[60:63]\n"
    "v_exp_f32_e32 v237, v237\n"
    "v_mfma_f32_16x16x32_bf16 v[64:67], a[160:163], v[192:195], v[64:67]\n"
    "ds_read_b64_tr_b16 a[160:161], v244 offset:36864\n"
    "v_mfma_f32_16x16x32_bf16 v[64:67], a[164:167], v[196:199], v[64:67]\n"
    "ds_read_b64_tr_b16 a[162:163], v244 offset:38912\n"
    "v_mfma_f32_16x16x32_bf16 v[76:79], a[168:171], v[192:195], v[76:79]\n"
    "ds_read_b64_tr_b16 a[164:165], v244 offset:45056\n"
    "v_mfma_f32_16x16x32_bf16 v[76:79], a[172:175], v[196:199], v[76:79]\n"
    "ds_read_b64_tr_b16 a[166:167], v244 offset:47104\n"
    "v_mfma_f32_16x16x32_bf16 v[72:75], a[172:175], v[188:191], v[72:75]\n"
    "v_exp_f32_e32 v238, v238\n"
    "v_mfma_f32_16x16x32_bf16 v[72:75], a[168:171], v[184:187], v[72:75]\n"
    "v_exp_f32_e32 v239, v239\n"
    "v_mfma_f32_16x16x32_bf16 v[68:71], a[168:171], v[176:179], v[68:71]\n"
    "v_cvt_pk_bf16_f32 v234, v236, v237\n"
    "v_mfma_f32_16x16x32_bf16 v[68:71], a[172:175], v[180:183], v[68:71]\n"
    "v_exp_f32_e32 v228, v228\n"
    "v_mfma_f32_16x16x32_bf16 a[252:255], a[172:175], v[172:175], a[252:255]\n"
    "v_exp_f32_e32 v229, v229\n"
    "v_mfma_f32_16x16x32_bf16 a[252:255], a[168:171], v[168:171], a[252:255]\n"
    "v_cvt_pk_bf16_f32 v235, v238, v239\n"
    "v_mfma_f32_16x16x32_bf16 v[4:7], a[168:171], v[160:163], v[4:7]\n"
    "ds_read_b64_tr_b16 a[168:169], v245 offset:36864\n"
    "v_mfma_f32_16x16x32_bf16 v[4:7], a[172:175], v[164:167], v[4:7]\n"
    "s_waitcnt lgkmcnt(5)\n"
    "ds_read_b64_tr_b16 a[170:171], v245 offset:38912\n"
    "v_mfma_f32_16x16x32_bf16 v[8:11], a[176:179], v[160:163], v[8:11]\n"
    "ds_read_b64_tr_b16 a[172:173], v245 offset:45056\n"
    "v_mfma_f32_16x16x32_bf16 v[8:11], a[180:183], v[164:167], v[8:11]\n"
    "ds_read_b64_tr_b16 a[174:175], v245 offset:47104\n"
    "v_mfma_f32_16x16x32_bf16 v[32:35], a[180:183], v[172:175], v[32:35]\n"
    "s_or_b32 s45, s44, s37\n"
    "s_cselect_b32 s46, 2.0, s81\n"
    "s_cselect_b32 s45, 2.0, s80\n"
    "v_mfma_f32_16x16x32_bf16 v[32:35], a[176:179], v[168:171], v[32:35]\n"
    "v_exp_f32_e32 v230, v230\n"
    "v_mfma_f32_16x16x32_bf16 v[80:83], a[176:179], v[176:179], v[80:83]\n"
    "v_exp_f32_e32 v231, v231\n"
    "v_mfma_f32_16x16x32_bf16 v[80:83], a[180:183], v[180:183], v[80:83]\n"
    "v_cvt_pk_bf16_f32 v226, v228, v229\n"
    "v_mfma_f32_16x16x32_bf16 v[84:87], a[180:183], v[188:191], v[84:87]\n"
    "v_exp_f32_e32 v220, v220\n"
    "v_mfma_f32_16x16x32_bf16 v[84:87], a[176:179], v[184:187], v[84:87]\n"
    "v_exp_f32_e32 v221, v221\n"
    "v_mfma_f32_16x16x32_bf16 v[88:91], a[176:179], v[192:195], v[88:91]\n"
    "ds_read_b64_tr_b16 a[176:177], v246 offset:36864\n"
    "v_mfma_f32_16x16x32_bf16 v[88:91], a[180:183], v[196:199], v[88:91]\n"
    "ds_read_b64_tr_b16 a[178:179], v246 offset:38912\n"
    "v_mfma_f32_16x16x32_bf16 v[100:103], a[184:187], v[192:195], v[100:103]\n"
    "ds_read_b64_tr_b16 a[180:181], v246 offset:45056\n"
    "v_mfma_f32_16x16x32_bf16 v[100:103], a[188:191], v[196:199], v[100:103]\n"
    "ds_read_b64_tr_b16 a[182:183], v246 offset:47104\n"
    "v_mfma_f32_16x16x32_bf16 v[96:99], a[188:191], v[188:191], v[96:99]\n"
    "v_cvt_pk_bf16_f32 v227, v230, v231\n"
    "v_mfma_f32_16x16x32_bf16 v[96:99], a[184:187], v[184:187], v[96:99]\n"
    "v_exp_f32_e32 v222, v222\n"
    "v_mfma_f32_16x16x32_bf16 v[92:95], a[184:187], v[176:179], v[92:95]\n"
    "v_exp_f32_e32 v223, v223\n"
    "v_mfma_f32_16x16x32_bf16 v[92:95], a[188:191], v[180:183], v[92:95]\n"
    "v_cvt_pk_bf16_f32 v218, v220, v221\n"
    "v_mfma_f32_16x16x32_bf16 v[36:39], a[188:191], v[172:175], v[36:39]\n"
    "v_exp_f32_e32 v212, v212\n"
    "v_mfma_f32_16x16x32_bf16 v[36:39], a[184:187], v[168:171], v[36:39]\n"
    "v_exp_f32_e32 v213, v213\n"
    "v_mfma_f32_16x16x32_bf16 v[12:15], a[184:187], v[160:163], v[12:15]\n"
    "s_mov_b32 m0, s49\n"
    "ds_read_b64_tr_b16 a[184:185], v247 offset:36864\n"
    "v_mfma_f32_16x16x32_bf16 v[12:15], a[188:191], v[164:167], v[12:15]\n"
    "s_waitcnt lgkmcnt(5)\n"
    "ds_read_b64_tr_b16 a[186:187], v247 offset:38912\n"
    "v_mfma_f32_16x16x32_bf16 v[16:19], a[160:163], v[160:163], v[16:19]\n"
    "ds_read_b64_tr_b16 a[188:189], v247 offset:45056\n"
    "v_mfma_f32_16x16x32_bf16 v[16:19], a[164:167], v[164:167], v[16:19]\n"
    "ds_read_b64_tr_b16 a[190:191], v247 offset:47104\n"
    "v_mfma_f32_16x16x32_bf16 v[40:43], a[164:167], v[172:175], v[40:43]\n"
    "buffer_load_dword v154, off, s[8:11], s45 sc1\n"
    "v_mfma_f32_16x16x32_bf16 v[40:43], a[160:163], v[168:171], v[40:43]\n"
    "v_cvt_pk_bf16_f32 v219, v222, v223\n"
    "v_mfma_f32_16x16x32_bf16 v[104:107], a[160:163], v[176:179], v[104:107]\n"
    "v_exp_f32_e32 v214, v214\n"
    "v_mfma_f32_16x16x32_bf16 v[104:107], a[164:167], v[180:183], v[104:107]\n"
    "s_waitcnt vmcnt(9)\n"
    "v_mfma_f32_16x16x32_bf16 v[108:111], a[164:167], v[188:191], v[108:111]\n"
    "s_barrier\n"
    "v_mfma_f32_16x16x32_bf16 v[108:111], a[160:163], v[184:187], v[108:111]\n"
    "v_exp_f32_e32 v215, v215\n"
    "v_mfma_f32_16x16x32_bf16 v[112:115], a[160:163], v[192:195], v[112:115]\n"
    "ds_read_b128 a[160:163], v243 offset:4096\n"
    "v_mfma_f32_16x16x32_bf16 v[112:115], a[164:167], v[196:199], v[112:115]\n"
    "ds_read_b128 a[164:167], v240 offset:0\n"
    "v_mfma_f32_16x16x32_bf16 v[124:127], a[168:171], v[192:195], v[124:127]\n"
    "ds_write_addtid_b32 v232 offset:16384\n"
    "v_cvt_pk_bf16_f32 v210, v212, v213\n"
    "v_mfma_f32_16x16x32_bf16 v[124:127], a[172:175], v[196:199], v[124:127]\n"
    "v_exp_f32_e32 v204, v204\n"
    "v_mfma_f32_16x16x32_bf16 v[120:123], a[172:175], v[188:191], v[120:123]\n"
    "v_exp_f32_e32 v205, v205\n"
    "v_mfma_f32_16x16x32_bf16 v[120:123], a[168:171], v[184:187], v[120:123]\n"
    "ds_write_addtid_b32 v233 offset:16640\n"
    "v_cvt_pk_bf16_f32 v211, v214, v215\n"
    "v_mfma_f32_16x16x32_bf16 v[116:119], a[168:171], v[176:179], v[116:119]\n"
    "v_exp_f32_e32 v206, v206\n"
    "v_mfma_f32_16x16x32_bf16 v[116:119], a[172:175], v[180:183], v[116:119]\n"
    "v_exp_f32_e32 v207, v207\n"
    "v_mfma_f32_16x16x32_bf16 v[44:47], a[172:175], v[172:175], v[44:47]\n"
    "ds_write_addtid_b32 v224 offset:12288\n"
    "v_cvt_pk_bf16_f32 v202, v204, v205\n"
    "v_mfma_f32_16x16x32_bf16 v[44:47], a[168:171], v[168:171], v[44:47]\n"
    "ds_write_addtid_b32 v225 offset:12544\n"
    "v_cvt_pk_bf16_f32 v203, v206, v207\n"
    "v_mfma_f32_16x16x32_bf16 v[20:23], a[168:171], v[160:163], v[20:23]\n"
    "ds_read_b128 a[168:171], v241 offset:0\n"
    "v_mfma_f32_16x16x32_bf16 v[20:23], a[172:175], v[164:167], v[20:23]\n"
    "s_waitcnt lgkmcnt(7)\n"
    "ds_read_b128 a[172:175], v240 offset:4096\n"
    "v_mfma_f32_16x16x32_bf16 v[24:27], a[176:179], v[160:163], v[24:27]\n"
    "ds_write_addtid_b32 v216 offset:8192\n"
    "v_mfma_f32_16x16x32_bf16 v[24:27], a[180:183], v[164:167], v[24:27]\n"
    "ds_write_addtid_b32 v217 offset:8448\n"
    "v_mfma_f32_16x16x32_bf16 v[48:51], a[180:183], v[172:175], v[48:51]\n"
    "ds_write_addtid_b32 v208 offset:4096\n"
    "v_mfma_f32_16x16x32_bf16 v[48:51], a[176:179], v[168:171], v[48:51]\n"
    "ds_write_addtid_b32 v209 offset:4352\n"
    "v_mfma_f32_16x16x32_bf16 v[128:131], a[176:179], v[176:179], v[128:131]\n"
    "ds_write_addtid_b32 v200 offset:0\n"
    "v_mfma_f32_16x16x32_bf16 v[128:131], a[180:183], v[180:183], v[128:131]\n"
    "ds_write_addtid_b32 v201 offset:256\n"
    "v_mfma_f32_16x16x32_bf16 v[132:135], a[180:183], v[188:191], v[132:135]\n"
    "ds_write_addtid_b32 v234 offset:16896\n"
    "v_mfma_f32_16x16x32_bf16 v[132:135], a[176:179], v[184:187], v[132:135]\n"
    "ds_write_addtid_b32 v235 offset:17152\n"
    "v_mfma_f32_16x16x32_bf16 v[136:139], a[176:179], v[192:195], v[136:139]\n"
    "ds_read_b128 a[176:179], v241 offset:4096\n"
    "v_mfma_f32_16x16x32_bf16 v[136:139], a[180:183], v[196:199], v[136:139]\n"
    "ds_read_b128 a[180:183], v242 offset:0\n"
    "v_mfma_f32_16x16x32_bf16 v[148:151], a[184:187], v[192:195], v[148:151]\n"
    "ds_write_addtid_b32 v226 offset:12800\n"
    "v_mfma_f32_16x16x32_bf16 v[148:151], a[188:191], v[196:199], v[148:151]\n"
    "ds_write_addtid_b32 v227 offset:13056\n"
    "v_mfma_f32_16x16x32_bf16 v[144:147], a[188:191], v[188:191], v[144:147]\n"
    "ds_write_addtid_b32 v218 offset:8704\n"
    "v_mfma_f32_16x16x32_bf16 v[144:147], a[184:187], v[184:187], v[144:147]\n"
    "ds_write_addtid_b32 v219 offset:8960\n"
    "v_mfma_f32_16x16x32_bf16 v[140:143], a[184:187], v[176:179], v[140:143]\n"
    "ds_write_addtid_b32 v210 offset:4608\n"
    "v_mfma_f32_16x16x32_bf16 v[140:143], a[188:191], v[180:183], v[140:143]\n"
    "ds_write_addtid_b32 v211 offset:4864\n"
    "v_mfma_f32_16x16x32_bf16 v[52:55], a[188:191], v[172:175], v[52:55]\n"
    "ds_write_addtid_b32 v202 offset:512\n"
    "v_mfma_f32_16x16x32_bf16 v[52:55], a[184:187], v[168:171], v[52:55]\n"
    "ds_write_addtid_b32 v203 offset:768\n"
    "v_mfma_f32_16x16x32_bf16 v[28:31], a[184:187], v[160:163], v[28:31]\n"
    "ds_read_b128 a[184:187], v243 offset:0\n"
    "v_mfma_f32_16x16x32_bf16 v[28:31], a[188:191], v[164:167], v[28:31]\n"
    "s_waitcnt lgkmcnt(15)\n"
    "ds_read_b128 a[188:191], v242 offset:4096\n"
    "v_mfma_f32_16x16x32_bf16 v[160:163], a[160:163], a[28:31], 0\n"
    "v_lshlrev_b32_e32 v253, 2, v249\n"
    "v_mfma_f32_16x16x32_bf16 v[160:163], a[164:167], a[0:3], v[160:163]\n"
    "v_mfma_f32_16x16x32_bf16 v[168:171], a[164:167], a[32:35], 0\n"
    "v_mfma_f32_16x16x32_bf16 v[168:171], a[160:163], a[60:63], v[168:171]\n"
    "v_mfma_f32_16x16x32_bf16 v[176:179], a[160:163], a[92:95], 0\n"
    "v_mfma_f32_16x16x32_bf16 v[176:179], a[164:167], a[64:67], v[176:179]\n"
    "s_waitcnt lgkmcnt(0)\n"
    "v_mfma_f32_16x16x32_bf16 v[184:187], a[164:167], a[96:99], 0\n"
    "s_barrier\n"
    "v_mfma_f32_16x16x32_bf16 v[184:187], a[160:163], a[124:127], v[184:187]\n"
    "s_add_u32 m0, s43, 32768\n"
    "ds_read2st64_b32 v[204:205], v250 offset0:0 offset1:1\n"
    "v_mfma_f32_16x16x32_bf16 v[192:195], a[160:163], a[156:159], 0\n"
    "ds_read_b128 a[160:163], v243 offset:6144\n"
    "v_mfma_f32_16x16x32_bf16 v[192:195], a[164:167], a[128:131], v[192:195]\n"
    "ds_read_b128 a[164:167], v240 offset:2048\n"
    "v_mfma_f32_16x16x32_bf16 v[192:195], a[168:171], a[132:135], v[192:195]\n"
    "buffer_load_dwordx4 v248, s[8:11], s40 offen offset:0 sc1 lds\n"
    "v_mfma_f32_16x16x32_bf16 v[192:195], a[172:175], a[136:139], v[192:195]\n"
    "v_mfma_f32_16x16x32_bf16 v[184:187], a[172:175], a[104:107], v[184:187]\n"
    "v_mfma_f32_16x16x32_bf16 v[184:187], a[168:171], a[100:103], v[184:187]\n"
    "ds_read2st64_b32 v[206:207], v250 offset0:2 offset1:3\n"
    "v_mfma_f32_16x16x32_bf16 v[176:179], a[168:171], a[68:71], v[176:179]\n"
    "v_mfma_f32_16x16x32_bf16 v[176:179], a[172:175], a[72:75], v[176:179]\n"
    "buffer_load_dwordx4 v248, s[8:11], s40 offen offset:1024 sc1 lds\n"
    "v_mfma_f32_16x16x32_bf16 v[168:171], a[172:175], a[40:43], v[168:171]\n"
    "v_mfma_f32_16x16x32_bf16 v[168:171], a[168:171], a[36:39], v[168:171]\n"
    "ds_read2st64_b32 v[212:213], v250 offset0:16 offset1:17\n"
    "v_mfma_f32_16x16x32_bf16 v[160:163], a[168:171], a[4:7], v[160:163]\n"
    "ds_read_b128 a[168:171], v241 offset:2048\n"
    "v_mfma_f32_16x16x32_bf16 v[160:163], a[172:175], a[8:11], v[160:163]\n"
    "ds_read_b128 a[172:175], v240 offset:6144\n"
    "v_mfma_f32_16x16x32_bf16 v[160:163], a[176:179], a[12:15], v[160:163]\n"
    "buffer_load_dwordx4 v248, s[8:11], s40 offen offset:2048 sc1 lds\n"
    "v_mfma_f32_16x16x32_bf16 v[160:163], a[180:183], a[16:19], v[160:163]\n"
    "ds_read2st64_b32 v[214:215], v250 offset0:18 offset1:19\n"
    "v_mfma_f32_16x16x32_bf16 v[168:171], a[180:183], a[48:51], v[168:171]\n"
    "v_mfma_f32_16x16x32_bf16 v[168:171], a[176:179], a[44:47], v[168:171]\n"
    "v_mfma_f32_16x16x32_bf16 v[176:179], a[176:179], a[76:79], v[176:179]\n"
    "ds_read2st64_b32 v[220:221], v250 offset0:32 offset1:33\n"
    "v_mfma_f32_16x16x32_bf16 v[176:179], a[180:183], a[80:83], v[176:179]\n"
    "buffer_load_dwordx4 v248, s[8:11], s40 offen offset:3072 sc1 lds\n"
    "v_mfma_f32_16x16x32_bf16 v[184:187], a[180:183], a[112:115], v[184:187]\n"
    "s_add_u32 m0, s43, 36864\n"
    "v_mfma_f32_16x16x32_bf16 v[184:187], a[176:179], a[108:111], v[184:187]\n"
    "s_add_u32 s40, s40, 0x8000\n"
    "ds_read2st64_b32 v[222:223], v250 offset0:34 offset1:35\n"
    "v_mfma_f32_16x16x32_bf16 v[192:195], a[176:179], a[140:143], v[192:195]\n"
    "ds_read_b128 a[176:179], v241 offset:6144\n"
    "v_mfma_f32_16x16x32_bf16 v[192:195], a[180:183], a[144:147], v[192:195]\n"
    "ds_read_b128 a[180:183], v242 offset:2048\n"
    "v_mfma_f32_16x16x32_bf16 v[192:195], a[184:187], a[148:151], v[192:195]\n"
    "buffer_load_dwordx4 v248, s[8:11], s41 offen offset:0 sc1 lds\n"
    "v_mfma_f32_16x16x32_bf16 v[192:195], a[188:191], a[152:155], v[192:195]\n"
    "v_mfma_f32_16x16x32_bf16 v[184:187], a[188:191], a[120:123], v[184:187]\n"
    "ds_read2st64_b32 v[228:229], v250 offset0:48 offset1:49\n"
    "v_mfma_f32_16x16x32_bf16 v[184:187], a[184:187], a[116:119], v[184:187]\n"
    "v_mfma_f32_16x16x32_bf16 v[176:179], a[184:187], a[84:87], v[176:179]\n"
    "v_exp_f32_e32 v192, v192\n"
    "v_mfma_f32_16x16x32_bf16 v[176:179], a[188:191], a[88:91], v[176:179]\n"
    "buffer_load_dwordx4 v248, s[8:11], s41 offen offset:1024 sc1 lds\n"
    "v_mfma_f32_16x16x32_bf16 v[168:171], a[188:191], a[56:59], v[168:171]\n"
    "ds_read2st64_b32 v[230:231], v250 offset0:50 offset1:51\n"
    "v_mfma_f32_16x16x32_bf16 v[168:171], a[184:187], a[52:55], v[168:171]\n"
    "v_exp_f32_e32 v193, v193\n"
    "v_mfma_f32_16x16x32_bf16 v[160:163], a[184:187], a[20:23], v[160:163]\n"
    "ds_read_b128 a[184:187], v243 offset:2048\n"
    "v_mfma_f32_16x16x32_bf16 v[160:163], a[188:191], a[24:27], v[160:163]\n"
    "s_waitcnt lgkmcnt(8)\n"
    "ds_read_b128 a[188:191], v242 offset:6144\n"
    "v_mfma_f32_16x16x32_bf16 v[164:167], a[160:163], a[28:31], 0\n"
    "buffer_load_dwordx4 v248, s[8:11], s41 offen offset:2048 sc1 lds\n"
    "v_mfma_f32_16x16x32_bf16 v[164:167], a[164:167], a[0:3], v[164:167]\n"
    "ds_read2st64_b32 v[236:237], v250 offset0:64 offset1:65\n"
    "v_mfma_f32_16x16x32_bf16 v[172:175], a[164:167], a[32:35], 0\n"
    "v_exp_f32_e32 v194, v194\n"
    "v_mfma_f32_16x16x32_bf16 v[172:175], a[160:163], a[60:63], v[172:175]\n"
    "ds_read2st64_b32 v[238:239], v250 offset0:66 offset1:67\n"
    "v_mfma_f32_16x16x32_bf16 v[180:183], a[160:163], a[92:95], 0\n"
    "v_exp_f32_e32 v195, v195\n"
    "v_mfma_f32_16x16x32_bf16 v[180:183], a[164:167], a[64:67], v[180:183]\n"
    "buffer_load_dwordx4 v248, s[8:11], s41 offen offset:3072 sc1 lds\n"
    "v_cvt_pk_bf16_f32 v192, v192, v193\n"
    "v_mfma_f32_16x16x32_bf16 v[188:191], a[164:167], a[96:99], 0\n"
    "s_add_u32 s41, s41, 0x8000\n"
    "v_exp_f32_e32 v184, v184\n"
    "v_mfma_f32_16x16x32_bf16 v[188:191], a[160:163], a[124:127], v[188:191]\n"
    "v_exp_f32_e32 v185, v185\n"
    "v_mfma_f32_16x16x32_bf16 v[196:199], a[160:163], a[156:159], 0\n"
    "ds_read_b64_tr_b16 a[160:161], v244 offset:49152\n"
    "v_mfma_f32_16x16x32_bf16 v[196:199], a[164:167], a[128:131], v[196:199]\n"
    "ds_read_b64_tr_b16 a[162:163], v244 offset:51200\n"
    "v_mfma_f32_16x16x32_bf16 v[196:199], a[168:171], a[132:135], v[196:199]\n"
    "ds_read_b64_tr_b16 a[164:165], v244 offset:57344\n"
    "v_mfma_f32_16x16x32_bf16 v[196:199], a[172:175], a[136:139], v[196:199]\n"
    "ds_read_b64_tr_b16 a[166:167], v244 offset:59392\n"
    "v_mfma_f32_16x16x32_bf16 v[188:191], a[172:175], a[104:107], v[188:191]\n"
    "v_cvt_pk_bf16_f32 v193, v194, v195\n"
    "v_mfma_f32_16x16x32_bf16 v[188:191], a[168:171], a[100:103], v[188:191]\n"
    "v_exp_f32_e32 v186, v186\n"
    "v_mfma_f32_16x16x32_bf16 v[180:183], a[168:171], a[68:71], v[180:183]\n"
    "v_exp_f32_e32 v187, v187\n"
    "v_mfma_f32_16x16x32_bf16 v[180:183], a[172:175], a[72:75], v[180:183]\n"
    "v_cvt_pk_bf16_f32 v184, v184, v185\n"
    "v_mfma_f32_16x16x32_bf16 v[172:175], a[172:175], a[40:43], v[172:175]\n"
    "v_exp_f32_e32 v176, v176\n"
    "v_mfma_f32_16x16x32_bf16 v[172:175], a[168:171], a[36:39], v[172:175]\n"
    "v_exp_f32_e32 v177, v177\n"
    "v_mfma_f32_16x16x32_bf16 v[164:167], a[168:171], a[4:7], v[164:167]\n"
    "ds_read_b64_tr_b16 a[168:169], v245 offset:49152\n"
    "v_mfma_f32_16x16x32_bf16 v[164:167], a[172:175], a[8:11], v[164:167]\n"
    "s_waitcnt lgkmcnt(7)\n"
    "ds_read_b64_tr_b16 a[170:171], v245 offset:51200\n"
    "v_mfma_f32_16x16x32_bf16 v[164:167], a[176:179], a[12:15], v[164:167]\n"
    "s_mov_b32 m0, s82\n"
    "ds_read_b64_tr_b16 a[172:173], v245 offset:57344\n"
    "v_mfma_f32_16x16x32_bf16 v[164:167], a[180:183], a[16:19], v[164:167]\n"
    "s_waitcnt vmcnt(8)\n"
    "ds_read_b64_tr_b16 a[174:175], v245 offset:59392\n"
    "v_mfma_f32_16x16x32_bf16 v[172:175], a[180:183], a[48:51], v[172:175]\n"
    "buffer_load_dwordx4 v253, s[8:11], s46 offen offset:0 sc1 lds\n"
    "v_mfma_f32_16x16x32_bf16 v[172:175], a[176:179], a[44:47], v[172:175]\n"
    "v_cvt_pk_bf16_f32 v185, v186, v187\n"
    "v_mfma_f32_16x16x32_bf16 v[180:183], a[176:179], a[76:79], v[180:183]\n"
    "v_exp_f32_e32 v178, v178\n"
    "v_mfma_f32_16x16x32_bf16 v[180:183], a[180:183], a[80:83], v[180:183]\n"
    "v_exp_f32_e32 v179, v179\n"
    "v_mfma_f32_16x16x32_bf16 v[188:191], a[180:183], a[112:115], v[188:191]\n"
    "v_cvt_pk_bf16_f32 v176, v176, v177\n"
    "v_mfma_f32_16x16x32_bf16 v[188:191], a[176:179], a[108:111], v[188:191]\n"
    "v_exp_f32_e32 v168, v168\n"
    "v_mfma_f32_16x16x32_bf16 v[196:199], a[176:179], a[140:143], v[196:199]\n"
    "ds_read_b64_tr_b16 a[176:177], v246 offset:49152\n"
    "v_mfma_f32_16x16x32_bf16 v[196:199], a[180:183], a[144:147], v[196:199]\n"
    "ds_read_b64_tr_b16 a[178:179], v246 offset:51200\n"
    "v_mfma_f32_16x16x32_bf16 v[196:199], a[184:187], a[148:151], v[196:199]\n"
    "ds_read_b64_tr_b16 a[180:181], v246 offset:57344\n"
    "v_mfma_f32_16x16x32_bf16 v[196:199], a[188:191], a[152:155], v[196:199]\n"
    "ds_read_b64_tr_b16 a[182:183], v246 offset:59392\n"
    "v_mfma_f32_16x16x32_bf16 v[188:191], a[188:191], a[120:123], v[188:191]\n"
    "v_exp_f32_e32 v169, v169\n"
    "v_mfma_f32_16x16x32_bf16 v[188:191], a[184:187], a[116:119], v[188:191]\n"
    "v_cvt_pk_bf16_f32 v177, v178, v179\n"
    "v_mfma_f32_16x16x32_bf16 v[180:183], a[184:187], a[84:87], v[180:183]\n"
    "v_exp_f32_e32 v170, v170\n"
    "v_mfma_f32_16x16x32_bf16 v[180:183], a[188:191], a[88:91], v[180:183]\n"
    "v_exp_f32_e32 v171, v171\n"
    "v_mfma_f32_16x16x32_bf16 v[172:175], a[188:191], a[56:59], v[172:175]\n"
    "v_cvt_pk_bf16_f32 v168, v168, v169\n"
    "v_mfma_f32_16x16x32_bf16 v[172:175], a[184:187], a[52:55], v[172:175]\n"
    "v_exp_f32_e32 v160, v160\n"
    "v_mfma_f32_16x16x32_bf16 v[164:167], a[184:187], a[20:23], v[164:167]\n"
    "ds_read_b64_tr_b16 a[184:185], v247 offset:49152\n"
    "v_mfma_f32_16x16x32_bf16 v[164:167], a[188:191], a[24:27], v[164:167]\n"
    "s_waitcnt lgkmcnt(5)\n"
    "ds_read_b64_tr_b16 a[186:187], v247 offset:51200\n"
    "v_mfma_f32_16x16x32_bf16 v[0:3], a[160:163], v[200:203], v[0:3]\n"
    "ds_read_b64_tr_b16 a[188:189], v247 offset:57344\n"
    "v_mfma_f32_16x16x32_bf16 v[0:3], a[164:167], v[204:207], v[0:3]\n"
    "ds_read_b64_tr_b16 a[190:191], v247 offset:59392\n"
    "v_mfma_f32_16x16x32_bf16 a[248:251], a[164:167], v[212:215], a[248:251]\n"
    "v_exp_f32_e32 v161, v161\n"
    "v_mfma_f32_16x16x32_bf16 a[248:251], a[160:163], v[208:211], a[248:251]\n"
    "v_cvt_pk_bf16_f32 v169, v170, v171\n"
    "v_mfma_f32_16x16x32_bf16 v[56:59], a[160:163], v[216:219], v[56:59]\n"
    "v_exp_f32_e32 v162, v162\n"
    "v_mfma_f32_16x16x32_bf16 v[56:59], a[164:167], v[220:223], v[56:59]\n"
    "v_exp_f32_e32 v163, v163\n"
    "v_mfma_f32_16x16x32_bf16 v[60:63], a[164:167], v[228:231], v[60:63]\n"
    "v_cvt_pk_bf16_f32 v160, v160, v161\n"
    "v_mfma_f32_16x16x32_bf16 v[60:63], a[160:163], v[224:227], v[60:63]\n"
    "v_cvt_pk_bf16_f32 v161, v162, v163\n"
    "v_mfma_f32_16x16x32_bf16 v[64:67], a[160:163], v[232:235], v[64:67]\n"
    "ds_read_b64_tr_b16 a[160:161], v244 offset:53248\n"
    "v_mfma_f32_16x16x32_bf16 v[64:67], a[164:167], v[236:239], v[64:67]\n"
    "ds_read_b64_tr_b16 a[162:163], v244 offset:55296\n"
    "v_mfma_f32_16x16x32_bf16 v[76:79], a[168:171], v[232:235], v[76:79]\n"
    "ds_read_b64_tr_b16 a[164:165], v244 offset:61440\n"
    "v_mfma_f32_16x16x32_bf16 v[76:79], a[172:175], v[236:239], v[76:79]\n"
    "ds_read_b64_tr_b16 a[166:167], v244 offset:63488\n"
    "v_mfma_f32_16x16x32_bf16 v[72:75], a[172:175], v[228:231], v[72:75]\n"
    "v_exp_f32_e32 v196, v196\n"
    "v_mfma_f32_16x16x32_bf16 v[72:75], a[168:171], v[224:227], v[72:75]\n"
    "v_exp_f32_e32 v197, v197\n"
    "v_mfma_f32_16x16x32_bf16 v[68:71], a[168:171], v[216:219], v[68:71]\n"
    "v_exp_f32_e32 v198, v198\n"
    "v_mfma_f32_16x16x32_bf16 v[68:71], a[172:175], v[220:223], v[68:71]\n"
    "v_exp_f32_e32 v199, v199\n"
    "v_mfma_f32_16x16x32_bf16 a[252:255], a[172:175], v[212:215], a[252:255]\n"
    "v_cvt_pk_bf16_f32 v194, v196, v197\n"
    "v_mfma_f32_16x16x32_bf16 a[252:255], a[168:171], v[208:211], a[252:255]\n"
    "v_exp_f32_e32 v188, v188\n"
    "v_mfma_f32_16x16x32_bf16 v[4:7], a[168:171], v[200:203], v[4:7]\n"
    "ds_read_b64_tr_b16 a[168:169], v245 offset:53248\n"
    "v_mfma_f32_16x16x32_bf16 v[4:7], a[172:175], v[204:207], v[4:7]\n"
    "s_waitcnt lgkmcnt(5)\n"
    "ds_read_b64_tr_b16 a[170:171], v245 offset:55296\n"
    "v_mfma_f32_16x16x32_bf16 v[8:11], a[176:179], v[200:203], v[8:11]\n"
    "ds_read_b64_tr_b16 a[172:173], v245 offset:61440\n"
    "v_mfma_f32_16x16x32_bf16 v[8:11], a[180:183], v[204:207], v[8:11]\n"
    "ds_read_b64_tr_b16 a[174:175], v245 offset:63488\n"
    "v_mfma_f32_16x16x32_bf16 v[32:35], a[180:183], v[212:215], v[32:35]\n"
    "v_exp_f32_e32 v189, v189\n"
    "v_mfma_f32_16x16x32_bf16 v[32:35], a[176:179], v[208:211], v[32:35]\n"
    "v_cvt_pk_bf16_f32 v195, v198, v199\n"
    "v_mfma_f32_16x16x32_bf16 v[80:83], a[176:179], v[216:219], v[80:83]\n"
    "v_exp_f32_e32 v190, v190\n"
    "v_mfma_f32_16x16x32_bf16 v[80:83], a[180:183], v[220:223], v[80:83]\n"
    "v_exp_f32_e32 v191, v191\n"
    "v_mfma_f32_16x16x32_bf16 v[84:87], a[180:183], v[228:231], v[84:87]\n"
    "v_cvt_pk_bf16_f32 v186, v188, v189\n"
    "v_mfma_f32_16x16x32_bf16 v[84:87], a[176:179], v[224:227], v[84:87]\n"
    "v_exp_f32_e32 v180, v180\n"
    "v_mfma_f32_16x16x32_bf16 v[88:91], a[176:179], v[232:235], v[88:91]\n"
    "ds_read_b64_tr_b16 a[176:177], v246 offset:53248\n"
    "v_mfma_f32_16x16x32_bf16 v[88:91], a[180:183], v[236:239], v[88:91]\n"
    "ds_read_b64_tr_b16 a[178:179], v246 offset:55296\n"
    "v_mfma_f32_16x16x32_bf16 v[100:103], a[184:187], v[232:235], v[100:103]\n"
    "ds_read_b64_tr_b16 a[180:181], v246 offset:61440\n"
    "v_mfma_f32_16x16x32_bf16 v[100:103], a[188:191], v[236:239], v[100:103]\n"
    "ds_read_b64_tr_b16 a[182:183], v246 offset:63488\n"
    "v_mfma_f32_16x16x32_bf16 v[96:99], a[188:191], v[228:231], v[96:99]\n"
    "v_exp_f32_e32 v181, v181\n"
    "v_mfma_f32_16x16x32_bf16 v[96:99], a[184:187], v[224:227], v[96:99]\n"
    "v_cvt_pk_bf16_f32 v187, v190, v191\n"
    "v_mfma_f32_16x16x32_bf16 v[92:95], a[184:187], v[216:219], v[92:95]\n"
    "v_exp_f32_e32 v182, v182\n"
    "v_mfma_f32_16x16x32_bf16 v[92:95], a[188:191], v[220:223], v[92:95]\n"
    "v_exp_f32_e32 v183, v183\n"
    "v_mfma_f32_16x16x32_bf16 v[36:39], a[188:191], v[212:215], v[36:39]\n"
    "v_cvt_pk_bf16_f32 v178, v180, v181\n"
    "v_mfma_f32_16x16x32_bf16 v[36:39], a[184:187], v[208:211], v[36:39]\n"
    "v_exp_f32_e32 v172, v172\n"
    "v_mfma_f32_16x16x32_bf16 v[12:15], a[184:187], v[200:203], v[12:15]\n"
    "s_mov_b32 m0, s49\n"
    "ds_read_b64_tr_b16 a[184:185], v247 offset:53248\n"
    "v_mfma_f32_16x16x32_bf16 v[12:15], a[188:191], v[204:207], v[12:15]\n"
    "s_waitcnt lgkmcnt(5)\n"
    "ds_read_b64_tr_b16 a[186:187], v247 offset:55296\n"
    "v_mfma_f32_16x16x32_bf16 v[16:19], a[160:163], v[200:203], v[16:19]\n"
    "ds_read_b64_tr_b16 a[188:189], v247 offset:61440\n"
    "v_mfma_f32_16x16x32_bf16 v[16:19], a[164:167], v[204:207], v[16:19]\n"
    "ds_read_b64_tr_b16 a[190:191], v247 offset:63488\n"
    "v_mfma_f32_16x16x32_bf16 v[40:43], a[164:167], v[212:215], v[40:43]\n"
    "v_exp_f32_e32 v173, v173\n"
    "v_mfma_f32_16x16x32_bf16 v[40:43], a[160:163], v[208:211], v[40:43]\n"
    "v_cvt_pk_bf16_f32 v179, v182, v183\n"
    "v_mfma_f32_16x16x32_bf16 v[104:107], a[160:163], v[216:219], v[104:107]\n"
    "v_exp_f32_e32 v174, v174\n"
    "v_mfma_f32_16x16x32_bf16 v[104:107], a[164:167], v[220:223], v[104:107]\n"
    "s_waitcnt vmcnt(9)\n"
    "v_mfma_f32_16x16x32_bf16 v[108:111], a[164:167], v[228:231], v[108:111]\n"
    "s_barrier\n"
    "v_mfma_f32_16x16x32_bf16 v[108:111], a[160:163], v[224:227], v[108:111]\n"
    "v_exp_f32_e32 v175, v175\n"
    "v_mfma_f32_16x16x32_bf16 v[112:115], a[160:163], v[232:235], v[112:115]\n"
    "ds_read_b128 a[160:163], v243 offset:20480\n"
    "v_mfma_f32_16x16x32_bf16 v[112:115], a[164:167], v[236:239], v[112:115]\n"
    "ds_read_b128 a[164:167], v240 offset:16384\n"
    "v_mfma_f32_16x16x32_bf16 v[124:127], a[168:171], v[232:235], v[124:127]\n"
    "ds_write_addtid_b32 v192 offset:16384\n"
    "v_cvt_pk_bf16_f32 v170, v172, v173\n"
    "v_mfma_f32_16x16x32_bf16 v[124:127], a[172:175], v[236:239], v[124:127]\n"
    "v_exp_f32_e32 v164, v164\n"
    "v_mfma_f32_16x16x32_bf16 v[120:123], a[172:175], v[228:231], v[120:123]\n"
    "v_exp_f32_e32 v165, v165\n"
    "v_mfma_f32_16x16x32_bf16 v[120:123], a[168:171], v[224:227], v[120:123]\n"
    "ds_write_addtid_b32 v193 offset:16640\n"
    "v_cvt_pk_bf16_f32 v171, v174, v175\n"
    "v_mfma_f32_16x16x32_bf16 v[116:119], a[168:171], v[216:219], v[116:119]\n"
    "v_exp_f32_e32 v166, v166\n"
    "v_mfma_f32_16x16x32_bf16 v[116:119], a[172:175], v[220:223], v[116:119]\n"
    "v_exp_f32_e32 v167, v167\n"
    "v_mfma_f32_16x16x32_bf16 v[44:47], a[172:175], v[212:215], v[44:47]\n"
    "ds_write_addtid_b32 v184 offset:12288\n"
    "v_cvt_pk_bf16_f32 v162, v164, v165\n"
    "v_mfma_f32_16x16x32_bf16 v[44:47], a[168:171], v[208:211], v[44:47]\n"
    "ds_write_addtid_b32 v185 offset:12544\n"
    "v_cvt_pk_bf16_f32 v163, v166, v167\n"
    "v_mfma_f32_16x16x32_bf16 v[20:23], a[168:171], v[200:203], v[20:23]\n"
    "ds_read_b128 a[168:171], v241 offset:16384\n"
    "v_mfma_f32_16x16x32_bf16 v[20:23], a[172:175], v[204:207], v[20:23]\n"
    "s_waitcnt lgkmcnt(7)\n"
    "ds_read_b128 a[172:175], v240 offset:20480\n"
    "v_mfma_f32_16x16x32_bf16 v[24:27], a[176:179], v[200:203], v[24:27]\n"
    "ds_write_addtid_b32 v176 offset:8192\n"
    "v_mfma_f32_16x16x32_bf16 v[24:27], a[180:183], v[204:207], v[24:27]\n"
    "ds_write_addtid_b32 v177 offset:8448\n"
    "v_mfma_f32_16x16x32_bf16 v[48:51], a[180:183], v[212:215], v[48:51]\n"
    "ds_write_addtid_b32 v168 offset:4096\n"
    "v_mfma_f32_16x16x32_bf16 v[48:51], a[176:179], v[208:211], v[48:51]\n"
    "ds_write_addtid_b32 v169 offset:4352\n"
    "v_mfma_f32_16x16x32_bf16 v[128:131], a[176:179], v[216:219], v[128:131]\n"
    "ds_write_addtid_b32 v160 offset:0\n"
    "v_mfma_f32_16x16x32_bf16 v[128:131], a[180:183], v[220:223], v[128:131]\n"
    "ds_write_addtid_b32 v161 offset:256\n"
    "v_mfma_f32_16x16x32_bf16 v[132:135], a[180:183], v[228:231], v[132:135]\n"
    "ds_write_addtid_b32 v194 offset:16896\n"
    "v_mfma_f32_16x16x32_bf16 v[132:135], a[176:179], v[224:227], v[132:135]\n"
    "ds_write_addtid_b32 v195 offset:17152\n"
    "v_mfma_f32_16x16x32_bf16 v[136:139], a[176:179], v[232:235], v[136:139]\n"
    "ds_read_b128 a[176:179], v241 offset:20480\n"
    "v_mfma_f32_16x16x32_bf16 v[136:139], a[180:183], v[236:239], v[136:139]\n"
    "ds_read_b128 a[180:183], v242 offset:16384\n"
    "v_mfma_f32_16x16x32_bf16 v[148:151], a[184:187], v[232:235], v[148:151]\n"
    "ds_write_addtid_b32 v186 offset:12800\n"
    "v_mfma_f32_16x16x32_bf16 v[148:151], a[188:191], v[236:239], v[148:151]\n"
    "ds_write_addtid_b32 v187 offset:13056\n"
    "v_mfma_f32_16x16x32_bf16 v[144:147], a[188:191], v[228:231], v[144:147]\n"
    "ds_write_addtid_b32 v178 offset:8704\n"
    "v_mfma_f32_16x16x32_bf16 v[144:147], a[184:187], v[224:227], v[144:147]\n"
    "ds_write_addtid_b32 v179 offset:8960\n"
    "v_mfma_f32_16x16x32_bf16 v[140:143], a[184:187], v[216:219], v[140:143]\n"
    "s_sub_u32 s44, s44, 1\n"
    "ds_write_addtid_b32 v170 offset:4608\n"
    "v_mfma_f32_16x16x32_bf16 v[140:143], a[188:191], v[220:223], v[140:143]\n"
    "ds_write_addtid_b32 v171 offset:4864\n"
    "v_mfma_f32_16x16x32_bf16 v[52:55], a[188:191], v[212:215], v[52:55]\n"
    "ds_write_addtid_b32 v162 offset:512\n"
    "v_mfma_f32_16x16x32_bf16 v[52:55], a[184:187], v[208:211], v[52:55]\n"
    "ds_write_addtid_b32 v163 offset:768\n"
    "v_mfma_f32_16x16x32_bf16 v[28:31], a[184:187], v[200:203], v[28:31]\n"
    "ds_read_b128 a[184:187], v243 offset:16384\n"
    "v_mfma_f32_16x16x32_bf16 v[28:31], a[188:191], v[204:207], v[28:31]\n"
    "s_waitcnt lgkmcnt(15)\n"
    "ds_read_b128 a[188:191], v242 offset:20480\n"
    "s_cbranch_scc0 .Lat_p\n"
    "s_cmp_lg_u32 s37, 0\n"
    "s_cbranch_scc1 .Lat_tail\n"
    "s_waitcnt vmcnt(0)\n"
    "v_cmp_ne_u32_e32 vcc, s39, v154\n"
    "s_mov_b32 s37, 1\n"
    "s_lshr_b32 s44, s34, 14\n"
    "s_add_u32 s44, s44, 3\n"
    "s_nop 1\n"
    "s_cbranch_vccnz .Lat_lpoll\n"
    ".p2align 4\n"
    ".Lat_g:\n"
    "v_mfma_f32_16x16x32_bf16 v[200:203], a[160:163], a[28:31], 0\n"
    "v_mfma_f32_16x16x32_bf16 v[200:203], a[164:167], a[0:3], v[200:203]\n"
    "v_mfma_f32_16x16x32_bf16 v[208:211], a[164:167], a[32:35], 0\n"
    "v_mfma_f32_16x16x32_bf16 v[208:211], a[160:163], a[60:63], v[208:211]\n"
    "v_mfma_f32_16x16x32_bf16 v[216:219], a[160:163], a[92:95], 0\n"
    "v_mfma_f32_16x16x32_bf16 v[216:219], a[164:167], a[64:67], v[216:219]\n"
    "s_waitcnt lgkmcnt(0)\n"
    "v_mfma_f32_16x16x32_bf16 v[224:227], a[164:167], a[96:99], 0\n"
    "s_barrier\n"
    "v_mfma_f32_16x16x32_bf16 v[224:227], a[160:163], a[124:127], v[224:227]\n"
    "s_add_u32 m0, s43, 49152\n"
    "ds_read2st64_b32 v[164:165], v250 offset0:0 offset1:1\n"
    "v_mfma_f32_16x16x32_bf16 v[232:235], a[160:163], a[156:159], 0\n"
    "ds_read_b128 a[160:163], v243 offset:22528\n"
    "v_mfma_f32_16x16x32_bf16 v[232:235], a[164:167], a[128:131], v[232:235]\n"
    "ds_read_b128 a[164:167], v240 offset:18432\n"
    "v_mfma_f32_16x16x32_bf16 v[232:235], a[168:171], a[132:135], v[232:235]\n"
    "buffer_load_dwordx4 v248, s[8:11], s40 offen offset:0 sc1 lds\n"
    "v_mfma_f32_16x16x32_bf16 v[232:235], a[172:175], a[136:139], v[232:235]\n"
    "v_mfma_f32_16x16x32_bf16 v[224:227], a[172:175], a[104:107], v[224:227]\n"
    "v_mfma_f32_16x16x32_bf16 v[224:227], a[168:171], a[100:103], v[224:227]\n"
    "ds_read2st64_b32 v[166:167], v250 offset0:2 offset1:3\n"
    "v_mfma_f32_16x16x32_bf16 v[216:219], a[168:171], a[68:71], v[216:219]\n"
    "buffer_load_dwordx4 v248, s[8:11], s40 offen offset:1024 sc1 lds\n"
    "v_mfma_f32_16x16x32_bf16 v[216:219], a[172:175], a[72:75], v[216:219]\n"
    "v_mfma_f32_16x16x32_bf16 v[208:211], a[172:175], a[40:43], v[208:211]\n"
    "v_mfma_f32_16x16x32_bf16 v[208:211], a[168:171], a[36:39], v[208:211]\n"
    "ds_read2st64_b32 v[172:173], v250 offset0:16 offset1:17\n"
    "v_mfma_f32_16x16x32_bf16 v[200:203], a[168:171], a[4:7], v[200:203]\n"
    "ds_read_b128 a[168:171], v241 offset:18432\n"
    "buffer_load_dwordx4 v248, s[8:11], s40 offen offset:2048 sc1 lds\n"
    "v_mfma_f32_16x16x32_bf16 v[200:203], a[172:175], a[8:11], v[200:203]\n"
    "ds_read_b128 a[172:175], v240 offset:22528\n"
    "v_mfma_f32_16x16x32_bf16 v[200:203], a[176:179], a[12:15], v[200:203]\n"
    "ds_read2st64_b32 v[174:175], v250 offset0:18 offset1:19\n"
    "v_mfma_f32_16x16x32_bf16 v[200:203], a[180:183], a[16:19], v[200:203]\n"
    "v_mfma_f32_16x16x32_bf16 v[208:211], a[180:183], a[48:51], v[208:211]\n"
    "buffer_load_dwordx4 v248, s[8:11], s40 offen offset:3072 sc1 lds\n"
    "v_mfma_f32_16x16x32_bf16 v[208:211], a[176:179], a[44:47], v[208:211]\n"
    "s_add_u32 m0, s43, 53248\n"
    "v_mfma_f32_16x16x32_bf16 v[216:219], a[176:179], a[76:79], v[216:219]\n"
    "s_add_u32 s40, s40, 0x8000\n"
    "ds_read2st64_b32 v[180:181], v250 offset0:32 offset1:33\n"
    "v_mfma_f32_16x16x32_bf16 v[216:219], a[180:183], a[80:83], v[216:219]\n"
    "v_mfma_f32_16x16x32_bf16 v[224:227], a[180:183], a[112:115], v[224:227]\n"
    "buffer_load_dwordx4 v248, s[8:11], s41 offen offset:0 sc1 lds\n"
    "v_mfma_f32_16x16x32_bf16 v[224:227], a[176:179], a[108:111], v[224:227]\n"
    "ds_read2st64_b32 v[182:183], v250 offset0:34 offset1:35\n"
    "v_mfma_f32_16x16x32_bf16 v[232:235], a[176:179], a[140:143], v[232:235]\n"
    "ds_read_b128 a[176:179], v241 offset:22528\n"
    "v_mfma_f32_16x16x32_bf16 v[232:235], a[180:183], a[144:147], v[232:235]\n"
    "ds_read_b128 a[180:183], v242 offset:18432\n"
    "v_mfma_f32_16x16x32_bf16 v[232:235], a[184:187], a[148:151], v[232:235]\n"
    "buffer_load_dwordx4 v248, s[8:11], s41 offen offset:1024 sc1 lds\n"
    "v_mfma_f32_16x16x32_bf16 v[232:235], a[188:191], a[152:155], v[232:235]\n"
    "v_mfma_f32_16x16x32_bf16 v[224:227], a[188:191], a[120:123], v[224:227]\n"
    "ds_read2st64_b32 v[188:189], v250 offset0:48 offset1:49\n"
    "v_mfma_f32_16x16x32_bf16 v[224:227], a[184:187], a[116:119], v[224:227]\n"
    "v_mfma_f32_16x16x32_bf16 v[216:219], a[184:187], a[84:87], v[216:219]\n"
    "buffer_load_dwordx4 v248, s[8:11], s41 offen offset:2048 sc1 lds\n"
    "v_mfma_f32_16x16x32_bf16 v[216:219], a[188:191], a[88:91], v[216:219]\n"
    "ds_read2st64_b32 v[190:191], v250 offset0:50 offset1:51\n"
    "v_mfma_f32_16x16x32_bf16 v[208:211], a[188:191], a[56:59], v[208:211]\n"
    "ds_read_b64 v[254:255], v252 offset:0\n"
    "v_mfma_f32_16x16x32_bf16 v[208:211], a[184:187], a[52:55], v[208:211]\n"
    "ds_read2st64_b32 v[196:197], v250 offset0:64 offset1:65\n"
    "v_mfma_f32_16x16x32_bf16 v[200:203], a[184:187], a[20:23], v[200:203]\n"
    "ds_read_b128 a[184:187], v243 offset:18432\n"
    "buffer_load_dwordx4 v248, s[8:11], s41 offen offset:3072 sc1 lds\n"
    "v_mfma_f32_16x16x32_bf16 v[200:203], a[188:191], a[24:27], v[200:203]\n"
    "s_waitcnt lgkmcnt(15)\n"
    "s_add_u32 s41, s41, 0x8000\n"
    "ds_read_b128 a[188:191], v242 offset:22528\n"
    "v_mfma_f32_16x16x32_bf16 v[204:207], a[160:163], a[28:31], 0\n"
    "s_waitcnt lgkmcnt(3)\n"
    "v_mad_u32_u16 v253, v254, s42, v251 op_sel:[0,0,0,0]\n"
    "v_mfma_f32_16x16x32_bf16 v[204:207], a[164:167], a[0:3], v[204:207]\n"
    "buffer_load_dwordx4 a[200:203], v253, s[20:23], 0 offen offset:0 sc1\n"
    "v_mfma_f32_16x16x32_bf16 v[212:215], a[164:167], a[32:35], 0\n"
    "v_exp_f32_e32 v232, v232\n"
    "v_mfma_f32_16x16x32_bf16 v[212:215], a[160:163], a[60:63], v[212:215]\n"
    "ds_read2st64_b32 v[198:199], v250 offset0:66 offset1:67\n"
    "v_mfma_f32_16x16x32_bf16 v[220:223], a[160:163], a[92:95], 0\n"
    "v_mad_u32_u16 v253, v254, s42, v251 op_sel:[1,0,0,0]\n"
    "v_mfma_f32_16x16x32_bf16 v[220:223], a[164:167], a[64:67], v[220:223]\n"
    "buffer_load_dwordx4 a[204:207], v253, s[20:23], 0 offen offset:0 sc1\n"
    "v_mfma_f32_16x16x32_bf16 v[228:231], a[164:167], a[96:99], 0\n"
    "v_exp_f32_e32 v233, v233\n"
    "v_mfma_f32_16x16x32_bf16 v[228:231], a[160:163], a[124:127], v[228:231]\n"
    "v_exp_f32_e32 v234, v234\n"
    "v_mfma_f32_16x16x32_bf16 v[236:239], a[160:163], a[156:159], 0\n"
    "ds_read_b64_tr_b16 a[160:161], v244 offset:0\n"
    "v_mad_u32_u16 v253, v255, s42, v251 op_sel:[0,0,0,0]\n"
    "v_mfma_f32_16x16x32_bf16 v[236:239], a[164:167], a[128:131], v[236:239]\n"
    "ds_read_b64_tr_b16 a[162:163], v244 offset:2048\n"
    "buffer_load_dwordx4 a[208:211], v253, s[20:23], 0 offen offset:0 sc1\n"
    "v_mfma_f32_16x16x32_bf16 v[236:239], a[168:171], a[132:135], v[236:239]\n"
    "ds_read_b64_tr_b16 a[164:165], v244 offset:8192\n"
    "v_mfma_f32_16x16x32_bf16 v[236:239], a[172:175], a[136:139], v[236:239]\n"
    "ds_read_b64_tr_b16 a[166:167], v244 offset:10240\n"
    "v_mfma_f32_16x16x32_bf16 v[228:231], a[172:175], a[104:107], v[228:231]\n"
    "v_mad_u32_u16 v253, v255, s42, v251 op_sel:[1,0,0,0]\n"
    "v_mfma_f32_16x16x32_bf16 v[228:231], a[168:171], a[100:103], v[228:231]\n"
    "buffer_load_dwordx4 a[212:215], v253, s[20:23], 0 offen offset:0 sc1\n"
    "ds_read_b64 v[254:255], v252 offset:8\n"
    "v_mfma_f32_16x16x32_bf16 v[220:223], a[168:171], a[68:71], v[220:223]\n"
    "v_exp_f32_e32 v235, v235\n"
    "v_mfma_f32_16x16x32_bf16 v[220:223], a[172:175], a[72:75], v[220:223]\n"
    "v_cvt_pk_bf16_f32 v232, v232, v233\n"
    "v_mfma_f32_16x16x32_bf16 v[212:215], a[172:175], a[40:43], v[212:215]\n"
    "v_exp_f32_e32 v224, v224\n"
    "v_mfma_f32_16x16x32_bf16 v[212:215], a[168:171], a[36:39], v[212:215]\n"
    "v_exp_f32_e32 v225, v225\n"
    "v_mfma_f32_16x16x32_bf16 v[204:207], a[168:171], a[4:7], v[204:207]\n"
    "ds_read_b64_tr_b16 a[168:169], v245 offset:0\n"
    "v_cvt_pk_bf16_f32 v233, v234, v235\n"
    "v_mfma_f32_16x16x32_bf16 v[204:207], a[172:175], a[8:11], v[204:207]\n"
    "ds_read_b64_tr_b16 a[170:171], v245 offset:2048\n"
    "v_mfma_f32_16x16x32_bf16 v[204:207], a[176:179], a[12:15], v[204:207]\n"
    "s_waitcnt lgkmcnt(2)\n"
    "ds_read_b64_tr_b16 a[172:173], v245 offset:8192\n"
    "v_mad_u32_u16 v253, v254, s42, v251 op_sel:[0,0,0,0]\n"
    "v_mfma_f32_16x16x32_bf16 v[204:207], a[180:183], a[16:19], v[204:207]\n"
    "ds_read_b64_tr_b16 a[174:175], v245 offset:10240\n"
    "buffer_load_dwordx4 a[216:219], v253, s[20:23], 0 offen offset:0 sc1\n"
    "v_mfma_f32_16x16x32_bf16 v[212:215], a[180:183], a[48:51], v[212:215]\n"
    "v_exp_f32_e32 v226, v226\n"
    "v_mfma_f32_16x16x32_bf16 v[212:215], a[176:179], a[44:47], v[212:215]\n"
    "v_exp_f32_e32 v227, v227\n"
    "v_mfma_f32_16x16x32_bf16 v[220:223], a[176:179], a[76:79], v[220:223]\n"
    "v_mad_u32_u16 v253, v254, s42, v251 op_sel:[1,0,0,0]\n"
    "v_cvt_pk_bf16_f32 v224, v224, v225\n"
    "v_mfma_f32_16x16x32_bf16 v[220:223], a[180:183], a[80:83], v[220:223]\n"
    "buffer_load_dwordx4 a[220:223], v253, s[20:23], 0 offen offset:0 sc1\n"
    "v_mfma_f32_16x16x32_bf16 v[228:231], a[180:183], a[112:115], v[228:231]\n"
    "v_exp_f32_e32 v216, v216\n"
    "v_mfma_f32_16x16x32_bf16 v[228:231], a[176:179], a[108:111], v[228:231]\n"
    "v_exp_f32_e32 v217, v217\n"
    "v_mfma_f32_16x16x32_bf16 v[236:239], a[176:179], a[140:143], v[236:239]\n"
    "ds_read_b64_tr_b16 a[176:177], v246 offset:0\n"
    "v_cvt_pk_bf16_f32 v225, v226, v227\n"
    "v_mfma_f32_16x16x32_bf16 v[236:239], a[180:183], a[144:147], v[236:239]\n"
    "ds_read_b64_tr_b16 a[178:179], v246 offset:2048\n"
    "v_mfma_f32_16x16x32_bf16 v[236:239], a[184:187], a[148:151], v[236:239]\n"
    "ds_read_b64_tr_b16 a[180:181], v246 offset:8192\n"
    "v_mfma_f32_16x16x32_bf16 v[236:239], a[188:191], a[152:155], v[236:239]\n"
    "ds_read_b64_tr_b16 a[182:183], v246 offset:10240\n"
    "v_mfma_f32_16x16x32_bf16 v[228:231], a[188:191], a[120:123], v[228:231]\n"
    "ds_read_b64 v[254:255], v252 offset:32\n"
    "v_mfma_f32_16x16x32_bf16 v[228:231], a[184:187], a[116:119], v[228:231]\n"
    "v_exp_f32_e32 v218, v218\n"
    "v_mfma_f32_16x16x32_bf16 v[220:223], a[184:187], a[84:87], v[220:223]\n"
    "v_exp_f32_e32 v219, v219\n"
    "v_mfma_f32_16x16x32_bf16 v[220:223], a[188:191], a[88:91], v[220:223]\n"
    "v_cvt_pk_bf16_f32 v216, v216, v217\n"
    "v_mfma_f32_16x16x32_bf16 v[212:215], a[188:191], a[56:59], v[212:215]\n"
    "s_waitcnt lgkmcnt(0)\n"
    "v_mad_u32_u16 v253, v254, s42, v251 op_sel:[0,0,0,0]\n"
    "v_mfma_f32_16x16x32_bf16 v[212:215], a[184:187], a[52:55], v[212:215]\n"
    "buffer_load_dwordx4 a[224:227], v253, s[20:23], 0 offen offset:0 sc1\n"
    "v_mfma_f32_16x16x32_bf16 v[204:207], a[184:187], a[20:23], v[204:207]\n"
    "ds_read_b64_tr_b16 a[184:185], v247 offset:0\n"
    "v_mfma_f32_16x16x32_bf16 v[204:207], a[188:191], a[24:27], v[204:207]\n"
    "ds_read_b64_tr_b16 a[186:187], v247 offset:2048\n"
    "v_mfma_f32_16x16x32_bf16 v[0:3], a[160:163], v[160:163], v[0:3]\n"
    "ds_read_b64_tr_b16 a[188:189], v247 offset:8192\n"
    "v_mad_u32_u16 v253, v254, s42, v251 op_sel:[1,0,0,0]\n"
    "v_mfma_f32_16x16x32_bf16 v[0:3], a[164:167], v[164:167], v[0:3]\n"
    "ds_read_b64_tr_b16 a[190:191], v247 offset:10240\n"
    "buffer_load_dwordx4 a[228:231], v253, s[20:23], 0 offen offset:0 sc1\n"
    "v_mfma_f32_16x16x32_bf16 a[248:251], a[164:167], v[172:175], a[248:251]\n"
    "v_exp_f32_e32 v208, v208\n"
    "v_mfma_f32_16x16x32_bf16 a[248:251], a[160:163], v[168:171], a[248:251]\n"
    "v_exp_f32_e32 v209, v209\n"
    "v_mfma_f32_16x16x32_bf16 v[56:59], a[160:163], v[176:179], v[56:59]\n"
    "v_mad_u32_u16 v253, v255, s42, v251 op_sel:[0,0,0,0]\n"
    "v_cvt_pk_bf16_f32 v217, v218, v219\n"
    "v_mfma_f32_16x16x32_bf16 v[56:59], a[164:167], v[180:183], v[56:59]\n"
    "buffer_load_dwordx4 a[232:235], v253, s[20:23], 0 offen offset:0 sc1\n"
    "v_mfma_f32_16x16x32_bf16 v[60:63], a[164:167], v[188:191], v[60:63]\n"
    "v_exp_f32_e32 v210, v210\n"
    "v_mfma_f32_16x16x32_bf16 v[60:63], a[160:163], v[184:187], v[60:63]\n"
    "v_exp_f32_e32 v211, v211\n"
    "v_mfma_f32_16x16x32_bf16 v[64:67], a[160:163], v[192:195], v[64:67]\n"
    "ds_read_b64_tr_b16 a[160:161], v244 offset:4096\n"
    "v_mad_u32_u16 v253, v255, s42, v251 op_sel:[1,0,0,0]\n"
    "v_mfma_f32_16x16x32_bf16 v[64:67], a[164:167], v[196:199], v[64:67]\n"
    "ds_read_b64_tr_b16 a[162:163], v244 offset:6144\n"
    "buffer_load_dwordx4 a[236:239], v253, s[20:23], 0 offen offset:0 sc1\n"
    "v_mfma_f32_16x16x32_bf16 v[76:79], a[168:171], v[192:195], v[76:79]\n"
    "ds_read_b64_tr_b16 a[164:165], v244 offset:12288\n"
    "v_cvt_pk_bf16_f32 v208, v208, v209\n"
    "v_mfma_f32_16x16x32_bf16 v[76:79], a[172:175], v[196:199], v[76:79]\n"
    "ds_read_b64_tr_b16 a[166:167], v244 offset:14336\n"
    "v_mfma_f32_16x16x32_bf16 v[72:75], a[172:175], v[188:191], v[72:75]\n"
    "ds_read_b64 v[254:255], v252 offset:40\n"
    "v_mfma_f32_16x16x32_bf16 v[72:75], a[168:171], v[184:187], v[72:75]\n"
    "v_exp_f32_e32 v200, v200\n"
    "v_mfma_f32_16x16x32_bf16 v[68:71], a[168:171], v[176:179], v[68:71]\n"
    "v_exp_f32_e32 v201, v201\n"
    "v_mfma_f32_16x16x32_bf16 v[68:71], a[172:175], v[180:183], v[68:71]\n"
    "v_cvt_pk_bf16_f32 v209, v210, v211\n"
    "v_mfma_f32_16x16x32_bf16 a[252:255], a[172:175], v[172:175], a[252:255]\n"
    "v_exp_f32_e32 v202, v202\n"
    "v_mfma_f32_16x16x32_bf16 a[252:255], a[168:171], v[168:171], a[252:255]\n"
    "v_exp_f32_e32 v203, v203\n"
    "v_mfma_f32_16x16x32_bf16 v[4:7], a[168:171], v[160:163], v[4:7]\n"
    "s_waitcnt lgkmcnt(0)\n"
    "ds_read_b64_tr_b16 a[168:169], v245 offset:4096\n"
    "v_mad_u32_u16 v253, v254, s42, v251 op_sel:[0,0,0,0]\n"
    "v_mfma_f32_16x16x32_bf16 v[4:7], a[172:175], v[164:167], v[4:7]\n"
    "ds_read_b64_tr_b16 a[170:171], v245 offset:6144\n"
    "v_cvt_pk_bf16_f32 v200, v200, v201\n"
    "v_mfma_f32_16x16x32_bf16 v[8:11], a[176:179], v[160:163], v[8:11]\n"
    "ds_read_b64_tr_b16 a[172:173], v245 offset:12288\n"
    "buffer_load_dwordx4 a[240:243], v253, s[20:23], 0 offen offset:0 sc1\n"
    "v_mfma_f32_16x16x32_bf16 v[8:11], a[180:183], v[164:167], v[8:11]\n"
    "ds_read_b64_tr_b16 a[174:175], v245 offset:14336\n"
    "v_cvt_pk_bf16_f32 v201, v202, v203\n"
    "v_mfma_f32_16x16x32_bf16 v[32:35], a[180:183], v[172:175], v[32:35]\n"
    "v_exp_f32_e32 v236, v236\n"
    "v_mfma_f32_16x16x32_bf16 v[32:35], a[176:179], v[168:171], v[32:35]\n"
    "v_mad_u32_u16 v253, v254, s42, v251 op_sel:[1,0,0,0]\n"
    "v_mfma_f32_16x16x32_bf16 v[80:83], a[176:179], v[176:179], v[80:83]\n"
    "buffer_load_dwordx4 a[244:247], v253, s[20:23], 0 offen offset:0 sc1\n"
    "v_mfma_f32_16x16x32_bf16 v[80:83], a[180:183], v[180:183], v[80:83]\n"
    "v_exp_f32_e32 v237, v237\n"
    "v_mfma_f32_16x16x32_bf16 v[84:87], a[180:183], v[188:191], v[84:87]\n"
    "v_exp_f32_e32 v238, v238\n"
    "v_mfma_f32_16x16x32_bf16 v[84:87], a[176:179], v[184:187], v[84:87]\n"
    "v_exp_f32_e32 v239, v239\n"
    "v_mfma_f32_16x16x32_bf16 v[88:91], a[176:179], v[192:195], v[88:91]\n"
    "ds_read_b64_tr_b16 a[176:177], v246 offset:4096\n"
    "v_cvt_pk_bf16_f32 v234, v236, v237\n"
    "v_mfma_f32_16x16x32_bf16 v[88:91], a[180:183], v[196:199], v[88:91]\n"
    "ds_read_b64_tr_b16 a[178:179], v246 offset:6144\n"
    "v_exp_f32_e32 v228, v228\n"
    "v_mfma_f32_16x16x32_bf16 v[100:103], a[184:187], v[192:195], v[100:103]\n"
    "ds_read_b64_tr_b16 a[180:181], v246 offset:12288\n"
    "v_exp_f32_e32 v229, v229\n"
    "v_mfma_f32_16x16x32_bf16 v[100:103], a[188:191], v[196:199], v[100:103]\n"
    "ds_read_b64_tr_b16 a[182:183], v246 offset:14336\n"
    "v_cvt_pk_bf16_f32 v235, v238, v239\n"
    "v_mfma_f32_16x16x32_bf16 v[96:99], a[188:191], v[188:191], v[96:99]\n"
    "v_exp_f32_e32 v230, v230\n"
    "v_mfma_f32_16x16x32_bf16 v[96:99], a[184:187], v[184:187], v[96:99]\n"
    "v_exp_f32_e32 v231, v231\n"
    "v_cvt_pk_bf16_f32 v226, v228, v229\n"
    "v_mfma_f32_16x16x32_bf16 v[92:95], a[184:187], v[176:179], v[92:95]\n"
    "v_exp_f32_e32 v220, v220\n"
    "v_mfma_f32_16x16x32_bf16 v[92:95], a[188:191], v[180:183], v[92:95]\n"
    "v_exp_f32_e32 v221, v221\n"
    "v_mfma_f32_16x16x32_bf16 v[36:39], a[188:191], v[172:175], v[36:39]\n"
    "v_cvt_pk_bf16_f32 v227, v230, v231\n"
    "v_mfma_f32_16x16x32_bf16 v[36:39], a[184:187], v[168:171], v[36:39]\n"
    "v_exp_f32_e32 v222, v222\n"
    "v_mfma_f32_16x16x32_bf16 v[12:15], a[184:187], v[160:163], v[12:15]\n"
    "s_mov_b32 m0, s49\n"
    "ds_read_b64_tr_b16 a[184:185], v247 offset:4096\n"
    "v_mfma_f32_16x16x32_bf16 v[12:15], a[188:191], v[164:167], v[12:15]\n"
    "s_waitcnt lgkmcnt(5)\n"
    "ds_read_b64_tr_b16 a[186:187], v247 offset:6144\n"
    "v_mfma_f32_16x16x32_bf16 v[16:19], a[160:163], v[160:163], v[16:19]\n"
    "ds_read_b64_tr_b16 a[188:189], v247 offset:12288\n"
    "v_mfma_f32_16x16x32_bf16 v[16:19], a[164:167], v[164:167], v[16:19]\n"
    "ds_read_b64_tr_b16 a[190:191], v247 offset:14336\n"
    "v_mfma_f32_16x16x32_bf16 v[40:43], a[164:167], v[172:175], v[40:43]\n"
    "v_exp_f32_e32 v223, v223\n"
    "v_mfma_f32_16x16x32_bf16 v[40:43], a[160:163], v[168:171], v[40:43]\n"
    "v_cvt_pk_bf16_f32 v218, v220, v221\n"
    "v_mfma_f32_16x16x32_bf16 v[104:107], a[160:163], v[176:179], v[104:107]\n"
    "v_exp_f32_e32 v212, v212\n"
    "v_mfma_f32_16x16x32_bf16 v[104:107], a[164:167], v[180:183], v[104:107]\n"
    "s_waitcnt vmcnt(20)\n"
    "v_mfma_f32_16x16x32_bf16 v[108:111], a[164:167], v[188:191], v[108:111]\n"
    "s_barrier\n"
    "v_mfma_f32_16x16x32_bf16 v[108:111], a[160:163], v[184:187], v[108:111]\n"
    "v_exp_f32_e32 v213, v213\n"
    "v_mfma_f32_16x16x32_bf16 v[112:115], a[160:163], v[192:195], v[112:115]\n"
    "ds_read_b128 a[160:163], v243 offset:36864\n"
    "v_cvt_pk_bf16_f32 v219, v222, v223\n"
    "v_mfma_f32_16x16x32_bf16 v[112:115], a[164:167], v[196:199], v[112:115]\n"
    "ds_read_b128 a[164:167], v240 offset:32768\n"
    "ds_write_addtid_b32 v232 offset:16384\n"
    "v_mfma_f32_16x16x32_bf16 v[124:127], a[168:171], v[192:195], v[124:127]\n"
    "v_exp_f32_e32 v214, v214\n"
    "v_mfma_f32_16x16x32_bf16 v[124:127], a[172:175], v[196:199], v[124:127]\n"
    "v_exp_f32_e32 v215, v215\n"
    "v_mfma_f32_16x16x32_bf16 v[120:123], a[172:175], v[188:191], v[120:123]\n"
    "ds_write_addtid_b32 v233 offset:16640\n"
    "v_cvt_pk_bf16_f32 v210, v212, v213\n"
    "v_mfma_f32_16x16x32_bf16 v[120:123], a[168:171], v[184:187], v[120:123]\n"
    "v_exp_f32_e32 v204, v204\n"
    "v_mfma_f32_16x16x32_bf16 v[116:119], a[168:171], v[176:179], v[116:119]\n"
    "v_exp_f32_e32 v205, v205\n"
    "v_mfma_f32_16x16x32_bf16 v[116:119], a[172:175], v[180:183], v[116:119]\n"
    "ds_write_addtid_b32 v224 offset:12288\n"
    "v_cvt_pk_bf16_f32 v211, v214, v215\n"
    "v_mfma_f32_16x16x32_bf16 v[44:47], a[172:175], v[172:175], v[44:47]\n"
    "v_exp_f32_e32 v206, v206\n"
    "v_mfma_f32_16x16x32_bf16 v[44:47], a[168:171], v[168:171], v[44:47]\n"
    "v_exp_f32_e32 v207, v207\n"
    "v_mfma_f32_16x16x32_bf16 v[20:23], a[168:171], v[160:163], v[20:23]\n"
    "ds_read_b128 a[168:171], v241 offset:32768\n"
    "v_cvt_pk_bf16_f32 v202, v204, v205\n"
    "v_mfma_f32_16x16x32_bf16 v[20:23], a[172:175], v[164:167], v[20:23]\n"
    "s_waitcnt lgkmcnt(6)\n"
    "ds_read_b128 a[172:175], v240 offset:36864\n"
    "v_cvt_pk_bf16_f32 v203, v206, v207\n"
    "v_mfma_f32_16x16x32_bf16 v[24:27], a[176:179], v[160:163], v[24:27]\n"
    "ds_write_addtid_b32 v225 offset:12544\n"
    "ds_write_addtid_b32 v216 offset:8192\n"
    "v_mfma_f32_16x16x32_bf16 v[24:27], a[180:183], v[164:167], v[24:27]\n"
    "ds_write_addtid_b32 v217 offset:8448\n"
    "v_mfma_f32_16x16x32_bf16 v[48:51], a[180:183], v[172:175], v[48:51]\n"
    "ds_write_addtid_b32 v208 offset:4096\n"
    "v_mfma_f32_16x16x32_bf16 v[48:51], a[176:179], v[168:171], v[48:51]\n"
    "ds_write_addtid_b32 v209 offset:4352\n"
    "v_mfma_f32_16x16x32_bf16 v[128:131], a[176:179], v[176:179], v[128:131]\n"
    "ds_write_addtid_b32 v200 offset:0\n"
    "v_mfma_f32_16x16x32_bf16 v[128:131], a[180:183], v[180:183], v[128:131]\n"
    "ds_write_addtid_b32 v201 offset:256\n"
    "v_mfma_f32_16x16x32_bf16 v[132:135], a[180:183], v[188:191], v[132:135]\n"
    "ds_write_addtid_b32 v234 offset:16896\n"
    "v_mfma_f32_16x16x32_bf16 v[132:135], a[176:179], v[184:187], v[132:135]\n"
    "ds_write_addtid_b32 v235 offset:17152\n"
    "v_mfma_f32_16x16x32_bf16 v[136:139], a[176:179], v[192:195], v[136:139]\n"
    "ds_read_b128 a[176:179], v241 offset:36864\n"
    "v_mfma_f32_16x16x32_bf16 v[136:139], a[180:183], v[196:199], v[136:139]\n"
    "ds_read_b128 a[180:183], v242 offset:32768\n"
    "v_mfma_f32_16x16x32_bf16 v[148:151], a[184:187], v[192:195], v[148:151]\n"
    "ds_write_addtid_b32 v226 offset:12800\n"
    "v_mfma_f32_16x16x32_bf16 v[148:151], a[188:191], v[196:199], v[148:151]\n"
    "ds_write_addtid_b32 v227 offset:13056\n"
    "v_mfma_f32_16x16x32_bf16 v[144:147], a[188:191], v[188:191], v[144:147]\n"
    "ds_write_addtid_b32 v218 offset:8704\n"
    "v_mfma_f32_16x16x32_bf16 v[144:147], a[184:187], v[184:187], v[144:147]\n"
    "ds_write_addtid_b32 v219 offset:8960\n"
    "v_mfma_f32_16x16x32_bf16 v[140:143], a[184:187], v[176:179], v[140:143]\n"
    "ds_write_addtid_b32 v210 offset:4608\n"
    "v_mfma_f32_16x16x32_bf16 v[140:143], a[188:191], v[180:183], v[140:143]\n"
    "ds_write_addtid_b32 v211 offset:4864\n"
    "v_mfma_f32_16x16x32_bf16 v[52:55], a[188:191], v[172:175], v[52:55]\n"
    "ds_write_addtid_b32 v202 offset:512\n"
    "v_mfma_f32_16x16x32_bf16 v[52:55], a[184:187], v[168:171], v[52:55]\n"
    "ds_write_addtid_b32 v203 offset:768\n"
    "v_mfma_f32_16x16x32_bf16 v[28:31], a[184:187], v[160:163], v[28:31]\n"
    "ds_read_b128 a[184:187], v243 offset:32768\n"
    "v_mfma_f32_16x16x32_bf16 v[28:31], a[188:191], v[164:167], v[28:31]\n"
    "s_waitcnt lgkmcnt(15)\n"
    "ds_read_b128 a[188:191], v242 offset:36864\n"
    "v_mfma_f32_16x16x32_bf16 v[160:163], a[160:163], a[28:31], 0\n"
    "v_mfma_f32_16x16x32_bf16 v[160:163], a[164:167], a[0:3], v[160:163]\n"
    "v_mfma_f32_16x16x32_bf16 v[168:171], a[164:167], a[32:35], 0\n"
    "v_mfma_f32_16x16x32_bf16 v[168:171], a[160:163], a[60:63], v[168:171]\n"
    "v_mfma_f32_16x16x32_bf16 v[176:179], a[160:163], a[92:95], 0\n"
    "v_mfma_f32_16x16x32_bf16 v[176:179], a[164:167], a[64:67], v[176:179]\n"
    "s_waitcnt lgkmcnt(0)\n"
    "v_mfma_f32_16x16x32_bf16 v[184:187], a[164:167], a[96:99], 0\n"
    "s_barrier\n"
    "v_mfma_f32_16x16x32_bf16 v[184:187], a[160:163], a[124:127], v[184:187]\n"
    "s_add_u32 m0, s43, 0\n"
    "v_mfma_f32_16x16x32_bf16 v[192:195], a[160:163], a[156:159], 0\n"
    "ds_read_b128 a[160:163], v243 offset:38912\n"
    "v_mfma_f32_16x16x32_bf16 v[192:195], a[164:167], a[128:131], v[192:195]\n"
    "ds_read_b128 a[164:167], v240 offset:34816\n"
    "v_mfma_f32_16x16x32_bf16 v[192:195], a[168:171], a[132:135], v[192:195]\n"
    "buffer_load_dwordx4 v248, s[8:11], s40 offen offset:0 sc1 lds\n"
    "v_mfma_f32_16x16x32_bf16 v[192:195], a[172:175], a[136:139], v[192:195]\n"
    "ds_read2st64_b32 v[204:205], v250 offset0:0 offset1:1\n"
    "v_mfma_f32_16x16x32_bf16 v[184:187], a[172:175], a[104:107], v[184:187]\n"
    "s_waitcnt vmcnt(11)\n"
    "v_smfmac_f32_16x16x64_bf16 v[156:159], a[192:195], a[200:207], v152\n"
    "v_mfma_f32_16x16x32_bf16 v[184:187], a[168:171], a[100:103], v[184:187]\n"
    "ds_read2st64_b32 v[206:207], v250 offset0:2 offset1:3\n"
    "v_mfma_f32_16x16x32_bf16 v[176:179], a[168:171], a[68:71], v[176:179]\n"
    "buffer_load_dwordx4 v248, s[8:11], s40 offen offset:1024 sc1 lds\n"
    "v_mfma_f32_16x16x32_bf16 v[176:179], a[172:175], a[72:75], v[176:179]\n"
    "v_mfma_f32_16x16x32_bf16 v[168:171], a[172:175], a[40:43], v[168:171]\n"
    "s_waitcnt vmcnt(10)\n"
    "v_smfmac_f32_16x16x64_bf16 v[156:159], a[192:195], a[208:215], v152\n"
    "ds_read2st64_b32 v[212:213], v250 offset0:16 offset1:17\n"
    "v_mfma_f32_16x16x32_bf16 v[168:171], a[168:171], a[36:39], v[168:171]\n"
    "v_mfma_f32_16x16x32_bf16 v[160:163], a[168:171], a[4:7], v[160:163]\n"
    "ds_read_b128 a[168:171], v241 offset:34816\n"
    "buffer_load_dwordx4 v248, s[8:11], s40 offen offset:2048 sc1 lds\n"
    "v_mfma_f32_16x16x32_bf16 v[160:163], a[172:175], a[8:11], v[160:163]\n"
    "ds_read_b128 a[172:175], v240 offset:38912\n"
    "v_mfma_f32_16x16x32_bf16 v[160:163], a[176:179], a[12:15], v[160:163]\n"
    "s_waitcnt vmcnt(9)\n"
    "ds_read2st64_b32 v[214:215], v250 offset0:18 offset1:19\n"
    "v_smfmac_f32_16x16x64_bf16 v[156:159], a[192:195], a[216:223], v152\n"
    "v_mfma_f32_16x16x32_bf16 v[160:163], a[180:183], a[16:19], v[160:163]\n"
    "v_mfma_f32_16x16x32_bf16 v[168:171], a[180:183], a[48:51], v[168:171]\n"
    "buffer_load_dwordx4 v248, s[8:11], s40 offen offset:3072 sc1 lds\n"
    "v_mfma_f32_16x16x32_bf16 v[168:171], a[176:179], a[44:47], v[168:171]\n"
    "s_add_u32 m0, s43, 4096\n"
    "v_mfma_f32_16x16x32_bf16 v[176:179], a[176:179], a[76:79], v[176:179]\n"
    "s_add_u32 s40, s40, 0x8000\n"
    "ds_read2st64_b32 v[220:221], v250 offset0:32 offset1:33\n"
    "v_mfma_f32_16x16x32_bf16 v[176:179], a[180:183], a[80:83], v[176:179]\n"
    "v_mfma_f32_16x16x32_bf16 v[184:187], a[180:183], a[112:115], v[184:187]\n"
    "v_mfma_f32_16x16x32_bf16 v[184:187], a[176:179], a[108:111], v[184:187]\n"
    "buffer_load_dwordx4 v248, s[8:11], s41 offen offset:0 sc1 lds\n"
    "v_mfma_f32_16x16x32_bf16 v[192:195], a[176:179], a[140:143], v[192:195]\n"
    "ds_read_b128 a[176:179], v241 offset:38912\n"
    "v_mfma_f32_16x16x32_bf16 v[192:195], a[180:183], a[144:147], v[192:195]\n"
    "ds_read_b128 a[180:183], v242 offset:34816\n"
    "v_mfma_f32_16x16x32_bf16 v[192:195], a[184:187], a[148:151], v[192:195]\n"
    "ds_read2st64_b32 v[222:223], v250 offset0:34 offset1:35\n"
    "v_mfma_f32_16x16x32_bf16 v[192:195], a[188:191], a[152:155], v[192:195]\n"
    "v_mfma_f32_16x16x32_bf16 v[184:187], a[188:191], a[120:123], v[184:187]\n"
    "buffer_load_dwordx4 v248, s[8:11], s41 offen offset:1024 sc1 lds\n"
    "v_mfma_f32_16x16x32_bf16 v[184:187], a[184:187], a[116:119], v[184:187]\n"
    "ds_read2st64_b32 v[228:229], v250 offset0:48 offset1:49\n"
    "v_mfma_f32_16x16x32_bf16 v[176:179], a[184:187], a[84:87], v[176:179]\n"
    "s_waitcnt vmcnt(10)\n"
    "v_exp_f32_e32 v192, v192\n"
    "v_smfmac_f32_16x16x64_bf16 v[156:159], a[192:195], a[224:231], v152\n"
    "v_exp_f32_e32 v193, v193\n"
    "v_mfma_f32_16x16x32_bf16 v[176:179], a[188:191], a[88:91], v[176:179]\n"
    "v_exp_f32_e32 v194, v194\n"
    "v_mfma_f32_16x16x32_bf16 v[168:171], a[188:191], a[56:59], v[168:171]\n"
    "buffer_load_dwordx4 v248, s[8:11], s41 offen offset:2048 sc1 lds\n"
    "v_mfma_f32_16x16x32_bf16 v[168:171], a[184:187], a[52:55], v[168:171]\n"
    "ds_read2st64_b32 v[230:231], v250 offset0:50 offset1:51\n"
    "v_mfma_f32_16x16x32_bf16 v[160:163], a[184:187], a[20:23], v[160:163]\n"
    "s_waitcnt vmcnt(9)\n"
    "ds_read_b128 a[184:187], v243 offset:34816\n"
    "v_smfmac_f32_16x16x64_bf16 v[156:159], a[192:195], a[232:239], v152\n"
    "ds_read2st64_b32 v[236:237], v250 offset0:64 offset1:65\n"
    "v_mfma_f32_16x16x32_bf16 v[160:163], a[188:191], a[24:27], v[160:163]\n"
    "s_waitcnt lgkmcnt(9)\n"
    "ds_read_b128 a[188:191], v242 offset:38912\n"
    "v_mfma_f32_16x16x32_bf16 v[164:167], a[160:163], a[28:31], 0\n"
    "buffer_load_dwordx4 v248, s[8:11], s41 offen offset:3072 sc1 lds\n"
    "v_mfma_f32_16x16x32_bf16 v[164:167], a[164:167], a[0:3], v[164:167]\n"
    "s_add_u32 s41, s41, 0x8000\n"
    "v_exp_f32_e32 v195, v195\n"
    "v_mfma_f32_16x16x32_bf16 v[172:175], a[164:167], a[32:35], 0\n"
    "s_waitcnt vmcnt(8)\n"
    "v_cvt_pk_bf16_f32 v192, v192, v193\n"
    "v_smfmac_f32_16x16x64_bf16 v[156:159], a[192:195], a[240:247], v152\n"
    "v_exp_f32_e32 v184, v184\n"
    "v_mfma_f32_16x16x32_bf16 v[172:175], a[160:163], a[60:63], v[172:175]\n"
    "ds_read2st64_b32 v[238:239], v250 offset0:66 offset1:67\n"
    "v_mfma_f32_16x16x32_bf16 v[180:183], a[160:163], a[92:95], 0\n"
    "v_exp_f32_e32 v185, v185\n"
    "v_mfma_f32_16x16x32_bf16 v[180:183], a[164:167], a[64:67], v[180:183]\n"
    "v_cvt_pk_bf16_f32 v193, v194, v195\n"
    "v_mfma_f32_16x16x32_bf16 v[188:191], a[164:167], a[96:99], 0\n"
    "v_exp_f32_e32 v186, v186\n"
    "v_mfma_f32_16x16x32_bf16 v[188:191], a[160:163], a[124:127], v[188:191]\n"
    "v_exp_f32_e32 v187, v187\n"
    "v_mfma_f32_16x16x32_bf16 v[196:199], a[160:163], a[156:159], 0\n"
    "ds_read_b64_tr_b16 a[160:161], v244 offset:16384\n"
    "v_and_b32_e32 v253, 0xffff0000, v255\n"
    "v_mfma_f32_16x16x32_bf16 v[196:199], a[164:167], a[128:131], v[196:199]\n"
    "ds_read_b64_tr_b16 a[162:163], v244 offset:18432\n"
    "v_mfma_f32_16x16x32_bf16 v[196:199], a[168:171], a[132:135], v[196:199]\n"
    "ds_read_b64_tr_b16 a[164:165], v244 offset:24576\n"
    "v_mfma_f32_16x16x32_bf16 v[196:199], a[172:175], a[136:139], v[196:199]\n"
    "ds_read_b64_tr_b16 a[166:167], v244 offset:26624\n"
    "v_mfma_f32_16x16x32_bf16 v[188:191], a[172:175], a[104:107], v[188:191]\n"
    "v_cvt_pk_bf16_f32 v184, v184, v185\n"
    "v_mfma_f32_16x16x32_bf16 v[188:191], a[168:171], a[100:103], v[188:191]\n"
    "v_exp_f32_e32 v176, v176\n"
    "v_mfma_f32_16x16x32_bf16 v[180:183], a[168:171], a[68:71], v[180:183]\n"
    "v_exp_f32_e32 v177, v177\n"
    "v_mfma_f32_16x16x32_bf16 v[180:183], a[172:175], a[72:75], v[180:183]\n"
    "v_cvt_pk_bf16_f32 v185, v186, v187\n"
    "v_mul_f32_e32 v156, v253, v156\n"
    "v_mfma_f32_16x16x32_bf16 v[172:175], a[172:175], a[40:43], v[172:175]\n"
    "v_exp_f32_e32 v178, v178\n"
    "v_mfma_f32_16x16x32_bf16 v[172:175], a[168:171], a[36:39], v[172:175]\n"
    "v_exp_f32_e32 v179, v179\n"
    "v_mfma_f32_16x16x32_bf16 v[164:167], a[168:171], a[4:7], v[164:167]\n"
    "ds_read_b64_tr_b16 a[168:169], v245 offset:16384\n"
    "v_mfma_f32_16x16x32_bf16 v[164:167], a[172:175], a[8:11], v[164:167]\n"
    "s_waitcnt lgkmcnt(6)\n"
    "ds_read_b64_tr_b16 a[170:171], v245 offset:18432\n"
    "v_mfma_f32_16x16x32_bf16 v[164:167], a[176:179], a[12:15], v[164:167]\n"
    "ds_read_b64_tr_b16 a[172:173], v245 offset:24576\n"
    "v_mfma_f32_16x16x32_bf16 v[164:167], a[180:183], a[16:19], v[164:167]\n"
    "ds_read_b64_tr_b16 a[174:175], v245 offset:26624\n"
    "v_mfma_f32_16x16x32_bf16 v[172:175], a[180:183], a[48:51], v[172:175]\n"
    "v_cvt_pk_bf16_f32 v176, v176, v177\n"
    "v_mfma_f32_16x16x32_bf16 v[172:175], a[176:179], a[44:47], v[172:175]\n"
    "v_exp_f32_e32 v168, v168\n"
    "v_mfma_f32_16x16x32_bf16 v[180:183], a[176:179], a[76:79], v[180:183]\n"
    "v_mul_f32_e32 v157, v253, v157\n"
    "v_mfma_f32_16x16x32_bf16 v[180:183], a[180:183], a[80:83], v[180:183]\n"
    "v_exp_f32_e32 v169, v169\n"
    "v_mfma_f32_16x16x32_bf16 v[188:191], a[180:183], a[112:115], v[188:191]\n"
    "v_cvt_pk_bf16_f32 v177, v178, v179\n"
    "v_mfma_f32_16x16x32_bf16 v[188:191], a[176:179], a[108:111], v[188:191]\n"
    "v_exp_f32_e32 v170, v170\n"
    "v_mfma_f32_16x16x32_bf16 v[196:199], a[176:179], a[140:143], v[196:199]\n"
    "ds_read_b64_tr_b16 a[176:177], v246 offset:16384\n"
    "v_mul_f32_e32 v158, v253, v158\n"
    "v_mfma_f32_16x16x32_bf16 v[196:199], a[180:183], a[144:147], v[196:199]\n"
    "ds_read_b64_tr_b16 a[178:179], v246 offset:18432\n"
    "v_mfma_f32_16x16x32_bf16 v[196:199], a[184:187], a[148:151], v[196:199]\n"
    "ds_read_b64_tr_b16 a[180:181], v246 offset:24576\n"
    "v_mfma_f32_16x16x32_bf16 v[196:199], a[188:191], a[152:155], v[196:199]\n"
    "ds_read_b64_tr_b16 a[182:183], v246 offset:26624\n"
    "v_mfma_f32_16x16x32_bf16 v[188:191], a[188:191], a[120:123], v[188:191]\n"
    "v_exp_f32_e32 v171, v171\n"
    "v_mfma_f32_16x16x32_bf16 v[188:191], a[184:187], a[116:119], v[188:191]\n"
    "v_cvt_pk_bf16_f32 v168, v168, v169\n"
    "v_mul_f32_e32 v159, v253, v159\n"
    "v_mfma_f32_16x16x32_bf16 v[180:183], a[184:187], a[84:87], v[180:183]\n"
    "v_exp_f32_e32 v160, v160\n"
    "v_mfma_f32_16x16x32_bf16 v[180:183], a[188:191], a[88:91], v[180:183]\n"
    "v_exp_f32_e32 v161, v161\n"
    "v_mfma_f32_16x16x32_bf16 v[172:175], a[188:191], a[56:59], v[172:175]\n"
    "v_cvt_pk_bf16_f32 v169, v170, v171\n"
    "v_mfma_f32_16x16x32_bf16 v[172:175], a[184:187], a[52:55], v[172:175]\n"
    "v_exp_f32_e32 v162, v162\n"
    "v_mfma_f32_16x16x32_bf16 v[164:167], a[184:187], a[20:23], v[164:167]\n"
    "ds_read_b64_tr_b16 a[184:185], v247 offset:16384\n"
    "v_mfma_f32_16x16x32_bf16 v[164:167], a[188:191], a[24:27], v[164:167]\n"
    "s_waitcnt lgkmcnt(5)\n"
    "ds_read_b64_tr_b16 a[186:187], v247 offset:18432\n"
    "v_mfma_f32_16x16x32_bf16 v[0:3], a[160:163], v[200:203], v[0:3]\n"
    "ds_read_b64_tr_b16 a[188:189], v247 offset:24576\n"
    "v_mad_u32_u16 v253, v255, s42, v153 op_sel:[0,0,0,0]\n"
    "v_mfma_f32_16x16x32_bf16 v[0:3], a[164:167], v[204:207], v[0:3]\n"
    "ds_read_b64_tr_b16 a[190:191], v247 offset:26624\n"
    "v_mfma_f32_16x16x32_bf16 a[248:251], a[164:167], v[212:215], a[248:251]\n"
    "v_exp_f32_e32 v163, v163\n"
    "v_mfma_f32_16x16x32_bf16 a[248:251], a[160:163], v[208:211], a[248:251]\n"
    "v_cvt_pk_bf16_f32 v160, v160, v161\n"
    "v_mfma_f32_16x16x32_bf16 v[56:59], a[160:163], v[216:219], v[56:59]\n"
    "v_cvt_pk_bf16_f32 v154, v156, v157\n"
    "v_mfma_f32_16x16x32_bf16 v[56:59], a[164:167], v[220:223], v[56:59]\n"
    "v_cvt_pk_bf16_f32 v161, v162, v163\n"
    "v_mfma_f32_16x16x32_bf16 v[60:63], a[164:167], v[228:231], v[60:63]\n"
    "v_exp_f32_e32 v196, v196\n"
    "v_mfma_f32_16x16x32_bf16 v[60:63], a[160:163], v[224:227], v[60:63]\n"
    "v_exp_f32_e32 v197, v197\n"
    "v_mfma_f32_16x16x32_bf16 v[64:67], a[160:163], v[232:235], v[64:67]\n"
    "ds_read_b64_tr_b16 a[160:161], v244 offset:20480\n"
    "v_mfma_f32_16x16x32_bf16 v[64:67], a[164:167], v[236:239], v[64:67]\n"
    "ds_read_b64_tr_b16 a[162:163], v244 offset:22528\n"
    "v_mfma_f32_16x16x32_bf16 v[76:79], a[168:171], v[232:235], v[76:79]\n"
    "ds_read_b64_tr_b16 a[164:165], v244 offset:28672\n"
    "v_mfma_f32_16x16x32_bf16 v[76:79], a[172:175], v[236:239], v[76:79]\n"
    "ds_read_b64_tr_b16 a[166:167], v244 offset:30720\n"
    "v_mfma_f32_16x16x32_bf16 v[72:75], a[172:175], v[228:231], v[72:75]\n"
    "v_exp_f32_e32 v198, v198\n"
    "v_mfma_f32_16x16x32_bf16 v[72:75], a[168:171], v[224:227], v[72:75]\n"
    "v_exp_f32_e32 v199, v199\n"
    "v_mfma_f32_16x16x32_bf16 v[68:71], a[168:171], v[216:219], v[68:71]\n"
    "v_cvt_pk_bf16_f32 v194, v196, v197\n"
    "v_cvt_pk_bf16_f32 v155, v158, v159\n"
    "v_mfma_f32_16x16x32_bf16 v[68:71], a[172:175], v[220:223], v[68:71]\n"
    "v_exp_f32_e32 v188, v188\n"
    "v_mfma_f32_16x16x32_bf16 a[252:255], a[172:175], v[212:215], a[252:255]\n"
    "v_exp_f32_e32 v189, v189\n"
    "v_mfma_f32_16x16x32_bf16 a[252:255], a[168:171], v[208:211], a[252:255]\n"
    "v_cvt_pk_bf16_f32 v195, v198, v199\n"
    "v_mfma_f32_16x16x32_bf16 v[4:7], a[168:171], v[200:203], v[4:7]\n"
    "ds_read_b64_tr_b16 a[168:169], v245 offset:20480\n"
    "v_mfma_f32_16x16x32_bf16 v[4:7], a[172:175], v[204:207], v[4:7]\n"
    "s_waitcnt lgkmcnt(5)\n"
    "ds_read_b64_tr_b16 a[170:171], v245 offset:22528\n"
    "v_mfma_f32_16x16x32_bf16 v[8:11], a[176:179], v[200:203], v[8:11]\n"
    "ds_read_b64_tr_b16 a[172:173], v245 offset:28672\n"
    "v_mov_b32_e32 v156, 0\n"
    "v_mfma_f32_16x16x32_bf16 v[8:11], a[180:183], v[204:207], v[8:11]\n"
    "ds_read_b64_tr_b16 a[174:175], v245 offset:30720\n"
    "v_mfma_f32_16x16x32_bf16 v[32:35], a[180:183], v[212:215], v[32:35]\n"
    "v_exp_f32_e32 v190, v190\n"
    "v_mfma_f32_16x16x32_bf16 v[32:35], a[176:179], v[208:211], v[32:35]\n"
    "v_exp_f32_e32 v191, v191\n"
    "v_mfma_f32_16x16x32_bf16 v[80:83], a[176:179], v[216:219], v[80:83]\n"
    "v_mov_b32_e32 v157, 0\n"
    "v_mfma_f32_16x16x32_bf16 v[80:83], a[180:183], v[220:223], v[80:83]\n"
    "v_cvt_pk_bf16_f32 v186, v188, v189\n"
    "v_mfma_f32_16x16x32_bf16 v[84:87], a[180:183], v[228:231], v[84:87]\n"
    "v_exp_f32_e32 v180, v180\n"
    "v_mfma_f32_16x16x32_bf16 v[84:87], a[176:179], v[224:227], v[84:87]\n"
    "v_exp_f32_e32 v181, v181\n"
    "v_mfma_f32_16x16x32_bf16 v[88:91], a[176:179], v[232:235], v[88:91]\n"
    "ds_read_b64_tr_b16 a[176:177], v246 offset:20480\n"
    "v_mfma_f32_16x16x32_bf16 v[88:91], a[180:183], v[236:239], v[88:91]\n"
    "ds_read_b64_tr_b16 a[178:179], v246 offset:22528\n"
    "v_mfma_f32_16x16x32_bf16 v[100:103], a[184:187], v[232:235], v[100:103]\n"
    "ds_read_b64_tr_b16 a[180:181], v246 offset:28672\n"
    "v_mfma_f32_16x16x32_bf16 v[100:103], a[188:191], v[236:239], v[100:103]\n"
    "ds_read_b64_tr_b16 a[182:183], v246 offset:30720\n"
    "v_mfma_f32_16x16x32_bf16 v[96:99], a[188:191], v[228:231], v[96:99]\n"
    "v_cvt_pk_bf16_f32 v187, v190, v191\n"
    "v_mov_b32_e32 v158, 0\n"
    "v_mfma_f32_16x16x32_bf16 v[96:99], a[184:187], v[224:227], v[96:99]\n"
    "v_exp_f32_e32 v182, v182\n"
    "v_mfma_f32_16x16x32_bf16 v[92:95], a[184:187], v[216:219], v[92:95]\n"
    "v_exp_f32_e32 v183, v183\n"
    "v_mfma_f32_16x16x32_bf16 v[92:95], a[188:191], v[220:223], v[92:95]\n"
    "v_cvt_pk_bf16_f32 v178, v180, v181\n"
    "v_mfma_f32_16x16x32_bf16 v[36:39], a[188:191], v[212:215], v[36:39]\n"
    "v_exp_f32_e32 v172, v172\n"
    "v_mfma_f32_16x16x32_bf16 v[36:39], a[184:187], v[208:211], v[36:39]\n"
    "v_exp_f32_e32 v173, v173\n"
    "v_mfma_f32_16x16x32_bf16 v[12:15], a[184:187], v[200:203], v[12:15]\n"
    "s_mov_b32 m0, s49\n"
    "ds_read_b64_tr_b16 a[184:185], v247 offset:20480\n"
    "v_mfma_f32_16x16x32_bf16 v[12:15], a[188:191], v[204:207], v[12:15]\n"
    "s_waitcnt lgkmcnt(5)\n"
    "ds_read_b64_tr_b16 a[186:187], v247 offset:22528\n"
    "v_mov_b32_e32 v159, 0\n"
    "v_mfma_f32_16x16x32_bf16 v[16:19], a[160:163], v[200:203], v[16:19]\n"
    "ds_read_b64_tr_b16 a[188:189], v247 offset:28672\n"
    "v_mfma_f32_16x16x32_bf16 v[16:19], a[164:167], v[204:207], v[16:19]\n"
    "ds_read_b64_tr_b16 a[190:191], v247 offset:30720\n"
    "v_mfma_f32_16x16x32_bf16 v[40:43], a[164:167], v[212:215], v[40:43]\n"
    "buffer_store_dwordx2 v[154:155], v253, s[24:27], 0 offen offset:0 sc1\n"
    "v_cvt_pk_bf16_f32 v179, v182, v183\n"
    "v_mfma_f32_16x16x32_bf16 v[40:43], a[160:163], v[208:211], v[40:43]\n"
    "v_exp_f32_e32 v174, v174\n"
    "v_mfma_f32_16x16x32_bf16 v[104:107], a[160:163], v[216:219], v[104:107]\n"
    "v_exp_f32_e32 v175, v175\n"
    "v_mfma_f32_16x16x32_bf16 v[104:107], a[164:167], v[220:223], v[104:107]\n"
    "s_waitcnt vmcnt(9)\n"
    "v_mfma_f32_16x16x32_bf16 v[108:111], a[164:167], v[228:231], v[108:111]\n"
    "s_barrier\n"
    "v_mfma_f32_16x16x32_bf16 v[108:111], a[160:163], v[224:227], v[108:111]\n"
    "ds_write_addtid_b32 v192 offset:16384\n"
    "v_cvt_pk_bf16_f32 v170, v172, v173\n"
    "v_mfma_f32_16x16x32_bf16 v[112:115], a[160:163], v[232:235], v[112:115]\n"
    "ds_read_b128 a[160:163], v243 offset:53248\n"
    "v_mfma_f32_16x16x32_bf16 v[112:115], a[164:167], v[236:239], v[112:115]\n"
    "ds_read_b128 a[164:167], v240 offset:49152\n"
    "v_mfma_f32_16x16x32_bf16 v[124:127], a[168:171], v[232:235], v[124:127]\n"
    "v_exp_f32_e32 v164, v164\n"
    "v_mfma_f32_16x16x32_bf16 v[124:127], a[172:175], v[236:239], v[124:127]\n"
    "v_exp_f32_e32 v165, v165\n"
    "v_mfma_f32_16x16x32_bf16 v[120:123], a[172:175], v[228:231], v[120:123]\n"
    "ds_write_addtid_b32 v193 offset:16640\n"
    "v_cvt_pk_bf16_f32 v171, v174, v175\n"
    "v_mfma_f32_16x16x32_bf16 v[120:123], a[168:171], v[224:227], v[120:123]\n"
    "ds_write_addtid_b32 v184 offset:12288\n"
    "v_mfma_f32_16x16x32_bf16 v[116:119], a[168:171], v[216:219], v[116:119]\n"
    "v_exp_f32_e32 v166, v166\n"
    "v_mfma_f32_16x16x32_bf16 v[116:119], a[172:175], v[220:223], v[116:119]\n"
    "v_exp_f32_e32 v167, v167\n"
    "v_mfma_f32_16x16x32_bf16 v[44:47], a[172:175], v[212:215], v[44:47]\n"
    "ds_write_addtid_b32 v185 offset:12544\n"
    "v_cvt_pk_bf16_f32 v162, v164, v165\n"
    "v_mfma_f32_16x16x32_bf16 v[44:47], a[168:171], v[208:211], v[44:47]\n"
    "v_cvt_pk_bf16_f32 v163, v166, v167\n"
    "v_mfma_f32_16x16x32_bf16 v[20:23], a[168:171], v[200:203], v[20:23]\n"
    "ds_read_b128 a[168:171], v241 offset:49152\n"
    "v_mfma_f32_16x16x32_bf16 v[20:23], a[172:175], v[204:207], v[20:23]\n"
    "s_waitcnt lgkmcnt(7)\n"
    "ds_read_b128 a[172:175], v240 offset:53248\n"
    "v_mfma_f32_16x16x32_bf16 v[24:27], a[176:179], v[200:203], v[24:27]\n"
    "ds_write_addtid_b32 v176 offset:8192\n"
    "v_mfma_f32_16x16x32_bf16 v[24:27], a[180:183], v[204:207], v[24:27]\n"
    "ds_write_addtid_b32 v177 offset:8448\n"
    "v_mfma_f32_16x16x32_bf16 v[48:51], a[180:183], v[212:215], v[48:51]\n"
    "ds_write_addtid_b32 v168 offset:4096\n"
    "v_mfma_f32_16x16x32_bf16 v[48:51], a[176:179], v[208:211], v[48:51]\n"
    "ds_write_addtid_b32 v169 offset:4352\n"
    "v_mfma_f32_16x16x32_bf16 v[128:131], a[176:179], v[216:219], v[128:131]\n"
    "ds_write_addtid_b32 v160 offset:0\n"
    "v_mfma_f32_16x16x32_bf16 v[128:131], a[180:183], v[220:223], v[128:131]\n"
    "ds_write_addtid_b32 v161 offset:256\n"
    "v_mfma_f32_16x16x32_bf16 v[132:135], a[180:183], v[228:231], v[132:135]\n"
    "ds_write_addtid_b32 v194 offset:16896\n"
    "v_mfma_f32_16x16x32_bf16 v[132:135], a[176:179], v[224:227], v[132:135]\n"
    "ds_write_addtid_b32 v195 offset:17152\n"
    "v_mfma_f32_16x16x32_bf16 v[136:139], a[176:179], v[232:235], v[136:139]\n"
    "ds_read_b128 a[176:179], v241 offset:53248\n"
    "v_mfma_f32_16x16x32_bf16 v[136:139], a[180:183], v[236:239], v[136:139]\n"
    "ds_read_b128 a[180:183], v242 offset:49152\n"
    "v_mfma_f32_16x16x32_bf16 v[148:151], a[184:187], v[232:235], v[148:151]\n"
    "ds_write_addtid_b32 v186 offset:12800\n"
    "v_mfma_f32_16x16x32_bf16 v[148:151], a[188:191], v[236:239], v[148:151]\n"
    "ds_write_addtid_b32 v187 offset:13056\n"
    "v_mfma_f32_16x16x32_bf16 v[144:147], a[188:191], v[228:231], v[144:147]\n"
    "ds_write_addtid_b32 v178 offset:8704\n"
    "v_mfma_f32_16x16x32_bf16 v[144:147], a[184:187], v[224:227], v[144:147]\n"
    "ds_write_addtid_b32 v179 offset:8960\n"
    "v_mfma_f32_16x16x32_bf16 v[140:143], a[184:187], v[216:219], v[140:143]\n"
    "ds_write_addtid_b32 v170 offset:4608\n"
    "v_mfma_f32_16x16x32_bf16 v[140:143], a[188:191], v[220:223], v[140:143]\n"
    "ds_write_addtid_b32 v171 offset:4864\n"
    "v_mfma_f32_16x16x32_bf16 v[52:55], a[188:191], v[212:215], v[52:55]\n"
    "ds_write_addtid_b32 v162 offset:512\n"
    "v_mfma_f32_16x16x32_bf16 v[52:55], a[184:187], v[208:211], v[52:55]\n"
    "ds_write_addtid_b32 v163 offset:768\n"
    "v_mfma_f32_16x16x32_bf16 v[28:31], a[184:187], v[200:203], v[28:31]\n"
    "ds_read_b128 a[184:187], v243 offset:49152\n"
    "v_mfma_f32_16x16x32_bf16 v[28:31], a[188:191], v[204:207], v[28:31]\n"
    "s_waitcnt lgkmcnt(15)\n"
    "ds_read_b128 a[188:191], v242 offset:53248\n"
    "v_mfma_f32_16x16x32_bf16 v[200:203], a[160:163], a[28:31], 0\n"
    "v_mfma_f32_16x16x32_bf16 v[200:203], a[164:167], a[0:3], v[200:203]\n"
    "v_mfma_f32_16x16x32_bf16 v[208:211], a[164:167], a[32:35], 0\n"
    "v_mfma_f32_16x16x32_bf16 v[208:211], a[160:163], a[60:63], v[208:211]\n"
    "v_mfma_f32_16x16x32_bf16 v[216:219], a[160:163], a[92:95], 0\n"
    "v_mfma_f32_16x16x32_bf16 v[216:219], a[164:167], a[64:67], v[216:219]\n"
    "s_waitcnt lgkmcnt(0)\n"
    "v_mfma_f32_16x16x32_bf16 v[224:227], a[164:167], a[96:99], 0\n"
    "s_barrier\n"
    "v_mfma_f32_16x16x32_bf16 v[224:227], a[160:163], a[124:127], v[224:227]\n"
    "s_add_u32 m0, s43, 16384\n"
    "ds_read2st64_b32 v[164:165], v250 offset0:0 offset1:1\n"
    "v_mfma_f32_16x16x32_bf16 v[232:235], a[160:163], a[156:159], 0\n"
    "ds_read_b128 a[160:163], v243 offset:55296\n"
    "v_mfma_f32_16x16x32_bf16 v[232:235], a[164:167], a[128:131], v[232:235]\n"
    "ds_read_b128 a[164:167], v240 offset:51200\n"
    "v_mfma_f32_16x16x32_bf16 v[232:235], a[168:171], a[132:135], v[232:235]\n"
    "buffer_load_dwordx4 v248, s[8:11], s40 offen offset:0 sc1 lds\n"
    "v_mfma_f32_16x16x32_bf16 v[232:235], a[172:175], a[136:139], v[232:235]\n"
    "v_mfma_f32_16x16x32_bf16 v[224:227], a[172:175], a[104:107], v[224:227]\n"
    "v_mfma_f32_16x16x32_bf16 v[224:227], a[168:171], a[100:103], v[224:227]\n"
    "ds_read2st64_b32 v[166:167], v250 offset0:2 offset1:3\n"
    "v_mfma_f32_16x16x32_bf16 v[216:219], a[168:171], a[68:71], v[216:219]\n"
    "buffer_load_dwordx4 v248, s[8:11], s40 offen offset:1024 sc1 lds\n"
    "v_mfma_f32_16x16x32_bf16 v[216:219], a[172:175], a[72:75], v[216:219]\n"
    "v_mfma_f32_16x16x32_bf16 v[208:211], a[172:175], a[40:43], v[208:211]\n"
    "v_mfma_f32_16x16x32_bf16 v[208:211], a[168:171], a[36:39], v[208:211]\n"
    "ds_read2st64_b32 v[172:173], v250 offset0:16 offset1:17\n"
    "v_mfma_f32_16x16x32_bf16 v[200:203], a[168:171], a[4:7], v[200:203]\n"
    "ds_read_b128 a[168:171], v241 offset:51200\n"
    "buffer_load_dwordx4 v248, s[8:11], s40 offen offset:2048 sc1 lds\n"
    "v_mfma_f32_16x16x32_bf16 v[200:203], a[172:175], a[8:11], v[200:203]\n"
    "ds_read_b128 a[172:175], v240 offset:55296\n"
    "v_mfma_f32_16x16x32_bf16 v[200:203], a[176:179], a[12:15], v[200:203]\n"
    "ds_read2st64_b32 v[174:175], v250 offset0:18 offset1:19\n"
    "v_mfma_f32_16x16x32_bf16 v[200:203], a[180:183], a[16:19], v[200:203]\n"
    "v_mfma_f32_16x16x32_bf16 v[208:211], a[180:183], a[48:51], v[208:211]\n"
    "buffer_load_dwordx4 v248, s[8:11], s40 offen offset:3072 sc1 lds\n"
    "v_mfma_f32_16x16x32_bf16 v[208:211], a[176:179], a[44:47], v[208:211]\n"
    "s_add_u32 m0, s43, 20480\n"
    "v_mfma_f32_16x16x32_bf16 v[216:219], a[176:179], a[76:79], v[216:219]\n"
    "s_add_u32 s40, s40, 0x8000\n"
    "ds_read2st64_b32 v[180:181], v250 offset0:32 offset1:33\n"
    "v_mfma_f32_16x16x32_bf16 v[216:219], a[180:183], a[80:83], v[216:219]\n"
    "v_mfma_f32_16x16x32_bf16 v[224:227], a[180:183], a[112:115], v[224:227]\n"
    "buffer_load_dwordx4 v248, s[8:11], s41 offen offset:0 sc1 lds\n"
    "v_mfma_f32_16x16x32_bf16 v[224:227], a[176:179], a[108:111], v[224:227]\n"
    "ds_read2st64_b32 v[182:183], v250 offset0:34 offset1:35\n"
    "v_mfma_f32_16x16x32_bf16 v[232:235], a[176:179], a[140:143], v[232:235]\n"
    "ds_read_b128 a[176:179], v241 offset:55296\n"
    "v_mfma_f32_16x16x32_bf16 v[232:235], a[180:183], a[144:147], v[232:235]\n"
    "ds_read_b128 a[180:183], v242 offset:51200\n"
    "v_mfma_f32_16x16x32_bf16 v[232:235], a[184:187], a[148:151], v[232:235]\n"
    "buffer_load_dwordx4 v248, s[8:11], s41 offen offset:1024 sc1 lds\n"
    "v_mfma_f32_16x16x32_bf16 v[232:235], a[188:191], a[152:155], v[232:235]\n"
    "v_mfma_f32_16x16x32_bf16 v[224:227], a[188:191], a[120:123], v[224:227]\n"
    "ds_read2st64_b32 v[188:189], v250 offset0:48 offset1:49\n"
    "v_mfma_f32_16x16x32_bf16 v[224:227], a[184:187], a[116:119], v[224:227]\n"
    "v_mfma_f32_16x16x32_bf16 v[216:219], a[184:187], a[84:87], v[216:219]\n"
    "buffer_load_dwordx4 v248, s[8:11], s41 offen offset:2048 sc1 lds\n"
    "v_mfma_f32_16x16x32_bf16 v[216:219], a[188:191], a[88:91], v[216:219]\n"
    "ds_read2st64_b32 v[190:191], v250 offset0:50 offset1:51\n"
    "v_mfma_f32_16x16x32_bf16 v[208:211], a[188:191], a[56:59], v[208:211]\n"
    "ds_read_b64 v[254:255], v252 offset:64\n"
    "v_mfma_f32_16x16x32_bf16 v[208:211], a[184:187], a[52:55], v[208:211]\n"
    "ds_read2st64_b32 v[196:197], v250 offset0:64 offset1:65\n"
    "v_mfma_f32_16x16x32_bf16 v[200:203], a[184:187], a[20:23], v[200:203]\n"
    "ds_read_b128 a[184:187], v243 offset:51200\n"
    "buffer_load_dwordx4 v248, s[8:11], s41 offen offset:3072 sc1 lds\n"
    "v_mfma_f32_16x16x32_bf16 v[200:203], a[188:191], a[24:27], v[200:203]\n"
    "s_waitcnt lgkmcnt(15)\n"
    "s_add_u32 s41, s41, 0x8000\n"
    "ds_read_b128 a[188:191], v242 offset:55296\n"
    "v_mfma_f32_16x16x32_bf16 v[204:207], a[160:163], a[28:31], 0\n"
    "s_waitcnt lgkmcnt(3)\n"
    "v_mad_u32_u16 v253, v254, s42, v251 op_sel:[0,0,0,0]\n"
    "v_mfma_f32_16x16x32_bf16 v[204:207], a[164:167], a[0:3], v[204:207]\n"
    "buffer_load_dwordx4 a[200:203], v253, s[20:23], 0 offen offset:0 sc1\n"
    "v_mfma_f32_16x16x32_bf16 v[212:215], a[164:167], a[32:35], 0\n"
    "v_exp_f32_e32 v232, v232\n"
    "v_mfma_f32_16x16x32_bf16 v[212:215], a[160:163], a[60:63], v[212:215]\n"
    "ds_read2st64_b32 v[198:199], v250 offset0:66 offset1:67\n"
    "v_mfma_f32_16x16x32_bf16 v[220:223], a[160:163], a[92:95], 0\n"
    "v_mad_u32_u16 v253, v254, s42, v251 op_sel:[1,0,0,0]\n"
    "v_mfma_f32_16x16x32_bf16 v[220:223], a[164:167], a[64:67], v[220:223]\n"
    "buffer_load_dwordx4 a[204:207], v253, s[20:23], 0 offen offset:0 sc1\n"
    "v_mfma_f32_16x16x32_bf16 v[228:231], a[164:167], a[96:99], 0\n"
    "v_exp_f32_e32 v233, v233\n"
    "v_mfma_f32_16x16x32_bf16 v[228:231], a[160:163], a[124:127], v[228:231]\n"
    "v_exp_f32_e32 v234, v234\n"
    "v_mfma_f32_16x16x32_bf16 v[236:239], a[160:163], a[156:159], 0\n"
    "ds_read_b64_tr_b16 a[160:161], v244 offset:32768\n"
    "v_mad_u32_u16 v253, v255, s42, v251 op_sel:[0,0,0,0]\n"
    "v_mfma_f32_16x16x32_bf16 v[236:239], a[164:167], a[128:131], v[236:239]\n"
    "ds_read_b64_tr_b16 a[162:163], v244 offset:34816\n"
    "buffer_load_dwordx4 a[208:211], v253, s[20:23], 0 offen offset:0 sc1\n"
    "v_mfma_f32_16x16x32_bf16 v[236:239], a[168:171], a[132:135], v[236:239]\n"
    "ds_read_b64_tr_b16 a[164:165], v244 offset:40960\n"
    "v_mfma_f32_16x16x32_bf16 v[236:239], a[172:175], a[136:139], v[236:239]\n"
    "ds_read_b64_tr_b16 a[166:167], v244 offset:43008\n"
    "v_mfma_f32_16x16x32_bf16 v[228:231], a[172:175], a[104:107], v[228:231]\n"
    "v_mad_u32_u16 v253, v255, s42, v251 op_sel:[1,0,0,0]\n"
    "v_mfma_f32_16x16x32_bf16 v[228:231], a[168:171], a[100:103], v[228:231]\n"
    "buffer_load_dwordx4 a[212:215], v253, s[20:23], 0 offen offset:0 sc1\n"
    "ds_read_b64 v[254:255], v252 offset:72\n"
    "v_mfma_f32_16x16x32_bf16 v[220:223], a[168:171], a[68:71], v[220:223]\n"
    "v_exp_f32_e32 v235, v235\n"
    "v_mfma_f32_16x16x32_bf16 v[220:223], a[172:175], a[72:75], v[220:223]\n"
    "v_cvt_pk_bf16_f32 v232, v232, v233\n"
    "v_mfma_f32_16x16x32_bf16 v[212:215], a[172:175], a[40:43], v[212:215]\n"
    "v_exp_f32_e32 v224, v224\n"
    "v_mfma_f32_16x16x32_bf16 v[212:215], a[168:171], a[36:39], v[212:215]\n"
    "v_exp_f32_e32 v225, v225\n"
    "v_mfma_f32_16x16x32_bf16 v[204:207], a[168:171], a[4:7], v[204:207]\n"
    "ds_read_b64_tr_b16 a[168:169], v245 offset:32768\n"
    "v_cvt_pk_bf16_f32 v233, v234, v235\n"
    "v_mfma_f32_16x16x32_bf16 v[204:207], a[172:175], a[8:11], v[204:207]\n"
    "ds_read_b64_tr_b16 a[170:171], v245 offset:34816\n"
    "v_mfma_f32_16x16x32_bf16 v[204:207], a[176:179], a[12:15], v[204:207]\n"
    "s_waitcnt lgkmcnt(2)\n"
    "ds_read_b64_tr_b16 a[172:173], v245 offset:40960\n"
    "v_mad_u32_u16 v253, v254, s42, v251 op_sel:[0,0,0,0]\n"
    "v_mfma_f32_16x16x32_bf16 v[204:207], a[180:183], a[16:19], v[204:207]\n"
    "ds_read_b64_tr_b16 a[174:175], v245 offset:43008\n"
    "buffer_load_dwordx4 a[216:219], v253, s[20:23], 0 offen offset:0 sc1\n"
    "v_mfma_f32_16x16x32_bf16 v[212:215], a[180:183], a[48:51], v[212:215]\n"
    "v_exp_f32_e32 v226, v226\n"
    "v_mfma_f32_16x16x32_bf16 v[212:215], a[176:179], a[44:47], v[212:215]\n"
    "v_exp_f32_e32 v227, v227\n"
    "v_mfma_f32_16x16x32_bf16 v[220:223], a[176:179], a[76:79], v[220:223]\n"
    "v_mad_u32_u16 v253, v254, s42, v251 op_sel:[1,0,0,0]\n"
    "v_cvt_pk_bf16_f32 v224, v224, v225\n"
    "v_mfma_f32_16x16x32_bf16 v[220:223], a[180:183], a[80:83], v[220:223]\n"
    "buffer_load_dwordx4 a[220:223], v253, s[20:23], 0 offen offset:0 sc1\n"
    "v_mfma_f32_16x16x32_bf16 v[228:231], a[180:183], a[112:115], v[228:231]\n"
    "v_exp_f32_e32 v216, v216\n"
    "v_mfma_f32_16x16x32_bf16 v[228:231], a[176:179], a[108:111], v[228:231]\n"
    "v_exp_f32_e32 v217, v217\n"
    "v_mfma_f32_16x16x32_bf16 v[236:239], a[176:179], a[140:143], v[236:239]\n"
    "ds_read_b64_tr_b16 a[176:177], v246 offset:32768\n"
    "v_cvt_pk_bf16_f32 v225, v226, v227\n"
    "v_mfma_f32_16x16x32_bf16 v[236:239], a[180:183], a[144:147], v[236:239]\n"
    "ds_read_b64_tr_b16 a[178:179], v246 offset:34816\n"
    "v_mfma_f32_16x16x32_bf16 v[236:239], a[184:187], a[148:151], v[236:239]\n"
    "ds_read_b64_tr_b16 a[180:181], v246 offset:40960\n"
    "v_mfma_f32_16x16x32_bf16 v[236:239], a[188:191], a[152:155], v[236:239]\n"
    "ds_read_b64_tr_b16 a[182:183], v246 offset:43008\n"
    "v_mfma_f32_16x16x32_bf16 v[228:231], a[188:191], a[120:123], v[228:231]\n"
    "ds_read_b64 v[254:255], v252 offset:96\n"
    "v_mfma_f32_16x16x32_bf16 v[228:231], a[184:187], a[116:119], v[228:231]\n"
    "v_exp_f32_e32 v218, v218\n"
    "v_mfma_f32_16x16x32_bf16 v[220:223], a[184:187], a[84:87], v[220:223]\n"
    "v_exp_f32_e32 v219, v219\n"
    "v_mfma_f32_16x16x32_bf16 v[220:223], a[188:191], a[88:91], v[220:223]\n"
    "v_cvt_pk_bf16_f32 v216, v216, v217\n"
    "v_mfma_f32_16x16x32_bf16 v[212:215], a[188:191], a[56:59], v[212:215]\n"
    "s_waitcnt lgkmcnt(0)\n"
    "v_mad_u32_u16 v253, v254, s42, v251 op_sel:[0,0,0,0]\n"
    "v_mfma_f32_16x16x32_bf16 v[212:215], a[184:187], a[52:55], v[212:215]\n"
    "buffer_load_dwordx4 a[224:227], v253, s[20:23], 0 offen offset:0 sc1\n"
    "v_mfma_f32_16x16x32_bf16 v[204:207], a[184:187], a[20:23], v[204:207]\n"
    "ds_read_b64_tr_b16 a[184:185], v247 offset:32768\n"
    "v_mfma_f32_16x16x32_bf16 v[204:207], a[188:191], a[24:27], v[204:207]\n"
    "ds_read_b64_tr_b16 a[186:187], v247 offset:34816\n"
    "v_mfma_f32_16x16x32_bf16 v[0:3], a[160:163], v[160:163], v[0:3]\n"
    "ds_read_b64_tr_b16 a[188:189], v247 offset:40960\n"
    "v_mad_u32_u16 v253, v254, s42, v251 op_sel:[1,0,0,0]\n"
    "v_mfma_f32_16x16x32_bf16 v[0:3], a[164:167], v[164:167], v[0:3]\n"
    "ds_read_b64_tr_b16 a[190:191], v247 offset:43008\n"
    "buffer_load_dwordx4 a[228:231], v253, s[20:23], 0 offen offset:0 sc1\n"
    "v_mfma_f32_16x16x32_bf16 a[248:251], a[164:167], v[172:175], a[248:251]\n"
    "v_exp_f32_e32 v208, v208\n"
    "v_mfma_f32_16x16x32_bf16 a[248:251], a[160:163], v[168:171], a[248:251]\n"
    "v_exp_f32_e32 v209, v209\n"
    "v_mfma_f32_16x16x32_bf16 v[56:59], a[160:163], v[176:179], v[56:59]\n"
    "v_mad_u32_u16 v253, v255, s42, v251 op_sel:[0,0,0,0]\n"
    "v_cvt_pk_bf16_f32 v217, v218, v219\n"
    "v_mfma_f32_16x16x32_bf16 v[56:59], a[164:167], v[180:183], v[56:59]\n"
    "buffer_load_dwordx4 a[232:235], v253, s[20:23], 0 offen offset:0 sc1\n"
    "v_mfma_f32_16x16x32_bf16 v[60:63], a[164:167], v[188:191], v[60:63]\n"
    "v_exp_f32_e32 v210, v210\n"
    "v_mfma_f32_16x16x32_bf16 v[60:63], a[160:163], v[184:187], v[60:63]\n"
    "v_exp_f32_e32 v211, v211\n"
    "v_mfma_f32_16x16x32_bf16 v[64:67], a[160:163], v[192:195], v[64:67]\n"
    "ds_read_b64_tr_b16 a[160:161], v244 offset:36864\n"
    "v_mad_u32_u16 v253, v255, s42, v251 op_sel:[1,0,0,0]\n"
    "v_mfma_f32_16x16x32_bf16 v[64:67], a[164:167], v[196:199], v[64:67]\n"
    "ds_read_b64_tr_b16 a[162:163], v244 offset:38912\n"
    "buffer_load_dwordx4 a[236:239], v253, s[20:23], 0 offen offset:0 sc1\n"
    "v_mfma_f32_16x16x32_bf16 v[76:79], a[168:171], v[192:195], v[76:79]\n"
    "ds_read_b64_tr_b16 a[164:165], v244 offset:45056\n"
    "v_cvt_pk_bf16_f32 v208, v208, v209\n"
    "v_mfma_f32_16x16x32_bf16 v[76:79], a[172:175], v[196:199], v[76:79]\n"
    "ds_read_b64_tr_b16 a[166:167], v244 offset:47104\n"
    "v_mfma_f32_16x16x32_bf16 v[72:75], a[172:175], v[188:191], v[72:75]\n"
    "ds_read_b64 v[254:255], v252 offset:104\n"
    "v_mfma_f32_16x16x32_bf16 v[72:75], a[168:171], v[184:187], v[72:75]\n"
    "v_exp_f32_e32 v200, v200\n"
    "v_mfma_f32_16x16x32_bf16 v[68:71], a[168:171], v[176:179], v[68:71]\n"
    "v_exp_f32_e32 v201, v201\n"
    "v_mfma_f32_16x16x32_bf16 v[68:71], a[172:175], v[180:183], v[68:71]\n"
    "v_cvt_pk_bf16_f32 v209, v210, v211\n"
    "v_mfma_f32_16x16x32_bf16 a[252:255], a[172:175], v[172:175], a[252:255]\n"
    "v_exp_f32_e32 v202, v202\n"
    "v_mfma_f32_16x16x32_bf16 a[252:255], a[168:171], v[168:171], a[252:255]\n"
    "v_exp_f32_e32 v203, v203\n"
    "v_mfma_f32_16x16x32_bf16 v[4:7], a[168:171], v[160:163], v[4:7]\n"
    "s_waitcnt lgkmcnt(0)\n"
    "ds_read_b64_tr_b16 a[168:169], v245 offset:36864\n"
    "v_mad_u32_u16 v253, v254, s42, v251 op_sel:[0,0,0,0]\n"
    "v_mfma_f32_16x16x32_bf16 v[4:7], a[172:175], v[164:167], v[4:7]\n"
    "ds_read_b64_tr_b16 a[170:171], v245 offset:38912\n"
    "v_cvt_pk_bf16_f32 v200, v200, v201\n"
    "v_mfma_f32_16x16x32_bf16 v[8:11], a[176:179], v[160:163], v[8:11]\n"
    "ds_read_b64_tr_b16 a[172:173], v245 offset:45056\n"
    "buffer_load_dwordx4 a[240:243], v253, s[20:23], 0 offen offset:0 sc1\n"
    "v_mfma_f32_16x16x32_bf16 v[8:11], a[180:183], v[164:167], v[8:11]\n"
    "ds_read_b64_tr_b16 a[174:175], v245 offset:47104\n"
    "v_cvt_pk_bf16_f32 v201, v202, v203\n"
    "v_mfma_f32_16x16x32_bf16 v[32:35], a[180:183], v[172:175], v[32:35]\n"
    "v_exp_f32_e32 v236, v236\n"
    "v_mfma_f32_16x16x32_bf16 v[32:35], a[176:179], v[168:171], v[32:35]\n"
    "v_mad_u32_u16 v253, v254, s42, v251 op_sel:[1,0,0,0]\n"
    "v_mfma_f32_16x16x32_bf16 v[80:83], a[176:179], v[176:179], v[80:83]\n"
    "buffer_load_dwordx4 a[244:247], v253, s[20:23], 0 offen offset:0 sc1\n"
    "v_mfma_f32_16x16x32_bf16 v[80:83], a[180:183], v[180:183], v[80:83]\n"
    "v_exp_f32_e32 v237, v237\n"
    "v_mfma_f32_16x16x32_bf16 v[84:87], a[180:183], v[188:191], v[84:87]\n"
    "v_exp_f32_e32 v238, v238\n"
    "v_mfma_f32_16x16x32_bf16 v[84:87], a[176:179], v[184:187], v[84:87]\n"
    "v_exp_f32_e32 v239, v239\n"
    "v_mfma_f32_16x16x32_bf16 v[88:91], a[176:179], v[192:195], v[88:91]\n"
    "ds_read_b64_tr_b16 a[176:177], v246 offset:36864\n"
    "v_cvt_pk_bf16_f32 v234, v236, v237\n"
    "v_mfma_f32_16x16x32_bf16 v[88:91], a[180:183], v[196:199], v[88:91]\n"
    "ds_read_b64_tr_b16 a[178:179], v246 offset:38912\n"
    "v_exp_f32_e32 v228, v228\n"
    "v_mfma_f32_16x16x32_bf16 v[100:103], a[184:187], v[192:195], v[100:103]\n"
    "ds_read_b64_tr_b16 a[180:181], v246 offset:45056\n"
    "v_exp_f32_e32 v229, v229\n"
    "v_mfma_f32_16x16x32_bf16 v[100:103], a[188:191], v[196:199], v[100:103]\n"
    "ds_read_b64_tr_b16 a[182:183], v246 offset:47104\n"
    "v_cvt_pk_bf16_f32 v235, v238, v239\n"
    "v_mfma_f32_16x16x32_bf16 v[96:99], a[188:191], v[188:191], v[96:99]\n"
    "v_exp_f32_e32 v230, v230\n"
    "v_mfma_f32_16x16x32_bf16 v[96:99], a[184:187], v[184:187], v[96:99]\n"
    "v_exp_f32_e32 v231, v231\n"
    "v_cvt_pk_bf16_f32 v226, v228, v229\n"
    "v_mfma_f32_16x16x32_bf16 v[92:95], a[184:187], v[176:179], v[92:95]\n"
    "v_exp_f32_e32 v220, v220\n"
    "v_mfma_f32_16x16x32_bf16 v[92:95], a[188:191], v[180:183], v[92:95]\n"
    "v_exp_f32_e32 v221, v221\n"
    "v_mfma_f32_16x16x32_bf16 v[36:39], a[188:191], v[172:175], v[36:39]\n"
    "v_cvt_pk_bf16_f32 v227, v230, v231\n"
    "v_mfma_f32_16x16x32_bf16 v[36:39], a[184:187], v[168:171], v[36:39]\n"
    "v_exp_f32_e32 v222, v222\n"
    "v_mfma_f32_16x16x32_bf16 v[12:15], a[184:187], v[160:163], v[12:15]\n"
    "s_mov_b32 m0, s49\n"
    "ds_read_b64_tr_b16 a[184:185], v247 offset:36864\n"
    "v_mfma_f32_16x16x32_bf16 v[12:15], a[188:191], v[164:167], v[12:15]\n"
    "s_waitcnt lgkmcnt(5)\n"
    "ds_read_b64_tr_b16 a[186:187], v247 offset:38912\n"
    "v_mfma_f32_16x16x32_bf16 v[16:19], a[160:163], v[160:163], v[16:19]\n"
    "ds_read_b64_tr_b16 a[188:189], v247 offset:45056\n"
    "v_mfma_f32_16x16x32_bf16 v[16:19], a[164:167], v[164:167], v[16:19]\n"
    "ds_read_b64_tr_b16 a[190:191], v247 offset:47104\n"
    "v_mfma_f32_16x16x32_bf16 v[40:43], a[164:167], v[172:175], v[40:43]\n"
    "v_exp_f32_e32 v223, v223\n"
    "v_mfma_f32_16x16x32_bf16 v[40:43], a[160:163], v[168:171], v[40:43]\n"
    "v_cvt_pk_bf16_f32 v218, v220, v221\n"
    "v_mfma_f32_16x16x32_bf16 v[104:107], a[160:163], v[176:179], v[104:107]\n"
    "v_exp_f32_e32 v212, v212\n"
    "v_mfma_f32_16x16x32_bf16 v[104:107], a[164:167], v[180:183], v[104:107]\n"
    "s_waitcnt vmcnt(20)\n"
    "v_mfma_f32_16x16x32_bf16 v[108:111], a[164:167], v[188:191], v[108:111]\n"
    "s_barrier\n"
    "v_mfma_f32_16x16x32_bf16 v[108:111], a[160:163], v[184:187], v[108:111]\n"
    "v_exp_f32_e32 v213, v213\n"
    "v_mfma_f32_16x16x32_bf16 v[112:115], a[160:163], v[192:195], v[112:115]\n"
    "ds_read_b128 a[160:163], v243 offset:4096\n"
    "v_cvt_pk_bf16_f32 v219, v222, v223\n"
    "v_mfma_f32_16x16x32_bf16 v[112:115], a[164:167], v[196:199], v[112:115]\n"
    "ds_read_b128 a[164:167], v240 offset:0\n"
    "ds_write_addtid_b32 v232 offset:16384\n"
    "v_mfma_f32_16x16x32_bf16 v[124:127], a[168:171], v[192:195], v[124:127]\n"
    "v_exp_f32_e32 v214, v214\n"
    "v_mfma_f32_16x16x32_bf16 v[124:127], a[172:175], v[196:199], v[124:127]\n"
    "v_exp_f32_e32 v215, v215\n"
    "v_mfma_f32_16x16x32_bf16 v[120:123], a[172:175], v[188:191], v[120:123]\n"
    "ds_write_addtid_b32 v233 offset:16640\n"
    "v_cvt_pk_bf16_f32 v210, v212, v213\n"
    "v_mfma_f32_16x16x32_bf16 v[120:123], a[168:171], v[184:187], v[120:123]\n"
    "v_exp_f32_e32 v204, v204\n"
    "v_mfma_f32_16x16x32_bf16 v[116:119], a[168:171], v[176:179], v[116:119]\n"
    "v_exp_f32_e32 v205, v205\n"
    "v_mfma_f32_16x16x32_bf16 v[116:119], a[172:175], v[180:183], v[116:119]\n"
    "ds_write_addtid_b32 v224 offset:12288\n"
    "v_cvt_pk_bf16_f32 v211, v214, v215\n"
    "v_mfma_f32_16x16x32_bf16 v[44:47], a[172:175], v[172:175], v[44:47]\n"
    "v_exp_f32_e32 v206, v206\n"
    "v_mfma_f32_16x16x32_bf16 v[44:47], a[168:171], v[168:171], v[44:47]\n"
    "v_exp_f32_e32 v207, v207\n"
    "v_mfma_f32_16x16x32_bf16 v[20:23], a[168:171], v[160:163], v[20:23]\n"
    "ds_read_b128 a[168:171], v241 offset:0\n"
    "v_cvt_pk_bf16_f32 v202, v204, v205\n"
    "v_mfma_f32_16x16x32_bf16 v[20:23], a[172:175], v[164:167], v[20:23]\n"
    "s_waitcnt lgkmcnt(6)\n"
    "ds_read_b128 a[172:175], v240 offset:4096\n"
    "v_cvt_pk_bf16_f32 v203, v206, v207\n"
    "v_mfma_f32_16x16x32_bf16 v[24:27], a[176:179], v[160:163], v[24:27]\n"
    "ds_write_addtid_b32 v225 offset:12544\n"
    "ds_write_addtid_b32 v216 offset:8192\n"
    "v_mfma_f32_16x16x32_bf16 v[24:27], a[180:183], v[164:167], v[24:27]\n"
    "ds_write_addtid_b32 v217 offset:8448\n"
    "v_mfma_f32_16x16x32_bf16 v[48:51], a[180:183], v[172:175], v[48:51]\n"
    "ds_write_addtid_b32 v208 offset:4096\n"
    "v_mfma_f32_16x16x32_bf16 v[48:51], a[176:179], v[168:171], v[48:51]\n"
    "ds_write_addtid_b32 v209 offset:4352\n"
    "v_mfma_f32_16x16x32_bf16 v[128:131], a[176:179], v[176:179], v[128:131]\n"
    "ds_write_addtid_b32 v200 offset:0\n"
    "v_mfma_f32_16x16x32_bf16 v[128:131], a[180:183], v[180:183], v[128:131]\n"
    "ds_write_addtid_b32 v201 offset:256\n"
    "v_mfma_f32_16x16x32_bf16 v[132:135], a[180:183], v[188:191], v[132:135]\n"
    "ds_write_addtid_b32 v234 offset:16896\n"
    "v_mfma_f32_16x16x32_bf16 v[132:135], a[176:179], v[184:187], v[132:135]\n"
    "ds_write_addtid_b32 v235 offset:17152\n"
    "v_mfma_f32_16x16x32_bf16 v[136:139], a[176:179], v[192:195], v[136:139]\n"
    "ds_read_b128 a[176:179], v241 offset:4096\n"
    "v_mfma_f32_16x16x32_bf16 v[136:139], a[180:183], v[196:199], v[136:139]\n"
    "ds_read_b128 a[180:183], v242 offset:0\n"
    "v_mfma_f32_16x16x32_bf16 v[148:151], a[184:187], v[192:195], v[148:151]\n"
    "ds_write_addtid_b32 v226 offset:12800\n"
    "v_mfma_f32_16x16x32_bf16 v[148:151], a[188:191], v[196:199], v[148:151]\n"
    "ds_write_addtid_b32 v227 offset:13056\n"
    "v_mfma_f32_16x16x32_bf16 v[144:147], a[188:191], v[188:191], v[144:147]\n"
    "ds_write_addtid_b32 v218 offset:8704\n"
    "v_mfma_f32_16x16x32_bf16 v[144:147], a[184:187], v[184:187], v[144:147]\n"
    "ds_write_addtid_b32 v219 offset:8960\n"
    "v_mfma_f32_16x16x32_bf16 v[140:143], a[184:187], v[176:179], v[140:143]\n"
    "ds_write_addtid_b32 v210 offset:4608\n"
    "v_mfma_f32_16x16x32_bf16 v[140:143], a[188:191], v[180:183], v[140:143]\n"
    "ds_write_addtid_b32 v211 offset:4864\n"
    "v_mfma_f32_16x16x32_bf16 v[52:55], a[188:191], v[172:175], v[52:55]\n"
    "ds_write_addtid_b32 v202 offset:512\n"
    "v_mfma_f32_16x16x32_bf16 v[52:55], a[184:187], v[168:171], v[52:55]\n"
    "ds_write_addtid_b32 v203 offset:768\n"
    "v_mfma_f32_16x16x32_bf16 v[28:31], a[184:187], v[160:163], v[28:31]\n"
    "ds_read_b128 a[184:187], v243 offset:0\n"
    "v_mfma_f32_16x16x32_bf16 v[28:31], a[188:191], v[164:167], v[28:31]\n"
    "s_waitcnt lgkmcnt(15)\n"
    "ds_read_b128 a[188:191], v242 offset:4096\n"
    "v_mfma_f32_16x16x32_bf16 v[160:163], a[160:163], a[28:31], 0\n"
    "v_mfma_f32_16x16x32_bf16 v[160:163], a[164:167], a[0:3], v[160:163]\n"
    "v_mfma_f32_16x16x32_bf16 v[168:171], a[164:167], a[32:35], 0\n"
    "v_mfma_f32_16x16x32_bf16 v[168:171], a[160:163], a[60:63], v[168:171]\n"
    "v_mfma_f32_16x16x32_bf16 v[176:179], a[160:163], a[92:95], 0\n"
    "v_mfma_f32_16x16x32_bf16 v[176:179], a[164:167], a[64:67], v[176:179]\n"
    "s_waitcnt lgkmcnt(0)\n"
    "v_mfma_f32_16x16x32_bf16 v[184:187], a[164:167], a[96:99], 0\n"
    "s_barrier\n"
    "v_mfma_f32_16x16x32_bf16 v[184:187], a[160:163], a[124:127], v[184:187]\n"
    "s_add_u32 m0, s43, 32768\n"
    "v_mfma_f32_16x16x32_bf16 v[192:195], a[160:163], a[156:159], 0\n"
    "ds_read_b128 a[160:163], v243 offset:6144\n"
    "v_mfma_f32_16x16x32_bf16 v[192:195], a[164:167], a[128:131], v[192:195]\n"
    "ds_read_b128 a[164:167], v240 offset:2048\n"
    "v_mfma_f32_16x16x32_bf16 v[192:195], a[168:171], a[132:135], v[192:195]\n"
    "buffer_load_dwordx4 v248, s[8:11], s40 offen offset:0 sc1 lds\n"
    "v_mfma_f32_16x16x32_bf16 v[192:195], a[172:175], a[136:139], v[192:195]\n"
    "ds_read2st64_b32 v[204:205], v250 offset0:0 offset1:1\n"
    "v_mfma_f32_16x16x32_bf16 v[184:187], a[172:175], a[104:107], v[184:187]\n"
    "s_waitcnt vmcnt(11)\n"
    "v_smfmac_f32_16x16x64_bf16 v[156:159], a[192:195], a[200:207], v152\n"
    "v_mfma_f32_16x16x32_bf16 v[184:187], a[168:171], a[100:103], v[184:187]\n"
    "ds_read2st64_b32 v[206:207], v250 offset0:2 offset1:3\n"
    "v_mfma_f32_16x16x32_bf16 v[176:179], a[168:171], a[68:71], v[176:179]\n"
    "buffer_load_dwordx4 v248, s[8:11], s40 offen offset:1024 sc1 lds\n"
    "v_mfma_f32_16x16x32_bf16 v[176:179], a[172:175], a[72:75], v[176:179]\n"
    "v_mfma_f32_16x16x32_bf16 v[168:171], a[172:175], a[40:43], v[168:171]\n"
    "s_waitcnt vmcnt(10)\n"
    "v_smfmac_f32_16x16x64_bf16 v[156:159], a[192:195], a[208:215], v152\n"
    "ds_read2st64_b32 v[212:213], v250 offset0:16 offset1:17\n"
    "v_mfma_f32_16x16x32_bf16 v[168:171], a[168:171], a[36:39], v[168:171]\n"
    "v_mfma_f32_16x16x32_bf16 v[160:163], a[168:171], a[4:7], v[160:163]\n"
    "ds_read_b128 a[168:171], v241 offset:2048\n"
    "buffer_load_dwordx4 v248, s[8:11], s40 offen offset:2048 sc1 lds\n"
    "v_mfma_f32_16x16x32_bf16 v[160:163], a[172:175], a[8:11], v[160:163]\n"
    "ds_read_b128 a[172:175], v240 offset:6144\n"
    "v_mfma_f32_16x16x32_bf16 v[160:163], a[176:179], a[12:15], v[160:163]\n"
    "s_waitcnt vmcnt(9)\n"
    "ds_read2st64_b32 v[214:215], v250 offset0:18 offset1:19\n"
    "v_smfmac_f32_16x16x64_bf16 v[156:159], a[192:195], a[216:223], v152\n"
    "v_mfma_f32_16x16x32_bf16 v[160:163], a[180:183], a[16:19], v[160:163]\n"
    "v_mfma_f32_16x16x32_bf16 v[168:171], a[180:183], a[48:51], v[168:171]\n"
    "buffer_load_dwordx4 v248, s[8:11], s40 offen offset:3072 sc1 lds\n"
    "v_mfma_f32_16x16x32_bf16 v[168:171], a[176:179], a[44:47], v[168:171]\n"
    "s_add_u32 m0, s43, 36864\n"
    "v_mfma_f32_16x16x32_bf16 v[176:179], a[176:179], a[76:79], v[176:179]\n"
    "s_add_u32 s40, s40, 0x8000\n"
    "ds_read2st64_b32 v[220:221], v250 offset0:32 offset1:33\n"
    "v_mfma_f32_16x16x32_bf16 v[176:179], a[180:183], a[80:83], v[176:179]\n"
    "v_mfma_f32_16x16x32_bf16 v[184:187], a[180:183], a[112:115], v[184:187]\n"
    "v_mfma_f32_16x16x32_bf16 v[184:187], a[176:179], a[108:111], v[184:187]\n"
    "buffer_load_dwordx4 v248, s[8:11], s41 offen offset:0 sc1 lds\n"
    "v_mfma_f32_16x16x32_bf16 v[192:195], a[176:179], a[140:143], v[192:195]\n"
    "ds_read_b128 a[176:179], v241 offset:6144\n"
    "v_mfma_f32_16x16x32_bf16 v[192:195], a[180:183], a[144:147], v[192:195]\n"
    "ds_read_b128 a[180:183], v242 offset:2048\n"
    "v_mfma_f32_16x16x32_bf16 v[192:195], a[184:187], a[148:151], v[192:195]\n"
    "ds_read2st64_b32 v[222:223], v250 offset0:34 offset1:35\n"
    "v_mfma_f32_16x16x32_bf16 v[192:195], a[188:191], a[152:155], v[192:195]\n"
    "v_mfma_f32_16x16x32_bf16 v[184:187], a[188:191], a[120:123], v[184:187]\n"
    "buffer_load_dwordx4 v248, s[8:11], s41 offen offset:1024 sc1 lds\n"
    "v_mfma_f32_16x16x32_bf16 v[184:187], a[184:187], a[116:119], v[184:187]\n"
    "ds_read2st64_b32 v[228:229], v250 offset0:48 offset1:49\n"
    "v_mfma_f32_16x16x32_bf16 v[176:179], a[184:187], a[84:87], v[176:179]\n"
    "s_waitcnt vmcnt(10)\n"
    "v_exp_f32_e32 v192, v192\n"
    "v_smfmac_f32_16x16x64_bf16 v[156:159], a[192:195], a[224:231], v152\n"
    "v_exp_f32_e32 v193, v193\n"
    "v_mfma_f32_16x16x32_bf16 v[176:179], a[188:191], a[88:91], v[176:179]\n"
    "v_exp_f32_e32 v194, v194\n"
    "v_mfma_f32_16x16x32_bf16 v[168:171], a[188:191], a[56:59], v[168:171]\n"
    "buffer_load_dwordx4 v248, s[8:11], s41 offen offset:2048 sc1 lds\n"
    "v_mfma_f32_16x16x32_bf16 v[168:171], a[184:187], a[52:55], v[168:171]\n"
    "ds_read2st64_b32 v[230:231], v250 offset0:50 offset1:51\n"
    "v_mfma_f32_16x16x32_bf16 v[160:163], a[184:187], a[20:23], v[160:163]\n"
    "s_waitcnt vmcnt(9)\n"
    "ds_read_b128 a[184:187], v243 offset:2048\n"
    "v_smfmac_f32_16x16x64_bf16 v[156:159], a[192:195], a[232:239], v152\n"
    "ds_read2st64_b32 v[236:237], v250 offset0:64 offset1:65\n"
    "v_mfma_f32_16x16x32_bf16 v[160:163], a[188:191], a[24:27], v[160:163]\n"
    "s_waitcnt lgkmcnt(9)\n"
    "ds_read_b128 a[188:191], v242 offset:6144\n"
    "v_mfma_f32_16x16x32_bf16 v[164:167], a[160:163], a[28:31], 0\n"
    "buffer_load_dwordx4 v248, s[8:11], s41 offen offset:3072 sc1 lds\n"
    "v_mfma_f32_16x16x32_bf16 v[164:167], a[164:167], a[0:3], v[164:167]\n"
    "s_add_u32 s41, s41, 0x8000\n"
    "v_exp_f32_e32 v195, v195\n"
    "v_mfma_f32_16x16x32_bf16 v[172:175], a[164:167], a[32:35], 0\n"
    "s_waitcnt vmcnt(8)\n"
    "v_cvt_pk_bf16_f32 v192, v192, v193\n"
    "v_smfmac_f32_16x16x64_bf16 v[156:159], a[192:195], a[240:247], v152\n"
    "v_exp_f32_e32 v184, v184\n"
    "v_mfma_f32_16x16x32_bf16 v[172:175], a[160:163], a[60:63], v[172:175]\n"
    "ds_read2st64_b32 v[238:239], v250 offset0:66 offset1:67\n"
    "v_mfma_f32_16x16x32_bf16 v[180:183], a[160:163], a[92:95], 0\n"
    "v_exp_f32_e32 v185, v185\n"
    "v_mfma_f32_16x16x32_bf16 v[180:183], a[164:167], a[64:67], v[180:183]\n"
    "v_cvt_pk_bf16_f32 v193, v194, v195\n"
    "v_mfma_f32_16x16x32_bf16 v[188:191], a[164:167], a[96:99], 0\n"
    "v_exp_f32_e32 v186, v186\n"
    "v_mfma_f32_16x16x32_bf16 v[188:191], a[160:163], a[124:127], v[188:191]\n"
    "v_exp_f32_e32 v187, v187\n"
    "v_mfma_f32_16x16x32_bf16 v[196:199], a[160:163], a[156:159], 0\n"
    "ds_read_b64_tr_b16 a[160:161], v244 offset:49152\n"
    "v_and_b32_e32 v253, 0xffff0000, v255\n"
    "v_mfma_f32_16x16x32_bf16 v[196:199], a[164:167], a[128:131], v[196:199]\n"
    "ds_read_b64_tr_b16 a[162:163], v244 offset:51200\n"
    "v_mfma_f32_16x16x32_bf16 v[196:199], a[168:171], a[132:135], v[196:199]\n"
    "ds_read_b64_tr_b16 a[164:165], v244 offset:57344\n"
    "v_mfma_f32_16x16x32_bf16 v[196:199], a[172:175], a[136:139], v[196:199]\n"
    "ds_read_b64_tr_b16 a[166:167], v244 offset:59392\n"
    "v_mfma_f32_16x16x32_bf16 v[188:191], a[172:175], a[104:107], v[188:191]\n"
    "v_cvt_pk_bf16_f32 v184, v184, v185\n"
    "v_mfma_f32_16x16x32_bf16 v[188:191], a[168:171], a[100:103], v[188:191]\n"
    "v_exp_f32_e32 v176, v176\n"
    "v_mfma_f32_16x16x32_bf16 v[180:183], a[168:171], a[68:71], v[180:183]\n"
    "v_exp_f32_e32 v177, v177\n"
    "v_mfma_f32_16x16x32_bf16 v[180:183], a[172:175], a[72:75], v[180:183]\n"
    "v_cvt_pk_bf16_f32 v185, v186, v187\n"
    "v_mul_f32_e32 v156, v253, v156\n"
    "v_mfma_f32_16x16x32_bf16 v[172:175], a[172:175], a[40:43], v[172:175]\n"
    "v_exp_f32_e32 v178, v178\n"
    "v_mfma_f32_16x16x32_bf16 v[172:175], a[168:171], a[36:39], v[172:175]\n"
    "v_exp_f32_e32 v179, v179\n"
    "v_mfma_f32_16x16x32_bf16 v[164:167], a[168:171], a[4:7], v[164:167]\n"
    "ds_read_b64_tr_b16 a[168:169], v245 offset:49152\n"
    "v_mfma_f32_16x16x32_bf16 v[164:167], a[172:175], a[8:11], v[164:167]\n"
    "s_waitcnt lgkmcnt(6)\n"
    "ds_read_b64_tr_b16 a[170:171], v245 offset:51200\n"
    "v_mfma_f32_16x16x32_bf16 v[164:167], a[176:179], a[12:15], v[164:167]\n"
    "ds_read_b64_tr_b16 a[172:173], v245 offset:57344\n"
    "v_mul_f32_e32 v157, v253, v157\n"
    "v_mfma_f32_16x16x32_bf16 v[164:167], a[180:183], a[16:19], v[164:167]\n"
    "ds_read_b64_tr_b16 a[174:175], v245 offset:59392\n"
    "v_mfma_f32_16x16x32_bf16 v[172:175], a[180:183], a[48:51], v[172:175]\n"
    "v_cvt_pk_bf16_f32 v176, v176, v177\n"
    "v_mfma_f32_16x16x32_bf16 v[172:175], a[176:179], a[44:47], v[172:175]\n"
    "v_exp_f32_e32 v168, v168\n"
    "v_mfma_f32_16x16x32_bf16 v[180:183], a[176:179], a[76:79], v[180:183]\n"
    "v_mul_f32_e32 v158, v253, v158\n"
    "v_mfma_f32_16x16x32_bf16 v[180:183], a[180:183], a[80:83], v[180:183]\n"
    "v_exp_f32_e32 v169, v169\n"
    "v_mfma_f32_16x16x32_bf16 v[188:191], a[180:183], a[112:115], v[188:191]\n"
    "v_cvt_pk_bf16_f32 v177, v178, v179\n"
    "v_mfma_f32_16x16x32_bf16 v[188:191], a[176:179], a[108:111], v[188:191]\n"
    "v_exp_f32_e32 v170, v170\n"
    "v_mfma_f32_16x16x32_bf16 v[196:199], a[176:179], a[140:143], v[196:199]\n"
    "ds_read_b64_tr_b16 a[176:177], v246 offset:49152\n"
    "v_mfma_f32_16x16x32_bf16 v[196:199], a[180:183], a[144:147], v[196:199]\n"
    "ds_read_b64_tr_b16 a[178:179], v246 offset:51200\n"
    "v_mfma_f32_16x16x32_bf16 v[196:199], a[184:187], a[148:151], v[196:199]\n"
    "ds_read_b64_tr_b16 a[180:181], v246 offset:57344\n"
    "v_mfma_f32_16x16x32_bf16 v[196:199], a[188:191], a[152:155], v[196:199]\n"
    "ds_read_b64_tr_b16 a[182:183], v246 offset:59392\n"
    "v_mul_f32_e32 v159, v253, v159\n"
    "v_mfma_f32_16x16x32_bf16 v[188:191], a[188:191], a[120:123], v[188:191]\n"
    "v_exp_f32_e32 v171, v171\n"
    "v_mfma_f32_16x16x32_bf16 v[188:191], a[184:187], a[116:119], v[188:191]\n"
    "v_cvt_pk_bf16_f32 v168, v168, v169\n"
    "v_mfma_f32_16x16x32_bf16 v[180:183], a[184:187], a[84:87], v[180:183]\n"
    "v_exp_f32_e32 v160, v160\n"
    "v_mfma_f32_16x16x32_bf16 v[180:183], a[188:191], a[88:91], v[180:183]\n"
    "v_exp_f32_e32 v161, v161\n"
    "v_mfma_f32_16x16x32_bf16 v[172:175], a[188:191], a[56:59], v[172:175]\n"
    "v_cvt_pk_bf16_f32 v169, v170, v171\n"
    "v_mfma_f32_16x16x32_bf16 v[172:175], a[184:187], a[52:55], v[172:175]\n"
    "v_exp_f32_e32 v162, v162\n"
    "v_mfma_f32_16x16x32_bf16 v[164:167], a[184:187], a[20:23], v[164:167]\n"
    "ds_read_b64_tr_b16 a[184:185], v247 offset:49152\n"
    "v_mad_u32_u16 v253, v255, s42, v153 op_sel:[0,0,0,0]\n"
    "v_mfma_f32_16x16x32_bf16 v[164:167], a[188:191], a[24:27], v[164:167]\n"
    "s_waitcnt lgkmcnt(5)\n"
    "ds_read_b64_tr_b16 a[186:187], v247 offset:51200\n"
    "v_mfma_f32_16x16x32_bf16 v[0:3], a[160:163], v[200:203], v[0:3]\n"
    "ds_read_b64_tr_b16 a[188:189], v247 offset:57344\n"
    "v_mfma_f32_16x16x32_bf16 v[0:3], a[164:167], v[204:207], v[0:3]\n"
    "ds_read_b64_tr_b16 a[190:191], v247 offset:59392\n"
    "v_mfma_f32_16x16x32_bf16 a[248:251], a[164:167], v[212:215], a[248:251]\n"
    "v_exp_f32_e32 v163, v163\n"
    "v_mfma_f32_16x16x32_bf16 a[248:251], a[160:163], v[208:211], a[248:251]\n"
    "v_cvt_pk_bf16_f32 v160, v160, v161\n"
    "v_mfma_f32_16x16x32_bf16 v[56:59], a[160:163], v[216:219], v[56:59]\n"
    "v_cvt_pk_bf16_f32 v154, v156, v157\n"
    "v_mfma_f32_16x16x32_bf16 v[56:59], a[164:167], v[220:223], v[56:59]\n"
    "v_cvt_pk_bf16_f32 v161, v162, v163\n"
    "v_mfma_f32_16x16x32_bf16 v[60:63], a[164:167], v[228:231], v[60:63]\n"
    "v_exp_f32_e32 v196, v196\n"
    "v_mfma_f32_16x16x32_bf16 v[60:63], a[160:163], v[224:227], v[60:63]\n"
    "v_exp_f32_e32 v197, v197\n"
    "v_mfma_f32_16x16x32_bf16 v[64:67], a[160:163], v[232:235], v[64:67]\n"
    "ds_read_b64_tr_b16 a[160:161], v244 offset:53248\n"
    "v_mfma_f32_16x16x32_bf16 v[64:67], a[164:167], v[236:239], v[64:67]\n"
    "ds_read_b64_tr_b16 a[162:163], v244 offset:55296\n"
    "v_cvt_pk_bf16_f32 v155, v158, v159\n"
    "v_mfma_f32_16x16x32_bf16 v[76:79], a[168:171], v[232:235], v[76:79]\n"
    "ds_read_b64_tr_b16 a[164:165], v244 offset:61440\n"
    "v_mfma_f32_16x16x32_bf16 v[76:79], a[172:175], v[236:239], v[76:79]\n"
    "ds_read_b64_tr_b16 a[166:167], v244 offset:63488\n"
    "v_mfma_f32_16x16x32_bf16 v[72:75], a[172:175], v[228:231], v[72:75]\n"
    "v_exp_f32_e32 v198, v198\n"
    "v_mfma_f32_16x16x32_bf16 v[72:75], a[168:171], v[224:227], v[72:75]\n"
    "v_exp_f32_e32 v199, v199\n"
    "v_mfma_f32_16x16x32_bf16 v[68:71], a[168:171], v[216:219], v[68:71]\n"
    "v_cvt_pk_bf16_f32 v194, v196, v197\n"
    "v_mov_b32_e32 v156, 0\n"
    "v_mfma_f32_16x16x32_bf16 v[68:71], a[172:175], v[220:223], v[68:71]\n"
    "v_exp_f32_e32 v188, v188\n"
    "v_mfma_f32_16x16x32_bf16 a[252:255], a[172:175], v[212:215], a[252:255]\n"
    "v_exp_f32_e32 v189, v189\n"
    "v_mfma_f32_16x16x32_bf16 a[252:255], a[168:171], v[208:211], a[252:255]\n"
    "v_cvt_pk_bf16_f32 v195, v198, v199\n"
    "v_mfma_f32_16x16x32_bf16 v[4:7], a[168:171], v[200:203], v[4:7]\n"
    "ds_read_b64_tr_b16 a[168:169], v245 offset:53248\n"
    "v_mfma_f32_16x16x32_bf16 v[4:7], a[172:175], v[204:207], v[4:7]\n"
    "s_waitcnt lgkmcnt(5)\n"
    "ds_read_b64_tr_b16 a[170:171], v245 offset:55296\n"
    "v_mfma_f32_16x16x32_bf16 v[8:11], a[176:179], v[200:203], v[8:11]\n"
    "ds_read_b64_tr_b16 a[172:173], v245 offset:61440\n"
    "v_mfma_f32_16x16x32_bf16 v[8:11], a[180:183], v[204:207], v[8:11]\n"
    "ds_read_b64_tr_b16 a[174:175], v245 offset:63488\n"
    "v_mov_b32_e32 v157, 0\n"
    "v_mfma_f32_16x16x32_bf16 v[32:35], a[180:183], v[212:215], v[32:35]\n"
    "v_exp_f32_e32 v190, v190\n"
    "v_mfma_f32_16x16x32_bf16 v[32:35], a[176:179], v[208:211], v[32:35]\n"
    "v_exp_f32_e32 v191, v191\n"
    "v_mfma_f32_16x16x32_bf16 v[80:83], a[176:179], v[216:219], v[80:83]\n"
    "v_mov_b32_e32 v158, 0\n"
    "v_mfma_f32_16x16x32_bf16 v[80:83], a[180:183], v[220:223], v[80:83]\n"
    "v_cvt_pk_bf16_f32 v186, v188, v189\n"
    "v_mfma_f32_16x16x32_bf16 v[84:87], a[180:183], v[228:231], v[84:87]\n"
    "v_exp_f32_e32 v180, v180\n"
    "v_mfma_f32_16x16x32_bf16 v[84:87], a[176:179], v[224:227], v[84:87]\n"
    "v_exp_f32_e32 v181, v181\n"
    "v_mfma_f32_16x16x32_bf16 v[88:91], a[176:179], v[232:235], v[88:91]\n"
    "ds_read_b64_tr_b16 a[176:177], v246 offset:53248\n"
    "v_mfma_f32_16x16x32_bf16 v[88:91], a[180:183], v[236:239], v[88:91]\n"
    "ds_read_b64_tr_b16 a[178:179], v246 offset:55296\n"
    "v_mfma_f32_16x16x32_bf16 v[100:103], a[184:187], v[232:235], v[100:103]\n"
    "ds_read_b64_tr_b16 a[180:181], v246 offset:61440\n"
    "v_mfma_f32_16x16x32_bf16 v[100:103], a[188:191], v[236:239], v[100:103]\n"
    "ds_read_b64_tr_b16 a[182:183], v246 offset:63488\n"
    "v_mfma_f32_16x16x32_bf16 v[96:99], a[188:191], v[228:231], v[96:99]\n"
    "v_cvt_pk_bf16_f32 v187, v190, v191\n"
    "v_mov_b32_e32 v159, 0\n"
    "v_mfma_f32_16x16x32_bf16 v[96:99], a[184:187], v[224:227], v[96:99]\n"
    "v_exp_f32_e32 v182, v182\n"
    "v_mfma_f32_16x16x32_bf16 v[92:95], a[184:187], v[216:219], v[92:95]\n"
    "v_exp_f32_e32 v183, v183\n"
    "v_mfma_f32_16x16x32_bf16 v[92:95], a[188:191], v[220:223], v[92:95]\n"
    "v_cvt_pk_bf16_f32 v178, v180, v181\n"
    "v_mfma_f32_16x16x32_bf16 v[36:39], a[188:191], v[212:215], v[36:39]\n"
    "v_exp_f32_e32 v172, v172\n"
    "v_mfma_f32_16x16x32_bf16 v[36:39], a[184:187], v[208:211], v[36:39]\n"
    "v_exp_f32_e32 v173, v173\n"
    "v_mfma_f32_16x16x32_bf16 v[12:15], a[184:187], v[200:203], v[12:15]\n"
    "s_mov_b32 m0, s49\n"
    "ds_read_b64_tr_b16 a[184:185], v247 offset:53248\n"
    "v_mfma_f32_16x16x32_bf16 v[12:15], a[188:191], v[204:207], v[12:15]\n"
    "s_waitcnt lgkmcnt(5)\n"
    "ds_read_b64_tr_b16 a[186:187], v247 offset:55296\n"
    "v_add_u32_e32 v252, 0x80, v252\n"
    "v_mfma_f32_16x16x32_bf16 v[16:19], a[160:163], v[200:203], v[16:19]\n"
    "ds_read_b64_tr_b16 a[188:189], v247 offset:61440\n"
    "v_mfma_f32_16x16x32_bf16 v[16:19], a[164:167], v[204:207], v[16:19]\n"
    "ds_read_b64_tr_b16 a[190:191], v247 offset:63488\n"
    "v_mfma_f32_16x16x32_bf16 v[40:43], a[164:167], v[212:215], v[40:43]\n"
    "buffer_store_dwordx2 v[154:155], v253, s[24:27], 0 offen offset:0 sc1\n"
    "v_cvt_pk_bf16_f32 v179, v182, v183\n"
    "v_mfma_f32_16x16x32_bf16 v[40:43], a[160:163], v[208:211], v[40:43]\n"
    "v_exp_f32_e32 v174, v174\n"
    "v_mfma_f32_16x16x32_bf16 v[104:107], a[160:163], v[216:219], v[104:107]\n"
    "v_exp_f32_e32 v175, v175\n"
    "v_mfma_f32_16x16x32_bf16 v[104:107], a[164:167], v[220:223], v[104:107]\n"
    "s_waitcnt vmcnt(9)\n"
    "v_mfma_f32_16x16x32_bf16 v[108:111], a[164:167], v[228:231], v[108:111]\n"
    "s_barrier\n"
    "v_mfma_f32_16x16x32_bf16 v[108:111], a[160:163], v[224:227], v[108:111]\n"
    "ds_write_addtid_b32 v192 offset:16384\n"
    "v_cvt_pk_bf16_f32 v170, v172, v173\n"
    "v_mfma_f32_16x16x32_bf16 v[112:115], a[160:163], v[232:235], v[112:115]\n"
    "ds_read_b128 a[160:163], v243 offset:20480\n"
    "v_mfma_f32_16x16x32_bf16 v[112:115], a[164:167], v[236:239], v[112:115]\n"
    "ds_read_b128 a[164:167], v240 offset:16384\n"
    "v_mfma_f32_16x16x32_bf16 v[124:127], a[168:171], v[232:235], v[124:127]\n"
    "v_exp_f32_e32 v164, v164\n"
    "v_mfma_f32_16x16x32_bf16 v[124:127], a[172:175], v[236:239], v[124:127]\n"
    "v_exp_f32_e32 v165, v165\n"
    "v_mfma_f32_16x16x32_bf16 v[120:123], a[172:175], v[228:231], v[120:123]\n"
    "ds_write_addtid_b32 v193 offset:16640\n"
    "v_cvt_pk_bf16_f32 v171, v174, v175\n"
    "v_mfma_f32_16x16x32_bf16 v[120:123], a[168:171], v[224:227], v[120:123]\n"
    "ds_write_addtid_b32 v184 offset:12288\n"
    "v_mfma_f32_16x16x32_bf16 v[116:119], a[168:171], v[216:219], v[116:119]\n"
    "v_exp_f32_e32 v166, v166\n"
    "v_mfma_f32_16x16x32_bf16 v[116:119], a[172:175], v[220:223], v[116:119]\n"
    "v_exp_f32_e32 v167, v167\n"
    "v_mfma_f32_16x16x32_bf16 v[44:47], a[172:175], v[212:215], v[44:47]\n"
    "ds_write_addtid_b32 v185 offset:12544\n"
    "v_cvt_pk_bf16_f32 v162, v164, v165\n"
    "v_mfma_f32_16x16x32_bf16 v[44:47], a[168:171], v[208:211], v[44:47]\n"
    "v_cvt_pk_bf16_f32 v163, v166, v167\n"
    "v_mfma_f32_16x16x32_bf16 v[20:23], a[168:171], v[200:203], v[20:23]\n"
    "ds_read_b128 a[168:171], v241 offset:16384\n"
    "v_mfma_f32_16x16x32_bf16 v[20:23], a[172:175], v[204:207], v[20:23]\n"
    "s_waitcnt lgkmcnt(7)\n"
    "ds_read_b128 a[172:175], v240 offset:20480\n"
    "v_mfma_f32_16x16x32_bf16 v[24:27], a[176:179], v[200:203], v[24:27]\n"
    "ds_write_addtid_b32 v176 offset:8192\n"
    "v_mfma_f32_16x16x32_bf16 v[24:27], a[180:183], v[204:207], v[24:27]\n"
    "ds_write_addtid_b32 v177 offset:8448\n"
    "v_mfma_f32_16x16x32_bf16 v[48:51], a[180:183], v[212:215], v[48:51]\n"
    "ds_write_addtid_b32 v168 offset:4096\n"
    "v_mfma_f32_16x16x32_bf16 v[48:51], a[176:179], v[208:211], v[48:51]\n"
    "ds_write_addtid_b32 v169 offset:4352\n"
    "v_mfma_f32_16x16x32_bf16 v[128:131], a[176:179], v[216:219], v[128:131]\n"
    "ds_write_addtid_b32 v160 offset:0\n"
    "v_mfma_f32_16x16x32_bf16 v[128:131], a[180:183], v[220:223], v[128:131]\n"
    "ds_write_addtid_b32 v161 offset:256\n"
    "v_mfma_f32_16x16x32_bf16 v[132:135], a[180:183], v[228:231], v[132:135]\n"
    "ds_write_addtid_b32 v194 offset:16896\n"
    "v_mfma_f32_16x16x32_bf16 v[132:135], a[176:179], v[224:227], v[132:135]\n"
    "ds_write_addtid_b32 v195 offset:17152\n"
    "v_mfma_f32_16x16x32_bf16 v[136:139], a[176:179], v[232:235], v[136:139]\n"
    "ds_read_b128 a[176:179], v241 offset:20480\n"
    "v_mfma_f32_16x16x32_bf16 v[136:139], a[180:183], v[236:239], v[136:139]\n"
    "ds_read_b128 a[180:183], v242 offset:16384\n"
    "v_mfma_f32_16x16x32_bf16 v[148:151], a[184:187], v[232:235], v[148:151]\n"
    "ds_write_addtid_b32 v186 offset:12800\n"
    "v_mfma_f32_16x16x32_bf16 v[148:151], a[188:191], v[236:239], v[148:151]\n"
    "ds_write_addtid_b32 v187 offset:13056\n"
    "v_mfma_f32_16x16x32_bf16 v[144:147], a[188:191], v[228:231], v[144:147]\n"
    "ds_write_addtid_b32 v178 offset:8704\n"
    "v_mfma_f32_16x16x32_bf16 v[144:147], a[184:187], v[224:227], v[144:147]\n"
    "ds_write_addtid_b32 v179 offset:8960\n"
    "v_mfma_f32_16x16x32_bf16 v[140:143], a[184:187], v[216:219], v[140:143]\n"
    "s_sub_u32 s44, s44, 1\n"
    "ds_write_addtid_b32 v170 offset:4608\n"
    "v_mfma_f32_16x16x32_bf16 v[140:143], a[188:191], v[220:223], v[140:143]\n"
    "ds_write_addtid_b32 v171 offset:4864\n"
    "v_mfma_f32_16x16x32_bf16 v[52:55], a[188:191], v[212:215], v[52:55]\n"
    "ds_write_addtid_b32 v162 offset:512\n"
    "v_mfma_f32_16x16x32_bf16 v[52:55], a[184:187], v[208:211], v[52:55]\n"
    "ds_write_addtid_b32 v163 offset:768\n"
    "v_mfma_f32_16x16x32_bf16 v[28:31], a[184:187], v[200:203], v[28:31]\n"
    "ds_read_b128 a[184:187], v243 offset:16384\n"
    "v_mfma_f32_16x16x32_bf16 v[28:31], a[188:191], v[204:207], v[28:31]\n"
    "s_waitcnt lgkmcnt(15)\n"
    "ds_read_b128 a[188:191], v242 offset:20480\n"
    "s_cbranch_scc0 .Lat_g\n"
    "s_mov_b32 s44, 1\n"
    "s_cmp_eq_u32 s34, 0\n"
    "s_cbranch_scc1 .Lat_p\n"
    ".Lat_tail:\n"
    "v_mfma_f32_16x16x32_bf16 v[216:219], a[160:163], a[92:95], 0\n"
    "s_mov_b32 s45, 5693440\n"
    "v_mbcnt_lo_u32_b32 v253, -1, 0\n"
    "v_mfma_f32_16x16x32_bf16 v[216:219], a[164:167], a[64:67], v[216:219]\n"
    "s_add_u32 m0, s43, 49152\n"
    "v_mbcnt_hi_u32_b32 v253, -1, v253\n"
    "v_mfma_f32_16x16x32_bf16 v[224:227], a[164:167], a[96:99], 0\n"
    "buffer_load_dwordx4 v248, s[8:11], s40 offen offset:0 sc1 lds\n"
    "v_mfma_f32_16x16x32_bf16 v[224:227], a[160:163], a[124:127], v[224:227]\n"
    "s_lshl_b32 s56, %2, 15\n"
    "v_and_b32_e32 v254, 3, v253\n"
    "v_mfma_f32_16x16x32_bf16 v[232:235], a[160:163], a[156:159], 0\n"
    "ds_read_b128 a[160:163], v243 offset:22528\n"
    "v_mfma_f32_16x16x32_bf16 v[232:235], a[164:167], a[128:131], v[232:235]\n"
    "ds_read_b128 a[164:167], v240 offset:18432\n"
    "v_mfma_f32_16x16x32_bf16 v[232:235], a[168:171], a[132:135], v[232:235]\n"
    "s_waitcnt lgkmcnt(0)\n"
    "s_barrier\n"
    "v_mfma_f32_16x16x32_bf16 v[232:235], a[172:175], a[136:139], v[232:235]\n"
    "buffer_load_dwordx4 v248, s[8:11], s40 offen offset:1024 sc1 lds\n"
    "v_mfma_f32_16x16x32_bf16 v[224:227], a[172:175], a[104:107], v[224:227]\n"
    "s_add_u32 s57, s56, 5505024\n"
    "v_lshlrev_b32_e32 v254, 2, v254\n"
    "v_mfma_f32_16x16x32_bf16 v[224:227], a[168:171], a[100:103], v[224:227]\n"
    "s_add_u32 s56, s56, 5373952\n"
    "ds_read2st64_b32 v[164:165], v250 offset0:0 offset1:1\n"
    "v_and_b32_e32 v158, 15, v253\n"
    "v_mfma_f32_16x16x32_bf16 v[216:219], a[168:171], a[68:71], v[216:219]\n"
    "ds_read_b128 a[168:171], v241 offset:18432\n"
    "v_mfma_f32_16x16x32_bf16 v[216:219], a[172:175], a[72:75], v[216:219]\n"
    "ds_read_b128 a[172:175], v240 offset:22528\n"
    "v_mfma_f32_16x16x32_bf16 v[216:219], a[176:179], a[76:79], v[216:219]\n"
    "buffer_load_dwordx4 v248, s[8:11], s40 offen offset:2048 sc1 lds\n"
    "ds_read2st64_b32 v[166:167], v250 offset0:2 offset1:3\n"
    "v_mfma_f32_16x16x32_bf16 v[216:219], a[180:183], a[80:83], v[216:219]\n"
    "s_lshl_b32 s47, %2, 7\n"
    "v_lshlrev_b32_e32 v158, 9, v158\n"
    "v_mfma_f32_16x16x32_bf16 v[224:227], a[180:183], a[112:115], v[224:227]\n"
    "s_add_u32 s47, s47, s35\n"
    "v_and_or_b32 v158, v253, 48, v158\n"
    "v_mfma_f32_16x16x32_bf16 v[224:227], a[176:179], a[108:111], v[224:227]\n"
    "ds_read2st64_b32 v[172:173], v250 offset0:16 offset1:17\n"
    "v_bfe_u32 v252, v253, 4, 2\n"
    "v_mfma_f32_16x16x32_bf16 v[232:235], a[176:179], a[140:143], v[232:235]\n"
    "ds_read_b128 a[176:179], v241 offset:22528\n"
    "buffer_load_dwordx4 v248, s[8:11], s40 offen offset:3072 sc1 lds\n"
    "v_mfma_f32_16x16x32_bf16 v[232:235], a[180:183], a[144:147], v[232:235]\n"
    "s_add_u32 m0, s43, 53248\n"
    "ds_read_b128 a[180:183], v242 offset:18432\n"
    "v_mfma_f32_16x16x32_bf16 v[232:235], a[184:187], a[148:151], v[232:235]\n"
    "v_lshlrev_b32_e32 v252, 1, v252\n"
    "v_mfma_f32_16x16x32_bf16 v[232:235], a[188:191], a[152:155], v[232:235]\n"
    "ds_read2st64_b32 v[174:175], v250 offset0:18 offset1:19\n"
    "v_xor_b32_e32 v252, v252, v253\n"
    "v_mfma_f32_16x16x32_bf16 v[224:227], a[188:191], a[120:123], v[224:227]\n"
    "buffer_load_dwordx4 v248, s[8:11], s41 offen offset:0 sc1 lds\n"
    "v_mfma_f32_16x16x32_bf16 v[224:227], a[184:187], a[116:119], v[224:227]\n"
    "v_and_b32_e32 v252, 7, v252\n"
    "v_mfma_f32_16x16x32_bf16 v[216:219], a[184:187], a[84:87], v[216:219]\n"
    "ds_read_b128 a[184:187], v243 offset:18432\n"
    "v_mfma_f32_16x16x32_bf16 v[216:219], a[188:191], a[88:91], v[216:219]\n"
    "ds_read_b128 a[188:191], v242 offset:22528\n"
    "v_mfma_f32_16x16x32_bf16 v[220:223], a[160:163], a[92:95], 0\n"
    "buffer_load_dwordx4 v248, s[8:11], s41 offen offset:1024 sc1 lds\n"
    "ds_read2st64_b32 v[180:181], v250 offset0:32 offset1:33\n"
    "v_mfma_f32_16x16x32_bf16 v[220:223], a[164:167], a[64:67], v[220:223]\n"
    "v_lshlrev_b32_e32 v252, 4, v252\n"
    "v_mfma_f32_16x16x32_bf16 v[228:231], a[164:167], a[96:99], 0\n"
    "ds_read2st64_b32 v[182:183], v250 offset0:34 offset1:35\n"
    "v_lshrrev_b32_e32 v255, 3, v253\n"
    "v_mfma_f32_16x16x32_bf16 v[228:231], a[160:163], a[124:127], v[228:231]\n"
    "v_lshl_add_u32 v252, v255, 9, v252\n"
    "v_mfma_f32_16x16x32_bf16 v[236:239], a[160:163], a[156:159], 0\n"
    "ds_read_b64_tr_b16 a[160:161], v244 offset:0\n"
    "buffer_load_dwordx4 v248, s[8:11], s41 offen offset:2048 sc1 lds\n"
    "v_mfma_f32_16x16x32_bf16 v[236:239], a[164:167], a[128:131], v[236:239]\n"
    "s_waitcnt lgkmcnt(10)\n"
    "ds_read_b64_tr_b16 a[162:163], v244 offset:2048\n"
    "v_mfma_f32_16x16x32_bf16 v[236:239], a[168:171], a[132:135], v[236:239]\n"
    "ds_read_b64_tr_b16 a[164:165], v244 offset:8192\n"
    "v_mfma_f32_16x16x32_bf16 v[236:239], a[172:175], a[136:139], v[236:239]\n"
    "ds_read_b64_tr_b16 a[166:167], v244 offset:10240\n"
    "v_mfma_f32_16x16x32_bf16 v[228:231], a[172:175], a[104:107], v[228:231]\n"
    "buffer_load_dwordx4 v248, s[8:11], s41 offen offset:3072 sc1 lds\n"
    "ds_read2st64_b32 v[188:189], v250 offset0:48 offset1:49\n"
    "v_mfma_f32_16x16x32_bf16 v[228:231], a[168:171], a[100:103], v[228:231]\n"
    "s_mov_b32 m0, s49\n"
    "ds_read2st64_b32 v[190:191], v250 offset0:50 offset1:51\n"
    "v_lshlrev_b32_e32 v253, 4, v253\n"
    "v_mfma_f32_16x16x32_bf16 v[220:223], a[168:171], a[68:71], v[220:223]\n"
    "ds_read_b64_tr_b16 a[168:169], v245 offset:0\n"
    "v_mfma_f32_16x16x32_bf16 v[220:223], a[172:175], a[72:75], v[220:223]\n"
    "s_waitcnt lgkmcnt(12)\n"
    "ds_read_b64_tr_b16 a[170:171], v245 offset:2048\n"
    "v_mfma_f32_16x16x32_bf16 v[220:223], a[176:179], a[76:79], v[220:223]\n"
    "ds_read_b64_tr_b16 a[172:173], v245 offset:8192\n"
    "buffer_load_dword v255, v254, s[8:11], s45 offen offset:0 sc1\n"
    "v_mfma_f32_16x16x32_bf16 v[220:223], a[180:183], a[80:83], v[220:223]\n"
    "s_add_u32 s61, s47, 0\n"
    "ds_read_b64_tr_b16 a[174:175], v245 offset:10240\n"
    "v_mfma_f32_16x16x32_bf16 v[228:231], a[180:183], a[112:115], v[228:231]\n"
    "ds_read2st64_b32 v[196:197], v250 offset0:64 offset1:65\n"
    "v_mfma_f32_16x16x32_bf16 v[228:231], a[176:179], a[108:111], v[228:231]\n"
    "ds_read2st64_b32 v[198:199], v250 offset0:66 offset1:67\n"
    "v_mfma_f32_16x16x32_bf16 v[236:239], a[176:179], a[140:143], v[236:239]\n"
    "ds_read_b64_tr_b16 a[176:177], v246 offset:0\n"
    "v_mfma_f32_16x16x32_bf16 v[236:239], a[180:183], a[144:147], v[236:239]\n"
    "s_waitcnt lgkmcnt(15)\n"
    "ds_read_b64_tr_b16 a[178:179], v246 offset:2048\n"
    "v_mfma_f32_16x16x32_bf16 v[236:239], a[184:187], a[148:151], v[236:239]\n"
    "ds_read_b64_tr_b16 a[180:181], v246 offset:8192\n"
    "buffer_load_dwordx4 a[224:227], v158, s[12:15], s61 offen offset:0\n"
    "v_mfma_f32_16x16x32_bf16 v[236:239], a[188:191], a[152:155], v[236:239]\n"
    "ds_read_b64_tr_b16 a[182:183], v246 offset:10240\n"
    "v_mfma_f32_16x16x32_bf16 v[228:231], a[188:191], a[120:123], v[228:231]\n"
    "v_exp_f32_e32 v232, v232\n"
    "v_mfma_f32_16x16x32_bf16 v[228:231], a[184:187], a[116:119], v[228:231]\n"
    "v_exp_f32_e32 v233, v233\n"
    "v_mfma_f32_16x16x32_bf16 v[220:223], a[184:187], a[84:87], v[220:223]\n"
    "ds_read_b64_tr_b16 a[184:185], v247 offset:0\n"
    "v_mfma_f32_16x16x32_bf16 v[220:223], a[188:191], a[88:91], v[220:223]\n"
    "s_waitcnt lgkmcnt(5)\n"
    "ds_read_b64_tr_b16 a[186:187], v247 offset:2048\n"
    "v_mfma_f32_16x16x32_bf16 v[56:59], a[160:163], v[176:179], v[56:59]\n"
    "ds_read_b64_tr_b16 a[188:189], v247 offset:8192\n"
    "v_mfma_f32_16x16x32_bf16 v[56:59], a[164:167], v[180:183], v[56:59]\n"
    "ds_read_b64_tr_b16 a[190:191], v247 offset:10240\n"
    "v_mfma_f32_16x16x32_bf16 v[60:63], a[164:167], v[188:191], v[60:63]\n"
    "v_exp_f32_e32 v234, v234\n"
    "v_mfma_f32_16x16x32_bf16 v[60:63], a[160:163], v[184:187], v[60:63]\n"
    "v_exp_f32_e32 v235, v235\n"
    "v_mfma_f32_16x16x32_bf16 v[64:67], a[160:163], v[192:195], v[64:67]\n"
    "ds_read_b64_tr_b16 a[160:161], v244 offset:4096\n"
    "v_cvt_pk_bf16_f32 v232, v232, v233\n"
    "v_mfma_f32_16x16x32_bf16 v[64:67], a[164:167], v[196:199], v[64:67]\n"
    "ds_read_b64_tr_b16 a[162:163], v244 offset:6144\n"
    "v_exp_f32_e32 v224, v224\n"
    "v_mfma_f32_16x16x32_bf16 v[76:79], a[168:171], v[192:195], v[76:79]\n"
    "ds_read_b64_tr_b16 a[164:165], v244 offset:12288\n"
    "buffer_load_dwordx4 a[228:231], v158, s[12:15], s61 offen offset:64\n"
    "v_mfma_f32_16x16x32_bf16 v[76:79], a[172:175], v[196:199], v[76:79]\n"
    "s_add_u32 s61, s47, 8192\n"
    "ds_read_b64_tr_b16 a[166:167], v244 offset:14336\n"
    "v_mfma_f32_16x16x32_bf16 v[72:75], a[172:175], v[188:191], v[72:75]\n"
    "s_waitcnt vmcnt(11)\n"
    "s_barrier\n"
    "v_mfma_f32_16x16x32_bf16 v[72:75], a[168:171], v[184:187], v[72:75]\n"
    "v_exp_f32_e32 v225, v225\n"
    "v_mfma_f32_16x16x32_bf16 v[68:71], a[168:171], v[176:179], v[68:71]\n"
    "ds_read_b64_tr_b16 a[168:169], v245 offset:4096\n"
    "v_cvt_pk_bf16_f32 v233, v234, v235\n"
    "v_mfma_f32_16x16x32_bf16 v[68:71], a[172:175], v[180:183], v[68:71]\n"
    "s_waitcnt lgkmcnt(9)\n"
    "ds_read_b64_tr_b16 a[170:171], v245 offset:6144\n"
    "v_mfma_f32_16x16x32_bf16 v[80:83], a[176:179], v[176:179], v[80:83]\n"
    "ds_read_b64_tr_b16 a[172:173], v245 offset:12288\n"
    "v_mfma_f32_16x16x32_bf16 v[80:83], a[180:183], v[180:183], v[80:83]\n"
    "ds_read_b64_tr_b16 a[174:175], v245 offset:14336\n"
    "v_mfma_f32_16x16x32_bf16 v[84:87], a[180:183], v[188:191], v[84:87]\n"
    "v_exp_f32_e32 v226, v226\n"
    "v_mfma_f32_16x16x32_bf16 v[84:87], a[176:179], v[184:187], v[84:87]\n"
    "v_exp_f32_e32 v227, v227\n"
    "v_mfma_f32_16x16x32_bf16 v[88:91], a[176:179], v[192:195], v[88:91]\n"
    "ds_read_b64_tr_b16 a[176:177], v246 offset:4096\n"
    "v_cvt_pk_bf16_f32 v224, v224, v225\n"
    "v_mfma_f32_16x16x32_bf16 v[88:91], a[180:183], v[196:199], v[88:91]\n"
    "s_waitcnt lgkmcnt(9)\n"
    "ds_read_b64_tr_b16 a[178:179], v246 offset:6144\n"
    "v_mfma_f32_16x16x32_bf16 v[100:103], a[184:187], v[192:195], v[100:103]\n"
    "ds_read_b64_tr_b16 a[180:181], v246 offset:12288\n"
    "buffer_load_dwordx4 a[232:235], v158, s[12:15], s61 offen offset:0\n"
    "v_mfma_f32_16x16x32_bf16 v[100:103], a[188:191], v[196:199], v[100:103]\n"
    "ds_read_b64_tr_b16 a[182:183], v246 offset:14336\n"
    "v_mfma_f32_16x16x32_bf16 v[96:99], a[188:191], v[188:191], v[96:99]\n"
    "v_exp_f32_e32 v216, v216\n"
    "v_mfma_f32_16x16x32_bf16 v[96:99], a[184:187], v[184:187], v[96:99]\n"
    "v_exp_f32_e32 v217, v217\n"
    "v_mfma_f32_16x16x32_bf16 v[92:95], a[184:187], v[176:179], v[92:95]\n"
    "ds_read_b64_tr_b16 a[184:185], v247 offset:4096\n"
    "v_cvt_pk_bf16_f32 v225, v226, v227\n"
    "v_mfma_f32_16x16x32_bf16 v[92:95], a[188:191], v[180:183], v[92:95]\n"
    "s_waitcnt lgkmcnt(9)\n"
    "ds_read_b64_tr_b16 a[186:187], v247 offset:6144\n"
    "ds_write_addtid_b32 v232 offset:16384\n"
    "v_mfma_f32_16x16x32_bf16 v[104:107], a[160:163], v[176:179], v[104:107]\n"
    "ds_read_b64_tr_b16 a[188:189], v247 offset:12288\n"
    "ds_write_addtid_b32 v233 offset:16640\n"
    "v_mfma_f32_16x16x32_bf16 v[104:107], a[164:167], v[180:183], v[104:107]\n"
    "ds_read_b64_tr_b16 a[190:191], v247 offset:14336\n"
    "ds_write_addtid_b32 v224 offset:12288\n"
    "v_mfma_f32_16x16x32_bf16 v[108:111], a[164:167], v[188:191], v[108:111]\n"
    "v_exp_f32_e32 v218, v218\n"
    "v_mfma_f32_16x16x32_bf16 v[108:111], a[160:163], v[184:187], v[108:111]\n"
    "v_exp_f32_e32 v219, v219\n"
    "v_mfma_f32_16x16x32_bf16 v[112:115], a[160:163], v[192:195], v[112:115]\n"
    "ds_read_b128 a[160:163], v243 offset:36864\n"
    "v_cvt_pk_bf16_f32 v216, v216, v217\n"
    "v_mfma_f32_16x16x32_bf16 v[112:115], a[164:167], v[196:199], v[112:115]\n"
    "s_waitcnt lgkmcnt(12)\n"
    "ds_read_b128 a[164:167], v240 offset:32768\n"
    "v_cvt_pk_bf16_f32 v217, v218, v219\n"
    "v_mfma_f32_16x16x32_bf16 v[124:127], a[168:171], v[192:195], v[124:127]\n"
    "buffer_load_dwordx4 a[236:239], v158, s[12:15], s61 offen offset:64\n"
    "v_exp_f32_e32 v236, v236\n"
    "v_mfma_f32_16x16x32_bf16 v[124:127], a[172:175], v[196:199], v[124:127]\n"
    "s_add_u32 s61, s47, 16384\n"
    "ds_write_addtid_b32 v225 offset:12544\n"
    "v_exp_f32_e32 v237, v237\n"
    "v_mfma_f32_16x16x32_bf16 v[120:123], a[172:175], v[188:191], v[120:123]\n"
    "v_exp_f32_e32 v238, v238\n"
    "v_mfma_f32_16x16x32_bf16 v[120:123], a[168:171], v[184:187], v[120:123]\n"
    "v_exp_f32_e32 v239, v239\n"
    "v_mfma_f32_16x16x32_bf16 v[116:119], a[168:171], v[176:179], v[116:119]\n"
    "ds_read_b128 a[168:171], v241 offset:32768\n"
    "v_cvt_pk_bf16_f32 v234, v236, v237\n"
    "v_mfma_f32_16x16x32_bf16 v[116:119], a[172:175], v[180:183], v[116:119]\n"
    "s_waitcnt lgkmcnt(11)\n"
    "ds_read_b128 a[172:175], v240 offset:36864\n"
    "v_exp_f32_e32 v228, v228\n"
    "v_mfma_f32_16x16x32_bf16 v[128:131], a[176:179], v[176:179], v[128:131]\n"
    "v_exp_f32_e32 v229, v229\n"
    "v_mfma_f32_16x16x32_bf16 v[128:131], a[180:183], v[180:183], v[128:131]\n"
    "ds_write_addtid_b32 v216 offset:8192\n"
    "v_cvt_pk_bf16_f32 v235, v238, v239\n"
    "v_mfma_f32_16x16x32_bf16 v[132:135], a[180:183], v[188:191], v[132:135]\n"
    "v_exp_f32_e32 v230, v230\n"
    "v_mfma_f32_16x16x32_bf16 v[132:135], a[176:179], v[184:187], v[132:135]\n"
    "v_exp_f32_e32 v231, v231\n"
    "v_mfma_f32_16x16x32_bf16 v[136:139], a[176:179], v[192:195], v[136:139]\n"
    "ds_read_b128 a[176:179], v241 offset:36864\n"
    "v_cvt_pk_bf16_f32 v226, v228, v229\n"
    "v_mfma_f32_16x16x32_bf16 v[136:139], a[180:183], v[196:199], v[136:139]\n"
    "s_waitcnt lgkmcnt(8)\n"
    "ds_read_b128 a[180:183], v242 offset:32768\n"
    "v_exp_f32_e32 v220, v220\n"
    "v_mfma_f32_16x16x32_bf16 v[148:151], a[184:187], v[192:195], v[148:151]\n"
    "buffer_load_dwordx4 a[240:243], v158, s[12:15], s61 offen offset:0\n"
    "v_exp_f32_e32 v221, v221\n"
    "v_mfma_f32_16x16x32_bf16 v[148:151], a[188:191], v[196:199], v[148:151]\n"
    "ds_write_addtid_b32 v217 offset:8448\n"
    "v_cvt_pk_bf16_f32 v227, v230, v231\n"
    "v_mfma_f32_16x16x32_bf16 v[144:147], a[188:191], v[188:191], v[144:147]\n"
    "v_exp_f32_e32 v222, v222\n"
    "v_mfma_f32_16x16x32_bf16 v[144:147], a[184:187], v[184:187], v[144:147]\n"
    "v_exp_f32_e32 v223, v223\n"
    "v_mfma_f32_16x16x32_bf16 v[140:143], a[184:187], v[176:179], v[140:143]\n"
    "ds_read_b128 a[184:187], v243 offset:32768\n"
    "v_cvt_pk_bf16_f32 v218, v220, v221\n"
    "v_mfma_f32_16x16x32_bf16 v[140:143], a[188:191], v[180:183], v[140:143]\n"
    "s_waitcnt lgkmcnt(8)\n"
    "ds_read_b128 a[188:191], v242 offset:36864\n"
    "v_cvt_pk_bf16_f32 v219, v222, v223\n"
    "v_mfma_f32_16x16x32_bf16 v[176:179], a[160:163], a[92:95], 0\n"
    "s_waitcnt vmcnt(5)\n"
    "ds_write_addtid_b32 v234 offset:16896\n"
    "v_cmp_ne_u32_e32 vcc, s39, v255\n"
    "v_mfma_f32_16x16x32_bf16 v[176:179], a[164:167], a[64:67], v[176:179]\n"
    "ds_write_addtid_b32 v235 offset:17152\n"
    "ds_write_addtid_b32 v226 offset:12800\n"
    "v_mfma_f32_16x16x32_bf16 v[184:187], a[164:167], a[96:99], 0\n"
    "s_cbranch_vccnz .Lat_wpoll\n"
    ".Lat_wpoll_ret:\n"
    "v_mfma_f32_16x16x32_bf16 v[184:187], a[160:163], a[124:127], v[184:187]\n"
    "ds_write_addtid_b32 v227 offset:13056\n"
    "ds_write_addtid_b32 v218 offset:8704\n"
    "v_mfma_f32_16x16x32_bf16 v[192:195], a[160:163], a[156:159], 0\n"
    "ds_read_b128 a[160:163], v243 offset:38912\n"
    "ds_write_addtid_b32 v219 offset:8960\n"
    "v_mfma_f32_16x16x32_bf16 v[192:195], a[164:167], a[128:131], v[192:195]\n"
    "s_waitcnt lgkmcnt(13)\n"
    "ds_read_b128 a[164:167], v240 offset:34816\n"
    "v_mfma_f32_16x16x32_bf16 v[192:195], a[168:171], a[132:135], v[192:195]\n"
    "buffer_load_dwordx4 a[244:247], v158, s[12:15], s61 offen offset:64\n"
    "v_mfma_f32_16x16x32_bf16 v[192:195], a[172:175], a[136:139], v[192:195]\n"
    "v_mfma_f32_16x16x32_bf16 v[184:187], a[172:175], a[104:107], v[184:187]\n"
    "s_waitcnt lgkmcnt(1)\n"
    "s_barrier\n"
    "v_mfma_f32_16x16x32_bf16 v[184:187], a[168:171], a[100:103], v[184:187]\n"
    "v_mfma_f32_16x16x32_bf16 v[176:179], a[168:171], a[68:71], v[176:179]\n"
    "ds_read_b128 a[168:171], v241 offset:34816\n"
    "v_mfma_f32_16x16x32_bf16 v[176:179], a[172:175], a[72:75], v[176:179]\n"
    "ds_read_b128 a[172:175], v240 offset:38912\n"
    "v_mfma_f32_16x16x32_bf16 v[176:179], a[176:179], a[76:79], v[176:179]\n"
    "ds_read2st64_b32 v[220:221], v250 offset0:32 offset1:33\n"
    "v_mfma_f32_16x16x32_bf16 v[176:179], a[180:183], a[80:83], v[176:179]\n"
    "v_mfma_f32_16x16x32_bf16 v[184:187], a[180:183], a[112:115], v[184:187]\n"
    "s_add_u32 s61, s47, 5709824\n"
    "v_mfma_f32_16x16x32_bf16 v[184:187], a[176:179], a[108:111], v[184:187]\n"
    "s_add_u32 m0, s49, 20480\n"
    "ds_read2st64_b32 v[222:223], v250 offset0:34 offset1:35\n"
    "v_mfma_f32_16x16x32_bf16 v[192:195], a[176:179], a[140:143], v[192:195]\n"
    "ds_read_b128 a[176:179], v241 offset:38912\n"
    "v_mfma_f32_16x16x32_bf16 v[192:195], a[180:183], a[144:147], v[192:195]\n"
    "ds_read_b128 a[180:183], v242 offset:34816\n"
    "v_mfma_f32_16x16x32_bf16 v[192:195], a[184:187], a[148:151], v[192:195]\n"
    "buffer_load_dwordx4 v252, s[8:11], s61 offen offset:0 sc1 lds\n"
    "v_mfma_f32_16x16x32_bf16 v[192:195], a[188:191], a[152:155], v[192:195]\n"
    "s_add_u32 s61, s47, 5713920\n"
    "v_mfma_f32_16x16x32_bf16 v[184:187], a[188:191], a[120:123], v[184:187]\n"
    "s_add_u32 m0, s49, 24576\n"
    "v_mfma_f32_16x16x32_bf16 v[184:187], a[184:187], a[116:119], v[184:187]\n"
    "ds_read2st64_b32 v[228:229], v250 offset0:48 offset1:49\n"
    "v_exp_f32_e32 v192, v192\n"
    "v_mfma_f32_16x16x32_bf16 v[176:179], a[184:187], a[84:87], v[176:179]\n"
    "ds_read_b128 a[184:187], v243 offset:34816\n"
    "buffer_load_dwordx4 v252, s[8:11], s61 offen offset:0 sc1 lds\n"
    "v_mfma_f32_16x16x32_bf16 v[176:179], a[188:191], a[88:91], v[176:179]\n"
    "s_waitcnt lgkmcnt(8)\n"
    "s_add_u32 s61, s47, 5718016\n"
    "ds_read_b128 a[188:191], v242 offset:38912\n"
    "v_mfma_f32_16x16x32_bf16 v[180:183], a[160:163], a[92:95], 0\n"
    "s_add_u32 m0, s49, 28672\n"
    "v_exp_f32_e32 v193, v193\n"
    "v_mfma_f32_16x16x32_bf16 v[180:183], a[164:167], a[64:67], v[180:183]\n"
    "v_exp_f32_e32 v194, v194\n"
    "v_mfma_f32_16x16x32_bf16 v[188:191], a[164:167], a[96:99], 0\n"
    "buffer_load_dwordx4 v252, s[8:11], s61 offen offset:0 sc1 lds\n"
    "v_mfma_f32_16x16x32_bf16 v[188:191], a[160:163], a[124:127], v[188:191]\n"
    "s_mov_b32 m0, s49\n"
    "ds_read2st64_b32 v[230:231], v250 offset0:50 offset1:51\n"
    "v_mfma_f32_16x16x32_bf16 v[196:199], a[160:163], a[156:159], 0\n"
    "s_add_u32 s48, s57, 30720\n"
    "ds_read_b64_tr_b16 a[160:161], v244 offset:16384\n"
    "v_mfma_f32_16x16x32_bf16 v[196:199], a[164:167], a[128:131], v[196:199]\n"
    "s_waitcnt lgkmcnt(9)\n"
    "ds_read_b64_tr_b16 a[162:163], v244 offset:18432\n"
    "v_mfma_f32_16x16x32_bf16 v[196:199], a[168:171], a[132:135], v[196:199]\n"
    "ds_read_b64_tr_b16 a[164:165], v244 offset:24576\n"
    "buffer_load_dwordx4 a[192:195], v253, s[8:11], s48 offen offset:0 sc1\n"
    "v_mfma_f32_16x16x32_bf16 v[196:199], a[172:175], a[136:139], v[196:199]\n"
    "ds_read_b64_tr_b16 a[166:167], v244 offset:26624\n"
    "v_mfma_f32_16x16x32_bf16 v[188:191], a[172:175], a[104:107], v[188:191]\n"
    "ds_read2st64_b32 v[236:237], v250 offset0:64 offset1:65\n"
    "v_mfma_f32_16x16x32_bf16 v[188:191], a[168:171], a[100:103], v[188:191]\n"
    "v_exp_f32_e32 v195, v195\n"
    "v_mfma_f32_16x16x32_bf16 v[180:183], a[168:171], a[68:71], v[180:183]\n"
    "ds_read_b64_tr_b16 a[168:169], v245 offset:16384\n"
    "buffer_load_dwordx4 a[196:199], v253, s[8:11], s48 offen offset:1024 sc1\n"
    "v_mfma_f32_16x16x32_bf16 v[180:183], a[172:175], a[72:75], v[180:183]\n"
    "s_waitcnt lgkmcnt(10)\n"
    "s_add_u32 s48, s57, 14336\n"
    "ds_read_b64_tr_b16 a[170:171], v245 offset:18432\n"
    "v_mfma_f32_16x16x32_bf16 v[180:183], a[176:179], a[76:79], v[180:183]\n"
    "ds_read_b64_tr_b16 a[172:173], v245 offset:24576\n"
    "v_cvt_pk_bf16_f32 v192, v192, v193\n"
    "v_mfma_f32_16x16x32_bf16 v[180:183], a[180:183], a[80:83], v[180:183]\n"
    "ds_read_b64_tr_b16 a[174:175], v245 offset:26624\n"
    "v_mfma_f32_16x16x32_bf16 v[188:191], a[180:183], a[112:115], v[188:191]\n"
    "buffer_load_dwordx4 a[200:203], v253, s[8:11], s48 offen offset:0 sc1\n"
    "v_mfma_f32_16x16x32_bf16 v[188:191], a[176:179], a[108:111], v[188:191]\n"
    "ds_read2st64_b32 v[238:239], v250 offset0:66 offset1:67\n"
    "v_mfma_f32_16x16x32_bf16 v[196:199], a[176:179], a[140:143], v[196:199]\n"
    "ds_read_b64_tr_b16 a[176:177], v246 offset:16384\n"
    "v_mfma_f32_16x16x32_bf16 v[196:199], a[180:183], a[144:147], v[196:199]\n"
    "s_waitcnt lgkmcnt(12)\n"
    "ds_read_b64_tr_b16 a[178:179], v246 offset:18432\n"
    "v_mfma_f32_16x16x32_bf16 v[196:199], a[184:187], a[148:151], v[196:199]\n"
    "ds_read_b64_tr_b16 a[180:181], v246 offset:24576\n"
    "buffer_load_dwordx4 a[204:207], v253, s[8:11], s48 offen offset:1024 sc1\n"
    "v_mfma_f32_16x16x32_bf16 v[196:199], a[188:191], a[152:155], v[196:199]\n"
    "s_add_u32 s48, s57, 16384\n"
    "ds_read_b64_tr_b16 a[182:183], v246 offset:26624\n"
    "v_mfma_f32_16x16x32_bf16 v[188:191], a[188:191], a[120:123], v[188:191]\n"
    "v_exp_f32_e32 v184, v184\n"
    "v_mfma_f32_16x16x32_bf16 v[188:191], a[184:187], a[116:119], v[188:191]\n"
    "v_exp_f32_e32 v185, v185\n"
    "v_mfma_f32_16x16x32_bf16 v[180:183], a[184:187], a[84:87], v[180:183]\n"
    "ds_read_b64_tr_b16 a[184:185], v247 offset:16384\n"
    "buffer_load_dwordx4 a[208:211], v253, s[8:11], s48 offen offset:0 sc1\n"
    "v_mfma_f32_16x16x32_bf16 v[180:183], a[188:191], a[88:91], v[180:183]\n"
    "s_waitcnt lgkmcnt(5)\n"
    "ds_read_b64_tr_b16 a[186:187], v247 offset:18432\n"
    "v_cvt_pk_bf16_f32 v193, v194, v195\n"
    "v_mfma_f32_16x16x32_bf16 v[56:59], a[160:163], v[216:219], v[56:59]\n"
    "ds_read_b64_tr_b16 a[188:189], v247 offset:24576\n"
    "v_exp_f32_e32 v186, v186\n"
    "v_mfma_f32_16x16x32_bf16 v[56:59], a[164:167], v[220:223], v[56:59]\n"
    "ds_read_b64_tr_b16 a[190:191], v247 offset:26624\n"
    "v_mfma_f32_16x16x32_bf16 v[60:63], a[164:167], v[228:231], v[60:63]\n"
    "buffer_load_dwordx4 a[212:215], v253, s[8:11], s48 offen offset:1024 sc1\n"
    "v_mfma_f32_16x16x32_bf16 v[60:63], a[160:163], v[224:227], v[60:63]\n"
    "s_add_u32 s48, s57, 0\n"
    "v_exp_f32_e32 v187, v187\n"
    "v_mfma_f32_16x16x32_bf16 v[64:67], a[160:163], v[232:235], v[64:67]\n"
    "ds_read_b64_tr_b16 a[160:161], v244 offset:20480\n"
    "v_cvt_pk_bf16_f32 v184, v184, v185\n"
    "v_mfma_f32_16x16x32_bf16 v[64:67], a[164:167], v[236:239], v[64:67]\n"
    "ds_read_b64_tr_b16 a[162:163], v244 offset:22528\n"
    "v_mfma_f32_16x16x32_bf16 v[76:79], a[168:171], v[232:235], v[76:79]\n"
    "ds_read_b64_tr_b16 a[164:165], v244 offset:28672\n"
    "buffer_load_dwordx4 a[216:219], v253, s[8:11], s48 offen offset:0 sc1\n"
    "v_mfma_f32_16x16x32_bf16 v[76:79], a[172:175], v[236:239], v[76:79]\n"
    "ds_read_b64_tr_b16 a[166:167], v244 offset:30720\n"
    "v_mfma_f32_16x16x32_bf16 v[72:75], a[172:175], v[228:231], v[72:75]\n"
    "v_exp_f32_e32 v176, v176\n"
    "v_mfma_f32_16x16x32_bf16 v[72:75], a[168:171], v[224:227], v[72:75]\n"
    "v_exp_f32_e32 v177, v177\n"
    "v_mfma_f32_16x16x32_bf16 v[68:71], a[168:171], v[216:219], v[68:71]\n"
    "ds_read_b64_tr_b16 a[168:169], v245 offset:20480\n"
    "buffer_load_dwordx4 a[220:223], v253, s[8:11], s48 offen offset:1024 sc1\n"
    "v_mfma_f32_16x16x32_bf16 v[68:71], a[172:175], v[220:223], v[68:71]\n"
    "s_waitcnt lgkmcnt(9)\n"
    "ds_read_b64_tr_b16 a[170:171], v245 offset:22528\n"
    "v_cvt_pk_bf16_f32 v185, v186, v187\n"
    "v_mfma_f32_16x16x32_bf16 v[80:83], a[176:179], v[216:219], v[80:83]\n"
    "ds_read_b64_tr_b16 a[172:173], v245 offset:28672\n"
    "v_mfma_f32_16x16x32_bf16 v[80:83], a[180:183], v[220:223], v[80:83]\n"
    "ds_read_b64_tr_b16 a[174:175], v245 offset:30720\n"
    "v_mfma_f32_16x16x32_bf16 v[84:87], a[180:183], v[228:231], v[84:87]\n"
    "v_exp_f32_e32 v178, v178\n"
    "v_mfma_f32_16x16x32_bf16 v[84:87], a[176:179], v[224:227], v[84:87]\n"
    "v_exp_f32_e32 v179, v179\n"
    "v_mfma_f32_16x16x32_bf16 v[88:91], a[176:179], v[232:235], v[88:91]\n"
    "s_waitcnt vmcnt(18)\n"
    "s_barrier\n"
    "v_mfma_f32_16x16x32_bf16 v[88:91], a[180:183], v[236:239], v[88:91]\n"
    "s_waitcnt lgkmcnt(8)\n"
    "ds_read_b64_tr_b16 a[176:177], v246 offset:20480\n"
    "v_cvt_pk_bf16_f32 v176, v176, v177\n"
    "v_mfma_f32_16x16x32_bf16 v[100:103], a[184:187], v[232:235], v[100:103]\n"
    "ds_read_b64_tr_b16 a[178:179], v246 offset:22528\n"
    "v_cvt_pk_bf16_f32 v177, v178, v179\n"
    "v_mfma_f32_16x16x32_bf16 v[100:103], a[188:191], v[236:239], v[100:103]\n"
    "ds_read_b64_tr_b16 a[180:181], v246 offset:28672\n"
    "ds_write_addtid_b32 v192 offset:16384\n"
    "v_mfma_f32_16x16x32_bf16 v[96:99], a[188:191], v[228:231], v[96:99]\n"
    "ds_read_b64_tr_b16 a[182:183], v246 offset:30720\n"
    "v_mfma_f32_16x16x32_bf16 v[96:99], a[184:187], v[224:227], v[96:99]\n"
    "v_exp_f32_e32 v196, v196\n"
    "v_mfma_f32_16x16x32_bf16 v[92:95], a[184:187], v[216:219], v[92:95]\n"
    "ds_read_b64_tr_b16 a[184:185], v247 offset:20480\n"
    "ds_write_addtid_b32 v193 offset:16640\n"
    "v_mfma_f32_16x16x32_bf16 v[92:95], a[188:191], v[220:223], v[92:95]\n"
    "s_waitcnt lgkmcnt(11)\n"
    "ds_read_b64_tr_b16 a[186:187], v247 offset:22528\n"
    "v_mfma_f32_16x16x32_bf16 v[104:107], a[160:163], v[216:219], v[104:107]\n"
    "ds_read_b64_tr_b16 a[188:189], v247 offset:28672\n"
    "v_mfma_f32_16x16x32_bf16 v[104:107], a[164:167], v[220:223], v[104:107]\n"
    "ds_read_b64_tr_b16 a[190:191], v247 offset:30720\n"
    "ds_write_addtid_b32 v184 offset:12288\n"
    "v_mfma_f32_16x16x32_bf16 v[108:111], a[164:167], v[228:231], v[108:111]\n"
    "v_exp_f32_e32 v197, v197\n"
    "v_mfma_f32_16x16x32_bf16 v[108:111], a[160:163], v[224:227], v[108:111]\n"
    "v_exp_f32_e32 v198, v198\n"
    "v_mfma_f32_16x16x32_bf16 v[112:115], a[160:163], v[232:235], v[112:115]\n"
    "ds_read_b128 a[160:163], v243 offset:53248\n"
    "ds_write_addtid_b32 v185 offset:12544\n"
    "v_mfma_f32_16x16x32_bf16 v[112:115], a[164:167], v[236:239], v[112:115]\n"
    "s_waitcnt lgkmcnt(13)\n"
    "ds_read_b128 a[164:167], v240 offset:49152\n"
    "v_mfma_f32_16x16x32_bf16 v[124:127], a[168:171], v[232:235], v[124:127]\n"
    "v_exp_f32_e32 v199, v199\n"
    "v_mfma_f32_16x16x32_bf16 v[124:127], a[172:175], v[236:239], v[124:127]\n"
    "ds_write_addtid_b32 v176 offset:8192\n"
    "v_cvt_pk_bf16_f32 v194, v196, v197\n"
    "v_mfma_f32_16x16x32_bf16 v[120:123], a[172:175], v[228:231], v[120:123]\n"
    "v_exp_f32_e32 v188, v188\n"
    "v_mfma_f32_16x16x32_bf16 v[120:123], a[168:171], v[224:227], v[120:123]\n"
    "v_exp_f32_e32 v189, v189\n"
    "v_mfma_f32_16x16x32_bf16 v[116:119], a[168:171], v[216:219], v[116:119]\n"
    "ds_read_b128 a[168:171], v241 offset:49152\n"
    "v_cvt_pk_bf16_f32 v195, v198, v199\n"
    "v_mfma_f32_16x16x32_bf16 v[116:119], a[172:175], v[220:223], v[116:119]\n"
    "s_waitcnt lgkmcnt(11)\n"
    "ds_read_b128 a[172:175], v240 offset:53248\n"
    "ds_write_addtid_b32 v177 offset:8448\n"
    "v_mfma_f32_16x16x32_bf16 v[128:131], a[176:179], v[216:219], v[128:131]\n"
    "v_exp_f32_e32 v190, v190\n"
    "v_mfma_f32_16x16x32_bf16 v[128:131], a[180:183], v[220:223], v[128:131]\n"
    "v_exp_f32_e32 v191, v191\n"
    "v_mfma_f32_16x16x32_bf16 v[132:135], a[180:183], v[228:231], v[132:135]\n"
    "ds_write_addtid_b32 v194 offset:16896\n"
    "v_cvt_pk_bf16_f32 v186, v188, v189\n"
    "v_mfma_f32_16x16x32_bf16 v[132:135], a[176:179], v[224:227], v[132:135]\n"
    "v_exp_f32_e32 v180, v180\n"
    "v_mfma_f32_16x16x32_bf16 v[136:139], a[176:179], v[232:235], v[136:139]\n"
    "ds_read_b128 a[176:179], v241 offset:53248\n"
    "v_mfma_f32_16x16x32_bf16 v[136:139], a[180:183], v[236:239], v[136:139]\n"
    "s_waitcnt lgkmcnt(10)\n"
    "ds_read_b128 a[180:183], v242 offset:49152\n"
    "v_mfma_f32_16x16x32_bf16 v[148:151], a[184:187], v[232:235], v[148:151]\n"
    "v_exp_f32_e32 v181, v181\n"
    "v_mfma_f32_16x16x32_bf16 v[148:151], a[188:191], v[236:239], v[148:151]\n"
    "ds_write_addtid_b32 v195 offset:17152\n"
    "v_cvt_pk_bf16_f32 v187, v190, v191\n"
    "v_mfma_f32_16x16x32_bf16 v[144:147], a[188:191], v[228:231], v[144:147]\n"
    "v_exp_f32_e32 v182, v182\n"
    "v_mfma_f32_16x16x32_bf16 v[144:147], a[184:187], v[224:227], v[144:147]\n"
    "v_exp_f32_e32 v183, v183\n"
    "v_mfma_f32_16x16x32_bf16 v[140:143], a[184:187], v[216:219], v[140:143]\n"
    "ds_read_b128 a[184:187], v243 offset:49152\n"
    "v_cvt_pk_bf16_f32 v178, v180, v181\n"
    "v_mfma_f32_16x16x32_bf16 v[140:143], a[188:191], v[220:223], v[140:143]\n"
    "s_waitcnt lgkmcnt(9)\n"
    "ds_read_b128 a[188:191], v242 offset:53248\n"
    "v_cvt_pk_bf16_f32 v179, v182, v183\n"
    "v_mfma_f32_16x16x32_bf16 v[216:219], a[160:163], a[92:95], 0\n"
    "ds_write_addtid_b32 v186 offset:12800\n"
    "v_mfma_f32_16x16x32_bf16 v[216:219], a[164:167], a[64:67], v[216:219]\n"
    "ds_write_addtid_b32 v187 offset:13056\n"
    "v_mfma_f32_16x16x32_bf16 v[224:227], a[164:167], a[96:99], 0\n"
    "ds_write_addtid_b32 v178 offset:8704\n"
    "v_mfma_f32_16x16x32_bf16 v[224:227], a[160:163], a[124:127], v[224:227]\n"
    "ds_write_addtid_b32 v179 offset:8960\n"
    "v_mfma_f32_16x16x32_bf16 v[232:235], a[160:163], a[156:159], 0\n"
    "ds_read_b128 a[160:163], v243 offset:55296\n"
    "v_mfma_f32_16x16x32_bf16 v[232:235], a[164:167], a[128:131], v[232:235]\n"
    "s_waitcnt lgkmcnt(12)\n"
    "ds_read_b128 a[164:167], v240 offset:51200\n"
    "v_mfma_f32_16x16x32_bf16 v[232:235], a[168:171], a[132:135], v[232:235]\n"
    "v_mfma_f32_16x16x32_bf16 v[232:235], a[172:175], a[136:139], v[232:235]\n"
    "v_mfma_f32_16x16x32_bf16 v[224:227], a[172:175], a[104:107], v[224:227]\n"
    "s_waitcnt lgkmcnt(2)\n"
    "s_barrier\n"
    "v_mfma_f32_16x16x32_bf16 v[224:227], a[168:171], a[100:103], v[224:227]\n"
    "ds_read2st64_b32 v[180:181], v250 offset0:32 offset1:33\n"
    "v_mfma_f32_16x16x32_bf16 v[216:219], a[168:171], a[68:71], v[216:219]\n"
    "ds_read_b128 a[168:171], v241 offset:51200\n"
    "v_mfma_f32_16x16x32_bf16 v[216:219], a[172:175], a[72:75], v[216:219]\n"
    "ds_read_b128 a[172:175], v240 offset:55296\n"
    "v_mfma_f32_16x16x32_bf16 v[216:219], a[176:179], a[76:79], v[216:219]\n"
    "v_mfma_f32_16x16x32_bf16 v[216:219], a[180:183], a[80:83], v[216:219]\n"
    "ds_read2st64_b32 v[182:183], v250 offset0:34 offset1:35\n"
    "v_mfma_f32_16x16x32_bf16 v[224:227], a[180:183], a[112:115], v[224:227]\n"
    "v_mfma_f32_16x16x32_bf16 v[224:227], a[176:179], a[108:111], v[224:227]\n"
    "ds_read2st64_b32 v[188:189], v250 offset0:48 offset1:49\n"
    "v_mfma_f32_16x16x32_bf16 v[232:235], a[176:179], a[140:143], v[232:235]\n"
    "ds_read_b128 a[176:179], v241 offset:55296\n"
    "v_mfma_f32_16x16x32_bf16 v[232:235], a[180:183], a[144:147], v[232:235]\n"
    "ds_read_b128 a[180:183], v242 offset:51200\n"
    "v_mfma_f32_16x16x32_bf16 v[232:235], a[184:187], a[148:151], v[232:235]\n"
    "ds_read2st64_b32 v[190:191], v250 offset0:50 offset1:51\n"
    "v_mfma_f32_16x16x32_bf16 v[232:235], a[188:191], a[152:155], v[232:235]\n"
    "v_mfma_f32_16x16x32_bf16 v[224:227], a[188:191], a[120:123], v[224:227]\n"
    "ds_read2st64_b32 v[196:197], v250 offset0:64 offset1:65\n"
    "v_mfma_f32_16x16x32_bf16 v[224:227], a[184:187], a[116:119], v[224:227]\n"
    "ds_read2st64_b32 v[198:199], v250 offset0:66 offset1:67\n"
    "v_mfma_f32_16x16x32_bf16 v[216:219], a[184:187], a[84:87], v[216:219]\n"
    "ds_read_b128 a[184:187], v243 offset:51200\n"
    "v_mfma_f32_16x16x32_bf16 v[216:219], a[188:191], a[88:91], v[216:219]\n"
    "s_waitcnt lgkmcnt(11)\n"
    "ds_read_b128 a[188:191], v242 offset:55296\n"
    "v_mfma_f32_16x16x32_bf16 v[220:223], a[160:163], a[92:95], 0\n"
    "v_exp_f32_e32 v232, v232\n"
    "v_mfma_f32_16x16x32_bf16 v[220:223], a[164:167], a[64:67], v[220:223]\n"
    "v_exp_f32_e32 v233, v233\n"
    "v_mfma_f32_16x16x32_bf16 v[228:231], a[164:167], a[96:99], 0\n"
    "v_exp_f32_e32 v234, v234\n"
    "v_mfma_f32_16x16x32_bf16 v[228:231], a[160:163], a[124:127], v[228:231]\n"
    "s_add_u32 s48, s57, 18432\n"
    "v_exp_f32_e32 v235, v235\n"
    "v_mfma_f32_16x16x32_bf16 v[236:239], a[160:163], a[156:159], 0\n"
    "ds_read_b64_tr_b16 a[160:161], v244 offset:32768\n"
    "v_mfma_f32_16x16x32_bf16 v[236:239], a[164:167], a[128:131], v[236:239]\n"
    "s_waitcnt lgkmcnt(10)\n"
    "ds_read_b64_tr_b16 a[162:163], v244 offset:34816\n"
    "v_cvt_pk_bf16_f32 v232, v232, v233\n"
    "v_mfma_f32_16x16x32_bf16 v[236:239], a[168:171], a[132:135], v[236:239]\n"
    "buffer_load_dwordx4 a[92:95], v253, s[8:11], s48 offen offset:0 sc1\n"
    "s_waitcnt lgkmcnt(4)\n"
    "s_barrier\n"
    "v_mfma_f32_16x16x32_bf16 v[236:239], a[172:175], a[136:139], v[236:239]\n"
    "ds_read_b64_tr_b16 a[164:165], v244 offset:40960\n"
    "v_exp_f32_e32 v224, v224\n"
    "v_mfma_f32_16x16x32_bf16 v[228:231], a[172:175], a[104:107], v[228:231]\n"
    "ds_read_b64_tr_b16 a[166:167], v244 offset:43008\n"
    "v_exp_f32_e32 v225, v225\n"
    "v_mfma_f32_16x16x32_bf16 v[228:231], a[168:171], a[100:103], v[228:231]\n"
    "buffer_load_dwordx4 a[124:127], v253, s[8:11], s48 offen offset:1024 sc1\n"
    "v_mfma_f32_16x16x32_bf16 v[220:223], a[168:171], a[68:71], v[220:223]\n"
    "s_add_u32 s48, s57, 2048\n"
    "ds_read_b64_tr_b16 a[168:169], v245 offset:32768\n"
    "v_cvt_pk_bf16_f32 v233, v234, v235\n"
    "v_mfma_f32_16x16x32_bf16 v[220:223], a[172:175], a[72:75], v[220:223]\n"
    "ds_read_b64_tr_b16 a[170:171], v245 offset:34816\n"
    "v_exp_f32_e32 v226, v226\n"
    "v_mfma_f32_16x16x32_bf16 v[220:223], a[176:179], a[76:79], v[220:223]\n"
    "ds_read_b64_tr_b16 a[172:173], v245 offset:40960\n"
    "buffer_load_dwordx4 a[156:159], v253, s[8:11], s48 offen offset:0 sc1\n"
    "v_mfma_f32_16x16x32_bf16 v[220:223], a[180:183], a[80:83], v[220:223]\n"
    "ds_read_b64_tr_b16 a[174:175], v245 offset:43008\n"
    "v_mfma_f32_16x16x32_bf16 v[228:231], a[180:183], a[112:115], v[228:231]\n"
    "v_exp_f32_e32 v227, v227\n"
    "v_mfma_f32_16x16x32_bf16 v[228:231], a[176:179], a[108:111], v[228:231]\n"
    "buffer_load_dwordx4 a[64:67], v253, s[8:11], s48 offen offset:1024 sc1\n"
    "v_cvt_pk_bf16_f32 v224, v224, v225\n"
    "v_mfma_f32_16x16x32_bf16 v[236:239], a[176:179], a[140:143], v[236:239]\n"
    "s_add_u32 s48, s57, 20480\n"
    "ds_read_b64_tr_b16 a[176:177], v246 offset:32768\n"
    "v_exp_f32_e32 v216, v216\n"
    "v_mfma_f32_16x16x32_bf16 v[236:239], a[180:183], a[144:147], v[236:239]\n"
    "s_waitcnt lgkmcnt(9)\n"
    "ds_read_b64_tr_b16 a[178:179], v246 offset:34816\n"
    "v_mfma_f32_16x16x32_bf16 v[236:239], a[184:187], a[148:151], v[236:239]\n"
    "ds_read_b64_tr_b16 a[180:181], v246 offset:40960\n"
    "buffer_load_dwordx4 a[96:99], v253, s[8:11], s48 offen offset:0 sc1\n"
    "v_mfma_f32_16x16x32_bf16 v[236:239], a[188:191], a[152:155], v[236:239]\n"
    "ds_read_b64_tr_b16 a[182:183], v246 offset:43008\n"
    "v_mfma_f32_16x16x32_bf16 v[228:231], a[188:191], a[120:123], v[228:231]\n"
    "v_exp_f32_e32 v217, v217\n"
    "v_mfma_f32_16x16x32_bf16 v[228:231], a[184:187], a[116:119], v[228:231]\n"
    "buffer_load_dwordx4 a[128:131], v253, s[8:11], s48 offen offset:1024 sc1\n"
    "v_cvt_pk_bf16_f32 v225, v226, v227\n"
    "v_mfma_f32_16x16x32_bf16 v[220:223], a[184:187], a[84:87], v[220:223]\n"
    "s_add_u32 s48, s57, 4096\n"
    "ds_read_b64_tr_b16 a[184:185], v247 offset:32768\n"
    "v_exp_f32_e32 v218, v218\n"
    "v_mfma_f32_16x16x32_bf16 v[220:223], a[188:191], a[88:91], v[220:223]\n"
    "s_waitcnt lgkmcnt(9)\n"
    "ds_read_b64_tr_b16 a[186:187], v247 offset:34816\n"
    "v_mfma_f32_16x16x32_bf16 v[56:59], a[160:163], v[176:179], v[56:59]\n"
    "ds_read_b64_tr_b16 a[188:189], v247 offset:40960\n"
    "buffer_load_dwordx4 a[68:71], v253, s[8:11], s48 offen offset:0 sc1\n"
    "v_mfma_f32_16x16x32_bf16 v[56:59], a[164:167], v[180:183], v[56:59]\n"
    "ds_read_b64_tr_b16 a[190:191], v247 offset:43008\n"
    "v_mfma_f32_16x16x32_bf16 v[60:63], a[164:167], v[188:191], v[60:63]\n"
    "v_exp_f32_e32 v219, v219\n"
    "v_mfma_f32_16x16x32_bf16 v[60:63], a[160:163], v[184:187], v[60:63]\n"
    "buffer_load_dwordx4 a[100:103], v253, s[8:11], s48 offen offset:1024 sc1\n"
    "v_cvt_pk_bf16_f32 v216, v216, v217\n"
    "v_mfma_f32_16x16x32_bf16 v[64:67], a[160:163], v[192:195], v[64:67]\n"
    "s_add_u32 s48, s57, 22528\n"
    "ds_read_b64_tr_b16 a[160:161], v244 offset:36864\n"
    "v_cvt_pk_bf16_f32 v217, v218, v219\n"
    "v_mfma_f32_16x16x32_bf16 v[64:67], a[164:167], v[196:199], v[64:67]\n"
    "s_waitcnt lgkmcnt(9)\n"
    "ds_read_b64_tr_b16 a[162:163], v244 offset:38912\n"
    "v_exp_f32_e32 v236, v236\n"
    "v_mfma_f32_16x16x32_bf16 v[76:79], a[168:171], v[192:195], v[76:79]\n"
    "ds_read_b64_tr_b16 a[164:165], v244 offset:45056\n"
    "buffer_load_dwordx4 a[132:135], v253, s[8:11], s48 offen offset:0 sc1\n"
    "v_mfma_f32_16x16x32_bf16 v[76:79], a[172:175], v[196:199], v[76:79]\n"
    "ds_read_b64_tr_b16 a[166:167], v244 offset:47104\n"
    "v_exp_f32_e32 v237, v237\n"
    "v_mfma_f32_16x16x32_bf16 v[72:75], a[172:175], v[188:191], v[72:75]\n"
    "v_exp_f32_e32 v238, v238\n"
    "v_mfma_f32_16x16x32_bf16 v[72:75], a[168:171], v[184:187], v[72:75]\n"
    "buffer_load_dwordx4 a[72:75], v253, s[8:11], s48 offen offset:1024 sc1\n"
    "v_exp_f32_e32 v239, v239\n"
    "v_mfma_f32_16x16x32_bf16 v[68:71], a[168:171], v[176:179], v[68:71]\n"
    "s_add_u32 s48, s57, 6144\n"
    "ds_read_b64_tr_b16 a[168:169], v245 offset:36864\n"
    "ds_write_addtid_b32 v232 offset:16384\n"
    "v_mfma_f32_16x16x32_bf16 v[68:71], a[172:175], v[180:183], v[68:71]\n"
    "s_waitcnt lgkmcnt(10)\n"
    "ds_read_b64_tr_b16 a[170:171], v245 offset:38912\n"
    "v_cvt_pk_bf16_f32 v234, v236, v237\n"
    "v_mfma_f32_16x16x32_bf16 v[80:83], a[176:179], v[176:179], v[80:83]\n"
    "ds_read_b64_tr_b16 a[172:173], v245 offset:45056\n"
    "buffer_load_dwordx4 a[104:107], v253, s[8:11], s48 offen offset:0 sc1\n"
    "v_mfma_f32_16x16x32_bf16 v[80:83], a[180:183], v[180:183], v[80:83]\n"
    "ds_read_b64_tr_b16 a[174:175], v245 offset:47104\n"
    "v_exp_f32_e32 v228, v228\n"
    "v_mfma_f32_16x16x32_bf16 v[84:87], a[180:183], v[188:191], v[84:87]\n"
    "v_exp_f32_e32 v229, v229\n"
    "v_mfma_f32_16x16x32_bf16 v[84:87], a[176:179], v[184:187], v[84:87]\n"
    "buffer_load_dwordx4 a[136:139], v253, s[8:11], s48 offen offset:1024 sc1\n"
    "ds_write_addtid_b32 v233 offset:16640\n"
    "v_mfma_f32_16x16x32_bf16 v[88:91], a[176:179], v[192:195], v[88:91]\n"
    "s_add_u32 s48, s57, 24576\n"
    "ds_read_b64_tr_b16 a[176:177], v246 offset:36864\n"
    "v_cvt_pk_bf16_f32 v235, v238, v239\n"
    "v_mfma_f32_16x16x32_bf16 v[88:91], a[180:183], v[196:199], v[88:91]\n"
    "s_waitcnt lgkmcnt(11)\n"
    "ds_read_b64_tr_b16 a[178:179], v246 offset:38912\n"
    "v_exp_f32_e32 v230, v230\n"
    "v_mfma_f32_16x16x32_bf16 v[100:103], a[184:187], v[192:195], v[100:103]\n"
    "ds_read_b64_tr_b16 a[180:181], v246 offset:45056\n"
    "buffer_load_dwordx4 a[76:79], v253, s[8:11], s48 offen offset:0 sc1\n"
    "v_mfma_f32_16x16x32_bf16 v[100:103], a[188:191], v[196:199], v[100:103]\n"
    "ds_read_b64_tr_b16 a[182:183], v246 offset:47104\n"
    "ds_write_addtid_b32 v224 offset:12288\n"
    "v_mfma_f32_16x16x32_bf16 v[96:99], a[188:191], v[188:191], v[96:99]\n"
    "v_exp_f32_e32 v231, v231\n"
    "v_mfma_f32_16x16x32_bf16 v[96:99], a[184:187], v[184:187], v[96:99]\n"
    "buffer_load_dwordx4 a[108:111], v253, s[8:11], s48 offen offset:1024 sc1\n"
    "v_cvt_pk_bf16_f32 v226, v228, v229\n"
    "v_mfma_f32_16x16x32_bf16 v[92:95], a[184:187], v[176:179], v[92:95]\n"
    "s_add_u32 s48, s57, 8192\n"
    "ds_read_b64_tr_b16 a[184:185], v247 offset:36864\n"
    "ds_write_addtid_b32 v225 offset:12544\n"
    "v_mfma_f32_16x16x32_bf16 v[92:95], a[188:191], v[180:183], v[92:95]\n"
    "s_waitcnt lgkmcnt(13)\n"
    "ds_read_b64_tr_b16 a[186:187], v247 offset:38912\n"
    "v_exp_f32_e32 v220, v220\n"
    "v_mfma_f32_16x16x32_bf16 v[104:107], a[160:163], v[176:179], v[104:107]\n"
    "ds_read_b64_tr_b16 a[188:189], v247 offset:45056\n"
    "buffer_load_dwordx4 a[140:143], v253, s[8:11], s48 offen offset:0 sc1\n"
    "v_mfma_f32_16x16x32_bf16 v[104:107], a[164:167], v[180:183], v[104:107]\n"
    "ds_read_b64_tr_b16 a[190:191], v247 offset:47104\n"
    "ds_write_addtid_b32 v216 offset:8192\n"
    "v_mfma_f32_16x16x32_bf16 v[108:111], a[164:167], v[188:191], v[108:111]\n"
    "v_exp_f32_e32 v221, v221\n"
    "v_mfma_f32_16x16x32_bf16 v[108:111], a[160:163], v[184:187], v[108:111]\n"
    "buffer_load_dwordx4 a[80:83], v253, s[8:11], s48 offen offset:1024 sc1\n"
    "v_cvt_pk_bf16_f32 v227, v230, v231\n"
    "v_mfma_f32_16x16x32_bf16 v[112:115], a[160:163], v[192:195], v[112:115]\n"
    "s_add_u32 s48, s57, 26624\n"
    "ds_read_b64_tr_b16 a[160:161], v244 offset:49152\n"
    "v_exp_f32_e32 v222, v222\n"
    "v_mfma_f32_16x16x32_bf16 v[112:115], a[164:167], v[196:199], v[112:115]\n"
    "s_waitcnt lgkmcnt(13)\n"
    "ds_read_b64_tr_b16 a[162:163], v244 offset:51200\n"
    "v_exp_f32_e32 v223, v223\n"
    "v_mfma_f32_16x16x32_bf16 v[124:127], a[168:171], v[192:195], v[124:127]\n"
    "ds_read_b64_tr_b16 a[164:165], v244 offset:57344\n"
    "buffer_load_dwordx4 a[112:115], v253, s[8:11], s48 offen offset:0 sc1\n"
    "v_mfma_f32_16x16x32_bf16 v[124:127], a[172:175], v[196:199], v[124:127]\n"
    "ds_read_b64_tr_b16 a[166:167], v244 offset:59392\n"
    "ds_write_addtid_b32 v217 offset:8448\n"
    "v_mfma_f32_16x16x32_bf16 v[120:123], a[172:175], v[188:191], v[120:123]\n"
    "ds_write_addtid_b32 v234 offset:16896\n"
    "v_cvt_pk_bf16_f32 v218, v220, v221\n"
    "v_mfma_f32_16x16x32_bf16 v[120:123], a[168:171], v[184:187], v[120:123]\n"
    "buffer_load_dwordx4 a[144:147], v253, s[8:11], s48 offen offset:1024 sc1\n"
    "v_cvt_pk_bf16_f32 v219, v222, v223\n"
    "v_mfma_f32_16x16x32_bf16 v[116:119], a[168:171], v[176:179], v[116:119]\n"
    "s_add_u32 s48, s57, 10240\n"
    "ds_read_b64_tr_b16 a[168:169], v245 offset:49152\n"
    "ds_write_addtid_b32 v235 offset:17152\n"
    "v_mfma_f32_16x16x32_bf16 v[116:119], a[172:175], v[180:183], v[116:119]\n"
    "s_waitcnt lgkmcnt(15)\n"
    "ds_read_b64_tr_b16 a[170:171], v245 offset:51200\n"
    "ds_write_addtid_b32 v226 offset:12800\n"
    "v_mfma_f32_16x16x32_bf16 v[128:131], a[176:179], v[176:179], v[128:131]\n"
    "ds_read_b64_tr_b16 a[172:173], v245 offset:57344\n"
    "buffer_load_dwordx4 a[84:87], v253, s[8:11], s48 offen offset:0 sc1\n"
    "v_mfma_f32_16x16x32_bf16 v[128:131], a[180:183], v[180:183], v[128:131]\n"
    "ds_read_b64_tr_b16 a[174:175], v245 offset:59392\n"
    "ds_write_addtid_b32 v227 offset:13056\n"
    "v_mfma_f32_16x16x32_bf16 v[132:135], a[180:183], v[188:191], v[132:135]\n"
    "ds_write_addtid_b32 v218 offset:8704\n"
    "ds_write_addtid_b32 v219 offset:8960\n"
    "v_mfma_f32_16x16x32_bf16 v[132:135], a[176:179], v[184:187], v[132:135]\n"
    "buffer_load_dwordx4 a[116:119], v253, s[8:11], s48 offen offset:1024 sc1\n"
    "v_mfma_f32_16x16x32_bf16 v[136:139], a[176:179], v[192:195], v[136:139]\n"
    "s_add_u32 s48, s57, 28672\n"
    "ds_read_b64_tr_b16 a[176:177], v246 offset:49152\n"
    "v_mfma_f32_16x16x32_bf16 v[136:139], a[180:183], v[196:199], v[136:139]\n"
    "s_waitcnt lgkmcnt(15)\n"
    "ds_read_b64_tr_b16 a[178:179], v246 offset:51200\n"
    "v_mfma_f32_16x16x32_bf16 v[148:151], a[184:187], v[192:195], v[148:151]\n"
    "buffer_load_dwordx4 a[148:151], v253, s[8:11], s48 offen offset:0 sc1\n"
    "s_waitcnt lgkmcnt(2)\n"
    "s_barrier\n"
    "v_mfma_f32_16x16x32_bf16 v[148:151], a[188:191], v[196:199], v[148:151]\n"
    "ds_read_b64_tr_b16 a[180:181], v246 offset:57344\n"
    "v_mfma_f32_16x16x32_bf16 v[144:147], a[188:191], v[188:191], v[144:147]\n"
    "ds_read_b64_tr_b16 a[182:183], v246 offset:59392\n"
    "ds_read2st64_b32 v[220:221], v250 offset0:32 offset1:33\n"
    "v_mfma_f32_16x16x32_bf16 v[144:147], a[184:187], v[184:187], v[144:147]\n"
    "buffer_load_dwordx4 a[88:91], v253, s[8:11], s48 offen offset:1024 sc1\n"
    "ds_read2st64_b32 v[222:223], v250 offset0:34 offset1:35\n"
    "v_mfma_f32_16x16x32_bf16 v[140:143], a[184:187], v[176:179], v[140:143]\n"
    "s_add_u32 s48, s57, 12288\n"
    "ds_read_b64_tr_b16 a[184:185], v247 offset:49152\n"
    "ds_read2st64_b32 v[228:229], v250 offset0:48 offset1:49\n"
    "v_mfma_f32_16x16x32_bf16 v[140:143], a[188:191], v[180:183], v[140:143]\n"
    "ds_read_b64_tr_b16 a[186:187], v247 offset:51200\n"
    "ds_read2st64_b32 v[230:231], v250 offset0:50 offset1:51\n"
    "v_mfma_f32_16x16x32_bf16 v[56:59], a[160:163], v[216:219], v[56:59]\n"
    "ds_read_b64_tr_b16 a[188:189], v247 offset:57344\n"
    "buffer_load_dwordx4 a[120:123], v253, s[8:11], s48 offen offset:0 sc1\n"
    "v_mfma_f32_16x16x32_bf16 v[60:63], a[160:163], v[224:227], v[60:63]\n"
    "ds_read_b64_tr_b16 a[190:191], v247 offset:59392\n"
    "ds_read2st64_b32 v[236:237], v250 offset0:64 offset1:65\n"
    "v_mfma_f32_16x16x32_bf16 v[64:67], a[160:163], v[232:235], v[64:67]\n"
    "ds_read_b64_tr_b16 a[160:161], v244 offset:53248\n"
    "ds_read2st64_b32 v[238:239], v250 offset0:66 offset1:67\n"
    "v_mfma_f32_16x16x32_bf16 v[76:79], a[168:171], v[232:235], v[76:79]\n"
    "ds_read_b64_tr_b16 a[162:163], v244 offset:55296\n"
    "buffer_load_dwordx4 a[152:155], v253, s[8:11], s48 offen offset:1024 sc1\n"
    "v_mfma_f32_16x16x32_bf16 v[72:75], a[168:171], v[224:227], v[72:75]\n"
    "v_lshrrev_b32_e32 v158, 4, v253\n"
    "v_mfma_f32_16x16x32_bf16 v[68:71], a[168:171], v[216:219], v[68:71]\n"
    "s_waitcnt lgkmcnt(1)\n"
    "v_lshrrev_b32_e32 v159, 4, v158\n"
    "v_mfma_f32_16x16x32_bf16 v[56:59], a[164:167], v[220:223], v[56:59]\n"
    "v_bfe_u32 v255, v158, 2, 2\n"
    "v_and_b32_e32 v158, 3, v158\n"
    "v_mfma_f32_16x16x32_bf16 v[60:63], a[164:167], v[228:231], v[60:63]\n"
    "v_xor_b32_e32 v159, v159, v255\n"
    "v_lshl_or_b32 v158, v159, 2, v158\n"
    "v_mfma_f32_16x16x32_bf16 v[64:67], a[164:167], v[236:239], v[64:67]\n"
    "ds_read_b64_tr_b16 a[164:165], v244 offset:61440\n"
    "v_mov_b32_e32 v159, 0x3f80\n"
    "v_mfma_f32_16x16x32_bf16 v[76:79], a[172:175], v[236:239], v[76:79]\n"
    "ds_read_b64_tr_b16 a[166:167], v244 offset:63488\n"
    "v_mov_b32_e32 v152, 0\n"
    "v_mfma_f32_16x16x32_bf16 v[72:75], a[172:175], v[228:231], v[72:75]\n"
    "s_waitcnt lgkmcnt(3)\n"
    "s_barrier\n"
    "v_mfma_f32_16x16x32_bf16 v[68:71], a[172:175], v[220:223], v[68:71]\n"
    "s_add_u32 s61, s47, 5697536\n"
    "ds_read_b64_tr_b16 a[168:169], v245 offset:53248\n"
    "v_mfma_f32_16x16x32_bf16 v[80:83], a[176:179], v[216:219], v[80:83]\n"
    "s_add_u32 m0, s49, 8192\n"
    "ds_read_b64_tr_b16 a[170:171], v245 offset:55296\n"
    "v_cvt_pk_bf16_f32 v64, v64, v65\n"
    "v_mfma_f32_16x16x32_bf16 v[80:83], a[180:183], v[220:223], v[80:83]\n"
    "ds_read_b64_tr_b16 a[172:173], v245 offset:61440\n"
    "buffer_load_dwordx4 v252, s[8:11], s61 offen offset:0 sc1 lds\n"
    "v_mfma_f32_16x16x32_bf16 v[84:87], a[180:183], v[228:231], v[84:87]\n"
    "s_add_u32 s61, s47, 5701632\n"
    "ds_read_b64_tr_b16 a[174:175], v245 offset:63488\n"
    "v_cvt_pk_bf16_f32 v65, v66, v67\n"
    "v_mfma_f32_16x16x32_bf16 v[84:87], a[176:179], v[224:227], v[84:87]\n"
    "s_add_u32 m0, s49, 12288\n"
    "v_cvt_pk_bf16_f32 v66, v76, v77\n"
    "v_cvt_pk_bf16_f32 v67, v78, v79\n"
    "v_mfma_f32_16x16x32_bf16 v[88:91], a[176:179], v[232:235], v[88:91]\n"
    "ds_read_b64_tr_b16 a[176:177], v246 offset:53248\n"
    "buffer_load_dwordx4 v252, s[8:11], s61 offen offset:0 sc1 lds\n"
    "buffer_store_dwordx4 v[64:67], v253, s[8:11], s86 offen offset:0 sc1\n"
    "v_cvt_pk_bf16_f32 v60, v60, v61\n"
    "v_mov_b32_e32 v153, 0\n"
    "v_mfma_f32_16x16x32_bf16 v[88:91], a[180:183], v[236:239], v[88:91]\n"
    "s_add_u32 s61, s47, 5705728\n"
    "ds_read_b64_tr_b16 a[178:179], v246 offset:55296\n"
    "v_mfma_f32_16x16x32_bf16 v[100:103], a[184:187], v[232:235], v[100:103]\n"
    "s_add_u32 m0, s49, 16384\n"
    "ds_read_b64_tr_b16 a[180:181], v246 offset:61440\n"
    "v_mov_b32_e32 v156, 0\n"
    "v_mfma_f32_16x16x32_bf16 v[100:103], a[188:191], v[236:239], v[100:103]\n"
    "ds_read_b64_tr_b16 a[182:183], v246 offset:63488\n"
    "buffer_load_dwordx4 v252, s[8:11], s61 offen offset:0 sc1 lds\n"
    "v_mfma_f32_16x16x32_bf16 v[96:99], a[188:191], v[228:231], v[96:99]\n"
    "s_mov_b32 m0, s49\n"
    "v_cvt_pk_bf16_f32 v61, v62, v63\n"
    "v_mov_b32_e32 v157, 0\n"
    "v_mfma_f32_16x16x32_bf16 v[96:99], a[184:187], v[224:227], v[96:99]\n"
    "v_cvt_pk_bf16_f32 v62, v72, v73\n"
    "v_cmp_eq_u32_e32 vcc, 0, v158\n"
    "v_mfma_f32_16x16x32_bf16 v[92:95], a[184:187], v[216:219], v[92:95]\n"
    "ds_read_b64_tr_b16 a[184:185], v247 offset:53248\n"
    "v_cndmask_b32_e32 v154, 0, v159, vcc\n"
    "v_mfma_f32_16x16x32_bf16 v[92:95], a[188:191], v[220:223], v[92:95]\n"
    "s_waitcnt lgkmcnt(9)\n"
    "ds_read_b64_tr_b16 a[186:187], v247 offset:55296\n"
    "v_cmp_eq_u32_e32 vcc, 1, v158\n"
    "v_mfma_f32_16x16x32_bf16 v[104:107], a[160:163], v[216:219], v[104:107]\n"
    "ds_read_b64_tr_b16 a[188:189], v247 offset:61440\n"
    "v_cndmask_b32_e64 v154, v154, 1.0, vcc\n"
    "v_mfma_f32_16x16x32_bf16 v[104:107], a[164:167], v[220:223], v[104:107]\n"
    "ds_read_b64_tr_b16 a[190:191], v247 offset:63488\n"
    "v_cvt_pk_bf16_f32 v63, v74, v75\n"
    "v_mfma_f32_16x16x32_bf16 v[108:111], a[164:167], v[228:231], v[108:111]\n"
    "buffer_store_dwordx4 v[60:63], v253, s[8:11], s85 offen offset:0 sc1\n"
    "v_cmp_eq_u32_e32 vcc, 2, v158\n"
    "s_nop 1\n"
    "v_cndmask_b32_e32 v155, 0, v159, vcc\n"
    "v_mfma_f32_16x16x32_bf16 v[108:111], a[160:163], v[224:227], v[108:111]\n"
    "v_cvt_pk_bf16_f32 v56, v56, v57\n"
    "v_cmp_eq_u32_e32 vcc, 3, v158\n"
    "v_mfma_f32_16x16x32_bf16 v[112:115], a[160:163], v[232:235], v[112:115]\n"
    "ds_read_b128 a[160:163], v243 offset:20480\n"
    "v_cndmask_b32_e64 v155, v155, 1.0, vcc\n"
    "v_mfma_f32_16x16x32_bf16 v[112:115], a[164:167], v[236:239], v[112:115]\n"
    "s_waitcnt lgkmcnt(9)\n"
    "ds_read_b128 a[164:167], v240 offset:16384\n"
    "v_and_b32_e32 v251, 0xf0, v253\n"
    "v_mfma_f32_16x16x32_bf16 v[124:127], a[168:171], v[232:235], v[124:127]\n"
    "v_cvt_pk_bf16_f32 v57, v58, v59\n"
    "v_mov_b32_e32 v254, 2.0\n"
    "v_mfma_f32_16x16x32_bf16 v[124:127], a[172:175], v[236:239], v[124:127]\n"
    "v_cmp_le_u32_e32 vcc, 0x100, v253\n"
    "v_mov_b32_e32 v255, s29\n"
    "v_mfma_f32_16x16x32_bf16 v[120:123], a[172:175], v[228:231], v[120:123]\n"
    "v_cvt_pk_bf16_f32 v58, v68, v69\n"
    "v_mfma_f32_16x16x32_bf16 v[120:123], a[168:171], v[224:227], v[120:123]\n"
    "v_cvt_pk_bf16_f32 v59, v70, v71\n"
    "v_cndmask_b32_e32 v254, v254, v255, vcc\n"
    "v_mfma_f32_16x16x32_bf16 v[116:119], a[168:171], v[216:219], v[116:119]\n"
    "ds_read_b128 a[168:171], v241 offset:16384\n"
    "v_cmp_le_u32_e32 vcc, 0x200, v253\n"
    "v_mov_b32_e32 v255, s30\n"
    "v_mfma_f32_16x16x32_bf16 v[116:119], a[172:175], v[220:223], v[116:119]\n"
    "s_waitcnt lgkmcnt(7)\n"
    "ds_read_b128 a[172:175], v240 offset:20480\n"
    "v_cndmask_b32_e32 v254, v254, v255, vcc\n"
    "v_mfma_f32_16x16x32_bf16 v[128:131], a[176:179], v[216:219], v[128:131]\n"
    "buffer_store_dwordx4 v[56:59], v253, s[8:11], s84 offen offset:0 sc1\n"
    "v_cmp_le_u32_e32 vcc, 0x300, v253\n"
    "v_mov_b32_e32 v255, s31\n"
    "v_mfma_f32_16x16x32_bf16 v[128:131], a[180:183], v[220:223], v[128:131]\n"
    "s_cmp_eq_u32 s51, 0\n"
    "s_cselect_b32 s60, 2.0, 24576\n"
    "v_cndmask_b32_e32 v254, v254, v255, vcc\n"
    "v_mfma_f32_16x16x32_bf16 v[132:135], a[180:183], v[228:231], v[132:135]\n"
    "v_cvt_pk_bf16_f32 v88, v88, v89\n"
    "v_add_u32_e32 v251, v251, v254\n"
    "v_mfma_f32_16x16x32_bf16 v[132:135], a[176:179], v[224:227], v[132:135]\n"
    "s_lshl_b32 s45, s51, 16\n"
    "s_add_u32 s45, s45, %3\n"
    "s_add_u32 s45, s45, 57344\n"
    "v_mfma_f32_16x16x32_bf16 v[136:139], a[176:179], v[232:235], v[136:139]\n"
    "ds_read_b128 a[176:179], v241 offset:20480\n"
    "v_mfma_f32_16x16x32_bf16 v[136:139], a[180:183], v[236:239], v[136:139]\n"
    "s_waitcnt lgkmcnt(5)\n"
    "ds_read_b128 a[180:183], v242 offset:16384\n"
    "v_mfma_f32_16x16x32_bf16 v[148:151], a[184:187], v[232:235], v[148:151]\n"
    "v_cvt_pk_bf16_f32 v89, v90, v91\n"
    "v_mfma_f32_16x16x32_bf16 v[148:151], a[188:191], v[236:239], v[148:151]\n"
    "s_lshl_b32 s46, s89, 12\n"
    "s_add_u32 s46, s46, s45\n"
    "v_add_u32_e32 v159, s46, v253\n"
    "v_mfma_f32_16x16x32_bf16 v[144:147], a[188:191], v[228:231], v[144:147]\n"
    "v_cvt_pk_bf16_f32 v90, v100, v101\n"
    "v_mfma_f32_16x16x32_bf16 v[144:147], a[184:187], v[224:227], v[144:147]\n"
    "v_cvt_pk_bf16_f32 v91, v102, v103\n"
    "v_mfma_f32_16x16x32_bf16 v[140:143], a[184:187], v[216:219], v[140:143]\n"
    "s_add_u32 s45, s45, 16384\n"
    "ds_read_b128 a[184:187], v243 offset:16384\n"
    "v_lshrrev_b32_e32 v255, 2, v253\n"
    "v_mfma_f32_16x16x32_bf16 v[140:143], a[188:191], v[220:223], v[140:143]\n"
    "s_waitcnt lgkmcnt(3)\n"
    "ds_read_b128 a[188:191], v242 offset:20480\n"
    "v_mfma_f32_16x16x32_bf16 v[200:203], a[160:163], a[28:31], 0\n"
    "buffer_store_dwordx4 v[88:91], v253, s[8:11], s86 offen offset:1024 sc1\n"
    "v_add_u32_e32 v255, s45, v255\n"
    "v_mfma_f32_16x16x32_bf16 v[200:203], a[164:167], a[0:3], v[200:203]\n"
    "v_cvt_pk_bf16_f32 v84, v84, v85\n"
    "v_mfma_f32_16x16x32_bf16 v[208:211], a[164:167], a[32:35], 0\n"
    "v_cvt_pk_bf16_f32 v85, v86, v87\n"
    "v_mfma_f32_16x16x32_bf16 v[208:211], a[160:163], a[60:63], v[208:211]\n"
    "ds_read_b128 a[160:163], v243 offset:22528\n"
    "v_mfma_f32_16x16x32_bf16 v[208:211], a[168:171], a[36:39], v[208:211]\n"
    "ds_read_b128 a[164:167], v240 offset:18432\n"
    "v_mfma_f32_16x16x32_bf16 v[208:211], a[172:175], a[40:43], v[208:211]\n"
    "v_cvt_pk_bf16_f32 v86, v96, v97\n"
    "v_mfma_f32_16x16x32_bf16 v[200:203], a[172:175], a[8:11], v[200:203]\n"
    "v_cvt_pk_bf16_f32 v87, v98, v99\n"
    "v_mfma_f32_16x16x32_bf16 v[200:203], a[168:171], a[4:7], v[200:203]\n"
    "s_waitcnt lgkmcnt(2)\n"
    "ds_read_b128 a[168:171], v241 offset:18432\n"
    "v_mfma_f32_16x16x32_bf16 v[200:203], a[176:179], a[12:15], v[200:203]\n"
    "ds_read_b128 a[172:175], v240 offset:22528\n"
    "v_mfma_f32_16x16x32_bf16 v[200:203], a[180:183], a[16:19], v[200:203]\n"
    "buffer_store_dwordx4 v[84:87], v253, s[8:11], s85 offen offset:1024 sc1\n"
    "v_cvt_pk_bf16_f32 v80, v80, v81\n"
    "v_mfma_f32_16x16x32_bf16 v[208:211], a[180:183], a[48:51], v[208:211]\n"
    "v_cvt_pk_bf16_f32 v81, v82, v83\n"
    "v_mfma_f32_16x16x32_bf16 v[208:211], a[176:179], a[44:47], v[208:211]\n"
    "ds_read_b128 a[176:179], v241 offset:22528\n"
    "v_mfma_f32_16x16x32_bf16 v[208:211], a[184:187], a[52:55], v[208:211]\n"
    "ds_read_b128 a[180:183], v242 offset:18432\n"
    "v_mfma_f32_16x16x32_bf16 v[208:211], a[188:191], a[56:59], v[208:211]\n"
    "v_cvt_pk_bf16_f32 v82, v92, v93\n"
    "v_mfma_f32_16x16x32_bf16 v[200:203], a[188:191], a[24:27], v[200:203]\n"
    "v_cvt_pk_bf16_f32 v83, v94, v95\n"
    "v_mfma_f32_16x16x32_bf16 v[200:203], a[184:187], a[20:23], v[200:203]\n"
    "s_waitcnt lgkmcnt(2)\n"
    "ds_read_b128 a[184:187], v243 offset:18432\n"
    "v_mfma_f32_16x16x32_bf16 v[204:207], a[160:163], a[28:31], 0\n"
    "ds_read_b128 a[188:191], v242 offset:22528\n"
    "v_mfma_f32_16x16x32_bf16 v[204:207], a[164:167], a[0:3], v[204:207]\n"
    "v_exp_f32_e32 v208, v208\n"
    "v_mfma_f32_16x16x32_bf16 v[212:215], a[164:167], a[32:35], 0\n"
    "v_exp_f32_e32 v209, v209\n"
    "v_mfma_f32_16x16x32_bf16 v[212:215], a[160:163], a[60:63], v[212:215]\n"
    "ds_read_b64_tr_b16 a[160:161], v244 offset:0\n"
    "v_mfma_f32_16x16x32_bf16 v[212:215], a[168:171], a[36:39], v[212:215]\n"
    "ds_read_b64_tr_b16 a[162:163], v244 offset:2048\n"
    "buffer_store_dwordx4 v[80:83], v253, s[8:11], s84 offen offset:1024 sc1\n"
    "v_exp_f32_e32 v210, v210\n"
    "v_mfma_f32_16x16x32_bf16 v[212:215], a[172:175], a[40:43], v[212:215]\n"
    "ds_read_b64_tr_b16 a[164:165], v244 offset:8192\n"
    "v_cvt_pk_bf16_f32 v112, v112, v113\n"
    "v_mfma_f32_16x16x32_bf16 v[204:207], a[172:175], a[8:11], v[204:207]\n"
    "ds_read_b64_tr_b16 a[166:167], v244 offset:10240\n"
    "v_exp_f32_e32 v211, v211\n"
    "v_mfma_f32_16x16x32_bf16 v[204:207], a[168:171], a[4:7], v[204:207]\n"
    "s_waitcnt lgkmcnt(4)\n"
    "ds_read_b64_tr_b16 a[168:169], v245 offset:0\n"
    "v_cvt_pk_bf16_f32 v113, v114, v115\n"
    "v_mfma_f32_16x16x32_bf16 v[204:207], a[176:179], a[12:15], v[204:207]\n"
    "ds_read_b64_tr_b16 a[170:171], v245 offset:2048\n"
    "v_mfma_f32_16x16x32_bf16 v[204:207], a[180:183], a[16:19], v[204:207]\n"
    "ds_read_b64_tr_b16 a[172:173], v245 offset:8192\n"
    "v_cvt_pk_bf16_f32 v208, v208, v209\n"
    "v_mfma_f32_16x16x32_bf16 v[212:215], a[180:183], a[48:51], v[212:215]\n"
    "ds_read_b64_tr_b16 a[174:175], v245 offset:10240\n"
    "v_cvt_pk_bf16_f32 v114, v124, v125\n"
    "v_mfma_f32_16x16x32_bf16 v[212:215], a[176:179], a[44:47], v[212:215]\n"
    "ds_read_b64_tr_b16 a[176:177], v246 offset:0\n"
    "v_exp_f32_e32 v200, v200\n"
    "v_mfma_f32_16x16x32_bf16 v[212:215], a[184:187], a[52:55], v[212:215]\n"
    "ds_read_b64_tr_b16 a[178:179], v246 offset:2048\n"
    "v_cvt_pk_bf16_f32 v115, v126, v127\n"
    "v_mfma_f32_16x16x32_bf16 v[212:215], a[188:191], a[56:59], v[212:215]\n"
    "ds_read_b64_tr_b16 a[180:181], v246 offset:8192\n"
    "buffer_store_dwordx4 v[112:115], v253, s[8:11], s86 offen offset:2048 sc1\n"
    "v_exp_f32_e32 v201, v201\n"
    "v_mfma_f32_16x16x32_bf16 v[204:207], a[188:191], a[24:27], v[204:207]\n"
    "ds_read_b64_tr_b16 a[182:183], v246 offset:10240\n"
    "v_cvt_pk_bf16_f32 v108, v108, v109\n"
    "v_mfma_f32_16x16x32_bf16 v[204:207], a[184:187], a[20:23], v[204:207]\n"
    "s_waitcnt lgkmcnt(4)\n"
    "ds_read_b64_tr_b16 a[184:185], v247 offset:0\n"
    "v_cvt_pk_bf16_f32 v209, v210, v211\n"
    "v_mfma_f32_16x16x32_bf16 v[0:3], a[160:163], v[160:163], v[0:3]\n"
    "ds_read_b64_tr_b16 a[186:187], v247 offset:2048\n"
    "v_cvt_pk_bf16_f32 v109, v110, v111\n"
    "v_mfma_f32_16x16x32_bf16 v[0:3], a[164:167], v[164:167], v[0:3]\n"
    "ds_read_b64_tr_b16 a[188:189], v247 offset:8192\n"
    "v_exp_f32_e32 v202, v202\n"
    "v_mfma_f32_16x16x32_bf16 a[248:251], a[164:167], v[172:175], a[248:251]\n"
    "ds_read_b64_tr_b16 a[190:191], v247 offset:10240\n"
    "v_cvt_pk_bf16_f32 v110, v120, v121\n"
    "v_mfma_f32_16x16x32_bf16 a[248:251], a[160:163], v[168:171], a[248:251]\n"
    "ds_read_b64_tr_b16 a[160:161], v244 offset:4096\n"
    "v_exp_f32_e32 v203, v203\n"
    "v_mfma_f32_16x16x32_bf16 a[252:255], a[168:171], v[168:171], a[252:255]\n"
    "ds_read_b64_tr_b16 a[162:163], v244 offset:6144\n"
    "v_cvt_pk_bf16_f32 v111, v122, v123\n"
    "v_mfma_f32_16x16x32_bf16 a[252:255], a[172:175], v[172:175], a[252:255]\n"
    "ds_read_b64_tr_b16 a[164:165], v244 offset:12288\n"
    "v_cvt_pk_bf16_f32 v200, v200, v201\n"
    "v_mfma_f32_16x16x32_bf16 v[4:7], a[172:175], v[164:167], v[4:7]\n"
    "ds_read_b64_tr_b16 a[166:167], v244 offset:14336\n"
    "ds_write_addtid_b32 v208 offset:4096\n"
    "v_mfma_f32_16x16x32_bf16 v[4:7], a[168:171], v[160:163], v[4:7]\n"
    "s_waitcnt lgkmcnt(5)\n"
    "ds_read_b64_tr_b16 a[168:169], v245 offset:4096\n"
    "v_cvt_pk_bf16_f32 v201, v202, v203\n"
    "v_mfma_f32_16x16x32_bf16 v[8:11], a[176:179], v[160:163], v[8:11]\n"
    "ds_read_b64_tr_b16 a[170:171], v245 offset:6144\n"
    "buffer_store_dwordx4 v[108:111], v253, s[8:11], s85 offen offset:2048 sc1\n"
    "v_cvt_pk_bf16_f32 v104, v104, v105\n"
    "v_mfma_f32_16x16x32_bf16 v[8:11], a[180:183], v[164:167], v[8:11]\n"
    "ds_read_b64_tr_b16 a[172:173], v245 offset:12288\n"
    "v_cvt_pk_bf16_f32 v105, v106, v107\n"
    "v_mfma_f32_16x16x32_bf16 v[32:35], a[180:183], v[172:175], v[32:35]\n"
    "ds_read_b64_tr_b16 a[174:175], v245 offset:14336\n"
    "v_exp_f32_e32 v212, v212\n"
    "v_mfma_f32_16x16x32_bf16 v[32:35], a[176:179], v[168:171], v[32:35]\n"
    "ds_read_b64_tr_b16 a[176:177], v246 offset:4096\n"
    "ds_write_addtid_b32 v209 offset:4352\n"
    "v_mfma_f32_16x16x32_bf16 v[36:39], a[184:187], v[168:171], v[36:39]\n"
    "ds_read_b64_tr_b16 a[178:179], v246 offset:6144\n"
    "v_exp_f32_e32 v213, v213\n"
    "v_mfma_f32_16x16x32_bf16 v[36:39], a[188:191], v[172:175], v[36:39]\n"
    "ds_read_b64_tr_b16 a[180:181], v246 offset:12288\n"
    "v_cvt_pk_bf16_f32 v106, v116, v117\n"
    "v_mfma_f32_16x16x32_bf16 v[12:15], a[188:191], v[164:167], v[12:15]\n"
    "ds_read_b64_tr_b16 a[182:183], v246 offset:14336\n"
    "v_cvt_pk_bf16_f32 v107, v118, v119\n"
    "v_mfma_f32_16x16x32_bf16 v[12:15], a[184:187], v[160:163], v[12:15]\n"
    "s_waitcnt lgkmcnt(5)\n"
    "ds_read_b64_tr_b16 a[184:185], v247 offset:4096\n"
    "ds_write_addtid_b32 v200 offset:0\n"
    "v_mfma_f32_16x16x32_bf16 v[16:19], a[160:163], v[160:163], v[16:19]\n"
    "ds_read_b64_tr_b16 a[186:187], v247 offset:6144\n"
    "buffer_store_dwordx4 v[104:107], v253, s[8:11], s84 offen offset:2048 sc1\n"
    "v_cvt_pk_bf16_f32 v136, v136, v137\n"
    "v_mfma_f32_16x16x32_bf16 v[16:19], a[164:167], v[164:167], v[16:19]\n"
    "ds_read_b64_tr_b16 a[188:189], v247 offset:12288\n"
    "v_mfma_f32_16x16x32_bf16 v[40:43], a[164:167], v[172:175], v[40:43]\n"
    "ds_read_b64_tr_b16 a[190:191], v247 offset:14336\n"
    "v_cvt_pk_bf16_f32 v137, v138, v139\n"
    "v_mfma_f32_16x16x32_bf16 v[40:43], a[160:163], v[168:171], v[40:43]\n"
    "ds_read_b128 a[160:163], v243 offset:36864\n"
    "ds_write_addtid_b32 v201 offset:256\n"
    "v_mfma_f32_16x16x32_bf16 v[44:47], a[168:171], v[168:171], v[44:47]\n"
    "ds_read_b128 a[164:167], v240 offset:32768\n"
    "v_cvt_pk_bf16_f32 v138, v148, v149\n"
    "v_mfma_f32_16x16x32_bf16 v[44:47], a[172:175], v[172:175], v[44:47]\n"
    "v_exp_f32_e32 v214, v214\n"
    "v_mfma_f32_16x16x32_bf16 v[20:23], a[172:175], v[164:167], v[20:23]\n"
    "v_exp_f32_e32 v215, v215\n"
    "v_mfma_f32_16x16x32_bf16 v[20:23], a[168:171], v[160:163], v[20:23]\n"
    "s_waitcnt lgkmcnt(3)\n"
    "ds_read_b128 a[168:171], v241 offset:32768\n"
    "v_cvt_pk_bf16_f32 v210, v212, v213\n"
    "v_mfma_f32_16x16x32_bf16 v[24:27], a[176:179], v[160:163], v[24:27]\n"
    "ds_read_b128 a[172:175], v240 offset:36864\n"
    "v_cvt_pk_bf16_f32 v139, v150, v151\n"
    "v_mfma_f32_16x16x32_bf16 v[24:27], a[180:183], v[164:167], v[24:27]\n"
    "v_exp_f32_e32 v204, v204\n"
    "v_mfma_f32_16x16x32_bf16 v[48:51], a[180:183], v[172:175], v[48:51]\n"
    "buffer_store_dwordx4 v[136:139], v253, s[8:11], s86 offen offset:3072 sc1\n"
    "v_exp_f32_e32 v205, v205\n"
    "v_mfma_f32_16x16x32_bf16 v[48:51], a[176:179], v[168:171], v[48:51]\n"
    "ds_read_b128 a[176:179], v241 offset:36864\n"
    "v_cvt_pk_bf16_f32 v211, v214, v215\n"
    "v_mfma_f32_16x16x32_bf16 v[52:55], a[184:187], v[168:171], v[52:55]\n"
    "ds_read_b128 a[180:183], v242 offset:32768\n"
    "v_cvt_pk_bf16_f32 v132, v132, v133\n"
    "v_mfma_f32_16x16x32_bf16 v[52:55], a[188:191], v[172:175], v[52:55]\n"
    "v_exp_f32_e32 v206, v206\n"
    "v_mfma_f32_16x16x32_bf16 v[28:31], a[188:191], v[164:167], v[28:31]\n"
    "v_exp_f32_e32 v207, v207\n"
    "v_mfma_f32_16x16x32_bf16 v[28:31], a[184:187], v[160:163], v[28:31]\n"
    "s_waitcnt lgkmcnt(2)\n"
    "ds_read_b128 a[184:187], v243 offset:32768\n"
    "v_cvt_pk_bf16_f32 v202, v204, v205\n"
    "v_mfma_f32_16x16x32_bf16 v[160:163], a[160:163], a[28:31], 0\n"
    "ds_read_b128 a[188:191], v242 offset:36864\n"
    "v_cvt_pk_bf16_f32 v133, v134, v135\n"
    "v_mfma_f32_16x16x32_bf16 v[160:163], a[164:167], a[0:3], v[160:163]\n"
    "ds_write_addtid_b32 v210 offset:4608\n"
    "v_cvt_pk_bf16_f32 v203, v206, v207\n"
    "v_mfma_f32_16x16x32_bf16 v[168:171], a[164:167], a[32:35], 0\n"
    "ds_write_addtid_b32 v211 offset:4864\n"
    "v_cvt_pk_bf16_f32 v134, v144, v145\n"
    "v_mfma_f32_16x16x32_bf16 v[168:171], a[160:163], a[60:63], v[168:171]\n"
    "ds_read_b128 a[160:163], v243 offset:38912\n"
    "v_cvt_pk_bf16_f32 v135, v146, v147\n"
    "v_mfma_f32_16x16x32_bf16 v[168:171], a[168:171], a[36:39], v[168:171]\n"
    "ds_read_b128 a[164:167], v240 offset:34816\n"
    "buffer_store_dwordx4 v[132:135], v253, s[8:11], s85 offen offset:3072 sc1\n"
    "v_cvt_pk_bf16_f32 v128, v128, v129\n"
    "v_mfma_f32_16x16x32_bf16 v[168:171], a[172:175], a[40:43], v[168:171]\n"
    "ds_write_addtid_b32 v202 offset:512\n"
    "v_mfma_f32_16x16x32_bf16 v[160:163], a[172:175], a[8:11], v[160:163]\n"
    "ds_write_addtid_b32 v203 offset:768\n"
    "v_mfma_f32_16x16x32_bf16 v[160:163], a[168:171], a[4:7], v[160:163]\n"
    "s_waitcnt lgkmcnt(6)\n"
    "ds_read_b128 a[168:171], v241 offset:34816\n"
    "v_mfma_f32_16x16x32_bf16 v[160:163], a[176:179], a[12:15], v[160:163]\n"
    "ds_read_b128 a[172:175], v240 offset:38912\n"
    "v_mfma_f32_16x16x32_bf16 v[160:163], a[180:183], a[16:19], v[160:163]\n"
    "v_cvt_pk_bf16_f32 v129, v130, v131\n"
    "v_mfma_f32_16x16x32_bf16 v[168:171], a[180:183], a[48:51], v[168:171]\n"
    "s_waitcnt lgkmcnt(2)\n"
    "s_barrier\n"
    "v_mfma_f32_16x16x32_bf16 v[168:171], a[176:179], a[44:47], v[168:171]\n"
    "s_lshl_b32 s45, s89, 8\n"
    "ds_read_b128 a[176:179], v241 offset:38912\n"
    "v_add_u32_e32 v254, s45, v255\n"
    "v_mfma_f32_16x16x32_bf16 v[168:171], a[184:187], a[52:55], v[168:171]\n"
    "ds_read_b128 a[180:183], v242 offset:34816\n"
    "v_cvt_pk_bf16_f32 v130, v140, v141\n"
    "v_mfma_f32_16x16x32_bf16 v[168:171], a[188:191], a[56:59], v[168:171]\n"
    "ds_read2st64_b32 v[204:205], v250 offset0:0 offset1:1\n"
    "v_mfma_f32_16x16x32_bf16 v[160:163], a[188:191], a[24:27], v[160:163]\n"
    "s_lshl_b32 s45, s50, 8\n"
    "ds_read2st64_b32 v[206:207], v250 offset0:2 offset1:3\n"
    "v_add_u32_e32 v255, s45, v255\n"
    "v_mfma_f32_16x16x32_bf16 v[160:163], a[184:187], a[20:23], v[160:163]\n"
    "s_waitcnt lgkmcnt(4)\n"
    "ds_read_b128 a[184:187], v243 offset:34816\n"
    "v_cvt_pk_bf16_f32 v131, v142, v143\n"
    "v_mfma_f32_16x16x32_bf16 v[164:167], a[160:163], a[28:31], 0\n"
    "ds_read_b128 a[188:191], v242 offset:38912\n"
    "buffer_store_dwordx4 v[128:131], v253, s[8:11], s84 offen offset:3072 sc1\n"
    "v_exp_f32_e32 v168, v168\n"
    "v_mfma_f32_16x16x32_bf16 v[164:167], a[164:167], a[0:3], v[164:167]\n"
    "ds_read2st64_b32 v[212:213], v250 offset0:16 offset1:17\n"
    "v_permlane16_swap_b32_e32 v147, v151\n"
    "v_mfma_f32_16x16x32_bf16 v[172:175], a[164:167], a[32:35], 0\n"
    "ds_read2st64_b32 v[214:215], v250 offset0:18 offset1:19\n"
    "v_exp_f32_e32 v169, v169\n"
    "v_mfma_f32_16x16x32_bf16 v[172:175], a[160:163], a[60:63], v[172:175]\n"
    "ds_read_b64_tr_b16 a[160:161], v244 offset:16384\n"
    "v_mov_b32_e32 v148, s39\n"
    "v_mov_b32_e32 v149, 0\n"
    "v_mfma_f32_16x16x32_bf16 v[172:175], a[168:171], a[36:39], v[172:175]\n"
    "ds_read_b64_tr_b16 a[162:163], v244 offset:18432\n"
    "v_exp_f32_e32 v170, v170\n"
    "v_mfma_f32_16x16x32_bf16 v[172:175], a[172:175], a[40:43], v[172:175]\n"
    "ds_read_b64_tr_b16 a[164:165], v244 offset:24576\n"
    "v_permlane32_swap_b32_e32 v143, v151\n"
    "v_mfma_f32_16x16x32_bf16 v[164:167], a[172:175], a[8:11], v[164:167]\n"
    "ds_read_b64_tr_b16 a[166:167], v244 offset:26624\n"
    "buffer_store_dword v151, v251, s[8:11], s60 offen offset:0 sc1\n"
    "v_exp_f32_e32 v171, v171\n"
    "v_mfma_f32_16x16x32_bf16 v[164:167], a[168:171], a[4:7], v[164:167]\n"
    "s_waitcnt lgkmcnt(6)\n"
    "ds_read_b64_tr_b16 a[168:169], v245 offset:16384\n"
    "v_mfma_f32_16x16x32_bf16 v[164:167], a[176:179], a[12:15], v[164:167]\n"
    "ds_read_b64_tr_b16 a[170:171], v245 offset:18432\n"
    "v_lshrrev_b32_e32 v147, 4, v253\n"
    "v_mfma_f32_16x16x32_bf16 v[164:167], a[180:183], a[16:19], v[164:167]\n"
    "ds_read_b64_tr_b16 a[172:173], v245 offset:24576\n"
    "v_cvt_pk_bf16_f32 v168, v168, v169\n"
    "v_mfma_f32_16x16x32_bf16 v[172:175], a[180:183], a[48:51], v[172:175]\n"
    "ds_read_b64_tr_b16 a[174:175], v245 offset:26624\n"
    "v_bfe_u32 v145, v147, 2, 2\n"
    "v_mfma_f32_16x16x32_bf16 v[172:175], a[176:179], a[44:47], v[172:175]\n"
    "ds_read_b64_tr_b16 a[176:177], v246 offset:16384\n"
    "v_exp_f32_e32 v160, v160\n"
    "v_mfma_f32_16x16x32_bf16 v[172:175], a[184:187], a[52:55], v[172:175]\n"
    "ds_read_b64_tr_b16 a[178:179], v246 offset:18432\n"
    "v_mfma_f32_16x16x32_bf16 v[172:175], a[188:191], a[56:59], v[172:175]\n"
    "ds_read_b64_tr_b16 a[180:181], v246 offset:24576\n"
    "v_exp_f32_e32 v161, v161\n"
    "v_mfma_f32_16x16x32_bf16 v[164:167], a[188:191], a[24:27], v[164:167]\n"
    "ds_read_b64_tr_b16 a[182:183], v246 offset:26624\n"
    "v_min_u32_e32 v145, 2, v145\n"
    "v_mfma_f32_16x16x32_bf16 v[164:167], a[184:187], a[20:23], v[164:167]\n"
    "s_waitcnt lgkmcnt(4)\n"
    "ds_read_b64_tr_b16 a[184:185], v247 offset:16384\n"
    "v_cvt_pk_bf16_f32 v169, v170, v171\n"
    "v_mfma_f32_16x16x32_bf16 v[0:3], a[160:163], v[200:203], v[0:3]\n"
    "s_add_u32 s46, s32, 1\n"
    "ds_read_b64_tr_b16 a[186:187], v247 offset:18432\n"
    "v_mfma_f32_16x16x32_bf16 v[0:3], a[164:167], v[204:207], v[0:3]\n"
    "ds_read_b64_tr_b16 a[188:189], v247 offset:24576\n"
    "v_exp_f32_e32 v162, v162\n"
    "v_mfma_f32_16x16x32_bf16 a[248:251], a[164:167], v[212:215], a[248:251]\n"
    "ds_read_b64_tr_b16 a[190:191], v247 offset:26624\n"
    "v_add_u32_e32 v145, s46, v145\n"
    "v_mfma_f32_16x16x32_bf16 a[248:251], a[160:163], v[208:211], a[248:251]\n"
    "ds_read_b64_tr_b16 a[160:161], v244 offset:20480\n"
    "v_exp_f32_e32 v163, v163\n"
    "v_mfma_f32_16x16x32_bf16 a[252:255], a[168:171], v[208:211], a[252:255]\n"
    "ds_read_b64_tr_b16 a[162:163], v244 offset:22528\n"
    "v_and_b32_e32 v145, 3, v145\n"
    "v_mfma_f32_16x16x32_bf16 a[252:255], a[172:175], v[212:215], a[252:255]\n"
    "s_barrier\n"
    "v_mfma_f32_16x16x32_bf16 v[4:7], a[172:175], v[204:207], v[4:7]\n"
    "ds_read_b64_tr_b16 a[164:165], v244 offset:28672\n"
    "v_cvt_pk_bf16_f32 v160, v160, v161\n"
    "v_mfma_f32_16x16x32_bf16 v[4:7], a[168:171], v[200:203], v[4:7]\n"
    "s_waitcnt lgkmcnt(3)\n"
    "ds_read_b64_tr_b16 a[166:167], v244 offset:30720\n"
    "v_cvt_pk_bf16_f32 v161, v162, v163\n"
    "v_mfma_f32_16x16x32_bf16 v[8:11], a[176:179], v[200:203], v[8:11]\n"
    "ds_read_b64_tr_b16 a[168:169], v245 offset:20480\n"
    "ds_write_addtid_b32 v168 offset:4096\n"
    "v_mfma_f32_16x16x32_bf16 v[8:11], a[180:183], v[204:207], v[8:11]\n"
    "ds_read_b64_tr_b16 a[170:171], v245 offset:22528\n"
    "v_lshlrev_b32_e32 v145, 4, v145\n"
    "v_mfma_f32_16x16x32_bf16 v[32:35], a[180:183], v[212:215], v[32:35]\n"
    "ds_read_b64_tr_b16 a[172:173], v245 offset:28672\n"
    "v_exp_f32_e32 v172, v172\n"
    "v_mfma_f32_16x16x32_bf16 v[32:35], a[176:179], v[208:211], v[32:35]\n"
    "ds_read_b64_tr_b16 a[174:175], v245 offset:30720\n"
    "v_and_or_b32 v145, v147, 3, v145\n"
    "v_mfma_f32_16x16x32_bf16 v[36:39], a[184:187], v[208:211], v[36:39]\n"
    "ds_read_b64_tr_b16 a[176:177], v246 offset:20480\n"
    "v_exp_f32_e32 v173, v173\n"
    "v_mfma_f32_16x16x32_bf16 v[36:39], a[188:191], v[212:215], v[36:39]\n"
    "ds_read_b64_tr_b16 a[178:179], v246 offset:22528\n"
    "ds_write_addtid_b32 v169 offset:4352\n"
    "v_mfma_f32_16x16x32_bf16 v[12:15], a[188:191], v[204:207], v[12:15]\n"
    "ds_read_b64_tr_b16 a[180:181], v246 offset:28672\n"
    "v_exp_f32_e32 v174, v174\n"
    "v_mfma_f32_16x16x32_bf16 v[12:15], a[184:187], v[200:203], v[12:15]\n"
    "s_waitcnt lgkmcnt(4)\n"
    "ds_read_b64_tr_b16 a[182:183], v246 offset:30720\n"
    "v_lshlrev_b32_e32 v145, 2, v145\n"
    "v_mfma_f32_16x16x32_bf16 v[16:19], a[160:163], v[200:203], v[16:19]\n"
    "s_lshl_b32 s47, s33, 8\n"
    "ds_read_b64_tr_b16 a[184:185], v247 offset:20480\n"
    "ds_write_addtid_b32 v160 offset:0\n"
    "v_mfma_f32_16x16x32_bf16 v[16:19], a[164:167], v[204:207], v[16:19]\n"
    "ds_read_b64_tr_b16 a[186:187], v247 offset:22528\n"
    "v_mfma_f32_16x16x32_bf16 v[40:43], a[164:167], v[212:215], v[40:43]\n"
    "s_add_u32 s47, s47, 5636096\n"
    "ds_read_b64_tr_b16 a[188:189], v247 offset:28672\n"
    "v_mfma_f32_16x16x32_bf16 v[40:43], a[160:163], v[208:211], v[40:43]\n"
    "ds_read_b64_tr_b16 a[190:191], v247 offset:30720\n"
    "v_mfma_f32_16x16x32_bf16 v[44:47], a[168:171], v[208:211], v[44:47]\n"
    "ds_read_b128 a[160:163], v243 offset:53248\n"
    "ds_write_addtid_b32 v161 offset:256\n"
    "v_mfma_f32_16x16x32_bf16 v[44:47], a[172:175], v[212:215], v[44:47]\n"
    "ds_read_b128 a[164:167], v240 offset:49152\n"
    "v_cmp_gt_u32_e32 vcc, 0x300, v253\n"
    "v_mfma_f32_16x16x32_bf16 v[20:23], a[172:175], v[204:207], v[20:23]\n"
    "v_exp_f32_e32 v175, v175\n"
    "v_mfma_f32_16x16x32_bf16 v[20:23], a[168:171], v[200:203], v[20:23]\n"
    "s_waitcnt lgkmcnt(3)\n"
    "ds_read_b128 a[168:171], v241 offset:49152\n"
    "v_cvt_pk_bf16_f32 v170, v172, v173\n"
    "v_mfma_f32_16x16x32_bf16 v[24:27], a[176:179], v[200:203], v[24:27]\n"
    "s_add_u32 s60, s60, s28\n"
    "ds_read_b128 a[172:175], v240 offset:53248\n"
    "v_and_b32_e32 v251, 0xf0, v253\n"
    "v_mfma_f32_16x16x32_bf16 v[24:27], a[180:183], v[204:207], v[24:27]\n"
    "s_add_u32 s48, s56, 0\n"
    "v_exp_f32_e32 v164, v164\n"
    "v_mfma_f32_16x16x32_bf16 v[48:51], a[180:183], v[212:215], v[48:51]\n"
    "v_exp_f32_e32 v165, v165\n"
    "v_mfma_f32_16x16x32_bf16 v[48:51], a[176:179], v[208:211], v[48:51]\n"
    "ds_read_b128 a[176:179], v241 offset:53248\n"
    "v_cvt_pk_bf16_f32 v171, v174, v175\n"
    "v_mfma_f32_16x16x32_bf16 v[52:55], a[184:187], v[208:211], v[52:55]\n"
    "ds_read_b128 a[180:183], v242 offset:49152\n"
    "buffer_load_dwordx4 v[56:59], v253, s[8:11], s48 offen offset:0 sc1\n"
    "v_cndmask_b32_e64 v251, v251, 2.0, vcc\n"
    "v_mfma_f32_16x16x32_bf16 v[52:55], a[188:191], v[212:215], v[52:55]\n"
    "v_exp_f32_e32 v166, v166\n"
    "v_mfma_f32_16x16x32_bf16 v[28:31], a[188:191], v[204:207], v[28:31]\n"
    "v_exp_f32_e32 v167, v167\n"
    "v_mfma_f32_16x16x32_bf16 v[28:31], a[184:187], v[200:203], v[28:31]\n"
    "s_waitcnt lgkmcnt(2)\n"
    "ds_read_b128 a[184:187], v243 offset:49152\n"
    "v_cvt_pk_bf16_f32 v162, v164, v165\n"
    "v_mfma_f32_16x16x32_bf16 v[200:203], a[160:163], a[28:31], 0\n"
    "ds_read_b128 a[188:191], v242 offset:53248\n"
    "buffer_load_dwordx4 v[60:63], v253, s[8:11], s48 offen offset:1024 sc1\n"
    "v_mfma_f32_16x16x32_bf16 v[200:203], a[164:167], a[0:3], v[200:203]\n"
    "ds_write_addtid_b32 v170 offset:4608\n"
    "v_cvt_pk_bf16_f32 v163, v166, v167\n"
    "v_mfma_f32_16x16x32_bf16 v[208:211], a[164:167], a[32:35], 0\n"
    "ds_write_addtid_b32 v171 offset:4864\n"
    "v_mfma_f32_16x16x32_bf16 v[208:211], a[160:163], a[60:63], v[208:211]\n"
    "ds_read_b128 a[160:163], v243 offset:55296\n"
    "v_mfma_f32_16x16x32_bf16 v[208:211], a[168:171], a[36:39], v[208:211]\n"
    "ds_read_b128 a[164:167], v240 offset:51200\n"
    "buffer_load_dwordx4 v[64:67], v253, s[8:11], s48 offen offset:2048 sc1\n"
    "v_mfma_f32_16x16x32_bf16 v[208:211], a[172:175], a[40:43], v[208:211]\n"
    "ds_write_addtid_b32 v162 offset:512\n"
    "v_mfma_f32_16x16x32_bf16 v[200:203], a[172:175], a[8:11], v[200:203]\n"
    "ds_write_addtid_b32 v163 offset:768\n"
    "v_mfma_f32_16x16x32_bf16 v[200:203], a[168:171], a[4:7], v[200:203]\n"
    "s_waitcnt lgkmcnt(6)\n"
    "ds_read_b128 a[168:171], v241 offset:51200\n"
    "v_mfma_f32_16x16x32_bf16 v[200:203], a[176:179], a[12:15], v[200:203]\n"
    "ds_read_b128 a[172:175], v240 offset:55296\n"
    "buffer_load_dwordx4 v[68:71], v253, s[8:11], s48 offen offset:3072 sc1\n"
    "v_mfma_f32_16x16x32_bf16 v[200:203], a[180:183], a[16:19], v[200:203]\n"
    "s_waitcnt vmcnt(4)\n"
    "buffer_store_dword v148, v149, s[8:11], s87 offen offset:32 sc1\n"
    "v_mfma_f32_16x16x32_bf16 v[208:211], a[180:183], a[48:51], v[208:211]\n"
    "s_waitcnt lgkmcnt(2)\n"
    "s_barrier\n"
    "v_mfma_f32_16x16x32_bf16 v[208:211], a[176:179], a[44:47], v[208:211]\n"
    "s_add_u32 s48, s56, 4096\n"
    "ds_read_b128 a[176:179], v241 offset:55296\n"
    "v_mfma_f32_16x16x32_bf16 v[208:211], a[184:187], a[52:55], v[208:211]\n"
    "ds_read_b128 a[180:183], v242 offset:51200\n"
    "buffer_load_dwordx4 v[72:75], v253, s[8:11], s48 offen offset:0 sc1\n"
    "v_mfma_f32_16x16x32_bf16 v[208:211], a[188:191], a[56:59], v[208:211]\n"
    "ds_read2st64_b32 v[164:165], v250 offset0:0 offset1:1\n"
    "v_mfma_f32_16x16x32_bf16 v[200:203], a[188:191], a[24:27], v[200:203]\n"
    "ds_read2st64_b32 v[166:167], v250 offset0:2 offset1:3\n"
    "v_mfma_f32_16x16x32_bf16 v[200:203], a[184:187], a[20:23], v[200:203]\n"
    "s_waitcnt lgkmcnt(4)\n"
    "ds_read_b128 a[184:187], v243 offset:51200\n"
    "v_mfma_f32_16x16x32_bf16 v[204:207], a[160:163], a[28:31], 0\n"
    "ds_read_b128 a[188:191], v242 offset:55296\n"
    "buffer_load_dwordx4 v[76:79], v253, s[8:11], s48 offen offset:1024 sc1\n"
    "v_exp_f32_e32 v208, v208\n"
    "v_mfma_f32_16x16x32_bf16 v[204:207], a[164:167], a[0:3], v[204:207]\n"
    "ds_read2st64_b32 v[172:173], v250 offset0:16 offset1:17\n"
    "v_exp_f32_e32 v209, v209\n"
    "v_mfma_f32_16x16x32_bf16 v[212:215], a[164:167], a[32:35], 0\n"
    "ds_read2st64_b32 v[174:175], v250 offset0:18 offset1:19\n"
    "v_mfma_f32_16x16x32_bf16 v[212:215], a[160:163], a[60:63], v[212:215]\n"
    "ds_read_b64_tr_b16 a[160:161], v244 offset:32768\n"
    "v_exp_f32_e32 v210, v210\n"
    "v_mfma_f32_16x16x32_bf16 v[212:215], a[168:171], a[36:39], v[212:215]\n"
    "ds_read_b64_tr_b16 a[162:163], v244 offset:34816\n"
    "buffer_load_dwordx4 v[80:83], v253, s[8:11], s48 offen offset:2048 sc1\n"
    "v_mfma_f32_16x16x32_bf16 v[212:215], a[172:175], a[40:43], v[212:215]\n"
    "ds_read_b64_tr_b16 a[164:165], v244 offset:40960\n"
    "v_exp_f32_e32 v211, v211\n"
    "v_mfma_f32_16x16x32_bf16 v[204:207], a[172:175], a[8:11], v[204:207]\n"
    "ds_read_b64_tr_b16 a[166:167], v244 offset:43008\n"
    "v_cvt_pk_bf16_f32 v208, v208, v209\n"
    "v_mfma_f32_16x16x32_bf16 v[204:207], a[168:171], a[4:7], v[204:207]\n"
    "s_waitcnt lgkmcnt(6)\n"
    "ds_read_b64_tr_b16 a[168:169], v245 offset:32768\n"
    "v_mfma_f32_16x16x32_bf16 v[204:207], a[176:179], a[12:15], v[204:207]\n"
    "ds_read_b64_tr_b16 a[170:171], v245 offset:34816\n"
    "buffer_load_dwordx4 v[84:87], v253, s[8:11], s48 offen offset:3072 sc1\n"
    "v_exp_f32_e32 v200, v200\n"
    "v_mfma_f32_16x16x32_bf16 v[204:207], a[180:183], a[16:19], v[204:207]\n"
    "s_add_u32 s48, s56, 8192\n"
    "ds_read_b64_tr_b16 a[172:173], v245 offset:40960\n"
    "v_mfma_f32_16x16x32_bf16 v[212:215], a[180:183], a[48:51], v[212:215]\n"
    "ds_read_b64_tr_b16 a[174:175], v245 offset:43008\n"
    "v_exp_f32_e32 v201, v201\n"
    "v_mfma_f32_16x16x32_bf16 v[212:215], a[176:179], a[44:47], v[212:215]\n"
    "ds_read_b64_tr_b16 a[176:177], v246 offset:32768\n"
    "v_cvt_pk_bf16_f32 v209, v210, v211\n"
    "v_mfma_f32_16x16x32_bf16 v[212:215], a[184:187], a[52:55], v[212:215]\n"
    "ds_read_b64_tr_b16 a[178:179], v246 offset:34816\n"
    "buffer_load_dwordx4 v[88:91], v253, s[8:11], s48 offen offset:0 sc1\n"
    "v_mfma_f32_16x16x32_bf16 v[212:215], a[188:191], a[56:59], v[212:215]\n"
    "ds_read_b64_tr_b16 a[180:181], v246 offset:40960\n"
    "v_exp_f32_e32 v202, v202\n"
    "v_mfma_f32_16x16x32_bf16 v[204:207], a[188:191], a[24:27], v[204:207]\n"
    "ds_read_b64_tr_b16 a[182:183], v246 offset:43008\n"
    "v_exp_f32_e32 v203, v203\n"
    "v_mfma_f32_16x16x32_bf16 v[204:207], a[184:187], a[20:23], v[204:207]\n"
    "s_waitcnt lgkmcnt(4)\n"
    "ds_read_b64_tr_b16 a[184:185], v247 offset:32768\n"
    "v_mfma_f32_16x16x32_bf16 v[0:3], a[160:163], v[160:163], v[0:3]\n"
    "ds_read_b64_tr_b16 a[186:187], v247 offset:34816\n"
    "buffer_load_dwordx4 v[92:95], v253, s[8:11], s48 offen offset:1024 sc1\n"
    "v_cvt_pk_bf16_f32 v200, v200, v201\n"
    "v_mfma_f32_16x16x32_bf16 v[0:3], a[164:167], v[164:167], v[0:3]\n"
    "ds_read_b64_tr_b16 a[188:189], v247 offset:40960\n"
    "v_and_b32_e32 v242, 0x400, v240\n"
    "v_mfma_f32_16x16x32_bf16 a[248:251], a[164:167], v[172:175], a[248:251]\n"
    "ds_read_b64_tr_b16 a[190:191], v247 offset:43008\n"
    "v_cvt_pk_bf16_f32 v201, v202, v203\n"
    "v_mfma_f32_16x16x32_bf16 a[248:251], a[160:163], v[168:171], a[248:251]\n"
    "ds_read_b64_tr_b16 a[160:161], v244 offset:36864\n"
    "v_exp_f32_e32 v212, v212\n"
    "v_mfma_f32_16x16x32_bf16 a[252:255], a[168:171], v[168:171], a[252:255]\n"
    "ds_read_b64_tr_b16 a[162:163], v244 offset:38912\n"
    "buffer_load_dwordx4 v[96:99], v253, s[8:11], s48 offen offset:2048 sc1\n"
    "v_exp_f32_e32 v213, v213\n"
    "v_mfma_f32_16x16x32_bf16 a[252:255], a[172:175], v[172:175], a[252:255]\n"
    "s_waitcnt vmcnt(24)\n"
    "s_barrier\n"
    "v_mfma_f32_16x16x32_bf16 v[4:7], a[172:175], v[164:167], v[4:7]\n"
    "ds_read_b64_tr_b16 a[164:165], v244 offset:45056\n"
    "ds_write_addtid_b32 v208 offset:4096\n"
    "v_mfma_f32_16x16x32_bf16 v[4:7], a[168:171], v[160:163], v[4:7]\n"
    "s_waitcnt lgkmcnt(4)\n"
    "ds_read_b64_tr_b16 a[166:167], v244 offset:47104\n"
    "v_exp_f32_e32 v214, v214\n"
    "v_mfma_f32_16x16x32_bf16 v[8:11], a[176:179], v[160:163], v[8:11]\n"
    "ds_read_b64_tr_b16 a[168:169], v245 offset:36864\n"
    "buffer_load_dwordx4 v[100:103], v253, s[8:11], s48 offen offset:3072 sc1\n"
    "v_exp_f32_e32 v215, v215\n"
    "v_mfma_f32_16x16x32_bf16 v[8:11], a[180:183], v[164:167], v[8:11]\n"
    "s_add_u32 s48, s56, 12288\n"
    "ds_read_b64_tr_b16 a[170:171], v245 offset:38912\n"
    "ds_write_addtid_b32 v209 offset:4352\n"
    "v_mfma_f32_16x16x32_bf16 v[32:35], a[180:183], v[172:175], v[32:35]\n"
    "ds_read_b64_tr_b16 a[172:173], v245 offset:45056\n"
    "v_cvt_pk_bf16_f32 v210, v212, v213\n"
    "v_mfma_f32_16x16x32_bf16 v[32:35], a[176:179], v[168:171], v[32:35]\n"
    "ds_read_b64_tr_b16 a[174:175], v245 offset:47104\n"
    "v_exp_f32_e32 v204, v204\n"
    "v_mfma_f32_16x16x32_bf16 v[36:39], a[184:187], v[168:171], v[36:39]\n"
    "ds_read_b64_tr_b16 a[176:177], v246 offset:36864\n"
    "ds_write_addtid_b32 v200 offset:0\n"
    "buffer_load_dwordx4 v[104:107], v253, s[8:11], s48 offen offset:0 sc1\n"
    "v_mfma_f32_16x16x32_bf16 v[36:39], a[188:191], v[172:175], v[36:39]\n"
    "ds_read_b64_tr_b16 a[178:179], v246 offset:38912\n"
    "v_exp_f32_e32 v205, v205\n"
    "v_mfma_f32_16x16x32_bf16 v[12:15], a[188:191], v[164:167], v[12:15]\n"
    "ds_read_b64_tr_b16 a[180:181], v246 offset:45056\n"
    "ds_write_addtid_b32 v201 offset:256\n"
    "v_mfma_f32_16x16x32_bf16 v[12:15], a[184:187], v[160:163], v[12:15]\n"
    "s_waitcnt lgkmcnt(5)\n"
    "ds_read_b64_tr_b16 a[182:183], v246 offset:47104\n"
    "v_cvt_pk_bf16_f32 v211, v214, v215\n"
    "v_mfma_f32_16x16x32_bf16 v[16:19], a[160:163], v[160:163], v[16:19]\n"
    "ds_read_b64_tr_b16 a[184:185], v247 offset:36864\n"
    "buffer_load_dwordx4 v[108:111], v253, s[8:11], s48 offen offset:1024 sc1\n"
    "v_exp_f32_e32 v206, v206\n"
    "v_mfma_f32_16x16x32_bf16 v[16:19], a[164:167], v[164:167], v[16:19]\n"
    "ds_read_b64_tr_b16 a[186:187], v247 offset:38912\n"
    "ds_write_addtid_b32 v210 offset:4608\n"
    "v_mfma_f32_16x16x32_bf16 v[40:43], a[164:167], v[172:175], v[40:43]\n"
    "ds_read_b64_tr_b16 a[188:189], v247 offset:45056\n"
    "v_exp_f32_e32 v207, v207\n"
    "v_mfma_f32_16x16x32_bf16 v[40:43], a[160:163], v[168:171], v[40:43]\n"
    "ds_read_b64_tr_b16 a[190:191], v247 offset:47104\n"
    "ds_write_addtid_b32 v211 offset:4864\n"
    "v_mfma_f32_16x16x32_bf16 v[44:47], a[168:171], v[168:171], v[44:47]\n"
    "ds_read_b64_tr_b16 a[160:161], v244 offset:49152\n"
    "buffer_load_dwordx4 v[112:115], v253, s[8:11], s48 offen offset:2048 sc1\n"
    "v_cvt_pk_bf16_f32 v202, v204, v205\n"
    "v_mfma_f32_16x16x32_bf16 v[44:47], a[172:175], v[172:175], v[44:47]\n"
    "ds_read_b64_tr_b16 a[162:163], v244 offset:51200\n"
    "v_cvt_pk_bf16_f32 v203, v206, v207\n"
    "v_mfma_f32_16x16x32_bf16 v[20:23], a[172:175], v[164:167], v[20:23]\n"
    "ds_read_b64_tr_b16 a[164:165], v244 offset:57344\n"
    "v_and_b32_e32 v240, 0x3ff, v240\n"
    "v_mfma_f32_16x16x32_bf16 v[20:23], a[168:171], v[160:163], v[20:23]\n"
    "s_waitcnt lgkmcnt(4)\n"
    "ds_read_b64_tr_b16 a[166:167], v244 offset:59392\n"
    "ds_write_addtid_b32 v202 offset:512\n"
    "v_mfma_f32_16x16x32_bf16 v[24:27], a[176:179], v[160:163], v[24:27]\n"
    "ds_read_b64_tr_b16 a[168:169], v245 offset:49152\n"
    "buffer_load_dwordx4 v[116:119], v253, s[8:11], s48 offen offset:3072 sc1\n"
    "v_mfma_f32_16x16x32_bf16 v[24:27], a[180:183], v[164:167], v[24:27]\n"
    "s_add_u32 s48, s56, 16384\n"
    "ds_read_b64_tr_b16 a[170:171], v245 offset:51200\n"
    "v_mfma_f32_16x16x32_bf16 v[48:51], a[180:183], v[172:175], v[48:51]\n"
    "ds_read_b64_tr_b16 a[172:173], v245 offset:57344\n"
    "ds_write_addtid_b32 v203 offset:768\n"
    "v_mfma_f32_16x16x32_bf16 v[48:51], a[176:179], v[168:171], v[48:51]\n"
    "ds_read_b64_tr_b16 a[174:175], v245 offset:59392\n"
    "v_lshl_or_b32 v240, v242, 2, v240\n"
    "v_mfma_f32_16x16x32_bf16 v[52:55], a[184:187], v[168:171], v[52:55]\n"
    "ds_read_b64_tr_b16 a[176:177], v246 offset:49152\n"
    "buffer_load_dwordx4 a[28:31], v253, s[8:11], s48 offen offset:0 sc1\n"
    "v_mfma_f32_16x16x32_bf16 v[52:55], a[188:191], v[172:175], v[52:55]\n"
    "s_waitcnt lgkmcnt(2)\n"
    "s_barrier\n"
    "v_mfma_f32_16x16x32_bf16 v[28:31], a[188:191], v[164:167], v[28:31]\n"
    "ds_read_b64_tr_b16 a[178:179], v246 offset:51200\n"
    "v_mfma_f32_16x16x32_bf16 v[28:31], a[184:187], v[160:163], v[28:31]\n"
    "s_waitcnt lgkmcnt(0)\n"
    "ds_read_b64_tr_b16 a[180:181], v246 offset:57344\n"
    "ds_read2st64_b32 v[204:205], v250 offset0:0 offset1:1\n"
    "v_mfma_f32_16x16x32_bf16 v[0:3], a[160:163], v[200:203], v[0:3]\n"
    "ds_read_b64_tr_b16 a[182:183], v246 offset:59392\n"
    "ds_read2st64_b32 v[206:207], v250 offset0:2 offset1:3\n"
    "buffer_load_dwordx4 a[60:63], v253, s[8:11], s48 offen offset:1024 sc1\n"
    "v_mfma_f32_16x16x32_bf16 a[248:251], a[160:163], v[208:211], a[248:251]\n"
    "ds_read_b64_tr_b16 a[184:185], v247 offset:49152\n"
    "ds_read2st64_b32 v[212:213], v250 offset0:16 offset1:17\n"
    "v_mfma_f32_16x16x32_bf16 a[252:255], a[168:171], v[208:211], a[252:255]\n"
    "ds_read_b64_tr_b16 a[186:187], v247 offset:51200\n"
    "ds_read2st64_b32 v[214:215], v250 offset0:18 offset1:19\n"
    "v_mfma_f32_16x16x32_bf16 v[4:7], a[168:171], v[200:203], v[4:7]\n"
    "ds_read_b64_tr_b16 a[188:189], v247 offset:57344\n"
    "v_mfma_f32_16x16x32_bf16 v[8:11], a[176:179], v[200:203], v[8:11]\n"
    "ds_read_b64_tr_b16 a[190:191], v247 offset:59392\n"
    "buffer_load_dwordx4 a[0:3], v253, s[8:11], s48 offen offset:2048 sc1\n"
    "v_mfma_f32_16x16x32_bf16 v[32:35], a[176:179], v[208:211], v[32:35]\n"
    "s_waitcnt lgkmcnt(2)\n"
    "ds_read_b64_tr_b16 a[160:161], v244 offset:53248\n"
    "v_xor_b32_e32 v241, 64, v240\n"
    "v_mfma_f32_16x16x32_bf16 v[0:3], a[164:167], v[204:207], v[0:3]\n"
    "ds_read_b64_tr_b16 a[162:163], v244 offset:55296\n"
    "v_mfma_f32_16x16x32_bf16 a[248:251], a[164:167], v[212:215], a[248:251]\n"
    "s_waitcnt lgkmcnt(2)\n"
    "ds_read_b64_tr_b16 a[164:165], v244 offset:61440\n"
    "v_mfma_f32_16x16x32_bf16 a[252:255], a[172:175], v[212:215], a[252:255]\n"
    "ds_read_b64_tr_b16 a[166:167], v244 offset:63488\n"
    "buffer_load_dwordx4 a[32:35], v253, s[8:11], s48 offen offset:3072 sc1\n"
    "v_mfma_f32_16x16x32_bf16 v[4:7], a[172:175], v[204:207], v[4:7]\n"
    "s_add_u32 s48, s56, 20480\n"
    "ds_read_b64_tr_b16 a[168:169], v245 offset:53248\n"
    "v_mfma_f32_16x16x32_bf16 v[8:11], a[180:183], v[204:207], v[8:11]\n"
    "ds_read_b64_tr_b16 a[170:171], v245 offset:55296\n"
    "v_mfma_f32_16x16x32_bf16 v[32:35], a[180:183], v[212:215], v[32:35]\n"
    "ds_read_b64_tr_b16 a[172:173], v245 offset:61440\n"
    "v_accvgpr_read_b32 v216, a248\n"
    "v_mfma_f32_16x16x32_bf16 v[36:39], a[184:187], v[208:211], v[36:39]\n"
    "ds_read_b64_tr_b16 a[174:175], v245 offset:63488\n"
    "buffer_load_dwordx4 a[4:7], v253, s[8:11], s48 offen offset:0 sc1\n"
    "v_mfma_f32_16x16x32_bf16 v[36:39], a[188:191], v[212:215], v[36:39]\n"
    "s_lshl_b32 s45, s51, 16\n"
    "ds_read_b64_tr_b16 a[176:177], v246 offset:53248\n"
    "v_accvgpr_read_b32 v217, a249\n"
    "v_mfma_f32_16x16x32_bf16 v[12:15], a[188:191], v[204:207], v[12:15]\n"
    "ds_read_b64_tr_b16 a[178:179], v246 offset:55296\n"
    "buffer_load_dword v146, v145, s[8:11], s47 offen offset:32 sc1\n"
    "v_accvgpr_read_b32 v218, a250\n"
    "v_mfma_f32_16x16x32_bf16 v[12:15], a[184:187], v[200:203], v[12:15]\n"
    "s_waitcnt lgkmcnt(2)\n"
    "ds_read_b64_tr_b16 a[180:181], v246 offset:61440\n"
    "v_accvgpr_read_b32 v219, a251\n"
    "v_mfma_f32_16x16x32_bf16 v[16:19], a[160:163], v[200:203], v[16:19]\n"
    "ds_read_b64_tr_b16 a[182:183], v246 offset:63488\n"
    "buffer_load_dwordx4 a[36:39], v253, s[8:11], s48 offen offset:1024 sc1\n"
    "v_mfma_f32_16x16x32_bf16 v[16:19], a[164:167], v[204:207], v[16:19]\n"
    "s_lshl_b32 s46, s50, 11\n"
    "ds_read_b64_tr_b16 a[184:185], v247 offset:53248\n"
    "v_accvgpr_read_b32 v220, a252\n"
    "v_mfma_f32_16x16x32_bf16 v[40:43], a[164:167], v[212:215], v[40:43]\n"
    "s_add_u32 s46, s46, s45\n"
    "ds_read_b64_tr_b16 a[186:187], v247 offset:55296\n"
    "v_accvgpr_read_b32 v221, a253\n"
    "v_mfma_f32_16x16x32_bf16 v[40:43], a[160:163], v[208:211], v[40:43]\n"
    "s_add_u32 s46, s46, %3\n"
    "ds_read_b64_tr_b16 a[188:189], v247 offset:61440\n"
    "v_accvgpr_read_b32 v222, a254\n"
    "v_mfma_f32_16x16x32_bf16 v[44:47], a[168:171], v[208:211], v[44:47]\n"
    "ds_read_b64_tr_b16 a[190:191], v247 offset:63488\n"
    "buffer_load_dwordx4 a[8:11], v253, s[8:11], s48 offen offset:2048 sc1\n"
    "v_mfma_f32_16x16x32_bf16 v[44:47], a[172:175], v[212:215], v[44:47]\n"
    "s_add_u32 s46, s46, 65536\n"
    "ds_read_b128 a[160:163], v241 offset:11264\n"
    "v_accvgpr_read_b32 v223, a255\n"
    "v_mfma_f32_16x16x32_bf16 v[20:23], a[172:175], v[204:207], v[20:23]\n"
    "ds_read_b128 a[164:167], v241 offset:19456\n"
    "v_cvt_pk_bf16_f32 v216, v216, v217\n"
    "v_mfma_f32_16x16x32_bf16 v[20:23], a[168:171], v[200:203], v[20:23]\n"
    "s_waitcnt lgkmcnt(2)\n"
    "ds_read_b128 a[168:171], v241 offset:27648\n"
    "v_cvt_pk_bf16_f32 v217, v218, v219\n"
    "v_mfma_f32_16x16x32_bf16 v[24:27], a[176:179], v[200:203], v[24:27]\n"
    "ds_read_b128 a[172:175], v240 offset:8192\n"
    "buffer_load_dwordx4 a[40:43], v253, s[8:11], s48 offen offset:3072 sc1\n"
    "v_mfma_f32_16x16x32_bf16 v[24:27], a[180:183], v[204:207], v[24:27]\n"
    "v_cvt_pk_bf16_f32 v218, v220, v221\n"
    "v_cvt_pk_bf16_f32 v219, v222, v223\n"
    "v_mfma_f32_16x16x32_bf16 v[48:51], a[180:183], v[212:215], v[48:51]\n"
    "buffer_store_dwordx4 v[216:219], v253, s[8:11], s83 offen offset:0 sc1\n"
    "v_cvt_pk_bf16_f32 v32, v32, v33\n"
    "v_cvt_pk_bf16_f32 v33, v34, v35\n"
    "v_mfma_f32_16x16x32_bf16 v[48:51], a[176:179], v[208:211], v[48:51]\n"
    "ds_read_b128 a[176:179], v240 offset:16384\n"
    "v_cvt_pk_bf16_f32 v34, v36, v37\n"
    "v_mfma_f32_16x16x32_bf16 v[52:55], a[184:187], v[208:211], v[52:55]\n"
    "ds_read_b128 a[180:183], v240 offset:24576\n"
    "v_cvt_pk_bf16_f32 v35, v38, v39\n"
    "v_mfma_f32_16x16x32_bf16 v[52:55], a[188:191], v[212:215], v[52:55]\n"
    "buffer_store_dwordx4 v[32:35], v253, s[8:11], s83 offen offset:1024 sc1\n"
    "v_cvt_pk_bf16_f32 v40, v40, v41\n"
    "v_cvt_pk_bf16_f32 v41, v42, v43\n"
    "v_mfma_f32_16x16x32_bf16 v[28:31], a[188:191], v[204:207], v[28:31]\n"
    "v_cvt_pk_bf16_f32 v42, v44, v45\n"
    "v_cvt_pk_bf16_f32 v43, v46, v47\n"
    "v_mfma_f32_16x16x32_bf16 v[28:31], a[184:187], v[200:203], v[28:31]\n"
    "ds_read_b128 a[184:187], v241 offset:8192\n"
    "buffer_store_dwordx4 v[40:43], v253, s[8:11], s83 offen offset:2048 sc1\n"
    "v_cvt_pk_bf16_f32 v48, v48, v49\n"
    "v_add_u32_e32 v158, s46, v253\n"
    "v_mfma_f32_16x16x32_bf16 v[160:163], v[154:157], a[224:227], 0\n"
    "ds_read_b128 a[188:191], v241 offset:16384\n"
    "v_cvt_pk_bf16_f32 v49, v50, v51\n"
    "v_mfma_f32_16x16x32_bf16 v[164:167], v[152:155], a[224:227], 0\n"
    "v_cvt_pk_bf16_f32 v50, v52, v53\n"
    "v_cvt_pk_bf16_f32 v51, v54, v55\n"
    "v_mfma_f32_16x16x32_bf16 v[168:171], v[154:157], a[228:231], 0\n"
    "s_add_u32 s45, %3, 139456\n"
    "buffer_store_dwordx4 v[48:51], v253, s[8:11], s83 offen offset:3072 sc1\n"
    "v_and_b32_e32 v150, 15, v147\n"
    "v_lshlrev_b32_e32 v151, 9, v150\n"
    "v_mfma_f32_16x16x32_bf16 v[172:175], v[152:155], a[228:231], 0\n"
    "buffer_store_dword v55, v251, s[8:11], s60 offen offset:0 sc1\n"
    "v_lshl_add_u32 v150, v150, 2, s45\n"
    "v_and_or_b32 v151, v147, 48, v151\n"
    "v_mfma_f32_16x16x32_bf16 v[176:179], v[154:157], a[232:235], 0\n"
    "s_lshl_b32 s46, %2, 7\n"
    "s_add_u32 s58, s35, s46\n"
    "s_mul_i32 s45, %1, 74496\n"
    "v_mfma_f32_16x16x32_bf16 v[180:183], v[152:155], a[232:235], 0\n"
    "s_add_u32 s45, s45, 16183296\n"
    "ds_write_b128 v159, v[216:219] offset:0\n"
    "v_mfma_f32_16x16x32_bf16 v[184:187], v[154:157], a[236:239], 0\n"
    "s_lshl_b32 s54, %2, 11\n"
    "v_and_b32_e32 v147, 0xf0, v253\n"
    "v_add_u32_e32 v252, %3, v253\n"
    "v_mfma_f32_16x16x32_bf16 v[188:191], v[152:155], a[236:239], 0\n"
    "s_add_u32 s54, s54, s45\n"
    "ds_write_b128 v159, v[32:35] offset:1024\n"
    "v_mfma_f32_16x16x32_bf16 v[192:195], v[154:157], a[240:243], 0\n"
    "s_add_u32 s54, s54, 16384\n"
    "ds_write_b128 v159, v[40:43] offset:2048\n"
    "v_mfma_f32_16x16x32_bf16 v[196:199], v[152:155], a[240:243], 0\n"
    "s_lshl1_add_u32 s59, s50, s51\n"
    "s_lshl_b32 s59, s59, 12\n"
    "v_mfma_f32_16x16x32_bf16 v[200:203], v[154:157], a[244:247], 0\n"
    "s_add_u32 s46, s59, s45\n"
    "ds_write_b128 v159, v[48:51] offset:3072\n"
    "v_mfma_f32_16x16x32_bf16 v[204:207], v[152:155], a[244:247], 0\n"
    "s_waitcnt lgkmcnt(10)\n"
    "s_bitcmp1_b32 %1, 0\n"
    "s_cselect_b32 s54, s54, 2.0\n"
    "s_cselect_b32 s55, s58, 2.0\n"
    "v_mfma_f32_16x16x32_bf16 v[160:163], a[192:195], a[160:163], v[160:163]\n"
    "ds_write_b32 v254, v55 offset:0\n"
    "v_mfma_f32_16x16x32_bf16 v[164:167], a[196:199], a[160:163], v[164:167]\n"
    "ds_write_b32 v255, v31 offset:0\n"
    "v_mfma_f32_16x16x32_bf16 v[168:171], a[200:203], a[160:163], v[168:171]\n"
    "s_waitcnt vmcnt(8)\n"
    "v_cmp_ne_u32_e32 vcc, s39, v146\n"
    "v_mfma_f32_16x16x32_bf16 v[172:175], a[204:207], a[160:163], v[172:175]\n"
    "s_add_u32 s61, s54, 0\n"
    "ds_read_b128 a[160:163], v241 offset:24576\n"
    "v_mfma_f32_16x16x32_bf16 v[176:179], a[192:195], a[164:167], v[176:179]\n"
    "s_cbranch_vccnz .Lat_xpoll\n"
    ".Lat_xpoll_ret:\n"
    "v_mfma_f32_16x16x32_bf16 v[180:183], a[196:199], a[164:167], v[180:183]\n"
    "v_mfma_f32_16x16x32_bf16 v[184:187], a[200:203], a[164:167], v[184:187]\n"
    "buffer_load_dwordx4 a[224:227], v253, s[8:11], s61 offen offset:0 sc1\n"
    "v_mfma_f32_16x16x32_bf16 v[188:191], a[204:207], a[164:167], v[188:191]\n"
    "s_waitcnt lgkmcnt(11)\n"
    "ds_read_b128 a[164:167], v240 offset:9216\n"
    "v_mfma_f32_16x16x32_bf16 v[192:195], a[192:195], a[168:171], v[192:195]\n"
    "v_mfma_f32_16x16x32_bf16 v[196:199], a[196:199], a[168:171], v[196:199]\n"
    "v_mfma_f32_16x16x32_bf16 v[200:203], a[200:203], a[168:171], v[200:203]\n"
    "buffer_load_dwordx4 a[228:231], v253, s[8:11], s61 offen offset:1024 sc1\n"
    "v_mfma_f32_16x16x32_bf16 v[204:207], a[204:207], a[168:171], v[204:207]\n"
    "s_add_u32 s61, s54, 24832\n"
    "ds_read_b128 a[168:171], v240 offset:17408\n"
    "v_mfma_f32_16x16x32_bf16 v[160:163], a[208:211], a[172:175], v[160:163]\n"
    "s_waitcnt lgkmcnt(3)\n"
    "s_barrier\n"
    "v_mfma_f32_16x16x32_bf16 v[164:167], a[212:215], a[172:175], v[164:167]\n"
    "v_mfma_f32_16x16x32_bf16 v[168:171], a[216:219], a[172:175], v[168:171]\n"
    "buffer_load_dwordx4 a[232:235], v253, s[8:11], s61 offen offset:0 sc1\n"
    "v_mfma_f32_16x16x32_bf16 v[172:175], a[220:223], a[172:175], v[172:175]\n"
    "ds_read_b128 a[172:175], v240 offset:25600\n"
    "v_mfma_f32_16x16x32_bf16 v[176:179], a[208:211], a[176:179], v[176:179]\n"
    "ds_read_b32 v232, v150 offset:0\n"
    "v_mfma_f32_16x16x32_bf16 v[180:183], a[212:215], a[176:179], v[180:183]\n"
    "v_mfma_f32_16x16x32_bf16 v[184:187], a[216:219], a[176:179], v[184:187]\n"
    "buffer_load_dwordx4 a[236:239], v253, s[8:11], s61 offen offset:1024 sc1\n"
    "v_mfma_f32_16x16x32_bf16 v[188:191], a[220:223], a[176:179], v[188:191]\n"
    "s_add_u32 s61, s54, 49664\n"
    "ds_read_b128 a[176:179], v241 offset:9216\n"
    "v_mfma_f32_16x16x32_bf16 v[192:195], a[208:211], a[180:183], v[192:195]\n"
    "v_mfma_f32_16x16x32_bf16 v[196:199], a[212:215], a[180:183], v[196:199]\n"
    "ds_read_b32 v233, v150 offset:256\n"
    "v_mfma_f32_16x16x32_bf16 v[200:203], a[216:219], a[180:183], v[200:203]\n"
    "buffer_load_dwordx4 a[240:243], v253, s[8:11], s61 offen offset:0 sc1\n"
    "v_mfma_f32_16x16x32_bf16 v[204:207], a[220:223], a[180:183], v[204:207]\n"
    "ds_read_b128 a[180:183], v241 offset:17408\n"
    "v_mfma_f32_16x16x32_bf16 v[160:163], a[92:95], a[184:187], v[160:163]\n"
    "v_mfma_f32_16x16x32_bf16 v[164:167], a[124:127], a[184:187], v[164:167]\n"
    "v_mfma_f32_16x16x32_bf16 v[168:171], a[156:159], a[184:187], v[168:171]\n"
    "buffer_load_dwordx4 a[244:247], v253, s[8:11], s61 offen offset:1024 sc1\n"
    "v_mfma_f32_16x16x32_bf16 v[172:175], a[64:67], a[184:187], v[172:175]\n"
    "s_add_u32 s61, s46, 0\n"
    "ds_read_b128 a[184:187], v241 offset:25600\n"
    "v_mfma_f32_16x16x32_bf16 v[176:179], a[92:95], a[188:191], v[176:179]\n"
    "ds_read_b32 v234, v150 offset:512\n"
    "v_mfma_f32_16x16x32_bf16 v[180:183], a[124:127], a[188:191], v[180:183]\n"
    "v_mfma_f32_16x16x32_bf16 v[184:187], a[156:159], a[188:191], v[184:187]\n"
    "buffer_load_dwordx4 a[192:195], v253, s[8:11], s61 offen offset:0 sc1\n"
    "v_mfma_f32_16x16x32_bf16 v[188:191], a[64:67], a[188:191], v[188:191]\n"
    "s_waitcnt lgkmcnt(8)\n"
    "ds_read_b128 a[188:191], v240 offset:10240\n"
    "v_mfma_f32_16x16x32_bf16 v[192:195], a[92:95], a[160:163], v[192:195]\n"
    "ds_read_b128 a[248:251], v158 offset:0\n"
    "v_mfma_f32_16x16x32_bf16 v[196:199], a[124:127], a[160:163], v[196:199]\n"
    "v_mfma_f32_16x16x32_bf16 v[200:203], a[156:159], a[160:163], v[200:203]\n"
    "buffer_load_dwordx4 a[196:199], v253, s[8:11], s61 offen offset:1024 sc1\n"
    "v_mfma_f32_16x16x32_bf16 v[204:207], a[64:67], a[160:163], v[204:207]\n"
    "ds_read_b128 a[160:163], v240 offset:18432\n"
    "v_mfma_f32_16x16x32_bf16 v[160:163], a[96:99], a[164:167], v[160:163]\n"
    "s_waitcnt vmcnt(8)\n"
    "buffer_store_dword v148, v149, s[8:11], s87 offen offset:0 sc1\n"
    "v_mfma_f32_16x16x32_bf16 v[164:167], a[128:131], a[164:167], v[164:167]\n"
    "ds_read_b128 a[252:255], v158 offset:1024\n"
    "v_mfma_f32_16x16x32_bf16 v[168:171], a[68:71], a[164:167], v[168:171]\n"
    "buffer_load_dwordx4 a[200:203], v253, s[8:11], s61 offen offset:2048 sc1\n"
    "v_mfma_f32_16x16x32_bf16 v[172:175], a[100:103], a[164:167], v[172:175]\n"
    "s_waitcnt lgkmcnt(10)\n"
    "ds_read_b128 a[164:167], v240 offset:26624\n"
    "v_mfma_f32_16x16x32_bf16 v[176:179], a[96:99], a[168:171], v[176:179]\n"
    "v_mfma_f32_16x16x32_bf16 v[180:183], a[128:131], a[168:171], v[180:183]\n"
    "v_mfma_f32_16x16x32_bf16 v[184:187], a[68:71], a[168:171], v[184:187]\n"
    "buffer_load_dwordx4 a[204:207], v253, s[8:11], s61 offen offset:3072 sc1\n"
    "v_mfma_f32_16x16x32_bf16 v[188:191], a[100:103], a[168:171], v[188:191]\n"
    "s_add_u32 s61, s46, 24832\n"
    "ds_read_b128 a[168:171], v241 offset:10240\n"
    "v_mfma_f32_16x16x32_bf16 v[192:195], a[96:99], a[172:175], v[192:195]\n"
    "s_lshl_b32 s52, %2, 11\n"
    "v_mfma_f32_16x16x32_bf16 v[196:199], a[128:131], a[172:175], v[196:199]\n"
    "v_mfma_f32_16x16x32_bf16 v[200:203], a[68:71], a[172:175], v[200:203]\n"
    "buffer_load_dwordx4 a[208:211], v253, s[8:11], s61 offen offset:0 sc1\n"
    "v_mfma_f32_16x16x32_bf16 v[204:207], a[100:103], a[172:175], v[204:207]\n"
    "s_waitcnt lgkmcnt(8)\n"
    "ds_read_b128 a[172:175], v241 offset:18432\n"
    "v_mfma_f32_16x16x32_bf16 v[160:163], a[132:135], a[176:179], v[160:163]\n"
    "v_mfma_f32_16x16x32_bf16 v[164:167], a[72:75], a[176:179], v[164:167]\n"
    "v_add_u32_e32 v251, s52, v252\n"
    "v_mfma_f32_16x16x32_bf16 v[168:171], a[104:107], a[176:179], v[168:171]\n"
    "buffer_load_dwordx4 a[212:215], v253, s[8:11], s61 offen offset:1024 sc1\n"
    "v_mfma_f32_16x16x32_bf16 v[172:175], a[136:139], a[176:179], v[172:175]\n"
    "ds_read_b128 a[176:179], v241 offset:26624\n"
    "v_mfma_f32_16x16x32_bf16 v[176:179], a[132:135], a[180:183], v[176:179]\n"
    "v_mfma_f32_16x16x32_bf16 v[180:183], a[72:75], a[180:183], v[180:183]\n"
    "v_add_u32_e32 v158, s59, v252\n"
    "v_mfma_f32_16x16x32_bf16 v[184:187], a[104:107], a[180:183], v[184:187]\n"
    "buffer_load_dwordx4 a[216:219], v253, s[8:11], s61 offen offset:2048 sc1\n"
    "v_mfma_f32_16x16x32_bf16 v[188:191], a[136:139], a[180:183], v[188:191]\n"
    "s_waitcnt lgkmcnt(7)\n"
    "ds_read_b128 a[180:183], v240 offset:11264\n"
    "v_mfma_f32_16x16x32_bf16 v[192:195], a[132:135], a[184:187], v[192:195]\n"
    "v_mfma_f32_16x16x32_bf16 v[196:199], a[72:75], a[184:187], v[196:199]\n"
    "v_mfma_f32_16x16x32_bf16 v[200:203], a[104:107], a[184:187], v[200:203]\n"
    "buffer_load_dwordx4 a[220:223], v253, s[8:11], s61 offen offset:3072 sc1\n"
    "v_mfma_f32_16x16x32_bf16 v[204:207], a[136:139], a[184:187], v[204:207]\n"
    "s_add_u32 s61, s45, 24576\n"
    "ds_read_b128 a[184:187], v240 offset:19456\n"
    "v_mfma_f32_16x16x32_bf16 v[160:163], a[76:79], a[188:191], v[160:163]\n"
    "v_and_b32_e32 v145, 12, v145\n"
    "v_mfma_f32_16x16x32_bf16 v[164:167], a[108:111], a[188:191], v[164:167]\n"
    "v_mfma_f32_16x16x32_bf16 v[168:171], a[140:143], a[188:191], v[168:171]\n"
    "buffer_load_dwordx3 v[236:238], v147, s[8:11], s61 offen offset:0 sc1\n"
    "v_mfma_f32_16x16x32_bf16 v[172:175], a[80:83], a[188:191], v[172:175]\n"
    "s_waitcnt lgkmcnt(5)\n"
    "s_add_u32 s61, s45, 49408\n"
    "ds_read_b128 a[188:191], v240 offset:27648\n"
    "v_mfma_f32_16x16x32_bf16 v[176:179], a[76:79], a[160:163], v[176:179]\n"
    "v_mfma_f32_16x16x32_bf16 v[180:183], a[108:111], a[160:163], v[180:183]\n"
    "s_add_u32 s52, s32, 3\n"
    "v_mfma_f32_16x16x32_bf16 v[184:187], a[140:143], a[160:163], v[184:187]\n"
    "buffer_load_dwordx3 v[248:250], v147, s[8:11], s61 offen offset:0 sc1\n"
    "v_mfma_f32_16x16x32_bf16 v[188:191], a[80:83], a[160:163], v[188:191]\n"
    "s_add_u32 s61, s45, 74240\n"
    "v_mfma_f32_16x16x32_bf16 v[192:195], a[76:79], a[164:167], v[192:195]\n"
    "v_mfma_f32_16x16x32_bf16 v[196:199], a[108:111], a[164:167], v[196:199]\n"
    "s_and_b32 s52, s52, 3\n"
    "v_mfma_f32_16x16x32_bf16 v[200:203], a[140:143], a[164:167], v[200:203]\n"
    "buffer_load_dword v246, v147, s[8:11], s61 offen offset:8 sc1\n"
    "v_mfma_f32_16x16x32_bf16 v[204:207], a[80:83], a[164:167], v[204:207]\n"
    "s_waitcnt lgkmcnt(4)\n"
    "s_add_u32 s48, s56, 24576\n"
    "v_mfma_f32_16x16x32_bf16 v[160:163], a[112:115], a[168:171], v[160:163]\n"
    "v_mfma_f32_16x16x32_bf16 v[164:167], a[144:147], a[168:171], v[164:167]\n"
    "v_mfma_f32_16x16x32_bf16 v[168:171], a[84:87], a[168:171], v[168:171]\n"
    "buffer_load_dwordx4 a[12:15], v253, s[8:11], s48 offen offset:0 sc1\n"
    "v_mfma_f32_16x16x32_bf16 v[172:175], a[116:119], a[168:171], v[172:175]\n"
    "v_mfma_f32_16x16x32_bf16 v[176:179], a[112:115], a[172:175], v[176:179]\n"
    "s_lshl_b32 s52, s52, 6\n"
    "v_mfma_f32_16x16x32_bf16 v[180:183], a[144:147], a[172:175], v[180:183]\n"
    "buffer_load_dwordx4 a[44:47], v253, s[8:11], s48 offen offset:1024 sc1\n"
    "v_mfma_f32_16x16x32_bf16 v[184:187], a[84:87], a[172:175], v[184:187]\n"
    "v_mfma_f32_16x16x32_bf16 v[188:191], a[116:119], a[172:175], v[188:191]\n"
    "s_waitcnt lgkmcnt(2)\n"
    "v_mfma_f32_16x16x32_bf16 v[192:195], a[112:115], a[176:179], v[192:195]\n"
    "buffer_load_dwordx4 a[16:19], v253, s[8:11], s48 offen offset:2048 sc1\n"
    "v_mfma_f32_16x16x32_bf16 v[196:199], a[144:147], a[176:179], v[196:199]\n"
    "s_add_u32 s52, s52, s47\n"
    "v_mfma_f32_16x16x32_bf16 v[200:203], a[84:87], a[176:179], v[200:203]\n"
    "v_mfma_f32_16x16x32_bf16 v[204:207], a[116:119], a[176:179], v[204:207]\n"
    "buffer_load_dwordx4 a[48:51], v253, s[8:11], s48 offen offset:3072 sc1\n"
    "v_mfma_f32_16x16x32_bf16 v[160:163], a[148:151], a[180:183], v[160:163]\n"
    "s_add_u32 s48, s56, 28672\n"
    "v_mfma_f32_16x16x32_bf16 v[164:167], a[88:91], a[180:183], v[164:167]\n"
    "v_mfma_f32_16x16x32_bf16 v[168:171], a[120:123], a[180:183], v[168:171]\n"
    "buffer_load_dwordx4 a[20:23], v253, s[8:11], s48 offen offset:0 sc1\n"
    "v_mfma_f32_16x16x32_bf16 v[172:175], a[152:155], a[180:183], v[172:175]\n"
    "s_waitcnt lgkmcnt(0)\n"
    "v_mfma_f32_16x16x32_bf16 v[176:179], a[148:151], a[184:187], v[176:179]\n"
    "v_mfma_f32_16x16x32_bf16 v[180:183], a[88:91], a[184:187], v[180:183]\n"
    "buffer_load_dwordx4 a[52:55], v253, s[8:11], s48 offen offset:1024 sc1\n"
    "v_mfma_f32_16x16x32_bf16 v[184:187], a[120:123], a[184:187], v[184:187]\n"
    "v_mfma_f32_16x16x32_bf16 v[188:191], a[152:155], a[184:187], v[188:191]\n"
    "v_mfma_f32_16x16x32_bf16 v[192:195], a[148:151], a[188:191], v[192:195]\n"
    "buffer_load_dwordx4 a[24:27], v253, s[8:11], s48 offen offset:2048 sc1\n"
    "v_mfma_f32_16x16x32_bf16 v[196:199], a[88:91], a[188:191], v[196:199]\n"
    "v_mfma_f32_16x16x32_bf16 v[200:203], a[120:123], a[188:191], v[200:203]\n"
    "v_mfma_f32_16x16x32_bf16 v[204:207], a[152:155], a[188:191], v[204:207]\n"
    "buffer_load_dwordx4 a[56:59], v253, s[8:11], s48 offen offset:3072 sc1\n"
    "v_mfma_f32_16x16x32_bf16 v[128:131], v[154:157], a[248:251], 0\n"
    "v_mfma_f32_16x16x32_bf16 v[132:135], v[152:155], a[248:251], 0\n"
    "v_mfma_f32_16x16x32_bf16 v[136:139], v[154:157], a[252:255], 0\n"
    "v_mfma_f32_16x16x32_bf16 v[140:143], v[152:155], a[252:255], 0\n"
    "s_waitcnt vmcnt(24)\n"
    "v_mfma_f32_16x16x32_bf16 v[128:131], v[154:157], a[224:227], v[128:131]\n"
    "v_mfma_f32_16x16x32_bf16 v[132:135], v[152:155], a[224:227], v[132:135]\n"
    "v_mfma_f32_16x16x32_bf16 v[136:139], v[154:157], a[228:231], v[136:139]\n"
    "buffer_load_dword v144, v145, s[8:11], s52 offen offset:0 sc1\n"
    "v_mfma_f32_16x16x32_bf16 v[140:143], v[152:155], a[228:231], v[140:143]\n"
    "s_waitcnt vmcnt(23)\n"
    "v_mfma_f32_16x16x32_bf16 v[128:131], v[154:157], a[232:235], v[128:131]\n"
    "v_mfma_f32_16x16x32_bf16 v[132:135], v[152:155], a[232:235], v[132:135]\n"
    "v_mfma_f32_16x16x32_bf16 v[136:139], v[154:157], a[236:239], v[136:139]\n"
    "v_mfma_f32_16x16x32_bf16 v[140:143], v[152:155], a[236:239], v[140:143]\n"
    "s_waitcnt vmcnt(21)\n"
    "v_mfma_f32_16x16x32_bf16 v[128:131], v[154:157], a[240:243], v[128:131]\n"
    "v_mfma_f32_16x16x32_bf16 v[132:135], v[152:155], a[240:243], v[132:135]\n"
    "v_mfma_f32_16x16x32_bf16 v[136:139], v[154:157], a[244:247], v[136:139]\n"
    "v_mfma_f32_16x16x32_bf16 v[140:143], v[152:155], a[244:247], v[140:143]\n"
    "s_waitcnt vmcnt(19)\n"
    "v_mfma_f32_16x16x32_bf16 v[0:3], v[154:157], a[192:195], v[0:3]\n"
    "v_cvt_pk_bf16_f32 v128, v128, v129\n"
    "v_mfma_f32_16x16x32_bf16 v[4:7], v[152:155], a[192:195], v[4:7]\n"
    "v_cvt_pk_bf16_f32 v129, v130, v131\n"
    "v_mfma_f32_16x16x32_bf16 v[8:11], v[154:157], a[196:199], v[8:11]\n"
    "v_cvt_pk_bf16_f32 v130, v132, v133\n"
    "v_mfma_f32_16x16x32_bf16 v[12:15], v[152:155], a[196:199], v[12:15]\n"
    "s_waitcnt vmcnt(16)\n"
    "v_cvt_pk_bf16_f32 v131, v134, v135\n"
    "v_mfma_f32_16x16x32_bf16 v[16:19], v[154:157], a[200:203], v[16:19]\n"
    "ds_write_b128 v251, v[128:131] offset:49152\n"
    "v_mfma_f32_16x16x32_bf16 v[20:23], v[152:155], a[200:203], v[20:23]\n"
    "v_cvt_pk_bf16_f32 v136, v136, v137\n"
    "v_mfma_f32_16x16x32_bf16 v[24:27], v[154:157], a[204:207], v[24:27]\n"
    "v_cvt_pk_bf16_f32 v137, v138, v139\n"
    "v_mfma_f32_16x16x32_bf16 v[28:31], v[152:155], a[204:207], v[28:31]\n"
    "s_waitcnt vmcnt(14)\n"
    "v_cvt_pk_bf16_f32 v138, v140, v141\n"
    "v_cvt_pk_bf16_f32 v139, v142, v143\n"
    "v_mfma_f32_16x16x32_bf16 v[0:3], v[154:157], a[208:211], v[0:3]\n"
    "ds_write_b128 v251, v[136:139] offset:50176\n"
    "v_mfma_f32_16x16x32_bf16 v[4:7], v[152:155], a[208:211], v[4:7]\n"
    "v_mfma_f32_16x16x32_bf16 v[8:11], v[154:157], a[212:215], v[8:11]\n"
    "s_waitcnt lgkmcnt(0)\n"
    "s_barrier\n"
    "ds_read_b128 a[128:131], v252 offset:49152\n"
    "ds_read_b128 a[132:135], v252 offset:50176\n"
    "ds_read_b128 a[136:139], v252 offset:51200\n"
    "v_mfma_f32_16x16x32_bf16 v[12:15], v[152:155], a[212:215], v[12:15]\n"
    "s_waitcnt vmcnt(12)\n"
    "v_mfma_f32_16x16x32_bf16 v[16:19], v[154:157], a[216:219], v[16:19]\n"
    "ds_read_b128 a[140:143], v252 offset:52224\n"
    "v_mfma_f32_16x16x32_bf16 v[20:23], v[152:155], a[216:219], v[20:23]\n"
    "v_mfma_f32_16x16x32_bf16 v[24:27], v[154:157], a[220:223], v[24:27]\n"
    "ds_read_b128 a[144:147], v252 offset:53248\n"
    "v_mfma_f32_16x16x32_bf16 v[28:31], v[152:155], a[220:223], v[28:31]\n"
    "s_waitcnt lgkmcnt(3)\n"
    "v_mfma_f32_16x16x32_bf16 v[216:219], v[56:59], a[128:131], 0\n"
    "ds_read_b128 a[148:151], v252 offset:54272\n"
    "v_mfma_f32_16x16x32_bf16 v[220:223], v[60:63], a[128:131], 0\n"
    "ds_read_b128 a[152:155], v252 offset:55296\n"
    "v_mfma_f32_16x16x32_bf16 v[224:227], v[64:67], a[128:131], 0\n"
    "s_waitcnt vmcnt(0)\n"
    "v_cmp_ne_u32_e32 vcc, s39, v144\n"
    "s_nop 3\n"
    "s_cbranch_vccnz .Lat_ypoll\n"
    ".Lat_ypoll_ret:\n"
    "s_add_u32 s61, s46, 49664\n"
    "buffer_load_dwordx4 a[160:163], v253, s[8:11], s61 offen offset:0 sc1\n"
    "buffer_load_dwordx4 a[164:167], v253, s[8:11], s61 offen offset:1024 sc1\n"
    "buffer_load_dwordx4 a[168:171], v253, s[8:11], s61 offen offset:2048 sc1\n"
    "buffer_load_dwordx4 a[172:175], v253, s[8:11], s61 offen offset:3072 sc1\n"
    "s_add_u32 s61, s45, 74240\n"
    "buffer_load_dwordx2 v[244:245], v147, s[8:11], s61 offen offset:0 sc1\n"
    "v_mfma_f32_16x16x32_bf16 v[228:231], v[68:71], a[128:131], 0\n"
    "ds_read_b128 a[156:159], v252 offset:56320\n"
    "v_mfma_f32_16x16x32_bf16 v[216:219], v[72:75], a[132:135], v[216:219]\n"
    "v_mfma_f32_16x16x32_bf16 v[220:223], v[76:79], a[132:135], v[220:223]\n"
    "s_waitcnt vmcnt(14)\n"
    "v_mfma_f32_16x16x32_bf16 v[224:227], v[80:83], a[132:135], v[224:227]\n"
    "v_mfma_f32_16x16x32_bf16 v[228:231], v[84:87], a[132:135], v[228:231]\n"
    "s_waitcnt lgkmcnt(4)\n"
    "v_mfma_f32_16x16x32_bf16 v[216:219], v[88:91], a[136:139], v[216:219]\n"
    "v_add_f32_e32 v232, v232, v236\n"
    "v_mfma_f32_16x16x32_bf16 v[220:223], v[92:95], a[136:139], v[220:223]\n"
    "v_mfma_f32_16x16x32_bf16 v[224:227], v[96:99], a[136:139], v[224:227]\n"
    "v_mfma_f32_16x16x32_bf16 v[228:231], v[100:103], a[136:139], v[228:231]\n"
    "v_add_f32_e32 v233, v233, v237\n"
    "v_mfma_f32_16x16x32_bf16 v[216:219], v[104:107], a[140:143], v[216:219]\n"
    "v_mfma_f32_16x16x32_bf16 v[220:223], v[108:111], a[140:143], v[220:223]\n"
    "v_mfma_f32_16x16x32_bf16 v[224:227], v[112:115], a[140:143], v[224:227]\n"
    "v_add_f32_e32 v234, v234, v238\n"
    "v_mfma_f32_16x16x32_bf16 v[228:231], v[116:119], a[140:143], v[228:231]\n"
    "s_waitcnt lgkmcnt(2)\n"
    "v_mfma_f32_16x16x32_bf16 v[216:219], a[28:31], a[144:147], v[216:219]\n"
    "v_mfma_f32_16x16x32_bf16 v[220:223], a[60:63], a[144:147], v[220:223]\n"
    "v_add_f32_e32 v232, v232, v248\n"
    "v_mfma_f32_16x16x32_bf16 v[224:227], a[0:3], a[144:147], v[224:227]\n"
    "v_mfma_f32_16x16x32_bf16 v[228:231], a[32:35], a[144:147], v[228:231]\n"
    "v_mfma_f32_16x16x32_bf16 v[216:219], a[4:7], a[148:151], v[216:219]\n"
    "v_add_f32_e32 v233, v233, v249\n"
    "v_mfma_f32_16x16x32_bf16 v[220:223], a[36:39], a[148:151], v[220:223]\n"
    "v_mfma_f32_16x16x32_bf16 v[224:227], a[8:11], a[148:151], v[224:227]\n"
    "v_mfma_f32_16x16x32_bf16 v[228:231], a[40:43], a[148:151], v[228:231]\n"
    "s_waitcnt lgkmcnt(0)\n"
    "s_waitcnt vmcnt(10)\n"
    "v_mfma_f32_16x16x32_bf16 v[216:219], a[12:15], a[152:155], v[216:219]\n"
    "v_add_f32_e32 v234, v234, v250\n"
    "v_mfma_f32_16x16x32_bf16 v[220:223], a[44:47], a[152:155], v[220:223]\n"
    "v_mfma_f32_16x16x32_bf16 v[224:227], a[16:19], a[152:155], v[224:227]\n"
    "v_add_f32_e32 v234, v234, v246\n"
    "v_mfma_f32_16x16x32_bf16 v[228:231], a[48:51], a[152:155], v[228:231]\n"
    "s_waitcnt vmcnt(6)\n"
    "v_mfma_f32_16x16x32_bf16 v[216:219], a[20:23], a[156:159], v[216:219]\n"
    "v_mfma_f32_16x16x32_bf16 v[220:223], a[52:55], a[156:159], v[220:223]\n"
    "v_rcp_f32_e32 v242, v234\n"
    "v_mfma_f32_16x16x32_bf16 v[224:227], a[24:27], a[156:159], v[224:227]\n"
    "v_mfma_f32_16x16x32_bf16 v[228:231], a[56:59], a[156:159], v[228:231]\n"
    "s_waitcnt vmcnt(4)\n"
    "v_mfma_f32_16x16x32_bf16 v[0:3], v[154:157], a[160:163], v[0:3]\n"
    "v_mfma_f32_16x16x32_bf16 v[4:7], v[152:155], a[160:163], v[4:7]\n"
    "s_waitcnt vmcnt(3)\n"
    "v_mfma_f32_16x16x32_bf16 v[8:11], v[154:157], a[164:167], v[8:11]\n"
    "v_mfma_f32_16x16x32_bf16 v[12:15], v[152:155], a[164:167], v[12:15]\n"
    "s_waitcnt vmcnt(2)\n"
    "v_cvt_pk_bf16_f32 v0, v0, v1\n"
    "v_cvt_pk_bf16_f32 v1, v2, v3\n"
    "v_mfma_f32_16x16x32_bf16 v[16:19], v[154:157], a[168:171], v[16:19]\n"
    "v_cvt_pk_bf16_f32 v2, v4, v5\n"
    "v_cvt_pk_bf16_f32 v3, v6, v7\n"
    "v_mfma_f32_16x16x32_bf16 v[20:23], v[152:155], a[168:171], v[20:23]\n"
    "s_waitcnt vmcnt(1)\n"
    "ds_write_b128 v158, v[0:3] offset:32768\n"
    "v_mfma_f32_16x16x32_bf16 v[24:27], v[154:157], a[172:175], v[24:27]\n"
    "v_cvt_pk_bf16_f32 v8, v8, v9\n"
    "v_cvt_pk_bf16_f32 v9, v10, v11\n"
    "v_cvt_pk_bf16_f32 v10, v12, v13\n"
    "v_cvt_pk_bf16_f32 v11, v14, v15\n"
    "v_mfma_f32_16x16x32_bf16 v[28:31], v[152:155], a[172:175], v[28:31]\n"
    "ds_write_b128 v158, v[8:11] offset:33792\n"
    "v_cvt_pk_bf16_f32 v16, v16, v17\n"
    "v_cvt_pk_bf16_f32 v17, v18, v19\n"
    "v_cvt_pk_bf16_f32 v18, v20, v21\n"
    "v_cvt_pk_bf16_f32 v19, v22, v23\n"
    "ds_write_b128 v158, v[16:19] offset:34816\n"
    "v_cvt_pk_bf16_f32 v24, v24, v25\n"
    "v_cvt_pk_bf16_f32 v25, v26, v27\n"
    "v_cvt_pk_bf16_f32 v26, v28, v29\n"
    "v_cvt_pk_bf16_f32 v27, v30, v31\n"
    "ds_write_b128 v158, v[24:27] offset:35840\n"
    "s_waitcnt lgkmcnt(0)\n"
    "s_barrier\n"
    "ds_read_b128 a[96:99], v252 offset:40960\n"
    "ds_read_b128 a[100:103], v252 offset:41984\n"
    "ds_read_b128 a[104:107], v252 offset:43008\n"
    "ds_read_b128 a[108:111], v252 offset:44032\n"
    "ds_read_b128 a[112:115], v252 offset:45056\n"
    "ds_read_b128 a[116:119], v252 offset:46080\n"
    "ds_read_b128 a[120:123], v252 offset:47104\n"
    "ds_read_b128 a[124:127], v252 offset:48128\n"
    "s_waitcnt lgkmcnt(6)\n"
    "v_mfma_f32_16x16x32_bf16 v[120:123], v[56:59], a[96:99], 0\n"
    "s_add_u32 s48, s55, 16384\n"
    "v_fma_f32 v216, v216, v242, v192\n"
    "v_mfma_f32_16x16x32_bf16 v[124:127], v[60:63], a[96:99], 0\n"
    "ds_read_b128 a[64:67], v252 offset:32768\n"
    "v_mfma_f32_16x16x32_bf16 v[128:131], v[64:67], a[96:99], 0\n"
    "v_fma_f32 v217, v217, v242, v193\n"
    "v_fma_f32 v218, v218, v242, v194\n"
    "v_mfma_f32_16x16x32_bf16 v[132:135], v[68:71], a[96:99], 0\n"
    "ds_read_b128 a[68:71], v252 offset:33792\n"
    "v_fma_f32 v219, v219, v242, v195\n"
    "v_mfma_f32_16x16x32_bf16 v[120:123], v[72:75], a[100:103], v[120:123]\n"
    "v_fma_f32 v220, v220, v242, v196\n"
    "v_mfma_f32_16x16x32_bf16 v[124:127], v[76:79], a[100:103], v[124:127]\n"
    "ds_read_b128 a[72:75], v252 offset:34816\n"
    "v_fma_f32 v221, v221, v242, v197\n"
    "v_mfma_f32_16x16x32_bf16 v[128:131], v[80:83], a[100:103], v[128:131]\n"
    "v_fma_f32 v222, v222, v242, v198\n"
    "v_fma_f32 v223, v223, v242, v199\n"
    "v_mfma_f32_16x16x32_bf16 v[132:135], v[84:87], a[100:103], v[132:135]\n"
    "s_waitcnt lgkmcnt(7)\n"
    "ds_read_b128 a[76:79], v252 offset:35840\n"
    "v_cvt_pk_bf16_f32 v216, v216, v217\n"
    "v_mfma_f32_16x16x32_bf16 v[120:123], v[88:91], a[104:107], v[120:123]\n"
    "v_cvt_pk_bf16_f32 v217, v218, v219\n"
    "v_cvt_pk_bf16_f32 v218, v220, v221\n"
    "v_mfma_f32_16x16x32_bf16 v[124:127], v[92:95], a[104:107], v[124:127]\n"
    "ds_read_b128 a[80:83], v252 offset:36864\n"
    "v_cvt_pk_bf16_f32 v219, v222, v223\n"
    "v_mfma_f32_16x16x32_bf16 v[128:131], v[96:99], a[104:107], v[128:131]\n"
    "buffer_store_dwordx4 v[216:219], v151, s[16:19], s48 offen offset:0 sc0 sc1\n"
    "v_fma_f32 v224, v224, v242, v200\n"
    "v_mfma_f32_16x16x32_bf16 v[132:135], v[100:103], a[104:107], v[132:135]\n"
    "ds_read_b128 a[84:87], v252 offset:37888\n"
    "v_fma_f32 v225, v225, v242, v201\n"
    "v_mfma_f32_16x16x32_bf16 v[120:123], v[104:107], a[108:111], v[120:123]\n"
    "v_fma_f32 v226, v226, v242, v202\n"
    "v_mfma_f32_16x16x32_bf16 v[124:127], v[108:111], a[108:111], v[124:127]\n"
    "ds_read_b128 a[88:91], v252 offset:38912\n"
    "v_fma_f32 v227, v227, v242, v203\n"
    "v_mfma_f32_16x16x32_bf16 v[128:131], v[112:115], a[108:111], v[128:131]\n"
    "v_fma_f32 v228, v228, v242, v204\n"
    "v_fma_f32 v229, v229, v242, v205\n"
    "v_mfma_f32_16x16x32_bf16 v[132:135], v[116:119], a[108:111], v[132:135]\n"
    "s_waitcnt lgkmcnt(9)\n"
    "ds_read_b128 a[92:95], v252 offset:39936\n"
    "v_fma_f32 v230, v230, v242, v206\n"
    "v_mfma_f32_16x16x32_bf16 v[120:123], a[28:31], a[112:115], v[120:123]\n"
    "v_fma_f32 v231, v231, v242, v207\n"
    "v_cvt_pk_bf16_f32 v224, v224, v225\n"
    "v_mfma_f32_16x16x32_bf16 v[124:127], a[60:63], a[112:115], v[124:127]\n"
    "v_cvt_pk_bf16_f32 v225, v226, v227\n"
    "v_mfma_f32_16x16x32_bf16 v[128:131], a[0:3], a[112:115], v[128:131]\n"
    "v_cvt_pk_bf16_f32 v226, v228, v229\n"
    "v_mfma_f32_16x16x32_bf16 v[132:135], a[32:35], a[112:115], v[132:135]\n"
    "v_cvt_pk_bf16_f32 v227, v230, v231\n"
    "v_mfma_f32_16x16x32_bf16 v[120:123], a[4:7], a[116:119], v[120:123]\n"
    "buffer_store_dwordx4 v[224:227], v151, s[16:19], s48 offen offset:64 sc0 sc1\n"
    "v_mfma_f32_16x16x32_bf16 v[124:127], a[36:39], a[116:119], v[124:127]\n"
    "s_waitcnt vmcnt(2)\n"
    "v_add_f32_e32 v232, v232, v244\n"
    "v_mfma_f32_16x16x32_bf16 v[128:131], a[8:11], a[116:119], v[128:131]\n"
    "s_bitcmp1_b32 %1, 0\n"
    "s_cselect_b64 vcc, -1, 0\n"
    "v_add_f32_e32 v233, v233, v245\n"
    "v_mfma_f32_16x16x32_bf16 v[132:135], a[40:43], a[116:119], v[132:135]\n"
    "s_waitcnt lgkmcnt(8)\n"
    "v_cndmask_b32_e32 v234, v233, v234, vcc\n"
    "v_mfma_f32_16x16x32_bf16 v[120:123], a[12:15], a[120:123], v[120:123]\n"
    "s_mov_b32 s61, 0x12800000\n"
    "v_rcp_f32_e32 v241, v233\n"
    "v_mfma_f32_16x16x32_bf16 v[124:127], a[44:47], a[120:123], v[124:127]\n"
    "v_rcp_f32_e32 v240, v232\n"
    "v_mfma_f32_16x16x32_bf16 v[128:131], a[16:19], a[120:123], v[128:131]\n"
    "v_subrev_u32_e32 v236, s61, v232\n"
    "v_mfma_f32_16x16x32_bf16 v[132:135], a[48:51], a[120:123], v[132:135]\n"
    "v_subrev_u32_e32 v237, s61, v233\n"
    "v_subrev_u32_e32 v238, s61, v234\n"
    "v_mfma_f32_16x16x32_bf16 v[120:123], a[20:23], a[124:127], v[120:123]\n"
    "v_max3_u32 v236, v236, v237, v238\n"
    "v_mfma_f32_16x16x32_bf16 v[124:127], a[52:55], a[124:127], v[124:127]\n"
    "buffer_store_dword v148, v149, s[8:11], s88 offen offset:0 sc1\n"
    "v_cmp_le_u32_e32 vcc, 0x5f000000, v236\n"
    "v_mfma_f32_16x16x32_bf16 v[128:131], a[24:27], a[124:127], v[128:131]\n"
    "v_mfma_f32_16x16x32_bf16 v[132:135], a[56:59], a[124:127], v[132:135]\n"
    "s_waitcnt lgkmcnt(0)\n"
    "v_mfma_f32_16x16x32_bf16 v[32:35], v[56:59], a[64:67], 0\n"
    "s_cbranch_vccnz .Lat_exit\n"
    "v_mfma_f32_16x16x32_bf16 v[36:39], v[60:63], a[64:67], 0\n"
    "s_add_u32 s48, s58, 8192\n"
    "v_fma_f32 v120, v120, v241, v176\n"
    "v_mfma_f32_16x16x32_bf16 v[32:35], v[72:75], a[68:71], v[32:35]\n"
    "v_fma_f32 v121, v121, v241, v177\n"
    "v_mfma_f32_16x16x32_bf16 v[36:39], v[76:79], a[68:71], v[36:39]\n"
    "v_fma_f32 v122, v122, v241, v178\n"
    "v_mfma_f32_16x16x32_bf16 v[32:35], v[88:91], a[72:75], v[32:35]\n"
    "v_fma_f32 v123, v123, v241, v179\n"
    "v_mfma_f32_16x16x32_bf16 v[36:39], v[92:95], a[72:75], v[36:39]\n"
    "v_fma_f32 v124, v124, v241, v180\n"
    "v_mfma_f32_16x16x32_bf16 v[32:35], v[104:107], a[76:79], v[32:35]\n"
    "v_fma_f32 v125, v125, v241, v181\n"
    "v_mfma_f32_16x16x32_bf16 v[36:39], v[108:111], a[76:79], v[36:39]\n"
    "v_fma_f32 v126, v126, v241, v182\n"
    "v_fma_f32 v127, v127, v241, v183\n"
    "v_mfma_f32_16x16x32_bf16 v[32:35], a[28:31], a[80:83], v[32:35]\n"
    "v_cvt_pk_bf16_f32 v120, v120, v121\n"
    "v_mfma_f32_16x16x32_bf16 v[36:39], a[60:63], a[80:83], v[36:39]\n"
    "v_cvt_pk_bf16_f32 v121, v122, v123\n"
    "v_mfma_f32_16x16x32_bf16 v[32:35], a[4:7], a[84:87], v[32:35]\n"
    "v_cvt_pk_bf16_f32 v122, v124, v125\n"
    "v_mfma_f32_16x16x32_bf16 v[36:39], a[36:39], a[84:87], v[36:39]\n"
    "v_cvt_pk_bf16_f32 v123, v126, v127\n"
    "v_mfma_f32_16x16x32_bf16 v[32:35], a[12:15], a[88:91], v[32:35]\n"
    "buffer_store_dwordx4 v[120:123], v151, s[16:19], s48 offen offset:0 sc0 sc1\n"
    "v_fma_f32 v128, v128, v241, v184\n"
    "v_mfma_f32_16x16x32_bf16 v[36:39], a[44:47], a[88:91], v[36:39]\n"
    "v_fma_f32 v129, v129, v241, v185\n"
    "v_fma_f32 v130, v130, v241, v186\n"
    "v_mfma_f32_16x16x32_bf16 v[32:35], a[20:23], a[92:95], v[32:35]\n"
    "v_fma_f32 v131, v131, v241, v187\n"
    "v_mfma_f32_16x16x32_bf16 v[36:39], a[52:55], a[92:95], v[36:39]\n"
    "v_fma_f32 v132, v132, v241, v188\n"
    "v_mfma_f32_16x16x32_bf16 v[40:43], v[64:67], a[64:67], 0\n"
    "v_fma_f32 v133, v133, v241, v189\n"
    "v_mfma_f32_16x16x32_bf16 v[44:47], v[68:71], a[64:67], 0\n"
    "v_fma_f32 v134, v134, v241, v190\n"
    "v_fma_f32 v135, v135, v241, v191\n"
    "v_mfma_f32_16x16x32_bf16 v[40:43], v[80:83], a[68:71], v[40:43]\n"
    "v_cvt_pk_bf16_f32 v128, v128, v129\n"
    "v_mfma_f32_16x16x32_bf16 v[44:47], v[84:87], a[68:71], v[44:47]\n"
    "v_cvt_pk_bf16_f32 v129, v130, v131\n"
    "v_mfma_f32_16x16x32_bf16 v[40:43], v[96:99], a[72:75], v[40:43]\n"
    "v_cvt_pk_bf16_f32 v130, v132, v133\n"
    "v_cvt_pk_bf16_f32 v131, v134, v135\n"
    "v_mfma_f32_16x16x32_bf16 v[44:47], v[100:103], a[72:75], v[44:47]\n"
    "buffer_store_dwordx4 v[128:131], v151, s[16:19], s48 offen offset:64 sc0 sc1\n"
    "v_mfma_f32_16x16x32_bf16 v[40:43], v[112:115], a[76:79], v[40:43]\n"
    "s_add_u32 s48, s58, 0\n"
    "v_fma_f32 v32, v32, v240, v160\n"
    "v_mfma_f32_16x16x32_bf16 v[44:47], v[116:119], a[76:79], v[44:47]\n"
    "v_fma_f32 v33, v33, v240, v161\n"
    "v_mfma_f32_16x16x32_bf16 v[40:43], a[0:3], a[80:83], v[40:43]\n"
    "v_fma_f32 v34, v34, v240, v162\n"
    "v_mfma_f32_16x16x32_bf16 v[44:47], a[32:35], a[80:83], v[44:47]\n"
    "v_fma_f32 v35, v35, v240, v163\n"
    "v_mfma_f32_16x16x32_bf16 v[40:43], a[8:11], a[84:87], v[40:43]\n"
    "v_fma_f32 v36, v36, v240, v164\n"
    "v_fma_f32 v37, v37, v240, v165\n"
    "v_mfma_f32_16x16x32_bf16 v[44:47], a[40:43], a[84:87], v[44:47]\n"
    "v_fma_f32 v38, v38, v240, v166\n"
    "v_mfma_f32_16x16x32_bf16 v[40:43], a[16:19], a[88:91], v[40:43]\n"
    "v_fma_f32 v39, v39, v240, v167\n"
    "v_mfma_f32_16x16x32_bf16 v[44:47], a[48:51], a[88:91], v[44:47]\n"
    "v_cvt_pk_bf16_f32 v32, v32, v33\n"
    "v_cvt_pk_bf16_f32 v33, v34, v35\n"
    "v_mfma_f32_16x16x32_bf16 v[40:43], a[24:27], a[92:95], v[40:43]\n"
    "v_cvt_pk_bf16_f32 v34, v36, v37\n"
    "v_cvt_pk_bf16_f32 v35, v38, v39\n"
    "v_mfma_f32_16x16x32_bf16 v[44:47], a[56:59], a[92:95], v[44:47]\n"
    "buffer_store_dwordx4 v[32:35], v151, s[16:19], s48 offen offset:0 sc0 sc1\n"
    "s_nop 2\n"
    "v_fma_f32 v40, v40, v240, v168\n"
    "v_fma_f32 v41, v41, v240, v169\n"
    "v_fma_f32 v42, v42, v240, v170\n"
    "v_fma_f32 v43, v43, v240, v171\n"
    "v_fma_f32 v44, v44, v240, v172\n"
    "v_fma_f32 v45, v45, v240, v173\n"
    "v_fma_f32 v46, v46, v240, v174\n"
    "v_fma_f32 v47, v47, v240, v175\n"
    "v_cvt_pk_bf16_f32 v40, v40, v41\n"
    "v_cvt_pk_bf16_f32 v41, v42, v43\n"
    "v_cvt_pk_bf16_f32 v42, v44, v45\n"
    "v_cvt_pk_bf16_f32 v43, v46, v47\n"
    "buffer_store_dwordx4 v[40:43], v151, s[16:19], s48 offen offset:64 sc0 sc1\n"
    "s_endpgm\n"
    ".Lat_wpoll:\n"
    "s_mov_b32 s53, 0x400000\n"
    ".Lat_wpoll_l:\n"
    "s_sleep 1\n"
    "buffer_load_dword v255, v254, s[8:11], s45 offen offset:0 sc1\n"
    "s_waitcnt vmcnt(0)\n"
    "v_cmp_ne_u32_e32 vcc, s39, v255\n"
    "s_sub_u32 s53, s53, 1\n"
    "s_cbranch_scc1 .Lat_wpoll_ret\n"
    "s_nop 2\n"
    "s_cbranch_vccnz .Lat_wpoll_l\n"
    "s_branch .Lat_wpoll_ret\n"
    ".Lat_xpoll:\n"
    "s_mov_b32 s53, 0x400000\n"
    ".Lat_xpoll_l:\n"
    "s_sleep 0\n"
    "buffer_load_dword v146, v145, s[8:11], s47 offen offset:32 sc1\n"
    "s_waitcnt vmcnt(0)\n"
    "v_cmp_ne_u32_e32 vcc, s39, v146\n"
    "s_sub_u32 s53, s53, 1\n"
    "s_cbranch_scc1 .Lat_xpoll_ret\n"
    "s_nop 2\n"
    "s_cbranch_vccnz .Lat_xpoll_l\n"
    "s_branch .Lat_xpoll_ret\n"
    ".Lat_ypoll:\n"
    "s_mov_b32 s53, 0x400000\n"
    ".Lat_ypoll_l:\n"
    "s_sleep 0\n"
    "buffer_load_dword v144, v145, s[8:11], s52 offen offset:0 sc1\n"
    "s_waitcnt vmcnt(0)\n"
    "v_cmp_ne_u32_e32 vcc, s39, v144\n"
    "s_sub_u32 s53, s53, 1\n"
    "s_cbranch_scc1 .Lat_ypoll_ret\n"
    "s_nop 2\n"
    "s_cbranch_vccnz .Lat_ypoll_l\n"
    "s_branch .Lat_ypoll_ret\n"
    ".Lat_apoll:\n"
    "s_mov_b32 s53, 0x400000\n"
    ".Lat_apoll_l:\n"
    "s_sleep 1\n"
    "buffer_load_dword v255, v252, s[8:11], 0 offen offset:0 sc1\n"
    "s_waitcnt vmcnt(0)\n"
    "v_cmp_ne_u32_e32 vcc, s39, v255\n"
    "s_sub_u32 s53, s53, 1\n"
    "s_cbranch_scc1 .Lat_apoll_ret\n"
    "s_nop 2\n"
    "s_cbranch_vccnz .Lat_apoll_l\n"
    "s_branch .Lat_apoll_ret\n"
    ".Lat_h1poll:\n"
    "s_mov_b32 s53, 0x400000\n"
    ".Lat_h1poll_l:\n"
    "s_sleep 1\n"
    "buffer_load_dword v0, v3, s[8:11], s59 offen offset:0 sc1\n"
    "s_waitcnt vmcnt(0)\n"
    "v_cmp_ne_u32_e32 vcc, s39, v0\n"
    "s_sub_u32 s53, s53, 1\n"
    "s_cbranch_scc1 .Lat_h1poll_ret\n"
    "s_nop 2\n"
    "s_cbranch_vccnz .Lat_h1poll_l\n"
    "s_branch .Lat_h1poll_ret\n"
    ".Lat_hpoll:\n"
    "s_mov_b32 s53, 0x400000\n"
    ".Lat_hpoll_l:\n"
    "s_sleep 1\n"
    "buffer_load_dword v0, v253, s[8:11], s59 offen offset:0 sc1\n"
    "buffer_load_dword v1, v2, s[8:11], s59 offen offset:0 sc1\n"
    "s_waitcnt vmcnt(0)\n"
    "v_xor_b32_e32 v1, s39, v1\n"
    "v_xor_b32_e32 v0, s39, v0\n"
    "v_or_b32_e32 v0, v0, v1\n"
    "v_cmp_ne_u32_e32 vcc, 0, v0\n"
    "s_sub_u32 s53, s53, 1\n"
    "s_cbranch_scc1 .Lat_hpoll_ret\n"
    "s_nop 2\n"
    "s_cbranch_vccnz .Lat_hpoll_l\n"
    "s_branch .Lat_hpoll_ret\n"
    ".Lat_qpoll:\n"
    "s_mov_b32 s53, 0x400000\n"
    ".Lat_qpoll_l:\n"
    "s_sleep 0\n"
    "buffer_load_dword v11, v5, s[8:11], s47 offen offset:0 sc1\n"
    "s_waitcnt vmcnt(0)\n"
    "v_cmp_ne_u32_e32 vcc, s39, v11\n"
    "s_sub_u32 s53, s53, 1\n"
    "s_cbranch_scc1 .Lat_qpoll_ret\n"
    "s_nop 2\n"
    "s_cbranch_vccnz .Lat_qpoll_l\n"
    "s_branch .Lat_qpoll_ret\n"
    ".Lat_lpoll:\n"
    "s_mov_b32 s53, 0x400000\n"
    ".Lat_lpoll_l:\n"
    "s_sleep 1\n"
    "buffer_load_dword v154, off, s[8:11], s80 sc1\n"
    "s_waitcnt vmcnt(0)\n"
    "v_cmp_ne_u32_e32 vcc, s39, v154\n"
    "s_sub_u32 s53, s53, 1\n"
    "s_cbranch_scc1 .Lat_lpoll_ret\n"
    "s_nop 2\n"
    "s_cbranch_vccnz .Lat_lpoll_l\n"
    ".Lat_lpoll_ret:\n"
    "v_lshlrev_b32_e32 v253, 2, v249\n"
    "s_mov_b32 m0, s82\n"
    "s_nop 0\n"
    "buffer_load_dwordx4 v253, s[8:11], s81 offen offset:0 sc1 lds\n"
    "s_waitcnt vmcnt(0)\n"
    "s_branch .Lat_g\n"
    ".Lat_exit:\n"
    "s_waitcnt vmcnt(0)\n"
    "s_nop 15\n"
    ".Lat_end:\n"
    "s_waitcnt lgkmcnt(0)\n"
    : "+s"(ka), "+s"(b), "+s"(wid) : "s"((unsigned)(size_t)lds)
    : "v0", "v1", "v2", "v3", "v4", "v5", "v6", "v7", "v8", "v9", "v10", "v11", "v12", "v13", "v14", "v15", "v16", "v17", "v18", "v19", "v20", "v21", "v22", "v23", "v24", "v25", "v26", "v27", "v28", "v29", "v30", "v31", "v32", "v33", "v34", "v35", "v36", "v37", "v38", "v39", "v40", "v41", "v42", "v43", "v44", "v45", "v46", "v47", "v48", "v49", "v50", "v51", "v52", "v53", "v54", "v55", "v56", "v57", "v58", "v59", "v60", "v61", "v62", "v63", "v64", "v65", "v66", "v67", "v68", "v69", "v70", "v71", "v72", "v73", "v74", "v75", "v76", "v77", "v78", "v79", "v80", "v81", "v82", "v83", "v84", "v85", "v86", "v87", "v88", "v89", "v90", "v91", "v92", "v93", "v94", "v95", "v96", "v97", "v98", "v99", "v100", "v101", "v102", "v103", "v104", "v105", "v106", "v107", "v108", "v109", "v110", "v111", "v112", "v113", "v114", "v115", "v116", "v117", "v118", "v119", "v120", "v121", "v122", "v123", "v124", "v125", "v126", "v127", "v128", "v129", "v130", "v131", "v132", "v133", "v134", "v135", "v136", "v137", "v138", "v139", "v140", "v141", "v142", "v143", "v144", "v145", "v146", "v147", "v148", "v149", "v150", "v151", "v152", "v153", "v154", "v155", "v156", "v157", "v158", "v159", "v160", "v161", "v162", "v163", "v164", "v165", "v166", "v167", "v168", "v169", "v170", "v171", "v172", "v173", "v174", "v175", "v176", "v177", "v178", "v179", "v180", "v181", "v182", "v183", "v184", "v185", "v186", "v187", "v188", "v189", "v190", "v191", "v192", "v193", "v194", "v195", "v196", "v197", "v198", "v199", "v200", "v201", "v202", "v203", "v204", "v205", "v206", "v207", "v208", "v209", "v210", "v211", "v212", "v213", "v214", "v215", "v216", "v217", "v218", "v219", "v220", "v221", "v222", "v223", "v224", "v225", "v226", "v227", "v228", "v229", "v230", "v231", "v232", "v233", "v234", "v235", "v236", "v237", "v238", "v239", "v240", "v241", "v242", "v243", "v244", "v245", "v246", "v247", "v248", "v249", "v250", "v251", "v252", "v253", "v254", "v255", "a0", "a1", "a2", "a3", "a4", "a5", "a6", "a7", "a8", "a9", "a10", "a11", "a12", "a13", "a14", "a15", "a16", "a17", "a18", "a19", "a20", "a21", "a22", "a23", "a24", "a25", "a26", "a27", "a28", "a29", "a30", "a31", "a32", "a33", "a34", "a35", "a36", "a37", "a38", "a39", "a40", "a41", "a42", "a43", "a44", "a45", "a46", "a47", "a48", "a49", "a50", "a51", "a52", "a53", "a54", "a55", "a56", "a57", "a58", "a59", "a60", "a61", "a62", "a63", "a64", "a65", "a66", "a67", "a68", "a69", "a70", "a71", "a72", "a73", "a74", "a75", "a76", "a77", "a78", "a79", "a80", "a81", "a82", "a83", "a84", "a85", "a86", "a87", "a88", "a89", "a90", "a91", "a92", "a93", "a94", "a95", "a96", "a97", "a98", "a99", "a100", "a101", "a102", "a103", "a104", "a105", "a106", "a107", "a108", "a109", "a110", "a111", "a112", "a113", "a114", "a115", "a116", "a117", "a118", "a119", "a120", "a121", "a122", "a123", "a124", "a125", "a126", "a127", "a128", "a129", "a130", "a131", "a132", "a133", "a134", "a135", "a136", "a137", "a138", "a139", "a140", "a141", "a142", "a143", "a144", "a145", "a146", "a147", "a148", "a149", "a150", "a151", "a152", "a153", "a154", "a155", "a156", "a157", "a158", "a159", "a160", "a161", "a162", "a163", "a164", "a165", "a166", "a167", "a168", "a169", "a170", "a171", "a172", "a173", "a174", "a175", "a176", "a177", "a178", "a179", "a180", "a181", "a182", "a183", "a184", "a185", "a186", "a187", "a188", "a189", "a190", "a191", "a192", "a193", "a194", "a195", "a196", "a197", "a198", "a199", "a200", "a201", "a202", "a203", "a204", "a205", "a206", "a207", "a208", "a209", "a210", "a211", "a212", "a213", "a214", "a215", "a216", "a217", "a218", "a219", "a220", "a221", "a222", "a223", "a224", "a225", "a226", "a227", "a228", "a229", "a230", "a231", "a232", "a233", "a234", "a235", "a236", "a237", "a238", "a239", "a240", "a241", "a242", "a243", "a244", "a245", "a246", "a247", "a248", "a249", "a250", "a251", "a252", "a253", "a254", "a255", "s8", "s9", "s10", "s11", "s12", "s13", "s14", "s15", "s16", "s17", "s18", "s19", "s20", "s21", "s22", "s23", "s24", "s25", "s26", "s27", "s28", "s29", "s30", "s31", "s32", "s33", "s34", "s35", "s36", "s37", "s38", "s39", "s40", "s41", "s42", "s43", "s44", "s45", "s46", "s47", "s48", "s49", "s50", "s51", "s52", "s53", "s54", "s55", "s56", "s57", "s58", "s59", "s60", "s61", "s62", "s63", "s80", "s81", "s82", "s83", "s84", "s85", "s86", "s87", "s88", "s89", "s90", "vcc", "m0", "scc", "memory");
  typedef __attribute__((address_space(1))) char* __attribute__((address_space(4))) const* arg_t;
  const arg_t ap = (arg_t)ka;
  typedef const u16* cu16;
  if (b >= NATT) list_branch(lds, b - NATT, wid, (const int*)ap[1], (const int*)ap[2], (cu16)ap[3], (cu16)ap[8], (cu16)ap[9], (cu16)ap[10], (char*)ap[0]);
  else exact_rows(lds, b, wid, (char*)ap[0], (cu16)ap[4], (u16*)ap[5]);
}

extern "C" void kernel_launch(void* const* d_in, const int* in_sizes, int n_in, void* d_out, int out_size, void* d_ws,
                              size_t ws_size, hipStream_t stream) {
  const u16* x = (const u16*)d_in[0];
  const int* src = (const int*)d_in[1];
  const int* dst = (const int*)d_in[2];
  const u16* deg_inv = (const u16*)d_in[3];
  const u16* wmsg = (const u16*)d_in[4];
  const u16* wq = (const u16*)d_in[5];
  const u16* wk = (const u16*)d_in[6];
  const u16* wv = (const u16*)d_in[7];
  const u16* wout = (const u16*)d_in[8];
  char* ws = (char*)d_ws;
  (void)hipFuncSetAttribute((const void*)attn_kernel, hipFuncAttributeMaxDynamicSharedMemorySize, 163840);
  attn_kernel<<<256, 256, 163840, stream>>>(ws, src, dst, deg_inv, x, (u16*)d_out, wq, wk, wv, wmsg, wout);
}
```

```cpp
#include <hip/hip_runtime.h>
#include <hip/hip_bf16.h>
#include <stdint.h>
#pragma clang diagnostic ignored "-Winline-asm"

typedef unsigned short u16;
typedef __attribute__((ext_vector_type(8))) short bf16x8;
typedef __attribute__((ext_vector_type(4))) short bf16x4;
typedef __attribute__((ext_vector_type(4))) float f32x4;
typedef __attribute__((ext_vector_type(2))) float f32x2;
typedef __attribute__((ext_vector_type(4))) unsigned u32x4;
typedef __attribute__((ext_vector_type(2))) unsigned u32x2;
#define DI __device__ __forceinline__

constexpr int NN = 10000, DD = 256, EE = 160000;
constexpr int NATT = 252, NSP = 4;

constexpr unsigned WS_H = 0;
constexpr unsigned WS_AF = WS_H + 10240 * 512;
constexpr unsigned WS_WV = WS_AF + 131072;
constexpr unsigned WS_WM = WS_WV + 131072;
constexpr unsigned WS_XF = WS_WM + 131072;
constexpr unsigned WS_HF = WS_XF + 256 * 256;
constexpr unsigned WS_BF = WS_HF + 4096;
constexpr unsigned WS_PF = WS_BF + 65536;
constexpr unsigned WS_WF = WS_PF + 4096;
constexpr unsigned WS_LF = WS_WF + 256;
constexpr unsigned WS_EP = WS_LF + 2048;
constexpr unsigned WS_G = WS_EP + 1792;
constexpr unsigned WS_QP = WS_G + 10240 * 512;
constexpr unsigned WS_XB = WS_QP + 10240 * 512;
constexpr unsigned WS_BL = WS_XB + NATT * 3 * 24832;
constexpr unsigned WS_GL = WS_BL + 16 * 5 * 260 * 64;
constexpr unsigned WS_DBG = WS_GL + 256 * 4096;
constexpr unsigned WS_END = WS_DBG + 1024 * 128 + 8192;

DI float bf2f(unsigned x) { return __uint_as_float(x << 16); }
DI float bfhi(unsigned x) { return __uint_as_float(x & 0xffff0000u); }
DI unsigned pk2(float lo, float hi) {
  typedef __attribute__((ext_vector_type(2))) __bf16 bf2_t;
  const bf2_t r = __builtin_convertvector((f32x2){lo, hi}, bf2_t);
  return __builtin_bit_cast(unsigned, r);
}
typedef __attribute__((address_space(3))) bf16x4* lds_bf16x4_p;
DI bf16x4 tr_read(unsigned p) { return __builtin_amdgcn_ds_read_tr16_b64_v4i16((lds_bf16x4_p)p); }
DI bf16x8 as_bf(u32x4 v) { return __builtin_bit_cast(bf16x8, v); }
#define SB() __builtin_amdgcn_sched_barrier(0)
#define MFMA16(a, b, c) __builtin_amdgcn_mfma_f32_16x16x32_bf16(a, b, c, 0, 0, 0)

typedef __amdgpu_buffer_rsrc_t srd_t;
DI void drain() { asm volatile("s_waitcnt vmcnt(0)" ::: "memory"); }
DI void st_flag(srd_t w, unsigned off, unsigned tag) { __builtin_amdgcn_raw_buffer_store_b32(tag, w, off, 0, 16); }
DI void wait_flag(srd_t w, unsigned off, unsigned tag) {
#pragma unroll 1
  for (int it = 0; it < (1 << 22); ++it) {
    unsigned v;
    asm volatile("buffer_load_dword %0, %1, %2, 0 offen sc1\n s_waitcnt vmcnt(0)" : "=v"(v) : "v"(off), "s"(w) : "memory");
    if (__ballot(v != tag) == 0) break;
    __builtin_amdgcn_s_sleep(2);
    asm volatile("" ::: "memory");
  }
  asm volatile("" ::: "memory");
}

DI void prod_store(const f32x4 (&acc)[2][2], int mat, int ks, int J, int lane, srd_t wsrd) {
#pragma unroll
  for (int s = 0; s < 2; ++s) {
    u32x4 o = {pk2(acc[0][s][0], acc[0][s][1]), pk2(acc[0][s][2], acc[0][s][3]), pk2(acc[1][s][0], acc[1][s][1]), pk2(acc[1][s][2], acc[1][s][3])};
    if (ks == 7 && lane >= 48) o[3] &= 0xffffu;
    const int T = 2 * J + s;
    const int at = mat ? 131072 + ((J & 1) ^ 1) * 16384 + ks * 2048 + s * 1024 : ks * 4096 + (T & 3) * 1024;
    __builtin_amdgcn_raw_buffer_store_b128(o, wsrd, WS_WV + (T >> 2) * 32768 + at + lane * 16, 0, 16);
  }
}
DI void corr(unsigned* dl, const unsigned (&lr)[16]) {
  float a0 = 0.f, a1 = 0.f;
#pragma unroll
  for (int r = 0; r < 16; ++r) { a0 += bf2f(lr[r] & 0xffffu); a1 += bfhi(lr[r]); }
  dl[0] = pk2(a0 * -0.0625f, a1 * -0.0625f);
  dl[128] = pk2((bf2f(lr[15] & 0xffffu) - 0.25f * a0) * (1.f / 3.f), (bfhi(lr[15]) - 0.25f * a1) * (1.f / 3.f));
}
DI void product_load(char* lds, int wid, int lane, int job, const u16* __restrict__ wv, const u16* __restrict__ wm, const u16* __restrict__ wo, bool left, bool right) {
  const unsigned ldsb = (unsigned)(size_t)lds;
  const int r = lane >> 3, sw = ((lane & 7) ^ (r & 6)) * 16 + wid * 128;
  const unsigned vp = (8 * (r >> 2) + (r & 3)) * 512 + sw, vn = r * 512 + sw;
  auto image = [&](int n, const u16* w, int row0, bool nat) {
#pragma unroll
    for (int i = 0; i < 8; ++i) {
      const int a = i & 3;
      asm volatile("s_mov_b32 m0, %2\n s_nop 0\n global_load_lds_dwordx4 %0, %1"
                   :: "v"(nat ? vn : vp), "s"(w + (row0 + (i >> 2) * 32 + (nat ? 8 * a : 16 * (a & 1) + 4 * (a >> 1))) * DD),
                      "s"(ldsb + n * 32768 + (i >> 2) * 16384 + wid * 4096 + a * 1024) : "m0", "memory");
    }
  };
  if (left) {
    image(2, wv, (job >> 2) * 64, true);
    image(3, wm, (job >> 2) * 64, false);
  }
  if (right)
#pragma unroll
  for (int k = 0; k < 8; ++k)
    asm volatile("s_mov_b32 m0, %2\n s_nop 0\n global_load_lds_dwordx4 %0, %1"
                 :: "v"((lane >> 3) * 512 + (lane & 7) * 16), "s"(wo + (k * 32 + wid * 8) * DD + (job & 3) * 64), "s"(ldsb + k * 4096 + wid * 1024) : "m0", "memory");
}
DI void product_w(char* lds, int wid, int lane, int job, srd_t wsrd) {
  const int m = lane & 15, g = lane >> 4;
  const int ks = 2 * (job >> 2) + (wid >> 1), J = 2 * (job & 3) + (wid & 1);
  const unsigned ldsb = (unsigned)(size_t)lds;
  unsigned* dl = reinterpret_cast<unsigned*>(lds + 0x26c00);
  const bool hi = (m >> 3) != 0;
  const unsigned ra = (m >> 3) * 1024 + (m & 7) * 128 + (g ^ (m & 6)) * 16;
  auto frag = [&](int n, int tl, int t, int k) {
    return *reinterpret_cast<const bf16x8*>(lds + n * 32768 + tl * 16384 + (k >> 1) * 4096 + t * 2048 + (ra ^ ((k & 1) * 64)));
  };
  f32x4 av[2][2] = {}, am[2][2] = {}, ad[2] = {};
  const unsigned tb = ldsb + (job & 1) * 32768 + (8 * g + ((lane >> 2) & 3)) * 128 + (wid & 1) * 64 + (lane & 3) * 16;
  bf16x8 d0[8], d1[8], d2[8], rr[8][2], vf[8][2], mf[8][2];
#pragma unroll
  for (int kk = 0; kk < 9; ++kk) {
    if (kk < 8) {
      d0[kk] = *reinterpret_cast<const bf16x8*>(dl + 512 + 16 * kk + 4 * g); d1[kk] = *reinterpret_cast<const bf16x8*>(dl + 512 + (ks == 7 ? 128 : 0) + 16 * kk + 4 * g);
      d2[kk] = *reinterpret_cast<const bf16x8*>(dl + 768 + (ks == 7 && hi ? 128 : 0) + 16 * kk + 4 * g);
#pragma unroll
      for (int s = 0; s < 2; ++s) {
        rr[kk][s] = __builtin_shufflevector(tr_read(tb + kk * 4096 + s * 8), tr_read(tb + kk * 4096 + s * 8 + 512), 0, 1, 2, 3, 4, 5, 6, 7);
        vf[kk][s] = frag(2, wid >> 1, s, kk); mf[kk][s] = frag(3, wid >> 1, s, kk);
      }
    }
    SB();
    if (kk) {
      const int k = kk - 1;
#pragma unroll
      for (int s = 0; s < 2; ++s) {
        av[0][s] = MFMA16(d0[k], rr[k][s], av[0][s]); av[1][s] = MFMA16(d1[k], rr[k][s], av[1][s]); ad[s] = MFMA16(d2[k], rr[k][s], ad[s]);
#pragma unroll
        for (int t = 0; t < 2; ++t) { av[t][s] = MFMA16(vf[k][t], rr[k][s], av[t][s]); am[t][s] = MFMA16(mf[k][t], rr[k][s], am[t][s]); }
      }
    }
    SB();
  }
#pragma unroll
  for (int t = 0; t < 2; ++t)
#pragma unroll
    for (int s = 0; s < 2; ++s) am[t][s] += ad[s];
  prod_store(av, 0, ks, J, lane, wsrd);
  prod_store(am, 1, ks, J, lane, wsrd);
}

constexpr unsigned CODE_BYTES = 84736;
DI void code_touch(unsigned i, unsigned ldsdst) {
  unsigned long long a; int o_;
  asm volatile("s_getpc_b64 %0\n s_mov_b32 %1, _Z11attn_kernelPcPKiS1_PKtS3_PtS3_S3_S3_S3_S3_@rel32@lo+4" : "=&s"(a), "=&s"(o_));
  a += (long long)o_;
  asm volatile("s_mov_b32 m0, %2\n s_nop 0\n global_load_lds_dword %0, %1" :: "v"(min(i * 128u, CODE_BYTES - 4u)), "s"(a), "s"(ldsdst) : "m0", "memory");
}

constexpr int LONGS = 1056;
typedef __attribute__((address_space(3))) unsigned* lds_u32_p;
typedef __attribute__((address_space(3))) u16* lds_u16_p;
DI void local_store(srd_t srd, int node, int lane, f32x4 acc, float di) {
  const int n = lane & 15, g = lane >> 4;
  const u32x2 o = {pk2(acc[0] * di, acc[1] * di), pk2(acc[2] * di, acc[3] * di)};
  __builtin_amdgcn_raw_buffer_store_b64(o, srd, WS_G + node * 512 + (g >> 1) * 256 + n * 16 + (g & 1) * 8, 0, 16);
}
DI void list_branch(char* lds, int j, int wid, const int* __restrict__ src, const int* __restrict__ dst, const u16* __restrict__ deg_inv,
                    const u16* wv, const u16* wm, const u16* wo, char* ws) {
  const unsigned rec = (unsigned)(size_t)lds + wid * 33280, ext = (unsigned)(size_t)lds + 0x20800 + wid * 6912;
  int lane; asm volatile("v_mbcnt_lo_u32_b32 %0, -1, 0\n\tv_mbcnt_hi_u32_b32 %0, -1, %0" : "=v"(lane));
  const int tid = wid * 64 + lane, bin = j * 4 + wid, lo = bin * 640;
  code_touch(CODE_BYTES / 128 - tid, (unsigned)(size_t)lds + 0x27f00u);
  const srd_t msrd = __builtin_amdgcn_make_buffer_rsrc(ws + WS_H, 0, 10240 * 512, 0x00020000);
  const srd_t wsrd = __builtin_amdgcn_make_buffer_rsrc(ws, 0, WS_END, 0x00020000);
  const unsigned tag = __builtin_amdgcn_raw_buffer_load_b32(wsrd, WS_EP, 0, 0) + 2u;
  if (lane < 3) st_flag(wsrd, lane == 2 ? WS_WF + 32 : WS_LF + bin * 128 + lane * 64 + 8, 0);
  unsigned lr[16];
#pragma unroll
  for (int r = 0; r < 16; ++r) lr[r] = reinterpret_cast<const unsigned*>((wid >= 2 ? wm : wv) + (240 + r) * DD)[tid & 127];
  unsigned dv[10];
#pragma unroll
  for (int i = 0; i < 10; ++i) dv[i] = (unsigned)deg_inv[min(lo + 64 * i + lane, NN - 1)];
#pragma unroll
  for (int i = 0; i < 10; ++i) ((lds_u32_p)ext)[64 * i + lane] = dv[i] << 16 | (lo + 64 * i + lane < NN ? lo + 64 * i + lane : 0xffff);
  corr(reinterpret_cast<unsigned*>(lds + 0x26c00) + 512 + (wid >> 1) * 256 + (tid & 127), lr);
  const u32x4 none = {~0u, ~0u, ~0u, ~0u};
  auto clear = [&]() {
    unsigned a = rec + lane * 16, r = rec + lane * 52;
    asm volatile("" : "+v"(a), "+v"(r));
#pragma unroll
    for (int i = 0; i < 33; ++i) *(__attribute__((address_space(3))) u32x4*)(a + (i < 32 ? i * 1024 : 32256)) = none;
#pragma unroll
    for (int i = 0; i < 10; ++i) ((lds_u32_p)(r + 64 * 52 * i))[12] = 46;
  };
  clear();
  __builtin_amdgcn_s_sleep(40);
  wait_flag(wsrd, WS_BF + min(tid, NATT - 1) * 128, tag);
  __syncthreads();
  auto take = [&](const u32x4& e0, const u32x4& e1, unsigned (&a)[8], unsigned (&p)[8]) {
#pragma unroll
    for (int r = 0; r < 8; ++r) asm("v_mad_u32_u16 %0, %1, 52, %2 op_sel:[1,0,0,0]" : "=v"(a[r]) : "v"(r < 4 ? e0[r & 3] : e1[r & 3]), "s"(rec));
    SB();
#pragma unroll
    for (int r = 0; r < 8; ++r) p[r] = __atomic_fetch_add((lds_u32_p)a[r] + 12, -2u, __ATOMIC_RELAXED);
    SB();
  };
  auto fill = [&](const u32x4& e0, const u32x4& e1, unsigned (&a)[8], const unsigned (&p)[8]) {
#pragma unroll
    for (int r = 7; r >= 0; --r) a[r] = __builtin_elementwise_add_sat(a[r], p[r]);
    SB();
#pragma unroll
    for (int r = 7; r >= 0; --r) *(lds_u16_p)a[r] = r < 4 ? e0[r & 3] : e1[r & 3];
    SB();
  };
  auto add8 = [&](u32x4 e0, u32x4 e1) { unsigned a[8], p[8]; take(e0, e1, a, p); fill(e0, e1, a, p); };
  const unsigned bo = WS_BL + bin * 5 * 260 * 64 + lane * 16;
  auto ld = [&](int g, int k) { return __builtin_amdgcn_raw_buffer_load_b128(wsrd, bo + (g * 260 + k * 16) * 64, 0, 16); };
  auto fix = [&](u32x4 e, unsigned last) { return e | (unsigned)(__builtin_amdgcn_mov_dpp(last, 0xff, 15, 15, 0) >> 31); };
  bool ovf = false;
  unsigned big = 0; int off[10], tot = 0;
#pragma unroll 1
  for (int pass = 0; pass < 2; ++pass) {
    if (!ovf) {
      u32x4 c[64];
#pragma unroll
      for (int k = 0; k < 16; ++k) c[k] = ld(0, k);
      unsigned m = ~0u, a[2][8], p[2][8];
#pragma unroll
      for (int k = 0; k < 66; k += 2) {
        SB();
        if (k < 64) {
          if (k >= 48) { c[k] = fix(c[k], c[k - 16][3]); c[k + 1] = fix(c[k + 1], c[k - 15][3]); }
          if ((k & 15) == 14 && lane >= 48) c[k + 1] = none;
          if (k >= 48) m &= c[k][3] & c[k + 1][3];
          take(c[k], c[k + 1], a[k >> 1 & 1], p[k >> 1 & 1]);
        }
        if (k < 16) {
#pragma unroll
          for (int i = 16 + 2 * k; i < 20 + 2 * k; ++i) c[i] = ld(i >> 4, i & 15);
        }
        if (k >= 24 && k < 40) {
#pragma unroll
          for (int i = k - 24; i < k - 22; ++i)
            c[48 + i] = __builtin_amdgcn_raw_buffer_load_b128(wsrd, bo + (780 + i * 16) * 64 + (__builtin_amdgcn_mov_dpp(c[32 + i][3], 0xff, 15, 15, 0) & 0x40000000), 0, 16);
        }
        SB();
        if (k) fill(c[k - 2], c[k - 1], a[~k >> 1 & 1], p[~k >> 1 & 1]);
      }
      SB();
      if (__ballot((lane & 3) == 3 && m != ~0u)) {
        unsigned more = 0;
#pragma unroll
        for (int k = 0; k < 16; ++k) more |= __ballot((lane & 3) == 3 && c[48 + k][3] != ~0u) ? 1u << k : 0u;
#pragma unroll 1
        for (; more; more &= more - 1) {
          const int k = __builtin_ctz(more);
          u32x4 e = ld(4, k);
          if (k == 15 && lane >= 48) e = none;
          ovf |= __ballot((lane & 3) == 3 && e[3] != ~0u) != 0;
          add8(e, none);
        }
      }
      if (ovf) clear();
    }
    if (ovf) {
      for (int q = lane; q < EE / 4; q += 64) {
        const u32x4 d = reinterpret_cast<const u32x4*>(dst)[q], s = reinterpret_cast<const u32x4*>(src)[q];
        u32x4 e;
#pragma unroll
        for (int r = 0; r < 4; ++r) e[r] = d[r] - lo < 640u ? (d[r] - lo) << 16 | s[r] : ~0u;
        add8(e, none);
      }
    }
    if (pass) break;
    asm volatile("" : "+v"(lane));
    const int qd = lane & 3, nl = lane >> 2;
    int inc = 0;
#pragma unroll
    for (int i = 0; i < 10; ++i) off[i] = 46 - (int)((lds_u32_p)(rec + (64 * i + lane) * 52))[12];
#pragma unroll
    for (int i = 0; i < 10; ++i) {
      const int cn = off[i];
      off[i] = cn > 48 ? cn : 0; inc += off[i];
      if (cn > 48) { big |= 1u << i; ((lds_u16_p)ext)[2 * (64 * i + lane)] = 0xffff; }
    }
    __builtin_amdgcn_wave_barrier();
    auto put = [&](int odd) {
#pragma unroll 2
      for (int bl = 0; bl < 4; ++bl) {
        u32x4 o[10];
#pragma unroll
        for (int T = 0; T < 10; ++T) if (((2 * T + 1) / 5 & 1) == odd) {
          const lds_u32_p r = (lds_u32_p)(rec + (bl * 160 + T * 16 + nl) * 52);
          o[T] = (u32x4){r[3 * qd], r[1 + 3 * qd], r[2 + 3 * qd], ((lds_u32_p)ext)[bl * 160 + T * 16 + nl]};
        }
#pragma unroll
        for (int T = 0; T < 10; ++T) if (((2 * T + 1) / 5 & 1) == odd) {
          const int c = (2 * T + 1) / 5, ro = 16 * (T - (5 * c >> 1)) + nl, w = c & 1 ? ro * 43 >> 9 : ro >> 3;
          __builtin_amdgcn_raw_buffer_store_b128(o[T], wsrd, WS_GL + c * 4096 + w * (c & 1 ? 256 : 512) + ro * 64 + qd * 16, (bin * 4 + bl) * 16384, 16);
        }
      }
    };
    put(1);
    asm volatile("" ::: "memory");
    put(0);
    asm volatile("s_waitcnt vmcnt(16)" ::: "memory");
    if (lane == 0) st_flag(wsrd, WS_LF + bin * 128 + 64, tag);
#pragma unroll
    for (int o = 1; o < 64; o <<= 1) { const int y = __shfl_up(inc, o); if (lane >= o) inc += y; }
    tot = __builtin_amdgcn_readlane(inc, 63);
    drain();
    if (lane == 0) st_flag(wsrd, WS_LF + bin * 128, tag);
    __syncthreads();
    int j2 = j * 4, w2 = wid, l2 = lane;
    asm volatile("" : "+s"(j2), "+s"(w2), "+v"(l2));
    product_load(lds, w2, l2, j2, wv, wm, wo, true, true);
#pragma unroll 1
    for (int job = 0; job < 4; ++job) {
      if (job) asm volatile("s_waitcnt vmcnt(4)" ::: "memory"); else drain();
      __syncthreads();
      asm volatile("" : "+s"(w2));
      if (job < 3) product_load(lds + (~job & 1) * 32768, w2, l2, j2 + job + 1, wv, wm, wo, false, true);
      product_w(lds, w2, l2, j2 + job, wsrd);
    }
    if (w2 == 0) *(lds_u32_p)(ext - 8) = 0;
    __syncthreads();
    if (tot == 0 || tot > 2 * LONGS) break;
#pragma unroll
    for (int i = 0; i < 10; ++i) {
      const unsigned r = rec + (64 * i + lane) * 52;
      ((lds_u32_p)r)[12] = off[i] ? ext + 2558 + inc - r : -2u;
      inc -= off[i]; off[i] = off[i] << 15 | inc;
    }
  }
  __syncthreads();
  const unsigned ex0 = (unsigned)(size_t)lds + 0x20800;
  const lds_u32_p lng = (lds_u32_p)(size_t)(unsigned)(size_t)lds;
#pragma unroll
  for (int i = 0; i < 10; ++i)
    if (big >> i & 1) {
      const unsigned q = __atomic_fetch_add((lds_u32_p)(ex0 - 8), 1u, __ATOMIC_RELAXED);
      lng[2 * q] = tot > 2 * LONGS ? ~0u : off[i] + wid * 6912 + 2560; lng[2 * q + 1] = (((lds_u32_p)ext)[64 * i + lane] & 0xffff0000u) | (lo + 64 * i + lane);
    }
  __syncthreads();
  const int nl_ = *(lds_u32_p)(ex0 - 8);
  bf16x8 b1;
#pragma unroll
  for (int e = 0; e < 8; ++e) b1[e] = (lane & 15) == e + ((lane >> 1) & 8) ? 0x3f80 : 0;
  const unsigned lo16 = (lane & 31) * 16;
  {
    u32x4 d[3][16]; unsigned nn[3]; int mode[3], q = wid, hf = 0;
    auto issue = [&](u32x4 (&v)[16], unsigned& n, int& md) {
      const unsigned w = __builtin_amdgcn_readfirstlane(lng[2 * q]);
      n = lng[2 * q + 1];
      const int dg = q < nl_ && w >> 16 <= 64 ? w >> 16 : 0, g = dg - hf - (lane >> 5);
      const lds_u16_p f = (lds_u16_p)(ex0 + (w & 0xffffu)) + hf + (lane >> 5);
      md = q >= nl_ ? -1 : dg == 0 ? 0 : dg - hf > 32 ? 1 : 2;
      hf = md == 1 ? 32 : 0; q += md == 1 ? 0 : 4;
      unsigned id[16];
#pragma unroll
      for (int t = 0; t < 16; ++t) id[t] = f[2 * t];
#pragma unroll
      for (int t = 0; t < 16; ++t) { asm volatile("" : "+v"(id[t])); v[t] = __builtin_amdgcn_raw_buffer_load_b128(msrd, (2 * t < g ? id[t] : 0xffffu) * 512u + lo16, 0, 16); }
    };
    issue(d[0], nn[0], mode[0]); issue(d[1], nn[1], mode[1]);
    f32x4 keep = {};
#pragma unroll 1
    for (bool go = true; go;) {
#pragma unroll
      for (int s = 0; s < 3; ++s) {
        if (mode[s] < 0) { go = false; break; }
        issue(d[(s + 2) % 3], nn[(s + 2) % 3], mode[(s + 2) % 3]); SB();
        f32x4 acc = {};
#pragma unroll
        for (int t = 0; t < 16; ++t) acc = MFMA16(b1, as_bf(d[s][t]), acc);
        local_store(wsrd, mode[s] == 2 ? nn[s] & 0xffffu : 0x200000u, lane, acc + keep, bfhi(nn[s]));
        keep = mode[s] == 1 ? acc : (f32x4){0.f, 0.f, 0.f, 0.f}; SB();
      }
    }
  }
#pragma unroll 1
  for (int q0 = 0; q0 < nl_; q0 += 256)
#pragma unroll 1
  for (unsigned long long m = __ballot(q0 + 4 * lane + 3 - wid < nl_ && lng[2 * (q0 + 4 * lane + 3 - wid)] >> 16 > 64); m; m &= m - 1) {
    const int q = q0 + 4 * __builtin_ctzll(m) + 3 - wid;
    const unsigned w = __builtin_amdgcn_readfirstlane(lng[2 * q]), n = __builtin_amdgcn_readfirstlane(lng[2 * q + 1]); const int dg = w >> 16;
    f32x4 acc = {};
    if (w == ~0u) {
      for (int e0 = 0; e0 < EE; e0 += 64) {
        unsigned long long h = __ballot(dst[e0 + lane] == (n & 0xffffu));
        while (h) {
          const int e = e0 + __builtin_ctzll(h); h &= h - 1;
          const unsigned vo = lane < 32 ? (unsigned)src[e] * 512 + lo16 : 0x7fffff00u;
          acc = MFMA16(b1, as_bf(__builtin_amdgcn_raw_buffer_load_b128(msrd, vo, 0, 16)), acc);
        }
      }
    } else {
      const lds_u16_p f = (lds_u16_p)(ex0 + (w & 0xffffu)), u = (lds_u16_p)(ext + 2624 + 2 * LONGS);
      for (int x = lane; x < dg; x += 64) {
        const int key = f[x]; int p = 0;
#pragma unroll 8
        for (int t = 0; t < dg; ++t) { const int v = f[t]; p += v < key || (v == key && t < x); }
        u[p] = key;
      }
      __builtin_amdgcn_wave_barrier();
      for (int x = lane >> 5; x < dg + (lane >> 5); x += 32) {
        u32x4 d[16]; unsigned id[16]; f32x4 a2 = {};
#pragma unroll
        for (int t = 0; t < 16; ++t) id[t] = u[min(x + 2 * t, dg)];
#pragma unroll
        for (int t = 0; t < 16; ++t) { asm volatile("" : "+v"(id[t])); d[t] = __builtin_amdgcn_raw_buffer_load_b128(msrd, (x + 2 * t < dg ? id[t] : 0xffffu) * 512u + lo16, 0, 16); }
#pragma unroll
        for (int t = 0; t < 16; ++t) a2 = MFMA16(b1, as_bf(d[t]), a2);
        acc += a2;
      }
      __builtin_amdgcn_wave_barrier();
    }
    local_store(wsrd, n & 0xffffu, lane, acc, bfhi(n));
  }
  drain();
  __syncthreads();
  if (wid == 0 && lane == 0) st_flag(wsrd, WS_WF + j * 4, tag);
}

DI void exact_rows(char* lds, int b, int wid, char* ws, const u16* x, u16* out) {
  int lane; asm volatile("v_mbcnt_lo_u32_b32 %0, -1, 0\n\tv_mbcnt_hi_u32_b32 %0, -1, %0" : "=v"(lane));
  const srd_t w = __builtin_amdgcn_make_buffer_rsrc(ws, 0, WS_END, 0x00020000);
  const srd_t xs = __builtin_amdgcn_make_buffer_rsrc(const_cast<u16*>(x), 0, NN * 512, 0x00020000);
  const srd_t os = __builtin_amdgcn_make_buffer_rsrc(out, 0, NN * 512, 0x00020000);
  const int n = lane & 15, g = lane >> 4, c = b & 3;
  f32x4* red = reinterpret_cast<f32x4*>(lds);
  float* mx = reinterpret_cast<float*>(lds + 65536);
  auto ld = [&](unsigned off) { return as_bf(__builtin_amdgcn_raw_buffer_load_b128(w, off, 0, 16)); };
#pragma unroll 1
  for (int i = 0; i < 2 + (c & 1); ++i) {
    const int tile = (b >> 2) * 10 + (5 * c >> 1) + i;
    bf16x8 q[8];
#pragma unroll
    for (int ks = 0; ks < 8; ++ks) q[ks] = ld(WS_QP + tile * 8192 + ks * 1024 + lane * 16);
    auto score = [&](int kt) {
      f32x4 s = {};
#pragma unroll
      for (int ks = 0; ks < 8; ++ks) s = MFMA16(ld(WS_H + (kt * 16 + n) * 512 + ks * 64 + g * 16), q[ks], s);
      return s;
    };
    float m = -__builtin_inff();
#pragma unroll 1
    for (int kt = wid; kt < NN / 16; kt += 4) { const f32x4 s = score(kt); m = fmaxf(fmaxf(m, fmaxf(s[0], s[1])), fmaxf(s[2], s[3])); }
    __syncthreads();
    mx[wid * 64 + lane] = m;
    __syncthreads();
#pragma unroll
    for (int r = 0; r < 16; ++r) m = fmaxf(m, mx[r * 16 + n]);
    f32x4 acc[16] = {};
#pragma unroll 1
    for (int kt = wid; kt < NN / 16; kt += 4) {
      const f32x4 s = score(kt);
      const u32x2 p = {pk2(__builtin_amdgcn_exp2f(s[0] - m), __builtin_amdgcn_exp2f(s[1] - m)), pk2(__builtin_amdgcn_exp2f(s[2] - m), __builtin_amdgcn_exp2f(s[3] - m))};
#pragma unroll
      for (int dt = 0; dt < 16; ++dt) {
        bf16x4 v;
#pragma unroll
        for (int e = 0; e < 4; ++e) v[e] = __builtin_amdgcn_raw_buffer_load_b16(w, WS_H + (kt * 16 + 4 * g + e) * 512 + n * 2, dt * 32, 16);
        acc[dt] = __builtin_amdgcn_mfma_f32_16x16x16bf16_1k(v, __builtin_bit_cast(bf16x4, p), acc[dt], 0, 0, 0);
      }
    }
#pragma unroll
    for (int dt = 0; dt < 16; ++dt) red[(wid * 16 + dt) * 64 + lane] = acc[dt];
    __syncthreads();
    float l = 0.f;
#pragma unroll
    for (int r = 0; r < 4; ++r) l += red[(r * 16 + 15) * 64 + 48 + n][3];
    l = 1.f / l;
    f32x4 y[4] = {};
#pragma unroll 1
    for (int ks = 0; ks < 8; ++ks) {
      f32x4 o[2];
#pragma unroll
      for (int h = 0; h < 2; ++h) o[h] = (red[(2 * ks + h) * 64 + lane] + red[(16 + 2 * ks + h) * 64 + lane] + red[(32 + 2 * ks + h) * 64 + lane] + red[(48 + 2 * ks + h) * 64 + lane]) * l;
      const bf16x8 of = as_bf((u32x4){pk2(o[0][0], o[0][1]), pk2(o[0][2], o[0][3]), pk2(o[1][0], o[1][1]), pk2(o[1][2], o[1][3])});
      const bf16x8 gf = ld(WS_G + (tile * 16 + n) * 512 + ks * 64 + g * 16);
#pragma unroll
      for (int et = 0; et < 4; ++et) {
        y[et] = MFMA16(ld(WS_WV + wid * 32768 + ks * 4096 + et * 1024 + lane * 16), of, y[et]);
        y[et] = MFMA16(ld(WS_WM + wid * 32768 + (et < 2 ? 16384 : 0) + ks * 2048 + (et & 1) * 1024 + lane * 16), gf, y[et]);
      }
    }
#pragma unroll
    for (int jj = 0; jj < 2; ++jj) {
      const unsigned at = (tile * 16 + n) * 512 + wid * 128 + jj * 64 + g * 16;
      const u32x4 xv = __builtin_amdgcn_raw_buffer_load_b128(xs, at, 0, 0);
      u32x4 o;
#pragma unroll
      for (int r = 0; r < 4; ++r) o[r] = pk2(y[2 * jj + (r >> 1)][2 * (r & 1)] + bf2f(xv[r] & 0xffffu), y[2 * jj + (r >> 1)][2 * (r & 1) + 1] + bfhi(xv[r]));
      __builtin_amdgcn_raw_buffer_store_b128(o, os, at, 0, 0);
    }
  }
}

__global__ __launch_bounds__(256, 1) void attn_kernel(char* ws, const int* __restrict__ src, const int* __restrict__ dst,
                                                      const u16* __restrict__ deg_inv, const u16* __restrict__ x, u16* out,
                                                      const u16* __restrict__ wq, const u16* __restrict__ wk, const u16* __restrict__ wv,
                                                      const u16* __restrict__ wm, const u16* __restrict__ wo) {
  extern __shared__ __attribute__((aligned(16))) char lds[];
  asm volatile("s_load_dword s99, %0, 0" :: "s"(__builtin_amdgcn_kernarg_segment_ptr()) : "s99");
  int b = (blockIdx.x & 6) << 5 | blockIdx.x >> 3 << 1 | (blockIdx.x & 1);
  code_touch((blockIdx.x >> 3 & 1) * 256 + threadIdx.x, (unsigned)(size_t)lds + 0x27f00u);
  int wid = __builtin_amdgcn_readfirstlane(threadIdx.x >> 6);
  static_assert(WS_END == 37557248 && WS_EP == 5777664, "workspace layout of the generated code");
  auto ka = __builtin_amdgcn_kernarg_segment_ptr();
  asm volatile(
    "s_cmpk_ge_u32 %1, 252\n"
    "s_cbranch_scc1 .Lat_end\n"
    "s_load_dwordx2 s[20:21], %0, 0x30\n"
    "s_load_dwordx2 s[24:25], %0, 0x38\n"
    "s_load_dwordx2 s[12:13], %0, 0x20\n"
    "s_load_dwordx2 s[8:9], %0, 0x0\n"
    "s_load_dwordx2 s[16:17], %0, 0x28\n"
    "s_load_dwordx4 s[28:31], %0, 0x8\n"
    "v_mbcnt_lo_u32_b32 v24, -1, 0\n"
    "v_mbcnt_hi_u32_b32 v24, -1, v24\n"
    "s_and_b32 s50, %2, 1\n"
    "s_lshr_b32 s51, %2, 1\n"
    "v_lshrrev_b32_e32 v25, 3, v24\n"
    "v_and_b32_e32 v26, 6, v25\n"
    "v_and_b32_e32 v27, 7, v24\n"
    "v_xor_b32_e32 v26, v26, v27\n"
    "v_lshlrev_b32_e32 v26, 4, v26\n"
    "v_and_b32_e32 v27, 4, v25\n"
    "v_add_u32_e32 v27, v27, v25\n"
    "v_lshl_add_u32 v254, v27, 9, v26\n"
    "v_add_u32_e32 v255, 0x1c00, v254\n"
    "s_mov_b32 s22, 0x20000\n"
    "s_mov_b32 s23, 0x20000\n"
    "s_mov_b32 s26, 0x20000\n"
    "s_mov_b32 s27, 0x20000\n"
    "s_lshr_b32 s36, %1, 7\n"
    "s_mul_i32 s36, s36, 3\n"
    "s_add_u32 s36, s36, %1\n"
    "s_and_b32 s36, s36, 127\n"
    "s_lshl_b32 s45, %2, 7\n"
    "s_lshl_b32 s46, %2, 12\n"
    "s_add_u32 s46, s46, %3\n"
    "s_and_b32 s47, s36, 7\n"
    "s_lshl_b32 s47, s47, 14\n"
    "s_add_u32 m0, s46, 32768\n"
    "s_add_u32 s47, s47, s45\n"
    "s_and_b32 s32, %1, 3\n"
    "s_lshr_b32 s33, %1, 2\n"
    "s_mul_i32 s38, s32, 5\n"
    "s_lshr_b32 s38, s38, 1\n"
    "s_mul_i32 s35, s33, 10\n"
    "s_add_u32 s35, s35, s38\n"
    "s_lshl_b32 s35, s35, 13\n"
    "s_mov_b32 s14, 5120000\n"
    "s_mov_b32 s15, 0x20000\n"
    "s_waitcnt lgkmcnt(0)\n"
    "buffer_load_dwordx4 v254, s[20:23], s47 offen offset:0 lds\n"
    "s_add_u32 s48, s47, 1024\n"
    "buffer_load_dwordx4 v255, s[20:23], s47 offen offset:3072 lds\n"
    "s_add_u32 s47, s47, 0x1800\n"
    "buffer_load_dwordx4 v254, s[20:23], s48 offen offset:1024 lds\n"
    "buffer_load_dwordx4 v254, s[20:23], s47 offen offset:2048 lds\n"
    "s_lshr_b32 s47, s36, 4\n"
    "s_lshl_b32 s47, s47, 14\n"
    "s_and_b32 s48, s36, 8\n"
    "s_lshl_b32 s48, s48, 8\n"
    "s_add_u32 s47, s47, s48\n"
    "s_add_u32 m0, s46, 49152\n"
    "s_add_u32 s47, s47, s45\n"
    "buffer_load_dwordx4 v254, s[24:27], s47 offen offset:0 lds\n"
    "buffer_load_dwordx4 v255, s[24:27], s47 offen offset:1024 lds\n"
    "s_add_u32 s47, s45, 120832\n"
    "buffer_load_dwordx4 v254, s[24:27], s47 offen offset:2048 lds\n"
    "s_add_u32 s47, s47, 1024\n"
    "buffer_load_dwordx4 v254, s[24:27], s47 offen offset:3072 lds\n"
    "s_mul_i32 s46, %1, 159\n"
    "s_lshl_b32 s45, s50, 6\n"
    "v_add_u32_e32 v111, s45, v24\n"
    "v_add_u32_e32 v108, s46, v111\n"
    "v_min_u32_e32 v108, 39999, v108\n"
    "v_lshlrev_b32_e32 v108, 4, v108\n"
    "s_lshl1_add_u32 s47, s46, 256\n"
    "v_add_u32_e32 v109, s47, v111\n"
    "v_min_u32_e32 v109, 79999, v109\n"
    "v_lshlrev_b32_e32 v109, 3, v109\n"
    "s_cmp_lg_u32 s51, 0\n"
    "s_cselect_b64 exec, -1, 1\n"
    "global_load_dwordx4 v[96:99], v108, s[30:31]\n"
    "global_load_dwordx2 v[92:93], v109, s[30:31]\n"
    "global_load_dwordx4 v[100:103], v108, s[28:29]\n"
    "global_load_dwordx2 v[94:95], v109, s[28:29]\n"
    "s_mov_b64 exec, -1\n"
    "s_lshl_b32 s45, %2, 6\n"
    "v_add_u32_e32 v108, s45, v24\n"
    "s_sub_u32 s59, 40000, s46\n"
    "s_lshl1_add_u32 s61, s59, -256\n"
    "s_min_i32 s61, s61, 62\n"
    "s_lshl_b32 s45, %2, 2\n"
    "s_add_u32 s54, s35, 0x2000\n"
    "s_add_u32 s55, s35, 0x4000\n"
    "v_lshlrev_b32_e32 v253, 4, v24\n"
    "v_lshrrev_b32_e32 v28, 4, v24\n"
    "v_add_u32_e32 v28, s45, v28\n"
    "v_and_b32_e32 v104, 0xf0, v253\n"
    "v_lshl_add_u32 v104, v28, 9, v104\n"
    "buffer_load_dwordx4 v[0:3], v104, s[12:15], s35 offen offset:0\n"
    "buffer_load_dwordx4 v[4:7], v104, s[12:15], s35 offen offset:256\n"
    "s_bitcmp1_b32 %1, 0\n"
    "s_cselect_b32 s60, s55, 2.0\n"
    "buffer_load_dwordx4 v[8:11], v104, s[12:15], s54 offen offset:0\n"
    "buffer_load_dwordx4 v[12:15], v104, s[12:15], s54 offen offset:256\n"
    "buffer_load_dwordx4 v[16:19], v104, s[12:15], s55 offen offset:0\n"
    "buffer_load_dwordx4 v[20:23], v104, s[12:15], s55 offen offset:256\n"
    "s_mov_b32 s10, 37557248\n"
    "s_mov_b32 s11, 0x20000\n"
    "s_mov_b32 s37, 5777664\n"
    "s_buffer_load_dword s39, s[8:11], s37\n"
    "s_bfe_u32 s37, %1, 0x20001\n"
    "v_and_b32_e32 v27, 1, v25\n"
    "v_lshl_add_u32 v248, v27, 9, v26\n"
    "v_lshrrev_b32_e32 v27, 1, v25\n"
    "v_lshl_add_u32 v248, v27, 12, v248\n"
    "s_mul_i32 s40, s32, 39\n"
    "s_and_b32 s34, s32, 1\n"
    "s_add_u32 s40, s40, s34\n"
    "s_lshl1_add_u32 s40, s40, s51\n"
    "s_lshl_b32 s40, s40, 14\n"
    "s_lshl_b32 s47, s50, 8\n"
    "s_add_u32 s40, s40, s47\n"
    "s_add_u32 s41, s40, 128\n"
    "s_lshl_b32 s34, s34, 15\n"
    "s_xor_b32 s43, s50, s51\n"
    "s_lshl3_add_u32 s43, s50, s43\n"
    "s_lshl_b32 s43, s43, 13\n"
    "s_add_u32 s47, %3, 32768\n"
    "s_add_u32 s43, s43, s47\n"
    "v_bfe_u32 v25, v24, 2, 3\n"
    "v_lshlrev_b32_e32 v244, 7, v25\n"
    "v_lshrrev_b32_e32 v26, 5, v24\n"
    "v_lshl_add_u32 v244, v26, 10, v244\n"
    "v_and_b32_e32 v25, 6, v25\n"
    "v_bfe_u32 v26, v24, 1, 1\n"
    "v_xor_b32_e32 v25, v25, v26\n"
    "v_lshl_add_u32 v244, v25, 4, v244\n"
    "v_and_b32_e32 v26, 1, v24\n"
    "v_lshl_add_u32 v244, v26, 3, v244\n"
    "s_lshl_b32 s48, s51, 16\n"
    "s_add_u32 s48, s48, s47\n"
    "v_add_u32_e32 v244, s48, v244\n"
    "v_xor_b32_e32 v245, 32, v244\n"
    "v_xor_b32_e32 v246, 64, v244\n"
    "v_xor_b32_e32 v247, 0x60, v244\n"
    "v_and_b32_e32 v25, 7, v24\n"
    "v_lshlrev_b32_e32 v27, 7, v25\n"
    "v_bfe_u32 v26, v24, 3, 1\n"
    "v_lshl_add_u32 v27, v26, 10, v27\n"
    "v_and_b32_e32 v25, 6, v24\n"
    "v_lshrrev_b32_e32 v26, 4, v24\n"
    "v_xor_b32_e32 v25, v25, v26\n"
    "v_lshl_add_u32 v27, v25, 4, v27\n"
    "s_lshl_b32 s48, s51, 13\n"
    "s_add_u32 s48, s48, s47\n"
    "v_add_u32_e32 v240, s48, v27\n"
    "v_xor_b32_e32 v241, 64, v240\n"
    "v_and_b32_e32 v29, 15, v24\n"
    "s_cmp_ge_u32 s36, 112\n"
    "s_cselect_b32 s47, 8, 16\n"
    "v_cmp_le_u32_e32 vcc, s47, v29\n"
    "v_mov_b32_e32 v64, 0xbd80bd80\n"
    "v_mov_b32_e32 v65, 0xbdabbdab\n"
    "v_mov_b32_e32 v66, 0x392b392b\n"
    "v_cndmask_b32_e32 v64, v64, v65, vcc\n"
    "v_cndmask_b32_e32 v66, 0, v66, vcc\n"
    "s_cselect_b32 s47, 56, 64\n"
    "v_cmp_le_u32_e32 vcc, s47, v24\n"
    "v_mov_b32_e32 v65, 0x3e80bdab\n"
    "v_mov_b32_e32 v67, 0x392b\n"
    "v_cndmask_b32_e32 v65, v64, v65, vcc\n"
    "v_cndmask_b32_e32 v67, v66, v67, vcc\n"
    "s_cmp_ge_u32 s36, 120\n"
    "s_cselect_b32 s47, 15, 16\n"
    "v_cmp_ne_u32_e32 vcc, s47, v29\n"
    "v_mov_b32_e32 v68, 0x3db8aa3b\n"
    "s_lshr_b32 s47, s36, 5\n"
    "v_cndmask_b32_e32 v68, 0, v68, vcc\n"
    "s_lshl_b32 s47, s47, 15\n"
    "s_and_b32 s48, s36, 7\n"
    "s_lshl_b32 s48, s48, 11\n"
    "s_add_u32 s47, s47, s48\n"
    "s_and_b32 s48, s36, 8\n"
    "s_lshl_b32 s48, s48, 7\n"
    "s_add_u32 s47, s47, s48\n"
    "s_and_b32 s48, s36, 16\n"
    "s_lshl_b32 s48, s48, 10\n"
    "s_add_u32 s47, s47, s48\n"
    "s_add_u32 s52, s47, 5242880\n"
    "s_lshl_b32 s48, s50, 9\n"
    "s_add_u32 s52, s52, s48\n"
    "s_lshl_b32 s48, s50, 11\n"
    "v_add_u32_e32 v72, s48, v240\n"
    "v_add_u32_e32 v73, s48, v241\n"
    "v_and_b32_e32 v74, 0x1f0, v253\n"
    "v_lshrrev_b32_e32 v29, 5, v24\n"
    "v_lshl_add_u32 v74, v29, 3, v74\n"
    "s_lshl_b32 s53, s36, 1\n"
    "s_add_u32 s53, s53, s50\n"
    "s_lshl_b32 s53, s53, 4\n"
    "s_add_u32 s53, s53, 5771264\n"
    "s_cmp_eq_u32 s51, 0\n"
    "s_cselect_b32 s52, s52, 2.0\n"
    "s_cselect_b32 s53, s53, 2.0\n"
    "s_lshl_b32 s47, %1, 4\n"
    "s_add_u32 s47, s47, 5701632\n"
    "s_lshl_b32 s48, %1, 7\n"
    "s_add_u32 s48, s48, 5705728\n"
    "s_cmp_eq_u32 s50, 0\n"
    "s_cselect_b32 s47, s47, s48\n"
    "s_cmp_eq_u32 s51, 0\n"
    "s_cselect_b32 s47, s53, s47\n"
    "buffer_store_dword v24, off, s[8:11], s47 offset:8 sc1\n"
    "v_lshrrev_b32_e32 v109, 2, v108\n"
    "v_mul_u32_u24_e32 v109, 0x4100, v109\n"
    "v_and_b32_e32 v110, 48, v253\n"
    "v_or_b32_e32 v109, v109, v110\n"
    "v_add_u32_e32 v110, 0x104000, v109\n"
    "v_cmp_gt_u32_e32 vcc, 64, v108\n"
    "s_add_u32 s44, %3, 24576\n"
    "v_lshl_add_u32 v108, v108, 4, s44\n"
    "v_cndmask_b32_e32 v110, 2.0, v110, vcc\n"
    "v_mov_b32_e32 v32, -1\n"
    "v_mov_b32_e32 v33, -1\n"
    "v_mov_b32_e32 v34, -1\n"
    "v_mov_b32_e32 v35, -1\n"
    "ds_write_b128 v108, v[32:35] offset:0\n"
    "ds_write_b128 v108, v[32:35] offset:1280\n"
    "v_and_b32_e32 v29, 6, v28\n"
    "v_xor_b32_e32 v29, v29, v24\n"
    "v_and_b32_e32 v29, 7, v29\n"
    "v_lshlrev_b32_e32 v105, 7, v28\n"
    "v_lshl_add_u32 v105, v29, 4, v105\n"
    "v_bfe_u32 v29, v24, 3, 1\n"
    "v_subrev_u32_e32 v29, s37, v29\n"
    "v_and_b32_e32 v29, 3, v29\n"
    "v_lshl_add_u32 v105, v29, 11, v105\n"
    "s_add_u32 s45, %3, 65536\n"
    "v_add_u32_e32 v105, s45, v105\n"
    "v_xor_b32_e32 v106, 0x1000, v105\n"
    "v_and_b32_e32 v29, 15, v24\n"
    "v_cmp_eq_u32_e32 vcc, 15, v29\n"
    "v_mov_b32_e32 v107, 0xffff\n"
    "s_mov_b32 s58, 0x3f803f80\n"
    "s_mov_b32 s57, 0x43c00000\n"
    "v_cndmask_b32_e32 v107, -1, v107, vcc\n"
    "s_mov_b32 s56, 0xc3c00000\n"
    "s_mov_b32 s62, 0xc000c000\n"
    "s_mov_b32 s63, 0xc000c000\n"
    "v_add_u32_e32 v250, s45, v27\n"
    "v_xor_b32_e32 v251, 64, v250\n"
    "s_lshl_b32 s47, %2, 10\n"
    "s_add_u32 s47, s47, 5771264\n"
    "v_lshl_add_u32 v252, v24, 4, s47\n"
    "s_waitcnt vmcnt(11)\n"
    "s_waitcnt lgkmcnt(0)\n"
    "s_barrier\n"
    "s_add_u32 s39, s39, 2\n"
    "s_cmp_lg_u32 s51, 0\n"
    "s_cbranch_scc1 .Lat_bin\n"
    "ds_read_b128 v[112:115], v72 offset:0\n"
    "ds_read_b128 v[208:211], v240 offset:18432\n"
    "ds_read_b128 v[176:179], v240 offset:16384\n"
    "ds_read_b128 v[116:119], v73 offset:0\n"
    "ds_read_b128 v[212:215], v241 offset:18432\n"
    "ds_read_b128 v[180:183], v241 offset:16384\n"
    "ds_read_b128 v[120:123], v72 offset:4096\n"
    "ds_read_b128 v[216:219], v240 offset:22528\n"
    "ds_read_b128 v[184:187], v240 offset:20480\n"
    "ds_read_b128 v[124:127], v73 offset:4096\n"
    "ds_read_b128 v[220:223], v241 offset:22528\n"
    "ds_read_b128 v[188:191], v241 offset:20480\n"
    "ds_read_b128 v[128:131], v72 offset:8192\n"
    "ds_read_b128 v[224:227], v240 offset:26624\n"
    "ds_read_b128 v[192:195], v240 offset:24576\n"
    "s_waitcnt lgkmcnt(12)\n"
    "v_mfma_f32_16x16x32_bf16 v[56:59], v[208:211], v[112:115], 0\n"
    "ds_read_b128 v[132:135], v73 offset:8192\n"
    "ds_read_b128 v[228:231], v241 offset:26624\n"
    "v_mfma_f32_16x16x32_bf16 v[48:51], v[112:115], v[176:179], 0\n"
    "ds_read_b128 v[196:199], v241 offset:24576\n"
    "s_waitcnt lgkmcnt(12)\n"
    "v_mfma_f32_16x16x32_bf16 v[56:59], v[212:215], v[116:119], v[56:59]\n"
    "ds_read_b128 v[136:139], v72 offset:12288\n"
    "ds_read_b128 v[232:235], v240 offset:30720\n"
    "v_mfma_f32_16x16x32_bf16 v[48:51], v[116:119], v[180:183], v[48:51]\n"
    "ds_read_b128 v[200:203], v240 offset:28672\n"
    "s_waitcnt lgkmcnt(12)\n"
    "v_mfma_f32_16x16x32_bf16 v[56:59], v[216:219], v[120:123], v[56:59]\n"
    "ds_read_b128 v[140:143], v73 offset:12288\n"
    "ds_read_b128 v[236:239], v241 offset:30720\n"
    "v_mfma_f32_16x16x32_bf16 v[48:51], v[120:123], v[184:187], v[48:51]\n"
    "ds_read_b128 v[204:207], v241 offset:28672\n"
    "s_waitcnt lgkmcnt(12)\n"
    "v_mfma_f32_16x16x32_bf16 v[56:59], v[220:223], v[124:127], v[56:59]\n"
    "v_mfma_f32_16x16x32_bf16 v[48:51], v[124:127], v[188:191], v[48:51]\n"
    "s_waitcnt lgkmcnt(9)\n"
    "v_mfma_f32_16x16x32_bf16 v[56:59], v[224:227], v[128:131], v[56:59]\n"
    "v_mfma_f32_16x16x32_bf16 v[48:51], v[128:131], v[192:195], v[48:51]\n"
    "s_waitcnt lgkmcnt(6)\n"
    "v_mfma_f32_16x16x32_bf16 v[56:59], v[228:231], v[132:135], v[56:59]\n"
    "v_mfma_f32_16x16x32_bf16 v[48:51], v[132:135], v[196:199], v[48:51]\n"
    "s_waitcnt lgkmcnt(3)\n"
    "v_mfma_f32_16x16x32_bf16 v[56:59], v[232:235], v[136:139], v[56:59]\n"
    "v_mfma_f32_16x16x32_bf16 v[48:51], v[136:139], v[200:203], v[48:51]\n"
    "s_waitcnt lgkmcnt(0)\n"
    "v_mfma_f32_16x16x32_bf16 v[56:59], v[236:239], v[140:143], v[56:59]\n"
    "v_mfma_f32_16x16x32_bf16 v[48:51], v[140:143], v[204:207], v[48:51]\n"
    "s_nop 3\n"
    "v_cvt_pk_bf16_f32 v56, v56, v57\n"
    "v_cvt_pk_bf16_f32 v57, v58, v59\n"
    "s_nop 1\n"
    "v_mfma_f32_16x16x16_bf16 v[48:51], v[56:57], v[64:65], v[48:51]\n"
    "v_mfma_f32_16x16x16_bf16 v[48:51], v[56:57], v[66:67], v[48:51]\n"
    "s_nop 7\n"
    "v_mul_f32_e32 v48, v48, v68\n"
    "v_mul_f32_e32 v49, v49, v68\n"
    "v_mul_f32_e32 v50, v50, v68\n"
    "v_mul_f32_e32 v51, v51, v68\n"
    "v_cvt_pk_bf16_f32 v48, v48, v49\n"
    "v_cvt_pk_bf16_f32 v49, v50, v51\n"
    "s_branch .Lat_noj2\n"
    ".Lat_bin:\n"
    "s_movk_i32 s45, -640\n"
    "s_movk_i32 s48, 80\n"
    "v_mov_b32_e32 v60, 1\n"
    "v_cmp_gt_u32_e32 vcc, s59, v111\n"
    "v_cmp_gt_i32_e64 s[46:47], s61, v111\n"
    "s_waitcnt vmcnt(9)\n"
    "v_mul_u32_u24_e32 v48, 0x6667, v96\n"
    "v_mul_u32_u24_e32 v49, 0x6667, v97\n"
    "v_mul_u32_u24_e32 v50, 0x6667, v98\n"
    "v_mul_u32_u24_e32 v51, 0x6667, v99\n"
    "v_mul_u32_u24_e32 v52, 0x6667, v92\n"
    "v_mul_u32_u24_e32 v53, 0x6667, v93\n"
    "v_lshrrev_b32_e32 v48, 24, v48\n"
    "v_lshrrev_b32_e32 v49, 24, v49\n"
    "v_lshrrev_b32_e32 v50, 24, v50\n"
    "v_lshrrev_b32_e32 v51, 24, v51\n"
    "v_lshrrev_b32_e32 v52, 24, v52\n"
    "v_lshrrev_b32_e32 v53, 24, v53\n"
    "v_min_u32_e32 v48, 15, v48\n"
    "v_min_u32_e32 v49, 15, v49\n"
    "v_min_u32_e32 v50, 15, v50\n"
    "v_min_u32_e32 v51, 15, v51\n"
    "v_min_u32_e32 v52, 15, v52\n"
    "v_min_u32_e32 v53, 15, v53\n"
    "v_lshl_add_u32 v54, v48, 2, s44\n"
    "v_lshl_add_u32 v55, v49, 2, s44\n"
    "v_lshl_add_u32 v56, v50, 2, s44\n"
    "v_lshl_add_u32 v57, v51, 2, s44\n"
    "v_lshl_add_u32 v58, v52, 2, s44\n"
    "v_lshl_add_u32 v59, v53, 2, s44\n"
    "s_mov_b64 exec, vcc\n"
    "ds_add_rtn_u32 v54, v54, v60 offset:5248\n"
    "ds_add_rtn_u32 v55, v55, v60 offset:5248\n"
    "ds_add_rtn_u32 v56, v56, v60 offset:5248\n"
    "ds_add_rtn_u32 v57, v57, v60 offset:5248\n"
    "s_mov_b64 exec, s[46:47]\n"
    "ds_add_rtn_u32 v58, v58, v60 offset:5248\n"
    "ds_add_rtn_u32 v59, v59, v60 offset:5248\n"
    "s_mov_b64 exec, -1\n"
    "s_waitcnt vmcnt(7)\n"
    "v_mad_i32_i24 v96, v48, s45, v96\n"
    "v_mad_i32_i24 v97, v49, s45, v97\n"
    "v_mad_i32_i24 v98, v50, s45, v98\n"
    "v_mad_i32_i24 v99, v51, s45, v99\n"
    "v_mad_i32_i24 v92, v52, s45, v92\n"
    "v_mad_i32_i24 v93, v53, s45, v93\n"
    "v_lshl_or_b32 v96, v96, 16, v100\n"
    "v_lshl_or_b32 v97, v97, 16, v101\n"
    "v_lshl_or_b32 v98, v98, 16, v102\n"
    "v_lshl_or_b32 v99, v99, 16, v103\n"
    "v_lshl_or_b32 v92, v92, 16, v94\n"
    "v_lshl_or_b32 v93, v93, 16, v95\n"
    "s_mov_b64 exec, vcc\n"
    "s_waitcnt lgkmcnt(0)\n"
    "v_min_i32_e32 v54, 78, v54\n"
    "v_min_i32_e32 v55, 78, v55\n"
    "v_min_i32_e32 v56, 78, v56\n"
    "v_min_i32_e32 v57, 78, v57\n"
    "v_mad_u32_u24 v48, v48, s48, v54\n"
    "v_mad_u32_u24 v49, v49, s48, v55\n"
    "v_mad_u32_u24 v50, v50, s48, v56\n"
    "v_mad_u32_u24 v51, v51, s48, v57\n"
    "v_lshl_add_u32 v48, v48, 2, s44\n"
    "v_lshl_add_u32 v49, v49, 2, s44\n"
    "v_lshl_add_u32 v50, v50, 2, s44\n"
    "v_lshl_add_u32 v51, v51, 2, s44\n"
    "ds_write_b32 v48, v96 offset:4\n"
    "ds_write_b32 v49, v97 offset:4\n"
    "ds_write_b32 v50, v98 offset:4\n"
    "ds_write_b32 v51, v99 offset:4\n"
    "s_mov_b64 exec, s[46:47]\n"
    "v_min_i32_e32 v58, 78, v58\n"
    "v_min_i32_e32 v59, 78, v59\n"
    "v_mad_u32_u24 v52, v52, s48, v58\n"
    "v_mad_u32_u24 v53, v53, s48, v59\n"
    "v_lshl_add_u32 v52, v52, 2, s44\n"
    "v_lshl_add_u32 v53, v53, 2, s44\n"
    "ds_write_b32 v52, v92 offset:4\n"
    "ds_write_b32 v53, v93 offset:4\n"
    "s_mov_b64 exec, -1\n"
    ".Lat_noj2:\n"
    "buffer_store_dwordx2 v[48:49], v74, s[8:11], s52 offen offset:0 sc1\n"
    "v_mov_b32_e32 v112, s39\n"
    "v_mov_b32_e32 v113, 0\n"
    "s_waitcnt vmcnt(2)\n"
    "v_dot2_f32_bf16 v24, v0, s58, 0\n"
    "v_dot2_f32_bf16 v32, v8, s58, 0\n"
    "v_dot2_f32_bf16 v40, v16, s58, 0\n"
    "v_dot2_f32_bf16 v25, v4, s58, 0\n"
    "v_dot2_f32_bf16 v33, v12, s58, 0\n"
    "v_dot2_f32_bf16 v41, v20, s58, 0\n"
    "v_dot2_f32_bf16 v26, v0, v0, 0\n"
    "v_dot2_f32_bf16 v34, v8, v8, 0\n"
    "v_dot2_f32_bf16 v42, v16, v16, 0\n"
    "v_dot2_f32_bf16 v27, v4, v4, 0\n"
    "v_dot2_f32_bf16 v35, v12, v12, 0\n"
    "v_dot2_f32_bf16 v43, v20, v20, 0\n"
    "v_dot2_f32_bf16 v24, v1, s58, v24\n"
    "v_dot2_f32_bf16 v32, v9, s58, v32\n"
    "v_dot2_f32_bf16 v40, v17, s58, v40\n"
    "v_dot2_f32_bf16 v25, v5, s58, v25\n"
    "v_dot2_f32_bf16 v33, v13, s58, v33\n"
    "v_dot2_f32_bf16 v41, v21, s58, v41\n"
    "v_dot2_f32_bf16 v26, v1, v1, v26\n"
    "v_dot2_f32_bf16 v34, v9, v9, v34\n"
    "v_dot2_f32_bf16 v42, v17, v17, v42\n"
    "v_dot2_f32_bf16 v27, v5, v5, v27\n"
    "v_dot2_f32_bf16 v35, v13, v13, v35\n"
    "v_dot2_f32_bf16 v43, v21, v21, v43\n"
    "v_dot2_f32_bf16 v24, v2, s58, v24\n"
    "v_dot2_f32_bf16 v32, v10, s58, v32\n"
    "v_dot2_f32_bf16 v40, v18, s58, v40\n"
    "v_dot2_f32_bf16 v25, v6, s58, v25\n"
    "v_dot2_f32_bf16 v33, v14, s58, v33\n"
    "v_dot2_f32_bf16 v41, v22, s58, v41\n"
    "v_dot2_f32_bf16 v26, v2, v2, v26\n"
    "v_dot2_f32_bf16 v34, v10, v10, v34\n"
    "v_dot2_f32_bf16 v42, v18, v18, v42\n"
    "v_dot2_f32_bf16 v27, v6, v6, v27\n"
    "v_dot2_f32_bf16 v35, v14, v14, v35\n"
    "v_dot2_f32_bf16 v43, v22, v22, v43\n"
    "v_dot2_f32_bf16 v24, v3, s58, v24\n"
    "v_dot2_f32_bf16 v32, v11, s58, v32\n"
    "v_dot2_f32_bf16 v40, v19, s58, v40\n"
    "v_dot2_f32_bf16 v25, v7, s58, v25\n"
    "v_dot2_f32_bf16 v33, v15, s58, v33\n"
    "v_dot2_f32_bf16 v41, v23, s58, v41\n"
    "v_dot2_f32_bf16 v26, v3, v3, v26\n"
    "v_dot2_f32_bf16 v34, v11, v11, v34\n"
    "v_dot2_f32_bf16 v42, v19, v19, v42\n"
    "v_dot2_f32_bf16 v27, v7, v7, v27\n"
    "v_dot2_f32_bf16 v35, v15, v15, v35\n"
    "v_dot2_f32_bf16 v43, v23, v23, v43\n"
    "v_and_b32_e32 v30, 0xffff0000, v7\n"
    "v_and_b32_e32 v38, 0xffff0000, v15\n"
    "v_and_b32_e32 v46, 0xffff0000, v23\n"
    "v_add_f32_e32 v24, v24, v25\n"
    "v_add_f32_e32 v32, v32, v33\n"
    "v_add_f32_e32 v40, v40, v41\n"
    "v_add_f32_e32 v26, v26, v27\n"
    "v_add_f32_e32 v34, v34, v35\n"
    "v_add_f32_e32 v42, v42, v43\n"
    "v_add_f32_dpp v25, v25, v25 quad_perm:[1,0,3,2] row_mask:0xf bank_mask:0xf bound_ctrl:1\n"
    "v_lshlrev_b32_e32 v48, 16, v0\n"
    "v_add_f32_dpp v33, v33, v33 quad_perm:[1,0,3,2] row_mask:0xf bank_mask:0xf bound_ctrl:1\n"
    "v_and_b32_e32 v49, 0xffff0000, v0\n"
    "v_add_f32_dpp v41, v41, v41 quad_perm:[1,0,3,2] row_mask:0xf bank_mask:0xf bound_ctrl:1\n"
    "v_lshlrev_b32_e32 v50, 16, v1\n"
    "v_mov_b32_dpp v30, v30 quad_perm:[3,3,3,3] row_mask:0xf bank_mask:0xf bound_ctrl:1\n"
    "v_and_b32_e32 v51, 0xffff0000, v1\n"
    "v_mov_b32_dpp v38, v38 quad_perm:[3,3,3,3] row_mask:0xf bank_mask:0xf bound_ctrl:1\n"
    "v_lshlrev_b32_e32 v52, 16, v2\n"
    "v_mov_b32_dpp v46, v46 quad_perm:[3,3,3,3] row_mask:0xf bank_mask:0xf bound_ctrl:1\n"
    "v_and_b32_e32 v53, 0xffff0000, v2\n"
    "v_add_f32_dpp v24, v24, v24 quad_perm:[1,0,3,2] row_mask:0xf bank_mask:0xf bound_ctrl:1\n"
    "v_lshlrev_b32_e32 v54, 16, v3\n"
    "v_add_f32_dpp v32, v32, v32 quad_perm:[1,0,3,2] row_mask:0xf bank_mask:0xf bound_ctrl:1\n"
    "v_and_b32_e32 v55, 0xffff0000, v3\n"
    "v_add_f32_dpp v40, v40, v40 quad_perm:[1,0,3,2] row_mask:0xf bank_mask:0xf bound_ctrl:1\n"
    "v_lshlrev_b32_e32 v56, 16, v4\n"
    "v_add_f32_dpp v26, v26, v26 quad_perm:[1,0,3,2] row_mask:0xf bank_mask:0xf bound_ctrl:1\n"
    "v_and_b32_e32 v57, 0xffff0000, v4\n"
    "v_add_f32_dpp v34, v34, v34 quad_perm:[1,0,3,2] row_mask:0xf bank_mask:0xf bound_ctrl:1\n"
    "v_lshlrev_b32_e32 v58, 16, v5\n"
    "s_waitcnt vmcnt(0)\n"
    "buffer_store_dword v112, v113, s[8:11], s53 offen offset:0 sc1\n"
    "v_add_f32_dpp v42, v42, v42 quad_perm:[1,0,3,2] row_mask:0xf bank_mask:0xf bound_ctrl:1\n"
    "v_and_b32_e32 v59, 0xffff0000, v5\n"
    "v_add_f32_dpp v24, v24, v24 quad_perm:[2,3,0,1] row_mask:0xf bank_mask:0xf bound_ctrl:1\n"
    "v_lshlrev_b32_e32 v60, 16, v6\n"
    "v_add_f32_dpp v32, v32, v32 quad_perm:[2,3,0,1] row_mask:0xf bank_mask:0xf bound_ctrl:1\n"
    "v_and_b32_e32 v61, 0xffff0000, v6\n"
    "s_waitcnt lgkmcnt(0)\n"
    "s_barrier\n"
    "ds_read_b128 v[96:99], v108 offset:0\n"
    "ds_read_b128 v[100:103], v108 offset:4096\n"
    "s_lshl_b32 s45, %1, 6\n"
    "s_add_u32 s45, s45, 35038208\n"
    "v_add_f32_dpp v40, v40, v40 quad_perm:[2,3,0,1] row_mask:0xf bank_mask:0xf bound_ctrl:1\n"
    "v_lshlrev_b32_e32 v62, 16, v7\n"
    "v_add_f32_dpp v26, v26, v26 quad_perm:[2,3,0,1] row_mask:0xf bank_mask:0xf bound_ctrl:1\n"
    "v_and_b32_e32 v63, 0xffff0000, v7\n"
    "v_add_f32_dpp v34, v34, v34 quad_perm:[2,3,0,1] row_mask:0xf bank_mask:0xf bound_ctrl:1\n"
    "v_lshlrev_b32_e32 v64, 16, v8\n"
    "v_add_f32_dpp v42, v42, v42 quad_perm:[2,3,0,1] row_mask:0xf bank_mask:0xf bound_ctrl:1\n"
    "v_and_b32_e32 v65, 0xffff0000, v8\n"
    "v_add_f32_dpp v24, v24, v24 row_half_mirror row_mask:0xf bank_mask:0xf bound_ctrl:1\n"
    "v_lshlrev_b32_e32 v66, 16, v9\n"
    "v_add_f32_dpp v32, v32, v32 row_half_mirror row_mask:0xf bank_mask:0xf bound_ctrl:1\n"
    "v_and_b32_e32 v67, 0xffff0000, v9\n"
    "v_add_f32_dpp v40, v40, v40 row_half_mirror row_mask:0xf bank_mask:0xf bound_ctrl:1\n"
    "v_lshlrev_b32_e32 v68, 16, v10\n"
    "v_add_f32_dpp v26, v26, v26 row_half_mirror row_mask:0xf bank_mask:0xf bound_ctrl:1\n"
    "v_and_b32_e32 v69, 0xffff0000, v10\n"
    "v_add_f32_dpp v34, v34, v34 row_half_mirror row_mask:0xf bank_mask:0xf bound_ctrl:1\n"
    "v_lshlrev_b32_e32 v70, 16, v11\n"
    "v_add_f32_dpp v42, v42, v42 row_half_mirror row_mask:0xf bank_mask:0xf bound_ctrl:1\n"
    "v_and_b32_e32 v71, 0xffff0000, v11\n"
    "v_add_f32_dpp v24, v24, v24 row_mirror row_mask:0xf bank_mask:0xf bound_ctrl:1\n"
    "v_lshlrev_b32_e32 v72, 16, v12\n"
    "v_add_f32_dpp v32, v32, v32 row_mirror row_mask:0xf bank_mask:0xf bound_ctrl:1\n"
    "v_and_b32_e32 v73, 0xffff0000, v12\n"
    "v_add_f32_dpp v40, v40, v40 row_mirror row_mask:0xf bank_mask:0xf bound_ctrl:1\n"
    "v_lshlrev_b32_e32 v74, 16, v13\n"
    "v_add_f32_dpp v26, v26, v26 row_mirror row_mask:0xf bank_mask:0xf bound_ctrl:1\n"
    "v_and_b32_e32 v75, 0xffff0000, v13\n"
    "v_add_f32_dpp v34, v34, v34 row_mirror row_mask:0xf bank_mask:0xf bound_ctrl:1\n"
    "v_lshlrev_b32_e32 v76, 16, v14\n"
    "s_waitcnt lgkmcnt(0)\n"
    "buffer_store_dwordx4 v[96:99], v109, s[8:11], s45 offen offset:0 sc1\n"
    "buffer_store_dwordx4 v[100:103], v110, s[8:11], s45 offen offset:0 sc1\n"
    "v_add_f32_dpp v42, v42, v42 row_mirror row_mask:0xf bank_mask:0xf bound_ctrl:1\n"
    "v_and_b32_e32 v77, 0xffff0000, v14\n"
    "v_mul_f32_e32 v24, 0x3b800000, v24\n"
    "v_lshlrev_b32_e32 v78, 16, v15\n"
    "v_mul_f32_e32 v32, 0x3b800000, v32\n"
    "v_and_b32_e32 v79, 0xffff0000, v15\n"
    "v_mul_f32_e32 v40, 0x3b800000, v40\n"
    "v_lshlrev_b32_e32 v80, 16, v16\n"
    "v_mul_f32_e32 v25, 0.25, v25\n"
    "v_and_b32_e32 v81, 0xffff0000, v16\n"
    "v_mul_f32_e32 v33, 0.25, v33\n"
    "v_lshlrev_b32_e32 v82, 16, v17\n"
    "v_mul_f32_e32 v41, 0.25, v41\n"
    "v_and_b32_e32 v83, 0xffff0000, v17\n"
    "v_mul_f32_e64 v27, v24, -v24\n"
    "v_lshlrev_b32_e32 v84, 16, v18\n"
    "v_mul_f32_e64 v35, v32, -v32\n"
    "v_and_b32_e32 v85, 0xffff0000, v18\n"
    "v_mul_f32_e64 v43, v40, -v40\n"
    "v_lshlrev_b32_e32 v86, 16, v19\n"
    "v_sub_f32_e32 v25, v25, v30\n"
    "v_and_b32_e32 v87, 0xffff0000, v19\n"
    "v_sub_f32_e32 v33, v33, v38\n"
    "v_lshlrev_b32_e32 v88, 16, v20\n"
    "v_sub_f32_e32 v41, v41, v46\n"
    "v_and_b32_e32 v89, 0xffff0000, v20\n"
    "v_fmac_f32_e32 v27, 0x3b800000, v26\n"
    "v_lshlrev_b32_e32 v90, 16, v21\n"
    "v_fmac_f32_e32 v35, 0x3b800000, v34\n"
    "v_and_b32_e32 v91, 0xffff0000, v21\n"
    "v_fmac_f32_e32 v43, 0x3b800000, v42\n"
    "v_lshlrev_b32_e32 v92, 16, v22\n"
    "v_mul_f32_e32 v25, 0x3eaaaaab, v25\n"
    "v_and_b32_e32 v93, 0xffff0000, v22\n"
    "v_mul_f32_e32 v33, 0x3eaaaaab, v33\n"
    "v_lshlrev_b32_e32 v94, 16, v23\n"
    "v_mul_f32_e32 v41, 0x3eaaaaab, v41\n"
    "v_and_b32_e32 v95, 0xffff0000, v23\n"
    "v_max_f32_e32 v27, 0, v27\n"
    "v_max_f32_e32 v35, 0, v35\n"
    "v_max_f32_e32 v43, 0, v43\n"
    "v_cndmask_b32_e64 v25, v24, v25, s[62:63]\n"
    "v_cndmask_b32_e64 v33, v32, v33, s[62:63]\n"
    "v_cndmask_b32_e64 v41, v40, v41, s[62:63]\n"
    "v_add_f32_e32 v27, 0x3727c5ac, v27\n"
    "v_add_f32_e32 v35, 0x3727c5ac, v35\n"
    "v_add_f32_e32 v43, 0x3727c5ac, v43\n"
    "v_rsq_f32_e32 v28, v27\n"
    "v_rsq_f32_e32 v36, v35\n"
    "v_rsq_f32_e32 v44, v43\n"
    "v_fma_f32 v29, -v24, v28, s57\n"
    "v_fma_f32 v37, -v32, v36, s57\n"
    "v_fma_f32 v45, -v40, v44, s57\n"
    "v_fma_f32 v31, -v25, v28, s57\n"
    "v_fma_f32 v39, -v33, v36, s57\n"
    "v_fma_f32 v47, -v41, v44, s57\n"
    "v_sub_f32_e32 v24, v29, v31\n"
    "v_sub_f32_e32 v32, v37, v39\n"
    "v_sub_f32_e32 v40, v45, v47\n"
    "v_pk_fma_f32 v[48:49], v[48:49], v[28:29], v[28:29] op_sel:[0,0,1] op_sel_hi:[1,0,1]\n"
    "v_pk_fma_f32 v[50:51], v[50:51], v[28:29], v[28:29] op_sel:[0,0,1] op_sel_hi:[1,0,1]\n"
    "v_pk_fma_f32 v[52:53], v[52:53], v[28:29], v[28:29] op_sel:[0,0,1] op_sel_hi:[1,0,1]\n"
    "v_pk_fma_f32 v[54:55], v[54:55], v[28:29], v[28:29] op_sel:[0,0,1] op_sel_hi:[1,0,1]\n"
    "v_pk_fma_f32 v[56:57], v[56:57], v[28:29], v[30:31] op_sel:[0,0,1] op_sel_hi:[1,0,1]\n"
    "v_pk_fma_f32 v[58:59], v[58:59], v[28:29], v[30:31] op_sel:[0,0,1] op_sel_hi:[1,0,1]\n"
    "v_pk_fma_f32 v[60:61], v[60:61], v[28:29], v[30:31] op_sel:[0,0,1] op_sel_hi:[1,0,1]\n"
    "v_pk_fma_f32 v[62:63], v[62:63], v[28:29], v[30:31] op_sel:[0,0,1] op_sel_hi:[1,0,1]\n"
    "v_pk_add_f32 v[48:49], v[48:49], s[56:57] op_sel_hi:[1,0]\n"
    "v_pk_add_f32 v[50:51], v[50:51], s[56:57] op_sel_hi:[1,0]\n"
    "v_pk_add_f32 v[52:53], v[52:53], s[56:57] op_sel_hi:[1,0]\n"
    "v_pk_add_f32 v[54:55], v[54:55], s[56:57] op_sel_hi:[1,0]\n"
    "v_pk_add_f32 v[56:57], v[56:57], s[56:57] op_sel_hi:[1,0]\n"
    "v_pk_add_f32 v[58:59], v[58:59], s[56:57] op_sel_hi:[1,0]\n"
    "v_pk_add_f32 v[60:61], v[60:61], s[56:57] op_sel_hi:[1,0]\n"
    "v_pk_add_f32 v[62:63], v[62:63], s[56:57] op_sel_hi:[1,0]\n"
    "v_cvt_pk_bf16_f32 v0, v48, v49\n"
    "v_cvt_pk_bf16_f32 v1, v50, v51\n"
    "v_cvt_pk_bf16_f32 v2, v52, v53\n"
    "v_cvt_pk_bf16_f32 v3, v54, v55\n"
    "v_cvt_pk_bf16_f32 v4, v56, v57\n"
    "v_cvt_pk_bf16_f32 v5, v58, v59\n"
    "v_cvt_pk_bf16_f32 v6, v60, v61\n"
    "v_cvt_pk_bf16_f32 v7, v62, v63\n"
    "buffer_store_dwordx4 v[0:3], v104, s[8:11], s35 offen offset:0 sc1\n"
    "v_bfi_b32 v7, v107, v7, 1.0\n"
    "ds_write_b128 v105, v[0:3] offset:0\n"
    "buffer_store_dwordx4 v[4:7], v104, s[8:11], s35 offen offset:256 sc1\n"
    "v_pk_fma_f32 v[64:65], v[64:65], v[36:37], v[36:37] op_sel:[0,0,1] op_sel_hi:[1,0,1]\n"
    "v_pk_fma_f32 v[66:67], v[66:67], v[36:37], v[36:37] op_sel:[0,0,1] op_sel_hi:[1,0,1]\n"
    "v_pk_fma_f32 v[68:69], v[68:69], v[36:37], v[36:37] op_sel:[0,0,1] op_sel_hi:[1,0,1]\n"
    "v_pk_fma_f32 v[70:71], v[70:71], v[36:37], v[36:37] op_sel:[0,0,1] op_sel_hi:[1,0,1]\n"
    "v_pk_fma_f32 v[72:73], v[72:73], v[36:37], v[38:39] op_sel:[0,0,1] op_sel_hi:[1,0,1]\n"
    "v_pk_fma_f32 v[74:75], v[74:75], v[36:37], v[38:39] op_sel:[0,0,1] op_sel_hi:[1,0,1]\n"
    "v_pk_fma_f32 v[76:77], v[76:77], v[36:37], v[38:39] op_sel:[0,0,1] op_sel_hi:[1,0,1]\n"
    "v_pk_fma_f32 v[78:79], v[78:79], v[36:37], v[38:39] op_sel:[0,0,1] op_sel_hi:[1,0,1]\n"
    "v_pk_add_f32 v[64:65], v[64:65], s[56:57] op_sel_hi:[1,0]\n"
    "v_pk_add_f32 v[66:67], v[66:67], s[56:57] op_sel_hi:[1,0]\n"
    "v_pk_add_f32 v[68:69], v[68:69], s[56:57] op_sel_hi:[1,0]\n"
    "v_pk_add_f32 v[70:71], v[70:71], s[56:57] op_sel_hi:[1,0]\n"
    "v_pk_add_f32 v[72:73], v[72:73], s[56:57] op_sel_hi:[1,0]\n"
    "v_pk_add_f32 v[74:75], v[74:75], s[56:57] op_sel_hi:[1,0]\n"
    "v_pk_add_f32 v[76:77], v[76:77], s[56:57] op_sel_hi:[1,0]\n"
    "v_pk_add_f32 v[78:79], v[78:79], s[56:57] op_sel_hi:[1,0]\n"
    "v_cvt_pk_bf16_f32 v8, v64, v65\n"
    "v_cvt_pk_bf16_f32 v9, v66, v67\n"
    "v_cvt_pk_bf16_f32 v10, v68, v69\n"
    "v_cvt_pk_bf16_f32 v11, v70, v71\n"
    "v_cvt_pk_bf16_f32 v12, v72, v73\n"
    "v_cvt_pk_bf16_f32 v13, v74, v75\n"
    "v_cvt_pk_bf16_f32 v14, v76, v77\n"
    "v_cvt_pk_bf16_f32 v15, v78, v79\n"
    "buffer_store_dwordx4 v[8:11], v104, s[8:11], s54 offen offset:0 sc1\n"
    "v_bfi_b32 v15, v107, v15, 1.0\n"
    "ds_write_b128 v105, v[8:11] offset:8192\n"
    "buffer_store_dwordx4 v[12:15], v104, s[8:11], s54 offen offset:256 sc1\n"
    "v_pk_fma_f32 v[80:81], v[80:81], v[44:45], v[44:45] op_sel:[0,0,1] op_sel_hi:[1,0,1]\n"
    "v_pk_fma_f32 v[82:83], v[82:83], v[44:45], v[44:45] op_sel:[0,0,1] op_sel_hi:[1,0,1]\n"
    "v_pk_fma_f32 v[84:85], v[84:85], v[44:45], v[44:45] op_sel:[0,0,1] op_sel_hi:[1,0,1]\n"
    "v_pk_fma_f32 v[86:87], v[86:87], v[44:45], v[44:45] op_sel:[0,0,1] op_sel_hi:[1,0,1]\n"
    "v_pk_fma_f32 v[88:89], v[88:89], v[44:45], v[46:47] op_sel:[0,0,1] op_sel_hi:[1,0,1]\n"
    "v_pk_fma_f32 v[90:91], v[90:91], v[44:45], v[46:47] op_sel:[0,0,1] op_sel_hi:[1,0,1]\n"
    "v_pk_fma_f32 v[92:93], v[92:93], v[44:45], v[46:47] op_sel:[0,0,1] op_sel_hi:[1,0,1]\n"
    "v_pk_fma_f32 v[94:95], v[94:95], v[44:45], v[46:47] op_sel:[0,0,1] op_sel_hi:[1,0,1]\n"
    "v_pk_add_f32 v[80:81], v[80:81], s[56:57] op_sel_hi:[1,0]\n"
    "v_pk_add_f32 v[82:83], v[82:83], s[56:57] op_sel_hi:[1,0]\n"
    "v_pk_add_f32 v[84:85], v[84:85], s[56:57] op_sel_hi:[1,0]\n"
    "v_pk_add_f32 v[86:87], v[86:87], s[56:57] op_sel_hi:[1,0]\n"
    "v_pk_add_f32 v[88:89], v[88:89], s[56:57] op_sel_hi:[1,0]\n"
    "v_pk_add_f32 v[90:91], v[90:91], s[56:57] op_sel_hi:[1,0]\n"
    "v_pk_add_f32 v[92:93], v[92:93], s[56:57] op_sel_hi:[1,0]\n"
    "v_pk_add_f32 v[94:95], v[94:95], s[56:57] op_sel_hi:[1,0]\n"
    "v_cvt_pk_bf16_f32 v16, v80, v81\n"
    "v_cvt_pk_bf16_f32 v17, v82, v83\n"
    "v_cvt_pk_bf16_f32 v18, v84, v85\n"
    "v_cvt_pk_bf16_f32 v19, v86, v87\n"
    "v_cvt_pk_bf16_f32 v20, v88, v89\n"
    "v_cvt_pk_bf16_f32 v21, v90, v91\n"
    "v_cvt_pk_bf16_f32 v22, v92, v93\n"
    "v_cvt_pk_bf16_f32 v23, v94, v95\n"
    "buffer_store_dwordx4 v[16:19], v104, s[8:11], s60 offen offset:0 sc1\n"
    "v_bfi_b32 v23, v107, v23, 1.0\n"
    "ds_write_b128 v105, v[16:19] offset:16384\n"
    "buffer_store_dwordx4 v[20:23], v104, s[8:11], s60 offen offset:256 sc1\n"
    "v_pk_add_f32 v[56:57], v[56:57], v[24:25] op_sel_hi:[1,0]\n"
    "v_pk_add_f32 v[58:59], v[58:59], v[24:25] op_sel_hi:[1,0]\n"
    "v_pk_add_f32 v[60:61], v[60:61], v[24:25] op_sel_hi:[1,0]\n"
    "v_pk_add_f32 v[62:63], v[62:63], v[24:25] op_sel_hi:[1,0]\n"
    "v_cvt_pk_bf16_f32 v56, v56, v57\n"
    "v_cvt_pk_bf16_f32 v57, v58, v59\n"
    "v_cvt_pk_bf16_f32 v58, v60, v61\n"
    "v_cvt_pk_bf16_f32 v59, v62, v63\n"
    "ds_write_b128 v106, v[56:59] offset:0\n"
    "v_pk_add_f32 v[72:73], v[72:73], v[32:33] op_sel_hi:[1,0]\n"
    "v_pk_add_f32 v[74:75], v[74:75], v[32:33] op_sel_hi:[1,0]\n"
    "v_pk_add_f32 v[76:77], v[76:77], v[32:33] op_sel_hi:[1,0]\n"
    "v_pk_add_f32 v[78:79], v[78:79], v[32:33] op_sel_hi:[1,0]\n"
    "v_cvt_pk_bf16_f32 v72, v72, v73\n"
    "v_cvt_pk_bf16_f32 v73, v74, v75\n"
    "v_cvt_pk_bf16_f32 v74, v76, v77\n"
    "v_cvt_pk_bf16_f32 v75, v78, v79\n"
    "ds_write_b128 v106, v[72:75] offset:8192\n"
    "v_pk_add_f32 v[88:89], v[88:89], v[40:41] op_sel_hi:[1,0]\n"
    "v_pk_add_f32 v[90:91], v[90:91], v[40:41] op_sel_hi:[1,0]\n"
    "v_pk_add_f32 v[92:93], v[92:93], v[40:41] op_sel_hi:[1,0]\n"
    "v_pk_add_f32 v[94:95], v[94:95], v[40:41] op_sel_hi:[1,0]\n"
    "v_cvt_pk_bf16_f32 v88, v88, v89\n"
    "v_cvt_pk_bf16_f32 v89, v90, v91\n"
    "v_cvt_pk_bf16_f32 v90, v92, v93\n"
    "v_cvt_pk_bf16_f32 v91, v94, v95\n"
    "ds_write_b128 v106, v[88:91] offset:16384\n"
    "s_waitcnt lgkmcnt(0)\n"
    "s_barrier\n"
    "ds_read_b128 a[0:3], v250 offset:0\n"
    "ds_read_b128 a[32:35], v250 offset:8192\n"
    "ds_read_b128 a[64:67], v250 offset:16384\n"
    "ds_read_b128 a[4:7], v251 offset:0\n"
    "ds_read_b128 a[36:39], v251 offset:8192\n"
    "ds_read_b128 a[68:71], v251 offset:16384\n"
    "ds_read_b128 a[8:11], v250 offset:2048\n"
    "ds_read_b128 a[40:43], v250 offset:10240\n"
    "ds_read_b128 a[72:75], v250 offset:18432\n"
    "ds_read_b128 a[12:15], v251 offset:2048\n"
    "ds_read_b128 a[44:47], v251 offset:10240\n"
    "ds_read_b128 a[76:79], v251 offset:18432\n"
    "buffer_load_dword v255, v252, s[8:11], 0 offen offset:0 sc1\n"
    "ds_read_b128 a[16:19], v250 offset:4096\n"
    "ds_read_b128 a[48:51], v250 offset:12288\n"
    "ds_read_b128 a[80:83], v250 offset:20480\n"
    "ds_read_b128 a[20:23], v251 offset:4096\n"
    "ds_read_b128 a[52:55], v251 offset:12288\n"
    "ds_read_b128 a[84:87], v251 offset:20480\n"
    "ds_read_b128 a[24:27], v250 offset:6144\n"
    "ds_read_b128 a[56:59], v250 offset:14336\n"
    "ds_read_b128 a[88:91], v250 offset:22528\n"
    "ds_read_b128 a[28:31], v251 offset:6144\n"
    "ds_read_b128 a[60:63], v251 offset:14336\n"
    "ds_read_b128 a[92:95], v251 offset:22528\n"
    "s_lshl_b32 s52, %2, 15\n"
    "s_add_u32 s52, s52, 5242880\n"
    "s_lshl_b32 s61, s37, 12\n"
    "s_lshl_b32 s46, %1, 4\n"
    "s_lshl_b32 s47, %1, 7\n"
    "s_add_u32 s47, s47, 4096\n"
    "s_cmp_eq_u32 s50, 0\n"
    "s_cselect_b32 s46, s46, s47\n"
    "s_add_u32 s46, s46, 5701632\n"
    "s_cmp_eq_u32 s51, 0\n"
    "s_cselect_b32 s46, s46, 2.0\n"
    "v_mov_b32_e32 v40, s39\n"
    "v_mov_b32_e32 v41, 0\n"
    "s_waitcnt vmcnt(1)\n"
    "s_waitcnt lgkmcnt(0)\n"
    "s_barrier\n"
    "buffer_store_dword v40, v41, s[8:11], s46 offen offset:0 sc1\n"
    "s_waitcnt vmcnt(1)\n"
    "v_cmp_ne_u32_e32 vcc, s39, v255\n"
    "s_nop 4\n"
    "s_cbranch_vccnz .Lat_apoll\n"
    ".Lat_apoll_ret:\n"
    "s_add_u32 s47, s52, s61\n"
    "buffer_load_dwordx4 v[112:115], v253, s[8:11], s47 offen offset:0 sc1\n"
    "buffer_load_dwordx4 v[116:119], v253, s[8:11], s47 offen offset:1024 sc1\n"
    "buffer_load_dwordx4 v[128:131], v253, s[8:11], s47 offen offset:2048 sc1\n"
    "s_add_u32 s61, s61, 0x1000\n"
    "buffer_load_dwordx4 v[132:135], v253, s[8:11], s47 offen offset:3072 sc1\n"
    "s_bitset0_b32 s61, 14\n"
    "s_add_u32 s47, s52, s61\n"
    "buffer_load_dwordx4 v[144:147], v253, s[8:11], s47 offen offset:0 sc1\n"
    "buffer_load_dwordx4 v[148:151], v253, s[8:11], s47 offen offset:1024 sc1\n"
    "buffer_load_dwordx4 v[160:163], v253, s[8:11], s47 offen offset:2048 sc1\n"
    "s_add_u32 s61, s61, 0x1000\n"
    "buffer_load_dwordx4 v[164:167], v253, s[8:11], s47 offen offset:3072 sc1\n"
    "s_bitset0_b32 s61, 14\n"
    "s_add_u32 s47, s52, s61\n"
    "buffer_load_dwordx4 v[176:179], v253, s[8:11], s47 offen offset:0 sc1\n"
    "buffer_load_dwordx4 v[180:183], v253, s[8:11], s47 offen offset:1024 sc1\n"
    "buffer_load_dwordx4 v[192:195], v253, s[8:11], s47 offen offset:2048 sc1\n"
    "s_add_u32 s61, s61, 0x1000\n"
    "s_lshl_b32 s59, s32, 4\n"
    "s_lshr_b32 s48, s32, 1\n"
    "s_sub_u32 s59, s59, s48\n"
    "s_lshl_b32 s59, s59, 6\n"
    "s_sub_u32 s48, 3984, s59\n"
    "v_cmp_le_u32_e32 vcc, 0x40, v253\n"
    "v_mov_b32_e32 v0, s48\n"
    "s_cmp_eq_u32 s32, 2\n"
    "s_cselect_b32 s48, 1024, 0\n"
    "v_mov_b32_e32 v2, s48\n"
    "s_add_u32 s59, s59, 5701632\n"
    "v_cndmask_b32_e32 v3, v253, v0, vcc\n"
    "s_lshl_b32 s45, %2, 11\n"
    "s_add_u32 s48, s45, %3\n"
    "v_add_u32_e32 v9, s48, v253\n"
    "s_add_u32 s45, s45, 11022336\n"
    "v_add_u32_e32 v8, s45, v253\n"
    "v_lshrrev_b32_e32 v13, 7, v253\n"
    "v_and_b32_e32 v12, 0x70, v254\n"
    "v_lshl_add_u32 v12, v13, 9, v12\n"
    "v_add_u32_e32 v13, 0xc00, v12\n"
    "v_add_u32_e32 v14, 0x1800, v12\n"
    "v_add_u32_e32 v15, 0x2400, v12\n"
    "s_lshl_b32 s57, s50, 8\n"
    "s_add_u32 s57, s57, 5111808\n"
    "s_add_u32 s62, s43, s34\n"
    "s_xor_b32 s63, s34, 0xc000\n"
    "s_add_u32 s63, s63, s43\n"
    "s_waitcnt vmcnt(9)\n"
    "v_mfma_f32_16x16x32_bf16 v[24:27], v[112:115], a[0:3], 0\n"
    "buffer_load_dwordx4 v[196:199], v253, s[8:11], s47 offen offset:3072 sc1\n"
    "v_mfma_f32_16x16x32_bf16 v[28:31], v[116:119], a[0:3], 0\n"
    "s_bitset0_b32 s61, 14\n"
    "s_mov_b32 s18, 5120000\n"
    "s_mov_b32 s19, 0x20000\n"
    "v_mfma_f32_16x16x32_bf16 v[48:51], v[112:115], a[32:35], 0\n"
    "s_add_u32 s47, s52, s61\n"
    "v_xor_b32_e32 v242, 0x12000, v240\n"
    "v_xor_b32_e32 v243, 0x12000, v241\n"
    "v_mfma_f32_16x16x32_bf16 v[52:55], v[116:119], a[32:35], 0\n"
    "buffer_load_dwordx4 v[208:211], v253, s[8:11], s47 offen offset:0 sc1\n"
    "v_mfma_f32_16x16x32_bf16 v[64:67], v[112:115], a[64:67], 0\n"
    "s_cmp_eq_u32 %2, 3\n"
    "s_cselect_b32 s88, 0x2ff, -1\n"
    "v_mov_b32_e32 v10, 0xc2980000\n"
    "v_mfma_f32_16x16x32_bf16 v[68:71], v[116:119], a[64:67], 0\n"
    "s_waitcnt vmcnt(9)\n"
    "v_cmp_lt_u32_e32 vcc, s88, v253\n"
    "s_nop 1\n"
    "v_cndmask_b32_e32 v10, 0, v10, vcc\n"
    "v_mfma_f32_16x16x32_bf16 v[24:27], v[128:131], a[4:7], v[24:27]\n"
    "buffer_load_dwordx4 v[212:215], v253, s[8:11], s47 offen offset:1024 sc1\n"
    "v_mfma_f32_16x16x32_bf16 v[28:31], v[132:135], a[4:7], v[28:31]\n"
    "s_add_u32 s52, s52, 0x4000\n"
    "v_lshrrev_b32_e32 v5, 4, v253\n"
    "v_min_u32_e32 v5, 2, v5\n"
    "v_mfma_f32_16x16x32_bf16 v[48:51], v[128:131], a[36:39], v[48:51]\n"
    "s_add_u32 s88, s32, 1\n"
    "v_add_u32_e32 v5, s88, v5\n"
    "v_mov_b32_e32 v80, 0\n"
    "v_mfma_f32_16x16x32_bf16 v[52:55], v[132:135], a[36:39], v[52:55]\n"
    "buffer_load_dwordx4 v[224:227], v253, s[8:11], s47 offen offset:2048 sc1\n"
    "v_mfma_f32_16x16x32_bf16 v[64:67], v[128:131], a[68:71], v[64:67]\n"
    "s_add_u32 s61, s61, 0x1000\n"
    "v_and_b32_e32 v5, 3, v5\n"
    "v_lshlrev_b32_e32 v5, 7, v5\n"
    "v_mfma_f32_16x16x32_bf16 v[68:71], v[132:135], a[68:71], v[68:71]\n"
    "s_waitcnt vmcnt(9)\n"
    "s_lshl_b32 s58, %1, 7\n"
    "s_add_u32 s58, s58, 5738432\n"
    "s_cmp_eq_u32 %2, 2\n"
    "s_cselect_b32 s58, s58, 2.0\n"
    "v_mfma_f32_16x16x32_bf16 v[24:27], v[144:147], a[8:11], v[24:27]\n"
    "buffer_load_dwordx4 v[228:231], v253, s[8:11], s47 offen offset:3072 sc1\n"
    "v_mfma_f32_16x16x32_bf16 v[28:31], v[148:151], a[8:11], v[28:31]\n"
    "s_bitset0_b32 s61, 14\n"
    "s_lshl_b32 s88, s50, 13\n"
    "s_add_u32 s88, s88, %3\n"
    "v_mfma_f32_16x16x32_bf16 v[48:51], v[144:147], a[40:43], v[48:51]\n"
    "s_add_u32 s47, s52, s61\n"
    "v_add_u32_e32 v6, s88, v253\n"
    "v_add_u32_e32 v7, %3, v253\n"
    "v_mfma_f32_16x16x32_bf16 v[52:55], v[148:151], a[40:43], v[52:55]\n"
    "buffer_load_dwordx4 v[120:123], v253, s[8:11], s47 offen offset:0 sc1\n"
    "v_mfma_f32_16x16x32_bf16 v[64:67], v[144:147], a[72:75], v[64:67]\n"
    "s_mul_i32 s88, %2, 1024\n"
    "s_add_u32 s88, s88, %3\n"
    "v_lshrrev_b32_e32 v250, 2, v253\n"
    "v_mfma_f32_16x16x32_bf16 v[68:71], v[148:151], a[72:75], v[68:71]\n"
    "s_waitcnt vmcnt(9)\n"
    "v_add_u32_e32 v249, s88, v250\n"
    "s_mov_b32 s49, s88\n"
    "v_mfma_f32_16x16x32_bf16 v[24:27], v[160:163], a[12:15], v[24:27]\n"
    "buffer_load_dwordx4 v[124:127], v253, s[8:11], s47 offen offset:1024 sc1\n"
    "v_mfma_f32_16x16x32_bf16 v[28:31], v[164:167], a[12:15], v[28:31]\n"
    "s_lshl_b32 s81, s88, 2\n"
    "s_lshl2_add_u32 s90, %1, %2\n"
    "s_lshl_b32 s90, s90, 10\n"
    "v_mfma_f32_16x16x32_bf16 v[48:51], v[160:163], a[44:47], v[48:51]\n"
    "s_sub_u32 s81, s90, s81\n"
    "s_add_u32 s81, s81, 36369408\n"
    "s_xor_b32 s88, %2, 2\n"
    "v_mfma_f32_16x16x32_bf16 v[52:55], v[164:167], a[44:47], v[52:55]\n"
    "buffer_load_dwordx4 v[136:139], v253, s[8:11], s47 offen offset:2048 sc1\n"
    "v_mfma_f32_16x16x32_bf16 v[64:67], v[160:163], a[76:79], v[64:67]\n"
    "s_add_u32 s61, s61, 0x1000\n"
    "s_mul_i32 s88, s88, 1024\n"
    "s_add_u32 s88, s88, %3\n"
    "v_mfma_f32_16x16x32_bf16 v[68:71], v[164:167], a[76:79], v[68:71]\n"
    "s_waitcnt vmcnt(9)\n"
    "v_add_u32_e32 v250, s88, v250\n"
    "v_and_b32_e32 v251, 0x1f0, v253\n"
    "v_mfma_f32_16x16x32_bf16 v[24:27], v[176:179], a[16:19], v[24:27]\n"
    "buffer_load_dwordx4 v[140:143], v253, s[8:11], s47 offen offset:3072 sc1\n"
    "v_mfma_f32_16x16x32_bf16 v[28:31], v[180:183], a[16:19], v[28:31]\n"
    "s_bitset0_b32 s61, 14\n"
    "s_lshl_b32 s82, %2, 10\n"
    "s_add_u32 s82, s82, %3\n"
    "v_mfma_f32_16x16x32_bf16 v[48:51], v[176:179], a[48:51], v[48:51]\n"
    "s_add_u32 s47, s52, s61\n"
    "s_add_u32 s82, s82, 20480\n"
    "v_lshrrev_b32_e32 v252, 9, v253\n"
    "v_mfma_f32_16x16x32_bf16 v[52:55], v[180:183], a[48:51], v[52:55]\n"
    "buffer_load_dwordx4 v[152:155], v253, s[8:11], s47 offen offset:0 sc1\n"
    "v_mfma_f32_16x16x32_bf16 v[64:67], v[176:179], a[80:83], v[64:67]\n"
    "buffer_load_dword v0, v3, s[8:11], s59 offen offset:0 sc1\n"
    "v_mfma_f32_16x16x32_bf16 v[68:71], v[180:183], a[80:83], v[68:71]\n"
    "s_waitcnt vmcnt(10)\n"
    "s_lshr_b32 s80, %1, 4\n"
    "v_lshl_add_u32 v252, v252, 4, s82\n"
    "v_mfma_f32_16x16x32_bf16 v[24:27], v[192:195], a[20:23], v[24:27]\n"
    "buffer_load_dwordx4 v[156:159], v253, s[8:11], s47 offen offset:1024 sc1\n"
    "v_mfma_f32_16x16x32_bf16 v[28:31], v[196:199], a[20:23], v[28:31]\n"
    "s_lshl_b32 s80, s80, 7\n"
    "s_add_u32 s80, s80, 5775616\n"
    "s_lshr_b32 s88, s34, 9\n"
    "v_mfma_f32_16x16x32_bf16 v[48:51], v[192:195], a[52:55], v[48:51]\n"
    "s_add_u32 s80, s80, s88\n"
    "s_mov_b64 s[20:21], s[8:9]\n"
    "s_mov_b32 s22, 5242880\n"
    "v_mfma_f32_16x16x32_bf16 v[52:55], v[196:199], a[52:55], v[52:55]\n"
    "buffer_load_dwordx4 v[168:171], v253, s[8:11], s47 offen offset:2048 sc1\n"
    "v_mfma_f32_16x16x32_bf16 v[64:67], v[192:195], a[84:87], v[64:67]\n"
    "s_add_u32 s61, s61, 0x1000\n"
    "s_add_u32 s24, s8, 5779456\n"
    "s_addc_u32 s25, s9, 0\n"
    "v_mfma_f32_16x16x32_bf16 v[68:71], v[196:199], a[84:87], v[68:71]\n"
    "s_waitcnt vmcnt(10)\n"
    "s_mov_b32 s26, 5242880\n"
    "v_mov_b32_e32 v81, 0\n"
    "v_mfma_f32_16x16x32_bf16 v[24:27], v[208:211], a[24:27], v[24:27]\n"
    "buffer_load_dwordx4 v[172:175], v253, s[8:11], s47 offen offset:3072 sc1\n"
    "v_mfma_f32_16x16x32_bf16 v[28:31], v[212:215], a[24:27], v[28:31]\n"
    "s_bitset0_b32 s61, 14\n"
    "s_movk_i32 s42, 512\n"
    "s_mov_b32 s37, 0\n"
    "v_mfma_f32_16x16x32_bf16 v[48:51], v[208:211], a[56:59], v[48:51]\n"
    "s_add_u32 s47, s52, s61\n"
    "s_add_u32 s38, s38, s50\n"
    "v_bfe_u32 v16, v253, 4, 2\n"
    "v_mfma_f32_16x16x32_bf16 v[52:55], v[212:215], a[56:59], v[52:55]\n"
    "buffer_load_dwordx4 v[184:187], v253, s[8:11], s47 offen offset:0 sc1\n"
    "v_mfma_f32_16x16x32_bf16 v[64:67], v[208:211], a[88:91], v[64:67]\n"
    "v_bfe_u32 v17, v253, 6, 2\n"
    "v_mov_b32_e32 v18, 0x3f80\n"
    "v_mfma_f32_16x16x32_bf16 v[68:71], v[212:215], a[88:91], v[68:71]\n"
    "s_waitcnt vmcnt(10)\n"
    "v_cmp_eq_u32_e32 vcc, 3, v16\n"
    "s_nop 1\n"
    "v_cndmask_b32_e64 v18, v18, 1.0, vcc\n"
    "v_cndmask_b32_e64 v16, v16, 0, vcc\n"
    "v_mfma_f32_16x16x32_bf16 v[24:27], v[224:227], a[28:31], v[24:27]\n"
    "buffer_load_dwordx4 v[188:191], v253, s[8:11], s47 offen offset:1024 sc1\n"
    "v_mfma_f32_16x16x32_bf16 v[28:31], v[228:231], a[28:31], v[28:31]\n"
    "v_or_b32_e32 v16, 12, v16\n"
    "v_mul_u32_u24_e32 v16, 0x1111, v16\n"
    "v_mfma_f32_16x16x32_bf16 v[48:51], v[224:227], a[60:63], v[48:51]\n"
    "v_cmp_eq_u32_e32 vcc, 0, v17\n"
    "s_nop 1\n"
    "v_cndmask_b32_e32 v19, 0, v18, vcc\n"
    "v_mfma_f32_16x16x32_bf16 v[52:55], v[228:231], a[60:63], v[52:55]\n"
    "buffer_load_dwordx4 v[200:203], v253, s[8:11], s47 offen offset:2048 sc1\n"
    "v_mfma_f32_16x16x32_bf16 v[64:67], v[224:227], a[92:95], v[64:67]\n"
    "s_add_u32 s61, s61, 0x1000\n"
    "v_cvt_pk_bf16_f32 v24, v24, v25\n"
    "v_accvgpr_write_b32 a192, v19\n"
    "v_mfma_f32_16x16x32_bf16 v[68:71], v[228:231], a[92:95], v[68:71]\n"
    "s_waitcnt vmcnt(10)\n"
    "v_cvt_pk_bf16_f32 v25, v26, v27\n"
    "v_cmp_eq_u32_e32 vcc, 1, v17\n"
    "s_nop 1\n"
    "v_cndmask_b32_e32 v19, 0, v18, vcc\n"
    "v_mfma_f32_16x16x32_bf16 v[32:35], v[120:123], a[0:3], 0\n"
    "buffer_load_dwordx4 v[204:207], v253, s[8:11], s47 offen offset:3072 sc1\n"
    "v_mfma_f32_16x16x32_bf16 v[36:39], v[124:127], a[0:3], 0\n"
    "s_bitset0_b32 s61, 14\n"
    "v_cvt_pk_bf16_f32 v26, v28, v29\n"
    "v_accvgpr_write_b32 a193, v19\n"
    "v_mfma_f32_16x16x32_bf16 v[56:59], v[120:123], a[32:35], 0\n"
    "s_add_u32 s47, s52, s61\n"
    "v_cvt_pk_bf16_f32 v27, v30, v31\n"
    "v_cmp_eq_u32_e32 vcc, 2, v17\n"
    "s_nop 1\n"
    "v_cndmask_b32_e32 v19, 0, v18, vcc\n"
    "v_mfma_f32_16x16x32_bf16 v[60:63], v[124:127], a[32:35], 0\n"
    "buffer_load_dwordx4 v[216:219], v253, s[8:11], s47 offen offset:0 sc1\n"
    "v_mfma_f32_16x16x32_bf16 v[72:75], v[120:123], a[64:67], 0\n"
    "buffer_store_dwordx4 v[24:27], v8, s[8:11], s35 offen offset:0 sc1\n"
    "v_cvt_pk_bf16_f32 v48, v48, v49\n"
    "v_accvgpr_write_b32 a194, v19\n"
    "v_mfma_f32_16x16x32_bf16 v[76:79], v[124:127], a[64:67], 0\n"
    "s_waitcnt vmcnt(11)\n"
    "v_cvt_pk_bf16_f32 v49, v50, v51\n"
    "v_cmp_eq_u32_e32 vcc, 3, v17\n"
    "s_nop 1\n"
    "v_cndmask_b32_e32 v19, 0, v18, vcc\n"
    "v_mfma_f32_16x16x32_bf16 v[32:35], v[136:139], a[4:7], v[32:35]\n"
    "buffer_load_dwordx4 v[220:223], v253, s[8:11], s47 offen offset:1024 sc1\n"
    "v_mfma_f32_16x16x32_bf16 v[36:39], v[140:143], a[4:7], v[36:39]\n"
    "s_add_u32 s52, s52, 0x4000\n"
    "v_cvt_pk_bf16_f32 v50, v52, v53\n"
    "v_accvgpr_write_b32 a195, v19\n"
    "v_mfma_f32_16x16x32_bf16 v[56:59], v[136:139], a[36:39], v[56:59]\n"
    "s_add_u32 s28, s38, 2\n"
    "v_cvt_pk_bf16_f32 v51, v54, v55\n"
    "v_mov_b32_e32 v82, 0\n"
    "v_mfma_f32_16x16x32_bf16 v[60:63], v[140:143], a[36:39], v[60:63]\n"
    "s_waitcnt vmcnt(10)\n"
    "buffer_load_dwordx4 v[232:235], v253, s[8:11], s47 offen offset:2048 sc1\n"
    "buffer_store_dwordx4 v[48:51], v8, s[8:11], s54 offen offset:0 sc1\n"
    "v_cmp_ne_u32_e32 vcc, s39, v0\n"
    "v_mfma_f32_16x16x32_bf16 v[72:75], v[136:139], a[68:71], v[72:75]\n"
    "s_add_u32 s61, s61, 0x1000\n"
    "v_cvt_pk_bf16_f32 v64, v64, v65\n"
    "v_mov_b32_e32 v83, 0\n"
    "v_mfma_f32_16x16x32_bf16 v[76:79], v[140:143], a[68:71], v[76:79]\n"
    "s_waitcnt vmcnt(11)\n"
    "v_cvt_pk_bf16_f32 v65, v66, v67\n"
    "v_mov_b32_e32 v84, 0\n"
    "v_mfma_f32_16x16x32_bf16 v[32:35], v[152:155], a[8:11], v[32:35]\n"
    "buffer_load_dwordx4 v[236:239], v253, s[8:11], s47 offen offset:3072 sc1\n"
    "v_mfma_f32_16x16x32_bf16 v[36:39], v[156:159], a[8:11], v[36:39]\n"
    "s_bitset0_b32 s61, 14\n"
    "s_cbranch_vccnz .Lat_h1poll\n"
    ".Lat_h1poll_ret:\n"
    "v_mfma_f32_16x16x32_bf16 v[56:59], v[152:155], a[40:43], v[56:59]\n"
    "s_sub_u32 s88, s28, 10\n"
    "v_cvt_pk_bf16_f32 v66, v68, v69\n"
    "v_cvt_pk_bf16_f32 v67, v70, v71\n"
    "v_mfma_f32_16x16x32_bf16 v[60:63], v[156:159], a[40:43], v[60:63]\n"
    "buffer_store_dwordx4 v[64:67], v8, s[8:11], s60 offen offset:0 sc1\n"
    "v_mov_b32_e32 v85, 0\n"
    "v_mov_b32_e32 v86, 0\n"
    "v_mfma_f32_16x16x32_bf16 v[72:75], v[152:155], a[72:75], v[72:75]\n"
    "s_min_u32 s28, s28, s88\n"
    "s_lshl_b32 s88, s28, 0\n"
    "v_mov_b32_e32 v87, 0\n"
    "v_mfma_f32_16x16x32_bf16 v[76:79], v[156:159], a[72:75], v[76:79]\n"
    "s_waitcnt vmcnt(11)\n"
    "s_or_b32 s36, s88, s88\n"
    "v_mov_b32_e32 v88, 0\n"
    "v_mfma_f32_16x16x32_bf16 v[32:35], v[168:171], a[12:15], v[32:35]\n"
    "s_lshl1_add_u32 s88, s28, 1\n"
    "s_mul_i32 s88, s88, 52\n"
    "v_mov_b32_e32 v89, 0\n"
    "v_mfma_f32_16x16x32_bf16 v[36:39], v[172:175], a[12:15], v[36:39]\n"
    "s_lshr_b32 s88, s88, 8\n"
    "s_mul_i32 s90, s88, 5\n"
    "v_mov_b32_e32 v90, 0\n"
    "v_mfma_f32_16x16x32_bf16 v[56:59], v[168:171], a[44:47], v[56:59]\n"
    "s_lshr_b32 s90, s90, 1\n"
    "ds_write_b128 v9, v[24:27] offset:0\n"
    "v_mfma_f32_16x16x32_bf16 v[60:63], v[172:175], a[44:47], v[60:63]\n"
    "s_sub_u32 s28, s28, s90\n"
    "s_sub_u32 s90, s32, s88\n"
    "v_mov_b32_e32 v91, 0\n"
    "v_mfma_f32_16x16x32_bf16 v[72:75], v[168:171], a[76:79], v[72:75]\n"
    "s_sub_u32 s90, s90, 1\n"
    "s_and_b32 s90, s90, 3\n"
    "v_mov_b32_e32 v92, 0\n"
    "v_mfma_f32_16x16x32_bf16 v[76:79], v[172:175], a[76:79], v[76:79]\n"
    "s_waitcnt vmcnt(9)\n"
    "s_lshl2_add_u32 s88, s33, s88\n"
    "v_mov_b32_e32 v93, 0\n"
    "v_mfma_f32_16x16x32_bf16 v[32:35], v[184:187], a[16:19], v[32:35]\n"
    "s_mul_i32 s88, s88, 3\n"
    "v_mov_b32_e32 v94, 0\n"
    "v_mov_b32_e32 v95, 0\n"
    "v_mfma_f32_16x16x32_bf16 v[36:39], v[188:191], a[16:19], v[36:39]\n"
    "s_add_u32 s88, s88, s90\n"
    "v_mov_b32_e32 v96, 0\n"
    "v_mfma_f32_16x16x32_bf16 v[56:59], v[184:187], a[48:51], v[56:59]\n"
    "s_mul_i32 s88, s88, 24832\n"
    "s_add_u32 s88, s88, 16265216\n"
    "v_mfma_f32_16x16x32_bf16 v[60:63], v[188:191], a[48:51], v[60:63]\n"
    "s_cmp_eq_u32 s90, 3\n"
    "s_cselect_b32 s88, 2.0, s88\n"
    "s_cselect_b32 s89, 2, 3\n"
    "v_mfma_f32_16x16x32_bf16 v[72:75], v[184:187], a[80:83], v[72:75]\n"
    "s_lshl1_add_u32 s83, s28, s51\n"
    "v_mov_b32_e32 v97, 0\n"
    "v_mfma_f32_16x16x32_bf16 v[76:79], v[188:191], a[80:83], v[76:79]\n"
    "s_waitcnt vmcnt(7)\n"
    "ds_write_b128 v9, v[48:51] offset:8192\n"
    "v_mfma_f32_16x16x32_bf16 v[32:35], v[200:203], a[20:23], v[32:35]\n"
    "s_lshl_b32 s83, s83, 12\n"
    "v_mov_b32_e32 v98, 0\n"
    "v_mfma_f32_16x16x32_bf16 v[36:39], v[204:207], a[20:23], v[36:39]\n"
    "s_add_u32 s83, s83, s88\n"
    "v_mov_b32_e32 v99, 0\n"
    "v_mfma_f32_16x16x32_bf16 v[56:59], v[200:203], a[52:55], v[56:59]\n"
    "s_lshl2_add_u32 s28, s28, s88\n"
    "v_mov_b32_e32 v100, 0\n"
    "v_mfma_f32_16x16x32_bf16 v[60:63], v[204:207], a[52:55], v[60:63]\n"
    "s_add_u32 s29, s38, 4\n"
    "v_mov_b32_e32 v101, 0\n"
    "v_mfma_f32_16x16x32_bf16 v[72:75], v[200:203], a[84:87], v[72:75]\n"
    "s_sub_u32 s88, s29, 10\n"
    "v_mov_b32_e32 v102, 0\n"
    "v_mfma_f32_16x16x32_bf16 v[76:79], v[204:207], a[84:87], v[76:79]\n"
    "s_waitcnt vmcnt(4)\n"
    "s_min_u32 s29, s29, s88\n"
    "v_mfma_f32_16x16x32_bf16 v[32:35], v[216:219], a[24:27], v[32:35]\n"
    "s_lshl_b32 s88, s29, 4\n"
    "v_mov_b32_e32 v103, 0\n"
    "v_mfma_f32_16x16x32_bf16 v[36:39], v[220:223], a[24:27], v[36:39]\n"
    "s_or_b32 s36, s88, s36\n"
    "v_mov_b32_e32 v104, 0\n"
    "v_mfma_f32_16x16x32_bf16 v[56:59], v[216:219], a[56:59], v[56:59]\n"
    "s_lshl1_add_u32 s88, s29, 1\n"
    "ds_write_b128 v9, v[64:67] offset:16384\n"
    "v_mfma_f32_16x16x32_bf16 v[60:63], v[220:223], a[56:59], v[60:63]\n"
    "s_mul_i32 s88, s88, 52\n"
    "v_mov_b32_e32 v105, 0\n"
    "v_mfma_f32_16x16x32_bf16 v[72:75], v[216:219], a[88:91], v[72:75]\n"
    "s_lshr_b32 s88, s88, 8\n"
    "v_mov_b32_e32 v106, 0\n"
    "v_mfma_f32_16x16x32_bf16 v[76:79], v[220:223], a[88:91], v[76:79]\n"
    "s_waitcnt vmcnt(1)\n"
    "s_mul_i32 s90, s88, 5\n"
    "v_mfma_f32_16x16x32_bf16 v[32:35], v[232:235], a[28:31], v[32:35]\n"
    "s_lshr_b32 s90, s90, 1\n"
    "v_mov_b32_e32 v107, 0\n"
    "v_mfma_f32_16x16x32_bf16 v[36:39], v[236:239], a[28:31], v[36:39]\n"
    "s_sub_u32 s29, s29, s90\n"
    "v_mov_b32_e32 v108, 0\n"
    "v_mfma_f32_16x16x32_bf16 v[56:59], v[232:235], a[60:63], v[56:59]\n"
    "s_sub_u32 s90, s32, s88\n"
    "v_mov_b32_e32 v109, 0\n"
    "v_mfma_f32_16x16x32_bf16 v[60:63], v[236:239], a[60:63], v[60:63]\n"
    "s_sub_u32 s90, s90, 1\n"
    "v_mov_b32_e32 v110, 0\n"
    "v_mfma_f32_16x16x32_bf16 v[72:75], v[232:235], a[92:95], v[72:75]\n"
    "s_and_b32 s90, s90, 3\n"
    "v_mov_b32_e32 v111, 0\n"
    "v_mfma_f32_16x16x32_bf16 v[76:79], v[236:239], a[92:95], v[76:79]\n"
    "s_lshl2_add_u32 s88, s33, s88\n"
    "s_mul_i32 s88, s88, 3\n"
    "v_cvt_pk_bf16_f32 v32, v32, v33\n"
    "v_cvt_pk_bf16_f32 v33, v34, v35\n"
    "v_cvt_pk_bf16_f32 v34, v36, v37\n"
    "v_cvt_pk_bf16_f32 v35, v38, v39\n"
    "v_or_b32_e32 v35, v10, v35\n"
    "ds_write_b128 v9, v[32:35] offset:1024\n"
    "buffer_store_dwordx4 v[32:35], v8, s[8:11], s35 offen offset:1024 sc1\n"
    "v_cvt_pk_bf16_f32 v56, v56, v57\n"
    "v_cvt_pk_bf16_f32 v57, v58, v59\n"
    "v_cvt_pk_bf16_f32 v58, v60, v61\n"
    "v_cvt_pk_bf16_f32 v59, v62, v63\n"
    "v_or_b32_e32 v59, v10, v59\n"
    "ds_write_b128 v9, v[56:59] offset:9216\n"
    "buffer_store_dwordx4 v[56:59], v8, s[8:11], s54 offen offset:1024 sc1\n"
    "v_cvt_pk_bf16_f32 v72, v72, v73\n"
    "v_cvt_pk_bf16_f32 v73, v74, v75\n"
    "v_cvt_pk_bf16_f32 v74, v76, v77\n"
    "v_cvt_pk_bf16_f32 v75, v78, v79\n"
    "v_or_b32_e32 v75, v10, v75\n"
    "ds_write_b128 v9, v[72:75] offset:17408\n"
    "buffer_store_dwordx4 v[72:75], v8, s[8:11], s60 offen offset:1024 sc1\n"
    "v_mov_b32_e32 v112, 0\n"
    "v_mov_b32_e32 v113, 0\n"
    "v_mov_b32_e32 v114, 0\n"
    "s_mov_b32 m0, s63\n"
    "v_mov_b32_e32 v115, 0\n"
    "v_mov_b32_e32 v116, 0\n"
    "v_mov_b32_e32 v117, 0\n"
    "buffer_load_dwordx4 v254, s[8:11], s57 offen offset:0 sc1 lds\n"
    "v_mov_b32_e32 v118, 0\n"
    "v_mov_b32_e32 v119, 0\n"
    "v_mov_b32_e32 v120, 0\n"
    "s_sub_u32 m0, s63, 1024\n"
    "v_mov_b32_e32 v121, 0\n"
    "v_mov_b32_e32 v122, 0\n"
    "v_mov_b32_e32 v123, 0\n"
    "buffer_load_dwordx4 v254, s[8:11], s57 offen offset:2048 sc1 lds\n"
    "v_mov_b32_e32 v124, 0\n"
    "v_mov_b32_e32 v125, 0\n"
    "v_mov_b32_e32 v126, 0\n"
    "s_add_u32 m0, s63, 3968\n"
    "v_mov_b32_e32 v127, 0\n"
    "v_mov_b32_e32 v128, 0\n"
    "v_mov_b32_e32 v129, 0\n"
    "buffer_load_dwordx4 v254, s[8:11], s57 offen offset:128 sc1 lds\n"
    "v_mov_b32_e32 v130, 0\n"
    "v_mov_b32_e32 v131, 0\n"
    "v_mov_b32_e32 v132, 0\n"
    "s_add_u32 m0, s63, 2944\n"
    "v_mov_b32_e32 v133, 0\n"
    "v_mov_b32_e32 v134, 0\n"
    "v_mov_b32_e32 v135, 0\n"
    "buffer_load_dwordx4 v254, s[8:11], s57 offen offset:2176 sc1 lds\n"
    "v_mov_b32_e32 v136, 0\n"
    "v_mov_b32_e32 v137, 0\n"
    "v_mov_b32_e32 v138, 0\n"
    "s_add_u32 m0, s62, 0\n"
    "v_mov_b32_e32 v139, 0\n"
    "v_mov_b32_e32 v140, 0\n"
    "v_mov_b32_e32 v141, 0\n"
    "buffer_load_dwordx4 v12, s[8:11], s40 offen offset:0 sc1 lds\n"
    "v_mov_b32_e32 v142, 0\n"
    "v_mov_b32_e32 v143, 0\n"
    "v_mov_b32_e32 v144, 0\n"
    "buffer_load_dwordx4 v13, s[8:11], s40 offen offset:1024 sc1 lds\n"
    "v_mov_b32_e32 v145, 0\n"
    "v_mov_b32_e32 v146, 0\n"
    "v_mov_b32_e32 v147, 0\n"
    "buffer_load_dwordx4 v14, s[8:11], s40 offen offset:2048 sc1 lds\n"
    "v_mov_b32_e32 v148, 0\n"
    "v_mov_b32_e32 v149, 0\n"
    "v_mov_b32_e32 v150, 0\n"
    "buffer_load_dwordx4 v15, s[8:11], s40 offen offset:3072 sc1 lds\n"
    "v_mov_b32_e32 v151, 0\n"
    "s_xor_b32 s45, s34, 0xc000\n"
    "v_add_u32_e32 v204, s45, v240\n"
    "s_add_u32 s40, s40, 0x8000\n"
    "v_add_u32_e32 v205, s45, v241\n"
    "v_add_u32_e32 v206, s45, v242\n"
    "v_add_u32_e32 v207, s45, v243\n"
    "s_add_u32 m0, s62, 4096\n"
    "v_add_u32_e32 v208, s45, v244\n"
    "v_add_u32_e32 v209, s45, v245\n"
    "v_add_u32_e32 v210, s45, v246\n"
    "buffer_load_dwordx4 v12, s[8:11], s41 offen offset:0 sc1 lds\n"
    "buffer_load_dwordx4 v13, s[8:11], s41 offen offset:1024 sc1 lds\n"
    "buffer_load_dwordx4 v14, s[8:11], s41 offen offset:2048 sc1 lds\n"
    "buffer_load_dwordx4 v15, s[8:11], s41 offen offset:3072 sc1 lds\n"
    "s_add_u32 s41, s41, 0x8000\n"
    "buffer_load_dword v0, v253, s[8:11], s59 offen offset:0 sc1\n"
    "buffer_load_dword v1, v2, s[8:11], s59 offen offset:0 sc1\n"
    "v_add_u32_e32 v211, s45, v247\n"
    "v_add_u32_e32 v200, s34, v240\n"
    "v_add_u32_e32 v201, s34, v241\n"
    "v_add_u32_e32 v202, s34, v242\n"
    "v_add_u32_e32 v203, s34, v243\n"
    "v_mov_b32_e32 v152, v16\n"
    "v_and_b32_e32 v153, 0xf0, v253\n"
    "v_lshrrev_b32_e32 v154, 9, v253\n"
    "v_lshl_add_u32 v153, v154, 8, v153\n"
    "v_bfe_u32 v154, v253, 8, 1\n"
    "v_lshl_add_u32 v153, v154, 3, v153\n"
    "v_mov_b32_e32 v156, 0\n"
    "v_mov_b32_e32 v157, 0\n"
    "v_mov_b32_e32 v158, 0\n"
    "v_mov_b32_e32 v159, 0\n"
    "v_mov_b32_e32 v56, 0\n"
    "v_mov_b32_e32 v57, 0\n"
    "v_mov_b32_e32 v58, 0\n"
    "v_mov_b32_e32 v59, 0\n"
    "v_mov_b32_e32 v60, 0\n"
    "v_mov_b32_e32 v61, 0\n"
    "v_mov_b32_e32 v62, 0\n"
    "v_mov_b32_e32 v63, 0\n"
    "v_mov_b32_e32 v64, 0\n"
    "v_mov_b32_e32 v65, 0\n"
    "v_mov_b32_e32 v66, 0\n"
    "v_mov_b32_e32 v67, 0\n"
    "v_mov_b32_e32 v68, 0\n"
    "v_mov_b32_e32 v69, 0\n"
    "v_mov_b32_e32 v70, 0\n"
    "v_mov_b32_e32 v71, 0\n"
    "v_mov_b32_e32 v72, 0\n"
    "v_mov_b32_e32 v73, 0\n"
    "v_mov_b32_e32 v74, 0\n"
    "v_mov_b32_e32 v75, 0\n"
    "v_mov_b32_e32 v76, 0\n"
    "v_mov_b32_e32 v77, 0\n"
    "v_mov_b32_e32 v78, 0\n"
    "v_mov_b32_e32 v79, 0\n"
    "s_add_u32 s88, s88, s90\n"
    "s_mul_i32 s88, s88, 24832\n"
    "s_add_u32 s88, s88, 16265216\n"
    "s_lshl1_add_u32 s84, s29, s51\n"
    "s_lshl_b32 s84, s84, 12\n"
    "s_add_u32 s84, s84, s88\n"
    "s_lshl2_add_u32 s29, s29, s88\n"
    "s_add_u32 s30, s38, 6\n"
    "s_sub_u32 s88, s30, 10\n"
    "s_min_u32 s30, s30, s88\n"
    "s_lshl_b32 s88, s30, 8\n"
    "s_or_b32 s36, s88, s36\n"
    "s_lshl1_add_u32 s88, s30, 1\n"
    "s_mul_i32 s88, s88, 52\n"
    "s_lshr_b32 s88, s88, 8\n"
    "s_mul_i32 s90, s88, 5\n"
    "s_lshr_b32 s90, s90, 1\n"
    "s_sub_u32 s30, s30, s90\n"
    "s_sub_u32 s90, s32, s88\n"
    "s_sub_u32 s90, s90, 1\n"
    "s_and_b32 s90, s90, 3\n"
    "s_lshl2_add_u32 s88, s33, s88\n"
    "s_mul_i32 s88, s88, 3\n"
    "s_add_u32 s88, s88, s90\n"
    "s_mul_i32 s88, s88, 24832\n"
    "s_add_u32 s88, s88, 16265216\n"
    "s_lshl1_add_u32 s85, s30, s51\n"
    "s_lshl_b32 s85, s85, 12\n"
    "s_add_u32 s85, s85, s88\n"
    "s_lshl2_add_u32 s30, s30, s88\n"
    "s_add_u32 s31, s38, 8\n"
    "s_sub_u32 s88, s31, 10\n"
    "s_min_u32 s31, s31, s88\n"
    "s_lshl_b32 s88, s31, 12\n"
    "s_or_b32 s36, s88, s36\n"
    "s_lshl1_add_u32 s88, s31, 1\n"
    "s_mul_i32 s88, s88, 52\n"
    "s_lshr_b32 s88, s88, 8\n"
    "s_mul_i32 s90, s88, 5\n"
    "s_lshr_b32 s90, s90, 1\n"
    "s_sub_u32 s31, s31, s90\n"
    "s_sub_u32 s90, s32, s88\n"
    "s_sub_u32 s90, s90, 1\n"
    "s_and_b32 s90, s90, 3\n"
    "s_lshl2_add_u32 s88, s33, s88\n"
    "s_mul_i32 s88, s88, 3\n"
    "s_add_u32 s88, s88, s90\n"
    "s_mul_i32 s88, s88, 24832\n"
    "s_add_u32 s88, s88, 16265216\n"
    "s_lshl1_add_u32 s86, s31, s51\n"
    "s_lshl_b32 s86, s86, 12\n"
    "s_add_u32 s86, s86, s88\n"
    "s_lshl2_add_u32 s31, s31, s88\n"
    "s_lshl_b32 s87, %1, 6\n"
    "s_add_u32 s87, s87, %2\n"
    "s_lshl2_add_u32 s87, s87, 5636096\n"
    "s_or_b32 s88, %1, %2\n"
    "s_cselect_b32 s88, 2.0, 5777664\n"
    "s_lshl_b32 s47, s33, 9\n"
    "s_add_u32 s47, s47, 5738496\n"
    "v_lshlrev_b32_e32 v9, 3, v253\n"
    "s_cmp_eq_u32 s50, 0\n"
    "s_cselect_b32 s46, s35, s54\n"
    "s_cselect_b32 s45, s60, 2.0\n"
    "s_cmp_eq_u32 s51, 0\n"
    "s_cselect_b32 s46, s46, s45\n"
    "s_add_u32 s46, s46, 11022336\n"
    "s_waitcnt vmcnt(14)\n"
    "s_waitcnt lgkmcnt(0)\n"
    "s_barrier\n"
    "buffer_store_dword v40, v41, s[8:11], s58 offen offset:64 sc1\n"
    "buffer_load_dword v228, v9, s[8:11], s46 offen offset:0 sc1\n"
    "ds_read_b128 a[0:3], v6 offset:0\n"
    "ds_read_b128 a[4:7], v6 offset:1024\n"
    "ds_read_b128 a[8:11], v6 offset:2048\n"
    "ds_read_b128 a[12:15], v6 offset:3072\n"
    "ds_read_b128 a[16:19], v6 offset:4096\n"
    "ds_read_b128 a[20:23], v6 offset:5120\n"
    "ds_read_b128 a[24:27], v6 offset:6144\n"
    "ds_read_b128 a[28:31], v6 offset:7168\n"
    "s_waitcnt vmcnt(4)\n"
    "s_barrier\n"
    "ds_read_b128 a[160:163], v204 offset:0\n"
    "ds_read_b128 a[164:167], v205 offset:0\n"
    "ds_read_b128 a[168:171], v204 offset:4096\n"
    "ds_read_b128 a[172:175], v205 offset:4096\n"
    "ds_read_b128 a[176:179], v206 offset:0\n"
    "ds_read_b128 a[180:183], v207 offset:0\n"
    "ds_read_b128 a[184:187], v206 offset:4096\n"
    "ds_read_b128 a[188:191], v207 offset:4096\n"
    "s_add_u32 m0, s62, 16384\n"
    "s_nop 0\n"
    "buffer_load_dwordx4 v12, s[8:11], s40 offen offset:0 sc1 lds\n"
    "buffer_load_dwordx4 v13, s[8:11], s40 offen offset:1024 sc1 lds\n"
    "buffer_load_dwordx4 v14, s[8:11], s40 offen offset:2048 sc1 lds\n"
    "buffer_load_dwordx4 v15, s[8:11], s40 offen offset:3072 sc1 lds\n"
    "s_add_u32 s40, s40, 0x8000\n"
    "s_add_u32 m0, s62, 20480\n"
    "s_nop 0\n"
    "buffer_load_dwordx4 v12, s[8:11], s41 offen offset:0 sc1 lds\n"
    "buffer_load_dwordx4 v13, s[8:11], s41 offen offset:1024 sc1 lds\n"
    "buffer_load_dwordx4 v14, s[8:11], s41 offen offset:2048 sc1 lds\n"
    "buffer_load_dwordx4 v15, s[8:11], s41 offen offset:3072 sc1 lds\n"
    "s_add_u32 s41, s41, 0x8000\n"
    "s_waitcnt lgkmcnt(4)\n"
    "v_mfma_f32_16x16x32_bf16 v[220:223], a[160:163], a[0:3], 0\n"
    "ds_read_b128 a[32:35], v7 offset:16384\n"
    "ds_read_b128 a[36:39], v7 offset:17408\n"
    "v_mfma_f32_16x16x32_bf16 v[220:223], a[164:167], a[4:7], v[220:223]\n"
    "ds_read_b128 a[40:43], v7 offset:18432\n"
    "ds_read_b128 a[44:47], v7 offset:19456\n"
    "v_mfma_f32_16x16x32_bf16 v[220:223], a[168:171], a[8:11], v[220:223]\n"
    "ds_read_b128 a[48:51], v7 offset:20480\n"
    "ds_read_b128 a[52:55], v7 offset:21504\n"
    "v_mfma_f32_16x16x32_bf16 v[220:223], a[172:175], a[12:15], v[220:223]\n"
    "ds_read_b128 a[56:59], v7 offset:22528\n"
    "ds_read_b128 a[60:63], v7 offset:23552\n"
    "s_waitcnt lgkmcnt(8)\n"
    "v_mfma_f32_16x16x32_bf16 v[220:223], a[176:179], a[16:19], v[220:223]\n"
    "ds_read_b64_tr_b16 a[232:233], v208 offset:0\n"
    "ds_read_b64_tr_b16 a[234:235], v209 offset:0\n"
    "v_mfma_f32_16x16x32_bf16 v[220:223], a[180:183], a[20:23], v[220:223]\n"
    "ds_read_b64_tr_b16 a[236:237], v210 offset:0\n"
    "ds_read_b64_tr_b16 a[238:239], v211 offset:0\n"
    "v_mfma_f32_16x16x32_bf16 v[220:223], a[184:187], a[24:27], v[220:223]\n"
    "ds_read_b64_tr_b16 a[240:241], v208 offset:4096\n"
    "ds_read_b64_tr_b16 a[242:243], v209 offset:4096\n"
    "v_mfma_f32_16x16x32_bf16 v[220:223], a[188:191], a[28:31], v[220:223]\n"
    "buffer_load_dword v11, v5, s[8:11], s47 offen offset:0 sc1\n"
    "ds_read_b64_tr_b16 a[244:245], v210 offset:4096\n"
    "s_waitcnt lgkmcnt(11)\n"
    "v_mfma_f32_16x16x32_bf16 v[224:227], a[160:163], a[32:35], 0\n"
    "ds_read_b64_tr_b16 a[246:247], v211 offset:4096\n"
    "ds_read_b128 a[200:203], v203 offset:6144\n"
    "v_mfma_f32_16x16x32_bf16 v[224:227], a[164:167], a[36:39], v[224:227]\n"
    "ds_read_b128 a[204:207], v200 offset:2048\n"
    "ds_read_b128 a[208:211], v201 offset:2048\n"
    "v_mfma_f32_16x16x32_bf16 v[224:227], a[168:171], a[40:43], v[224:227]\n"
    "ds_read_b128 a[212:215], v200 offset:6144\n"
    "ds_read_b128 a[216:219], v201 offset:6144\n"
    "v_mfma_f32_16x16x32_bf16 v[224:227], a[172:175], a[44:47], v[224:227]\n"
    "ds_read_b128 a[220:223], v202 offset:2048\n"
    "ds_read_b128 a[224:227], v203 offset:2048\n"
    "s_waitcnt lgkmcnt(15)\n"
    "v_mfma_f32_16x16x32_bf16 v[224:227], a[176:179], a[48:51], v[224:227]\n"
    "ds_read_b128 a[228:231], v202 offset:6144\n"
    "v_mfma_f32_16x16x32_bf16 v[224:227], a[180:183], a[52:55], v[224:227]\n"
    "v_mfma_f32_16x16x32_bf16 v[224:227], a[184:187], a[56:59], v[224:227]\n"
    "v_mfma_f32_16x16x32_bf16 v[224:227], a[188:191], a[60:63], v[224:227]\n"
    "s_andn2_b32 s61, s32, s50\n"
    "s_bfe_i32 s61, s61, 0x10000\n"
    "ds_read_b128 a[160:163], v203 offset:4096\n"
    "ds_read_b128 a[164:167], v200 offset:0\n"
    "ds_read_b128 a[168:171], v201 offset:0\n"
    "ds_read_b128 a[172:175], v200 offset:4096\n"
    "ds_read_b128 a[176:179], v201 offset:4096\n"
    "ds_read_b128 a[180:183], v202 offset:0\n"
    "ds_read_b128 a[184:187], v203 offset:0\n"
    "ds_read_b128 a[188:191], v202 offset:4096\n"
    "s_and_b32 s57, s63, 0xffffdfff\n"
    "s_sub_u32 s57, s57, 16384\n"
    "v_add_u32_e32 v216, s57, v253\n"
    "s_lshl_b32 s45, s51, 12\n"
    "s_add_u32 s57, s57, s45\n"
    "s_mul_i32 s48, s33, 81920\n"
    "s_add_u32 s48, s48, s45\n"
    "s_add_u32 s48, s48, 11022336\n"
    "v_exp_f32_e32 v220, v220\n"
    "v_exp_f32_e32 v221, v221\n"
    "v_exp_f32_e32 v222, v222\n"
    "v_exp_f32_e32 v223, v223\n"
    "v_exp_f32_e32 v224, v224\n"
    "v_exp_f32_e32 v225, v225\n"
    "v_exp_f32_e32 v226, v226\n"
    "v_exp_f32_e32 v227, v227\n"
    "v_cvt_pk_bf16_f32 v220, v220, v221\n"
    "v_cvt_pk_bf16_f32 v221, v222, v223\n"
    "v_cvt_pk_bf16_f32 v222, v224, v225\n"
    "v_cvt_pk_bf16_f32 v223, v226, v227\n"
    "v_and_b32_e32 v222, s61, v222\n"
    "v_and_b32_e32 v223, s61, v223\n"
    "s_waitcnt vmcnt(11)\n"
    "s_waitcnt lgkmcnt(0)\n"
    "s_barrier\n"
    "v_xor_b32_e32 v1, s39, v1\n"
    "v_xor_b32_e32 v0, s39, v0\n"
    "v_or_b32_e32 v0, v0, v1\n"
    "v_cmp_ne_u32_e32 vcc, 0, v0\n"
    "s_nop 3\n"
    "s_cbranch_vccnz .Lat_hpoll\n"
    ".Lat_hpoll_ret:\n"
    "s_waitcnt vmcnt(0)\n"
    "v_cmp_ne_u32_e32 vcc, s39, v11\n"
    "s_nop 3\n"
    "s_cbranch_vccnz .Lat_qpoll\n"
    ".Lat_qpoll_ret:\n"
    "s_cmp_eq_u32 s50, 0\n"
    "s_cbranch_scc1 .Lat_qx\n"
    "s_bfe_u32 s45, s36, 0x40000\n"
    "s_add_u32 m0, s57, 0\n"
    "s_lshl_b32 s45, s45, 13\n"
    "s_add_u32 s45, s45, s48\n"
    "buffer_load_dwordx4 v253, s[8:11], s45 offen offset:0 sc1 lds\n"
    "buffer_load_dwordx4 v253, s[8:11], s45 offen offset:1024 sc1 lds\n"
    "buffer_load_dwordx4 v253, s[8:11], s45 offen offset:2048 sc1 lds\n"
    "buffer_load_dwordx4 v253, s[8:11], s45 offen offset:3072 sc1 lds\n"
    ".Lat_qx:\n"
    "s_bfe_u32 s45, s36, 0x40004\n"
    "s_add_u32 m0, s57, 8192\n"
    "s_lshl_b32 s45, s45, 13\n"
    "s_add_u32 s45, s45, s48\n"
    "buffer_load_dwordx4 v253, s[8:11], s45 offen offset:0 sc1 lds\n"
    "buffer_load_dwordx4 v253, s[8:11], s45 offen offset:1024 sc1 lds\n"
    "buffer_load_dwordx4 v253, s[8:11], s45 offen offset:2048 sc1 lds\n"
    "buffer_load_dwordx4 v253, s[8:11], s45 offen offset:3072 sc1 lds\n"
    "v_mfma_f32_16x16x16_bf16 v[52:55], a[246:247], v[222:223], 0\n"
    "v_mfma_f32_16x16x16_bf16 v[48:51], a[244:245], v[222:223], 0\n"
    "s_bfe_u32 s45, s36, 0x40008\n"
    "s_add_u32 m0, s57, 16384\n"
    "s_lshl_b32 s45, s45, 13\n"
    "s_add_u32 s45, s45, s48\n"
    "v_mfma_f32_16x16x16_bf16 v[44:47], a[242:243], v[222:223], 0\n"
    "buffer_load_dwordx4 v253, s[8:11], s45 offen offset:0 sc1 lds\n"
    "v_mfma_f32_16x16x16_bf16 v[40:43], a[240:241], v[222:223], 0\n"
    "v_mfma_f32_16x16x16_bf16 v[36:39], a[238:239], v[222:223], 0\n"
    "buffer_load_dwordx4 v253, s[8:11], s45 offen offset:1024 sc1 lds\n"
    "v_mfma_f32_16x16x16_bf16 v[32:35], a[236:237], v[222:223], 0\n"
    "v_mfma_f32_16x16x16_bf16 a[252:255], a[234:235], v[222:223], 0\n"
    "buffer_load_dwordx4 v253, s[8:11], s45 offen offset:2048 sc1 lds\n"
    "v_mfma_f32_16x16x16_bf16 a[248:251], a[232:233], v[222:223], 0\n"
    "v_mfma_f32_16x16x16_bf16 v[28:31], a[246:247], v[220:221], 0\n"
    "buffer_load_dwordx4 v253, s[8:11], s45 offen offset:3072 sc1 lds\n"
    "v_mfma_f32_16x16x16_bf16 v[24:27], a[244:245], v[220:221], 0\n"
    "s_bfe_u32 s45, s36, 0x4000c\n"
    "s_add_u32 m0, s57, 24576\n"
    "s_lshl_b32 s45, s45, 13\n"
    "s_add_u32 s45, s45, s48\n"
    "v_mfma_f32_16x16x16_bf16 v[20:23], a[242:243], v[220:221], 0\n"
    "buffer_load_dwordx4 v253, s[8:11], s45 offen offset:0 sc1 lds\n"
    "v_mfma_f32_16x16x16_bf16 v[16:19], a[240:241], v[220:221], 0\n"
    "v_mfma_f32_16x16x16_bf16 v[12:15], a[238:239], v[220:221], 0\n"
    "buffer_load_dwordx4 v253, s[8:11], s45 offen offset:1024 sc1 lds\n"
    "v_mfma_f32_16x16x16_bf16 v[8:11], a[236:237], v[220:221], 0\n"
    "v_mfma_f32_16x16x16_bf16 v[4:7], a[234:235], v[220:221], 0\n"
    "buffer_load_dwordx4 v253, s[8:11], s45 offen offset:2048 sc1 lds\n"
    "v_mfma_f32_16x16x16_bf16 v[0:3], a[232:233], v[220:221], 0\n"
    "v_mfma_f32_16x16x32_bf16 v[160:163], a[160:163], a[28:31], 0\n"
    "buffer_load_dwordx4 v253, s[8:11], s45 offen offset:3072 sc1 lds\n"
    "v_mfma_f32_16x16x32_bf16 v[160:163], a[164:167], a[0:3], v[160:163]\n"
    "s_mov_b32 m0, s49\n"
    "v_mfma_f32_16x16x32_bf16 v[160:163], a[168:171], a[4:7], v[160:163]\n"
    "v_mfma_f32_16x16x32_bf16 v[160:163], a[172:175], a[8:11], v[160:163]\n"
    "v_mfma_f32_16x16x32_bf16 v[160:163], a[176:179], a[12:15], v[160:163]\n"
    "v_mfma_f32_16x16x32_bf16 v[160:163], a[180:183], a[16:19], v[160:163]\n"
    "v_mfma_f32_16x16x32_bf16 v[160:163], a[184:187], a[20:23], v[160:163]\n"
    "v_mfma_f32_16x16x32_bf16 v[160:163], a[188:191], a[24:27], v[160:163]\n"
    "v_mfma_f32_16x16x32_bf16 v[164:167], a[200:203], a[28:31], 0\n"
    "v_mfma_f32_16x16x32_bf16 v[164:167], a[204:207], a[0:3], v[164:167]\n"
    "v_mfma_f32_16x16x32_bf16 v[164:167], a[208:211], a[4:7], v[164:167]\n"
    "v_exp_f32_e32 v160, v160\n"
    "v_mfma_f32_16x16x32_bf16 v[164:167], a[212:215], a[8:11], v[164:167]\n"
    "v_exp_f32_e32 v161, v161\n"
    "v_mfma_f32_16x16x32_bf16 v[164:167], a[216:219], a[12:15], v[164:167]\n"
    "v_exp_f32_e32 v162, v162\n"
    "v_mfma_f32_16x16x32_bf16 v[164:167], a[220:223], a[16:19], v[164:167]\n"
    "v_exp_f32_e32 v163, v163\n"
    "v_mfma_f32_16x16x32_bf16 v[164:167], a[224:227], a[20:23], v[164:167]\n"
    "v_cvt_pk_bf16_f32 v160, v160, v161\n"
    "v_mfma_f32_16x16x32_bf16 v[164:167], a[228:231], a[24:27], v[164:167]\n"
    "s_waitcnt vmcnt(8)\n"
    "s_barrier\n"
    "s_cmp_eq_u32 s50, 0\n"
    "s_cbranch_scc1 .Lat_qy\n"
    "ds_read_b128 a[60:63], v216 offset:7168\n"
    "ds_read_b128 a[32:35], v216 offset:0\n"
    "ds_read_b128 a[36:39], v216 offset:1024\n"
    "ds_read_b128 a[40:43], v216 offset:2048\n"
    "ds_read_b128 a[44:47], v216 offset:3072\n"
    "ds_read_b128 a[48:51], v216 offset:4096\n"
    "ds_read_b128 a[52:55], v216 offset:5120\n"
    "ds_read_b128 a[56:59], v216 offset:6144\n"
    ".Lat_qy:\n"
    "ds_read_b128 a[92:95], v216 offset:15360\n"
    "ds_read_b128 a[64:67], v216 offset:8192\n"
    "s_waitcnt lgkmcnt(8)\n"
    "v_mfma_f32_16x16x32_bf16 v[168:171], a[160:163], a[60:63], 0\n"
    "v_cvt_pk_bf16_f32 v161, v162, v163\n"
    "v_mfma_f32_16x16x32_bf16 v[168:171], a[164:167], a[32:35], v[168:171]\n"
    "s_waitcnt lgkmcnt(6)\n"
    "ds_read_b128 a[68:71], v216 offset:9216\n"
    "v_mfma_f32_16x16x32_bf16 v[168:171], a[168:171], a[36:39], v[168:171]\n"
    "ds_read_b128 a[72:75], v216 offset:10240\n"
    "v_mfma_f32_16x16x32_bf16 v[168:171], a[172:175], a[40:43], v[168:171]\n"
    "s_waitcnt lgkmcnt(6)\n"
    "ds_read_b128 a[76:79], v216 offset:11264\n"
    "ds_write_addtid_b32 v160 offset:0\n"
    "v_mfma_f32_16x16x32_bf16 v[168:171], a[176:179], a[44:47], v[168:171]\n"
    "v_exp_f32_e32 v164, v164\n"
    "v_mfma_f32_16x16x32_bf16 v[168:171], a[180:183], a[48:51], v[168:171]\n"
    "s_waitcnt lgkmcnt(6)\n"
    "ds_read_b128 a[80:83], v216 offset:12288\n"
    "v_mfma_f32_16x16x32_bf16 v[168:171], a[184:187], a[52:55], v[168:171]\n"
    "ds_read_b128 a[84:87], v216 offset:13312\n"
    "v_mfma_f32_16x16x32_bf16 v[168:171], a[188:191], a[56:59], v[168:171]\n"
    "ds_read_b128 a[88:91], v216 offset:14336\n"
    "ds_write_addtid_b32 v161 offset:256\n"
    "v_mfma_f32_16x16x32_bf16 v[172:175], a[200:203], a[60:63], 0\n"
    "v_exp_f32_e32 v165, v165\n"
    "v_mfma_f32_16x16x32_bf16 v[172:175], a[204:207], a[32:35], v[172:175]\n"
    "v_exp_f32_e32 v166, v166\n"
    "v_mfma_f32_16x16x32_bf16 v[172:175], a[208:211], a[36:39], v[172:175]\n"
    "v_exp_f32_e32 v167, v167\n"
    "v_mfma_f32_16x16x32_bf16 v[172:175], a[212:215], a[40:43], v[172:175]\n"
    "v_cvt_pk_bf16_f32 v162, v164, v165\n"
    "v_cvt_pk_bf16_f32 v163, v166, v167\n"
    "v_mfma_f32_16x16x32_bf16 v[172:175], a[216:219], a[44:47], v[172:175]\n"
    "ds_write_addtid_b32 v162 offset:512\n"
    "v_exp_f32_e32 v168, v168\n"
    "v_mfma_f32_16x16x32_bf16 v[172:175], a[220:223], a[48:51], v[172:175]\n"
    "v_exp_f32_e32 v169, v169\n"
    "v_mfma_f32_16x16x32_bf16 v[172:175], a[224:227], a[52:55], v[172:175]\n"
    "v_exp_f32_e32 v170, v170\n"
    "v_mfma_f32_16x16x32_bf16 v[172:175], a[228:231], a[56:59], v[172:175]\n"
    "s_waitcnt lgkmcnt(9)\n"
    "v_exp_f32_e32 v171, v171\n"
    "v_mfma_f32_16x16x32_bf16 v[176:179], a[160:163], a[92:95], 0\n"
    "ds_write_addtid_b32 v163 offset:768\n"
    "v_cvt_pk_bf16_f32 v168, v168, v169\n"
    "v_mfma_f32_16x16x32_bf16 v[176:179], a[164:167], a[64:67], v[176:179]\n"
    "s_waitcnt lgkmcnt(8)\n"
    "ds_write_addtid_b32 v168 offset:4096\n"
    "v_cvt_pk_bf16_f32 v169, v170, v171\n"
    "v_mfma_f32_16x16x32_bf16 v[176:179], a[168:171], a[68:71], v[176:179]\n"
    "v_exp_f32_e32 v172, v172\n"
    "v_mfma_f32_16x16x32_bf16 v[176:179], a[172:175], a[72:75], v[176:179]\n"
    "s_waitcnt lgkmcnt(6)\n"
    "v_exp_f32_e32 v173, v173\n"
    "v_mfma_f32_16x16x32_bf16 v[176:179], a[176:179], a[76:79], v[176:179]\n"
    "v_exp_f32_e32 v174, v174\n"
    "v_mfma_f32_16x16x32_bf16 v[176:179], a[180:183], a[80:83], v[176:179]\n"
    "s_waitcnt vmcnt(0)\n"
    "s_barrier\n"
    "s_waitcnt lgkmcnt(4)\n"
    "v_mfma_f32_16x16x32_bf16 v[176:179], a[184:187], a[84:87], v[176:179]\n"
    "ds_write_addtid_b32 v169 offset:4352\n"
    "v_exp_f32_e32 v175, v175\n"
    "v_mfma_f32_16x16x32_bf16 v[176:179], a[188:191], a[88:91], v[176:179]\n"
    "ds_read_b128 a[124:127], v216 offset:23552\n"
    "v_cvt_pk_bf16_f32 v170, v172, v173\n"
    "v_mfma_f32_16x16x32_bf16 v[180:183], a[200:203], a[92:95], 0\n"
    "ds_read_b128 a[96:99], v216 offset:16384\n"
    "v_cvt_pk_bf16_f32 v171, v174, v175\n"
    "v_mfma_f32_16x16x32_bf16 v[180:183], a[204:207], a[64:67], v[180:183]\n"
    "ds_read_b128 a[100:103], v216 offset:17408\n"
    "v_exp_f32_e32 v176, v176\n"
    "v_mfma_f32_16x16x32_bf16 v[180:183], a[208:211], a[68:71], v[180:183]\n"
    "ds_read_b128 a[104:107], v216 offset:18432\n"
    "ds_write_addtid_b32 v170 offset:4608\n"
    "v_mfma_f32_16x16x32_bf16 v[180:183], a[212:215], a[72:75], v[180:183]\n"
    "ds_read_b128 a[108:111], v216 offset:19456\n"
    "v_exp_f32_e32 v177, v177\n"
    "v_mfma_f32_16x16x32_bf16 v[180:183], a[216:219], a[76:79], v[180:183]\n"
    "ds_read_b128 a[112:115], v216 offset:20480\n"
    "v_exp_f32_e32 v178, v178\n"
    "v_mfma_f32_16x16x32_bf16 v[180:183], a[220:223], a[80:83], v[180:183]\n"
    "ds_read_b128 a[116:119], v216 offset:21504\n"
    "ds_write_addtid_b32 v171 offset:4864\n"
    "v_mfma_f32_16x16x32_bf16 v[180:183], a[224:227], a[84:87], v[180:183]\n"
    "ds_read_b128 a[120:123], v216 offset:22528\n"
    "v_mfma_f32_16x16x32_bf16 v[180:183], a[228:231], a[88:91], v[180:183]\n"
    "s_waitcnt lgkmcnt(8)\n"
    "s_sub_u32 m0, s63, 16384\n"
    "v_exp_f32_e32 v179, v179\n"
    "v_mfma_f32_16x16x32_bf16 v[184:187], a[160:163], a[124:127], 0\n"
    "ds_read_b128 a[156:159], v216 offset:31744\n"
    "buffer_load_dwordx4 v248, s[8:11], s40 offen offset:0 sc1 lds\n"
    "v_cvt_pk_bf16_f32 v176, v176, v177\n"
    "v_mfma_f32_16x16x32_bf16 v[184:187], a[164:167], a[96:99], v[184:187]\n"
    "s_waitcnt lgkmcnt(7)\n"
    "ds_read_b128 a[128:131], v216 offset:24576\n"
    "v_cvt_pk_bf16_f32 v177, v178, v179\n"
    "v_mfma_f32_16x16x32_bf16 v[184:187], a[168:171], a[100:103], v[184:187]\n"
    "ds_read_b128 a[132:135], v216 offset:25600\n"
    "v_mfma_f32_16x16x32_bf16 v[184:187], a[172:175], a[104:107], v[184:187]\n"
    "s_waitcnt lgkmcnt(6)\n"
    "ds_read_b128 a[136:139], v216 offset:26624\n"
    "v_mfma_f32_16x16x32_bf16 v[184:187], a[176:179], a[108:111], v[184:187]\n"
    "ds_read_b128 a[140:143], v216 offset:27648\n"
    "buffer_load_dwordx4 v248, s[8:11], s40 offen offset:1024 sc1 lds\n"
    "v_mfma_f32_16x16x32_bf16 v[184:187], a[180:183], a[112:115], v[184:187]\n"
    "s_waitcnt lgkmcnt(5)\n"
    "ds_read_b128 a[144:147], v216 offset:28672\n"
    "v_mfma_f32_16x16x32_bf16 v[184:187], a[184:187], a[116:119], v[184:187]\n"
    "ds_read_b128 a[148:151], v216 offset:29696\n"
    "v_mfma_f32_16x16x32_bf16 v[184:187], a[188:191], a[120:123], v[184:187]\n"
    "ds_read_b128 a[152:155], v216 offset:30720\n"
    "v_mfma_f32_16x16x32_bf16 v[188:191], a[200:203], a[124:127], 0\n"
    "buffer_load_dwordx4 v248, s[8:11], s40 offen offset:2048 sc1 lds\n"
    "v_exp_f32_e32 v180, v180\n"
    "v_mfma_f32_16x16x32_bf16 v[188:191], a[204:207], a[96:99], v[188:191]\n"
    "ds_write_b32 v249, v176 offset:8192\n"
    "v_exp_f32_e32 v181, v181\n"
    "v_mfma_f32_16x16x32_bf16 v[188:191], a[208:211], a[100:103], v[188:191]\n"
    "ds_write_b32 v249, v177 offset:8448\n"
    "v_exp_f32_e32 v182, v182\n"
    "v_mfma_f32_16x16x32_bf16 v[188:191], a[212:215], a[104:107], v[188:191]\n"
    "v_exp_f32_e32 v183, v183\n"
    "v_mfma_f32_16x16x32_bf16 v[188:191], a[216:219], a[108:111], v[188:191]\n"
    "buffer_load_dwordx4 v248, s[8:11], s40 offen offset:3072 sc1 lds\n"
    "v_cvt_pk_bf16_f32 v178, v180, v181\n"
    "v_cvt_pk_bf16_f32 v179, v182, v183\n"
    "v_mfma_f32_16x16x32_bf16 v[188:191], a[220:223], a[112:115], v[188:191]\n"
    "s_add_u32 s40, s40, 0x8000\n"
    "ds_write_b32 v249, v178 offset:8704\n"
    "v_exp_f32_e32 v184, v184\n"
    "v_mfma_f32_16x16x32_bf16 v[188:191], a[224:227], a[116:119], v[188:191]\n"
    "s_sub_u32 m0, s63, 12288\n"
    "ds_write_b32 v249, v179 offset:8960\n"
    "v_exp_f32_e32 v185, v185\n"
    "v_mfma_f32_16x16x32_bf16 v[188:191], a[228:231], a[120:123], v[188:191]\n"
    "s_waitcnt lgkmcnt(10)\n"
    "v_exp_f32_e32 v186, v186\n"
    "v_mfma_f32_16x16x32_bf16 v[192:195], a[160:163], a[156:159], 0\n"
    "buffer_load_dwordx4 v248, s[8:11], s41 offen offset:0 sc1 lds\n"
    "v_exp_f32_e32 v187, v187\n"
    "v_mfma_f32_16x16x32_bf16 v[192:195], a[164:167], a[128:131], v[192:195]\n"
    "s_waitcnt lgkmcnt(8)\n"
    "ds_read_b128 a[160:163], v203 offset:20480\n"
    "v_cvt_pk_bf16_f32 v184, v184, v185\n"
    "v_mfma_f32_16x16x32_bf16 v[192:195], a[168:171], a[132:135], v[192:195]\n"
    "ds_read_b128 a[164:167], v200 offset:16384\n"
    "v_cvt_pk_bf16_f32 v185, v186, v187\n"
    "v_mfma_f32_16x16x32_bf16 v[192:195], a[172:175], a[136:139], v[192:195]\n"
    "s_waitcnt lgkmcnt(8)\n"
    "ds_read_b128 a[168:171], v201 offset:16384\n"
    "v_exp_f32_e32 v188, v188\n"
    "v_mfma_f32_16x16x32_bf16 v[192:195], a[176:179], a[140:143], v[192:195]\n"
    "ds_read_b128 a[172:175], v200 offset:20480\n"
    "buffer_load_dwordx4 v248, s[8:11], s41 offen offset:1024 sc1 lds\n"
    "v_exp_f32_e32 v189, v189\n"
    "v_mfma_f32_16x16x32_bf16 v[192:195], a[180:183], a[144:147], v[192:195]\n"
    "s_waitcnt lgkmcnt(8)\n"
    "ds_read_b128 a[176:179], v201 offset:20480\n"
    "v_exp_f32_e32 v190, v190\n"
    "v_mfma_f32_16x16x32_bf16 v[192:195], a[184:187], a[148:151], v[192:195]\n"
    "ds_read_b128 a[180:183], v202 offset:16384\n"
    "v_mfma_f32_16x16x32_bf16 v[192:195], a[188:191], a[152:155], v[192:195]\n"
    "ds_read_b128 a[184:187], v203 offset:16384\n"
    "v_mfma_f32_16x16x32_bf16 v[196:199], a[200:203], a[156:159], 0\n"
    "ds_read_b128 a[188:191], v202 offset:20480\n"
    "buffer_load_dwordx4 v248, s[8:11], s41 offen offset:2048 sc1 lds\n"
    "v_mfma_f32_16x16x32_bf16 v[196:199], a[204:207], a[128:131], v[196:199]\n"
    "ds_write_b32 v249, v184 offset:12288\n"
    "v_exp_f32_e32 v191, v191\n"
    "v_mfma_f32_16x16x32_bf16 v[196:199], a[208:211], a[132:135], v[196:199]\n"
    "ds_write_b32 v249, v185 offset:12544\n"
    "v_cvt_pk_bf16_f32 v186, v188, v189\n"
    "v_cvt_pk_bf16_f32 v187, v190, v191\n"
    "v_mfma_f32_16x16x32_bf16 v[196:199], a[212:215], a[136:139], v[196:199]\n"
    "ds_write_b32 v249, v186 offset:12800\n"
    "v_exp_f32_e32 v192, v192\n"
    "v_mfma_f32_16x16x32_bf16 v[196:199], a[216:219], a[140:143], v[196:199]\n"
    "buffer_load_dwordx4 v248, s[8:11], s41 offen offset:3072 sc1 lds\n"
    "v_exp_f32_e32 v193, v193\n"
    "v_mfma_f32_16x16x32_bf16 v[196:199], a[220:223], a[144:147], v[196:199]\n"
    "s_add_u32 s41, s41, 0x8000\n"
    "ds_write_b32 v249, v187 offset:13056\n"
    "v_exp_f32_e32 v194, v194\n"
    "v_mfma_f32_16x16x32_bf16 v[196:199], a[224:227], a[148:151], v[196:199]\n"
    "v_exp_f32_e32 v195, v195\n"
    "v_mfma_f32_16x16x32_bf16 v[196:199], a[228:231], a[152:155], v[196:199]\n"
    "v_cvt_pk_bf16_f32 v192, v192, v193\n"
    "ds_write_b32 v249, v192 offset:16384\n"
    "v_cvt_pk_bf16_f32 v193, v194, v195\n"
    "ds_write_b32 v249, v193 offset:16640\n"
    "s_mov_b32 s44, 2\n"
    "s_cmp_lg_u32 s34, 0\n"
    "s_waitcnt lgkmcnt(4)\n"
    "s_nop 0\n"
    "v_exp_f32_e32 v196, v196\n"
    "v_exp_f32_e32 v197, v197\n"
    "v_exp_f32_e32 v198, v198\n"
    "v_exp_f32_e32 v199, v199\n"
    "v_cvt_pk_bf16_f32 v194, v196, v197\n"
    "v_cvt_pk_bf16_f32 v195, v198, v199\n"
    "ds_write_b32 v249, v194 offset:16896\n"
    "ds_write_b32 v249, v195 offset:17152\n"
    "s_cbranch_scc1 .Lat_p2e\n"
    ".p2align 6\n"
    ".Lat_p:\n"
    "v_mfma_f32_16x16x32_bf16 v[200:203], a[160:163], a[28:31], 0\n"
    "v_mfma_f32_16x16x32_bf16 v[200:203], a[164:167], a[0:3], v[200:203]\n"
    "v_mfma_f32_16x16x32_bf16 v[208:211], a[164:167], a[32:35], 0\n"
    "v_mfma_f32_16x16x32_bf16 v[208:211], a[160:163], a[60:63], v[208:211]\n"
    "v_mfma_f32_16x16x32_bf16 v[216:219], a[160:163], a[92:95], 0\n"
    "v_mfma_f32_16x16x32_bf16 v[216:219], a[164:167], a[64:67], v[216:219]\n"
    "s_waitcnt lgkmcnt(0)\n"
    "v_mfma_f32_16x16x32_bf16 v[224:227], a[164:167], a[96:99], 0\n"
    "s_barrier\n"
    "v_mfma_f32_16x16x32_bf16 v[224:227], a[160:163], a[124:127], v[224:227]\n"
    "s_add_u32 m0, s43, 49152\n"
    "ds_read2st64_b32 v[164:165], v250 offset0:0 offset1:1\n"
    "v_mfma_f32_16x16x32_bf16 v[232:235], a[160:163], a[156:159], 0\n"
    "ds_read_b128 a[160:163], v243 offset:22528\n"
    "v_mfma_f32_16x16x32_bf16 v[232:235], a[164:167], a[128:131], v[232:235]\n"
    "ds_read_b128 a[164:167], v240 offset:18432\n"
    "v_mfma_f32_16x16x32_bf16 v[232:235], a[168:171], a[132:135], v[232:235]\n"
    "buffer_load_dwordx4 v248, s[8:11], s40 offen offset:0 sc1 lds\n"
    "v_mfma_f32_16x16x32_bf16 v[232:235], a[172:175], a[136:139], v[232:235]\n"
    "v_mfma_f32_16x16x32_bf16 v[224:227], a[172:175], a[104:107], v[224:227]\n"
    "v_mfma_f32_16x16x32_bf16 v[224:227], a[168:171], a[100:103], v[224:227]\n"
    "ds_read2st64_b32 v[166:167], v250 offset0:2 offset1:3\n"
    "v_mfma_f32_16x16x32_bf16 v[216:219], a[168:171], a[68:71], v[216:219]\n"
    "v_mfma_f32_16x16x32_bf16 v[216:219], a[172:175], a[72:75], v[216:219]\n"
    "buffer_load_dwordx4 v248, s[8:11], s40 offen offset:1024 sc1 lds\n"
    "v_mfma_f32_16x16x32_bf16 v[208:211], a[172:175], a[40:43], v[208:211]\n"
    "v_mfma_f32_16x16x32_bf16 v[208:211], a[168:171], a[36:39], v[208:211]\n"
    "ds_read2st64_b32 v[172:173], v250 offset0:16 offset1:17\n"
    "v_mfma_f32_16x16x32_bf16 v[200:203], a[168:171], a[4:7], v[200:203]\n"
    "ds_read_b128 a[168:171], v241 offset:18432\n"
    "v_mfma_f32_16x16x32_bf16 v[200:203], a[172:175], a[8:11], v[200:203]\n"
    "ds_read_b128 a[172:175], v240 offset:22528\n"
    "v_mfma_f32_16x16x32_bf16 v[200:203], a[176:179], a[12:15], v[200:203]\n"
    "buffer_load_dwordx4 v248, s[8:11], s40 offen offset:2048 sc1 lds\n"
    "v_mfma_f32_16x16x32_bf16 v[200:203], a[180:183], a[16:19], v[200:203]\n"
    "ds_read2st64_b32 v[174:175], v250 offset0:18 offset1:19\n"
    "v_mfma_f32_16x16x32_bf16 v[208:211], a[180:183], a[48:51], v[208:211]\n"
    "v_mfma_f32_16x16x32_bf16 v[208:211], a[176:179], a[44:47], v[208:211]\n"
    "v_mfma_f32_16x16x32_bf16 v[216:219], a[176:179], a[76:79], v[216:219]\n"
    "ds_read2st64_b32 v[180:181], v250 offset0:32 offset1:33\n"
    "v_mfma_f32_16x16x32_bf16 v[216:219], a[180:183], a[80:83], v[216:219]\n"
    "buffer_load_dwordx4 v248, s[8:11], s40 offen offset:3072 sc1 lds\n"
    "v_mfma_f32_16x16x32_bf16 v[224:227], a[180:183], a[112:115], v[224:227]\n"
    "s_add_u32 m0, s43, 53248\n"
    "v_mfma_f32_16x16x32_bf16 v[224:227], a[176:179], a[108:111], v[224:227]\n"
    "s_add_u32 s40, s40, 0x8000\n"
    "ds_read2st64_b32 v[182:183], v250 offset0:34 offset1:35\n"
    "v_mfma_f32_16x16x32_bf16 v[232:235], a[176:179], a[140:143], v[232:235]\n"
    "ds_read_b128 a[176:179], v241 offset:22528\n"
    "v_mfma_f32_16x16x32_bf16 v[232:235], a[180:183], a[144:147], v[232:235]\n"
    "ds_read_b128 a[180:183], v242 offset:18432\n"
    "v_mfma_f32_16x16x32_bf16 v[232:235], a[184:187], a[148:151], v[232:235]\n"
    "buffer_load_dwordx4 v248, s[8:11], s41 offen offset:0 sc1 lds\n"
    "v_mfma_f32_16x16x32_bf16 v[232:235], a[188:191], a[152:155], v[232:235]\n"
    "v_mfma_f32_16x16x32_bf16 v[224:227], a[188:191], a[120:123], v[224:227]\n"
    "ds_read2st64_b32 v[188:189], v250 offset0:48 offset1:49\n"
    "v_mfma_f32_16x16x32_bf16 v[224:227], a[184:187], a[116:119], v[224:227]\n"
    "v_mfma_f32_16x16x32_bf16 v[216:219], a[184:187], a[84:87], v[216:219]\n"
    "v_exp_f32_e32 v232, v232\n"
    "v_mfma_f32_16x16x32_bf16 v[216:219], a[188:191], a[88:91], v[216:219]\n"
    "buffer_load_dwordx4 v248, s[8:11], s41 offen offset:1024 sc1 lds\n"
    "v_mfma_f32_16x16x32_bf16 v[208:211], a[188:191], a[56:59], v[208:211]\n"
    "ds_read2st64_b32 v[190:191], v250 offset0:50 offset1:51\n"
    "v_mfma_f32_16x16x32_bf16 v[208:211], a[184:187], a[52:55], v[208:211]\n"
    "v_exp_f32_e32 v233, v233\n"
    "v_mfma_f32_16x16x32_bf16 v[200:203], a[184:187], a[20:23], v[200:203]\n"
    "ds_read_b128 a[184:187], v243 offset:18432\n"
    "v_mfma_f32_16x16x32_bf16 v[200:203], a[188:191], a[24:27], v[200:203]\n"
    "s_waitcnt lgkmcnt(8)\n"
    "ds_read_b128 a[188:191], v242 offset:22528\n"
    "v_mfma_f32_16x16x32_bf16 v[204:207], a[160:163], a[28:31], 0\n"
    "buffer_load_dwordx4 v248, s[8:11], s41 offen offset:2048 sc1 lds\n"
    "v_mfma_f32_16x16x32_bf16 v[204:207], a[164:167], a[0:3], v[204:207]\n"
    "ds_read2st64_b32 v[196:197], v250 offset0:64 offset1:65\n"
    "v_mfma_f32_16x16x32_bf16 v[212:215], a[164:167], a[32:35], 0\n"
    "v_exp_f32_e32 v234, v234\n"
    "v_mfma_f32_16x16x32_bf16 v[212:215], a[160:163], a[60:63], v[212:215]\n"
    "ds_read2st64_b32 v[198:199], v250 offset0:66 offset1:67\n"
    "v_mfma_f32_16x16x32_bf16 v[220:223], a[160:163], a[92:95], 0\n"
    "v_exp_f32_e32 v235, v235\n"
    "v_mfma_f32_16x16x32_bf16 v[220:223], a[164:167], a[64:67], v[220:223]\n"
    "buffer_load_dwordx4 v248, s[8:11], s41 offen offset:3072 sc1 lds\n"
    "v_mfma_f32_16x16x32_bf16 v[228:231], a[164:167], a[96:99], 0\n"
    "s_add_u32 s41, s41, 0x8000\n"
    "v_cvt_pk_bf16_f32 v232, v232, v233\n"
    "v_mfma_f32_16x16x32_bf16 v[228:231], a[160:163], a[124:127], v[228:231]\n"
    "v_exp_f32_e32 v224, v224\n"
    "v_mfma_f32_16x16x32_bf16 v[236:239], a[160:163], a[156:159], 0\n"
    "ds_read_b64_tr_b16 a[160:161], v244 offset:0\n"
    "v_mfma_f32_16x16x32_bf16 v[236:239], a[164:167], a[128:131], v[236:239]\n"
    "ds_read_b64_tr_b16 a[162:163], v244 offset:2048\n"
    "v_mfma_f32_16x16x32_bf16 v[236:239], a[168:171], a[132:135], v[236:239]\n"
    "ds_read_b64_tr_b16 a[164:165], v244 offset:8192\n"
    "v_mfma_f32_16x16x32_bf16 v[236:239], a[172:175], a[136:139], v[236:239]\n"
    "ds_read_b64_tr_b16 a[166:167], v244 offset:10240\n"
    "v_mfma_f32_16x16x32_bf16 v[228:231], a[172:175], a[104:107], v[228:231]\n"
    "v_exp_f32_e32 v225, v225\n"
    "v_mfma_f32_16x16x32_bf16 v[228:231], a[168:171], a[100:103], v[228:231]\n"
    "v_cvt_pk_bf16_f32 v233, v234, v235\n"
    "v_mfma_f32_16x16x32_bf16 v[220:223], a[168:171], a[68:71], v[220:223]\n"
    "v_exp_f32_e32 v226, v226\n"
    "v_mfma_f32_16x16x32_bf16 v[220:223], a[172:175], a[72:75], v[220:223]\n"
    "v_exp_f32_e32 v227, v227\n"
    "v_mfma_f32_16x16x32_bf16 v[212:215], a[172:175], a[40:43], v[212:215]\n"
    "v_cvt_pk_bf16_f32 v224, v224, v225\n"
    "v_mfma_f32_16x16x32_bf16 v[212:215], a[168:171], a[36:39], v[212:215]\n"
    "v_exp_f32_e32 v216, v216\n"
    "v_mfma_f32_16x16x32_bf16 v[204:207], a[168:171], a[4:7], v[204:207]\n"
    "ds_read_b64_tr_b16 a[168:169], v245 offset:0\n"
    "v_mfma_f32_16x16x32_bf16 v[204:207], a[172:175], a[8:11], v[204:207]\n"
    "s_waitcnt lgkmcnt(7)\n"
    "ds_read_b64_tr_b16 a[170:171], v245 offset:2048\n"
    "v_mfma_f32_16x16x32_bf16 v[204:207], a[176:179], a[12:15], v[204:207]\n"
    "ds_read_b64_tr_b16 a[172:173], v245 offset:8192\n"
    "v_mfma_f32_16x16x32_bf16 v[204:207], a[180:183], a[16:19], v[204:207]\n"
    "ds_read_b64_tr_b16 a[174:175], v245 offset:10240\n"
    "v_mfma_f32_16x16x32_bf16 v[212:215], a[180:183], a[48:51], v[212:215]\n"
    "v_exp_f32_e32 v217, v217\n"
    "v_mfma_f32_16x16x32_bf16 v[212:215], a[176:179], a[44:47], v[212:215]\n"
    "v_cvt_pk_bf16_f32 v225, v226, v227\n"
    "v_mfma_f32_16x16x32_bf16 v[220:223], a[176:179], a[76:79], v[220:223]\n"
    "v_exp_f32_e32 v218, v218\n"
    "v_mfma_f32_16x16x32_bf16 v[220:223], a[180:183], a[80:83], v[220:223]\n"
    "v_exp_f32_e32 v219, v219\n"
    "v_mfma_f32_16x16x32_bf16 v[228:231], a[180:183], a[112:115], v[228:231]\n"
    "v_cvt_pk_bf16_f32 v216, v216, v217\n"
    "v_mfma_f32_16x16x32_bf16 v[228:231], a[176:179], a[108:111], v[228:231]\n"
    "v_exp_f32_e32 v208, v208\n"
    "v_mfma_f32_16x16x32_bf16 v[236:239], a[176:179], a[140:143], v[236:239]\n"
    "ds_read_b64_tr_b16 a[176:177], v246 offset:0\n"
    "v_mfma_f32_16x16x32_bf16 v[236:239], a[180:183], a[144:147], v[236:239]\n"
    "ds_read_b64_tr_b16 a[178:179], v246 offset:2048\n"
    "v_mfma_f32_16x16x32_bf16 v[236:239], a[184:187], a[148:151], v[236:239]\n"
    "ds_read_b64_tr_b16 a[180:181], v246 offset:8192\n"
    "v_mfma_f32_16x16x32_bf16 v[236:239], a[188:191], a[152:155], v[236:239]\n"
    "ds_read_b64_tr_b16 a[182:183], v246 offset:10240\n"
    "v_mfma_f32_16x16x32_bf16 v[228:231], a[188:191], a[120:123], v[228:231]\n"
    "v_exp_f32_e32 v209, v209\n"
    "v_mfma_f32_16x16x32_bf16 v[228:231], a[184:187], a[116:119], v[228:231]\n"
    "v_cvt_pk_bf16_f32 v217, v218, v219\n"
    "v_mfma_f32_16x16x32_bf16 v[220:223], a[184:187], a[84:87], v[220:223]\n"
    "v_exp_f32_e32 v210, v210\n"
    "v_mfma_f32_16x16x32_bf16 v[220:223], a[188:191], a[88:91], v[220:223]\n"
    "v_exp_f32_e32 v211, v211\n"
    "v_mfma_f32_16x16x32_bf16 v[212:215], a[188:191], a[56:59], v[212:215]\n"
    "v_cvt_pk_bf16_f32 v208, v208, v209\n"
    "v_mfma_f32_16x16x32_bf16 v[212:215], a[184:187], a[52:55], v[212:215]\n"
    "v_exp_f32_e32 v200, v200\n"
    "v_mfma_f32_16x16x32_bf16 v[204:207], a[184:187], a[20:23], v[204:207]\n"
    "ds_read_b64_tr_b16 a[184:185], v247 offset:0\n"
    "v_mfma_f32_16x16x32_bf16 v[204:207], a[188:191], a[24:27], v[204:207]\n"
    "s_waitcnt lgkmcnt(5)\n"
    "ds_read_b64_tr_b16 a[186:187], v247 offset:2048\n"
    "v_mfma_f32_16x16x32_bf16 v[0:3], a[160:163], v[160:163], v[0:3]\n"
    "ds_read_b64_tr_b16 a[188:189], v247 offset:8192\n"
    "v_mfma_f32_16x16x32_bf16 v[0:3], a[164:167], v[164:167], v[0:3]\n"
    "ds_read_b64_tr_b16 a[190:191], v247 offset:10240\n"
    "v_mfma_f32_16x16x32_bf16 a[248:251], a[164:167], v[172:175], a[248:251]\n"
    "v_exp_f32_e32 v201, v201\n"
    "v_mfma_f32_16x16x32_bf16 a[248:251], a[160:163], v[168:171], a[248:251]\n"
    "v_cvt_pk_bf16_f32 v209, v210, v211\n"
    "v_mfma_f32_16x16x32_bf16 v[56:59], a[160:163], v[176:179], v[56:59]\n"
    "v_exp_f32_e32 v202, v202\n"
    "v_mfma_f32_16x16x32_bf16 v[56:59], a[164:167], v[180:183], v[56:59]\n"
    "v_exp_f32_e32 v203, v203\n"
    "v_mfma_f32_16x16x32_bf16 v[60:63], a[164:167], v[188:191], v[60:63]\n"
    "v_cvt_pk_bf16_f32 v200, v200, v201\n"
    "v_mfma_f32_16x16x32_bf16 v[60:63], a[160:163], v[184:187], v[60:63]\n"
    "v_cvt_pk_bf16_f32 v201, v202, v203\n"
    "v_mfma_f32_16x16x32_bf16 v[64:67], a[160:163], v[192:195], v[64:67]\n"
    "ds_read_b64_tr_b16 a[160:161], v244 offset:4096\n"
    "v_mfma_f32_16x16x32_bf16 v[64:67], a[164:167], v[196:199], v[64:67]\n"
    "ds_read_b64_tr_b16 a[162:163], v244 offset:6144\n"
    "v_mfma_f32_16x16x32_bf16 v[76:79], a[168:171], v[192:195], v[76:79]\n"
    "ds_read_b64_tr_b16 a[164:165], v244 offset:12288\n"
    "v_mfma_f32_16x16x32_bf16 v[76:79], a[172:175], v[196:199], v[76:79]\n"
    "ds_read_b64_tr_b16 a[166:167], v244 offset:14336\n"
    "v_mfma_f32_16x16x32_bf16 v[72:75], a[172:175], v[188:191], v[72:75]\n"
    "v_exp_f32_e32 v236, v236\n"
    "v_mfma_f32_16x16x32_bf16 v[72:75], a[168:171], v[184:187], v[72:75]\n"
    "v_exp_f32_e32 v237, v237\n"
    "v_mfma_f32_16x16x32_bf16 v[68:71], a[168:171], v[176:179], v[68:71]\n"
    "v_exp_f32_e32 v238, v238\n"
    "v_mfma_f32_16x16x32_bf16 v[68:71], a[172:175], v[180:183], v[68:71]\n"
    "v_exp_f32_e32 v239, v239\n"
    "v_mfma_f32_16x16x32_bf16 a[252:255], a[172:175], v[172:175], a[252:255]\n"
    "v_cvt_pk_bf16_f32 v234, v236, v237\n"
    "v_mfma_f32_16x16x32_bf16 a[252:255], a[168:171], v[168:171], a[252:255]\n"
    "v_exp_f32_e32 v228, v228\n"
    "v_mfma_f32_16x16x32_bf16 v[4:7], a[168:171], v[160:163], v[4:7]\n"
    "ds_read_b64_tr_b16 a[168:169], v245 offset:4096\n"
    "v_mfma_f32_16x16x32_bf16 v[4:7], a[172:175], v[164:167], v[4:7]\n"
    "s_waitcnt lgkmcnt(5)\n"
    "ds_read_b64_tr_b16 a[170:171], v245 offset:6144\n"
    "v_mfma_f32_16x16x32_bf16 v[8:11], a[176:179], v[160:163], v[8:11]\n"
    "ds_read_b64_tr_b16 a[172:173], v245 offset:12288\n"
    "v_mfma_f32_16x16x32_bf16 v[8:11], a[180:183], v[164:167], v[8:11]\n"
    "ds_read_b64_tr_b16 a[174:175], v245 offset:14336\n"
    "v_mfma_f32_16x16x32_bf16 v[32:35], a[180:183], v[172:175], v[32:35]\n"
    "v_exp_f32_e32 v229, v229\n"
    "v_mfma_f32_16x16x32_bf16 v[32:35], a[176:179], v[168:171], v[32:35]\n"
    "v_cvt_pk_bf16_f32 v235, v238, v239\n"
    "v_mfma_f32_16x16x32_bf16 v[80:83], a[176:179], v[176:179], v[80:83]\n"
    "v_exp_f32_e32 v230, v230\n"
    "v_mfma_f32_16x16x32_bf16 v[80:83], a[180:183], v[180:183], v[80:83]\n"
    "v_exp_f32_e32 v231, v231\n"
    "v_mfma_f32_16x16x32_bf16 v[84:87], a[180:183], v[188:191], v[84:87]\n"
    "v_cvt_pk_bf16_f32 v226, v228, v229\n"
    "v_mfma_f32_16x16x32_bf16 v[84:87], a[176:179], v[184:187], v[84:87]\n"
    "v_exp_f32_e32 v220, v220\n"
    "v_mfma_f32_16x16x32_bf16 v[88:91], a[176:179], v[192:195], v[88:91]\n"
    "ds_read_b64_tr_b16 a[176:177], v246 offset:4096\n"
    "v_mfma_f32_16x16x32_bf16 v[88:91], a[180:183], v[196:199], v[88:91]\n"
    "ds_read_b64_tr_b16 a[178:179], v246 offset:6144\n"
    "v_mfma_f32_16x16x32_bf16 v[100:103], a[184:187], v[192:195], v[100:103]\n"
    "ds_read_b64_tr_b16 a[180:181], v246 offset:12288\n"
    "v_mfma_f32_16x16x32_bf16 v[100:103], a[188:191], v[196:199], v[100:103]\n"
    "ds_read_b64_tr_b16 a[182:183], v246 offset:14336\n"
    "v_mfma_f32_16x16x32_bf16 v[96:99], a[188:191], v[188:191], v[96:99]\n"
    "v_exp_f32_e32 v221, v221\n"
    "v_mfma_f32_16x16x32_bf16 v[96:99], a[184:187], v[184:187], v[96:99]\n"
    "v_cvt_pk_bf16_f32 v227, v230, v231\n"
    "v_mfma_f32_16x16x32_bf16 v[92:95], a[184:187], v[176:179], v[92:95]\n"
    "v_exp_f32_e32 v222, v222\n"
    "v_mfma_f32_16x16x32_bf16 v[92:95], a[188:191], v[180:183], v[92:95]\n"
    "v_exp_f32_e32 v223, v223\n"
    "v_mfma_f32_16x16x32_bf16 v[36:39], a[188:191], v[172:175], v[36:39]\n"
    "v_cvt_pk_bf16_f32 v218, v220, v221\n"
    "v_mfma_f32_16x16x32_bf16 v[36:39], a[184:187], v[168:171], v[36:39]\n"
    "v_exp_f32_e32 v212, v212\n"
    "v_mfma_f32_16x16x32_bf16 v[12:15], a[184:187], v[160:163], v[12:15]\n"
    "s_mov_b32 m0, s49\n"
    "ds_read_b64_tr_b16 a[184:185], v247 offset:4096\n"
    "v_mfma_f32_16x16x32_bf16 v[12:15], a[188:191], v[164:167], v[12:15]\n"
    "s_waitcnt lgkmcnt(5)\n"
    "ds_read_b64_tr_b16 a[186:187], v247 offset:6144\n"
    "v_mfma_f32_16x16x32_bf16 v[16:19], a[160:163], v[160:163], v[16:19]\n"
    "ds_read_b64_tr_b16 a[188:189], v247 offset:12288\n"
    "v_mfma_f32_16x16x32_bf16 v[16:19], a[164:167], v[164:167], v[16:19]\n"
    "ds_read_b64_tr_b16 a[190:191], v247 offset:14336\n"
    "v_mfma_f32_16x16x32_bf16 v[40:43], a[164:167], v[172:175], v[40:43]\n"
    "v_exp_f32_e32 v213, v213\n"
    "v_mfma_f32_16x16x32_bf16 v[40:43], a[160:163], v[168:171], v[40:43]\n"
    "v_cvt_pk_bf16_f32 v219, v222, v223\n"
    "v_mfma_f32_16x16x32_bf16 v[104:107], a[160:163], v[176:179], v[104:107]\n"
    "v_exp_f32_e32 v214, v214\n"
    "v_mfma_f32_16x16x32_bf16 v[104:107], a[164:167], v[180:183], v[104:107]\n"
    "s_waitcnt vmcnt(8)\n"
    "v_mfma_f32_16x16x32_bf16 v[108:111], a[164:167], v[188:191], v[108:111]\n"
    "s_barrier\n"
    "v_mfma_f32_16x16x32_bf16 v[108:111], a[160:163], v[184:187], v[108:111]\n"
    "v_exp_f32_e32 v215, v215\n"
    "v_mfma_f32_16x16x32_bf16 v[112:115], a[160:163], v[192:195], v[112:115]\n"
    "ds_read_b128 a[160:163], v243 offset:36864\n"
    "v_mfma_f32_16x16x32_bf16 v[112:115], a[164:167], v[196:199], v[112:115]\n"
    "ds_read_b128 a[164:167], v240 offset:32768\n"
    "v_mfma_f32_16x16x32_bf16 v[124:127], a[168:171], v[192:195], v[124:127]\n"
    "ds_write_addtid_b32 v232 offset:16384\n"
    "v_cvt_pk_bf16_f32 v210, v212, v213\n"
    "v_mfma_f32_16x16x32_bf16 v[124:127], a[172:175], v[196:199], v[124:127]\n"
    "v_exp_f32_e32 v204, v204\n"
    "v_mfma_f32_16x16x32_bf16 v[120:123], a[172:175], v[188:191], v[120:123]\n"
    "v_exp_f32_e32 v205, v205\n"
    "v_mfma_f32_16x16x32_bf16 v[120:123], a[168:171], v[184:187], v[120:123]\n"
    "ds_write_addtid_b32 v233 offset:16640\n"
    "v_cvt_pk_bf16_f32 v211, v214, v215\n"
    "v_mfma_f32_16x16x32_bf16 v[116:119], a[168:171], v[176:179], v[116:119]\n"
    "v_exp_f32_e32 v206, v206\n"
    "v_mfma_f32_16x16x32_bf16 v[116:119], a[172:175], v[180:183], v[116:119]\n"
    "v_exp_f32_e32 v207, v207\n"
    "v_mfma_f32_16x16x32_bf16 v[44:47], a[172:175], v[172:175], v[44:47]\n"
    "ds_write_addtid_b32 v224 offset:12288\n"
    "v_cvt_pk_bf16_f32 v202, v204, v205\n"
    "v_mfma_f32_16x16x32_bf16 v[44:47], a[168:171], v[168:171], v[44:47]\n"
    "ds_write_addtid_b32 v225 offset:12544\n"
    "v_cvt_pk_bf16_f32 v203, v206, v207\n"
    "v_mfma_f32_16x16x32_bf16 v[20:23], a[168:171], v[160:163], v[20:23]\n"
    "ds_read_b128 a[168:171], v241 offset:32768\n"
    "v_mfma_f32_16x16x32_bf16 v[20:23], a[172:175], v[164:167], v[20:23]\n"
    "s_waitcnt lgkmcnt(7)\n"
    "ds_read_b128 a[172:175], v240 offset:36864\n"
    "v_mfma_f32_16x16x32_bf16 v[24:27], a[176:179], v[160:163], v[24:27]\n"
    "ds_write_addtid_b32 v216 offset:8192\n"
    "v_mfma_f32_16x16x32_bf16 v[24:27], a[180:183], v[164:167], v[24:27]\n"
    "ds_write_addtid_b32 v217 offset:8448\n"
    "v_mfma_f32_16x16x32_bf16 v[48:51], a[180:183], v[172:175], v[48:51]\n"
    "ds_write_addtid_b32 v208 offset:4096\n"
    "v_mfma_f32_16x16x32_bf16 v[48:51], a[176:179], v[168:171], v[48:51]\n"
    "ds_write_addtid_b32 v209 offset:4352\n"
    "v_mfma_f32_16x16x32_bf16 v[128:131], a[176:179], v[176:179], v[128:131]\n"
    "ds_write_addtid_b32 v200 offset:0\n"
    "v_mfma_f32_16x16x32_bf16 v[128:131], a[180:183], v[180:183], v[128:131]\n"
    "ds_write_addtid_b32 v201 offset:256\n"
    "v_mfma_f32_16x16x32_bf16 v[132:135], a[180:183], v[188:191], v[132:135]\n"
    "ds_write_addtid_b32 v234 offset:16896\n"
    "v_mfma_f32_16x16x32_bf16 v[132:135], a[176:179], v[184:187], v[132:135]\n"
    "ds_write_addtid_b32 v235 offset:17152\n"
    "v_mfma_f32_16x16x32_bf16 v[136:139], a[176:179], v[192:195], v[136:139]\n"
    "ds_read_b128 a[176:179], v241 offset:36864\n"
    "v_mfma_f32_16x16x32_bf16 v[136:139], a[180:183], v[196:199], v[136:139]\n"
    "ds_read_b128 a[180:183], v242 offset:32768\n"
    "v_mfma_f32_16x16x32_bf16 v[148:151], a[184:187], v[192:195], v[148:151]\n"
    "ds_write_addtid_b32 v226 offset:12800\n"
    "v_mfma_f32_16x16x32_bf16 v[148:151], a[188:191], v[196:199], v[148:151]\n"
    "ds_write_addtid_b32 v227 offset:13056\n"
    "v_mfma_f32_16x16x32_bf16 v[144:147], a[188:191], v[188:191], v[144:147]\n"
    "ds_write_addtid_b32 v218 offset:8704\n"
    "v_mfma_f32_16x16x32_bf16 v[144:147], a[184:187], v[184:187], v[144:147]\n"
    "ds_write_addtid_b32 v219 offset:8960\n"
    "v_mfma_f32_16x16x32_bf16 v[140:143], a[184:187], v[176:179], v[140:143]\n"
    "ds_write_addtid_b32 v210 offset:4608\n"
    "v_mfma_f32_16x16x32_bf16 v[140:143], a[188:191], v[180:183], v[140:143]\n"
    "ds_write_addtid_b32 v211 offset:4864\n"
    "v_mfma_f32_16x16x32_bf16 v[52:55], a[188:191], v[172:175], v[52:55]\n"
    "ds_write_addtid_b32 v202 offset:512\n"
    "v_mfma_f32_16x16x32_bf16 v[52:55], a[184:187], v[168:171], v[52:55]\n"
    "ds_write_addtid_b32 v203 offset:768\n"
    "v_mfma_f32_16x16x32_bf16 v[28:31], a[184:187], v[160:163], v[28:31]\n"
    "ds_read_b128 a[184:187], v243 offset:32768\n"
    "v_mfma_f32_16x16x32_bf16 v[28:31], a[188:191], v[164:167], v[28:31]\n"
    "s_waitcnt lgkmcnt(15)\n"
    "ds_read_b128 a[188:191], v242 offset:36864\n"
    "v_mfma_f32_16x16x32_bf16 v[160:163], a[160:163], a[28:31], 0\n"
    "v_mfma_f32_16x16x32_bf16 v[160:163], a[164:167], a[0:3], v[160:163]\n"
    "v_mfma_f32_16x16x32_bf16 v[168:171], a[164:167], a[32:35], 0\n"
    "v_mfma_f32_16x16x32_bf16 v[168:171], a[160:163], a[60:63], v[168:171]\n"
    "v_mfma_f32_16x16x32_bf16 v[176:179], a[160:163], a[92:95], 0\n"
    "v_mfma_f32_16x16x32_bf16 v[176:179], a[164:167], a[64:67], v[176:179]\n"
    "s_waitcnt lgkmcnt(0)\n"
    "v_mfma_f32_16x16x32_bf16 v[184:187], a[164:167], a[96:99], 0\n"
    "s_barrier\n"
    "v_mfma_f32_16x16x32_bf16 v[184:187], a[160:163], a[124:127], v[184:187]\n"
    "s_add_u32 m0, s43, 0\n"
    "ds_read2st64_b32 v[204:205], v250 offset0:0 offset1:1\n"
    "v_mfma_f32_16x16x32_bf16 v[192:195], a[160:163], a[156:159], 0\n"
    "ds_read_b128 a[160:163], v243 offset:38912\n"
    "v_mfma_f32_16x16x32_bf16 v[192:195], a[164:167], a[128:131], v[192:195]\n"
    "ds_read_b128 a[164:167], v240 offset:34816\n"
    "v_mfma_f32_16x16x32_bf16 v[192:195], a[168:171], a[132:135], v[192:195]\n"
    "buffer_load_dwordx4 v248, s[8:11], s40 offen offset:0 sc1 lds\n"
    "v_mfma_f32_16x16x32_bf16 v[192:195], a[172:175], a[136:139], v[192:195]\n"
    "v_mfma_f32_16x16x32_bf16 v[184:187], a[172:175], a[104:107], v[184:187]\n"
    "v_mfma_f32_16x16x32_bf16 v[184:187], a[168:171], a[100:103], v[184:187]\n"
    "ds_read2st64_b32 v[206:207], v250 offset0:2 offset1:3\n"
    "v_mfma_f32_16x16x32_bf16 v[176:179], a[168:171], a[68:71], v[176:179]\n"
    "v_mfma_f32_16x16x32_bf16 v[176:179], a[172:175], a[72:75], v[176:179]\n"
    "buffer_load_dwordx4 v248, s[8:11], s40 offen offset:1024 sc1 lds\n"
    "v_mfma_f32_16x16x32_bf16 v[168:171], a[172:175], a[40:43], v[168:171]\n"
    "v_mfma_f32_16x16x32_bf16 v[168:171], a[168:171], a[36:39], v[168:171]\n"
    "ds_read2st64_b32 v[212:213], v250 offset0:16 offset1:17\n"
    "v_mfma_f32_16x16x32_bf16 v[160:163], a[168:171], a[4:7], v[160:163]\n"
    "ds_read_b128 a[168:171], v241 offset:34816\n"
    "v_mfma_f32_16x16x32_bf16 v[160:163], a[172:175], a[8:11], v[160:163]\n"
    "ds_read_b128 a[172:175], v240 offset:38912\n"
    "v_mfma_f32_16x16x32_bf16 v[160:163], a[176:179], a[12:15], v[160:163]\n"
    "buffer_load_dwordx4 v248, s[8:11], s40 offen offset:2048 sc1 lds\n"
    "v_mfma_f32_16x16x32_bf16 v[160:163], a[180:183], a[16:19], v[160:163]\n"
    "ds_read2st64_b32 v[214:215], v250 offset0:18 offset1:19\n"
    "v_mfma_f32_16x16x32_bf16 v[168:171], a[180:183], a[48:51], v[168:171]\n"
    "v_mfma_f32_16x16x32_bf16 v[168:171], a[176:179], a[44:47], v[168:171]\n"
    "v_mfma_f32_16x16x32_bf16 v[176:179], a[176:179], a[76:79], v[176:179]\n"
    "ds_read2st64_b32 v[220:221], v250 offset0:32 offset1:33\n"
    "v_mfma_f32_16x16x32_bf16 v[176:179], a[180:183], a[80:83], v[176:179]\n"
    "buffer_load_dwordx4 v248, s[8:11], s40 offen offset:3072 sc1 lds\n"
    "v_mfma_f32_16x16x32_bf16 v[184:187], a[180:183], a[112:115], v[184:187]\n"
    "s_add_u32 m0, s43, 4096\n"
    "v_mfma_f32_16x16x32_bf16 v[184:187], a[176:179], a[108:111], v[184:187]\n"
    "s_add_u32 s40, s40, 0x8000\n"
    "ds_read2st64_b32 v[222:223], v250 offset0:34 offset1:35\n"
    "v_mfma_f32_16x16x32_bf16 v[192:195], a[176:179], a[140:143], v[192:195]\n"
    "ds_read_b128 a[176:179], v241 offset:38912\n"
    "v_mfma_f32_16x16x32_bf16 v[192:195], a[180:183], a[144:147], v[192:195]\n"
    "ds_read_b128 a[180:183], v242 offset:34816\n"
    "v_mfma_f32_16x16x32_bf16 v[192:195], a[184:187], a[148:151], v[192:195]\n"
    "buffer_load_dwordx4 v248, s[8:11], s41 offen offset:0 sc1 lds\n"
    "v_mfma_f32_16x16x32_bf16 v[192:195], a[188:191], a[152:155], v[192:195]\n"
    "v_mfma_f32_16x16x32_bf16 v[184:187], a[188:191], a[120:123], v[184:187]\n"
    "ds_read2st64_b32 v[228:229], v250 offset0:48 offset1:49\n"
    "v_mfma_f32_16x16x32_bf16 v[184:187], a[184:187], a[116:119], v[184:187]\n"
    "v_mfma_f32_16x16x32_bf16 v[176:179], a[184:187], a[84:87], v[176:179]\n"
    "v_exp_f32_e32 v192, v192\n"
    "v_mfma_f32_16x16x32_bf16 v[176:179], a[188:191], a[88:91], v[176:179]\n"
    "buffer_load_dwordx4 v248, s[8:11], s41 offen offset:1024 sc1 lds\n"
    "v_mfma_f32_16x16x32_bf16 v[168:171], a[188:191], a[56:59], v[168:171]\n"
    "ds_read2st64_b32 v[230:231], v250 offset0:50 offset1:51\n"
    "v_mfma_f32_16x16x32_bf16 v[168:171], a[184:187], a[52:55], v[168:171]\n"
    "v_exp_f32_e32 v193, v193\n"
    "v_mfma_f32_16x16x32_bf16 v[160:163], a[184:187], a[20:23], v[160:163]\n"
    "ds_read_b128 a[184:187], v243 offset:34816\n"
    "v_mfma_f32_16x16x32_bf16 v[160:163], a[188:191], a[24:27], v[160:163]\n"
    "s_waitcnt lgkmcnt(8)\n"
    "ds_read_b128 a[188:191], v242 offset:38912\n"
    "v_mfma_f32_16x16x32_bf16 v[164:167], a[160:163], a[28:31], 0\n"
    "buffer_load_dwordx4 v248, s[8:11], s41 offen offset:2048 sc1 lds\n"
    "v_mfma_f32_16x16x32_bf16 v[164:167], a[164:167], a[0:3], v[164:167]\n"
    "ds_read2st64_b32 v[236:237], v250 offset0:64 offset1:65\n"
    "v_mfma_f32_16x16x32_bf16 v[172:175], a[164:167], a[32:35], 0\n"
    "v_exp_f32_e32 v194, v194\n"
    "v_mfma_f32_16x16x32_bf16 v[172:175], a[160:163], a[60:63], v[172:175]\n"
    "ds_read2st64_b32 v[238:239], v250 offset0:66 offset1:67\n"
    "v_mfma_f32_16x16x32_bf16 v[180:183], a[160:163], a[92:95], 0\n"
    "v_exp_f32_e32 v195, v195\n"
    "v_mfma_f32_16x16x32_bf16 v[180:183], a[164:167], a[64:67], v[180:183]\n"
    "buffer_load_dwordx4 v248, s[8:11], s41 offen offset:3072 sc1 lds\n"
    "v_mfma_f32_16x16x32_bf16 v[188:191], a[164:167], a[96:99], 0\n"
    "s_add_u32 s41, s41, 0x8000\n"
    "v_cvt_pk_bf16_f32 v192, v192, v193\n"
    "v_mfma_f32_16x16x32_bf16 v[188:191], a[160:163], a[124:127], v[188:191]\n"
    "v_exp_f32_e32 v184, v184\n"
    "v_mfma_f32_16x16x32_bf16 v[196:199], a[160:163], a[156:159], 0\n"
    "ds_read_b64_tr_b16 a[160:161], v244 offset:16384\n"
    "v_mfma_f32_16x16x32_bf16 v[196:199], a[164:167], a[128:131], v[196:199]\n"
    "ds_read_b64_tr_b16 a[162:163], v244 offset:18432\n"
    "v_mfma_f32_16x16x32_bf16 v[196:199], a[168:171], a[132:135], v[196:199]\n"
    "ds_read_b64_tr_b16 a[164:165], v244 offset:24576\n"
    "v_mfma_f32_16x16x32_bf16 v[196:199], a[172:175], a[136:139], v[196:199]\n"
    "ds_read_b64_tr_b16 a[166:167], v244 offset:26624\n"
    "v_mfma_f32_16x16x32_bf16 v[188:191], a[172:175], a[104:107], v[188:191]\n"
    "v_exp_f32_e32 v185, v185\n"
    "v_mfma_f32_16x16x32_bf16 v[188:191], a[168:171], a[100:103], v[188:191]\n"
    "v_cvt_pk_bf16_f32 v193, v194, v195\n"
    "v_mfma_f32_16x16x32_bf16 v[180:183], a[168:171], a[68:71], v[180:183]\n"
    "v_exp_f32_e32 v186, v186\n"
    "v_mfma_f32_16x16x32_bf16 v[180:183], a[172:175], a[72:75], v[180:183]\n"
    "v_exp_f32_e32 v187, v187\n"
    "v_mfma_f32_16x16x32_bf16 v[172:175], a[172:175], a[40:43], v[172:175]\n"
    "v_cvt_pk_bf16_f32 v184, v184, v185\n"
    "v_mfma_f32_16x16x32_bf16 v[172:175], a[168:171], a[36:39], v[172:175]\n"
    "v_exp_f32_e32 v176, v176\n"
    "v_mfma_f32_16x16x32_bf16 v[164:167], a[168:171], a[4:7], v[164:167]\n"
    "ds_read_b64_tr_b16 a[168:169], v245 offset:16384\n"
    "v_mfma_f32_16x16x32_bf16 v[164:167], a[172:175], a[8:11], v[164:167]\n"
    "s_waitcnt lgkmcnt(7)\n"
    "ds_read_b64_tr_b16 a[170:171], v245 offset:18432\n"
    "v_mfma_f32_16x16x32_bf16 v[164:167], a[176:179], a[12:15], v[164:167]\n"
    "ds_read_b64_tr_b16 a[172:173], v245 offset:24576\n"
    "v_mfma_f32_16x16x32_bf16 v[164:167], a[180:183], a[16:19], v[164:167]\n"
    "ds_read_b64_tr_b16 a[174:175], v245 offset:26624\n"
    "v_mfma_f32_16x16x32_bf16 v[172:175], a[180:183], a[48:51], v[172:175]\n"
    "v_exp_f32_e32 v177, v177\n"
    "v_mfma_f32_16x16x32_bf16 v[172:175], a[176:179], a[44:47], v[172:175]\n"
    "v_cvt_pk_bf16_f32 v185, v186, v187\n"
    "v_mfma_f32_16x16x32_bf16 v[180:183], a[176:179], a[76:79], v[180:183]\n"
    "v_exp_f32_e32 v178, v178\n"
    "v_mfma_f32_16x16x32_bf16 v[180:183], a[180:183], a[80:83], v[180:183]\n"
    "v_exp_f32_e32 v179, v179\n"
    "v_mfma_f32_16x16x32_bf16 v[188:191], a[180:183], a[112:115], v[188:191]\n"
    "v_cvt_pk_bf16_f32 v176, v176, v177\n"
    "v_mfma_f32_16x16x32_bf16 v[188:191], a[176:179], a[108:111], v[188:191]\n"
    "v_exp_f32_e32 v168, v168\n"
    "v_mfma_f32_16x16x32_bf16 v[196:199], a[176:179], a[140:143], v[196:199]\n"
    "ds_read_b64_tr_b16 a[176:177], v246 offset:16384\n"
    "v_mfma_f32_16x16x32_bf16 v[196:199], a[180:183], a[144:147], v[196:199]\n"
    "ds_read_b64_tr_b16 a[178:179], v246 offset:18432\n"
    "v_mfma_f32_16x16x32_bf16 v[196:199], a[184:187], a[148:151], v[196:199]\n"
    "ds_read_b64_tr_b16 a[180:181], v246 offset:24576\n"
    "v_mfma_f32_16x16x32_bf16 v[196:199], a[188:191], a[152:155], v[196:199]\n"
    "ds_read_b64_tr_b16 a[182:183], v246 offset:26624\n"
    "v_mfma_f32_16x16x32_bf16 v[188:191], a[188:191], a[120:123], v[188:191]\n"
    "v_exp_f32_e32 v169, v169\n"
    "v_mfma_f32_16x16x32_bf16 v[188:191], a[184:187], a[116:119], v[188:191]\n"
    "v_cvt_pk_bf16_f32 v177, v178, v179\n"
    "v_mfma_f32_16x16x32_bf16 v[180:183], a[184:187], a[84:87], v[180:183]\n"
    "v_exp_f32_e32 v170, v170\n"
    "v_mfma_f32_16x16x32_bf16 v[180:183], a[188:191], a[88:91], v[180:183]\n"
    "v_exp_f32_e32 v171, v171\n"
    "v_mfma_f32_16x16x32_bf16 v[172:175], a[188:191], a[56:59], v[172:175]\n"
    "v_cvt_pk_bf16_f32 v168, v168, v169\n"
    "v_mfma_f32_16x16x32_bf16 v[172:175], a[184:187], a[52:55], v[172:175]\n"
    "v_exp_f32_e32 v160, v160\n"
    "v_mfma_f32_16x16x32_bf16 v[164:167], a[184:187], a[20:23], v[164:167]\n"
    "ds_read_b64_tr_b16 a[184:185], v247 offset:16384\n"
    "v_mfma_f32_16x16x32_bf16 v[164:167], a[188:191], a[24:27], v[164:167]\n"
    "s_waitcnt lgkmcnt(5)\n"
    "ds_read_b64_tr_b16 a[186:187], v247 offset:18432\n"
    "v_mfma_f32_16x16x32_bf16 v[0:3], a[160:163], v[200:203], v[0:3]\n"
    "ds_read_b64_tr_b16 a[188:189], v247 offset:24576\n"
    "v_mfma_f32_16x16x32_bf16 v[0:3], a[164:167], v[204:207], v[0:3]\n"
    "ds_read_b64_tr_b16 a[190:191], v247 offset:26624\n"
    "v_mfma_f32_16x16x32_bf16 a[248:251], a[164:167], v[212:215], a[248:251]\n"
    "v_exp_f32_e32 v161, v161\n"
    "v_mfma_f32_16x16x32_bf16 a[248:251], a[160:163], v[208:211], a[248:251]\n"
    "v_cvt_pk_bf16_f32 v169, v170, v171\n"
    "v_mfma_f32_16x16x32_bf16 v[56:59], a[160:163], v[216:219], v[56:59]\n"
    "v_exp_f32_e32 v162, v162\n"
    "v_mfma_f32_16x16x32_bf16 v[56:59], a[164:167], v[220:223], v[56:59]\n"
    "v_exp_f32_e32 v163, v163\n"
    "v_mfma_f32_16x16x32_bf16 v[60:63], a[164:167], v[228:231], v[60:63]\n"
    "v_cvt_pk_bf16_f32 v160, v160, v161\n"
    "v_mfma_f32_16x16x32_bf16 v[60:63], a[160:163], v[224:227], v[60:63]\n"
    "v_cvt_pk_bf16_f32 v161, v162, v163\n"
    "v_mfma_f32_16x16x32_bf16 v[64:67], a[160:163], v[232:235], v[64:67]\n"
    "ds_read_b64_tr_b16 a[160:161], v244 offset:20480\n"
    "v_mfma_f32_16x16x32_bf16 v[64:67], a[164:167], v[236:239], v[64:67]\n"
    "ds_read_b64_tr_b16 a[162:163], v244 offset:22528\n"
    "v_mfma_f32_16x16x32_bf16 v[76:79], a[168:171], v[232:235], v[76:79]\n"
    "ds_read_b64_tr_b16 a[164:165], v244 offset:28672\n"
    "v_mfma_f32_16x16x32_bf16 v[76:79], a[172:175], v[236:239], v[76:79]\n"
    "ds_read_b64_tr_b16 a[166:167], v244 offset:30720\n"
    "v_mfma_f32_16x16x32_bf16 v[72:75], a[172:175], v[228:231], v[72:75]\n"
    "v_exp_f32_e32 v196, v196\n"
    "v_mfma_f32_16x16x32_bf16 v[72:75], a[168:171], v[224:227], v[72:75]\n"
    "v_exp_f32_e32 v197, v197\n"
    "v_mfma_f32_16x16x32_bf16 v[68:71], a[168:171], v[216:219], v[68:71]\n"
    "v_exp_f32_e32 v198, v198\n"
    "v_mfma_f32_16x16x32_bf16 v[68:71], a[172:175], v[220:223], v[68:71]\n"
    "v_exp_f32_e32 v199, v199\n"
    "v_mfma_f32_16x16x32_bf16 a[252:255], a[172:175], v[212:215], a[252:255]\n"
    "v_cvt_pk_bf16_f32 v194, v196, v197\n"
    "v_mfma_f32_16x16x32_bf16 a[252:255], a[168:171], v[208:211], a[252:255]\n"
    "v_exp_f32_e32 v188, v188\n"
    "v_mfma_f32_16x16x32_bf16 v[4:7], a[168:171], v[200:203], v[4:7]\n"
    "ds_read_b64_tr_b16 a[168:169], v245 offset:20480\n"
    "v_mfma_f32_16x16x32_bf16 v[4:7], a[172:175], v[204:207], v[4:7]\n"
    "s_waitcnt lgkmcnt(5)\n"
    "ds_read_b64_tr_b16 a[170:171], v245 offset:22528\n"
    "v_mfma_f32_16x16x32_bf16 v[8:11], a[176:179], v[200:203], v[8:11]\n"
    "ds_read_b64_tr_b16 a[172:173], v245 offset:28672\n"
    "v_mfma_f32_16x16x32_bf16 v[8:11], a[180:183], v[204:207], v[8:11]\n"
    "ds_read_b64_tr_b16 a[174:175], v245 offset:30720\n"
    "v_mfma_f32_16x16x32_bf16 v[32:35], a[180:183], v[212:215], v[32:35]\n"
    "v_exp_f32_e32 v189, v189\n"
    "v_mfma_f32_16x16x32_bf16 v[32:35], a[176:179], v[208:211], v[32:35]\n"
    "v_cvt_pk_bf16_f32 v195, v198, v199\n"
    "v_mfma_f32_16x16x32_bf16 v[80:83], a[176:179], v[216:219], v[80:83]\n"
    "v_exp_f32_e32 v190, v190\n"
    "v_mfma_f32_16x16x32_bf16 v[80:83], a[180:183], v[220:223], v[80:83]\n"
    "v_exp_f32_e32 v191, v191\n"
    "v_mfma_f32_16x16x32_bf16 v[84:87], a[180:183], v[228:231], v[84:87]\n"
    "v_cvt_pk_bf16_f32 v186, v188, v189\n"
    "v_mfma_f32_16x16x32_bf16 v[84:87], a[176:179], v[224:227], v[84:87]\n"
    "v_exp_f32_e32 v180, v180\n"
    "v_mfma_f32_16x16x32_bf16 v[88:91], a[176:179], v[232:235], v[88:91]\n"
    "ds_read_b64_tr_b16 a[176:177], v246 offset:20480\n"
    "v_mfma_f32_16x16x32_bf16 v[88:91], a[180:183], v[236:239], v[88:91]\n"
    "ds_read_b64_tr_b16 a[178:179], v246 offset:22528\n"
    "v_mfma_f32_16x16x32_bf16 v[100:103], a[184:187], v[232:235], v[100:103]\n"
    "ds_read_b64_tr_b16 a[180:181], v246 offset:28672\n"
    "v_mfma_f32_16x16x32_bf16 v[100:103], a[188:191], v[236:239], v[100:103]\n"
    "ds_read_b64_tr_b16 a[182:183], v246 offset:30720\n"
    "v_mfma_f32_16x16x32_bf16 v[96:99], a[188:191], v[228:231], v[96:99]\n"
    "v_exp_f32_e32 v181, v181\n"
    "v_mfma_f32_16x16x32_bf16 v[96:99], a[184:187], v[224:227], v[96:99]\n"
    "v_cvt_pk_bf16_f32 v187, v190, v191\n"
    "v_mfma_f32_16x16x32_bf16 v[92:95], a[184:187], v[216:219], v[92:95]\n"
    "v_exp_f32_e32 v182, v182\n"
    "v_mfma_f32_16x16x32_bf16 v[92:95], a[188:191], v[220:223], v[92:95]\n"
    "v_exp_f32_e32 v183, v183\n"
    "v_mfma_f32_16x16x32_bf16 v[36:39], a[188:191], v[212:215], v[36:39]\n"
    "v_cvt_pk_bf16_f32 v178, v180, v181\n"
    "v_mfma_f32_16x16x32_bf16 v[36:39], a[184:187], v[208:211], v[36:39]\n"
    "v_exp_f32_e32 v172, v172\n"
    "v_mfma_f32_16x16x32_bf16 v[12:15], a[184:187], v[200:203], v[12:15]\n"
    "s_mov_b32 m0, s49\n"
    "ds_read_b64_tr_b16 a[184:185], v247 offset:20480\n"
    "v_mfma_f32_16x16x32_bf16 v[12:15], a[188:191], v[204:207], v[12:15]\n"
    "s_waitcnt lgkmcnt(5)\n"
    "ds_read_b64_tr_b16 a[186:187], v247 offset:22528\n"
    "v_mfma_f32_16x16x32_bf16 v[16:19], a[160:163], v[200:203], v[16:19]\n"
    "ds_read_b64_tr_b16 a[188:189], v247 offset:28672\n"
    "v_mfma_f32_16x16x32_bf16 v[16:19], a[164:167], v[204:207], v[16:19]\n"
    "ds_read_b64_tr_b16 a[190:191], v247 offset:30720\n"
    "v_mfma_f32_16x16x32_bf16 v[40:43], a[164:167], v[212:215], v[40:43]\n"
    "v_exp_f32_e32 v173, v173\n"
    "v_mfma_f32_16x16x32_bf16 v[40:43], a[160:163], v[208:211], v[40:43]\n"
    "v_cvt_pk_bf16_f32 v179, v182, v183\n"
    "v_mfma_f32_16x16x32_bf16 v[104:107], a[160:163], v[216:219], v[104:107]\n"
    "v_exp_f32_e32 v174, v174\n"
    "v_mfma_f32_16x16x32_bf16 v[104:107], a[164:167], v[220:223], v[104:107]\n"
    "s_waitcnt vmcnt(8)\n"
    "v_mfma_f32_16x16x32_bf16 v[108:111], a[164:167], v[228:231], v[108:111]\n"
    "s_barrier\n"
    "v_mfma_f32_16x16x32_bf16 v[108:111], a[160:163], v[224:227], v[108:111]\n"
    "v_exp_f32_e32 v175, v175\n"
    "v_mfma_f32_16x16x32_bf16 v[112:115], a[160:163], v[232:235], v[112:115]\n"
    "ds_read_b128 a[160:163], v243 offset:53248\n"
    "v_mfma_f32_16x16x32_bf16 v[112:115], a[164:167], v[236:239], v[112:115]\n"
    "ds_read_b128 a[164:167], v240 offset:49152\n"
    "v_mfma_f32_16x16x32_bf16 v[124:127], a[168:171], v[232:235], v[124:127]\n"
    "ds_write_addtid_b32 v192 offset:16384\n"
    "v_cvt_pk_bf16_f32 v170, v172, v173\n"
    "v_mfma_f32_16x16x32_bf16 v[124:127], a[172:175], v[236:239], v[124:127]\n"
    "v_exp_f32_e32 v164, v164\n"
    "v_mfma_f32_16x16x32_bf16 v[120:123], a[172:175], v[228:231], v[120:123]\n"
    "v_exp_f32_e32 v165, v165\n"
    "v_mfma_f32_16x16x32_bf16 v[120:123], a[168:171], v[224:227], v[120:123]\n"
    "ds_write_addtid_b32 v193 offset:16640\n"
    "v_cvt_pk_bf16_f32 v171, v174, v175\n"
    "v_mfma_f32_16x16x32_bf16 v[116:119], a[168:171], v[216:219], v[116:119]\n"
    "v_exp_f32_e32 v166, v166\n"
    "v_mfma_f32_16x16x32_bf16 v[116:119], a[172:175], v[220:223], v[116:119]\n"
    "v_exp_f32_e32 v167, v167\n"
    "v_mfma_f32_16x16x32_bf16 v[44:47], a[172:175], v[212:215], v[44:47]\n"
    "ds_write_addtid_b32 v184 offset:12288\n"
    "v_cvt_pk_bf16_f32 v162, v164, v165\n"
    "v_mfma_f32_16x16x32_bf16 v[44:47], a[168:171], v[208:211], v[44:47]\n"
    "ds_write_addtid_b32 v185 offset:12544\n"
    "v_cvt_pk_bf16_f32 v163, v166, v167\n"
    "v_mfma_f32_16x16x32_bf16 v[20:23], a[168:171], v[200:203], v[20:23]\n"
    "ds_read_b128 a[168:171], v241 offset:49152\n"
    "v_mfma_f32_16x16x32_bf16 v[20:23], a[172:175], v[204:207], v[20:23]\n"
    "s_waitcnt lgkmcnt(7)\n"
    "ds_read_b128 a[172:175], v240 offset:53248\n"
    "v_mfma_f32_16x16x32_bf16 v[24:27], a[176:179], v[200:203], v[24:27]\n"
    "ds_write_addtid_b32 v176 offset:8192\n"
    "v_mfma_f32_16x16x32_bf16 v[24:27], a[180:183], v[204:207], v[24:27]\n"
    "ds_write_addtid_b32 v177 offset:8448\n"
    "v_mfma_f32_16x16x32_bf16 v[48:51], a[180:183], v[212:215], v[48:51]\n"
    "ds_write_addtid_b32 v168 offset:4096\n"
    "v_mfma_f32_16x16x32_bf16 v[48:51], a[176:179], v[208:211], v[48:51]\n"
    "ds_write_addtid_b32 v169 offset:4352\n"
    "v_mfma_f32_16x16x32_bf16 v[128:131], a[176:179], v[216:219], v[128:131]\n"
    "ds_write_addtid_b32 v160 offset:0\n"
    "v_mfma_f32_16x16x32_bf16 v[128:131], a[180:183], v[220:223], v[128:131]\n"
    "ds_write_addtid_b32 v161 offset:256\n"
    "v_mfma_f32_16x16x32_bf16 v[132:135], a[180:183], v[228:231], v[132:135]\n"
    "ds_write_addtid_b32 v194 offset:16896\n"
    "v_mfma_f32_16x16x32_bf16 v[132:135], a[176:179], v[224:227], v[132:135]\n"
    "ds_write_addtid_b32 v195 offset:17152\n"
    "v_mfma_f32_16x16x32_bf16 v[136:139], a[176:179], v[232:235], v[136:139]\n"
    "ds_read_b128 a[176:179], v241 offset:53248\n"
    "v_mfma_f32_16x16x32_bf16 v[136:139], a[180:183], v[236:239], v[136:139]\n"
    "ds_read_b128 a[180:183], v242 offset:49152\n"
    "v_mfma_f32_16x16x32_bf16 v[148:151], a[184:187], v[232:235], v[148:151]\n"
    "ds_write_addtid_b32 v186 offset:12800\n"
    "v_mfma_f32_16x16x32_bf16 v[148:151], a[188:191], v[236:239], v[148:151]\n"
    "ds_write_addtid_b32 v187 offset:13056\n"
    "v_mfma_f32_16x16x32_bf16 v[144:147], a[188:191], v[228:231], v[144:147]\n"
    "ds_write_addtid_b32 v178 offset:8704\n"
    "v_mfma_f32_16x16x32_bf16 v[144:147], a[184:187], v[224:227], v[144:147]\n"
    "ds_write_addtid_b32 v179 offset:8960\n"
    "v_mfma_f32_16x16x32_bf16 v[140:143], a[184:187], v[216:219], v[140:143]\n"
    "ds_write_addtid_b32 v170 offset:4608\n"
    "v_mfma_f32_16x16x32_bf16 v[140:143], a[188:191], v[220:223], v[140:143]\n"
    "ds_write_addtid_b32 v171 offset:4864\n"
    "v_mfma_f32_16x16x32_bf16 v[52:55], a[188:191], v[212:215], v[52:55]\n"
    "ds_write_addtid_b32 v162 offset:512\n"
    "v_mfma_f32_16x16x32_bf16 v[52:55], a[184:187], v[208:211], v[52:55]\n"
    "ds_write_addtid_b32 v163 offset:768\n"
    "v_mfma_f32_16x16x32_bf16 v[28:31], a[184:187], v[200:203], v[28:31]\n"
    "ds_read_b128 a[184:187], v243 offset:49152\n"
    "v_mfma_f32_16x16x32_bf16 v[28:31], a[188:191], v[204:207], v[28:31]\n"
    "s_waitcnt lgkmcnt(15)\n"
    "ds_read_b128 a[188:191], v242 offset:53248\n"
    ".Lat_p2e:\n"
    "v_mfma_f32_16x16x32_bf16 v[200:203], a[160:163], a[28:31], 0\n"
    "v_mfma_f32_16x16x32_bf16 v[200:203], a[164:167], a[0:3], v[200:203]\n"
    "v_mfma_f32_16x16x32_bf16 v[208:211], a[164:167], a[32:35], 0\n"
    "v_mfma_f32_16x16x32_bf16 v[208:211], a[160:163], a[60:63], v[208:211]\n"
    "v_mfma_f32_16x16x32_bf16 v[216:219], a[160:163], a[92:95], 0\n"
    "v_mfma_f32_16x16x32_bf16 v[216:219], a[164:167], a[64:67], v[216:219]\n"
    "s_waitcnt lgkmcnt(0)\n"
    "v_mfma_f32_16x16x32_bf16 v[224:227], a[164:167], a[96:99], 0\n"
    "s_barrier\n"
    "v_mfma_f32_16x16x32_bf16 v[224:227], a[160:163], a[124:127], v[224:227]\n"
    "s_add_u32 m0, s43, 16384\n"
    "ds_read2st64_b32 v[164:165], v250 offset0:0 offset1:1\n"
    "v_mfma_f32_16x16x32_bf16 v[232:235], a[160:163], a[156:159], 0\n"
    "ds_read_b128 a[160:163], v243 offset:55296\n"
    "v_mfma_f32_16x16x32_bf16 v[232:235], a[164:167], a[128:131], v[232:235]\n"
    "ds_read_b128 a[164:167], v240 offset:51200\n"
    "v_mfma_f32_16x16x32_bf16 v[232:235], a[168:171], a[132:135], v[232:235]\n"
    "buffer_load_dwordx4 v248, s[8:11], s40 offen offset:0 sc1 lds\n"
    "v_mfma_f32_16x16x32_bf16 v[232:235], a[172:175], a[136:139], v[232:235]\n"
    "v_mfma_f32_16x16x32_bf16 v[224:227], a[172:175], a[104:107], v[224:227]\n"
    "v_mfma_f32_16x16x32_bf16 v[224:227], a[168:171], a[100:103], v[224:227]\n"
    "ds_read2st64_b32 v[166:167], v250 offset0:2 offset1:3\n"
    "v_mfma_f32_16x16x32_bf16 v[216:219], a[168:171], a[68:71], v[216:219]\n"
    "v_mfma_f32_16x16x32_bf16 v[216:219], a[172:175], a[72:75], v[216:219]\n"
    "buffer_load_dwordx4 v248, s[8:11], s40 offen offset:1024 sc1 lds\n"
    "v_mfma_f32_16x16x32_bf16 v[208:211], a[172:175], a[40:43], v[208:211]\n"
    "v_mfma_f32_16x16x32_bf16 v[208:211], a[168:171], a[36:39], v[208:211]\n"
    "ds_read2st64_b32 v[172:173], v250 offset0:16 offset1:17\n"
    "v_mfma_f32_16x16x32_bf16 v[200:203], a[168:171], a[4:7], v[200:203]\n"
    "ds_read_b128 a[168:171], v241 offset:51200\n"
    "v_mfma_f32_16x16x32_bf16 v[200:203], a[172:175], a[8:11], v[200:203]\n"
    "ds_read_b128 a[172:175], v240 offset:55296\n"
    "v_mfma_f32_16x16x32_bf16 v[200:203], a[176:179], a[12:15], v[200:203]\n"
    "buffer_load_dwordx4 v248, s[8:11], s40 offen offset:2048 sc1 lds\n"
    "v_mfma_f32_16x16x32_bf16 v[200:203], a[180:183], a[16:19], v[200:203]\n"
    "ds_read2st64_b32 v[174:175], v250 offset0:18 offset1:19\n"
    "v_mfma_f32_16x16x32_bf16 v[208:211], a[180:183], a[48:51], v[208:211]\n"
    "v_mfma_f32_16x16x32_bf16 v[208:211], a[176:179], a[44:47], v[208:211]\n"
    "v_mfma_f32_16x16x32_bf16 v[216:219], a[176:179], a[76:79], v[216:219]\n"
    "ds_read2st64_b32 v[180:181], v250 offset0:32 offset1:33\n"
    "v_mfma_f32_16x16x32_bf16 v[216:219], a[180:183], a[80:83], v[216:219]\n"
    "buffer_load_dwordx4 v248, s[8:11], s40 offen offset:3072 sc1 lds\n"
    "v_mfma_f32_16x16x32_bf16 v[224:227], a[180:183], a[112:115], v[224:227]\n"
    "s_add_u32 m0, s43, 20480\n"
    "v_mfma_f32_16x16x32_bf16 v[224:227], a[176:179], a[108:111], v[224:227]\n"
    "s_add_u32 s40, s40, 0x8000\n"
    "ds_read2st64_b32 v[182:183], v250 offset0:34 offset1:35\n"
    "v_mfma_f32_16x16x32_bf16 v[232:235], a[176:179], a[140:143], v[232:235]\n"
    "ds_read_b128 a[176:179], v241 offset:55296\n"
    "v_mfma_f32_16x16x32_bf16 v[232:235], a[180:183], a[144:147], v[232:235]\n"
    "ds_read_b128 a[180:183], v242 offset:51200\n"
    "v_mfma_f32_16x16x32_bf16 v[232:235], a[184:187], a[148:151], v[232:235]\n"
    "buffer_load_dwordx4 v248, s[8:11], s41 offen offset:0 sc1 lds\n"
    "v_mfma_f32_16x16x32_bf16 v[232:235], a[188:191], a[152:155], v[232:235]\n"
    "v_mfma_f32_16x16x32_bf16 v[224:227], a[188:191], a[120:123], v[224:227]\n"
    "ds_read2st64_b32 v[188:189], v250 offset0:48 offset1:49\n"
    "v_mfma_f32_16x16x32_bf16 v[224:227], a[184:187], a[116:119], v[224:227]\n"
    "v_mfma_f32_16x16x32_bf16 v[216:219], a[184:187], a[84:87], v[216:219]\n"
    "v_exp_f32_e32 v232, v232\n"
    "v_mfma_f32_16x16x32_bf16 v[216:219], a[188:191], a[88:91], v[216:219]\n"
    "buffer_load_dwordx4 v248, s[8:11], s41 offen offset:1024 sc1 lds\n"
    "v_mfma_f32_16x16x32_bf16 v[208:211], a[188:191], a[56:59], v[208:211]\n"
    "ds_read2st64_b32 v[190:191], v250 offset0:50 offset1:51\n"
    "v_mfma_f32_16x16x32_bf16 v[208:211], a[184:187], a[52:55], v[208:211]\n"
    "v_exp_f32_e32 v233, v233\n"
    "v_mfma_f32_16x16x32_bf16 v[200:203], a[184:187], a[20:23], v[200:203]\n"
    "ds_read_b128 a[184:187], v243 offset:51200\n"
    "v_mfma_f32_16x16x32_bf16 v[200:203], a[188:191], a[24:27], v[200:203]\n"
    "s_waitcnt lgkmcnt(8)\n"
    "ds_read_b128 a[188:191], v242 offset:55296\n"
    "v_mfma_f32_16x16x32_bf16 v[204:207], a[160:163], a[28:31], 0\n"
    "buffer_load_dwordx4 v248, s[8:11], s41 offen offset:2048 sc1 lds\n"
    "v_mfma_f32_16x16x32_bf16 v[204:207], a[164:167], a[0:3], v[204:207]\n"
    "ds_read2st64_b32 v[196:197], v250 offset0:64 offset1:65\n"
    "v_mfma_f32_16x16x32_bf16 v[212:215], a[164:167], a[32:35], 0\n"
    "v_exp_f32_e32 v234, v234\n"
    "v_mfma_f32_16x16x32_bf16 v[212:215], a[160:163], a[60:63], v[212:215]\n"
    "ds_read2st64_b32 v[198:199], v250 offset0:66 offset1:67\n"
    "v_mfma_f32_16x16x32_bf16 v[220:223], a[160:163], a[92:95], 0\n"
    "v_exp_f32_e32 v235, v235\n"
    "v_mfma_f32_16x16x32_bf16 v[220:223], a[164:167], a[64:67], v[220:223]\n"
    "buffer_load_dwordx4 v248, s[8:11], s41 offen offset:3072 sc1 lds\n"
    "v_cvt_pk_bf16_f32 v232, v232, v233\n"
    "v_mfma_f32_16x16x32_bf16 v[228:231], a[164:167], a[96:99], 0\n"
    "s_add_u32 s41, s41, 0x8000\n"
    "v_exp_f32_e32 v224, v224\n"
    "v_mfma_f32_16x16x32_bf16 v[228:231], a[160:163], a[124:127], v[228:231]\n"
    "v_exp_f32_e32 v225, v225\n"
    "v_mfma_f32_16x16x32_bf16 v[236:239], a[160:163], a[156:159], 0\n"
    "ds_read_b64_tr_b16 a[160:161], v244 offset:32768\n"
    "v_cvt_pk_bf16_f32 v233, v234, v235\n"
    "v_mfma_f32_16x16x32_bf16 v[236:239], a[164:167], a[128:131], v[236:239]\n"
    "ds_read_b64_tr_b16 a[162:163], v244 offset:34816\n"
    "v_mfma_f32_16x16x32_bf16 v[236:239], a[168:171], a[132:135], v[236:239]\n"
    "ds_read_b64_tr_b16 a[164:165], v244 offset:40960\n"
    "v_mfma_f32_16x16x32_bf16 v[236:239], a[172:175], a[136:139], v[236:239]\n"
    "ds_read_b64_tr_b16 a[166:167], v244 offset:43008\n"
    "v_mfma_f32_16x16x32_bf16 v[228:231], a[172:175], a[104:107], v[228:231]\n"
    "v_exp_f32_e32 v226, v226\n"
    "v_mfma_f32_16x16x32_bf16 v[228:231], a[168:171], a[100:103], v[228:231]\n"
    "v_exp_f32_e32 v227, v227\n"
    "v_mfma_f32_16x16x32_bf16 v[220:223], a[168:171], a[68:71], v[220:223]\n"
    "v_cvt_pk_bf16_f32 v224, v224, v225\n"
    "v_mfma_f32_16x16x32_bf16 v[220:223], a[172:175], a[72:75], v[220:223]\n"
    "v_exp_f32_e32 v216, v216\n"
    "v_mfma_f32_16x16x32_bf16 v[212:215], a[172:175], a[40:43], v[212:215]\n"
    "v_exp_f32_e32 v217, v217\n"
    "v_mfma_f32_16x16x32_bf16 v[212:215], a[168:171], a[36:39], v[212:215]\n"
    "v_cvt_pk_bf16_f32 v225, v226, v227\n"
    "v_mfma_f32_16x16x32_bf16 v[204:207], a[168:171], a[4:7], v[204:207]\n"
    "ds_read_b64_tr_b16 a[168:169], v245 offset:32768\n"
    "v_mfma_f32_16x16x32_bf16 v[204:207], a[172:175], a[8:11], v[204:207]\n"
    "s_waitcnt lgkmcnt(7)\n"
    "ds_read_b64_tr_b16 a[170:171], v245 offset:34816\n"
    "v_mfma_f32_16x16x32_bf16 v[204:207], a[176:179], a[12:15], v[204:207]\n"
    "ds_read_b64_tr_b16 a[172:173], v245 offset:40960\n"
    "v_mfma_f32_16x16x32_bf16 v[204:207], a[180:183], a[16:19], v[204:207]\n"
    "ds_read_b64_tr_b16 a[174:175], v245 offset:43008\n"
    "v_mfma_f32_16x16x32_bf16 v[212:215], a[180:183], a[48:51], v[212:215]\n"
    "v_exp_f32_e32 v218, v218\n"
    "v_mfma_f32_16x16x32_bf16 v[212:215], a[176:179], a[44:47], v[212:215]\n"
    "v_exp_f32_e32 v219, v219\n"
    "v_mfma_f32_16x16x32_bf16 v[220:223], a[176:179], a[76:79], v[220:223]\n"
    "v_cvt_pk_bf16_f32 v216, v216, v217\n"
    "v_mfma_f32_16x16x32_bf16 v[220:223], a[180:183], a[80:83], v[220:223]\n"
    "v_exp_f32_e32 v208, v208\n"
    "v_mfma_f32_16x16x32_bf16 v[228:231], a[180:183], a[112:115], v[228:231]\n"
    "v_exp_f32_e32 v209, v209\n"
    "v_mfma_f32_16x16x32_bf16 v[228:231], a[176:179], a[108:111], v[228:231]\n"
    "v_cvt_pk_bf16_f32 v217, v218, v219\n"
    "v_mfma_f32_16x16x32_bf16 v[236:239], a[176:179], a[140:143], v[236:239]\n"
    "ds_read_b64_tr_b16 a[176:177], v246 offset:32768\n"
    "v_mfma_f32_16x16x32_bf16 v[236:239], a[180:183], a[144:147], v[236:239]\n"
    "ds_read_b64_tr_b16 a[178:179], v246 offset:34816\n"
    "v_mfma_f32_16x16x32_bf16 v[236:239], a[184:187], a[148:151], v[236:239]\n"
    "ds_read_b64_tr_b16 a[180:181], v246 offset:40960\n"
    "v_mfma_f32_16x16x32_bf16 v[236:239], a[188:191], a[152:155], v[236:239]\n"
    "ds_read_b64_tr_b16 a[182:183], v246 offset:43008\n"
    "v_mfma_f32_16x16x32_bf16 v[228:231], a[188:191], a[120:123], v[228:231]\n"
    "v_exp_f32_e32 v210, v210\n"
    "v_mfma_f32_16x16x32_bf16 v[228:231], a[184:187], a[116:119], v[228:231]\n"
    "v_exp_f32_e32 v211, v211\n"
    "v_mfma_f32_16x16x32_bf16 v[220:223], a[184:187], a[84:87], v[220:223]\n"
    "v_cvt_pk_bf16_f32 v208, v208, v209\n"
    "v_mfma_f32_16x16x32_bf16 v[220:223], a[188:191], a[88:91], v[220:223]\n"
    "v_exp_f32_e32 v200, v200\n"
    "v_mfma_f32_16x16x32_bf16 v[212:215], a[188:191], a[56:59], v[212:215]\n"
    "v_exp_f32_e32 v201, v201\n"
    "v_mfma_f32_16x16x32_bf16 v[212:215], a[184:187], a[52:55], v[212:215]\n"
    "v_cvt_pk_bf16_f32 v209, v210, v211\n"
    "v_mfma_f32_16x16x32_bf16 v[204:207], a[184:187], a[20:23], v[204:207]\n"
    "ds_read_b64_tr_b16 a[184:185], v247 offset:32768\n"
    "v_mfma_f32_16x16x32_bf16 v[204:207], a[188:191], a[24:27], v[204:207]\n"
    "s_waitcnt lgkmcnt(5)\n"
    "ds_read_b64_tr_b16 a[186:187], v247 offset:34816\n"
    "v_mfma_f32_16x16x32_bf16 v[0:3], a[160:163], v[160:163], v[0:3]\n"
    "ds_read_b64_tr_b16 a[188:189], v247 offset:40960\n"
    "v_mfma_f32_16x16x32_bf16 v[0:3], a[164:167], v[164:167], v[0:3]\n"
    "ds_read_b64_tr_b16 a[190:191], v247 offset:43008\n"
    "v_mfma_f32_16x16x32_bf16 a[248:251], a[164:167], v[172:175], a[248:251]\n"
    "v_exp_f32_e32 v202, v202\n"
    "v_mfma_f32_16x16x32_bf16 a[248:251], a[160:163], v[168:171], a[248:251]\n"
    "v_exp_f32_e32 v203, v203\n"
    "v_mfma_f32_16x16x32_bf16 v[56:59], a[160:163], v[176:179], v[56:59]\n"
    "v_cvt_pk_bf16_f32 v200, v200, v201\n"
    "v_mfma_f32_16x16x32_bf16 v[56:59], a[164:167], v[180:183], v[56:59]\n"
    "v_cvt_pk_bf16_f32 v201, v202, v203\n"
    "v_mfma_f32_16x16x32_bf16 v[60:63], a[164:167], v[188:191], v[60:63]\n"
    "v_exp_f32_e32 v236, v236\n"
    "v_mfma_f32_16x16x32_bf16 v[60:63], a[160:163], v[184:187], v[60:63]\n"
    "v_exp_f32_e32 v237, v237\n"
    "v_mfma_f32_16x16x32_bf16 v[64:67], a[160:163], v[192:195], v[64:67]\n"
    "ds_read_b64_tr_b16 a[160:161], v244 offset:36864\n"
    "v_mfma_f32_16x16x32_bf16 v[64:67], a[164:167], v[196:199], v[64:67]\n"
    "ds_read_b64_tr_b16 a[162:163], v244 offset:38912\n"
    "v_mfma_f32_16x16x32_bf16 v[76:79], a[168:171], v[192:195], v[76:79]\n"
    "ds_read_b64_tr_b16 a[164:165], v244 offset:45056\n"
    "v_mfma_f32_16x16x32_bf16 v[76:79], a[172:175], v[196:199], v[76:79]\n"
    "ds_read_b64_tr_b16 a[166:167], v244 offset:47104\n"
    "v_mfma_f32_16x16x32_bf16 v[72:75], a[172:175], v[188:191], v[72:75]\n"
    "v_exp_f32_e32 v238, v238\n"
    "v_mfma_f32_16x16x32_bf16 v[72:75], a[168:171], v[184:187], v[72:75]\n"
    "v_exp_f32_e32 v239, v239\n"
    "v_mfma_f32_16x16x32_bf16 v[68:71], a[168:171], v[176:179], v[68:71]\n"
    "v_cvt_pk_bf16_f32 v234, v236, v237\n"
    "v_mfma_f32_16x16x32_bf16 v[68:71], a[172:175], v[180:183], v[68:71]\n"
    "v_exp_f32_e32 v228, v228\n"
    "v_mfma_f32_16x16x32_bf16 a[252:255], a[172:175], v[172:175], a[252:255]\n"
    "v_exp_f32_e32 v229, v229\n"
    "v_mfma_f32_16x16x32_bf16 a[252:255], a[168:171], v[168:171], a[252:255]\n"
    "v_cvt_pk_bf16_f32 v235, v238, v239\n"
    "v_mfma_f32_16x16x32_bf16 v[4:7], a[168:171], v[160:163], v[4:7]\n"
    "ds_read_b64_tr_b16 a[168:169], v245 offset:36864\n"
    "v_mfma_f32_16x16x32_bf16 v[4:7], a[172:175], v[164:167], v[4:7]\n"
    "s_waitcnt lgkmcnt(5)\n"
    "ds_read_b64_tr_b16 a[170:171], v245 offset:38912\n"
    "v_mfma_f32_16x16x32_bf16 v[8:11], a[176:179], v[160:163], v[8:11]\n"
    "ds_read_b64_tr_b16 a[172:173], v245 offset:45056\n"
    "v_mfma_f32_16x16x32_bf16 v[8:11], a[180:183], v[164:167], v[8:11]\n"
    "ds_read_b64_tr_b16 a[174:175], v245 offset:47104\n"
    "v_mfma_f32_16x16x32_bf16 v[32:35], a[180:183], v[172:175], v[32:35]\n"
    "s_or_b32 s45, s44, s37\n"
    "s_cselect_b32 s46, 2.0, s81\n"
    "s_cselect_b32 s45, 2.0, s80\n"
    "v_mfma_f32_16x16x32_bf16 v[32:35], a[176:179], v[168:171], v[32:35]\n"
    "v_exp_f32_e32 v230, v230\n"
    "v_mfma_f32_16x16x32_bf16 v[80:83], a[176:179], v[176:179], v[80:83]\n"
    "v_exp_f32_e32 v231, v231\n"
    "v_mfma_f32_16x16x32_bf16 v[80:83], a[180:183], v[180:183], v[80:83]\n"
    "v_cvt_pk_bf16_f32 v226, v228, v229\n"
    "v_mfma_f32_16x16x32_bf16 v[84:87], a[180:183], v[188:191], v[84:87]\n"
    "v_exp_f32_e32 v220, v220\n"
    "v_mfma_f32_16x16x32_bf16 v[84:87], a[176:179], v[184:187], v[84:87]\n"
    "v_exp_f32_e32 v221, v221\n"
    "v_mfma_f32_16x16x32_bf16 v[88:91], a[176:179], v[192:195], v[88:91]\n"
    "ds_read_b64_tr_b16 a[176:177], v246 offset:36864\n"
    "v_mfma_f32_16x16x32_bf16 v[88:91], a[180:183], v[196:199], v[88:91]\n"
    "ds_read_b64_tr_b16 a[178:179], v246 offset:38912\n"
    "v_mfma_f32_16x16x32_bf16 v[100:103], a[184:187], v[192:195], v[100:103]\n"
    "ds_read_b64_tr_b16 a[180:181], v246 offset:45056\n"
    "v_mfma_f32_16x16x32_bf16 v[100:103], a[188:191], v[196:199], v[100:103]\n"
    "ds_read_b64_tr_b16 a[182:183], v246 offset:47104\n"
    "v_mfma_f32_16x16x32_bf16 v[96:99], a[188:191], v[188:191], v[96:99]\n"
    "v_cvt_pk_bf16_f32 v227, v230, v231\n"
    "v_mfma_f32_16x16x32_bf16 v[96:99], a[184:187], v[184:187], v[96:99]\n"
    "v_exp_f32_e32 v222, v222\n"
    "v_mfma_f32_16x16x32_bf16 v[92:95], a[184:187], v[176:179], v[92:95]\n"
    "v_exp_f32_e32 v223, v223\n"
    "v_mfma_f32_16x16x32_bf16 v[92:95], a[188:191], v[180:183], v[92:95]\n"
    "v_cvt_pk_bf16_f32 v218, v220, v221\n"
    "v_mfma_f32_16x16x32_bf16 v[36:39], a[188:191], v[172:175], v[36:39]\n"
    "v_exp_f32_e32 v212, v212\n"
    "v_mfma_f32_16x16x32_bf16 v[36:39], a[184:187], v[168:171], v[36:39]\n"
    "v_exp_f32_e32 v213, v213\n"
    "v_mfma_f32_16x16x32_bf16 v[12:15], a[184:187], v[160:163], v[12:15]\n"
    "s_mov_b32 m0, s49\n"
    "ds_read_b64_tr_b16 a[184:185], v247 offset:36864\n"
    "v_mfma_f32_16x16x32_bf16 v[12:15], a[188:191], v[164:167], v[12:15]\n"
    "s_waitcnt lgkmcnt(5)\n"
    "ds_read_b64_tr_b16 a[186:187], v247 offset:38912\n"
    "v_mfma_f32_16x16x32_bf16 v[16:19], a[160:163], v[160:163], v[16:19]\n"
    "ds_read_b64_tr_b16 a[188:189], v247 offset:45056\n"
    "v_mfma_f32_16x16x32_bf16 v[16:19], a[164:167], v[164:167], v[16:19]\n"
    "ds_read_b64_tr_b16 a[190:191], v247 offset:47104\n"
    "v_mfma_f32_16x16x32_bf16 v[40:43], a[164:167], v[172:175], v[40:43]\n"
    "buffer_load_dword v154, off, s[8:11], s45 sc1\n"
    "v_mfma_f32_16x16x32_bf16 v[40:43], a[160:163], v[168:171], v[40:43]\n"
    "v_cvt_pk_bf16_f32 v219, v222, v223\n"
    "v_mfma_f32_16x16x32_bf16 v[104:107], a[160:163], v[176:179], v[104:107]\n"
    "v_exp_f32_e32 v214, v214\n"
    "v_mfma_f32_16x16x32_bf16 v[104:107], a[164:167], v[180:183], v[104:107]\n"
    "s_waitcnt vmcnt(9)\n"
    "v_mfma_f32_16x16x32_bf16 v[108:111], a[164:167], v[188:191], v[108:111]\n"
    "s_barrier\n"
    "v_mfma_f32_16x16x32_bf16 v[108:111], a[160:163], v[184:187], v[108:111]\n"
    "v_exp_f32_e32 v215, v215\n"
    "v_mfma_f32_16x16x32_bf16 v[112:115], a[160:163], v[192:195], v[112:115]\n"
    "ds_read_b128 a[160:163], v243 offset:4096\n"
    "v_mfma_f32_16x16x32_bf16 v[112:115], a[164:167], v[196:199], v[112:115]\n"
    "ds_read_b128 a[164:167], v240 offset:0\n"
    "v_mfma_f32_16x16x32_bf16 v[124:127], a[168:171], v[192:195], v[124:127]\n"
    "ds_write_addtid_b32 v232 offset:16384\n"
    "v_cvt_pk_bf16_f32 v210, v212, v213\n"
    "v_mfma_f32_16x16x32_bf16 v[124:127], a[172:175], v[196:199], v[124:127]\n"
    "v_exp_f32_e32 v204, v204\n"
    "v_mfma_f32_16x16x32_bf16 v[120:123], a[172:175], v[188:191], v[120:123]\n"
    "v_exp_f32_e32 v205, v205\n"
    "v_mfma_f32_16x16x32_bf16 v[120:123], a[168:171], v[184:187], v[120:123]\n"
    "ds_write_addtid_b32 v233 offset:16640\n"
    "v_cvt_pk_bf16_f32 v211, v214, v215\n"
    "v_mfma_f32_16x16x32_bf16 v[116:119], a[168:171], v[176:179], v[116:119]\n"
    "v_exp_f32_e32 v206, v206\n"
    "v_mfma_f32_16x16x32_bf16 v[116:119], a[172:175], v[180:183], v[116:119]\n"
    "v_exp_f32_e32 v207, v207\n"
    "v_mfma_f32_16x16x32_bf16 v[44:47], a[172:175], v[172:175], v[44:47]\n"
    "ds_write_addtid_b32 v224 offset:12288\n"
    "v_cvt_pk_bf16_f32 v202, v204, v205\n"
    "v_mfma_f32_16x16x32_bf16 v[44:47], a[168:171], v[168:171], v[44:47]\n"
    "ds_write_addtid_b32 v225 offset:12544\n"
    "v_cvt_pk_bf16_f32 v203, v206, v207\n"
    "v_mfma_f32_16x16x32_bf16 v[20:23], a[168:171], v[160:163], v[20:23]\n"
    "ds_read_b128 a[168:171], v241 offset:0\n"
    "v_mfma_f32_16x16x32_bf16 v[20:23], a[172:175], v[164:167], v[20:23]\n"
    "s_waitcnt lgkmcnt(7)\n"
    "ds_read_b128 a[172:175], v240 offset:4096\n"
    "v_mfma_f32_16x16x32_bf16 v[24:27], a[176:179], v[160:163], v[24:27]\n"
    "ds_write_addtid_b32 v216 offset:8192\n"
    "v_mfma_f32_16x16x32_bf16 v[24:27], a[180:183], v[164:167], v[24:27]\n"
    "ds_write_addtid_b32 v217 offset:8448\n"
    "v_mfma_f32_16x16x32_bf16 v[48:51], a[180:183], v[172:175], v[48:51]\n"
    "ds_write_addtid_b32 v208 offset:4096\n"
    "v_mfma_f32_16x16x32_bf16 v[48:51], a[176:179], v[168:171], v[48:51]\n"
    "ds_write_addtid_b32 v209 offset:4352\n"
    "v_mfma_f32_16x16x32_bf16 v[128:131], a[176:179], v[176:179], v[128:131]\n"
    "ds_write_addtid_b32 v200 offset:0\n"
    "v_mfma_f32_16x16x32_bf16 v[128:131], a[180:183], v[180:183], v[128:131]\n"
    "ds_write_addtid_b32 v201 offset:256\n"
    "v_mfma_f32_16x16x32_bf16 v[132:135], a[180:183], v[188:191], v[132:135]\n"
    "ds_write_addtid_b32 v234 offset:16896\n"
    "v_mfma_f32_16x16x32_bf16 v[132:135], a[176:179], v[184:187], v[132:135]\n"
    "ds_write_addtid_b32 v235 offset:17152\n"
    "v_mfma_f32_16x16x32_bf16 v[136:139], a[176:179], v[192:195], v[136:139]\n"
    "ds_read_b128 a[176:179], v241 offset:4096\n"
    "v_mfma_f32_16x16x32_bf16 v[136:139], a[180:183], v[196:199], v[136:139]\n"
    "ds_read_b128 a[180:183], v242 offset:0\n"
    "v_mfma_f32_16x16x32_bf16 v[148:151], a[184:187], v[192:195], v[148:151]\n"
    "ds_write_addtid_b32 v226 offset:12800\n"
    "v_mfma_f32_16x16x32_bf16 v[148:151], a[188:191], v[196:199], v[148:151]\n"
    "ds_write_addtid_b32 v227 offset:13056\n"
    "v_mfma_f32_16x16x32_bf16 v[144:147], a[188:191], v[188:191], v[144:147]\n"
    "ds_write_addtid_b32 v218 offset:8704\n"
    "v_mfma_f32_16x16x32_bf16 v[144:147], a[184:187], v[184:187], v[144:147]\n"
    "ds_write_addtid_b32 v219 offset:8960\n"
    "v_mfma_f32_16x16x32_bf16 v[140:143], a[184:187], v[176:179], v[140:143]\n"
    "ds_write_addtid_b32 v210 offset:4608\n"
    "v_mfma_f32_16x16x32_bf16 v[140:143], a[188:191], v[180:183], v[140:143]\n"
    "ds_write_addtid_b32 v211 offset:4864\n"
    "v_mfma_f32_16x16x32_bf16 v[52:55], a[188:191], v[172:175], v[52:55]\n"
    "ds_write_addtid_b32 v202 offset:512\n"
    "v_mfma_f32_16x16x32_bf16 v[52:55], a[184:187], v[168:171], v[52:55]\n"
    "ds_write_addtid_b32 v203 offset:768\n"
    "v_mfma_f32_16x16x32_bf16 v[28:31], a[184:187], v[160:163], v[28:31]\n"
    "ds_read_b128 a[184:187], v243 offset:0\n"
    "v_mfma_f32_16x16x32_bf16 v[28:31], a[188:191], v[164:167], v[28:31]\n"
    "s_waitcnt lgkmcnt(15)\n"
    "ds_read_b128 a[188:191], v242 offset:4096\n"
    "v_mfma_f32_16x16x32_bf16 v[160:163], a[160:163], a[28:31], 0\n"
    "v_lshlrev_b32_e32 v253, 2, v249\n"
    "v_mfma_f32_16x16x32_bf16 v[160:163], a[164:167], a[0:3], v[160:163]\n"
    "v_mfma_f32_16x16x32_bf16 v[168:171], a[164:167], a[32:35], 0\n"
    "v_mfma_f32_16x16x32_bf16 v[168:171], a[160:163], a[60:63], v[168:171]\n"
    "v_mfma_f32_16x16x32_bf16 v[176:179], a[160:163], a[92:95], 0\n"
    "v_mfma_f32_16x16x32_bf16 v[176:179], a[164:167], a[64:67], v[176:179]\n"
    "s_waitcnt lgkmcnt(0)\n"
    "v_mfma_f32_16x16x32_bf16 v[184:187], a[164:167], a[96:99], 0\n"
    "s_barrier\n"
    "v_mfma_f32_16x16x32_bf16 v[184:187], a[160:163], a[124:127], v[184:187]\n"
    "s_add_u32 m0, s43, 32768\n"
    "ds_read2st64_b32 v[204:205], v250 offset0:0 offset1:1\n"
    "v_mfma_f32_16x16x32_bf16 v[192:195], a[160:163], a[156:159], 0\n"
    "ds_read_b128 a[160:163], v243 offset:6144\n"
    "v_mfma_f32_16x16x32_bf16 v[192:195], a[164:167], a[128:131], v[192:195]\n"
    "ds_read_b128 a[164:167], v240 offset:2048\n"
    "v_mfma_f32_16x16x32_bf16 v[192:195], a[168:171], a[132:135], v[192:195]\n"
    "buffer_load_dwordx4 v248, s[8:11], s40 offen offset:0 sc1 lds\n"
    "v_mfma_f32_16x16x32_bf16 v[192:195], a[172:175], a[136:139], v[192:195]\n"
    "v_mfma_f32_16x16x32_bf16 v[184:187], a[172:175], a[104:107], v[184:187]\n"
    "v_mfma_f32_16x16x32_bf16 v[184:187], a[168:171], a[100:103], v[184:187]\n"
    "ds_read2st64_b32 v[206:207], v250 offset0:2 offset1:3\n"
    "v_mfma_f32_16x16x32_bf16 v[176:179], a[168:171], a[68:71], v[176:179]\n"
    "v_mfma_f32_16x16x32_bf16 v[176:179], a[172:175], a[72:75], v[176:179]\n"
    "buffer_load_dwordx4 v248, s[8:11], s40 offen offset:1024 sc1 lds\n"
    "v_mfma_f32_16x16x32_bf16 v[168:171], a[172:175], a[40:43], v[168:171]\n"
    "v_mfma_f32_16x16x32_bf16 v[168:171], a[168:171], a[36:39], v[168:171]\n"
    "ds_read2st64_b32 v[212:213], v250 offset0:16 offset1:17\n"
    "v_mfma_f32_16x16x32_bf16 v[160:163], a[168:171], a[4:7], v[160:163]\n"
    "ds_read_b128 a[168:171], v241 offset:2048\n"
    "v_mfma_f32_16x16x32_bf16 v[160:163], a[172:175], a[8:11], v[160:163]\n"
    "ds_read_b128 a[172:175], v240 offset:6144\n"
    "v_mfma_f32_16x16x32_bf16 v[160:163], a[176:179], a[12:15], v[160:163]\n"
    "buffer_load_dwordx4 v248, s[8:11], s40 offen offset:2048 sc1 lds\n"
    "v_mfma_f32_16x16x32_bf16 v[160:163], a[180:183], a[16:19], v[160:163]\n"
    "ds_read2st64_b32 v[214:215], v250 offset0:18 offset1:19\n"
    "v_mfma_f32_16x16x32_bf16 v[168:171], a[180:183], a[48:51], v[168:171]\n"
    "v_mfma_f32_16x16x32_bf16 v[168:171], a[176:179], a[44:47], v[168:171]\n"
    "v_mfma_f32_16x16x32_bf16 v[176:179], a[176:179], a[76:79], v[176:179]\n"
    "ds_read2st64_b32 v[220:221], v250 offset0:32 offset1:33\n"
    "v_mfma_f32_16x16x32_bf16 v[176:179], a[180:183], a[80:83], v[176:179]\n"
    "buffer_load_dwordx4 v248, s[8:11], s40 offen offset:3072 sc1 lds\n"
    "v_mfma_f32_16x16x32_bf16 v[184:187], a[180:183], a[112:115], v[184:187]\n"
    "s_add_u32 m0, s43, 36864\n"
    "v_mfma_f32_16x16x32_bf16 v[184:187], a[176:179], a[108:111], v[184:187]\n"
    "s_add_u32 s40, s40, 0x8000\n"
    "ds_read2st64_b32 v[222:223], v250 offset0:34 offset1:35\n"
    "v_mfma_f32_16x16x32_bf16 v[192:195], a[176:179], a[140:143], v[192:195]\n"
    "ds_read_b128 a[176:179], v241 offset:6144\n"
    "v_mfma_f32_16x16x32_bf16 v[192:195], a[180:183], a[144:147], v[192:195]\n"
    "ds_read_b128 a[180:183], v242 offset:2048\n"
    "v_mfma_f32_16x16x32_bf16 v[192:195], a[184:187], a[148:151], v[192:195]\n"
    "buffer_load_dwordx4 v248, s[8:11], s41 offen offset:0 sc1 lds\n"
    "v_mfma_f32_16x16x32_bf16 v[192:195], a[188:191], a[152:155], v[192:195]\n"
    "v_mfma_f32_16x16x32_bf16 v[184:187], a[188:191], a[120:123], v[184:187]\n"
    "ds_read2st64_b32 v[228:229], v250 offset0:48 offset1:49\n"
    "v_mfma_f32_16x16x32_bf16 v[184:187], a[184:187], a[116:119], v[184:187]\n"
    "v_mfma_f32_16x16x32_bf16 v[176:179], a[184:187], a[84:87], v[176:179]\n"
    "v_exp_f32_e32 v192, v192\n"
    "v_mfma_f32_16x16x32_bf16 v[176:179], a[188:191], a[88:91], v[176:179]\n"
    "buffer_load_dwordx4 v248, s[8:11], s41 offen offset:1024 sc1 lds\n"
    "v_mfma_f32_16x16x32_bf16 v[168:171], a[188:191], a[56:59], v[168:171]\n"
    "ds_read2st64_b32 v[230:231], v250 offset0:50 offset1:51\n"
    "v_mfma_f32_16x16x32_bf16 v[168:171], a[184:187], a[52:55], v[168:171]\n"
    "v_exp_f32_e32 v193, v193\n"
    "v_mfma_f32_16x16x32_bf16 v[160:163], a[184:187], a[20:23], v[160:163]\n"
    "ds_read_b128 a[184:187], v243 offset:2048\n"
    "v_mfma_f32_16x16x32_bf16 v[160:163], a[188:191], a[24:27], v[160:163]\n"
    "s_waitcnt lgkmcnt(8)\n"
    "ds_read_b128 a[188:191], v242 offset:6144\n"
    "v_mfma_f32_16x16x32_bf16 v[164:167], a[160:163], a[28:31], 0\n"
    "buffer_load_dwordx4 v248, s[8:11], s41 offen offset:2048 sc1 lds\n"
    "v_mfma_f32_16x16x32_bf16 v[164:167], a[164:167], a[0:3], v[164:167]\n"
    "ds_read2st64_b32 v[236:237], v250 offset0:64 offset1:65\n"
    "v_mfma_f32_16x16x32_bf16 v[172:175], a[164:167], a[32:35], 0\n"
    "v_exp_f32_e32 v194, v194\n"
    "v_mfma_f32_16x16x32_bf16 v[172:175], a[160:163], a[60:63], v[172:175]\n"
    "ds_read2st64_b32 v[238:239], v250 offset0:66 offset1:67\n"
    "v_mfma_f32_16x16x32_bf16 v[180:183], a[160:163], a[92:95], 0\n"
    "v_exp_f32_e32 v195, v195\n"
    "v_mfma_f32_16x16x32_bf16 v[180:183], a[164:167], a[64:67], v[180:183]\n"
    "buffer_load_dwordx4 v248, s[8:11], s41 offen offset:3072 sc1 lds\n"
    "v_cvt_pk_bf16_f32 v192, v192, v193\n"
    "v_mfma_f32_16x16x32_bf16 v[188:191], a[164:167], a[96:99], 0\n"
    "s_add_u32 s41, s41, 0x8000\n"
    "v_exp_f32_e32 v184, v184\n"
    "v_mfma_f32_16x16x32_bf16 v[188:191], a[160:163], a[124:127], v[188:191]\n"
    "v_exp_f32_e32 v185, v185\n"
    "v_mfma_f32_16x16x32_bf16 v[196:199], a[160:163], a[156:159], 0\n"
    "ds_read_b64_tr_b16 a[160:161], v244 offset:49152\n"
    "v_mfma_f32_16x16x32_bf16 v[196:199], a[164:167], a[128:131], v[196:199]\n"
    "ds_read_b64_tr_b16 a[162:163], v244 offset:51200\n"
    "v_mfma_f32_16x16x32_bf16 v[196:199], a[168:171], a[132:135], v[196:199]\n"
    "ds_read_b64_tr_b16 a[164:165], v244 offset:57344\n"
    "v_mfma_f32_16x16x32_bf16 v[196:199], a[172:175], a[136:139], v[196:199]\n"
    "ds_read_b64_tr_b16 a[166:167], v244 offset:59392\n"
    "v_mfma_f32_16x16x32_bf16 v[188:191], a[172:175], a[104:107], v[188:191]\n"
    "v_cvt_pk_bf16_f32 v193, v194, v195\n"
    "v_mfma_f32_16x16x32_bf16 v[188:191], a[168:171], a[100:103], v[188:191]\n"
    "v_exp_f32_e32 v186, v186\n"
    "v_mfma_f32_16x16x32_bf16 v[180:183], a[168:171], a[68:71], v[180:183]\n"
    "v_exp_f32_e32 v187, v187\n"
    "v_mfma_f32_16x16x32_bf16 v[180:183], a[172:175], a[72:75], v[180:183]\n"
    "v_cvt_pk_bf16_f32 v184, v184, v185\n"
    "v_mfma_f32_16x16x32_bf16 v[172:175], a[172:175], a[40:43], v[172:175]\n"
    "v_exp_f32_e32 v176, v176\n"
    "v_mfma_f32_16x16x32_bf16 v[172:175], a[168:171], a[36:39], v[172:175]\n"
    "v_exp_f32_e32 v177, v177\n"
    "v_mfma_f32_16x16x32_bf16 v[164:167], a[168:171], a[4:7], v[164:167]\n"
    "ds_read_b64_tr_b16 a[168:169], v245 offset:49152\n"
    "v_mfma_f32_16x16x32_bf16 v[164:167], a[172:175], a[8:11], v[164:167]\n"
    "s_waitcnt lgkmcnt(7)\n"
    "ds_read_b64_tr_b16 a[170:171], v245 offset:51200\n"
    "v_mfma_f32_16x16x32_bf16 v[164:167], a[176:179], a[12:15], v[164:167]\n"
    "s_mov_b32 m0, s82\n"
    "ds_read_b64_tr_b16 a[172:173], v245 offset:57344\n"
    "v_mfma_f32_16x16x32_bf16 v[164:167], a[180:183], a[16:19], v[164:167]\n"
    "s_waitcnt vmcnt(8)\n"
    "ds_read_b64_tr_b16 a[174:175], v245 offset:59392\n"
    "v_mfma_f32_16x16x32_bf16 v[172:175], a[180:183], a[48:51], v[172:175]\n"
    "buffer_load_dwordx4 v253, s[8:11], s46 offen offset:0 sc1 lds\n"
    "v_mfma_f32_16x16x32_bf16 v[172:175], a[176:179], a[44:47], v[172:175]\n"
    "v_cvt_pk_bf16_f32 v185, v186, v187\n"
    "v_mfma_f32_16x16x32_bf16 v[180:183], a[176:179], a[76:79], v[180:183]\n"
    "v_exp_f32_e32 v178, v178\n"
    "v_mfma_f32_16x16x32_bf16 v[180:183], a[180:183], a[80:83], v[180:183]\n"
    "v_exp_f32_e32 v179, v179\n"
    "v_mfma_f32_16x16x32_bf16 v[188:191], a[180:183], a[112:115], v[188:191]\n"
    "v_cvt_pk_bf16_f32 v176, v176, v177\n"
    "v_mfma_f32_16x16x32_bf16 v[188:191], a[176:179], a[108:111], v[188:191]\n"
    "v_exp_f32_e32 v168, v168\n"
    "v_mfma_f32_16x16x32_bf16 v[196:199], a[176:179], a[140:143], v[196:199]\n"
    "ds_read_b64_tr_b16 a[176:177], v246 offset:49152\n"
    "v_mfma_f32_16x16x32_bf16 v[196:199], a[180:183], a[144:147], v[196:199]\n"
    "ds_read_b64_tr_b16 a[178:179], v246 offset:51200\n"
    "v_mfma_f32_16x16x32_bf16 v[196:199], a[184:187], a[148:151], v[196:199]\n"
    "ds_read_b64_tr_b16 a[180:181], v246 offset:57344\n"
    "v_mfma_f32_16x16x32_bf16 v[196:199], a[188:191], a[152:155], v[196:199]\n"
    "ds_read_b64_tr_b16 a[182:183], v246 offset:59392\n"
    "v_mfma_f32_16x16x32_bf16 v[188:191], a[188:191], a[120:123], v[188:191]\n"
    "v_exp_f32_e32 v169, v169\n"
    "v_mfma_f32_16x16x32_bf16 v[188:191], a[184:187], a[116:119], v[188:191]\n"
    "v_cvt_pk_bf16_f32 v177, v178, v179\n"
    "v_mfma_f32_16x16x32_bf16 v[180:183], a[184:187], a[84:87], v[180:183]\n"
    "v_exp_f32_e32 v170, v170\n"
    "v_mfma_f32_16x16x32_bf16 v[180:183], a[188:191], a[88:91], v[180:183]\n"
    "v_exp_f32_e32 v171, v171\n"
    "v_mfma_f32_16x16x32_bf16 v[172:175], a[188:191], a[56:59], v[172:175]\n"
    "v_cvt_pk_bf16_f32 v168, v168, v169\n"
    "v_mfma_f32_16x16x32_bf16 v[172:175], a[184:187], a[52:55], v[172:175]\n"
    "v_exp_f32_e32 v160, v160\n"
    "v_mfma_f32_16x16x32_bf16 v[164:167], a[184:187], a[20:23], v[164:167]\n"
    "ds_read_b64_tr_b16 a[184:185], v247 offset:49152\n"
    "v_mfma_f32_16x16x32_bf16 v[164:167], a[188:191], a[24:27], v[164:167]\n"
    "s_waitcnt lgkmcnt(5)\n"
    "ds_read_b64_tr_b16 a[186:187], v247 offset:51200\n"
    "v_mfma_f32_16x16x32_bf16 v[0:3], a[160:163], v[200:203], v[0:3]\n"
    "ds_read_b64_tr_b16 a[188:189], v247 offset:57344\n"
    "v_mfma_f32_16x16x32_bf16 v[0:3], a[164:167], v[204:207], v[0:3]\n"
    "ds_read_b64_tr_b16 a[190:191], v247 offset:59392\n"
    "v_mfma_f32_16x16x32_bf16 a[248:251], a[164:167], v[212:215], a[248:251]\n"
    "v_exp_f32_e32 v161, v161\n"
    "v_mfma_f32_16x16x32_bf16 a[248:251], a[160:163], v[208:211], a[248:251]\n"
    "v_cvt_pk_bf16_f32 v169, v170, v171\n"
    "v_mfma_f32_16x16x32_bf16 v[56:59], a[160:163], v[216:219], v[56:59]\n"
    "v_exp_f32_e32 v162, v162\n"
    "v_mfma_f32_16x16x32_bf16 v[56:59], a[164:167], v[220:223], v[56:59]\n"
    "v_exp_f32_e32 v163, v163\n"
    "v_mfma_f32_16x16x32_bf16 v[60:63], a[164:167], v[228:231], v[60:63]\n"
    "v_cvt_pk_bf16_f32 v160, v160, v161\n"
    "v_mfma_f32_16x16x32_bf16 v[60:63], a[160:163], v[224:227], v[60:63]\n"
    "v_cvt_pk_bf16_f32 v161, v162, v163\n"
    "v_mfma_f32_16x16x32_bf16 v[64:67], a[160:163], v[232:235], v[64:67]\n"
    "ds_read_b64_tr_b16 a[160:161], v244 offset:53248\n"
    "v_mfma_f32_16x16x32_bf16 v[64:67], a[164:167], v[236:239], v[64:67]\n"
    "ds_read_b64_tr_b16 a[162:163], v244 offset:55296\n"
    "v_mfma_f32_16x16x32_bf16 v[76:79], a[168:171], v[232:235], v[76:79]\n"
    "ds_read_b64_tr_b16 a[164:165], v244 offset:61440\n"
    "v_mfma_f32_16x16x32_bf16 v[76:79], a[172:175], v[236:239], v[76:79]\n"
    "ds_read_b64_tr_b16 a[166:167], v244 offset:63488\n"
    "v_mfma_f32_16x16x32_bf16 v[72:75], a[172:175], v[228:231], v[72:75]\n"
    "v_exp_f32_e32 v196, v196\n"
    "v_mfma_f32_16x16x32_bf16 v[72:75], a[168:171], v[224:227], v[72:75]\n"
    "v_exp_f32_e32 v197, v197\n"
    "v_mfma_f32_16x16x32_bf16 v[68:71], a[168:171], v[216:219], v[68:71]\n"
    "v_exp_f32_e32 v198, v198\n"
    "v_mfma_f32_16x16x32_bf16 v[68:71], a[172:175], v[220:223], v[68:71]\n"
    "v_exp_f32_e32 v199, v199\n"
    "v_mfma_f32_16x16x32_bf16 a[252:255], a[172:175], v[212:215], a[252:255]\n"
    "v_cvt_pk_bf16_f32 v194, v196, v197\n"
    "v_mfma_f32_16x16x32_bf16 a[252:255], a[168:171], v[208:211], a[252:255]\n"
    "v_exp_f32_e32 v188, v188\n"
    "v_mfma_f32_16x16x32_bf16 v[4:7], a[168:171], v[200:203], v[4:7]\n"
    "ds_read_b64_tr_b16 a[168:169], v245 offset:53248\n"
    "v_mfma_f32_16x16x32_bf16 v[4:7], a[172:175], v[204:207], v[4:7]\n"
    "s_waitcnt lgkmcnt(5)\n"
    "ds_read_b64_tr_b16 a[170:171], v245 offset:55296\n"
    "v_mfma_f32_16x16x32_bf16 v[8:11], a[176:179], v[200:203], v[8:11]\n"
    "ds_read_b64_tr_b16 a[172:173], v245 offset:61440\n"
    "v_mfma_f32_16x16x32_bf16 v[8:11], a[180:183], v[204:207], v[8:11]\n"
    "ds_read_b64_tr_b16 a[174:175], v245 offset:63488\n"
    "v_mfma_f32_16x16x32_bf16 v[32:35], a[180:183], v[212:215], v[32:35]\n"
    "v_exp_f32_e32 v189, v189\n"
    "v_mfma_f32_16x16x32_bf16 v[32:35], a[176:179], v[208:211], v[32:35]\n"
    "v_cvt_pk_bf16_f32 v195, v198, v199\n"
    "v_mfma_f32_16x16x32_bf16 v[80:83], a[176:179], v[216:219], v[80:83]\n"
    "v_exp_f32_e32 v190, v190\n"
    "v_mfma_f32_16x16x32_bf16 v[80:83], a[180:183], v[220:223], v[80:83]\n"
    "v_exp_f32_e32 v191, v191\n"
    "v_mfma_f32_16x16x32_bf16 v[84:87], a[180:183], v[228:231], v[84:87]\n"
    "v_cvt_pk_bf16_f32 v186, v188, v189\n"
    "v_mfma_f32_16x16x32_bf16 v[84:87], a[176:179], v[224:227], v[84:87]\n"
    "v_exp_f32_e32 v180, v180\n"
    "v_mfma_f32_16x16x32_bf16 v[88:91], a[176:179], v[232:235], v[88:91]\n"
    "ds_read_b64_tr_b16 a[176:177], v246 offset:53248\n"
    "v_mfma_f32_16x16x32_bf16 v[88:91], a[180:183], v[236:239], v[88:91]\n"
    "ds_read_b64_tr_b16 a[178:179], v246 offset:55296\n"
    "v_mfma_f32_16x16x32_bf16 v[100:103], a[184:187], v[232:235], v[100:103]\n"
    "ds_read_b64_tr_b16 a[180:181], v246 offset:61440\n"
    "v_mfma_f32_16x16x32_bf16 v[100:103], a[188:191], v[236:239], v[100:103]\n"
    "ds_read_b64_tr_b16 a[182:183], v246 offset:63488\n"
    "v_mfma_f32_16x16x32_bf16 v[96:99], a[188:191], v[228:231], v[96:99]\n"
    "v_exp_f32_e32 v181, v181\n"
    "v_mfma_f32_16x16x32_bf16 v[96:99], a[184:187], v[224:227], v[96:99]\n"
    "v_cvt_pk_bf16_f32 v187, v190, v191\n"
    "v_mfma_f32_16x16x32_bf16 v[92:95], a[184:187], v[216:219], v[92:95]\n"
    "v_exp_f32_e32 v182, v182\n"
    "v_mfma_f32_16x16x32_bf16 v[92:95], a[188:191], v[220:223], v[92:95]\n"
    "v_exp_f32_e32 v183, v183\n"
    "v_mfma_f32_16x16x32_bf16 v[36:39], a[188:191], v[212:215], v[36:39]\n"
    "v_cvt_pk_bf16_f32 v178, v180, v181\n"
    "v_mfma_f32_16x16x32_bf16 v[36:39], a[184:187], v[208:211], v[36:39]\n"
    "v_exp_f32_e32 v172, v172\n"
    "v_mfma_f32_16x16x32_bf16 v[12:15], a[184:187], v[200:203], v[12:15]\n"
    "s_mov_b32 m0, s49\n"
    "ds_read_b64_tr_b16 a[184:185], v247 offset:53248\n"
    "v_mfma_f32_16x16x32_bf16 v[12:15], a[188:191], v[204:207], v[12:15]\n"
    "s_waitcnt lgkmcnt(5)\n"
    "ds_read_b64_tr_b16 a[186:187], v247 offset:55296\n"
    "v_mfma_f32_16x16x32_bf16 v[16:19], a[160:163], v[200:203], v[16:19]\n"
    "ds_read_b64_tr_b16 a[188:189], v247 offset:61440\n"
    "v_mfma_f32_16x16x32_bf16 v[16:19], a[164:167], v[204:207], v[16:19]\n"
    "ds_read_b64_tr_b16 a[190:191], v247 offset:63488\n"
    "v_mfma_f32_16x16x32_bf16 v[40:43], a[164:167], v[212:215], v[40:43]\n"
    "v_exp_f32_e32 v173, v173\n"
    "v_mfma_f32_16x16x32_bf16 v[40:43], a[160:163], v[208:211], v[40:43]\n"
    "v_cvt_pk_bf16_f32 v179, v182, v183\n"
    "v_mfma_f32_16x16x32_bf16 v[104:107], a[160:163], v[216:219], v[104:107]\n"
    "v_exp_f32_e32 v174, v174\n"
    "v_mfma_f32_16x16x32_bf16 v[104:107], a[164:167], v[220:223], v[104:107]\n"
    "s_waitcnt vmcnt(9)\n"
    "v_mfma_f32_16x16x32_bf16 v[108:111], a[164:167], v[228:231], v[108:111]\n"
    "s_barrier\n"
    "v_mfma_f32_16x16x32_bf16 v[108:111], a[160:163], v[224:227], v[108:111]\n"
    "v_exp_f32_e32 v175, v175\n"
    "v_mfma_f32_16x16x32_bf16 v[112:115], a[160:163], v[232:235], v[112:115]\n"
    "ds_read_b128 a[160:163], v243 offset:20480\n"
    "v_mfma_f32_16x16x32_bf16 v[112:115], a[164:167], v[236:239], v[112:115]\n"
    "ds_read_b128 a[164:167], v240 offset:16384\n"
    "v_mfma_f32_16x16x32_bf16 v[124:127], a[168:171], v[232:235], v[124:127]\n"
    "ds_write_addtid_b32 v192 offset:16384\n"
    "v_cvt_pk_bf16_f32 v170, v172, v173\n"
    "v_mfma_f32_16x16x32_bf16 v[124:127], a[172:175], v[236:239], v[124:127]\n"
    "v_exp_f32_e32 v164, v164\n"
    "v_mfma_f32_16x16x32_bf16 v[120:123], a[172:175], v[228:231], v[120:123]\n"
    "v_exp_f32_e32 v165, v165\n"
    "v_mfma_f32_16x16x32_bf16 v[120:123], a[168:171], v[224:227], v[120:123]\n"
    "ds_write_addtid_b32 v193 offset:16640\n"
    "v_cvt_pk_bf16_f32 v171, v174, v175\n"
    "v_mfma_f32_16x16x32_bf16 v[116:119], a[168:171], v[216:219], v[116:119]\n"
    "v_exp_f32_e32 v166, v166\n"
    "v_mfma_f32_16x16x32_bf16 v[116:119], a[172:175], v[220:223], v[116:119]\n"
    "v_exp_f32_e32 v167, v167\n"
    "v_mfma_f32_16x16x32_bf16 v[44:47], a[172:175], v[212:215], v[44:47]\n"
    "ds_write_addtid_b32 v184 offset:12288\n"
    "v_cvt_pk_bf16_f32 v162, v164, v165\n"
    "v_mfma_f32_16x16x32_bf16 v[44:47], a[168:171], v[208:211], v[44:47]\n"
    "ds_write_addtid_b32 v185 offset:12544\n"
    "v_cvt_pk_bf16_f32 v163, v166, v167\n"
    "v_mfma_f32_16x16x32_bf16 v[20:23], a[168:171], v[200:203], v[20:23]\n"
    "ds_read_b128 a[168:171], v241 offset:16384\n"
    "v_mfma_f32_16x16x32_bf16 v[20:23], a[172:175], v[204:207], v[20:23]\n"
    "s_waitcnt lgkmcnt(7)\n"
    "ds_read_b128 a[172:175], v240 offset:20480\n"
    "v_mfma_f32_16x16x32_bf16 v[24:27], a[176:179], v[200:203], v[24:27]\n"
    "ds_write_addtid_b32 v176 offset:8192\n"
    "v_mfma_f32_16x16x32_bf16 v[24:27], a[180:183], v[204:207], v[24:27]\n"
    "ds_write_addtid_b32 v177 offset:8448\n"
    "v_mfma_f32_16x16x32_bf16 v[48:51], a[180:183], v[212:215], v[48:51]\n"
    "ds_write_addtid_b32 v168 offset:4096\n"
    "v_mfma_f32_16x16x32_bf16 v[48:51], a[176:179], v[208:211], v[48:51]\n"
    "ds_write_addtid_b32 v169 offset:4352\n"
    "v_mfma_f32_16x16x32_bf16 v[128:131], a[176:179], v[216:219], v[128:131]\n"
    "ds_write_addtid_b32 v160 offset:0\n"
    "v_mfma_f32_16x16x32_bf16 v[128:131], a[180:183], v[220:223], v[128:131]\n"
    "ds_write_addtid_b32 v161 offset:256\n"
    "v_mfma_f32_16x16x32_bf16 v[132:135], a[180:183], v[228:231], v[132:135]\n"
    "ds_write_addtid_b32 v194 offset:16896\n"
    "v_mfma_f32_16x16x32_bf16 v[132:135], a[176:179], v[224:227], v[132:135]\n"
    "ds_write_addtid_b32 v195 offset:17152\n"
    "v_mfma_f32_16x16x32_bf16 v[136:139], a[176:179], v[232:235], v[136:139]\n"
    "ds_read_b128 a[176:179], v241 offset:20480\n"
    "v_mfma_f32_16x16x32_bf16 v[136:139], a[180:183], v[236:239], v[136:139]\n"
    "ds_read_b128 a[180:183], v242 offset:16384\n"
    "v_mfma_f32_16x16x32_bf16 v[148:151], a[184:187], v[232:235], v[148:151]\n"
    "ds_write_addtid_b32 v186 offset:12800\n"
    "v_mfma_f32_16x16x32_bf16 v[148:151], a[188:191], v[236:239], v[148:151]\n"
    "ds_write_addtid_b32 v187 offset:13056\n"
    "v_mfma_f32_16x16x32_bf16 v[144:147], a[188:191], v[228:231], v[144:147]\n"
    "ds_write_addtid_b32 v178 offset:8704\n"
    "v_mfma_f32_16x16x32_bf16 v[144:147], a[184:187], v[224:227], v[144:147]\n"
    "ds_write_addtid_b32 v179 offset:8960\n"
    "v_mfma_f32_16x16x32_bf16 v[140:143], a[184:187], v[216:219], v[140:143]\n"
    "s_sub_u32 s44, s44, 1\n"
    "ds_write_addtid_b32 v170 offset:4608\n"
    "v_mfma_f32_16x16x32_bf16 v[140:143], a[188:191], v[220:223], v[140:143]\n"
    "ds_write_addtid_b32 v171 offset:4864\n"
    "v_mfma_f32_16x16x32_bf16 v[52:55], a[188:191], v[212:215], v[52:55]\n"
    "ds_write_addtid_b32 v162 offset:512\n"
    "v_mfma_f32_16x16x32_bf16 v[52:55], a[184:187], v[208:211], v[52:55]\n"
    "ds_write_addtid_b32 v163 offset:768\n"
    "v_mfma_f32_16x16x32_bf16 v[28:31], a[184:187], v[200:203], v[28:31]\n"
    "ds_read_b128 a[184:187], v243 offset:16384\n"
    "v_mfma_f32_16x16x32_bf16 v[28:31], a[188:191], v[204:207], v[28:31]\n"
    "s_waitcnt lgkmcnt(15)\n"
    "ds_read_b128 a[188:191], v242 offset:20480\n"
    "s_cbranch_scc0 .Lat_p\n"
    "s_cmp_lg_u32 s37, 0\n"
    "s_cbranch_scc1 .Lat_tail\n"
    "s_waitcnt vmcnt(0)\n"
    "v_cmp_ne_u32_e32 vcc, s39, v154\n"
    "s_mov_b32 s37, 1\n"
    "s_lshr_b32 s44, s34, 14\n"
    "s_add_u32 s44, s44, 3\n"
    "s_nop 1\n"
    "s_cbranch_vccnz .Lat_lpoll\n"
    ".p2align 4\n"
    ".Lat_g:\n"
    "v_mfma_f32_16x16x32_bf16 v[200:203], a[160:163], a[28:31], 0\n"
    "v_mfma_f32_16x16x32_bf16 v[200:203], a[164:167], a[0:3], v[200:203]\n"
    "v_mfma_f32_16x16x32_bf16 v[208:211], a[164:167], a[32:35], 0\n"
    "v_mfma_f32_16x16x32_bf16 v[208:211], a[160:163], a[60:63], v[208:211]\n"
    "v_mfma_f32_16x16x32_bf16 v[216:219], a[160:163], a[92:95], 0\n"
    "v_mfma_f32_16x16x32_bf16 v[216:219], a[164:167], a[64:67], v[216:219]\n"
    "s_waitcnt lgkmcnt(0)\n"
    "v_mfma_f32_16x16x32_bf16 v[224:227], a[164:167], a[96:99], 0\n"
    "s_barrier\n"
    "v_mfma_f32_16x16x32_bf16 v[224:227], a[160:163], a[124:127], v[224:227]\n"
    "s_add_u32 m0, s43, 49152\n"
    "ds_read2st64_b32 v[164:165], v250 offset0:0 offset1:1\n"
    "v_mfma_f32_16x16x32_bf16 v[232:235], a[160:163], a[156:159], 0\n"
    "ds_read_b128 a[160:163], v243 offset:22528\n"
    "v_mfma_f32_16x16x32_bf16 v[232:235], a[164:167], a[128:131], v[232:235]\n"
    "ds_read_b128 a[164:167], v240 offset:18432\n"
    "v_mfma_f32_16x16x32_bf16 v[232:235], a[168:171], a[132:135], v[232:235]\n"
    "buffer_load_dwordx4 v248, s[8:11], s40 offen offset:0 sc1 lds\n"
    "v_mfma_f32_16x16x32_bf16 v[232:235], a[172:175], a[136:139], v[232:235]\n"
    "v_mfma_f32_16x16x32_bf16 v[224:227], a[172:175], a[104:107], v[224:227]\n"
    "v_mfma_f32_16x16x32_bf16 v[224:227], a[168:171], a[100:103], v[224:227]\n"
    "ds_read2st64_b32 v[166:167], v250 offset0:2 offset1:3\n"
    "v_mfma_f32_16x16x32_bf16 v[216:219], a[168:171], a[68:71], v[216:219]\n"
    "buffer_load_dwordx4 v248, s[8:11], s40 offen offset:1024 sc1 lds\n"
    "v_mfma_f32_16x16x32_bf16 v[216:219], a[172:175], a[72:75], v[216:219]\n"
    "v_mfma_f32_16x16x32_bf16 v[208:211], a[172:175], a[40:43], v[208:211]\n"
    "v_mfma_f32_16x16x32_bf16 v[208:211], a[168:171], a[36:39], v[208:211]\n"
    "ds_read2st64_b32 v[172:173], v250 offset0:16 offset1:17\n"
    "v_mfma_f32_16x16x32_bf16 v[200:203], a[168:171], a[4:7], v[200:203]\n"
    "ds_read_b128 a[168:171], v241 offset:18432\n"
    "buffer_load_dwordx4 v248, s[8:11], s40 offen offset:2048 sc1 lds\n"
    "v_mfma_f32_16x16x32_bf16 v[200:203], a[172:175], a[8:11], v[200:203]\n"
    "ds_read_b128 a[172:175], v240 offset:22528\n"
    "v_mfma_f32_16x16x32_bf16 v[200:203], a[176:179], a[12:15], v[200:203]\n"
    "ds_read2st64_b32 v[174:175], v250 offset0:18 offset1:19\n"
    "v_mfma_f32_16x16x32_bf16 v[200:203], a[180:183], a[16:19], v[200:203]\n"
    "v_mfma_f32_16x16x32_bf16 v[208:211], a[180:183], a[48:51], v[208:211]\n"
    "buffer_load_dwordx4 v248, s[8:11], s40 offen offset:3072 sc1 lds\n"
    "v_mfma_f32_16x16x32_bf16 v[208:211], a[176:179], a[44:47], v[208:211]\n"
    "s_add_u32 m0, s43, 53248\n"
    "v_mfma_f32_16x16x32_bf16 v[216:219], a[176:179], a[76:79], v[216:219]\n"
    "s_add_u32 s40, s40, 0x8000\n"
    "ds_read2st64_b32 v[180:181], v250 offset0:32 offset1:33\n"
    "v_mfma_f32_16x16x32_bf16 v[216:219], a[180:183], a[80:83], v[216:219]\n"
    "v_mfma_f32_16x16x32_bf16 v[224:227], a[180:183], a[112:115], v[224:227]\n"
    "buffer_load_dwordx4 v248, s[8:11], s41 offen offset:0 sc1 lds\n"
    "v_mfma_f32_16x16x32_bf16 v[224:227], a[176:179], a[108:111], v[224:227]\n"
    "ds_read2st64_b32 v[182:183], v250 offset0:34 offset1:35\n"
    "v_mfma_f32_16x16x32_bf16 v[232:235], a[176:179], a[140:143], v[232:235]\n"
    "ds_read_b128 a[176:179], v241 offset:22528\n"
    "v_mfma_f32_16x16x32_bf16 v[232:235], a[180:183], a[144:147], v[232:235]\n"
    "ds_read_b128 a[180:183], v242 offset:18432\n"
    "v_mfma_f32_16x16x32_bf16 v[232:235], a[184:187], a[148:151], v[232:235]\n"
    "buffer_load_dwordx4 v248, s[8:11], s41 offen offset:1024 sc1 lds\n"
    "v_mfma_f32_16x16x32_bf16 v[232:235], a[188:191], a[152:155], v[232:235]\n"
    "v_mfma_f32_16x16x32_bf16 v[224:227], a[188:191], a[120:123], v[224:227]\n"
    "ds_read2st64_b32 v[188:189], v250 offset0:48 offset1:49\n"
    "v_mfma_f32_16x16x32_bf16 v[224:227], a[184:187], a[116:119], v[224:227]\n"
    "v_mfma_f32_16x16x32_bf16 v[216:219], a[184:187], a[84:87], v[216:219]\n"
    "buffer_load_dwordx4 v248, s[8:11], s41 offen offset:2048 sc1 lds\n"
    "v_mfma_f32_16x16x32_bf16 v[216:219], a[188:191], a[88:91], v[216:219]\n"
    "ds_read2st64_b32 v[190:191], v250 offset0:50 offset1:51\n"
    "v_mfma_f32_16x16x32_bf16 v[208:211], a[188:191], a[56:59], v[208:211]\n"
    "ds_read_b64 v[254:255], v252 offset:0\n"
    "v_mfma_f32_16x16x32_bf16 v[208:211], a[184:187], a[52:55], v[208:211]\n"
    "ds_read2st64_b32 v[196:197], v250 offset0:64 offset1:65\n"
    "v_mfma_f32_16x16x32_bf16 v[200:203], a[184:187], a[20:23], v[200:203]\n"
    "ds_read_b128 a[184:187], v243 offset:18432\n"
    "buffer_load_dwordx4 v248, s[8:11], s41 offen offset:3072 sc1 lds\n"
    "v_mfma_f32_16x16x32_bf16 v[200:203], a[188:191], a[24:27], v[200:203]\n"
    "s_waitcnt lgkmcnt(15)\n"
    "s_add_u32 s41, s41, 0x8000\n"
    "ds_read_b128 a[188:191], v242 offset:22528\n"
    "v_mfma_f32_16x16x32_bf16 v[204:207], a[160:163], a[28:31], 0\n"
    "s_waitcnt lgkmcnt(3)\n"
    "v_mad_u32_u16 v253, v254, s42, v251 op_sel:[0,0,0,0]\n"
    "v_mfma_f32_16x16x32_bf16 v[204:207], a[164:167], a[0:3], v[204:207]\n"
    "buffer_load_dwordx4 a[200:203], v253, s[20:23], 0 offen offset:0 sc1\n"
    "v_mfma_f32_16x16x32_bf16 v[212:215], a[164:167], a[32:35], 0\n"
    "v_exp_f32_e32 v232, v232\n"
    "v_mfma_f32_16x16x32_bf16 v[212:215], a[160:163], a[60:63], v[212:215]\n"
    "ds_read2st64_b32 v[198:199], v250 offset0:66 offset1:67\n"
    "v_mfma_f32_16x16x32_bf16 v[220:223], a[160:163], a[92:95], 0\n"
    "v_mad_u32_u16 v253, v254, s42, v251 op_sel:[1,0,0,0]\n"
    "v_mfma_f32_16x16x32_bf16 v[220:223], a[164:167], a[64:67], v[220:223]\n"
    "buffer_load_dwordx4 a[204:207], v253, s[20:23], 0 offen offset:0 sc1\n"
    "v_mfma_f32_16x16x32_bf16 v[228:231], a[164:167], a[96:99], 0\n"
    "v_exp_f32_e32 v233, v233\n"
    "v_mfma_f32_16x16x32_bf16 v[228:231], a[160:163], a[124:127], v[228:231]\n"
    "v_exp_f32_e32 v234, v234\n"
    "v_mfma_f32_16x16x32_bf16 v[236:239], a[160:163], a[156:159], 0\n"
    "ds_read_b64_tr_b16 a[160:161], v244 offset:0\n"
    "v_mad_u32_u16 v253, v255, s42, v251 op_sel:[0,0,0,0]\n"
    "v_mfma_f32_16x16x32_bf16 v[236:239], a[164:167], a[128:131], v[236:239]\n"
    "ds_read_b64_tr_b16 a[162:163], v244 offset:2048\n"
    "buffer_load_dwordx4 a[208:211], v253, s[20:23], 0 offen offset:0 sc1\n"
    "v_mfma_f32_16x16x32_bf16 v[236:239], a[168:171], a[132:135], v[236:239]\n"
    "ds_read_b64_tr_b16 a[164:165], v244 offset:8192\n"
    "v_mfma_f32_16x16x32_bf16 v[236:239], a[172:175], a[136:139], v[236:239]\n"
    "ds_read_b64_tr_b16 a[166:167], v244 offset:10240\n"
    "v_mfma_f32_16x16x32_bf16 v[228:231], a[172:175], a[104:107], v[228:231]\n"
    "v_mad_u32_u16 v253, v255, s42, v251 op_sel:[1,0,0,0]\n"
    "v_mfma_f32_16x16x32_bf16 v[228:231], a[168:171], a[100:103], v[228:231]\n"
    "buffer_load_dwordx4 a[212:215], v253, s[20:23], 0 offen offset:0 sc1\n"
    "ds_read_b64 v[254:255], v252 offset:8\n"
    "v_mfma_f32_16x16x32_bf16 v[220:223], a[168:171], a[68:71], v[220:223]\n"
    "v_exp_f32_e32 v235, v235\n"
    "v_mfma_f32_16x16x32_bf16 v[220:223], a[172:175], a[72:75], v[220:223]\n"
    "v_cvt_pk_bf16_f32 v232, v232, v233\n"
    "v_mfma_f32_16x16x32_bf16 v[212:215], a[172:175], a[40:43], v[212:215]\n"
    "v_exp_f32_e32 v224, v224\n"
    "v_mfma_f32_16x16x32_bf16 v[212:215], a[168:171], a[36:39], v[212:215]\n"
    "v_exp_f32_e32 v225, v225\n"
    "v_mfma_f32_16x16x32_bf16 v[204:207], a[168:171], a[4:7], v[204:207]\n"
    "ds_read_b64_tr_b16 a[168:169], v245 offset:0\n"
    "v_cvt_pk_bf16_f32 v233, v234, v235\n"
    "v_mfma_f32_16x16x32_bf16 v[204:207], a[172:175], a[8:11], v[204:207]\n"
    "ds_read_b64_tr_b16 a[170:171], v245 offset:2048\n"
    "v_mfma_f32_16x16x32_bf16 v[204:207], a[176:179], a[12:15], v[204:207]\n"
    "s_waitcnt lgkmcnt(2)\n"
    "ds_read_b64_tr_b16 a[172:173], v245 offset:8192\n"
    "v_mad_u32_u16 v253, v254, s42, v251 op_sel:[0,0,0,0]\n"
    "v_mfma_f32_16x16x32_bf16 v[204:207], a[180:183], a[16:19], v[204:207]\n"
    "ds_read_b64_tr_b16 a[174:175], v245 offset:10240\n"
    "buffer_load_dwordx4 a[216:219], v253, s[20:23], 0 offen offset:0 sc1\n"
    "v_mfma_f32_16x16x32_bf16 v[212:215], a[180:183], a[48:51], v[212:215]\n"
    "v_exp_f32_e32 v226, v226\n"
    "v_mfma_f32_16x16x32_bf16 v[212:215], a[176:179], a[44:47], v[212:215]\n"
    "v_exp_f32_e32 v227, v227\n"
    "v_mfma_f32_16x16x32_bf16 v[220:223], a[176:179], a[76:79], v[220:223]\n"
    "v_mad_u32_u16 v253, v254, s42, v251 op_sel:[1,0,0,0]\n"
    "v_cvt_pk_bf16_f32 v224, v224, v225\n"
    "v_mfma_f32_16x16x32_bf16 v[220:223], a[180:183], a[80:83], v[220:223]\n"
    "buffer_load_dwordx4 a[220:223], v253, s[20:23], 0 offen offset:0 sc1\n"
    "v_mfma_f32_16x16x32_bf16 v[228:231], a[180:183], a[112:115], v[228:231]\n"
    "v_exp_f32_e32 v216, v216\n"
    "v_mfma_f32_16x16x32_bf16 v[228:231], a[176:179], a[108:111], v[228:231]\n"
    "v_exp_f32_e32 v217, v217\n"
    "v_mfma_f32_16x16x32_bf16 v[236:239], a[176:179], a[140:143], v[236:239]\n"
    "ds_read_b64_tr_b16 a[176:177], v246 offset:0\n"
    "v_cvt_pk_bf16_f32 v225, v226, v227\n"
    "v_mfma_f32_16x16x32_bf16 v[236:239], a[180:183], a[144:147], v[236:239]\n"
    "ds_read_b64_tr_b16 a[178:179], v246 offset:2048\n"
    "v_mfma_f32_16x16x32_bf16 v[236:239], a[184:187], a[148:151], v[236:239]\n"
    "ds_read_b64_tr_b16 a[180:181], v246 offset:8192\n"
    "v_mfma_f32_16x16x32_bf16 v[236:239], a[188:191], a[152:155], v[236:239]\n"
    "ds_read_b64_tr_b16 a[182:183], v246 offset:10240\n"
    "v_mfma_f32_16x16x32_bf16 v[228:231], a[188:191], a[120:123], v[228:231]\n"
    "ds_read_b64 v[254:255], v252 offset:32\n"
    "v_mfma_f32_16x16x32_bf16 v[228:231], a[184:187], a[116:119], v[228:231]\n"
    "v_exp_f32_e32 v218, v218\n"
    "v_mfma_f32_16x16x32_bf16 v[220:223], a[184:187], a[84:87], v[220:223]\n"
    "v_exp_f32_e32 v219, v219\n"
    "v_mfma_f32_16x16x32_bf16 v[220:223], a[188:191], a[88:91], v[220:223]\n"
    "v_cvt_pk_bf16_f32 v216, v216, v217\n"
    "v_mfma_f32_16x16x32_bf16 v[212:215], a[188:191], a[56:59], v[212:215]\n"
    "s_waitcnt lgkmcnt(0)\n"
    "v_mad_u32_u16 v253, v254, s42, v251 op_sel:[0,0,0,0]\n"
    "v_mfma_f32_16x16x32_bf16 v[212:215], a[184:187], a[52:55], v[212:215]\n"
    "buffer_load_dwordx4 a[224:227], v253, s[20:23], 0 offen offset:0 sc1\n"
    "v_mfma_f32_16x16x32_bf16 v[204:207], a[184:187], a[20:23], v[204:207]\n"
    "ds_read_b64_tr_b16 a[184:185], v247 offset:0\n"
    "v_mfma_f32_16x16x32_bf16 v[204:207], a[188:191], a[24:27], v[204:207]\n"
    "ds_read_b64_tr_b16 a[186:187], v247 offset:2048\n"
    "v_mfma_f32_16x16x32_bf16 v[0:3], a[160:163], v[160:163], v[0:3]\n"
    "ds_read_b64_tr_b16 a[188:189], v247 offset:8192\n"
    "v_mad_u32_u16 v253, v254, s42, v251 op_sel:[1,0,0,0]\n"
    "v_mfma_f32_16x16x32_bf16 v[0:3], a[164:167], v[164:167], v[0:3]\n"
    "ds_read_b64_tr_b16 a[190:191], v247 offset:10240\n"
    "buffer_load_dwordx4 a[228:231], v253, s[20:23], 0 offen offset:0 sc1\n"
    "v_mfma_f32_16x16x32_bf16 a[248:251], a[164:167], v[172:175], a[248:251]\n"
    "v_exp_f32_e32 v208, v208\n"
    "v_mfma_f32_16x16x32_bf16 a[248:251], a[160:163], v[168:171], a[248:251]\n"
    "v_exp_f32_e32 v209, v209\n"
    "v_mfma_f32_16x16x32_bf16 v[56:59], a[160:163], v[176:179], v[56:59]\n"
    "v_mad_u32_u16 v253, v255, s42, v251 op_sel:[0,0,0,0]\n"
    "v_cvt_pk_bf16_f32 v217, v218, v219\n"
    "v_mfma_f32_16x16x32_bf16 v[56:59], a[164:167], v[180:183], v[56:59]\n"
    "buffer_load_dwordx4 a[232:235], v253, s[20:23], 0 offen offset:0 sc1\n"
    "v_mfma_f32_16x16x32_bf16 v[60:63], a[164:167], v[188:191], v[60:63]\n"
    "v_exp_f32_e32 v210, v210\n"
    "v_mfma_f32_16x16x32_bf16 v[60:63], a[160:163], v[184:187], v[60:63]\n"
    "v_exp_f32_e32 v211, v211\n"
    "v_mfma_f32_16x16x32_bf16 v[64:67], a[160:163], v[192:195], v[64:67]\n"
    "ds_read_b64_tr_b16 a[160:161], v244 offset:4096\n"
    "v_mad_u32_u16 v253, v255, s42, v251 op_sel:[1,0,0,0]\n"
    "v_mfma_f32_16x16x32_bf16 v[64:67], a[164:167], v[196:199], v[64:67]\n"
    "ds_read_b64_tr_b16 a[162:163], v244 offset:6144\n"
    "buffer_load_dwordx4 a[236:239], v253, s[20:23], 0 offen offset:0 sc1\n"
    "v_mfma_f32_16x16x32_bf16 v[76:79], a[168:171], v[192:195], v[76:79]\n"
    "ds_read_b64_tr_b16 a[164:165], v244 offset:12288\n"
    "v_cvt_pk_bf16_f32 v208, v208, v209\n"
    "v_mfma_f32_16x16x32_bf16 v[76:79], a[172:175], v[196:199], v[76:79]\n"
    "ds_read_b64_tr_b16 a[166:167], v244 offset:14336\n"
    "v_mfma_f32_16x16x32_bf16 v[72:75], a[172:175], v[188:191], v[72:75]\n"
    "ds_read_b64 v[254:255], v252 offset:40\n"
    "v_mfma_f32_16x16x32_bf16 v[72:75], a[168:171], v[184:187], v[72:75]\n"
    "v_exp_f32_e32 v200, v200\n"
    "v_mfma_f32_16x16x32_bf16 v[68:71], a[168:171], v[176:179], v[68:71]\n"
    "v_exp_f32_e32 v201, v201\n"
    "v_mfma_f32_16x16x32_bf16 v[68:71], a[172:175], v[180:183], v[68:71]\n"
    "v_cvt_pk_bf16_f32 v209, v210, v211\n"
    "v_mfma_f32_16x16x32_bf16 a[252:255], a[172:175], v[172:175], a[252:255]\n"
    "v_exp_f32_e32 v202, v202\n"
    "v_mfma_f32_16x16x32_bf16 a[252:255], a[168:171], v[168:171], a[252:255]\n"
    "v_exp_f32_e32 v203, v203\n"
    "v_mfma_f32_16x16x32_bf16 v[4:7], a[168:171], v[160:163], v[4:7]\n"
    "s_waitcnt lgkmcnt(0)\n"
    "ds_read_b64_tr_b16 a[168:169], v245 offset:4096\n"
    "v_mad_u32_u16 v253, v254, s42, v251 op_sel:[0,0,0,0]\n"
    "v_mfma_f32_16x16x32_bf16 v[4:7], a[172:175], v[164:167], v[4:7]\n"
    "ds_read_b64_tr_b16 a[170:171], v245 offset:6144\n"
    "v_cvt_pk_bf16_f32 v200, v200, v201\n"
    "v_mfma_f32_16x16x32_bf16 v[8:11], a[176:179], v[160:163], v[8:11]\n"
    "ds_read_b64_tr_b16 a[172:173], v245 offset:12288\n"
    "buffer_load_dwordx4 a[240:243], v253, s[20:23], 0 offen offset:0 sc1\n"
    "v_mfma_f32_16x16x32_bf16 v[8:11], a[180:183], v[164:167], v[8:11]\n"
    "ds_read_b64_tr_b16 a[174:175], v245 offset:14336\n"
    "v_cvt_pk_bf16_f32 v201, v202, v203\n"
    "v_mfma_f32_16x16x32_bf16 v[32:35], a[180:183], v[172:175], v[32:35]\n"
    "v_exp_f32_e32 v236, v236\n"
    "v_mfma_f32_16x16x32_bf16 v[32:35], a[176:179], v[168:171], v[32:35]\n"
    "v_mad_u32_u16 v253, v254, s42, v251 op_sel:[1,0,0,0]\n"
    "v_mfma_f32_16x16x32_bf16 v[80:83], a[176:179], v[176:179], v[80:83]\n"
    "buffer_load_dwordx4 a[244:247], v253, s[20:23], 0 offen offset:0 sc1\n"
    "v_mfma_f32_16x16x32_bf16 v[80:83], a[180:183], v[180:183], v[80:83]\n"
    "v_exp_f32_e32 v237, v237\n"
    "v_mfma_f32_16x16x32_bf16 v[84:87], a[180:183], v[188:191], v[84:87]\n"
    "v_exp_f32_e32 v238, v238\n"
    "v_mfma_f32_16x16x32_bf16 v[84:87], a[176:179], v[184:187], v[84:87]\n"
    "v_exp_f32_e32 v239, v239\n"
    "v_mfma_f32_16x16x32_bf16 v[88:91], a[176:179], v[192:195], v[88:91]\n"
    "ds_read_b64_tr_b16 a[176:177], v246 offset:4096\n"
    "v_cvt_pk_bf16_f32 v234, v236, v237\n"
    "v_mfma_f32_16x16x32_bf16 v[88:91], a[180:183], v[196:199], v[88:91]\n"
    "ds_read_b64_tr_b16 a[178:179], v246 offset:6144\n"
    "v_exp_f32_e32 v228, v228\n"
    "v_mfma_f32_16x16x32_bf16 v[100:103], a[184:187], v[192:195], v[100:103]\n"
    "ds_read_b64_tr_b16 a[180:181], v246 offset:12288\n"
    "v_exp_f32_e32 v229, v229\n"
    "v_mfma_f32_16x16x32_bf16 v[100:103], a[188:191], v[196:199], v[100:103]\n"
    "ds_read_b64_tr_b16 a[182:183], v246 offset:14336\n"
    "v_cvt_pk_bf16_f32 v235, v238, v239\n"
    "v_mfma_f32_16x16x32_bf16 v[96:99], a[188:191], v[188:191], v[96:99]\n"
    "v_exp_f32_e32 v230, v230\n"
    "v_mfma_f32_16x16x32_bf16 v[96:99], a[184:187], v[184:187], v[96:99]\n"
    "v_exp_f32_e32 v231, v231\n"
    "v_cvt_pk_bf16_f32 v226, v228, v229\n"
    "v_mfma_f32_16x16x32_bf16 v[92:95], a[184:187], v[176:179], v[92:95]\n"
    "v_exp_f32_e32 v220, v220\n"
    "v_mfma_f32_16x16x32_bf16 v[92:95], a[188:191], v[180:183], v[92:95]\n"
    "v_exp_f32_e32 v221, v221\n"
    "v_mfma_f32_16x16x32_bf16 v[36:39], a[188:191], v[172:175], v[36:39]\n"
    "v_cvt_pk_bf16_f32 v227, v230, v231\n"
    "v_mfma_f32_16x16x32_bf16 v[36:39], a[184:187], v[168:171], v[36:39]\n"
    "v_exp_f32_e32 v222, v222\n"
    "v_mfma_f32_16x16x32_bf16 v[12:15], a[184:187], v[160:163], v[12:15]\n"
    "s_mov_b32 m0, s49\n"
    "ds_read_b64_tr_b16 a[184:185], v247 offset:4096\n"
    "v_mfma_f32_16x16x32_bf16 v[12:15], a[188:191], v[164:167], v[12:15]\n"
    "s_waitcnt lgkmcnt(5)\n"
    "ds_read_b64_tr_b16 a[186:187], v247 offset:6144\n"
    "v_mfma_f32_16x16x32_bf16 v[16:19], a[160:163], v[160:163], v[16:19]\n"
    "ds_read_b64_tr_b16 a[188:189], v247 offset:12288\n"
    "v_mfma_f32_16x16x32_bf16 v[16:19], a[164:167], v[164:167], v[16:19]\n"
    "ds_read_b64_tr_b16 a[190:191], v247 offset:14336\n"
    "v_mfma_f32_16x16x32_bf16 v[40:43], a[164:167], v[172:175], v[40:43]\n"
    "v_exp_f32_e32 v223, v223\n"
    "v_mfma_f32_16x16x32_bf16 v[40:43], a[160:163], v[168:171], v[40:43]\n"
    "v_cvt_pk_bf16_f32 v218, v220, v221\n"
    "v_mfma_f32_16x16x32_bf16 v[104:107], a[160:163], v[176:179], v[104:107]\n"
    "v_exp_f32_e32 v212, v212\n"
    "v_mfma_f32_16x16x32_bf16 v[104:107], a[164:167], v[180:183], v[104:107]\n"
    "s_waitcnt vmcnt(20)\n"
    "v_mfma_f32_16x16x32_bf16 v[108:111], a[164:167], v[188:191], v[108:111]\n"
    "s_barrier\n"
    "v_mfma_f32_16x16x32_bf16 v[108:111], a[160:163], v[184:187], v[108:111]\n"
    "v_exp_f32_e32 v213, v213\n"
    "v_mfma_f32_16x16x32_bf16 v[112:115], a[160:163], v[192:195], v[112:115]\n"
    "ds_read_b128 a[160:163], v243 offset:36864\n"
    "v_cvt_pk_bf16_f32 v219, v222, v223\n"
    "v_mfma_f32_16x16x32_bf16 v[112:115], a[164:167], v[196:199], v[112:115]\n"
    "ds_read_b128 a[164:167], v240 offset:32768\n"
    "ds_write_addtid_b32 v232 offset:16384\n"
    "v_mfma_f32_16x16x32_bf16 v[124:127], a[168:171], v[192:195], v[124:127]\n"
    "v_exp_f32_e32 v214, v214\n"
    "v_mfma_f32_16x16x32_bf16 v[124:127], a[172:175], v[196:199], v[124:127]\n"
    "v_exp_f32_e32 v215, v215\n"
    "v_mfma_f32_16x16x32_bf16 v[120:123], a[172:175], v[188:191], v[120:123]\n"
    "ds_write_addtid_b32 v233 offset:16640\n"
    "v_cvt_pk_bf16_f32 v210, v212, v213\n"
    "v_mfma_f32_16x16x32_bf16 v[120:123], a[168:171], v[184:187], v[120:123]\n"
    "v_exp_f32_e32 v204, v204\n"
    "v_mfma_f32_16x16x32_bf16 v[116:119], a[168:171], v[176:179], v[116:119]\n"
    "v_exp_f32_e32 v205, v205\n"
    "v_mfma_f32_16x16x32_bf16 v[116:119], a[172:175], v[180:183], v[116:119]\n"
    "ds_write_addtid_b32 v224 offset:12288\n"
    "v_cvt_pk_bf16_f32 v211, v214, v215\n"
    "v_mfma_f32_16x16x32_bf16 v[44:47], a[172:175], v[172:175], v[44:47]\n"
    "v_exp_f32_e32 v206, v206\n"
    "v_mfma_f32_16x16x32_bf16 v[44:47], a[168:171], v[168:171], v[44:47]\n"
    "v_exp_f32_e32 v207, v207\n"
    "v_mfma_f32_16x16x32_bf16 v[20:23], a[168:171], v[160:163], v[20:23]\n"
    "ds_read_b128 a[168:171], v241 offset:32768\n"
    "v_cvt_pk_bf16_f32 v202, v204, v205\n"
    "v_mfma_f32_16x16x32_bf16 v[20:23], a[172:175], v[164:167], v[20:23]\n"
    "s_waitcnt lgkmcnt(6)\n"
    "ds_read_b128 a[172:175], v240 offset:36864\n"
    "v_cvt_pk_bf16_f32 v203, v206, v207\n"
    "v_mfma_f32_16x16x32_bf16 v[24:27], a[176:179], v[160:163], v[24:27]\n"
    "ds_write_addtid_b32 v225 offset:12544\n"
    "ds_write_addtid_b32 v216 offset:8192\n"
    "v_mfma_f32_16x16x32_bf16 v[24:27], a[180:183], v[164:167], v[24:27]\n"
    "ds_write_addtid_b32 v217 offset:8448\n"
    "v_mfma_f32_16x16x32_bf16 v[48:51], a[180:183], v[172:175], v[48:51]\n"
    "ds_write_addtid_b32 v208 offset:4096\n"
    "v_mfma_f32_16x16x32_bf16 v[48:51], a[176:179], v[168:171], v[48:51]\n"
    "ds_write_addtid_b32 v209 offset:4352\n"
    "v_mfma_f32_16x16x32_bf16 v[128:131], a[176:179], v[176:179], v[128:131]\n"
    "ds_write_addtid_b32 v200 offset:0\n"
    "v_mfma_f32_16x16x32_bf16 v[128:131], a[180:183], v[180:183], v[128:131]\n"
    "ds_write_addtid_b32 v201 offset:256\n"
    "v_mfma_f32_16x16x32_bf16 v[132:135], a[180:183], v[188:191], v[132:135]\n"
    "ds_write_addtid_b32 v234 offset:16896\n"
    "v_mfma_f32_16x16x32_bf16 v[132:135], a[176:179], v[184:187], v[132:135]\n"
    "ds_write_addtid_b32 v235 offset:17152\n"
    "v_mfma_f32_16x16x32_bf16 v[136:139], a[176:179], v[192:195], v[136:139]\n"
    "ds_read_b128 a[176:179], v241 offset:36864\n"
    "v_mfma_f32_16x16x32_bf16 v[136:139], a[180:183], v[196:199], v[136:139]\n"
    "ds_read_b128 a[180:183], v242 offset:32768\n"
    "v_mfma_f32_16x16x32_bf16 v[148:151], a[184:187], v[192:195], v[148:151]\n"
    "ds_write_addtid_b32 v226 offset:12800\n"
    "v_mfma_f32_16x16x32_bf16 v[148:151], a[188:191], v[196:199], v[148:151]\n"
    "ds_write_addtid_b32 v227 offset:13056\n"
    "v_mfma_f32_16x16x32_bf16 v[144:147], a[188:191], v[188:191], v[144:147]\n"
    "ds_write_addtid_b32 v218 offset:8704\n"
    "v_mfma_f32_16x16x32_bf16 v[144:147], a[184:187], v[184:187], v[144:147]\n"
    "ds_write_addtid_b32 v219 offset:8960\n"
    "v_mfma_f32_16x16x32_bf16 v[140:143], a[184:187], v[176:179], v[140:143]\n"
    "ds_write_addtid_b32 v210 offset:4608\n"
    "v_mfma_f32_16x16x32_bf16 v[140:143], a[188:191], v[180:183], v[140:143]\n"
    "ds_write_addtid_b32 v211 offset:4864\n"
    "v_mfma_f32_16x16x32_bf16 v[52:55], a[188:191], v[172:175], v[52:55]\n"
    "ds_write_addtid_b32 v202 offset:512\n"
    "v_mfma_f32_16x16x32_bf16 v[52:55], a[184:187], v[168:171], v[52:55]\n"
    "ds_write_addtid_b32 v203 offset:768\n"
    "v_mfma_f32_16x16x32_bf16 v[28:31], a[184:187], v[160:163], v[28:31]\n"
    "ds_read_b128 a[184:187], v243 offset:32768\n"
    "v_mfma_f32_16x16x32_bf16 v[28:31], a[188:191], v[164:167], v[28:31]\n"
    "s_waitcnt lgkmcnt(15)\n"
    "ds_read_b128 a[188:191], v242 offset:36864\n"
    "v_mfma_f32_16x16x32_bf16 v[160:163], a[160:163], a[28:31], 0\n"
    "v_mfma_f32_16x16x32_bf16 v[160:163], a[164:167], a[0:3], v[160:163]\n"
    "v_mfma_f32_16x16x32_bf16 v[168:171], a[164:167], a[32:35], 0\n"
    "v_mfma_f32_16x16x32_bf16 v[168:171], a[160:163], a[60:63], v[168:171]\n"
    "v_mfma_f32_16x16x32_bf16 v[176:179], a[160:163], a[92:95], 0\n"
    "v_mfma_f32_16x16x32_bf16 v[176:179], a[164:167], a[64:67], v[176:179]\n"
    "s_waitcnt lgkmcnt(0)\n"
    "v_mfma_f32_16x16x32_bf16 v[184:187], a[164:167], a[96:99], 0\n"
    "s_barrier\n"
    "v_mfma_f32_16x16x32_bf16 v[184:187], a[160:163], a[124:127], v[184:187]\n"
    "s_add_u32 m0, s43, 0\n"
    "v_mfma_f32_16x16x32_bf16 v[192:195], a[160:163], a[156:159], 0\n"
    "ds_read_b128 a[160:163], v243 offset:38912\n"
    "v_mfma_f32_16x16x32_bf16 v[192:195], a[164:167], a[128:131], v[192:195]\n"
    "ds_read_b128 a[164:167], v240 offset:34816\n"
    "v_mfma_f32_16x16x32_bf16 v[192:195], a[168:171], a[132:135], v[192:195]\n"
    "buffer_load_dwordx4 v248, s[8:11], s40 offen offset:0 sc1 lds\n"
    "v_mfma_f32_16x16x32_bf16 v[192:195], a[172:175], a[136:139], v[192:195]\n"
    "ds_read2st64_b32 v[204:205], v250 offset0:0 offset1:1\n"
    "v_mfma_f32_16x16x32_bf16 v[184:187], a[172:175], a[104:107], v[184:187]\n"
    "s_waitcnt vmcnt(11)\n"
    "v_smfmac_f32_16x16x64_bf16 v[156:159], a[192:195], a[200:207], v152\n"
    "v_mfma_f32_16x16x32_bf16 v[184:187], a[168:171], a[100:103], v[184:187]\n"
    "ds_read2st64_b32 v[206:207], v250 offset0:2 offset1:3\n"
    "v_mfma_f32_16x16x32_bf16 v[176:179], a[168:171], a[68:71], v[176:179]\n"
    "buffer_load_dwordx4 v248, s[8:11], s40 offen offset:1024 sc1 lds\n"
    "v_mfma_f32_16x16x32_bf16 v[176:179], a[172:175], a[72:75], v[176:179]\n"
    "v_mfma_f32_16x16x32_bf16 v[168:171], a[172:175], a[40:43], v[168:171]\n"
    "s_waitcnt vmcnt(10)\n"
    "v_smfmac_f32_16x16x64_bf16 v[156:159], a[192:195], a[208:215], v152\n"
    "ds_read2st64_b32 v[212:213], v250 offset0:16 offset1:17\n"
    "v_mfma_f32_16x16x32_bf16 v[168:171], a[168:171], a[36:39], v[168:171]\n"
    "v_mfma_f32_16x16x32_bf16 v[160:163], a[168:171], a[4:7], v[160:163]\n"
    "ds_read_b128 a[168:171], v241 offset:34816\n"
    "buffer_load_dwordx4 v248, s[8:11], s40 offen offset:2048 sc1 lds\n"
    "v_mfma_f32_16x16x32_bf16 v[160:163], a[172:175], a[8:11], v[160:163]\n"
    "ds_read_b128 a[172:175], v240 offset:38912\n"
    "v_mfma_f32_16x16x32_bf16 v[160:163], a[176:179], a[12:15], v[160:163]\n"
    "s_waitcnt vmcnt(9)\n"
    "ds_read2st64_b32 v[214:215], v250 offset0:18 offset1:19\n"
    "v_smfmac_f32_16x16x64_bf16 v[156:159], a[192:195], a[216:223], v152\n"
    "v_mfma_f32_16x16x32_bf16 v[160:163], a[180:183], a[16:19], v[160:163]\n"
    "v_mfma_f32_16x16x32_bf16 v[168:171], a[180:183], a[48:51], v[168:171]\n"
    "buffer_load_dwordx4 v248, s[8:11], s40 offen offset:3072 sc1 lds\n"
    "v_mfma_f32_16x16x32_bf16 v[168:171], a[176:179], a[44:47], v[168:171]\n"
    "s_add_u32 m0, s43, 4096\n"
    "v_mfma_f32_16x16x32_bf16 v[176:179], a[176:179], a[76:79], v[176:179]\n"
    "s_add_u32 s40, s40, 0x8000\n"
    "ds_read2st64_b32 v[220:221], v250 offset0:32 offset1:33\n"
    "v_mfma_f32_16x16x32_bf16 v[176:179], a[180:183], a[80:83], v[176:179]\n"
    "v_mfma_f32_16x16x32_bf16 v[184:187], a[180:183], a[112:115], v[184:187]\n"
    "v_mfma_f32_16x16x32_bf16 v[184:187], a[176:179], a[108:111], v[184:187]\n"
    "buffer_load_dwordx4 v248, s[8:11], s41 offen offset:0 sc1 lds\n"
    "v_mfma_f32_16x16x32_bf16 v[192:195], a[176:179], a[140:143], v[192:195]\n"
    "ds_read_b128 a[176:179], v241 offset:38912\n"
    "v_mfma_f32_16x16x32_bf16 v[192:195], a[180:183], a[144:147], v[192:195]\n"
    "ds_read_b128 a[180:183], v242 offset:34816\n"
    "v_mfma_f32_16x16x32_bf16 v[192:195], a[184:187], a[148:151], v[192:195]\n"
    "ds_read2st64_b32 v[222:223], v250 offset0:34 offset1:35\n"
    "v_mfma_f32_16x16x32_bf16 v[192:195], a[188:191], a[152:155], v[192:195]\n"
    "v_mfma_f32_16x16x32_bf16 v[184:187], a[188:191], a[120:123], v[184:187]\n"
    "buffer_load_dwordx4 v248, s[8:11], s41 offen offset:1024 sc1 lds\n"
    "v_mfma_f32_16x16x32_bf16 v[184:187], a[184:187], a[116:119], v[184:187]\n"
    "ds_read2st64_b32 v[228:229], v250 offset0:48 offset1:49\n"
    "v_mfma_f32_16x16x32_bf16 v[176:179], a[184:187], a[84:87], v[176:179]\n"
    "s_waitcnt vmcnt(10)\n"
    "v_exp_f32_e32 v192, v192\n"
    "v_smfmac_f32_16x16x64_bf16 v[156:159], a[192:195], a[224:231], v152\n"
    "v_exp_f32_e32 v193, v193\n"
    "v_mfma_f32_16x16x32_bf16 v[176:179], a[188:191], a[88:91], v[176:179]\n"
    "v_exp_f32_e32 v194, v194\n"
    "v_mfma_f32_16x16x32_bf16 v[168:171], a[188:191], a[56:59], v[168:171]\n"
    "buffer_load_dwordx4 v248, s[8:11], s41 offen offset:2048 sc1 lds\n"
    "v_mfma_f32_16x16x32_bf16 v[168:171], a[184:187], a[52:55], v[168:171]\n"
    "ds_read2st64_b32 v[230:231], v250 offset0:50 offset1:51\n"
    "v_mfma_f32_16x16x32_bf16 v[160:163], a[184:187], a[20:23], v[160:163]\n"
    "s_waitcnt vmcnt(9)\n"
    "ds_read_b128 a[184:187], v243 offset:34816\n"
    "v_smfmac_f32_16x16x64_bf16 v[156:159], a[192:195], a[232:239], v152\n"
    "ds_read2st64_b32 v[236:237], v250 offset0:64 offset1:65\n"
    "v_mfma_f32_16x16x32_bf16 v[160:163], a[188:191], a[24:27], v[160:163]\n"
    "s_waitcnt lgkmcnt(9)\n"
    "ds_read_b128 a[188:191], v242 offset:38912\n"
    "v_mfma_f32_16x16x32_bf16 v[164:167], a[160:163], a[28:31], 0\n"
    "buffer_load_dwordx4 v248, s[8:11], s41 offen offset:3072 sc1 lds\n"
    "v_mfma_f32_16x16x32_bf16 v[164:167], a[164:167], a[0:3], v[164:167]\n"
    "s_add_u32 s41, s41, 0x8000\n"
    "v_exp_f32_e32 v195, v195\n"
    "v_mfma_f32_16x16x32_bf16 v[172:175], a[164:167], a[32:35], 0\n"
    "s_waitcnt vmcnt(8)\n"
    "v_cvt_pk_bf16_f32 v192, v192, v193\n"
    "v_smfmac_f32_16x16x64_bf16 v[156:159], a[192:195], a[240:247], v152\n"
    "v_exp_f32_e32 v184, v184\n"
    "v_mfma_f32_16x16x32_bf16 v[172:175], a[160:163], a[60:63], v[172:175]\n"
    "ds_read2st64_b32 v[238:239], v250 offset0:66 offset1:67\n"
    "v_mfma_f32_16x16x32_bf16 v[180:183], a[160:163], a[92:95], 0\n"
    "v_exp_f32_e32 v185, v185\n"
    "v_mfma_f32_16x16x32_bf16 v[180:183], a[164:167], a[64:67], v[180:183]\n"
    "v_cvt_pk_bf16_f32 v193, v194, v195\n"
    "v_mfma_f32_16x16x32_bf16 v[188:191], a[164:167], a[96:99], 0\n"
    "v_exp_f32_e32 v186, v186\n"
    "v_mfma_f32_16x16x32_bf16 v[188:191], a[160:163], a[124:127], v[188:191]\n"
    "v_exp_f32_e32 v187, v187\n"
    "v_mfma_f32_16x16x32_bf16 v[196:199], a[160:163], a[156:159], 0\n"
    "ds_read_b64_tr_b16 a[160:161], v244 offset:16384\n"
    "v_and_b32_e32 v253, 0xffff0000, v255\n"
    "v_mfma_f32_16x16x32_bf16 v[196:199], a[164:167], a[128:131], v[196:199]\n"
    "ds_read_b64_tr_b16 a[162:163], v244 offset:18432\n"
    "v_mfma_f32_16x16x32_bf16 v[196:199], a[168:171], a[132:135], v[196:199]\n"
    "ds_read_b64_tr_b16 a[164:165], v244 offset:24576\n"
    "v_mfma_f32_16x16x32_bf16 v[196:199], a[172:175], a[136:139], v[196:199]\n"
    "ds_read_b64_tr_b16 a[166:167], v244 offset:26624\n"
    "v_mfma_f32_16x16x32_bf16 v[188:191], a[172:175], a[104:107], v[188:191]\n"
    "v_cvt_pk_bf16_f32 v184, v184, v185\n"
    "v_mfma_f32_16x16x32_bf16 v[188:191], a[168:171], a[100:103], v[188:191]\n"
    "v_exp_f32_e32 v176, v176\n"
    "v_mfma_f32_16x16x32_bf16 v[180:183], a[168:171], a[68:71], v[180:183]\n"
    "v_exp_f32_e32 v177, v177\n"
    "v_mfma_f32_16x16x32_bf16 v[180:183], a[172:175], a[72:75], v[180:183]\n"
    "v_cvt_pk_bf16_f32 v185, v186, v187\n"
    "v_mul_f32_e32 v156, v253, v156\n"
    "v_mfma_f32_16x16x32_bf16 v[172:175], a[172:175], a[40:43], v[172:175]\n"
    "v_exp_f32_e32 v178, v178\n"
    "v_mfma_f32_16x16x32_bf16 v[172:175], a[168:171], a[36:39], v[172:175]\n"
    "v_exp_f32_e32 v179, v179\n"
    "v_mfma_f32_16x16x32_bf16 v[164:167], a[168:171], a[4:7], v[164:167]\n"
    "ds_read_b64_tr_b16 a[168:169], v245 offset:16384\n"
    "v_mfma_f32_16x16x32_bf16 v[164:167], a[172:175], a[8:11], v[164:167]\n"
    "s_waitcnt lgkmcnt(6)\n"
    "ds_read_b64_tr_b16 a[170:171], v245 offset:18432\n"
    "v_mfma_f32_16x16x32_bf16 v[164:167], a[176:179], a[12:15], v[164:167]\n"
    "ds_read_b64_tr_b16 a[172:173], v245 offset:24576\n"
    "v_mfma_f32_16x16x32_bf16 v[164:167], a[180:183], a[16:19], v[164:167]\n"
    "ds_read_b64_tr_b16 a[174:175], v245 offset:26624\n"
    "v_mfma_f32_16x16x32_bf16 v[172:175], a[180:183], a[48:51], v[172:175]\n"
    "v_cvt_pk_bf16_f32 v176, v176, v177\n"
    "v_mfma_f32_16x16x32_bf16 v[172:175], a[176:179], a[44:47], v[172:175]\n"
    "v_exp_f32_e32 v168, v168\n"
    "v_mfma_f32_16x16x32_bf16 v[180:183], a[176:179], a[76:79], v[180:183]\n"
    "v_mul_f32_e32 v157, v253, v157\n"
    "v_mfma_f32_16x16x32_bf16 v[180:183], a[180:183], a[80:83], v[180:183]\n"
    "v_exp_f32_e32 v169, v169\n"
    "v_mfma_f32_16x16x32_bf16 v[188:191], a[180:183], a[112:115], v[188:191]\n"
    "v_cvt_pk_bf16_f32 v177, v178, v179\n"
    "v_mfma_f32_16x16x32_bf16 v[188:191], a[176:179], a[108:111], v[188:191]\n"
    "v_exp_f32_e32 v170, v170\n"
    "v_mfma_f32_16x16x32_bf16 v[196:199], a[176:179], a[140:143], v[196:199]\n"
    "ds_read_b64_tr_b16 a[176:177], v246 offset:16384\n"
    "v_mul_f32_e32 v158, v253, v158\n"
    "v_mfma_f32_16x16x32_bf16 v[196:199], a[180:183], a[144:147], v[196:199]\n"
    "ds_read_b64_tr_b16 a[178:179], v246 offset:18432\n"
    "v_mfma_f32_16x16x32_bf16 v[196:199], a[184:187], a[148:151], v[196:199]\n"
    "ds_read_b64_tr_b16 a[180:181], v246 offset:24576\n"
    "v_mfma_f32_16x16x32_bf16 v[196:199], a[188:191], a[152:155], v[196:199]\n"
    "ds_read_b64_tr_b16 a[182:183], v246 offset:26624\n"
    "v_mfma_f32_16x16x32_bf16 v[188:191], a[188:191], a[120:123], v[188:191]\n"
    "v_exp_f32_e32 v171, v171\n"
    "v_mfma_f32_16x16x32_bf16 v[188:191], a[184:187], a[116:119], v[188:191]\n"
    "v_cvt_pk_bf16_f32 v168, v168, v169\n"
    "v_mul_f32_e32 v159, v253, v159\n"
    "v_mfma_f32_16x16x32_bf16 v[180:183], a[184:187], a[84:87], v[180:183]\n"
    "v_exp_f32_e32 v160, v160\n"
    "v_mfma_f32_16x16x32_bf16 v[180:183], a[188:191], a[88:91], v[180:183]\n"
    "v_exp_f32_e32 v161, v161\n"
    "v_mfma_f32_16x16x32_bf16 v[172:175], a[188:191], a[56:59], v[172:175]\n"
    "v_cvt_pk_bf16_f32 v169, v170, v171\n"
    "v_mfma_f32_16x16x32_bf16 v[172:175], a[184:187], a[52:55], v[172:175]\n"
    "v_exp_f32_e32 v162, v162\n"
    "v_mfma_f32_16x16x32_bf16 v[164:167], a[184:187], a[20:23], v[164:167]\n"
    "ds_read_b64_tr_b16 a[184:185], v247 offset:16384\n"
    "v_mfma_f32_16x16x32_bf16 v[164:167], a[188:191], a[24:27], v[164:167]\n"
    "s_waitcnt lgkmcnt(5)\n"
    "ds_read_b64_tr_b16 a[186:187], v247 offset:18432\n"
    "v_mfma_f32_16x16x32_bf16 v[0:3], a[160:163], v[200:203], v[0:3]\n"
    "ds_read_b64_tr_b16 a[188:189], v247 offset:24576\n"
    "v_mad_u32_u16 v253, v255, s42, v153 op_sel:[0,0,0,0]\n"
    "v_mfma_f32_16x16x32_bf16 v[0:3], a[164:167], v[204:207], v[0:3]\n"
    "ds_read_b64_tr_b16 a[190:191], v247 offset:26624\n"
    "v_mfma_f32_16x16x32_bf16 a[248:251], a[164:167], v[212:215], a[248:251]\n"
    "v_exp_f32_e32 v163, v163\n"
    "v_mfma_f32_16x16x32_bf16 a[248:251], a[160:163], v[208:211], a[248:251]\n"
    "v_cvt_pk_bf16_f32 v160, v160, v161\n"
    "v_mfma_f32_16x16x32_bf16 v[56:59], a[160:163], v[216:219], v[56:59]\n"
    "v_cvt_pk_bf16_f32 v154, v156, v157\n"
    "v_mfma_f32_16x16x32_bf16 v[56:59], a[164:167], v[220:223], v[56:59]\n"
    "v_cvt_pk_bf16_f32 v161, v162, v163\n"
    "v_mfma_f32_16x16x32_bf16 v[60:63], a[164:167], v[228:231], v[60:63]\n"
    "v_exp_f32_e32 v196, v196\n"
    "v_mfma_f32_16x16x32_bf16 v[60:63], a[160:163], v[224:227], v[60:63]\n"
    "v_exp_f32_e32 v197, v197\n"
    "v_mfma_f32_16x16x32_bf16 v[64:67], a[160:163], v[232:235], v[64:67]\n"
    "ds_read_b64_tr_b16 a[160:161], v244 offset:20480\n"
    "v_mfma_f32_16x16x32_bf16 v[64:67], a[164:167], v[236:239], v[64:67]\n"
    "ds_read_b64_tr_b16 a[162:163], v244 offset:22528\n"
    "v_mfma_f32_16x16x32_bf16 v[76:79], a[168:171], v[232:235], v[76:79]\n"
    "ds_read_b64_tr_b16 a[164:165], v244 offset:28672\n"
    "v_mfma_f32_16x16x32_bf16 v[76:79], a[172:175], v[236:239], v[76:79]\n"
    "ds_read_b64_tr_b16 a[166:167], v244 offset:30720\n"
    "v_mfma_f32_16x16x32_bf16 v[72:75], a[172:175], v[228:231], v[72:75]\n"
    "v_exp_f32_e32 v198, v198\n"
    "v_mfma_f32_16x16x32_bf16 v[72:75], a[168:171], v[224:227], v[72:75]\n"
    "v_exp_f32_e32 v199, v199\n"
    "v_mfma_f32_16x16x32_bf16 v[68:71], a[168:171], v[216:219], v[68:71]\n"
    "v_cvt_pk_bf16_f32 v194, v196, v197\n"
    "v_cvt_pk_bf16_f32 v155, v158, v159\n"
    "v_mfma_f32_16x16x32_bf16 v[68:71], a[172:175], v[220:223], v[68:71]\n"
    "v_exp_f32_e32 v188, v188\n"
    "v_mfma_f32_16x16x32_bf16 a[252:255], a[172:175], v[212:215], a[252:255]\n"
    "v_exp_f32_e32 v189, v189\n"
    "v_mfma_f32_16x16x32_bf16 a[252:255], a[168:171], v[208:211], a[252:255]\n"
    "v_cvt_pk_bf16_f32 v195, v198, v199\n"
    "v_mfma_f32_16x16x32_bf16 v[4:7], a[168:171], v[200:203], v[4:7]\n"
    "ds_read_b64_tr_b16 a[168:169], v245 offset:20480\n"
    "v_mfma_f32_16x16x32_bf16 v[4:7], a[172:175], v[204:207], v[4:7]\n"
    "s_waitcnt lgkmcnt(5)\n"
    "ds_read_b64_tr_b16 a[170:171], v245 offset:22528\n"
    "v_mfma_f32_16x16x32_bf16 v[8:11], a[176:179], v[200:203], v[8:11]\n"
    "ds_read_b64_tr_b16 a[172:173], v245 offset:28672\n"
    "v_mov_b32_e32 v156, 0\n"
    "v_mfma_f32_16x16x32_bf16 v[8:11], a[180:183], v[204:207], v[8:11]\n"
    "ds_read_b64_tr_b16 a[174:175], v245 offset:30720\n"
    "v_mfma_f32_16x16x32_bf16 v[32:35], a[180:183], v[212:215], v[32:35]\n"
    "v_exp_f32_e32 v190, v190\n"
    "v_mfma_f32_16x16x32_bf16 v[32:35], a[176:179], v[208:211], v[32:35]\n"
    "v_exp_f32_e32 v191, v191\n"
    "v_mfma_f32_16x16x32_bf16 v[80:83], a[176:179], v[216:219], v[80:83]\n"
    "v_mov_b32_e32 v157, 0\n"
    "v_mfma_f32_16x16x32_bf16 v[80:83], a[180:183], v[220:223], v[80:83]\n"
    "v_cvt_pk_bf16_f32 v186, v188, v189\n"
    "v_mfma_f32_16x16x32_bf16 v[84:87], a[180:183], v[228:231], v[84:87]\n"
    "v_exp_f32_e32 v180, v180\n"
    "v_mfma_f32_16x16x32_bf16 v[84:87], a[176:179], v[224:227], v[84:87]\n"
    "v_exp_f32_e32 v181, v181\n"
    "v_mfma_f32_16x16x32_bf16 v[88:91], a[176:179], v[232:235], v[88:91]\n"
    "ds_read_b64_tr_b16 a[176:177], v246 offset:20480\n"
    "v_mfma_f32_16x16x32_bf16 v[88:91], a[180:183], v[236:239], v[88:91]\n"
    "ds_read_b64_tr_b16 a[178:179], v246 offset:22528\n"
    "v_mfma_f32_16x16x32_bf16 v[100:103], a[184:187], v[232:235], v[100:103]\n"
    "ds_read_b64_tr_b16 a[180:181], v246 offset:28672\n"
    "v_mfma_f32_16x16x32_bf16 v[100:103], a[188:191], v[236:239], v[100:103]\n"
    "ds_read_b64_tr_b16 a[182:183], v246 offset:30720\n"
    "v_mfma_f32_16x16x32_bf16 v[96:99], a[188:191], v[228:231], v[96:99]\n"
    "v_cvt_pk_bf16_f32 v187, v190, v191\n"
    "v_mov_b32_e32 v158, 0\n"
    "v_mfma_f32_16x16x32_bf16 v[96:99], a[184:187], v[224:227], v[96:99]\n"
    "v_exp_f32_e32 v182, v182\n"
    "v_mfma_f32_16x16x32_bf16 v[92:95], a[184:187], v[216:219], v[92:95]\n"
    "v_exp_f32_e32 v183, v183\n"
    "v_mfma_f32_16x16x32_bf16 v[92:95], a[188:191], v[220:223], v[92:95]\n"
    "v_cvt_pk_bf16_f32 v178, v180, v181\n"
    "v_mfma_f32_16x16x32_bf16 v[36:39], a[188:191], v[212:215], v[36:39]\n"
    "v_exp_f32_e32 v172, v172\n"
    "v_mfma_f32_16x16x32_bf16 v[36:39], a[184:187], v[208:211], v[36:39]\n"
    "v_exp_f32_e32 v173, v173\n"
    "v_mfma_f32_16x16x32_bf16 v[12:15], a[184:187], v[200:203], v[12:15]\n"
    "s_mov_b32 m0, s49\n"
    "ds_read_b64_tr_b16 a[184:185], v247 offset:20480\n"
    "v_mfma_f32_16x16x32_bf16 v[12:15], a[188:191], v[204:207], v[12:15]\n"
    "s_waitcnt lgkmcnt(5)\n"
    "ds_read_b64_tr_b16 a[186:187], v247 offset:22528\n"
    "v_mov_b32_e32 v159, 0\n"
    "v_mfma_f32_16x16x32_bf16 v[16:19], a[160:163], v[200:203], v[16:19]\n"
    "ds_read_b64_tr_b16 a[188:189], v247 offset:28672\n"
    "v_mfma_f32_16x16x32_bf16 v[16:19], a[164:167], v[204:207], v[16:19]\n"
    "ds_read_b64_tr_b16 a[190:191], v247 offset:30720\n"
    "v_mfma_f32_16x16x32_bf16 v[40:43], a[164:167], v[212:215], v[40:43]\n"
    "buffer_store_dwordx2 v[154:155], v253, s[24:27], 0 offen offset:0 sc1\n"
    "v_cvt_pk_bf16_f32 v179, v182, v183\n"
    "v_mfma_f32_16x16x32_bf16 v[40:43], a[160:163], v[208:211], v[40:43]\n"
    "v_exp_f32_e32 v174, v174\n"
    "v_mfma_f32_16x16x32_bf16 v[104:107], a[160:163], v[216:219], v[104:107]\n"
    "v_exp_f32_e32 v175, v175\n"
    "v_mfma_f32_16x16x32_bf16 v[104:107], a[164:167], v[220:223], v[104:107]\n"
    "s_waitcnt vmcnt(9)\n"
    "v_mfma_f32_16x16x32_bf16 v[108:111], a[164:167], v[228:231], v[108:111]\n"
    "s_barrier\n"
    "v_mfma_f32_16x16x32_bf16 v[108:111], a[160:163], v[224:227], v[108:111]\n"
    "ds_write_addtid_b32 v192 offset:16384\n"
    "v_cvt_pk_bf16_f32 v170, v172, v173\n"
    "v_mfma_f32_16x16x32_bf16 v[112:115], a[160:163], v[232:235], v[112:115]\n"
    "ds_read_b128 a[160:163], v243 offset:53248\n"
    "v_mfma_f32_16x16x32_bf16 v[112:115], a[164:167], v[236:239], v[112:115]\n"
    "ds_read_b128 a[164:167], v240 offset:49152\n"
    "v_mfma_f32_16x16x32_bf16 v[124:127], a[168:171], v[232:235], v[124:127]\n"
    "v_exp_f32_e32 v164, v164\n"
    "v_mfma_f32_16x16x32_bf16 v[124:127], a[172:175], v[236:239], v[124:127]\n"
    "v_exp_f32_e32 v165, v165\n"
    "v_mfma_f32_16x16x32_bf16 v[120:123], a[172:175], v[228:231], v[120:123]\n"
    "ds_write_addtid_b32 v193 offset:16640\n"
    "v_cvt_pk_bf16_f32 v171, v174, v175\n"
    "v_mfma_f32_16x16x32_bf16 v[120:123], a[168:171], v[224:227], v[120:123]\n"
    "ds_write_addtid_b32 v184 offset:12288\n"
    "v_mfma_f32_16x16x32_bf16 v[116:119], a[168:171], v[216:219], v[116:119]\n"
    "v_exp_f32_e32 v166, v166\n"
    "v_mfma_f32_16x16x32_bf16 v[116:119], a[172:175], v[220:223], v[116:119]\n"
    "v_exp_f32_e32 v167, v167\n"
    "v_mfma_f32_16x16x32_bf16 v[44:47], a[172:175], v[212:215], v[44:47]\n"
    "ds_write_addtid_b32 v185 offset:12544\n"
    "v_cvt_pk_bf16_f32 v162, v164, v165\n"
    "v_mfma_f32_16x16x32_bf16 v[44:47], a[168:171], v[208:211], v[44:47]\n"
    "v_cvt_pk_bf16_f32 v163, v166, v167\n"
    "v_mfma_f32_16x16x32_bf16 v[20:23], a[168:171], v[200:203], v[20:23]\n"
    "ds_read_b128 a[168:171], v241 offset:49152\n"
    "v_mfma_f32_16x16x32_bf16 v[20:23], a[172:175], v[204:207], v[20:23]\n"
    "s_waitcnt lgkmcnt(7)\n"
    "ds_read_b128 a[172:175], v240 offset:53248\n"
    "v_mfma_f32_16x16x32_bf16 v[24:27], a[176:179], v[200:203], v[24:27]\n"
    "ds_write_addtid_b32 v176 offset:8192\n"
    "v_mfma_f32_16x16x32_bf16 v[24:27], a[180:183], v[204:207], v[24:27]\n"
    "ds_write_addtid_b32 v177 offset:8448\n"
    "v_mfma_f32_16x16x32_bf16 v[48:51], a[180:183], v[212:215], v[48:51]\n"
    "ds_write_addtid_b32 v168 offset:4096\n"
    "v_mfma_f32_16x16x32_bf16 v[48:51], a[176:179], v[208:211], v[48:51]\n"
    "ds_write_addtid_b32 v169 offset:4352\n"
    "v_mfma_f32_16x16x32_bf16 v[128:131], a[176:179], v[216:219], v[128:131]\n"
    "ds_write_addtid_b32 v160 offset:0\n"
    "v_mfma_f32_16x16x32_bf16 v[128:131], a[180:183], v[220:223], v[128:131]\n"
    "ds_write_addtid_b32 v161 offset:256\n"
    "v_mfma_f32_16x16x32_bf16 v[132:135], a[180:183], v[228:231], v[132:135]\n"
    "ds_write_addtid_b32 v194 offset:16896\n"
    "v_mfma_f32_16x16x32_bf16 v[132:135], a[176:179], v[224:227], v[132:135]\n"
    "ds_write_addtid_b32 v195 offset:17152\n"
    "v_mfma_f32_16x16x32_bf16 v[136:139], a[176:179], v[232:235], v[136:139]\n"
    "ds_read_b128 a[176:179], v241 offset:53248\n"
    "v_mfma_f32_16x16x32_bf16 v[136:139], a[180:183], v[236:239], v[136:139]\n"
    "ds_read_b128 a[180:183], v242 offset:49152\n"
    "v_mfma_f32_16x16x32_bf16 v[148:151], a[184:187], v[232:235], v[148:151]\n"
    "ds_write_addtid_b32 v186 offset:12800\n"
    "v_mfma_f32_16x16x32_bf16 v[148:151], a[188:191], v[236:239], v[148:151]\n"
    "ds_write_addtid_b32 v187 offset:13056\n"
    "v_mfma_f32_16x16x32_bf16 v[144:147], a[188:191], v[228:231], v[144:147]\n"
    "ds_write_addtid_b32 v178 offset:8704\n"
    "v_mfma_f32_16x16x32_bf16 v[144:147], a[184:187], v[224:227], v[144:147]\n"
    "ds_write_addtid_b32 v179 offset:8960\n"
    "v_mfma_f32_16x16x32_bf16 v[140:143], a[184:187], v[216:219], v[140:143]\n"
    "ds_write_addtid_b32 v170 offset:4608\n"
    "v_mfma_f32_16x16x32_bf16 v[140:143], a[188:191], v[220:223], v[140:143]\n"
    "ds_write_addtid_b32 v171 offset:4864\n"
    "v_mfma_f32_16x16x32_bf16 v[52:55], a[188:191], v[212:215], v[52:55]\n"
    "ds_write_addtid_b32 v162 offset:512\n"
    "v_mfma_f32_16x16x32_bf16 v[52:55], a[184:187], v[208:211], v[52:55]\n"
    "ds_write_addtid_b32 v163 offset:768\n"
    "v_mfma_f32_16x16x32_bf16 v[28:31], a[184:187], v[200:203], v[28:31]\n"
    "ds_read_b128 a[184:187], v243 offset:49152\n"
    "v_mfma_f32_16x16x32_bf16 v[28:31], a[188:191], v[204:207], v[28:31]\n"
    "s_waitcnt lgkmcnt(15)\n"
    "ds_read_b128 a[188:191], v242 offset:53248\n"
    "v_mfma_f32_16x16x32_bf16 v[200:203], a[160:163], a[28:31], 0\n"
    "v_mfma_f32_16x16x32_bf16 v[200:203], a[164:167], a[0:3], v[200:203]\n"
    "v_mfma_f32_16x16x32_bf16 v[208:211], a[164:167], a[32:35], 0\n"
    "v_mfma_f32_16x16x32_bf16 v[208:211], a[160:163], a[60:63], v[208:211]\n"
    "v_mfma_f32_16x16x32_bf16 v[216:219], a[160:163], a[92:95], 0\n"
    "v_mfma_f32_16x16x32_bf16 v[216:219], a[164:167], a[64:67], v[216:219]\n"
    "s_waitcnt lgkmcnt(0)\n"
    "v_mfma_f32_16x16x32_bf16 v[224:227], a[164:167], a[96:99], 0\n"
    "s_barrier\n"
    "v_mfma_f32_16x16x32_bf16 v[224:227], a[160:163], a[124:127], v[224:227]\n"
    "s_add_u32 m0, s43, 16384\n"
    "ds_read2st64_b32 v[164:165], v250 offset0:0 offset1:1\n"
    "v_mfma_f32_16x16x32_bf16 v[232:235], a[160:163], a[156:159], 0\n"
    "ds_read_b128 a[160:163], v243 offset:55296\n"
    "v_mfma_f32_16x16x32_bf16 v[232:235], a[164:167], a[128:131], v[232:235]\n"
    "ds_read_b128 a[164:167], v240 offset:51200\n"
    "v_mfma_f32_16x16x32_bf16 v[232:235], a[168:171], a[132:135], v[232:235]\n"
    "buffer_load_dwordx4 v248, s[8:11], s40 offen offset:0 sc1 lds\n"
    "v_mfma_f32_16x16x32_bf16 v[232:235], a[172:175], a[136:139], v[232:235]\n"
    "v_mfma_f32_16x16x32_bf16 v[224:227], a[172:175], a[104:107], v[224:227]\n"
    "v_mfma_f32_16x16x32_bf16 v[224:227], a[168:171], a[100:103], v[224:227]\n"
    "ds_read2st64_b32 v[166:167], v250 offset0:2 offset1:3\n"
    "v_mfma_f32_16x16x32_bf16 v[216:219], a[168:171], a[68:71], v[216:219]\n"
    "buffer_load_dwordx4 v248, s[8:11], s40 offen offset:1024 sc1 lds\n"
    "v_mfma_f32_16x16x32_bf16 v[216:219], a[172:175], a[72:75], v[216:219]\n"
    "v_mfma_f32_16x16x32_bf16 v[208:211], a[172:175], a[40:43], v[208:211]\n"
    "v_mfma_f32_16x16x32_bf16 v[208:211], a[168:171], a[36:39], v[208:211]\n"
    "ds_read2st64_b32 v[172:173], v250 offset0:16 offset1:17\n"
    "v_mfma_f32_16x16x32_bf16 v[200:203], a[168:171], a[4:7], v[200:203]\n"
    "ds_read_b128 a[168:171], v241 offset:51200\n"
    "buffer_load_dwordx4 v248, s[8:11], s40 offen offset:2048 sc1 lds\n"
    "v_mfma_f32_16x16x32_bf16 v[200:203], a[172:175], a[8:11], v[200:203]\n"
    "ds_read_b128 a[172:175], v240 offset:55296\n"
    "v_mfma_f32_16x16x32_bf16 v[200:203], a[176:179], a[12:15], v[200:203]\n"
    "ds_read2st64_b32 v[174:175], v250 offset0:18 offset1:19\n"
    "v_mfma_f32_16x16x32_bf16 v[200:203], a[180:183], a[16:19], v[200:203]\n"
    "v_mfma_f32_16x16x32_bf16 v[208:211], a[180:183], a[48:51], v[208:211]\n"
    "buffer_load_dwordx4 v248, s[8:11], s40 offen offset:3072 sc1 lds\n"
    "v_mfma_f32_16x16x32_bf16 v[208:211], a[176:179], a[44:47], v[208:211]\n"
    "s_add_u32 m0, s43, 20480\n"
    "v_mfma_f32_16x16x32_bf16 v[216:219], a[176:179], a[76:79], v[216:219]\n"
    "s_add_u32 s40, s40, 0x8000\n"
    "ds_read2st64_b32 v[180:181], v250 offset0:32 offset1:33\n"
    "v_mfma_f32_16x16x32_bf16 v[216:219], a[180:183], a[80:83], v[216:219]\n"
    "v_mfma_f32_16x16x32_bf16 v[224:227], a[180:183], a[112:115], v[224:227]\n"
    "buffer_load_dwordx4 v248, s[8:11], s41 offen offset:0 sc1 lds\n"
    "v_mfma_f32_16x16x32_bf16 v[224:227], a[176:179], a[108:111], v[224:227]\n"
    "ds_read2st64_b32 v[182:183], v250 offset0:34 offset1:35\n"
    "v_mfma_f32_16x16x32_bf16 v[232:235], a[176:179], a[140:143], v[232:235]\n"
    "ds_read_b128 a[176:179], v241 offset:55296\n"
    "v_mfma_f32_16x16x32_bf16 v[232:235], a[180:183], a[144:147], v[232:235]\n"
    "ds_read_b128 a[180:183], v242 offset:51200\n"
    "v_mfma_f32_16x16x32_bf16 v[232:235], a[184:187], a[148:151], v[232:235]\n"
    "buffer_load_dwordx4 v248, s[8:11], s41 offen offset:1024 sc1 lds\n"
    "v_mfma_f32_16x16x32_bf16 v[232:235], a[188:191], a[152:155], v[232:235]\n"
    "v_mfma_f32_16x16x32_bf16 v[224:227], a[188:191], a[120:123], v[224:227]\n"
    "ds_read2st64_b32 v[188:189], v250 offset0:48 offset1:49\n"
    "v_mfma_f32_16x16x32_bf16 v[224:227], a[184:187], a[116:119], v[224:227]\n"
    "v_mfma_f32_16x16x32_bf16 v[216:219], a[184:187], a[84:87], v[216:219]\n"
    "buffer_load_dwordx4 v248, s[8:11], s41 offen offset:2048 sc1 lds\n"
    "v_mfma_f32_16x16x32_bf16 v[216:219], a[188:191], a[88:91], v[216:219]\n"
    "ds_read2st64_b32 v[190:191], v250 offset0:50 offset1:51\n"
    "v_mfma_f32_16x16x32_bf16 v[208:211], a[188:191], a[56:59], v[208:211]\n"
    "ds_read_b64 v[254:255], v252 offset:64\n"
    "v_mfma_f32_16x16x32_bf16 v[208:211], a[184:187], a[52:55], v[208:211]\n"
    "ds_read2st64_b32 v[196:197], v250 offset0:64 offset1:65\n"
    "v_mfma_f32_16x16x32_bf16 v[200:203], a[184:187], a[20:23], v[200:203]\n"
    "ds_read_b128 a[184:187], v243 offset:51200\n"
    "buffer_load_dwordx4 v248, s[8:11], s41 offen offset:3072 sc1 lds\n"
    "v_mfma_f32_16x16x32_bf16 v[200:203], a[188:191], a[24:27], v[200:203]\n"
    "s_waitcnt lgkmcnt(15)\n"
    "s_add_u32 s41, s41, 0x8000\n"
    "ds_read_b128 a[188:191], v242 offset:55296\n"
    "v_mfma_f32_16x16x32_bf16 v[204:207], a[160:163], a[28:31], 0\n"
    "s_waitcnt lgkmcnt(3)\n"
    "v_mad_u32_u16 v253, v254, s42, v251 op_sel:[0,0,0,0]\n"
    "v_mfma_f32_16x16x32_bf16 v[204:207], a[164:167], a[0:3], v[204:207]\n"
    "buffer_load_dwordx4 a[200:203], v253, s[20:23], 0 offen offset:0 sc1\n"
    "v_mfma_f32_16x16x32_bf16 v[212:215], a[164:167], a[32:35], 0\n"
    "v_exp_f32_e32 v232, v232\n"
    "v_mfma_f32_16x16x32_bf16 v[212:215], a[160:163], a[60:63], v[212:215]\n"
    "ds_read2st64_b32 v[198:199], v250 offset0:66 offset1:67\n"
    "v_mfma_f32_16x16x32_bf16 v[220:223], a[160:163], a[92:95], 0\n"
    "v_mad_u32_u16 v253, v254, s42, v251 op_sel:[1,0,0,0]\n"
    "v_mfma_f32_16x16x32_bf16 v[220:223], a[164:167], a[64:67], v[220:223]\n"
    "buffer_load_dwordx4 a[204:207], v253, s[20:23], 0 offen offset:0 sc1\n"
    "v_mfma_f32_16x16x32_bf16 v[228:231], a[164:167], a[96:99], 0\n"
    "v_exp_f32_e32 v233, v233\n"
    "v_mfma_f32_16x16x32_bf16 v[228:231], a[160:163], a[124:127], v[228:231]\n"
    "v_exp_f32_e32 v234, v234\n"
    "v_mfma_f32_16x16x32_bf16 v[236:239], a[160:163], a[156:159], 0\n"
    "ds_read_b64_tr_b16 a[160:161], v244 offset:32768\n"
    "v_mad_u32_u16 v253, v255, s42, v251 op_sel:[0,0,0,0]\n"
    "v_mfma_f32_16x16x32_bf16 v[236:239], a[164:167], a[128:131], v[236:239]\n"
    "ds_read_b64_tr_b16 a[162:163], v244 offset:34816\n"
    "buffer_load_dwordx4 a[208:211], v253, s[20:23], 0 offen offset:0 sc1\n"
    "v_mfma_f32_16x16x32_bf16 v[236:239], a[168:171], a[132:135], v[236:239]\n"
    "ds_read_b64_tr_b16 a[164:165], v244 offset:40960\n"
    "v_mfma_f32_16x16x32_bf16 v[236:239], a[172:175], a[136:139], v[236:239]\n"
    "ds_read_b64_tr_b16 a[166:167], v244 offset:43008\n"
    "v_mfma_f32_16x16x32_bf16 v[228:231], a[172:175], a[104:107], v[228:231]\n"
    "v_mad_u32_u16 v253, v255, s42, v251 op_sel:[1,0,0,0]\n"
    "v_mfma_f32_16x16x32_bf16 v[228:231], a[168:171], a[100:103], v[228:231]\n"
    "buffer_load_dwordx4 a[212:215], v253, s[20:23], 0 offen offset:0 sc1\n"
    "ds_read_b64 v[254:255], v252 offset:72\n"
    "v_mfma_f32_16x16x32_bf16 v[220:223], a[168:171], a[68:71], v[220:223]\n"
    "v_exp_f32_e32 v235, v235\n"
    "v_mfma_f32_16x16x32_bf16 v[220:223], a[172:175], a[72:75], v[220:223]\n"
    "v_cvt_pk_bf16_f32 v232, v232, v233\n"
    "v_mfma_f32_16x16x32_bf16 v[212:215], a[172:175], a[40:43], v[212:215]\n"
    "v_exp_f32_e32 v224, v224\n"
    "v_mfma_f32_16x16x32_bf16 v[212:215], a[168:171], a[36:39], v[212:215]\n"
    "v_exp_f32_e32 v225, v225\n"
    "v_mfma_f32_16x16x32_bf16 v[204:207], a[168:171], a[4:7], v[204:207]\n"
    "ds_read_b64_tr_b16 a[168:169], v245 offset:32768\n"
    "v_cvt_pk_bf16_f32 v233, v234, v235\n"
    "v_mfma_f32_16x16x32_bf16 v[204:207], a[172:175], a[8:11], v[204:207]\n"
    "ds_read_b64_tr_b16 a[170:171], v245 offset:34816\n"
    "v_mfma_f32_16x16x32_bf16 v[204:207], a[176:179], a[12:15], v[204:207]\n"
    "s_waitcnt lgkmcnt(2)\n"
    "ds_read_b64_tr_b16 a[172:173], v245 offset:40960\n"
    "v_mad_u32_u16 v253, v254, s42, v251 op_sel:[0,0,0,0]\n"
    "v_mfma_f32_16x16x32_bf16 v[204:207], a[180:183], a[16:19], v[204:207]\n"
    "ds_read_b64_tr_b16 a[174:175], v245 offset:43008\n"
    "buffer_load_dwordx4 a[216:219], v253, s[20:23], 0 offen offset:0 sc1\n"
    "v_mfma_f32_16x16x32_bf16 v[212:215], a[180:183], a[48:51], v[212:215]\n"
    "v_exp_f32_e32 v226, v226\n"
    "v_mfma_f32_16x16x32_bf16 v[212:215], a[176:179], a[44:47], v[212:215]\n"
    "v_exp_f32_e32 v227, v227\n"
    "v_mfma_f32_16x16x32_bf16 v[220:223], a[176:179], a[76:79], v[220:223]\n"
    "v_mad_u32_u16 v253, v254, s42, v251 op_sel:[1,0,0,0]\n"
    "v_cvt_pk_bf16_f32 v224, v224, v225\n"
    "v_mfma_f32_16x16x32_bf16 v[220:223], a[180:183], a[80:83], v[220:223]\n"
    "buffer_load_dwordx4 a[220:223], v253, s[20:23], 0 offen offset:0 sc1\n"
    "v_mfma_f32_16x16x32_bf16 v[228:231], a[180:183], a[112:115], v[228:231]\n"
    "v_exp_f32_e32 v216, v216\n"
    "v_mfma_f32_16x16x32_bf16 v[228:231], a[176:179], a[108:111], v[228:231]\n"
    "v_exp_f32_e32 v217, v217\n"
    "v_mfma_f32_16x16x32_bf16 v[236:239], a[176:179], a[140:143], v[236:239]\n"
    "ds_read_b64_tr_b16 a[176:177], v246 offset:32768\n"
    "v_cvt_pk_bf16_f32 v225, v226, v227\n"
    "v_mfma_f32_16x16x32_bf16 v[236:239], a[180:183], a[144:147], v[236:239]\n"
    "ds_read_b64_tr_b16 a[178:179], v246 offset:34816\n"
    "v_mfma_f32_16x16x32_bf16 v[236:239], a[184:187], a[148:151], v[236:239]\n"
    "ds_read_b64_tr_b16 a[180:181], v246 offset:40960\n"
    "v_mfma_f32_16x16x32_bf16 v[236:239], a[188:191], a[152:155], v[236:239]\n"
    "ds_read_b64_tr_b16 a[182:183], v246 offset:43008\n"
    "v_mfma_f32_16x16x32_bf16 v[228:231], a[188:191], a[120:123], v[228:231]\n"
    "ds_read_b64 v[254:255], v252 offset:96\n"
    "v_mfma_f32_16x16x32_bf16 v[228:231], a[184:187], a[116:119], v[228:231]\n"
    "v_exp_f32_e32 v218, v218\n"
    "v_mfma_f32_16x16x32_bf16 v[220:223], a[184:187], a[84:87], v[220:223]\n"
    "v_exp_f32_e32 v219, v219\n"
    "v_mfma_f32_16x16x32_bf16 v[220:223], a[188:191], a[88:91], v[220:223]\n"
    "v_cvt_pk_bf16_f32 v216, v216, v217\n"
    "v_mfma_f32_16x16x32_bf16 v[212:215], a[188:191], a[56:59], v[212:215]\n"
    "s_waitcnt lgkmcnt(0)\n"
    "v_mad_u32_u16 v253, v254, s42, v251 op_sel:[0,0,0,0]\n"
    "v_mfma_f32_16x16x32_bf16 v[212:215], a[184:187], a[52:55], v[212:215]\n"
    "buffer_load_dwordx4 a[224:227], v253, s[20:23], 0 offen offset:0 sc1\n"
    "v_mfma_f32_16x16x32_bf16 v[204:207], a[184:187], a[20:23], v[204:207]\n"
    "ds_read_b64_tr_b16 a[184:185], v247 offset:32768\n"
    "v_mfma_f32_16x16x32_bf16 v[204:207], a[188:191], a[24:27], v[204:207]\n"
    "ds_read_b64_tr_b16 a[186:187], v247 offset:34816\n"
    "v_mfma_f32_16x16x32_bf16 v[0:3], a[160:163], v[160:163], v[0:3]\n"
    "ds_read_b64_tr_b16 a[188:189], v247 offset:40960\n"
    "v_mad_u32_u16 v253, v254, s42, v251 op_sel:[1,0,0,0]\n"
    "v_mfma_f32_16x16x32_bf16 v[0:3], a[164:167], v[164:167], v[0:3]\n"
    "ds_read_b64_tr_b16 a[190:191], v247 offset:43008\n"
    "buffer_load_dwordx4 a[228:231], v253, s[20:23], 0 offen offset:0 sc1\n"
    "v_mfma_f32_16x16x32_bf16 a[248:251], a[164:167], v[172:175], a[248:251]\n"
    "v_exp_f32_e32 v208, v208\n"
    "v_mfma_f32_16x16x32_bf16 a[248:251], a[160:163], v[168:171], a[248:251]\n"
    "v_exp_f32_e32 v209, v209\n"
    "v_mfma_f32_16x16x32_bf16 v[56:59], a[160:163], v[176:179], v[56:59]\n"
    "v_mad_u32_u16 v253, v255, s42, v251 op_sel:[0,0,0,0]\n"
    "v_cvt_pk_bf16_f32 v217, v218, v219\n"
    "v_mfma_f32_16x16x32_bf16 v[56:59], a[164:167], v[180:183], v[56:59]\n"
    "buffer_load_dwordx4 a[232:235], v253, s[20:23], 0 offen offset:0 sc1\n"
    "v_mfma_f32_16x16x32_bf16 v[60:63], a[164:167], v[188:191], v[60:63]\n"
    "v_exp_f32_e32 v210, v210\n"
    "v_mfma_f32_16x16x32_bf16 v[60:63], a[160:163], v[184:187], v[60:63]\n"
    "v_exp_f32_e32 v211, v211\n"
    "v_mfma_f32_16x16x32_bf16 v[64:67], a[160:163], v[192:195], v[64:67]\n"
    "ds_read_b64_tr_b16 a[160:161], v244 offset:36864\n"
    "v_mad_u32_u16 v253, v255, s42, v251 op_sel:[1,0,0,0]\n"
    "v_mfma_f32_16x16x32_bf16 v[64:67], a[164:167], v[196:199], v[64:67]\n"
    "ds_read_b64_tr_b16 a[162:163], v244 offset:38912\n"
    "buffer_load_dwordx4 a[236:239], v253, s[20:23], 0 offen offset:0 sc1\n"
    "v_mfma_f32_16x16x32_bf16 v[76:79], a[168:171], v[192:195], v[76:79]\n"
    "ds_read_b64_tr_b16 a[164:165], v244 offset:45056\n"
    "v_cvt_pk_bf16_f32 v208, v208, v209\n"
    "v_mfma_f32_16x16x32_bf16 v[76:79], a[172:175], v[196:199], v[76:79]\n"
    "ds_read_b64_tr_b16 a[166:167], v244 offset:47104\n"
    "v_mfma_f32_16x16x32_bf16 v[72:75], a[172:175], v[188:191], v[72:75]\n"
    "ds_read_b64 v[254:255], v252 offset:104\n"
    "v_mfma_f32_16x16x32_bf16 v[72:75], a[168:171], v[184:187], v[72:75]\n"
    "v_exp_f32_e32 v200, v200\n"
    "v_mfma_f32_16x16x32_bf16 v[68:71], a[168:171], v[176:179], v[68:71]\n"
    "v_exp_f32_e32 v201, v201\n"
    "v_mfma_f32_16x16x32_bf16 v[68:71], a[172:175], v[180:183], v[68:71]\n"
    "v_cvt_pk_bf16_f32 v209, v210, v211\n"
    "v_mfma_f32_16x16x32_bf16 a[252:255], a[172:175], v[172:175], a[252:255]\n"
    "v_exp_f32_e32 v202, v202\n"
    "v_mfma_f32_16x16x32_bf16 a[252:255], a[168:171], v[168:171], a[252:255]\n"
    "v_exp_f32_e32 v203, v203\n"
    "v_mfma_f32_16x16x32_bf16 v[4:7], a[168:171], v[160:163], v[4:7]\n"
    "s_waitcnt lgkmcnt(0)\n"
    "ds_read_b64_tr_b16 a[168:169], v245 offset:36864\n"
    "v_mad_u32_u16 v253, v254, s42, v251 op_sel:[0,0,0,0]\n"
    "v_mfma_f32_16x16x32_bf16 v[4:7], a[172:175], v[164:167], v[4:7]\n"
    "ds_read_b64_tr_b16 a[170:171], v245 offset:38912\n"
    "v_cvt_pk_bf16_f32 v200, v200, v201\n"
    "v_mfma_f32_16x16x32_bf16 v[8:11], a[176:179], v[160:163], v[8:11]\n"
    "ds_read_b64_tr_b16 a[172:173], v245 offset:45056\n"
    "buffer_load_dwordx4 a[240:243], v253, s[20:23], 0 offen offset:0 sc1\n"
    "v_mfma_f32_16x16x32_bf16 v[8:11], a[180:183], v[164:167], v[8:11]\n"
    "ds_read_b64_tr_b16 a[174:175], v245 offset:47104\n"
    "v_cvt_pk_bf16_f32 v201, v202, v203\n"
    "v_mfma_f32_16x16x32_bf16 v[32:35], a[180:183], v[172:175], v[32:35]\n"
    "v_exp_f32_e32 v236, v236\n"
    "v_mfma_f32_16x16x32_bf16 v[32:35], a[176:179], v[168:171], v[32:35]\n"
    "v_mad_u32_u16 v253, v254, s42, v251 op_sel:[1,0,0,0]\n"
    "v_mfma_f32_16x16x32_bf16 v[80:83], a[176:179], v[176:179], v[80:83]\n"
    "buffer_load_dwordx4 a[244:247], v253, s[20:23], 0 offen offset:0 sc1\n"
    "v_mfma_f32_16x16x32_bf16 v[80:83], a[180:183], v[180:183], v[80:83]\n"
    "v_exp_f32_e32 v237, v237\n"
    "v_mfma_f32_16x16x32_bf16 v[84:87], a[180:183], v[188:191], v[84:87]\n"
    "v_exp_f32_e32 v238, v238\n"
    "v_mfma_f32_16x16x32_bf16 v[84:87], a[176:179], v[184:187], v[84:87]\n"
    "v_exp_f32_e32 v239, v239\n"
    "v_mfma_f32_16x16x32_bf16 v[88:91], a[176:179], v[192:195], v[88:91]\n"
    "ds_read_b64_tr_b16 a[176:177], v246 offset:36864\n"
    "v_cvt_pk_bf16_f32 v234, v236, v237\n"
    "v_mfma_f32_16x16x32_bf16 v[88:91], a[180:183], v[196:199], v[88:91]\n"
    "ds_read_b64_tr_b16 a[178:179], v246 offset:38912\n"
    "v_exp_f32_e32 v228, v228\n"
    "v_mfma_f32_16x16x32_bf16 v[100:103], a[184:187], v[192:195], v[100:103]\n"
    "ds_read_b64_tr_b16 a[180:181], v246 offset:45056\n"
    "v_exp_f32_e32 v229, v229\n"
    "v_mfma_f32_16x16x32_bf16 v[100:103], a[188:191], v[196:199], v[100:103]\n"
    "ds_read_b64_tr_b16 a[182:183], v246 offset:47104\n"
    "v_cvt_pk_bf16_f32 v235, v238, v239\n"
    "v_mfma_f32_16x16x32_bf16 v[96:99], a[188:191], v[188:191], v[96:99]\n"
    "v_exp_f32_e32 v230, v230\n"
    "v_mfma_f32_16x16x32_bf16 v[96:99], a[184:187], v[184:187], v[96:99]\n"
    "v_exp_f32_e32 v231, v231\n"
    "v_cvt_pk_bf16_f32 v226, v228, v229\n"
    "v_mfma_f32_16x16x32_bf16 v[92:95], a[184:187], v[176:179], v[92:95]\n"
    "v_exp_f32_e32 v220, v220\n"
    "v_mfma_f32_16x16x32_bf16 v[92:95], a[188:191], v[180:183], v[92:95]\n"
    "v_exp_f32_e32 v221, v221\n"
    "v_mfma_f32_16x16x32_bf16 v[36:39], a[188:191], v[172:175], v[36:39]\n"
    "v_cvt_pk_bf16_f32 v227, v230, v231\n"
    "v_mfma_f32_16x16x32_bf16 v[36:39], a[184:187], v[168:171], v[36:39]\n"
    "v_exp_f32_e32 v222, v222\n"
    "v_mfma_f32_16x16x32_bf16 v[12:15], a[184:187], v[160:163], v[12:15]\n"
    "s_mov_b32 m0, s49\n"
    "ds_read_b64_tr_b16 a[184:185], v247 offset:36864\n"
    "v_mfma_f32_16x16x32_bf16 v[12:15], a[188:191], v[164:167], v[12:15]\n"
    "s_waitcnt lgkmcnt(5)\n"
    "ds_read_b64_tr_b16 a[186:187], v247 offset:38912\n"
    "v_mfma_f32_16x16x32_bf16 v[16:19], a[160:163], v[160:163], v[16:19]\n"
    "ds_read_b64_tr_b16 a[188:189], v247 offset:45056\n"
    "v_mfma_f32_16x16x32_bf16 v[16:19], a[164:167], v[164:167], v[16:19]\n"
    "ds_read_b64_tr_b16 a[190:191], v247 offset:47104\n"
    "v_mfma_f32_16x16x32_bf16 v[40:43], a[164:167], v[172:175], v[40:43]\n"
    "v_exp_f32_e32 v223, v223\n"
    "v_mfma_f32_16x16x32_bf16 v[40:43], a[160:163], v[168:171], v[40:43]\n"
    "v_cvt_pk_bf16_f32 v218, v220, v221\n"
    "v_mfma_f32_16x16x32_bf16 v[104:107], a[160:163], v[176:179], v[104:107]\n"
    "v_exp_f32_e32 v212, v212\n"
    "v_mfma_f32_16x16x32_bf16 v[104:107], a[164:167], v[180:183], v[104:107]\n"
    "s_waitcnt vmcnt(20)\n"
    "v_mfma_f32_16x16x32_bf16 v[108:111], a[164:167], v[188:191], v[108:111]\n"
    "s_barrier\n"
    "v_mfma_f32_16x16x32_bf16 v[108:111], a[160:163], v[184:187], v[108:111]\n"
    "v_exp_f32_e32 v213, v213\n"
    "v_mfma_f32_16x16x32_bf16 v[112:115], a[160:163], v[192:195], v[112:115]\n"
    "ds_read_b128 a[160:163], v243 offset:4096\n"
    "v_cvt_pk_bf16_f32 v219, v222, v223\n"
    "v_mfma_f32_16x16x32_bf16 v[112:115], a[164:167], v[196:199], v[112:115]\n"
    "ds_read_b128 a[164:167], v240 offset:0\n"
    "ds_write_addtid_b32 v232 offset:16384\n"
    "v_mfma_f32_16x16x32_bf16 v[124:127], a[168:171], v[192:195], v[124:127]\n"
    "v_exp_f32_e32 v214, v214\n"
    "v_mfma_f32_16x16x32_bf16 v[124:127], a[172:175], v[196:199], v[124:127]\n"
    "v_exp_f32_e32 v215, v215\n"
    "v_mfma_f32_16x16x32_bf16 v[120:123], a[172:175], v[188:191], v[120:123]\n"
    "ds_write_addtid_b32 v233 offset:16640\n"
    "v_cvt_pk_bf16_f32 v210, v212, v213\n"
    "v_mfma_f32_16x16x32_bf16 v[120:123], a[168:171], v[184:187], v[120:123]\n"
    "v_exp_f32_e32 v204, v204\n"
    "v_mfma_f32_16x16x32_bf16 v[116:119], a[168:171], v[176:179], v[116:119]\n"
    "v_exp_f32_e32 v205, v205\n"
    "v_mfma_f32_16x16x32_bf16 v[116:119], a[172:175], v[180:183], v[116:119]\n"
    "ds_write_addtid_b32 v224 offset:12288\n"
    "v_cvt_pk_bf16_f32 v211, v214, v215\n"
    "v_mfma_f32_16x16x32_bf16 v[44:47], a[172:175], v[172:175], v[44:47]\n"
    "v_exp_f32_e32 v206, v206\n"
    "v_mfma_f32_16x16x32_bf16 v[44:47], a[168:171], v[168:171], v[44:47]\n"
    "v_exp_f32_e32 v207, v207\n"
    "v_mfma_f32_16x16x32_bf16 v[20:23], a[168:171], v[160:163], v[20:23]\n"
    "ds_read_b128 a[168:171], v241 offset:0\n"
    "v_cvt_pk_bf16_f32 v202, v204, v205\n"
    "v_mfma_f32_16x16x32_bf16 v[20:23], a[172:175], v[164:167], v[20:23]\n"
    "s_waitcnt lgkmcnt(6)\n"
    "ds_read_b128 a[172:175], v240 offset:4096\n"
    "v_cvt_pk_bf16_f32 v203, v206, v207\n"
    "v_mfma_f32_16x16x32_bf16 v[24:27], a[176:179], v[160:163], v[24:27]\n"
    "ds_write_addtid_b32 v225 offset:12544\n"
    "ds_write_addtid_b32 v216 offset:8192\n"
    "v_mfma_f32_16x16x32_bf16 v[24:27], a[180:183], v[164:167], v[24:27]\n"
    "ds_write_addtid_b32 v217 offset:8448\n"
    "v_mfma_f32_16x16x32_bf16 v[48:51], a[180:183], v[172:175], v[48:51]\n"
    "ds_write_addtid_b32 v208 offset:4096\n"
    "v_mfma_f32_16x16x32_bf16 v[48:51], a[176:179], v[168:171], v[48:51]\n"
    "ds_write_addtid_b32 v209 offset:4352\n"
    "v_mfma_f32_16x16x32_bf16 v[128:131], a[176:179], v[176:179], v[128:131]\n"
    "ds_write_addtid_b32 v200 offset:0\n"
    "v_mfma_f32_16x16x32_bf16 v[128:131], a[180:183], v[180:183], v[128:131]\n"
    "ds_write_addtid_b32 v201 offset:256\n"
    "v_mfma_f32_16x16x32_bf16 v[132:135], a[180:183], v[188:191], v[132:135]\n"
    "ds_write_addtid_b32 v234 offset:16896\n"
    "v_mfma_f32_16x16x32_bf16 v[132:135], a[176:179], v[184:187], v[132:135]\n"
    "ds_write_addtid_b32 v235 offset:17152\n"
    "v_mfma_f32_16x16x32_bf16 v[136:139], a[176:179], v[192:195], v[136:139]\n"
    "ds_read_b128 a[176:179], v241 offset:4096\n"
    "v_mfma_f32_16x16x32_bf16 v[136:139], a[180:183], v[196:199], v[136:139]\n"
    "ds_read_b128 a[180:183], v242 offset:0\n"
    "v_mfma_f32_16x16x32_bf16 v[148:151], a[184:187], v[192:195], v[148:151]\n"
    "ds_write_addtid_b32 v226 offset:12800\n"
    "v_mfma_f32_16x16x32_bf16 v[148:151], a[188:191], v[196:199], v[148:151]\n"
    "ds_write_addtid_b32 v227 offset:13056\n"
    "v_mfma_f32_16x16x32_bf16 v[144:147], a[188:191], v[188:191], v[144:147]\n"
    "ds_write_addtid_b32 v218 offset:8704\n"
    "v_mfma_f32_16x16x32_bf16 v[144:147], a[184:187], v[184:187], v[144:147]\n"
    "ds_write_addtid_b32 v219 offset:8960\n"
    "v_mfma_f32_16x16x32_bf16 v[140:143], a[184:187], v[176:179], v[140:143]\n"
    "ds_write_addtid_b32 v210 offset:4608\n"
    "v_mfma_f32_16x16x32_bf16 v[140:143], a[188:191], v[180:183], v[140:143]\n"
    "ds_write_addtid_b32 v211 offset:4864\n"
    "v_mfma_f32_16x16x32_bf16 v[52:55], a[188:191], v[172:175], v[52:55]\n"
    "ds_write_addtid_b32 v202 offset:512\n"
    "v_mfma_f32_16x16x32_bf16 v[52:55], a[184:187], v[168:171], v[52:55]\n"
    "ds_write_addtid_b32 v203 offset:768\n"
    "v_mfma_f32_16x16x32_bf16 v[28:31], a[184:187], v[160:163], v[28:31]\n"
    "ds_read_b128 a[184:187], v243 offset:0\n"
    "v_mfma_f32_16x16x32_bf16 v[28:31], a[188:191], v[164:167], v[28:31]\n"
    "s_waitcnt lgkmcnt(15)\n"
    "ds_read_b128 a[188:191], v242 offset:4096\n"
    "v_mfma_f32_16x16x32_bf16 v[160:163], a[160:163], a[28:31], 0\n"
    "v_mfma_f32_16x16x32_bf16 v[160:163], a[164:167], a[0:3], v[160:163]\n"
    "v_mfma_f32_16x16x32_bf16 v[168:171], a[164:167], a[32:35], 0\n"
    "v_mfma_f32_16x16x32_bf16 v[168:171], a[160:163], a[60:63], v[168:171]\n"
    "v_mfma_f32_16x16x32_bf16 v[176:179], a[160:163], a[92:95], 0\n"
    "v_mfma_f32_16x16x32_bf16 v[176:179], a[164:167], a[64:67], v[176:179]\n"
    "s_waitcnt lgkmcnt(0)\n"
    "v_mfma_f32_16x16x32_bf16 v[184:187], a[164:167], a[96:99], 0\n"
    "s_barrier\n"
    "v_mfma_f32_16x16x32_bf16 v[184:187], a[160:163], a[124:127], v[184:187]\n"
    "s_add_u32 m0, s43, 32768\n"
    "v_mfma_f32_16x16x32_bf16 v[192:195], a[160:163], a[156:159], 0\n"
    "ds_read_b128 a[160:163], v243 offset:6144\n"
    "v_mfma_f32_16x16x32_bf16 v[192:195], a[164:167], a[128:131], v[192:195]\n"
    "ds_read_b128 a[164:167], v240 offset:2048\n"
    "v_mfma_f32_16x16x32_bf16 v[192:195], a[168:171], a[132:135], v[192:195]\n"
    "buffer_load_dwordx4 v248, s[8:11], s40 offen offset:0 sc1 lds\n"
    "v_mfma_f32_16x16x32_bf16 v[192:195], a[172:175], a[136:139], v[192:195]\n"
    "ds_read2st64_b32 v[204:205], v250 offset0:0 offset1:1\n"
    "v_mfma_f32_16x16x32_bf16 v[184:187], a[172:175], a[104:107], v[184:187]\n"
    "s_waitcnt vmcnt(11)\n"
    "v_smfmac_f32_16x16x64_bf16 v[156:159], a[192:195], a[200:207], v152\n"
    "v_mfma_f32_16x16x32_bf16 v[184:187], a[168:171], a[100:103], v[184:187]\n"
    "ds_read2st64_b32 v[206:207], v250 offset0:2 offset1:3\n"
    "v_mfma_f32_16x16x32_bf16 v[176:179], a[168:171], a[68:71], v[176:179]\n"
    "buffer_load_dwordx4 v248, s[8:11], s40 offen offset:1024 sc1 lds\n"
    "v_mfma_f32_16x16x32_bf16 v[176:179], a[172:175], a[72:75], v[176:179]\n"
    "v_mfma_f32_16x16x32_bf16 v[168:171], a[172:175], a[40:43], v[168:171]\n"
    "s_waitcnt vmcnt(10)\n"
    "v_smfmac_f32_16x16x64_bf16 v[156:159], a[192:195], a[208:215], v152\n"
    "ds_read2st64_b32 v[212:213], v250 offset0:16 offset1:17\n"
    "v_mfma_f32_16x16x32_bf16 v[168:171], a[168:171], a[36:39], v[168:171]\n"
    "v_mfma_f32_16x16x32_bf16 v[160:163], a[168:171], a[4:7], v[160:163]\n"
    "ds_read_b128 a[168:171], v241 offset:2048\n"
    "buffer_load_dwordx4 v248, s[8:11], s40 offen offset:2048 sc1 lds\n"
    "v_mfma_f32_16x16x32_bf16 v[160:163], a[172:175], a[8:11], v[160:163]\n"
    "ds_read_b128 a[172:175], v240 offset:6144\n"
    "v_mfma_f32_16x16x32_bf16 v[160:163], a[176:179], a[12:15], v[160:163]\n"
    "s_waitcnt vmcnt(9)\n"
    "ds_read2st64_b32 v[214:215], v250 offset0:18 offset1:19\n"
    "v_smfmac_f32_16x16x64_bf16 v[156:159], a[192:195], a[216:223], v152\n"
    "v_mfma_f32_16x16x32_bf16 v[160:163], a[180:183], a[16:19], v[160:163]\n"
    "v_mfma_f32_16x16x32_bf16 v[168:171], a[180:183], a[48:51], v[168:171]\n"
    "buffer_load_dwordx4 v248, s[8:11], s40 offen offset:3072 sc1 lds\n"
    "v_mfma_f32_16x16x32_bf16 v[168:171], a[176:179], a[44:47], v[168:171]\n"
    "s_add_u32 m0, s43, 36864\n"
    "v_mfma_f32_16x16x32_bf16 v[176:179], a[176:179], a[76:79], v[176:179]\n"
    "s_add_u32 s40, s40, 0x8000\n"
    "ds_read2st64_b32 v[220:221], v250 offset0:32 offset1:33\n"
    "v_mfma_f32_16x16x32_bf16 v[176:179], a[180:183], a[80:83], v[176:179]\n"
    "v_mfma_f32_16x16x32_bf16 v[184:187], a[180:183], a[112:115], v[184:187]\n"
    "v_mfma_f32_16x16x32_bf16 v[184:187], a[176:179], a[108:111], v[184:187]\n"
    "buffer_load_dwordx4 v248, s[8:11], s41 offen offset:0 sc1 lds\n"
    "v_mfma_f32_16x16x32_bf16 v[192:195], a[176:179], a[140:143], v[192:195]\n"
    "ds_read_b128 a[176:179], v241 offset:6144\n"
    "v_mfma_f32_16x16x32_bf16 v[192:195], a[180:183], a[144:147], v[192:195]\n"
    "ds_read_b128 a[180:183], v242 offset:2048\n"
    "v_mfma_f32_16x16x32_bf16 v[192:195], a[184:187], a[148:151], v[192:195]\n"
    "ds_read2st64_b32 v[222:223], v250 offset0:34 offset1:35\n"
    "v_mfma_f32_16x16x32_bf16 v[192:195], a[188:191], a[152:155], v[192:195]\n"
    "v_mfma_f32_16x16x32_bf16 v[184:187], a[188:191], a[120:123], v[184:187]\n"
    "buffer_load_dwordx4 v248, s[8:11], s41 offen offset:1024 sc1 lds\n"
    "v_mfma_f32_16x16x32_bf16 v[184:187], a[184:187], a[116:119], v[184:187]\n"
    "ds_read2st64_b32 v[228:229], v250 offset0:48 offset1:49\n"
    "v_mfma_f32_16x16x32_bf16 v[176:179], a[184:187], a[84:87], v[176:179]\n"
    "s_waitcnt vmcnt(10)\n"
    "v_exp_f32_e32 v192, v192\n"
    "v_smfmac_f32_16x16x64_bf16 v[156:159], a[192:195], a[224:231], v152\n"
    "v_exp_f32_e32 v193, v193\n"
    "v_mfma_f32_16x16x32_bf16 v[176:179], a[188:191], a[88:91], v[176:179]\n"
    "v_exp_f32_e32 v194, v194\n"
    "v_mfma_f32_16x16x32_bf16 v[168:171], a[188:191], a[56:59], v[168:171]\n"
    "buffer_load_dwordx4 v248, s[8:11], s41 offen offset:2048 sc1 lds\n"
    "v_mfma_f32_16x16x32_bf16 v[168:171], a[184:187], a[52:55], v[168:171]\n"
    "ds_read2st64_b32 v[230:231], v250 offset0:50 offset1:51\n"
    "v_mfma_f32_16x16x32_bf16 v[160:163], a[184:187], a[20:23], v[160:163]\n"
    "s_waitcnt vmcnt(9)\n"
    "ds_read_b128 a[184:187], v243 offset:2048\n"
    "v_smfmac_f32_16x16x64_bf16 v[156:159], a[192:195], a[232:239], v152\n"
    "ds_read2st64_b32 v[236:237], v250 offset0:64 offset1:65\n"
    "v_mfma_f32_16x16x32_bf16 v[160:163], a[188:191], a[24:27], v[160:163]\n"
    "s_waitcnt lgkmcnt(9)\n"
    "ds_read_b128 a[188:191], v242 offset:6144\n"
    "v_mfma_f32_16x16x32_bf16 v[164:167], a[160:163], a[28:31], 0\n"
    "buffer_load_dwordx4 v248, s[8:11], s41 offen offset:3072 sc1 lds\n"
    "v_mfma_f32_16x16x32_bf16 v[164:167], a[164:167], a[0:3], v[164:167]\n"
    "s_add_u32 s41, s41, 0x8000\n"
    "v_exp_f32_e32 v195, v195\n"
    "v_mfma_f32_16x16x32_bf16 v[172:175], a[164:167], a[32:35], 0\n"
    "s_waitcnt vmcnt(8)\n"
    "v_cvt_pk_bf16_f32 v192, v192, v193\n"
    "v_smfmac_f32_16x16x64_bf16 v[156:159], a[192:195], a[240:247], v152\n"
    "v_exp_f32_e32 v184, v184\n"
    "v_mfma_f32_16x16x32_bf16 v[172:175], a[160:163], a[60:63], v[172:175]\n"
    "ds_read2st64_b32 v[238:239], v250 offset0:66 offset1:67\n"
    "v_mfma_f32_16x16x32_bf16 v[180:183], a[160:163], a[92:95], 0\n"
    "v_exp_f32_e32 v185, v185\n"
    "v_mfma_f32_16x16x32_bf16 v[180:183], a[164:167], a[64:67], v[180:183]\n"
    "v_cvt_pk_bf16_f32 v193, v194, v195\n"
    "v_mfma_f32_16x16x32_bf16 v[188:191], a[164:167], a[96:99], 0\n"
    "v_exp_f32_e32 v186, v186\n"
    "v_mfma_f32_16x16x32_bf16 v[188:191], a[160:163], a[124:127], v[188:191]\n"
    "v_exp_f32_e32 v187, v187\n"
    "v_mfma_f32_16x16x32_bf16 v[196:199], a[160:163], a[156:159], 0\n"
    "ds_read_b64_tr_b16 a[160:161], v244 offset:49152\n"
    "v_and_b32_e32 v253, 0xffff0000, v255\n"
    "v_mfma_f32_16x16x32_bf16 v[196:199], a[164:167], a[128:131], v[196:199]\n"
    "ds_read_b64_tr_b16 a[162:163], v244 offset:51200\n"
    "v_mfma_f32_16x16x32_bf16 v[196:199], a[168:171], a[132:135], v[196:199]\n"
    "ds_read_b64_tr_b16 a[164:165], v244 offset:57344\n"
    "v_mfma_f32_16x16x32_bf16 v[196:199], a[172:175], a[136:139], v[196:199]\n"
    "ds_read_b64_tr_b16 a[166:167], v244 offset:59392\n"
    "v_mfma_f32_16x16x32_bf16 v[188:191], a[172:175], a[104:107], v[188:191]\n"
    "v_cvt_pk_bf16_f32 v184, v184, v185\n"
    "v_mfma_f32_16x16x32_bf16 v[188:191], a[168:171], a[100:103], v[188:191]\n"
    "v_exp_f32_e32 v176, v176\n"
    "v_mfma_f32_16x16x32_bf16 v[180:183], a[168:171], a[68:71], v[180:183]\n"
    "v_exp_f32_e32 v177, v177\n"
    "v_mfma_f32_16x16x32_bf16 v[180:183], a[172:175], a[72:75], v[180:183]\n"
    "v_cvt_pk_bf16_f32 v185, v186, v187\n"
    "v_mul_f32_e32 v156, v253, v156\n"
    "v_mfma_f32_16x16x32_bf16 v[172:175], a[172:175], a[40:43], v[172:175]\n"
    "v_exp_f32_e32 v178, v178\n"
    "v_mfma_f32_16x16x32_bf16 v[172:175], a[168:171], a[36:39], v[172:175]\n"
    "v_exp_f32_e32 v179, v179\n"
    "v_mfma_f32_16x16x32_bf16 v[164:167], a[168:171], a[4:7], v[164:167]\n"
    "ds_read_b64_tr_b16 a[168:169], v245 offset:49152\n"
    "v_mfma_f32_16x16x32_bf16 v[164:167], a[172:175], a[8:11], v[164:167]\n"
    "s_waitcnt lgkmcnt(6)\n"
    "ds_read_b64_tr_b16 a[170:171], v245 offset:51200\n"
    "v_mfma_f32_16x16x32_bf16 v[164:167], a[176:179], a[12:15], v[164:167]\n"
    "ds_read_b64_tr_b16 a[172:173], v245 offset:57344\n"
    "v_mul_f32_e32 v157, v253, v157\n"
    "v_mfma_f32_16x16x32_bf16 v[164:167], a[180:183], a[16:19], v[164:167]\n"
    "ds_read_b64_tr_b16 a[174:175], v245 offset:59392\n"
    "v_mfma_f32_16x16x32_bf16 v[172:175], a[180:183], a[48:51], v[172:175]\n"
    "v_cvt_pk_bf16_f32 v176, v176, v177\n"
    "v_mfma_f32_16x16x32_bf16 v[172:175], a[176:179], a[44:47], v[172:175]\n"
    "v_exp_f32_e32 v168, v168\n"
    "v_mfma_f32_16x16x32_bf16 v[180:183], a[176:179], a[76:79], v[180:183]\n"
    "v_mul_f32_e32 v158, v253, v158\n"
    "v_mfma_f32_16x16x32_bf16 v[180:183], a[180:183], a[80:83], v[180:183]\n"
    "v_exp_f32_e32 v169, v169\n"
    "v_mfma_f32_16x16x32_bf16 v[188:191], a[180:183], a[112:115], v[188:191]\n"
    "v_cvt_pk_bf16_f32 v177, v178, v179\n"
    "v_mfma_f32_16x16x32_bf16 v[188:191], a[176:179], a[108:111], v[188:191]\n"
    "v_exp_f32_e32 v170, v170\n"
    "v_mfma_f32_16x16x32_bf16 v[196:199], a[176:179], a[140:143], v[196:199]\n"
    "ds_read_b64_tr_b16 a[176:177], v246 offset:49152\n"
    "v_mfma_f32_16x16x32_bf16 v[196:199], a[180:183], a[144:147], v[196:199]\n"
    "ds_read_b64_tr_b16 a[178:179], v246 offset:51200\n"
    "v_mfma_f32_16x16x32_bf16 v[196:199], a[184:187], a[148:151], v[196:199]\n"
    "ds_read_b64_tr_b16 a[180:181], v246 offset:57344\n"
    "v_mfma_f32_16x16x32_bf16 v[196:199], a[188:191], a[152:155], v[196:199]\n"
    "ds_read_b64_tr_b16 a[182:183], v246 offset:59392\n"
    "v_mul_f32_e32 v159, v253, v159\n"
    "v_mfma_f32_16x16x32_bf16 v[188:191], a[188:191], a[120:123], v[188:191]\n"
    "v_exp_f32_e32 v171, v171\n"
    "v_mfma_f32_16x16x32_bf16 v[188:191], a[184:187], a[116:119], v[188:191]\n"
    "v_cvt_pk_bf16_f32 v168, v168, v169\n"
    "v_mfma_f32_16x16x32_bf16 v[180:183], a[184:187], a[84:87], v[180:183]\n"
    "v_exp_f32_e32 v160, v160\n"
    "v_mfma_f32_16x16x32_bf16 v[180:183], a[188:191], a[88:91], v[180:183]\n"
    "v_exp_f32_e32 v161, v161\n"
    "v_mfma_f32_16x16x32_bf16 v[172:175], a[188:191], a[56:59], v[172:175]\n"
    "v_cvt_pk_bf16_f32 v169, v170, v171\n"
    "v_mfma_f32_16x16x32_bf16 v[172:175], a[184:187], a[52:55], v[172:175]\n"
    "v_exp_f32_e32 v162, v162\n"
    "v_mfma_f32_16x16x32_bf16 v[164:167], a[184:187], a[20:23], v[164:167]\n"
    "ds_read_b64_tr_b16 a[184:185], v247 offset:49152\n"
    "v_mad_u32_u16 v253, v255, s42, v153 op_sel:[0,0,0,0]\n"
    "v_mfma_f32_16x16x32_bf16 v[164:167], a[188:191], a[24:27], v[164:167]\n"
    "s_waitcnt lgkmcnt(5)\n"
    "ds_read_b64_tr_b16 a[186:187], v247 offset:51200\n"
    "v_mfma_f32_16x16x32_bf16 v[0:3], a[160:163], v[200:203], v[0:3]\n"
    "ds_read_b64_tr_b16 a[188:189], v247 offset:57344\n"
    "v_mfma_f32_16x16x32_bf16 v[0:3], a[164:167], v[204:207], v[0:3]\n"
    "ds_read_b64_tr_b16 a[190:191], v247 offset:59392\n"
    "v_mfma_f32_16x16x32_bf16 a[248:251], a[164:167], v[212:215], a[248:251]\n"
    "v_exp_f32_e32 v163, v163\n"
    "v_mfma_f32_16x16x32_bf16 a[248:251], a[160:163], v[208:211], a[248:251]\n"
    "v_cvt_pk_bf16_f32 v160, v160, v161\n"
    "v_mfma_f32_16x16x32_bf16 v[56:59], a[160:163], v[216:219], v[56:59]\n"
    "v_cvt_pk_bf16_f32 v154, v156, v157\n"
    "v_mfma_f32_16x16x32_bf16 v[56:59], a[164:167], v[220:223], v[56:59]\n"
    "v_cvt_pk_bf16_f32 v161, v162, v163\n"
    "v_mfma_f32_16x16x32_bf16 v[60:63], a[164:167], v[228:231], v[60:63]\n"
    "v_exp_f32_e32 v196, v196\n"
    "v_mfma_f32_16x16x32_bf16 v[60:63], a[160:163], v[224:227], v[60:63]\n"
    "v_exp_f32_e32 v197, v197\n"
    "v_mfma_f32_16x16x32_bf16 v[64:67], a[160:163], v[232:235], v[64:67]\n"
    "ds_read_b64_tr_b16 a[160:161], v244 offset:53248\n"
    "v_mfma_f32_16x16x32_bf16 v[64:67], a[164:167], v[236:239], v[64:67]\n"
    "ds_read_b64_tr_b16 a[162:163], v244 offset:55296\n"
    "v_cvt_pk_bf16_f32 v155, v158, v159\n"
    "v_mfma_f32_16x16x32_bf16 v[76:79], a[168:171], v[232:235], v[76:79]\n"
    "ds_read_b64_tr_b16 a[164:165], v244 offset:61440\n"
    "v_mfma_f32_16x16x32_bf16 v[76:79], a[172:175], v[236:239], v[76:79]\n"
    "ds_read_b64_tr_b16 a[166:167], v244 offset:63488\n"
    "v_mfma_f32_16x16x32_bf16 v[72:75], a[172:175], v[228:231], v[72:75]\n"
    "v_exp_f32_e32 v198, v198\n"
    "v_mfma_f32_16x16x32_bf16 v[72:75], a[168:171], v[224:227], v[72:75]\n"
    "v_exp_f32_e32 v199, v199\n"
    "v_mfma_f32_16x16x32_bf16 v[68:71], a[168:171], v[216:219], v[68:71]\n"
    "v_cvt_pk_bf16_f32 v194, v196, v197\n"
    "v_mov_b32_e32 v156, 0\n"
    "v_mfma_f32_16x16x32_bf16 v[68:71], a[172:175], v[220:223], v[68:71]\n"
    "v_exp_f32_e32 v188, v188\n"
    "v_mfma_f32_16x16x32_bf16 a[252:255], a[172:175], v[212:215], a[252:255]\n"
    "v_exp_f32_e32 v189, v189\n"
    "v_mfma_f32_16x16x32_bf16 a[252:255], a[168:171], v[208:211], a[252:255]\n"
    "v_cvt_pk_bf16_f32 v195, v198, v199\n"
    "v_mfma_f32_16x16x32_bf16 v[4:7], a[168:171], v[200:203], v[4:7]\n"
    "ds_read_b64_tr_b16 a[168:169], v245 offset:53248\n"
    "v_mfma_f32_16x16x32_bf16 v[4:7], a[172:175], v[204:207], v[4:7]\n"
    "s_waitcnt lgkmcnt(5)\n"
    "ds_read_b64_tr_b16 a[170:171], v245 offset:55296\n"
    "v_mfma_f32_16x16x32_bf16 v[8:11], a[176:179], v[200:203], v[8:11]\n"
    "ds_read_b64_tr_b16 a[172:173], v245 offset:61440\n"
    "v_mfma_f32_16x16x32_bf16 v[8:11], a[180:183], v[204:207], v[8:11]\n"
    "ds_read_b64_tr_b16 a[174:175], v245 offset:63488\n"
    "v_mov_b32_e32 v157, 0\n"
    "v_mfma_f32_16x16x32_bf16 v[32:35], a[180:183], v[212:215], v[32:35]\n"
    "v_exp_f32_e32 v190, v190\n"
    "v_mfma_f32_16x16x32_bf16 v[32:35], a[176:179], v[208:211], v[32:35]\n"
    "v_exp_f32_e32 v191, v191\n"
    "v_mfma_f32_16x16x32_bf16 v[80:83], a[176:179], v[216:219], v[80:83]\n"
    "v_mov_b32_e32 v158, 0\n"
    "v_mfma_f32_16x16x32_bf16 v[80:83], a[180:183], v[220:223], v[80:83]\n"
    "v_cvt_pk_bf16_f32 v186, v188, v189\n"
    "v_mfma_f32_16x16x32_bf16 v[84:87], a[180:183], v[228:231], v[84:87]\n"
    "v_exp_f32_e32 v180, v180\n"
    "v_mfma_f32_16x16x32_bf16 v[84:87], a[176:179], v[224:227], v[84:87]\n"
    "v_exp_f32_e32 v181, v181\n"
    "v_mfma_f32_16x16x32_bf16 v[88:91], a[176:179], v[232:235], v[88:91]\n"
    "ds_read_b64_tr_b16 a[176:177], v246 offset:53248\n"
    "v_mfma_f32_16x16x32_bf16 v[88:91], a[180:183], v[236:239], v[88:91]\n"
    "ds_read_b64_tr_b16 a[178:179], v246 offset:55296\n"
    "v_mfma_f32_16x16x32_bf16 v[100:103], a[184:187], v[232:235], v[100:103]\n"
    "ds_read_b64_tr_b16 a[180:181], v246 offset:61440\n"
    "v_mfma_f32_16x16x32_bf16 v[100:103], a[188:191], v[236:239], v[100:103]\n"
    "ds_read_b64_tr_b16 a[182:183], v246 offset:63488\n"
    "v_mfma_f32_16x16x32_bf16 v[96:99], a[188:191], v[228:231], v[96:99]\n"
    "v_cvt_pk_bf16_f32 v187, v190, v191\n"
    "v_mov_b32_e32 v159, 0\n"
    "v_mfma_f32_16x16x32_bf16 v[96:99], a[184:187], v[224:227], v[96:99]\n"
    "v_exp_f32_e32 v182, v182\n"
    "v_mfma_f32_16x16x32_bf16 v[92:95], a[184:187], v[216:219], v[92:95]\n"
    "v_exp_f32_e32 v183, v183\n"
    "v_mfma_f32_16x16x32_bf16 v[92:95], a[188:191], v[220:223], v[92:95]\n"
    "v_cvt_pk_bf16_f32 v178, v180, v181\n"
    "v_mfma_f32_16x16x32_bf16 v[36:39], a[188:191], v[212:215], v[36:39]\n"
    "v_exp_f32_e32 v172, v172\n"
    "v_mfma_f32_16x16x32_bf16 v[36:39], a[184:187], v[208:211], v[36:39]\n"
    "v_exp_f32_e32 v173, v173\n"
    "v_mfma_f32_16x16x32_bf16 v[12:15], a[184:187], v[200:203], v[12:15]\n"
    "s_mov_b32 m0, s49\n"
    "ds_read_b64_tr_b16 a[184:185], v247 offset:53248\n"
    "v_mfma_f32_16x16x32_bf16 v[12:15], a[188:191], v[204:207], v[12:15]\n"
    "s_waitcnt lgkmcnt(5)\n"
    "ds_read_b64_tr_b16 a[186:187], v247 offset:55296\n"
    "v_add_u32_e32 v252, 0x80, v252\n"
    "v_mfma_f32_16x16x32_bf16 v[16:19], a[160:163], v[200:203], v[16:19]\n"
    "ds_read_b64_tr_b16 a[188:189], v247 offset:61440\n"
    "v_mfma_f32_16x16x32_bf16 v[16:19], a[164:167], v[204:207], v[16:19]\n"
    "ds_read_b64_tr_b16 a[190:191], v247 offset:63488\n"
    "v_mfma_f32_16x16x32_bf16 v[40:43], a[164:167], v[212:215], v[40:43]\n"
    "buffer_store_dwordx2 v[154:155], v253, s[24:27], 0 offen offset:0 sc1\n"
    "v_cvt_pk_bf16_f32 v179, v182, v183\n"
    "v_mfma_f32_16x16x32_bf16 v[40:43], a[160:163], v[208:211], v[40:43]\n"
    "v_exp_f32_e32 v174, v174\n"
    "v_mfma_f32_16x16x32_bf16 v[104:107], a[160:163], v[216:219], v[104:107]\n"
    "v_exp_f32_e32 v175, v175\n"
    "v_mfma_f32_16x16x32_bf16 v[104:107], a[164:167], v[220:223], v[104:107]\n"
    "s_waitcnt vmcnt(9)\n"
    "v_mfma_f32_16x16x32_bf16 v[108:111], a[164:167], v[228:231], v[108:111]\n"
    "s_barrier\n"
    "v_mfma_f32_16x16x32_bf16 v[108:111], a[160:163], v[224:227], v[108:111]\n"
    "ds_write_addtid_b32 v192 offset:16384\n"
    "v_cvt_pk_bf16_f32 v170, v172, v173\n"
    "v_mfma_f32_16x16x32_bf16 v[112:115], a[160:163], v[232:235], v[112:115]\n"
    "ds_read_b128 a[160:163], v243 offset:20480\n"
    "v_mfma_f32_16x16x32_bf16 v[112:115], a[164:167], v[236:239], v[112:115]\n"
    "ds_read_b128 a[164:167], v240 offset:16384\n"
    "v_mfma_f32_16x16x32_bf16 v[124:127], a[168:171], v[232:235], v[124:127]\n"
    "v_exp_f32_e32 v164, v164\n"
    "v_mfma_f32_16x16x32_bf16 v[124:127], a[172:175], v[236:239], v[124:127]\n"
    "v_exp_f32_e32 v165, v165\n"
    "v_mfma_f32_16x16x32_bf16 v[120:123], a[172:175], v[228:231], v[120:123]\n"
    "ds_write_addtid_b32 v193 offset:16640\n"
    "v_cvt_pk_bf16_f32 v171, v174, v175\n"
    "v_mfma_f32_16x16x32_bf16 v[120:123], a[168:171], v[224:227], v[120:123]\n"
    "ds_write_addtid_b32 v184 offset:12288\n"
    "v_mfma_f32_16x16x32_bf16 v[116:119], a[168:171], v[216:219], v[116:119]\n"
    "v_exp_f32_e32 v166, v166\n"
    "v_mfma_f32_16x16x32_bf16 v[116:119], a[172:175], v[220:223], v[116:119]\n"
    "v_exp_f32_e32 v167, v167\n"
    "v_mfma_f32_16x16x32_bf16 v[44:47], a[172:175], v[212:215], v[44:47]\n"
    "ds_write_addtid_b32 v185 offset:12544\n"
    "v_cvt_pk_bf16_f32 v162, v164, v165\n"
    "v_mfma_f32_16x16x32_bf16 v[44:47], a[168:171], v[208:211], v[44:47]\n"
    "v_cvt_pk_bf16_f32 v163, v166, v167\n"
    "v_mfma_f32_16x16x32_bf16 v[20:23], a[168:171], v[200:203], v[20:23]\n"
    "ds_read_b128 a[168:171], v241 offset:16384\n"
    "v_mfma_f32_16x16x32_bf16 v[20:23], a[172:175], v[204:207], v[20:23]\n"
    "s_waitcnt lgkmcnt(7)\n"
    "ds_read_b128 a[172:175], v240 offset:20480\n"
    "v_mfma_f32_16x16x32_bf16 v[24:27], a[176:179], v[200:203], v[24:27]\n"
    "ds_write_addtid_b32 v176 offset:8192\n"
    "v_mfma_f32_16x16x32_bf16 v[24:27], a[180:183], v[204:207], v[24:27]\n"
    "ds_write_addtid_b32 v177 offset:8448\n"
    "v_mfma_f32_16x16x32_bf16 v[48:51], a[180:183], v[212:215], v[48:51]\n"
    "ds_write_addtid_b32 v168 offset:4096\n"
    "v_mfma_f32_16x16x32_bf16 v[48:51], a[176:179], v[208:211], v[48:51]\n"
    "ds_write_addtid_b32 v169 offset:4352\n"
    "v_mfma_f32_16x16x32_bf16 v[128:131], a[176:179], v[216:219], v[128:131]\n"
    "ds_write_addtid_b32 v160 offset:0\n"
    "v_mfma_f32_16x16x32_bf16 v[128:131], a[180:183], v[220:223], v[128:131]\n"
    "ds_write_addtid_b32 v161 offset:256\n"
    "v_mfma_f32_16x16x32_bf16 v[132:135], a[180:183], v[228:231], v[132:135]\n"
    "ds_write_addtid_b32 v194 offset:16896\n"
    "v_mfma_f32_16x16x32_bf16 v[132:135], a[176:179], v[224:227], v[132:135]\n"
    "ds_write_addtid_b32 v195 offset:17152\n"
    "v_mfma_f32_16x16x32_bf16 v[136:139], a[176:179], v[232:235], v[136:139]\n"
    "ds_read_b128 a[176:179], v241 offset:20480\n"
    "v_mfma_f32_16x16x32_bf16 v[136:139], a[180:183], v[236:239], v[136:139]\n"
    "ds_read_b128 a[180:183], v242 offset:16384\n"
    "v_mfma_f32_16x16x32_bf16 v[148:151], a[184:187], v[232:235], v[148:151]\n"
    "ds_write_addtid_b32 v186 offset:12800\n"
    "v_mfma_f32_16x16x32_bf16 v[148:151], a[188:191], v[236:239], v[148:151]\n"
    "ds_write_addtid_b32 v187 offset:13056\n"
    "v_mfma_f32_16x16x32_bf16 v[144:147], a[188:191], v[228:231], v[144:147]\n"
    "ds_write_addtid_b32 v178 offset:8704\n"
    "v_mfma_f32_16x16x32_bf16 v[144:147], a[184:187], v[224:227], v[144:147]\n"
    "ds_write_addtid_b32 v179 offset:8960\n"
    "v_mfma_f32_16x16x32_bf16 v[140:143], a[184:187], v[216:219], v[140:143]\n"
    "s_sub_u32 s44, s44, 1\n"
    "ds_write_addtid_b32 v170 offset:4608\n"
    "v_mfma_f32_16x16x32_bf16 v[140:143], a[188:191], v[220:223], v[140:143]\n"
    "ds_write_addtid_b32 v171 offset:4864\n"
    "v_mfma_f32_16x16x32_bf16 v[52:55], a[188:191], v[212:215], v[52:55]\n"
    "ds_write_addtid_b32 v162 offset:512\n"
    "v_mfma_f32_16x16x32_bf16 v[52:55], a[184:187], v[208:211], v[52:55]\n"
    "ds_write_addtid_b32 v163 offset:768\n"
    "v_mfma_f32_16x16x32_bf16 v[28:31], a[184:187], v[200:203], v[28:31]\n"
    "ds_read_b128 a[184:187], v243 offset:16384\n"
    "v_mfma_f32_16x16x32_bf16 v[28:31], a[188:191], v[204:207], v[28:31]\n"
    "s_waitcnt lgkmcnt(15)\n"
    "ds_read_b128 a[188:191], v242 offset:20480\n"
    "s_cbranch_scc0 .Lat_g\n"
    "s_mov_b32 s44, 1\n"
    "s_cmp_eq_u32 s34, 0\n"
    "s_cbranch_scc1 .Lat_p\n"
    ".Lat_tail:\n"
    "v_mfma_f32_16x16x32_bf16 v[216:219], a[160:163], a[92:95], 0\n"
    "s_mov_b32 s45, 5775360\n"
    "v_mbcnt_lo_u32_b32 v253, -1, 0\n"
    "v_mfma_f32_16x16x32_bf16 v[216:219], a[164:167], a[64:67], v[216:219]\n"
    "s_add_u32 m0, s43, 49152\n"
    "v_mbcnt_hi_u32_b32 v253, -1, v253\n"
    "v_mfma_f32_16x16x32_bf16 v[224:227], a[164:167], a[96:99], 0\n"
    "buffer_load_dwordx4 v248, s[8:11], s40 offen offset:0 sc1 lds\n"
    "v_mfma_f32_16x16x32_bf16 v[224:227], a[160:163], a[124:127], v[224:227]\n"
    "s_lshl_b32 s56, %2, 15\n"
    "v_and_b32_e32 v254, 3, v253\n"
    "v_mfma_f32_16x16x32_bf16 v[232:235], a[160:163], a[156:159], 0\n"
    "ds_read_b128 a[160:163], v243 offset:22528\n"
    "v_mfma_f32_16x16x32_bf16 v[232:235], a[164:167], a[128:131], v[232:235]\n"
    "ds_read_b128 a[164:167], v240 offset:18432\n"
    "v_mfma_f32_16x16x32_bf16 v[232:235], a[168:171], a[132:135], v[232:235]\n"
    "s_waitcnt lgkmcnt(0)\n"
    "s_barrier\n"
    "v_mfma_f32_16x16x32_bf16 v[232:235], a[172:175], a[136:139], v[232:235]\n"
    "buffer_load_dwordx4 v248, s[8:11], s40 offen offset:1024 sc1 lds\n"
    "v_mfma_f32_16x16x32_bf16 v[224:227], a[172:175], a[104:107], v[224:227]\n"
    "s_add_u32 s57, s56, 5505024\n"
    "v_lshlrev_b32_e32 v254, 2, v254\n"
    "v_mfma_f32_16x16x32_bf16 v[224:227], a[168:171], a[100:103], v[224:227]\n"
    "s_add_u32 s56, s56, 5373952\n"
    "ds_read2st64_b32 v[164:165], v250 offset0:0 offset1:1\n"
    "v_and_b32_e32 v158, 15, v253\n"
    "v_mfma_f32_16x16x32_bf16 v[216:219], a[168:171], a[68:71], v[216:219]\n"
    "ds_read_b128 a[168:171], v241 offset:18432\n"
    "v_mfma_f32_16x16x32_bf16 v[216:219], a[172:175], a[72:75], v[216:219]\n"
    "ds_read_b128 a[172:175], v240 offset:22528\n"
    "v_mfma_f32_16x16x32_bf16 v[216:219], a[176:179], a[76:79], v[216:219]\n"
    "buffer_load_dwordx4 v248, s[8:11], s40 offen offset:2048 sc1 lds\n"
    "ds_read2st64_b32 v[166:167], v250 offset0:2 offset1:3\n"
    "v_mfma_f32_16x16x32_bf16 v[216:219], a[180:183], a[80:83], v[216:219]\n"
    "s_lshl_b32 s47, %2, 7\n"
    "v_lshlrev_b32_e32 v158, 9, v158\n"
    "v_mfma_f32_16x16x32_bf16 v[224:227], a[180:183], a[112:115], v[224:227]\n"
    "s_add_u32 s47, s47, s35\n"
    "v_and_or_b32 v158, v253, 48, v158\n"
    "v_mfma_f32_16x16x32_bf16 v[224:227], a[176:179], a[108:111], v[224:227]\n"
    "ds_read2st64_b32 v[172:173], v250 offset0:16 offset1:17\n"
    "v_bfe_u32 v252, v253, 4, 2\n"
    "v_mfma_f32_16x16x32_bf16 v[232:235], a[176:179], a[140:143], v[232:235]\n"
    "ds_read_b128 a[176:179], v241 offset:22528\n"
    "buffer_load_dwordx4 v248, s[8:11], s40 offen offset:3072 sc1 lds\n"
    "v_mfma_f32_16x16x32_bf16 v[232:235], a[180:183], a[144:147], v[232:235]\n"
    "s_add_u32 m0, s43, 53248\n"
    "ds_read_b128 a[180:183], v242 offset:18432\n"
    "v_mfma_f32_16x16x32_bf16 v[232:235], a[184:187], a[148:151], v[232:235]\n"
    "v_lshlrev_b32_e32 v252, 1, v252\n"
    "v_mfma_f32_16x16x32_bf16 v[232:235], a[188:191], a[152:155], v[232:235]\n"
    "ds_read2st64_b32 v[174:175], v250 offset0:18 offset1:19\n"
    "v_xor_b32_e32 v252, v252, v253\n"
    "v_mfma_f32_16x16x32_bf16 v[224:227], a[188:191], a[120:123], v[224:227]\n"
    "buffer_load_dwordx4 v248, s[8:11], s41 offen offset:0 sc1 lds\n"
    "v_mfma_f32_16x16x32_bf16 v[224:227], a[184:187], a[116:119], v[224:227]\n"
    "v_and_b32_e32 v252, 7, v252\n"
    "v_mfma_f32_16x16x32_bf16 v[216:219], a[184:187], a[84:87], v[216:219]\n"
    "ds_read_b128 a[184:187], v243 offset:18432\n"
    "v_mfma_f32_16x16x32_bf16 v[216:219], a[188:191], a[88:91], v[216:219]\n"
    "ds_read_b128 a[188:191], v242 offset:22528\n"
    "v_mfma_f32_16x16x32_bf16 v[220:223], a[160:163], a[92:95], 0\n"
    "buffer_load_dwordx4 v248, s[8:11], s41 offen offset:1024 sc1 lds\n"
    "ds_read2st64_b32 v[180:181], v250 offset0:32 offset1:33\n"
    "v_mfma_f32_16x16x32_bf16 v[220:223], a[164:167], a[64:67], v[220:223]\n"
    "v_lshlrev_b32_e32 v252, 4, v252\n"
    "v_mfma_f32_16x16x32_bf16 v[228:231], a[164:167], a[96:99], 0\n"
    "ds_read2st64_b32 v[182:183], v250 offset0:34 offset1:35\n"
    "v_lshrrev_b32_e32 v255, 3, v253\n"
    "v_mfma_f32_16x16x32_bf16 v[228:231], a[160:163], a[124:127], v[228:231]\n"
    "v_lshl_add_u32 v252, v255, 9, v252\n"
    "v_mfma_f32_16x16x32_bf16 v[236:239], a[160:163], a[156:159], 0\n"
    "ds_read_b64_tr_b16 a[160:161], v244 offset:0\n"
    "buffer_load_dwordx4 v248, s[8:11], s41 offen offset:2048 sc1 lds\n"
    "v_mfma_f32_16x16x32_bf16 v[236:239], a[164:167], a[128:131], v[236:239]\n"
    "s_waitcnt lgkmcnt(10)\n"
    "ds_read_b64_tr_b16 a[162:163], v244 offset:2048\n"
    "v_mfma_f32_16x16x32_bf16 v[236:239], a[168:171], a[132:135], v[236:239]\n"
    "ds_read_b64_tr_b16 a[164:165], v244 offset:8192\n"
    "v_mfma_f32_16x16x32_bf16 v[236:239], a[172:175], a[136:139], v[236:239]\n"
    "ds_read_b64_tr_b16 a[166:167], v244 offset:10240\n"
    "v_mfma_f32_16x16x32_bf16 v[228:231], a[172:175], a[104:107], v[228:231]\n"
    "buffer_load_dwordx4 v248, s[8:11], s41 offen offset:3072 sc1 lds\n"
    "ds_read2st64_b32 v[188:189], v250 offset0:48 offset1:49\n"
    "v_mfma_f32_16x16x32_bf16 v[228:231], a[168:171], a[100:103], v[228:231]\n"
    "s_mov_b32 m0, s49\n"
    "ds_read2st64_b32 v[190:191], v250 offset0:50 offset1:51\n"
    "v_lshlrev_b32_e32 v253, 4, v253\n"
    "v_mfma_f32_16x16x32_bf16 v[220:223], a[168:171], a[68:71], v[220:223]\n"
    "ds_read_b64_tr_b16 a[168:169], v245 offset:0\n"
    "v_mfma_f32_16x16x32_bf16 v[220:223], a[172:175], a[72:75], v[220:223]\n"
    "s_waitcnt lgkmcnt(12)\n"
    "ds_read_b64_tr_b16 a[170:171], v245 offset:2048\n"
    "v_mfma_f32_16x16x32_bf16 v[220:223], a[176:179], a[76:79], v[220:223]\n"
    "ds_read_b64_tr_b16 a[172:173], v245 offset:8192\n"
    "buffer_load_dword v255, v254, s[8:11], s45 offen offset:0 sc1\n"
    "v_mfma_f32_16x16x32_bf16 v[220:223], a[180:183], a[80:83], v[220:223]\n"
    "s_add_u32 s61, s47, 0\n"
    "ds_read_b64_tr_b16 a[174:175], v245 offset:10240\n"
    "v_mfma_f32_16x16x32_bf16 v[228:231], a[180:183], a[112:115], v[228:231]\n"
    "ds_read2st64_b32 v[196:197], v250 offset0:64 offset1:65\n"
    "v_mfma_f32_16x16x32_bf16 v[228:231], a[176:179], a[108:111], v[228:231]\n"
    "ds_read2st64_b32 v[198:199], v250 offset0:66 offset1:67\n"
    "v_mfma_f32_16x16x32_bf16 v[236:239], a[176:179], a[140:143], v[236:239]\n"
    "ds_read_b64_tr_b16 a[176:177], v246 offset:0\n"
    "v_mfma_f32_16x16x32_bf16 v[236:239], a[180:183], a[144:147], v[236:239]\n"
    "s_waitcnt lgkmcnt(15)\n"
    "ds_read_b64_tr_b16 a[178:179], v246 offset:2048\n"
    "v_mfma_f32_16x16x32_bf16 v[236:239], a[184:187], a[148:151], v[236:239]\n"
    "ds_read_b64_tr_b16 a[180:181], v246 offset:8192\n"
    "buffer_load_dwordx4 a[224:227], v158, s[12:15], s61 offen offset:0\n"
    "v_mfma_f32_16x16x32_bf16 v[236:239], a[188:191], a[152:155], v[236:239]\n"
    "ds_read_b64_tr_b16 a[182:183], v246 offset:10240\n"
    "v_mfma_f32_16x16x32_bf16 v[228:231], a[188:191], a[120:123], v[228:231]\n"
    "v_exp_f32_e32 v232, v232\n"
    "v_mfma_f32_16x16x32_bf16 v[228:231], a[184:187], a[116:119], v[228:231]\n"
    "v_exp_f32_e32 v233, v233\n"
    "v_mfma_f32_16x16x32_bf16 v[220:223], a[184:187], a[84:87], v[220:223]\n"
    "ds_read_b64_tr_b16 a[184:185], v247 offset:0\n"
    "v_mfma_f32_16x16x32_bf16 v[220:223], a[188:191], a[88:91], v[220:223]\n"
    "s_waitcnt lgkmcnt(5)\n"
    "ds_read_b64_tr_b16 a[186:187], v247 offset:2048\n"
    "v_mfma_f32_16x16x32_bf16 v[56:59], a[160:163], v[176:179], v[56:59]\n"
    "ds_read_b64_tr_b16 a[188:189], v247 offset:8192\n"
    "v_mfma_f32_16x16x32_bf16 v[56:59], a[164:167], v[180:183], v[56:59]\n"
    "ds_read_b64_tr_b16 a[190:191], v247 offset:10240\n"
    "v_mfma_f32_16x16x32_bf16 v[60:63], a[164:167], v[188:191], v[60:63]\n"
    "v_exp_f32_e32 v234, v234\n"
    "v_mfma_f32_16x16x32_bf16 v[60:63], a[160:163], v[184:187], v[60:63]\n"
    "v_exp_f32_e32 v235, v235\n"
    "v_mfma_f32_16x16x32_bf16 v[64:67], a[160:163], v[192:195], v[64:67]\n"
    "ds_read_b64_tr_b16 a[160:161], v244 offset:4096\n"
    "v_cvt_pk_bf16_f32 v232, v232, v233\n"
    "v_mfma_f32_16x16x32_bf16 v[64:67], a[164:167], v[196:199], v[64:67]\n"
    "ds_read_b64_tr_b16 a[162:163], v244 offset:6144\n"
    "v_exp_f32_e32 v224, v224\n"
    "v_mfma_f32_16x16x32_bf16 v[76:79], a[168:171], v[192:195], v[76:79]\n"
    "ds_read_b64_tr_b16 a[164:165], v244 offset:12288\n"
    "buffer_load_dwordx4 a[228:231], v158, s[12:15], s61 offen offset:64\n"
    "v_mfma_f32_16x16x32_bf16 v[76:79], a[172:175], v[196:199], v[76:79]\n"
    "s_add_u32 s61, s47, 8192\n"
    "ds_read_b64_tr_b16 a[166:167], v244 offset:14336\n"
    "v_mfma_f32_16x16x32_bf16 v[72:75], a[172:175], v[188:191], v[72:75]\n"
    "s_waitcnt vmcnt(11)\n"
    "s_barrier\n"
    "v_mfma_f32_16x16x32_bf16 v[72:75], a[168:171], v[184:187], v[72:75]\n"
    "v_exp_f32_e32 v225, v225\n"
    "v_mfma_f32_16x16x32_bf16 v[68:71], a[168:171], v[176:179], v[68:71]\n"
    "ds_read_b64_tr_b16 a[168:169], v245 offset:4096\n"
    "v_cvt_pk_bf16_f32 v233, v234, v235\n"
    "v_mfma_f32_16x16x32_bf16 v[68:71], a[172:175], v[180:183], v[68:71]\n"
    "s_waitcnt lgkmcnt(9)\n"
    "ds_read_b64_tr_b16 a[170:171], v245 offset:6144\n"
    "v_mfma_f32_16x16x32_bf16 v[80:83], a[176:179], v[176:179], v[80:83]\n"
    "ds_read_b64_tr_b16 a[172:173], v245 offset:12288\n"
    "v_mfma_f32_16x16x32_bf16 v[80:83], a[180:183], v[180:183], v[80:83]\n"
    "ds_read_b64_tr_b16 a[174:175], v245 offset:14336\n"
    "v_mfma_f32_16x16x32_bf16 v[84:87], a[180:183], v[188:191], v[84:87]\n"
    "v_exp_f32_e32 v226, v226\n"
    "v_mfma_f32_16x16x32_bf16 v[84:87], a[176:179], v[184:187], v[84:87]\n"
    "v_exp_f32_e32 v227, v227\n"
    "v_mfma_f32_16x16x32_bf16 v[88:91], a[176:179], v[192:195], v[88:91]\n"
    "ds_read_b64_tr_b16 a[176:177], v246 offset:4096\n"
    "v_cvt_pk_bf16_f32 v224, v224, v225\n"
    "v_mfma_f32_16x16x32_bf16 v[88:91], a[180:183], v[196:199], v[88:91]\n"
    "s_waitcnt lgkmcnt(9)\n"
    "ds_read_b64_tr_b16 a[178:179], v246 offset:6144\n"
    "v_mfma_f32_16x16x32_bf16 v[100:103], a[184:187], v[192:195], v[100:103]\n"
    "ds_read_b64_tr_b16 a[180:181], v246 offset:12288\n"
    "buffer_load_dwordx4 a[232:235], v158, s[12:15], s61 offen offset:0\n"
    "v_mfma_f32_16x16x32_bf16 v[100:103], a[188:191], v[196:199], v[100:103]\n"
    "ds_read_b64_tr_b16 a[182:183], v246 offset:14336\n"
    "v_mfma_f32_16x16x32_bf16 v[96:99], a[188:191], v[188:191], v[96:99]\n"
    "v_exp_f32_e32 v216, v216\n"
    "v_mfma_f32_16x16x32_bf16 v[96:99], a[184:187], v[184:187], v[96:99]\n"
    "v_exp_f32_e32 v217, v217\n"
    "v_mfma_f32_16x16x32_bf16 v[92:95], a[184:187], v[176:179], v[92:95]\n"
    "ds_read_b64_tr_b16 a[184:185], v247 offset:4096\n"
    "v_cvt_pk_bf16_f32 v225, v226, v227\n"
    "v_mfma_f32_16x16x32_bf16 v[92:95], a[188:191], v[180:183], v[92:95]\n"
    "s_waitcnt lgkmcnt(9)\n"
    "ds_read_b64_tr_b16 a[186:187], v247 offset:6144\n"
    "ds_write_addtid_b32 v232 offset:16384\n"
    "v_mfma_f32_16x16x32_bf16 v[104:107], a[160:163], v[176:179], v[104:107]\n"
    "ds_read_b64_tr_b16 a[188:189], v247 offset:12288\n"
    "ds_write_addtid_b32 v233 offset:16640\n"
    "v_mfma_f32_16x16x32_bf16 v[104:107], a[164:167], v[180:183], v[104:107]\n"
    "ds_read_b64_tr_b16 a[190:191], v247 offset:14336\n"
    "ds_write_addtid_b32 v224 offset:12288\n"
    "v_mfma_f32_16x16x32_bf16 v[108:111], a[164:167], v[188:191], v[108:111]\n"
    "v_exp_f32_e32 v218, v218\n"
    "v_mfma_f32_16x16x32_bf16 v[108:111], a[160:163], v[184:187], v[108:111]\n"
    "v_exp_f32_e32 v219, v219\n"
    "v_mfma_f32_16x16x32_bf16 v[112:115], a[160:163], v[192:195], v[112:115]\n"
    "ds_read_b128 a[160:163], v243 offset:36864\n"
    "v_cvt_pk_bf16_f32 v216, v216, v217\n"
    "v_mfma_f32_16x16x32_bf16 v[112:115], a[164:167], v[196:199], v[112:115]\n"
    "s_waitcnt lgkmcnt(12)\n"
    "ds_read_b128 a[164:167], v240 offset:32768\n"
    "v_cvt_pk_bf16_f32 v217, v218, v219\n"
    "v_mfma_f32_16x16x32_bf16 v[124:127], a[168:171], v[192:195], v[124:127]\n"
    "buffer_load_dwordx4 a[236:239], v158, s[12:15], s61 offen offset:64\n"
    "v_exp_f32_e32 v236, v236\n"
    "v_mfma_f32_16x16x32_bf16 v[124:127], a[172:175], v[196:199], v[124:127]\n"
    "s_add_u32 s61, s47, 16384\n"
    "ds_write_addtid_b32 v225 offset:12544\n"
    "v_exp_f32_e32 v237, v237\n"
    "v_mfma_f32_16x16x32_bf16 v[120:123], a[172:175], v[188:191], v[120:123]\n"
    "v_exp_f32_e32 v238, v238\n"
    "v_mfma_f32_16x16x32_bf16 v[120:123], a[168:171], v[184:187], v[120:123]\n"
    "v_exp_f32_e32 v239, v239\n"
    "v_mfma_f32_16x16x32_bf16 v[116:119], a[168:171], v[176:179], v[116:119]\n"
    "ds_read_b128 a[168:171], v241 offset:32768\n"
    "v_cvt_pk_bf16_f32 v234, v236, v237\n"
    "v_mfma_f32_16x16x32_bf16 v[116:119], a[172:175], v[180:183], v[116:119]\n"
    "s_waitcnt lgkmcnt(11)\n"
    "ds_read_b128 a[172:175], v240 offset:36864\n"
    "v_exp_f32_e32 v228, v228\n"
    "v_mfma_f32_16x16x32_bf16 v[128:131], a[176:179], v[176:179], v[128:131]\n"
    "v_exp_f32_e32 v229, v229\n"
    "v_mfma_f32_16x16x32_bf16 v[128:131], a[180:183], v[180:183], v[128:131]\n"
    "ds_write_addtid_b32 v216 offset:8192\n"
    "v_cvt_pk_bf16_f32 v235, v238, v239\n"
    "v_mfma_f32_16x16x32_bf16 v[132:135], a[180:183], v[188:191], v[132:135]\n"
    "v_exp_f32_e32 v230, v230\n"
    "v_mfma_f32_16x16x32_bf16 v[132:135], a[176:179], v[184:187], v[132:135]\n"
    "v_exp_f32_e32 v231, v231\n"
    "v_mfma_f32_16x16x32_bf16 v[136:139], a[176:179], v[192:195], v[136:139]\n"
    "ds_read_b128 a[176:179], v241 offset:36864\n"
    "v_cvt_pk_bf16_f32 v226, v228, v229\n"
    "v_mfma_f32_16x16x32_bf16 v[136:139], a[180:183], v[196:199], v[136:139]\n"
    "s_waitcnt lgkmcnt(8)\n"
    "ds_read_b128 a[180:183], v242 offset:32768\n"
    "v_exp_f32_e32 v220, v220\n"
    "v_mfma_f32_16x16x32_bf16 v[148:151], a[184:187], v[192:195], v[148:151]\n"
    "buffer_load_dwordx4 a[240:243], v158, s[12:15], s61 offen offset:0\n"
    "v_exp_f32_e32 v221, v221\n"
    "v_mfma_f32_16x16x32_bf16 v[148:151], a[188:191], v[196:199], v[148:151]\n"
    "ds_write_addtid_b32 v217 offset:8448\n"
    "v_cvt_pk_bf16_f32 v227, v230, v231\n"
    "v_mfma_f32_16x16x32_bf16 v[144:147], a[188:191], v[188:191], v[144:147]\n"
    "v_exp_f32_e32 v222, v222\n"
    "v_mfma_f32_16x16x32_bf16 v[144:147], a[184:187], v[184:187], v[144:147]\n"
    "v_exp_f32_e32 v223, v223\n"
    "v_mfma_f32_16x16x32_bf16 v[140:143], a[184:187], v[176:179], v[140:143]\n"
    "ds_read_b128 a[184:187], v243 offset:32768\n"
    "v_cvt_pk_bf16_f32 v218, v220, v221\n"
    "v_mfma_f32_16x16x32_bf16 v[140:143], a[188:191], v[180:183], v[140:143]\n"
    "s_waitcnt lgkmcnt(8)\n"
    "ds_read_b128 a[188:191], v242 offset:36864\n"
    "v_cvt_pk_bf16_f32 v219, v222, v223\n"
    "v_mfma_f32_16x16x32_bf16 v[176:179], a[160:163], a[92:95], 0\n"
    "s_waitcnt vmcnt(5)\n"
    "ds_write_addtid_b32 v234 offset:16896\n"
    "v_cmp_ne_u32_e32 vcc, s39, v255\n"
    "v_mfma_f32_16x16x32_bf16 v[176:179], a[164:167], a[64:67], v[176:179]\n"
    "ds_write_addtid_b32 v235 offset:17152\n"
    "ds_write_addtid_b32 v226 offset:12800\n"
    "v_mfma_f32_16x16x32_bf16 v[184:187], a[164:167], a[96:99], 0\n"
    "s_cbranch_vccnz .Lat_wpoll\n"
    ".Lat_wpoll_ret:\n"
    "v_mfma_f32_16x16x32_bf16 v[184:187], a[160:163], a[124:127], v[184:187]\n"
    "ds_write_addtid_b32 v227 offset:13056\n"
    "ds_write_addtid_b32 v218 offset:8704\n"
    "v_mfma_f32_16x16x32_bf16 v[192:195], a[160:163], a[156:159], 0\n"
    "ds_read_b128 a[160:163], v243 offset:38912\n"
    "ds_write_addtid_b32 v219 offset:8960\n"
    "v_mfma_f32_16x16x32_bf16 v[192:195], a[164:167], a[128:131], v[192:195]\n"
    "s_waitcnt lgkmcnt(13)\n"
    "ds_read_b128 a[164:167], v240 offset:34816\n"
    "v_mfma_f32_16x16x32_bf16 v[192:195], a[168:171], a[132:135], v[192:195]\n"
    "buffer_load_dwordx4 a[244:247], v158, s[12:15], s61 offen offset:64\n"
    "v_mfma_f32_16x16x32_bf16 v[192:195], a[172:175], a[136:139], v[192:195]\n"
    "v_mfma_f32_16x16x32_bf16 v[184:187], a[172:175], a[104:107], v[184:187]\n"
    "s_waitcnt lgkmcnt(1)\n"
    "s_barrier\n"
    "v_mfma_f32_16x16x32_bf16 v[184:187], a[168:171], a[100:103], v[184:187]\n"
    "v_mfma_f32_16x16x32_bf16 v[176:179], a[168:171], a[68:71], v[176:179]\n"
    "ds_read_b128 a[168:171], v241 offset:34816\n"
    "v_mfma_f32_16x16x32_bf16 v[176:179], a[172:175], a[72:75], v[176:179]\n"
    "ds_read_b128 a[172:175], v240 offset:38912\n"
    "v_mfma_f32_16x16x32_bf16 v[176:179], a[176:179], a[76:79], v[176:179]\n"
    "ds_read2st64_b32 v[220:221], v250 offset0:32 offset1:33\n"
    "v_mfma_f32_16x16x32_bf16 v[176:179], a[180:183], a[80:83], v[176:179]\n"
    "v_mfma_f32_16x16x32_bf16 v[184:187], a[180:183], a[112:115], v[184:187]\n"
    "s_add_u32 s61, s47, 5791744\n"
    "v_mfma_f32_16x16x32_bf16 v[184:187], a[176:179], a[108:111], v[184:187]\n"
    "s_add_u32 m0, s49, 20480\n"
    "ds_read2st64_b32 v[222:223], v250 offset0:34 offset1:35\n"
    "v_mfma_f32_16x16x32_bf16 v[192:195], a[176:179], a[140:143], v[192:195]\n"
    "ds_read_b128 a[176:179], v241 offset:38912\n"
    "v_mfma_f32_16x16x32_bf16 v[192:195], a[180:183], a[144:147], v[192:195]\n"
    "ds_read_b128 a[180:183], v242 offset:34816\n"
    "v_mfma_f32_16x16x32_bf16 v[192:195], a[184:187], a[148:151], v[192:195]\n"
    "buffer_load_dwordx4 v252, s[8:11], s61 offen offset:0 sc1 lds\n"
    "v_mfma_f32_16x16x32_bf16 v[192:195], a[188:191], a[152:155], v[192:195]\n"
    "s_add_u32 s61, s47, 5795840\n"
    "v_mfma_f32_16x16x32_bf16 v[184:187], a[188:191], a[120:123], v[184:187]\n"
    "s_add_u32 m0, s49, 24576\n"
    "v_mfma_f32_16x16x32_bf16 v[184:187], a[184:187], a[116:119], v[184:187]\n"
    "ds_read2st64_b32 v[228:229], v250 offset0:48 offset1:49\n"
    "v_exp_f32_e32 v192, v192\n"
    "v_mfma_f32_16x16x32_bf16 v[176:179], a[184:187], a[84:87], v[176:179]\n"
    "ds_read_b128 a[184:187], v243 offset:34816\n"
    "buffer_load_dwordx4 v252, s[8:11], s61 offen offset:0 sc1 lds\n"
    "v_mfma_f32_16x16x32_bf16 v[176:179], a[188:191], a[88:91], v[176:179]\n"
    "s_waitcnt lgkmcnt(8)\n"
    "s_add_u32 s61, s47, 5799936\n"
    "ds_read_b128 a[188:191], v242 offset:38912\n"
    "v_mfma_f32_16x16x32_bf16 v[180:183], a[160:163], a[92:95], 0\n"
    "s_add_u32 m0, s49, 28672\n"
    "v_exp_f32_e32 v193, v193\n"
    "v_mfma_f32_16x16x32_bf16 v[180:183], a[164:167], a[64:67], v[180:183]\n"
    "v_exp_f32_e32 v194, v194\n"
    "v_mfma_f32_16x16x32_bf16 v[188:191], a[164:167], a[96:99], 0\n"
    "buffer_load_dwordx4 v252, s[8:11], s61 offen offset:0 sc1 lds\n"
    "v_mfma_f32_16x16x32_bf16 v[188:191], a[160:163], a[124:127], v[188:191]\n"
    "s_mov_b32 m0, s49\n"
    "ds_read2st64_b32 v[230:231], v250 offset0:50 offset1:51\n"
    "v_mfma_f32_16x16x32_bf16 v[196:199], a[160:163], a[156:159], 0\n"
    "s_add_u32 s48, s57, 30720\n"
    "ds_read_b64_tr_b16 a[160:161], v244 offset:16384\n"
    "v_mfma_f32_16x16x32_bf16 v[196:199], a[164:167], a[128:131], v[196:199]\n"
    "s_waitcnt lgkmcnt(9)\n"
    "ds_read_b64_tr_b16 a[162:163], v244 offset:18432\n"
    "v_mfma_f32_16x16x32_bf16 v[196:199], a[168:171], a[132:135], v[196:199]\n"
    "ds_read_b64_tr_b16 a[164:165], v244 offset:24576\n"
    "buffer_load_dwordx4 a[192:195], v253, s[8:11], s48 offen offset:0 sc1\n"
    "v_mfma_f32_16x16x32_bf16 v[196:199], a[172:175], a[136:139], v[196:199]\n"
    "ds_read_b64_tr_b16 a[166:167], v244 offset:26624\n"
    "v_mfma_f32_16x16x32_bf16 v[188:191], a[172:175], a[104:107], v[188:191]\n"
    "ds_read2st64_b32 v[236:237], v250 offset0:64 offset1:65\n"
    "v_mfma_f32_16x16x32_bf16 v[188:191], a[168:171], a[100:103], v[188:191]\n"
    "v_exp_f32_e32 v195, v195\n"
    "v_mfma_f32_16x16x32_bf16 v[180:183], a[168:171], a[68:71], v[180:183]\n"
    "ds_read_b64_tr_b16 a[168:169], v245 offset:16384\n"
    "buffer_load_dwordx4 a[196:199], v253, s[8:11], s48 offen offset:1024 sc1\n"
    "v_mfma_f32_16x16x32_bf16 v[180:183], a[172:175], a[72:75], v[180:183]\n"
    "s_waitcnt lgkmcnt(10)\n"
    "s_add_u32 s48, s57, 14336\n"
    "ds_read_b64_tr_b16 a[170:171], v245 offset:18432\n"
    "v_mfma_f32_16x16x32_bf16 v[180:183], a[176:179], a[76:79], v[180:183]\n"
    "ds_read_b64_tr_b16 a[172:173], v245 offset:24576\n"
    "v_cvt_pk_bf16_f32 v192, v192, v193\n"
    "v_mfma_f32_16x16x32_bf16 v[180:183], a[180:183], a[80:83], v[180:183]\n"
    "ds_read_b64_tr_b16 a[174:175], v245 offset:26624\n"
    "v_mfma_f32_16x16x32_bf16 v[188:191], a[180:183], a[112:115], v[188:191]\n"
    "buffer_load_dwordx4 a[200:203], v253, s[8:11], s48 offen offset:0 sc1\n"
    "v_mfma_f32_16x16x32_bf16 v[188:191], a[176:179], a[108:111], v[188:191]\n"
    "ds_read2st64_b32 v[238:239], v250 offset0:66 offset1:67\n"
    "v_mfma_f32_16x16x32_bf16 v[196:199], a[176:179], a[140:143], v[196:199]\n"
    "ds_read_b64_tr_b16 a[176:177], v246 offset:16384\n"
    "v_mfma_f32_16x16x32_bf16 v[196:199], a[180:183], a[144:147], v[196:199]\n"
    "s_waitcnt lgkmcnt(12)\n"
    "ds_read_b64_tr_b16 a[178:179], v246 offset:18432\n"
    "v_mfma_f32_16x16x32_bf16 v[196:199], a[184:187], a[148:151], v[196:199]\n"
    "ds_read_b64_tr_b16 a[180:181], v246 offset:24576\n"
    "buffer_load_dwordx4 a[204:207], v253, s[8:11], s48 offen offset:1024 sc1\n"
    "v_mfma_f32_16x16x32_bf16 v[196:199], a[188:191], a[152:155], v[196:199]\n"
    "s_add_u32 s48, s57, 16384\n"
    "ds_read_b64_tr_b16 a[182:183], v246 offset:26624\n"
    "v_mfma_f32_16x16x32_bf16 v[188:191], a[188:191], a[120:123], v[188:191]\n"
    "v_exp_f32_e32 v184, v184\n"
    "v_mfma_f32_16x16x32_bf16 v[188:191], a[184:187], a[116:119], v[188:191]\n"
    "v_exp_f32_e32 v185, v185\n"
    "v_mfma_f32_16x16x32_bf16 v[180:183], a[184:187], a[84:87], v[180:183]\n"
    "ds_read_b64_tr_b16 a[184:185], v247 offset:16384\n"
    "buffer_load_dwordx4 a[208:211], v253, s[8:11], s48 offen offset:0 sc1\n"
    "v_mfma_f32_16x16x32_bf16 v[180:183], a[188:191], a[88:91], v[180:183]\n"
    "s_waitcnt lgkmcnt(5)\n"
    "ds_read_b64_tr_b16 a[186:187], v247 offset:18432\n"
    "v_cvt_pk_bf16_f32 v193, v194, v195\n"
    "v_mfma_f32_16x16x32_bf16 v[56:59], a[160:163], v[216:219], v[56:59]\n"
    "ds_read_b64_tr_b16 a[188:189], v247 offset:24576\n"
    "v_exp_f32_e32 v186, v186\n"
    "v_mfma_f32_16x16x32_bf16 v[56:59], a[164:167], v[220:223], v[56:59]\n"
    "ds_read_b64_tr_b16 a[190:191], v247 offset:26624\n"
    "v_mfma_f32_16x16x32_bf16 v[60:63], a[164:167], v[228:231], v[60:63]\n"
    "buffer_load_dwordx4 a[212:215], v253, s[8:11], s48 offen offset:1024 sc1\n"
    "v_mfma_f32_16x16x32_bf16 v[60:63], a[160:163], v[224:227], v[60:63]\n"
    "s_add_u32 s48, s57, 0\n"
    "v_exp_f32_e32 v187, v187\n"
    "v_mfma_f32_16x16x32_bf16 v[64:67], a[160:163], v[232:235], v[64:67]\n"
    "ds_read_b64_tr_b16 a[160:161], v244 offset:20480\n"
    "v_cvt_pk_bf16_f32 v184, v184, v185\n"
    "v_mfma_f32_16x16x32_bf16 v[64:67], a[164:167], v[236:239], v[64:67]\n"
    "ds_read_b64_tr_b16 a[162:163], v244 offset:22528\n"
    "v_mfma_f32_16x16x32_bf16 v[76:79], a[168:171], v[232:235], v[76:79]\n"
    "ds_read_b64_tr_b16 a[164:165], v244 offset:28672\n"
    "buffer_load_dwordx4 a[216:219], v253, s[8:11], s48 offen offset:0 sc1\n"
    "v_mfma_f32_16x16x32_bf16 v[76:79], a[172:175], v[236:239], v[76:79]\n"
    "ds_read_b64_tr_b16 a[166:167], v244 offset:30720\n"
    "v_mfma_f32_16x16x32_bf16 v[72:75], a[172:175], v[228:231], v[72:75]\n"
    "v_exp_f32_e32 v176, v176\n"
    "v_mfma_f32_16x16x32_bf16 v[72:75], a[168:171], v[224:227], v[72:75]\n"
    "v_exp_f32_e32 v177, v177\n"
    "v_mfma_f32_16x16x32_bf16 v[68:71], a[168:171], v[216:219], v[68:71]\n"
    "ds_read_b64_tr_b16 a[168:169], v245 offset:20480\n"
    "buffer_load_dwordx4 a[220:223], v253, s[8:11], s48 offen offset:1024 sc1\n"
    "v_mfma_f32_16x16x32_bf16 v[68:71], a[172:175], v[220:223], v[68:71]\n"
    "s_waitcnt lgkmcnt(9)\n"
    "ds_read_b64_tr_b16 a[170:171], v245 offset:22528\n"
    "v_cvt_pk_bf16_f32 v185, v186, v187\n"
    "v_mfma_f32_16x16x32_bf16 v[80:83], a[176:179], v[216:219], v[80:83]\n"
    "ds_read_b64_tr_b16 a[172:173], v245 offset:28672\n"
    "v_mfma_f32_16x16x32_bf16 v[80:83], a[180:183], v[220:223], v[80:83]\n"
    "s_add_u32 s61, s28, 24576\n"
    "ds_read_b64_tr_b16 a[174:175], v245 offset:30720\n"
    "v_mfma_f32_16x16x32_bf16 v[84:87], a[180:183], v[228:231], v[84:87]\n"
    "v_and_b32_e32 v254, 0x80, v253\n"
    "v_mfma_f32_16x16x32_bf16 v[84:87], a[176:179], v[224:227], v[84:87]\n"
    "v_exp_f32_e32 v178, v178\n"
    "v_mfma_f32_16x16x32_bf16 v[88:91], a[176:179], v[232:235], v[88:91]\n"
    "s_waitcnt vmcnt(18)\n"
    "s_barrier\n"
    "v_mfma_f32_16x16x32_bf16 v[88:91], a[180:183], v[236:239], v[88:91]\n"
    "s_waitcnt lgkmcnt(8)\n"
    "ds_read_b64_tr_b16 a[176:177], v246 offset:20480\n"
    "buffer_load_dword v248, v254, s[8:11], s61 offen offset:0 sc1\n"
    "v_mfma_f32_16x16x32_bf16 v[100:103], a[184:187], v[232:235], v[100:103]\n"
    "s_add_u32 s61, s29, 24576\n"
    "ds_read_b64_tr_b16 a[178:179], v246 offset:22528\n"
    "ds_write_addtid_b32 v192 offset:16384\n"
    "v_mfma_f32_16x16x32_bf16 v[100:103], a[188:191], v[236:239], v[100:103]\n"
    "ds_read_b64_tr_b16 a[180:181], v246 offset:28672\n"
    "ds_write_addtid_b32 v193 offset:16640\n"
    "v_mfma_f32_16x16x32_bf16 v[96:99], a[188:191], v[228:231], v[96:99]\n"
    "ds_read_b64_tr_b16 a[182:183], v246 offset:30720\n"
    "buffer_load_dword v248, v254, s[8:11], s61 offen offset:0 sc1\n"
    "v_mfma_f32_16x16x32_bf16 v[96:99], a[184:187], v[224:227], v[96:99]\n"
    "s_add_u32 s61, s30, 24576\n"
    "v_exp_f32_e32 v179, v179\n"
    "v_mfma_f32_16x16x32_bf16 v[92:95], a[184:187], v[216:219], v[92:95]\n"
    "ds_read_b64_tr_b16 a[184:185], v247 offset:20480\n"
    "v_cvt_pk_bf16_f32 v176, v176, v177\n"
    "v_mfma_f32_16x16x32_bf16 v[92:95], a[188:191], v[220:223], v[92:95]\n"
    "s_waitcnt lgkmcnt(11)\n"
    "ds_read_b64_tr_b16 a[186:187], v247 offset:22528\n"
    "v_cvt_pk_bf16_f32 v177, v178, v179\n"
    "v_mfma_f32_16x16x32_bf16 v[104:107], a[160:163], v[216:219], v[104:107]\n"
    "ds_read_b64_tr_b16 a[188:189], v247 offset:28672\n"
    "buffer_load_dword v248, v254, s[8:11], s61 offen offset:0 sc1\n"
    "v_mfma_f32_16x16x32_bf16 v[104:107], a[164:167], v[220:223], v[104:107]\n"
    "s_add_u32 s61, s31, 24576\n"
    "ds_read_b64_tr_b16 a[190:191], v247 offset:30720\n"
    "ds_write_addtid_b32 v184 offset:12288\n"
    "v_mfma_f32_16x16x32_bf16 v[108:111], a[164:167], v[228:231], v[108:111]\n"
    "v_exp_f32_e32 v196, v196\n"
    "v_mfma_f32_16x16x32_bf16 v[108:111], a[160:163], v[224:227], v[108:111]\n"
    "buffer_load_dword v248, v254, s[8:11], s61 offen offset:0 sc1\n"
    "ds_write_addtid_b32 v185 offset:12544\n"
    "v_mfma_f32_16x16x32_bf16 v[112:115], a[160:163], v[232:235], v[112:115]\n"
    "ds_read_b128 a[160:163], v243 offset:53248\n"
    "v_mfma_f32_16x16x32_bf16 v[112:115], a[164:167], v[236:239], v[112:115]\n"
    "s_waitcnt lgkmcnt(13)\n"
    "ds_read_b128 a[164:167], v240 offset:49152\n"
    "ds_write_addtid_b32 v176 offset:8192\n"
    "v_mfma_f32_16x16x32_bf16 v[124:127], a[168:171], v[232:235], v[124:127]\n"
    "buffer_load_dword v248, v254, s[8:11], s87 offen offset:0 sc1\n"
    "ds_write_addtid_b32 v177 offset:8448\n"
    "v_mfma_f32_16x16x32_bf16 v[124:127], a[172:175], v[236:239], v[124:127]\n"
    "v_exp_f32_e32 v197, v197\n"
    "v_mfma_f32_16x16x32_bf16 v[120:123], a[172:175], v[228:231], v[120:123]\n"
    "v_exp_f32_e32 v198, v198\n"
    "v_mfma_f32_16x16x32_bf16 v[120:123], a[168:171], v[224:227], v[120:123]\n"
    "v_exp_f32_e32 v199, v199\n"
    "v_mfma_f32_16x16x32_bf16 v[116:119], a[168:171], v[216:219], v[116:119]\n"
    "ds_read_b128 a[168:171], v241 offset:49152\n"
    "v_cvt_pk_bf16_f32 v194, v196, v197\n"
    "v_mfma_f32_16x16x32_bf16 v[116:119], a[172:175], v[220:223], v[116:119]\n"
    "s_waitcnt lgkmcnt(11)\n"
    "ds_read_b128 a[172:175], v240 offset:53248\n"
    "v_exp_f32_e32 v188, v188\n"
    "v_mfma_f32_16x16x32_bf16 v[128:131], a[176:179], v[216:219], v[128:131]\n"
    "v_exp_f32_e32 v189, v189\n"
    "v_mfma_f32_16x16x32_bf16 v[128:131], a[180:183], v[220:223], v[128:131]\n"
    "ds_write_addtid_b32 v194 offset:16896\n"
    "v_cvt_pk_bf16_f32 v195, v198, v199\n"
    "v_mfma_f32_16x16x32_bf16 v[132:135], a[180:183], v[228:231], v[132:135]\n"
    "v_exp_f32_e32 v190, v190\n"
    "v_mfma_f32_16x16x32_bf16 v[132:135], a[176:179], v[224:227], v[132:135]\n"
    "v_exp_f32_e32 v191, v191\n"
    "v_mfma_f32_16x16x32_bf16 v[136:139], a[176:179], v[232:235], v[136:139]\n"
    "ds_read_b128 a[176:179], v241 offset:53248\n"
    "v_cvt_pk_bf16_f32 v186, v188, v189\n"
    "v_mfma_f32_16x16x32_bf16 v[136:139], a[180:183], v[236:239], v[136:139]\n"
    "s_waitcnt lgkmcnt(10)\n"
    "ds_read_b128 a[180:183], v242 offset:49152\n"
    "v_exp_f32_e32 v180, v180\n"
    "v_mfma_f32_16x16x32_bf16 v[148:151], a[184:187], v[232:235], v[148:151]\n"
    "v_exp_f32_e32 v181, v181\n"
    "v_mfma_f32_16x16x32_bf16 v[148:151], a[188:191], v[236:239], v[148:151]\n"
    "ds_write_addtid_b32 v195 offset:17152\n"
    "v_cvt_pk_bf16_f32 v187, v190, v191\n"
    "v_mfma_f32_16x16x32_bf16 v[144:147], a[188:191], v[228:231], v[144:147]\n"
    "v_exp_f32_e32 v182, v182\n"
    "v_mfma_f32_16x16x32_bf16 v[144:147], a[184:187], v[224:227], v[144:147]\n"
    "v_exp_f32_e32 v183, v183\n"
    "v_mfma_f32_16x16x32_bf16 v[140:143], a[184:187], v[216:219], v[140:143]\n"
    "ds_read_b128 a[184:187], v243 offset:49152\n"
    "v_cvt_pk_bf16_f32 v178, v180, v181\n"
    "v_mfma_f32_16x16x32_bf16 v[140:143], a[188:191], v[220:223], v[140:143]\n"
    "s_waitcnt lgkmcnt(9)\n"
    "ds_read_b128 a[188:191], v242 offset:53248\n"
    "v_cvt_pk_bf16_f32 v179, v182, v183\n"
    "v_mfma_f32_16x16x32_bf16 v[216:219], a[160:163], a[92:95], 0\n"
    "ds_write_addtid_b32 v186 offset:12800\n"
    "v_mfma_f32_16x16x32_bf16 v[216:219], a[164:167], a[64:67], v[216:219]\n"
    "ds_write_addtid_b32 v187 offset:13056\n"
    "v_mfma_f32_16x16x32_bf16 v[224:227], a[164:167], a[96:99], 0\n"
    "ds_write_addtid_b32 v178 offset:8704\n"
    "v_mfma_f32_16x16x32_bf16 v[224:227], a[160:163], a[124:127], v[224:227]\n"
    "ds_write_addtid_b32 v179 offset:8960\n"
    "v_mfma_f32_16x16x32_bf16 v[232:235], a[160:163], a[156:159], 0\n"
    "ds_read_b128 a[160:163], v243 offset:55296\n"
    "v_mfma_f32_16x16x32_bf16 v[232:235], a[164:167], a[128:131], v[232:235]\n"
    "s_waitcnt lgkmcnt(11)\n"
    "ds_read_b128 a[164:167], v240 offset:51200\n"
    "v_mfma_f32_16x16x32_bf16 v[232:235], a[168:171], a[132:135], v[232:235]\n"
    "v_mfma_f32_16x16x32_bf16 v[232:235], a[172:175], a[136:139], v[232:235]\n"
    "v_mfma_f32_16x16x32_bf16 v[224:227], a[172:175], a[104:107], v[224:227]\n"
    "s_waitcnt lgkmcnt(2)\n"
    "s_barrier\n"
    "v_mfma_f32_16x16x32_bf16 v[224:227], a[168:171], a[100:103], v[224:227]\n"
    "ds_read2st64_b32 v[180:181], v250 offset0:32 offset1:33\n"
    "v_mfma_f32_16x16x32_bf16 v[216:219], a[168:171], a[68:71], v[216:219]\n"
    "ds_read_b128 a[168:171], v241 offset:51200\n"
    "v_mfma_f32_16x16x32_bf16 v[216:219], a[172:175], a[72:75], v[216:219]\n"
    "ds_read_b128 a[172:175], v240 offset:55296\n"
    "v_mfma_f32_16x16x32_bf16 v[216:219], a[176:179], a[76:79], v[216:219]\n"
    "v_mfma_f32_16x16x32_bf16 v[216:219], a[180:183], a[80:83], v[216:219]\n"
    "ds_read2st64_b32 v[182:183], v250 offset0:34 offset1:35\n"
    "v_mfma_f32_16x16x32_bf16 v[224:227], a[180:183], a[112:115], v[224:227]\n"
    "v_mfma_f32_16x16x32_bf16 v[224:227], a[176:179], a[108:111], v[224:227]\n"
    "ds_read2st64_b32 v[188:189], v250 offset0:48 offset1:49\n"
    "v_mfma_f32_16x16x32_bf16 v[232:235], a[176:179], a[140:143], v[232:235]\n"
    "ds_read_b128 a[176:179], v241 offset:55296\n"
    "v_mfma_f32_16x16x32_bf16 v[232:235], a[180:183], a[144:147], v[232:235]\n"
    "ds_read_b128 a[180:183], v242 offset:51200\n"
    "v_mfma_f32_16x16x32_bf16 v[232:235], a[184:187], a[148:151], v[232:235]\n"
    "ds_read2st64_b32 v[190:191], v250 offset0:50 offset1:51\n"
    "v_mfma_f32_16x16x32_bf16 v[232:235], a[188:191], a[152:155], v[232:235]\n"
    "v_mfma_f32_16x16x32_bf16 v[224:227], a[188:191], a[120:123], v[224:227]\n"
    "ds_read2st64_b32 v[196:197], v250 offset0:64 offset1:65\n"
    "v_mfma_f32_16x16x32_bf16 v[224:227], a[184:187], a[116:119], v[224:227]\n"
    "ds_read2st64_b32 v[198:199], v250 offset0:66 offset1:67\n"
    "v_mfma_f32_16x16x32_bf16 v[216:219], a[184:187], a[84:87], v[216:219]\n"
    "ds_read_b128 a[184:187], v243 offset:51200\n"
    "v_mfma_f32_16x16x32_bf16 v[216:219], a[188:191], a[88:91], v[216:219]\n"
    "s_waitcnt lgkmcnt(11)\n"
    "ds_read_b128 a[188:191], v242 offset:55296\n"
    "v_mfma_f32_16x16x32_bf16 v[220:223], a[160:163], a[92:95], 0\n"
    "v_exp_f32_e32 v232, v232\n"
    "v_mfma_f32_16x16x32_bf16 v[220:223], a[164:167], a[64:67], v[220:223]\n"
    "v_exp_f32_e32 v233, v233\n"
    "v_mfma_f32_16x16x32_bf16 v[228:231], a[164:167], a[96:99], 0\n"
    "v_exp_f32_e32 v234, v234\n"
    "v_mfma_f32_16x16x32_bf16 v[228:231], a[160:163], a[124:127], v[228:231]\n"
    "s_add_u32 s48, s57, 18432\n"
    "v_exp_f32_e32 v235, v235\n"
    "v_mfma_f32_16x16x32_bf16 v[236:239], a[160:163], a[156:159], 0\n"
    "ds_read_b64_tr_b16 a[160:161], v244 offset:32768\n"
    "v_mfma_f32_16x16x32_bf16 v[236:239], a[164:167], a[128:131], v[236:239]\n"
    "s_waitcnt lgkmcnt(10)\n"
    "ds_read_b64_tr_b16 a[162:163], v244 offset:34816\n"
    "v_cvt_pk_bf16_f32 v232, v232, v233\n"
    "v_mfma_f32_16x16x32_bf16 v[236:239], a[168:171], a[132:135], v[236:239]\n"
    "buffer_load_dwordx4 a[92:95], v253, s[8:11], s48 offen offset:0 sc1\n"
    "s_waitcnt lgkmcnt(4)\n"
    "s_barrier\n"
    "v_mfma_f32_16x16x32_bf16 v[236:239], a[172:175], a[136:139], v[236:239]\n"
    "ds_read_b64_tr_b16 a[164:165], v244 offset:40960\n"
    "v_exp_f32_e32 v224, v224\n"
    "v_mfma_f32_16x16x32_bf16 v[228:231], a[172:175], a[104:107], v[228:231]\n"
    "ds_read_b64_tr_b16 a[166:167], v244 offset:43008\n"
    "v_exp_f32_e32 v225, v225\n"
    "v_mfma_f32_16x16x32_bf16 v[228:231], a[168:171], a[100:103], v[228:231]\n"
    "buffer_load_dwordx4 a[124:127], v253, s[8:11], s48 offen offset:1024 sc1\n"
    "v_mfma_f32_16x16x32_bf16 v[220:223], a[168:171], a[68:71], v[220:223]\n"
    "s_add_u32 s48, s57, 2048\n"
    "ds_read_b64_tr_b16 a[168:169], v245 offset:32768\n"
    "v_cvt_pk_bf16_f32 v233, v234, v235\n"
    "v_mfma_f32_16x16x32_bf16 v[220:223], a[172:175], a[72:75], v[220:223]\n"
    "ds_read_b64_tr_b16 a[170:171], v245 offset:34816\n"
    "v_exp_f32_e32 v226, v226\n"
    "v_mfma_f32_16x16x32_bf16 v[220:223], a[176:179], a[76:79], v[220:223]\n"
    "ds_read_b64_tr_b16 a[172:173], v245 offset:40960\n"
    "buffer_load_dwordx4 a[156:159], v253, s[8:11], s48 offen offset:0 sc1\n"
    "v_mfma_f32_16x16x32_bf16 v[220:223], a[180:183], a[80:83], v[220:223]\n"
    "ds_read_b64_tr_b16 a[174:175], v245 offset:43008\n"
    "v_mfma_f32_16x16x32_bf16 v[228:231], a[180:183], a[112:115], v[228:231]\n"
    "v_exp_f32_e32 v227, v227\n"
    "v_mfma_f32_16x16x32_bf16 v[228:231], a[176:179], a[108:111], v[228:231]\n"
    "buffer_load_dwordx4 a[64:67], v253, s[8:11], s48 offen offset:1024 sc1\n"
    "v_cvt_pk_bf16_f32 v224, v224, v225\n"
    "v_mfma_f32_16x16x32_bf16 v[236:239], a[176:179], a[140:143], v[236:239]\n"
    "s_add_u32 s48, s57, 20480\n"
    "ds_read_b64_tr_b16 a[176:177], v246 offset:32768\n"
    "v_exp_f32_e32 v216, v216\n"
    "v_mfma_f32_16x16x32_bf16 v[236:239], a[180:183], a[144:147], v[236:239]\n"
    "s_waitcnt lgkmcnt(9)\n"
    "ds_read_b64_tr_b16 a[178:179], v246 offset:34816\n"
    "v_mfma_f32_16x16x32_bf16 v[236:239], a[184:187], a[148:151], v[236:239]\n"
    "ds_read_b64_tr_b16 a[180:181], v246 offset:40960\n"
    "buffer_load_dwordx4 a[96:99], v253, s[8:11], s48 offen offset:0 sc1\n"
    "v_mfma_f32_16x16x32_bf16 v[236:239], a[188:191], a[152:155], v[236:239]\n"
    "ds_read_b64_tr_b16 a[182:183], v246 offset:43008\n"
    "v_mfma_f32_16x16x32_bf16 v[228:231], a[188:191], a[120:123], v[228:231]\n"
    "v_exp_f32_e32 v217, v217\n"
    "v_mfma_f32_16x16x32_bf16 v[228:231], a[184:187], a[116:119], v[228:231]\n"
    "buffer_load_dwordx4 a[128:131], v253, s[8:11], s48 offen offset:1024 sc1\n"
    "v_cvt_pk_bf16_f32 v225, v226, v227\n"
    "v_mfma_f32_16x16x32_bf16 v[220:223], a[184:187], a[84:87], v[220:223]\n"
    "s_add_u32 s48, s57, 4096\n"
    "ds_read_b64_tr_b16 a[184:185], v247 offset:32768\n"
    "v_exp_f32_e32 v218, v218\n"
    "v_mfma_f32_16x16x32_bf16 v[220:223], a[188:191], a[88:91], v[220:223]\n"
    "s_waitcnt lgkmcnt(9)\n"
    "ds_read_b64_tr_b16 a[186:187], v247 offset:34816\n"
    "v_mfma_f32_16x16x32_bf16 v[56:59], a[160:163], v[176:179], v[56:59]\n"
    "ds_read_b64_tr_b16 a[188:189], v247 offset:40960\n"
    "buffer_load_dwordx4 a[68:71], v253, s[8:11], s48 offen offset:0 sc1\n"
    "v_mfma_f32_16x16x32_bf16 v[56:59], a[164:167], v[180:183], v[56:59]\n"
    "ds_read_b64_tr_b16 a[190:191], v247 offset:43008\n"
    "v_mfma_f32_16x16x32_bf16 v[60:63], a[164:167], v[188:191], v[60:63]\n"
    "v_exp_f32_e32 v219, v219\n"
    "v_mfma_f32_16x16x32_bf16 v[60:63], a[160:163], v[184:187], v[60:63]\n"
    "buffer_load_dwordx4 a[100:103], v253, s[8:11], s48 offen offset:1024 sc1\n"
    "v_cvt_pk_bf16_f32 v216, v216, v217\n"
    "v_mfma_f32_16x16x32_bf16 v[64:67], a[160:163], v[192:195], v[64:67]\n"
    "s_add_u32 s48, s57, 22528\n"
    "ds_read_b64_tr_b16 a[160:161], v244 offset:36864\n"
    "v_cvt_pk_bf16_f32 v217, v218, v219\n"
    "v_mfma_f32_16x16x32_bf16 v[64:67], a[164:167], v[196:199], v[64:67]\n"
    "s_waitcnt lgkmcnt(9)\n"
    "ds_read_b64_tr_b16 a[162:163], v244 offset:38912\n"
    "v_exp_f32_e32 v236, v236\n"
    "v_mfma_f32_16x16x32_bf16 v[76:79], a[168:171], v[192:195], v[76:79]\n"
    "ds_read_b64_tr_b16 a[164:165], v244 offset:45056\n"
    "buffer_load_dwordx4 a[132:135], v253, s[8:11], s48 offen offset:0 sc1\n"
    "v_mfma_f32_16x16x32_bf16 v[76:79], a[172:175], v[196:199], v[76:79]\n"
    "ds_read_b64_tr_b16 a[166:167], v244 offset:47104\n"
    "v_exp_f32_e32 v237, v237\n"
    "v_mfma_f32_16x16x32_bf16 v[72:75], a[172:175], v[188:191], v[72:75]\n"
    "v_exp_f32_e32 v238, v238\n"
    "v_mfma_f32_16x16x32_bf16 v[72:75], a[168:171], v[184:187], v[72:75]\n"
    "buffer_load_dwordx4 a[72:75], v253, s[8:11], s48 offen offset:1024 sc1\n"
    "v_exp_f32_e32 v239, v239\n"
    "v_mfma_f32_16x16x32_bf16 v[68:71], a[168:171], v[176:179], v[68:71]\n"
    "s_add_u32 s48, s57, 6144\n"
    "ds_read_b64_tr_b16 a[168:169], v245 offset:36864\n"
    "ds_write_addtid_b32 v232 offset:16384\n"
    "v_mfma_f32_16x16x32_bf16 v[68:71], a[172:175], v[180:183], v[68:71]\n"
    "s_waitcnt lgkmcnt(10)\n"
    "ds_read_b64_tr_b16 a[170:171], v245 offset:38912\n"
    "v_cvt_pk_bf16_f32 v234, v236, v237\n"
    "v_mfma_f32_16x16x32_bf16 v[80:83], a[176:179], v[176:179], v[80:83]\n"
    "ds_read_b64_tr_b16 a[172:173], v245 offset:45056\n"
    "buffer_load_dwordx4 a[104:107], v253, s[8:11], s48 offen offset:0 sc1\n"
    "v_mfma_f32_16x16x32_bf16 v[80:83], a[180:183], v[180:183], v[80:83]\n"
    "ds_read_b64_tr_b16 a[174:175], v245 offset:47104\n"
    "v_exp_f32_e32 v228, v228\n"
    "v_mfma_f32_16x16x32_bf16 v[84:87], a[180:183], v[188:191], v[84:87]\n"
    "v_exp_f32_e32 v229, v229\n"
    "v_mfma_f32_16x16x32_bf16 v[84:87], a[176:179], v[184:187], v[84:87]\n"
    "buffer_load_dwordx4 a[136:139], v253, s[8:11], s48 offen offset:1024 sc1\n"
    "ds_write_addtid_b32 v233 offset:16640\n"
    "v_mfma_f32_16x16x32_bf16 v[88:91], a[176:179], v[192:195], v[88:91]\n"
    "s_add_u32 s48, s57, 24576\n"
    "ds_read_b64_tr_b16 a[176:177], v246 offset:36864\n"
    "v_cvt_pk_bf16_f32 v235, v238, v239\n"
    "v_mfma_f32_16x16x32_bf16 v[88:91], a[180:183], v[196:199], v[88:91]\n"
    "s_waitcnt lgkmcnt(11)\n"
    "ds_read_b64_tr_b16 a[178:179], v246 offset:38912\n"
    "v_exp_f32_e32 v230, v230\n"
    "v_mfma_f32_16x16x32_bf16 v[100:103], a[184:187], v[192:195], v[100:103]\n"
    "ds_read_b64_tr_b16 a[180:181], v246 offset:45056\n"
    "buffer_load_dwordx4 a[76:79], v253, s[8:11], s48 offen offset:0 sc1\n"
    "v_mfma_f32_16x16x32_bf16 v[100:103], a[188:191], v[196:199], v[100:103]\n"
    "ds_read_b64_tr_b16 a[182:183], v246 offset:47104\n"
    "ds_write_addtid_b32 v224 offset:12288\n"
    "v_mfma_f32_16x16x32_bf16 v[96:99], a[188:191], v[188:191], v[96:99]\n"
    "v_exp_f32_e32 v231, v231\n"
    "v_mfma_f32_16x16x32_bf16 v[96:99], a[184:187], v[184:187], v[96:99]\n"
    "buffer_load_dwordx4 a[108:111], v253, s[8:11], s48 offen offset:1024 sc1\n"
    "v_cvt_pk_bf16_f32 v226, v228, v229\n"
    "v_mfma_f32_16x16x32_bf16 v[92:95], a[184:187], v[176:179], v[92:95]\n"
    "s_add_u32 s48, s57, 8192\n"
    "ds_read_b64_tr_b16 a[184:185], v247 offset:36864\n"
    "ds_write_addtid_b32 v225 offset:12544\n"
    "v_mfma_f32_16x16x32_bf16 v[92:95], a[188:191], v[180:183], v[92:95]\n"
    "s_waitcnt lgkmcnt(13)\n"
    "ds_read_b64_tr_b16 a[186:187], v247 offset:38912\n"
    "v_exp_f32_e32 v220, v220\n"
    "v_mfma_f32_16x16x32_bf16 v[104:107], a[160:163], v[176:179], v[104:107]\n"
    "ds_read_b64_tr_b16 a[188:189], v247 offset:45056\n"
    "buffer_load_dwordx4 a[140:143], v253, s[8:11], s48 offen offset:0 sc1\n"
    "v_mfma_f32_16x16x32_bf16 v[104:107], a[164:167], v[180:183], v[104:107]\n"
    "ds_read_b64_tr_b16 a[190:191], v247 offset:47104\n"
    "ds_write_addtid_b32 v216 offset:8192\n"
    "v_mfma_f32_16x16x32_bf16 v[108:111], a[164:167], v[188:191], v[108:111]\n"
    "v_exp_f32_e32 v221, v221\n"
    "v_mfma_f32_16x16x32_bf16 v[108:111], a[160:163], v[184:187], v[108:111]\n"
    "buffer_load_dwordx4 a[80:83], v253, s[8:11], s48 offen offset:1024 sc1\n"
    "v_cvt_pk_bf16_f32 v227, v230, v231\n"
    "v_mfma_f32_16x16x32_bf16 v[112:115], a[160:163], v[192:195], v[112:115]\n"
    "s_add_u32 s48, s57, 26624\n"
    "ds_read_b64_tr_b16 a[160:161], v244 offset:49152\n"
    "v_exp_f32_e32 v222, v222\n"
    "v_mfma_f32_16x16x32_bf16 v[112:115], a[164:167], v[196:199], v[112:115]\n"
    "s_waitcnt lgkmcnt(13)\n"
    "ds_read_b64_tr_b16 a[162:163], v244 offset:51200\n"
    "v_exp_f32_e32 v223, v223\n"
    "v_mfma_f32_16x16x32_bf16 v[124:127], a[168:171], v[192:195], v[124:127]\n"
    "ds_read_b64_tr_b16 a[164:165], v244 offset:57344\n"
    "buffer_load_dwordx4 a[112:115], v253, s[8:11], s48 offen offset:0 sc1\n"
    "v_mfma_f32_16x16x32_bf16 v[124:127], a[172:175], v[196:199], v[124:127]\n"
    "ds_read_b64_tr_b16 a[166:167], v244 offset:59392\n"
    "ds_write_addtid_b32 v217 offset:8448\n"
    "v_mfma_f32_16x16x32_bf16 v[120:123], a[172:175], v[188:191], v[120:123]\n"
    "ds_write_addtid_b32 v234 offset:16896\n"
    "v_cvt_pk_bf16_f32 v218, v220, v221\n"
    "v_mfma_f32_16x16x32_bf16 v[120:123], a[168:171], v[184:187], v[120:123]\n"
    "buffer_load_dwordx4 a[144:147], v253, s[8:11], s48 offen offset:1024 sc1\n"
    "v_cvt_pk_bf16_f32 v219, v222, v223\n"
    "v_mfma_f32_16x16x32_bf16 v[116:119], a[168:171], v[176:179], v[116:119]\n"
    "s_add_u32 s48, s57, 10240\n"
    "ds_read_b64_tr_b16 a[168:169], v245 offset:49152\n"
    "ds_write_addtid_b32 v235 offset:17152\n"
    "v_mfma_f32_16x16x32_bf16 v[116:119], a[172:175], v[180:183], v[116:119]\n"
    "s_waitcnt lgkmcnt(15)\n"
    "ds_read_b64_tr_b16 a[170:171], v245 offset:51200\n"
    "ds_write_addtid_b32 v226 offset:12800\n"
    "v_mfma_f32_16x16x32_bf16 v[128:131], a[176:179], v[176:179], v[128:131]\n"
    "ds_read_b64_tr_b16 a[172:173], v245 offset:57344\n"
    "buffer_load_dwordx4 a[84:87], v253, s[8:11], s48 offen offset:0 sc1\n"
    "v_mfma_f32_16x16x32_bf16 v[128:131], a[180:183], v[180:183], v[128:131]\n"
    "ds_read_b64_tr_b16 a[174:175], v245 offset:59392\n"
    "ds_write_addtid_b32 v227 offset:13056\n"
    "v_mfma_f32_16x16x32_bf16 v[132:135], a[180:183], v[188:191], v[132:135]\n"
    "ds_write_addtid_b32 v218 offset:8704\n"
    "ds_write_addtid_b32 v219 offset:8960\n"
    "v_mfma_f32_16x16x32_bf16 v[132:135], a[176:179], v[184:187], v[132:135]\n"
    "buffer_load_dwordx4 a[116:119], v253, s[8:11], s48 offen offset:1024 sc1\n"
    "v_mfma_f32_16x16x32_bf16 v[136:139], a[176:179], v[192:195], v[136:139]\n"
    "s_add_u32 s48, s57, 28672\n"
    "ds_read_b64_tr_b16 a[176:177], v246 offset:49152\n"
    "v_mfma_f32_16x16x32_bf16 v[136:139], a[180:183], v[196:199], v[136:139]\n"
    "s_waitcnt lgkmcnt(15)\n"
    "ds_read_b64_tr_b16 a[178:179], v246 offset:51200\n"
    "v_mfma_f32_16x16x32_bf16 v[148:151], a[184:187], v[192:195], v[148:151]\n"
    "buffer_load_dwordx4 a[148:151], v253, s[8:11], s48 offen offset:0 sc1\n"
    "s_waitcnt lgkmcnt(2)\n"
    "s_barrier\n"
    "v_mfma_f32_16x16x32_bf16 v[148:151], a[188:191], v[196:199], v[148:151]\n"
    "ds_read_b64_tr_b16 a[180:181], v246 offset:57344\n"
    "v_mfma_f32_16x16x32_bf16 v[144:147], a[188:191], v[188:191], v[144:147]\n"
    "ds_read_b64_tr_b16 a[182:183], v246 offset:59392\n"
    "ds_read2st64_b32 v[220:221], v250 offset0:32 offset1:33\n"
    "v_mfma_f32_16x16x32_bf16 v[144:147], a[184:187], v[184:187], v[144:147]\n"
    "buffer_load_dwordx4 a[88:91], v253, s[8:11], s48 offen offset:1024 sc1\n"
    "ds_read2st64_b32 v[222:223], v250 offset0:34 offset1:35\n"
    "v_mfma_f32_16x16x32_bf16 v[140:143], a[184:187], v[176:179], v[140:143]\n"
    "s_add_u32 s48, s57, 12288\n"
    "ds_read_b64_tr_b16 a[184:185], v247 offset:49152\n"
    "ds_read2st64_b32 v[228:229], v250 offset0:48 offset1:49\n"
    "v_mfma_f32_16x16x32_bf16 v[140:143], a[188:191], v[180:183], v[140:143]\n"
    "ds_read_b64_tr_b16 a[186:187], v247 offset:51200\n"
    "ds_read2st64_b32 v[230:231], v250 offset0:50 offset1:51\n"
    "v_mfma_f32_16x16x32_bf16 v[56:59], a[160:163], v[216:219], v[56:59]\n"
    "ds_read_b64_tr_b16 a[188:189], v247 offset:57344\n"
    "buffer_load_dwordx4 a[120:123], v253, s[8:11], s48 offen offset:0 sc1\n"
    "v_mfma_f32_16x16x32_bf16 v[60:63], a[160:163], v[224:227], v[60:63]\n"
    "ds_read_b64_tr_b16 a[190:191], v247 offset:59392\n"
    "ds_read2st64_b32 v[236:237], v250 offset0:64 offset1:65\n"
    "v_mfma_f32_16x16x32_bf16 v[64:67], a[160:163], v[232:235], v[64:67]\n"
    "ds_read_b64_tr_b16 a[160:161], v244 offset:53248\n"
    "ds_read2st64_b32 v[238:239], v250 offset0:66 offset1:67\n"
    "v_mfma_f32_16x16x32_bf16 v[76:79], a[168:171], v[232:235], v[76:79]\n"
    "ds_read_b64_tr_b16 a[162:163], v244 offset:55296\n"
    "buffer_load_dwordx4 a[152:155], v253, s[8:11], s48 offen offset:1024 sc1\n"
    "v_mfma_f32_16x16x32_bf16 v[72:75], a[168:171], v[224:227], v[72:75]\n"
    "v_lshrrev_b32_e32 v158, 4, v253\n"
    "v_mfma_f32_16x16x32_bf16 v[68:71], a[168:171], v[216:219], v[68:71]\n"
    "s_waitcnt lgkmcnt(1)\n"
    "v_lshrrev_b32_e32 v159, 4, v158\n"
    "v_mfma_f32_16x16x32_bf16 v[56:59], a[164:167], v[220:223], v[56:59]\n"
    "v_bfe_u32 v255, v158, 2, 2\n"
    "v_and_b32_e32 v158, 3, v158\n"
    "v_mfma_f32_16x16x32_bf16 v[60:63], a[164:167], v[228:231], v[60:63]\n"
    "v_xor_b32_e32 v159, v159, v255\n"
    "v_lshl_or_b32 v158, v159, 2, v158\n"
    "v_mfma_f32_16x16x32_bf16 v[64:67], a[164:167], v[236:239], v[64:67]\n"
    "ds_read_b64_tr_b16 a[164:165], v244 offset:61440\n"
    "v_mov_b32_e32 v159, 0x3f80\n"
    "v_mfma_f32_16x16x32_bf16 v[76:79], a[172:175], v[236:239], v[76:79]\n"
    "ds_read_b64_tr_b16 a[166:167], v244 offset:63488\n"
    "v_mov_b32_e32 v152, 0\n"
    "v_mfma_f32_16x16x32_bf16 v[72:75], a[172:175], v[228:231], v[72:75]\n"
    "s_waitcnt lgkmcnt(3)\n"
    "s_barrier\n"
    "v_mfma_f32_16x16x32_bf16 v[68:71], a[172:175], v[220:223], v[68:71]\n"
    "s_add_u32 s61, s47, 5779456\n"
    "ds_read_b64_tr_b16 a[168:169], v245 offset:53248\n"
    "v_mfma_f32_16x16x32_bf16 v[80:83], a[176:179], v[216:219], v[80:83]\n"
    "s_add_u32 m0, s49, 8192\n"
    "ds_read_b64_tr_b16 a[170:171], v245 offset:55296\n"
    "v_cvt_pk_bf16_f32 v64, v64, v65\n"
    "v_mfma_f32_16x16x32_bf16 v[80:83], a[180:183], v[220:223], v[80:83]\n"
    "ds_read_b64_tr_b16 a[172:173], v245 offset:61440\n"
    "buffer_load_dwordx4 v252, s[8:11], s61 offen offset:0 sc1 lds\n"
    "v_mfma_f32_16x16x32_bf16 v[84:87], a[180:183], v[228:231], v[84:87]\n"
    "s_add_u32 s61, s47, 5783552\n"
    "ds_read_b64_tr_b16 a[174:175], v245 offset:63488\n"
    "v_cvt_pk_bf16_f32 v65, v66, v67\n"
    "v_mfma_f32_16x16x32_bf16 v[84:87], a[176:179], v[224:227], v[84:87]\n"
    "s_add_u32 m0, s49, 12288\n"
    "v_cvt_pk_bf16_f32 v66, v76, v77\n"
    "v_cvt_pk_bf16_f32 v67, v78, v79\n"
    "v_mfma_f32_16x16x32_bf16 v[88:91], a[176:179], v[232:235], v[88:91]\n"
    "ds_read_b64_tr_b16 a[176:177], v246 offset:53248\n"
    "buffer_load_dwordx4 v252, s[8:11], s61 offen offset:0 sc1 lds\n"
    "buffer_store_dwordx4 v[64:67], v253, s[8:11], s86 offen offset:0 sc1\n"
    "v_cvt_pk_bf16_f32 v60, v60, v61\n"
    "v_mov_b32_e32 v153, 0\n"
    "v_mfma_f32_16x16x32_bf16 v[88:91], a[180:183], v[236:239], v[88:91]\n"
    "s_add_u32 s61, s47, 5787648\n"
    "ds_read_b64_tr_b16 a[178:179], v246 offset:55296\n"
    "v_mfma_f32_16x16x32_bf16 v[100:103], a[184:187], v[232:235], v[100:103]\n"
    "s_add_u32 m0, s49, 16384\n"
    "ds_read_b64_tr_b16 a[180:181], v246 offset:61440\n"
    "v_mov_b32_e32 v156, 0\n"
    "v_mfma_f32_16x16x32_bf16 v[100:103], a[188:191], v[236:239], v[100:103]\n"
    "ds_read_b64_tr_b16 a[182:183], v246 offset:63488\n"
    "buffer_load_dwordx4 v252, s[8:11], s61 offen offset:0 sc1 lds\n"
    "v_mfma_f32_16x16x32_bf16 v[96:99], a[188:191], v[228:231], v[96:99]\n"
    "s_mov_b32 m0, s49\n"
    "v_cvt_pk_bf16_f32 v61, v62, v63\n"
    "v_mov_b32_e32 v157, 0\n"
    "v_mfma_f32_16x16x32_bf16 v[96:99], a[184:187], v[224:227], v[96:99]\n"
    "v_cvt_pk_bf16_f32 v62, v72, v73\n"
    "v_cmp_eq_u32_e32 vcc, 0, v158\n"
    "v_mfma_f32_16x16x32_bf16 v[92:95], a[184:187], v[216:219], v[92:95]\n"
    "ds_read_b64_tr_b16 a[184:185], v247 offset:53248\n"
    "v_cndmask_b32_e32 v154, 0, v159, vcc\n"
    "v_mfma_f32_16x16x32_bf16 v[92:95], a[188:191], v[220:223], v[92:95]\n"
    "s_waitcnt lgkmcnt(9)\n"
    "ds_read_b64_tr_b16 a[186:187], v247 offset:55296\n"
    "v_cmp_eq_u32_e32 vcc, 1, v158\n"
    "v_mfma_f32_16x16x32_bf16 v[104:107], a[160:163], v[216:219], v[104:107]\n"
    "ds_read_b64_tr_b16 a[188:189], v247 offset:61440\n"
    "v_cndmask_b32_e64 v154, v154, 1.0, vcc\n"
    "v_mfma_f32_16x16x32_bf16 v[104:107], a[164:167], v[220:223], v[104:107]\n"
    "ds_read_b64_tr_b16 a[190:191], v247 offset:63488\n"
    "v_cvt_pk_bf16_f32 v63, v74, v75\n"
    "v_mfma_f32_16x16x32_bf16 v[108:111], a[164:167], v[228:231], v[108:111]\n"
    "buffer_store_dwordx4 v[60:63], v253, s[8:11], s85 offen offset:0 sc1\n"
    "v_cmp_eq_u32_e32 vcc, 2, v158\n"
    "s_nop 1\n"
    "v_cndmask_b32_e32 v155, 0, v159, vcc\n"
    "v_mfma_f32_16x16x32_bf16 v[108:111], a[160:163], v[224:227], v[108:111]\n"
    "v_cvt_pk_bf16_f32 v56, v56, v57\n"
    "v_cmp_eq_u32_e32 vcc, 3, v158\n"
    "v_mfma_f32_16x16x32_bf16 v[112:115], a[160:163], v[232:235], v[112:115]\n"
    "ds_read_b128 a[160:163], v243 offset:20480\n"
    "v_cndmask_b32_e64 v155, v155, 1.0, vcc\n"
    "v_mfma_f32_16x16x32_bf16 v[112:115], a[164:167], v[236:239], v[112:115]\n"
    "s_waitcnt lgkmcnt(9)\n"
    "ds_read_b128 a[164:167], v240 offset:16384\n"
    "v_and_b32_e32 v251, 0xf0, v253\n"
    "v_mfma_f32_16x16x32_bf16 v[124:127], a[168:171], v[232:235], v[124:127]\n"
    "v_cvt_pk_bf16_f32 v57, v58, v59\n"
    "v_mov_b32_e32 v254, 2.0\n"
    "v_mfma_f32_16x16x32_bf16 v[124:127], a[172:175], v[236:239], v[124:127]\n"
    "v_cmp_le_u32_e32 vcc, 0x100, v253\n"
    "v_mov_b32_e32 v255, s29\n"
    "v_mfma_f32_16x16x32_bf16 v[120:123], a[172:175], v[228:231], v[120:123]\n"
    "v_cvt_pk_bf16_f32 v58, v68, v69\n"
    "v_mfma_f32_16x16x32_bf16 v[120:123], a[168:171], v[224:227], v[120:123]\n"
    "v_cvt_pk_bf16_f32 v59, v70, v71\n"
    "v_cndmask_b32_e32 v254, v254, v255, vcc\n"
    "v_mfma_f32_16x16x32_bf16 v[116:119], a[168:171], v[216:219], v[116:119]\n"
    "ds_read_b128 a[168:171], v241 offset:16384\n"
    "v_cmp_le_u32_e32 vcc, 0x200, v253\n"
    "v_mov_b32_e32 v255, s30\n"
    "v_mfma_f32_16x16x32_bf16 v[116:119], a[172:175], v[220:223], v[116:119]\n"
    "s_waitcnt lgkmcnt(7)\n"
    "ds_read_b128 a[172:175], v240 offset:20480\n"
    "v_cndmask_b32_e32 v254, v254, v255, vcc\n"
    "v_mfma_f32_16x16x32_bf16 v[128:131], a[176:179], v[216:219], v[128:131]\n"
    "buffer_store_dwordx4 v[56:59], v253, s[8:11], s84 offen offset:0 sc1\n"
    "v_cmp_le_u32_e32 vcc, 0x300, v253\n"
    "v_mov_b32_e32 v255, s31\n"
    "v_mfma_f32_16x16x32_bf16 v[128:131], a[180:183], v[220:223], v[128:131]\n"
    "s_cmp_eq_u32 s51, 0\n"
    "s_cselect_b32 s60, 2.0, 24576\n"
    "v_cndmask_b32_e32 v254, v254, v255, vcc\n"
    "v_mfma_f32_16x16x32_bf16 v[132:135], a[180:183], v[228:231], v[132:135]\n"
    "v_cvt_pk_bf16_f32 v88, v88, v89\n"
    "v_add_u32_e32 v251, v251, v254\n"
    "v_mfma_f32_16x16x32_bf16 v[132:135], a[176:179], v[224:227], v[132:135]\n"
    "s_lshl_b32 s45, s51, 16\n"
    "s_add_u32 s45, s45, %3\n"
    "s_add_u32 s45, s45, 57344\n"
    "v_mfma_f32_16x16x32_bf16 v[136:139], a[176:179], v[232:235], v[136:139]\n"
    "ds_read_b128 a[176:179], v241 offset:20480\n"
    "v_mfma_f32_16x16x32_bf16 v[136:139], a[180:183], v[236:239], v[136:139]\n"
    "s_waitcnt lgkmcnt(5)\n"
    "ds_read_b128 a[180:183], v242 offset:16384\n"
    "v_mfma_f32_16x16x32_bf16 v[148:151], a[184:187], v[232:235], v[148:151]\n"
    "v_cvt_pk_bf16_f32 v89, v90, v91\n"
    "v_mfma_f32_16x16x32_bf16 v[148:151], a[188:191], v[236:239], v[148:151]\n"
    "s_lshl_b32 s46, s89, 12\n"
    "s_add_u32 s46, s46, s45\n"
    "v_add_u32_e32 v159, s46, v253\n"
    "v_mfma_f32_16x16x32_bf16 v[144:147], a[188:191], v[228:231], v[144:147]\n"
    "v_cvt_pk_bf16_f32 v90, v100, v101\n"
    "v_mfma_f32_16x16x32_bf16 v[144:147], a[184:187], v[224:227], v[144:147]\n"
    "v_cvt_pk_bf16_f32 v91, v102, v103\n"
    "v_mfma_f32_16x16x32_bf16 v[140:143], a[184:187], v[216:219], v[140:143]\n"
    "s_add_u32 s45, s45, 16384\n"
    "ds_read_b128 a[184:187], v243 offset:16384\n"
    "v_lshrrev_b32_e32 v255, 2, v253\n"
    "v_mfma_f32_16x16x32_bf16 v[140:143], a[188:191], v[220:223], v[140:143]\n"
    "s_waitcnt lgkmcnt(3)\n"
    "ds_read_b128 a[188:191], v242 offset:20480\n"
    "v_mfma_f32_16x16x32_bf16 v[200:203], a[160:163], a[28:31], 0\n"
    "buffer_store_dwordx4 v[88:91], v253, s[8:11], s86 offen offset:1024 sc1\n"
    "v_add_u32_e32 v255, s45, v255\n"
    "v_mfma_f32_16x16x32_bf16 v[200:203], a[164:167], a[0:3], v[200:203]\n"
    "v_cvt_pk_bf16_f32 v84, v84, v85\n"
    "v_mfma_f32_16x16x32_bf16 v[208:211], a[164:167], a[32:35], 0\n"
    "v_cvt_pk_bf16_f32 v85, v86, v87\n"
    "v_mfma_f32_16x16x32_bf16 v[208:211], a[160:163], a[60:63], v[208:211]\n"
    "ds_read_b128 a[160:163], v243 offset:22528\n"
    "v_mfma_f32_16x16x32_bf16 v[208:211], a[168:171], a[36:39], v[208:211]\n"
    "ds_read_b128 a[164:167], v240 offset:18432\n"
    "v_mfma_f32_16x16x32_bf16 v[208:211], a[172:175], a[40:43], v[208:211]\n"
    "v_cvt_pk_bf16_f32 v86, v96, v97\n"
    "v_mfma_f32_16x16x32_bf16 v[200:203], a[172:175], a[8:11], v[200:203]\n"
    "v_cvt_pk_bf16_f32 v87, v98, v99\n"
    "v_mfma_f32_16x16x32_bf16 v[200:203], a[168:171], a[4:7], v[200:203]\n"
    "s_waitcnt lgkmcnt(2)\n"
    "ds_read_b128 a[168:171], v241 offset:18432\n"
    "v_mfma_f32_16x16x32_bf16 v[200:203], a[176:179], a[12:15], v[200:203]\n"
    "ds_read_b128 a[172:175], v240 offset:22528\n"
    "v_mfma_f32_16x16x32_bf16 v[200:203], a[180:183], a[16:19], v[200:203]\n"
    "buffer_store_dwordx4 v[84:87], v253, s[8:11], s85 offen offset:1024 sc1\n"
    "v_cvt_pk_bf16_f32 v80, v80, v81\n"
    "v_mfma_f32_16x16x32_bf16 v[208:211], a[180:183], a[48:51], v[208:211]\n"
    "v_cvt_pk_bf16_f32 v81, v82, v83\n"
    "v_mfma_f32_16x16x32_bf16 v[208:211], a[176:179], a[44:47], v[208:211]\n"
    "ds_read_b128 a[176:179], v241 offset:22528\n"
    "v_mfma_f32_16x16x32_bf16 v[208:211], a[184:187], a[52:55], v[208:211]\n"
    "ds_read_b128 a[180:183], v242 offset:18432\n"
    "v_mfma_f32_16x16x32_bf16 v[208:211], a[188:191], a[56:59], v[208:211]\n"
    "v_cvt_pk_bf16_f32 v82, v92, v93\n"
    "v_mfma_f32_16x16x32_bf16 v[200:203], a[188:191], a[24:27], v[200:203]\n"
    "v_cvt_pk_bf16_f32 v83, v94, v95\n"
    "v_mfma_f32_16x16x32_bf16 v[200:203], a[184:187], a[20:23], v[200:203]\n"
    "s_waitcnt lgkmcnt(2)\n"
    "ds_read_b128 a[184:187], v243 offset:18432\n"
    "v_mfma_f32_16x16x32_bf16 v[204:207], a[160:163], a[28:31], 0\n"
    "ds_read_b128 a[188:191], v242 offset:22528\n"
    "v_mfma_f32_16x16x32_bf16 v[204:207], a[164:167], a[0:3], v[204:207]\n"
    "v_exp_f32_e32 v208, v208\n"
    "v_mfma_f32_16x16x32_bf16 v[212:215], a[164:167], a[32:35], 0\n"
    "v_exp_f32_e32 v209, v209\n"
    "v_mfma_f32_16x16x32_bf16 v[212:215], a[160:163], a[60:63], v[212:215]\n"
    "ds_read_b64_tr_b16 a[160:161], v244 offset:0\n"
    "v_mfma_f32_16x16x32_bf16 v[212:215], a[168:171], a[36:39], v[212:215]\n"
    "ds_read_b64_tr_b16 a[162:163], v244 offset:2048\n"
    "buffer_store_dwordx4 v[80:83], v253, s[8:11], s84 offen offset:1024 sc1\n"
    "v_exp_f32_e32 v210, v210\n"
    "v_mfma_f32_16x16x32_bf16 v[212:215], a[172:175], a[40:43], v[212:215]\n"
    "ds_read_b64_tr_b16 a[164:165], v244 offset:8192\n"
    "v_cvt_pk_bf16_f32 v112, v112, v113\n"
    "v_mfma_f32_16x16x32_bf16 v[204:207], a[172:175], a[8:11], v[204:207]\n"
    "ds_read_b64_tr_b16 a[166:167], v244 offset:10240\n"
    "v_exp_f32_e32 v211, v211\n"
    "v_mfma_f32_16x16x32_bf16 v[204:207], a[168:171], a[4:7], v[204:207]\n"
    "s_waitcnt lgkmcnt(4)\n"
    "ds_read_b64_tr_b16 a[168:169], v245 offset:0\n"
    "v_cvt_pk_bf16_f32 v113, v114, v115\n"
    "v_mfma_f32_16x16x32_bf16 v[204:207], a[176:179], a[12:15], v[204:207]\n"
    "ds_read_b64_tr_b16 a[170:171], v245 offset:2048\n"
    "v_mfma_f32_16x16x32_bf16 v[204:207], a[180:183], a[16:19], v[204:207]\n"
    "ds_read_b64_tr_b16 a[172:173], v245 offset:8192\n"
    "v_cvt_pk_bf16_f32 v208, v208, v209\n"
    "v_mfma_f32_16x16x32_bf16 v[212:215], a[180:183], a[48:51], v[212:215]\n"
    "ds_read_b64_tr_b16 a[174:175], v245 offset:10240\n"
    "v_cvt_pk_bf16_f32 v114, v124, v125\n"
    "v_mfma_f32_16x16x32_bf16 v[212:215], a[176:179], a[44:47], v[212:215]\n"
    "ds_read_b64_tr_b16 a[176:177], v246 offset:0\n"
    "v_exp_f32_e32 v200, v200\n"
    "v_mfma_f32_16x16x32_bf16 v[212:215], a[184:187], a[52:55], v[212:215]\n"
    "ds_read_b64_tr_b16 a[178:179], v246 offset:2048\n"
    "v_cvt_pk_bf16_f32 v115, v126, v127\n"
    "v_mfma_f32_16x16x32_bf16 v[212:215], a[188:191], a[56:59], v[212:215]\n"
    "ds_read_b64_tr_b16 a[180:181], v246 offset:8192\n"
    "buffer_store_dwordx4 v[112:115], v253, s[8:11], s86 offen offset:2048 sc1\n"
    "v_exp_f32_e32 v201, v201\n"
    "v_mfma_f32_16x16x32_bf16 v[204:207], a[188:191], a[24:27], v[204:207]\n"
    "ds_read_b64_tr_b16 a[182:183], v246 offset:10240\n"
    "v_cvt_pk_bf16_f32 v108, v108, v109\n"
    "v_mfma_f32_16x16x32_bf16 v[204:207], a[184:187], a[20:23], v[204:207]\n"
    "s_waitcnt lgkmcnt(4)\n"
    "ds_read_b64_tr_b16 a[184:185], v247 offset:0\n"
    "v_cvt_pk_bf16_f32 v209, v210, v211\n"
    "v_mfma_f32_16x16x32_bf16 v[0:3], a[160:163], v[160:163], v[0:3]\n"
    "ds_read_b64_tr_b16 a[186:187], v247 offset:2048\n"
    "v_cvt_pk_bf16_f32 v109, v110, v111\n"
    "v_mfma_f32_16x16x32_bf16 v[0:3], a[164:167], v[164:167], v[0:3]\n"
    "ds_read_b64_tr_b16 a[188:189], v247 offset:8192\n"
    "v_exp_f32_e32 v202, v202\n"
    "v_mfma_f32_16x16x32_bf16 a[248:251], a[164:167], v[172:175], a[248:251]\n"
    "ds_read_b64_tr_b16 a[190:191], v247 offset:10240\n"
    "v_cvt_pk_bf16_f32 v110, v120, v121\n"
    "v_mfma_f32_16x16x32_bf16 a[248:251], a[160:163], v[168:171], a[248:251]\n"
    "ds_read_b64_tr_b16 a[160:161], v244 offset:4096\n"
    "v_exp_f32_e32 v203, v203\n"
    "v_mfma_f32_16x16x32_bf16 a[252:255], a[168:171], v[168:171], a[252:255]\n"
    "ds_read_b64_tr_b16 a[162:163], v244 offset:6144\n"
    "v_cvt_pk_bf16_f32 v111, v122, v123\n"
    "v_mfma_f32_16x16x32_bf16 a[252:255], a[172:175], v[172:175], a[252:255]\n"
    "ds_read_b64_tr_b16 a[164:165], v244 offset:12288\n"
    "v_cvt_pk_bf16_f32 v200, v200, v201\n"
    "v_mfma_f32_16x16x32_bf16 v[4:7], a[172:175], v[164:167], v[4:7]\n"
    "ds_read_b64_tr_b16 a[166:167], v244 offset:14336\n"
    "ds_write_addtid_b32 v208 offset:4096\n"
    "v_mfma_f32_16x16x32_bf16 v[4:7], a[168:171], v[160:163], v[4:7]\n"
    "s_waitcnt lgkmcnt(5)\n"
    "ds_read_b64_tr_b16 a[168:169], v245 offset:4096\n"
    "v_cvt_pk_bf16_f32 v201, v202, v203\n"
    "v_mfma_f32_16x16x32_bf16 v[8:11], a[176:179], v[160:163], v[8:11]\n"
    "ds_read_b64_tr_b16 a[170:171], v245 offset:6144\n"
    "buffer_store_dwordx4 v[108:111], v253, s[8:11], s85 offen offset:2048 sc1\n"
    "v_cvt_pk_bf16_f32 v104, v104, v105\n"
    "v_mfma_f32_16x16x32_bf16 v[8:11], a[180:183], v[164:167], v[8:11]\n"
    "ds_read_b64_tr_b16 a[172:173], v245 offset:12288\n"
    "v_cvt_pk_bf16_f32 v105, v106, v107\n"
    "v_mfma_f32_16x16x32_bf16 v[32:35], a[180:183], v[172:175], v[32:35]\n"
    "ds_read_b64_tr_b16 a[174:175], v245 offset:14336\n"
    "v_exp_f32_e32 v212, v212\n"
    "v_mfma_f32_16x16x32_bf16 v[32:35], a[176:179], v[168:171], v[32:35]\n"
    "ds_read_b64_tr_b16 a[176:177], v246 offset:4096\n"
    "ds_write_addtid_b32 v209 offset:4352\n"
    "v_mfma_f32_16x16x32_bf16 v[36:39], a[184:187], v[168:171], v[36:39]\n"
    "ds_read_b64_tr_b16 a[178:179], v246 offset:6144\n"
    "v_exp_f32_e32 v213, v213\n"
    "v_mfma_f32_16x16x32_bf16 v[36:39], a[188:191], v[172:175], v[36:39]\n"
    "ds_read_b64_tr_b16 a[180:181], v246 offset:12288\n"
    "v_cvt_pk_bf16_f32 v106, v116, v117\n"
    "v_mfma_f32_16x16x32_bf16 v[12:15], a[188:191], v[164:167], v[12:15]\n"
    "ds_read_b64_tr_b16 a[182:183], v246 offset:14336\n"
    "v_cvt_pk_bf16_f32 v107, v118, v119\n"
    "v_mfma_f32_16x16x32_bf16 v[12:15], a[184:187], v[160:163], v[12:15]\n"
    "s_waitcnt lgkmcnt(5)\n"
    "ds_read_b64_tr_b16 a[184:185], v247 offset:4096\n"
    "ds_write_addtid_b32 v200 offset:0\n"
    "v_mfma_f32_16x16x32_bf16 v[16:19], a[160:163], v[160:163], v[16:19]\n"
    "ds_read_b64_tr_b16 a[186:187], v247 offset:6144\n"
    "buffer_store_dwordx4 v[104:107], v253, s[8:11], s84 offen offset:2048 sc1\n"
    "v_cvt_pk_bf16_f32 v136, v136, v137\n"
    "v_mfma_f32_16x16x32_bf16 v[16:19], a[164:167], v[164:167], v[16:19]\n"
    "ds_read_b64_tr_b16 a[188:189], v247 offset:12288\n"
    "v_mfma_f32_16x16x32_bf16 v[40:43], a[164:167], v[172:175], v[40:43]\n"
    "ds_read_b64_tr_b16 a[190:191], v247 offset:14336\n"
    "v_cvt_pk_bf16_f32 v137, v138, v139\n"
    "v_mfma_f32_16x16x32_bf16 v[40:43], a[160:163], v[168:171], v[40:43]\n"
    "ds_read_b128 a[160:163], v243 offset:36864\n"
    "ds_write_addtid_b32 v201 offset:256\n"
    "v_mfma_f32_16x16x32_bf16 v[44:47], a[168:171], v[168:171], v[44:47]\n"
    "ds_read_b128 a[164:167], v240 offset:32768\n"
    "v_cvt_pk_bf16_f32 v138, v148, v149\n"
    "v_mfma_f32_16x16x32_bf16 v[44:47], a[172:175], v[172:175], v[44:47]\n"
    "v_exp_f32_e32 v214, v214\n"
    "v_mfma_f32_16x16x32_bf16 v[20:23], a[172:175], v[164:167], v[20:23]\n"
    "v_exp_f32_e32 v215, v215\n"
    "v_mfma_f32_16x16x32_bf16 v[20:23], a[168:171], v[160:163], v[20:23]\n"
    "s_waitcnt lgkmcnt(3)\n"
    "ds_read_b128 a[168:171], v241 offset:32768\n"
    "v_cvt_pk_bf16_f32 v210, v212, v213\n"
    "v_mfma_f32_16x16x32_bf16 v[24:27], a[176:179], v[160:163], v[24:27]\n"
    "ds_read_b128 a[172:175], v240 offset:36864\n"
    "v_cvt_pk_bf16_f32 v139, v150, v151\n"
    "v_mfma_f32_16x16x32_bf16 v[24:27], a[180:183], v[164:167], v[24:27]\n"
    "v_exp_f32_e32 v204, v204\n"
    "v_mfma_f32_16x16x32_bf16 v[48:51], a[180:183], v[172:175], v[48:51]\n"
    "buffer_store_dwordx4 v[136:139], v253, s[8:11], s86 offen offset:3072 sc1\n"
    "v_exp_f32_e32 v205, v205\n"
    "v_mfma_f32_16x16x32_bf16 v[48:51], a[176:179], v[168:171], v[48:51]\n"
    "ds_read_b128 a[176:179], v241 offset:36864\n"
    "v_cvt_pk_bf16_f32 v211, v214, v215\n"
    "v_mfma_f32_16x16x32_bf16 v[52:55], a[184:187], v[168:171], v[52:55]\n"
    "ds_read_b128 a[180:183], v242 offset:32768\n"
    "v_cvt_pk_bf16_f32 v132, v132, v133\n"
    "v_mfma_f32_16x16x32_bf16 v[52:55], a[188:191], v[172:175], v[52:55]\n"
    "v_exp_f32_e32 v206, v206\n"
    "v_mfma_f32_16x16x32_bf16 v[28:31], a[188:191], v[164:167], v[28:31]\n"
    "v_exp_f32_e32 v207, v207\n"
    "v_mfma_f32_16x16x32_bf16 v[28:31], a[184:187], v[160:163], v[28:31]\n"
    "s_waitcnt lgkmcnt(2)\n"
    "ds_read_b128 a[184:187], v243 offset:32768\n"
    "v_cvt_pk_bf16_f32 v202, v204, v205\n"
    "v_mfma_f32_16x16x32_bf16 v[160:163], a[160:163], a[28:31], 0\n"
    "ds_read_b128 a[188:191], v242 offset:36864\n"
    "v_cvt_pk_bf16_f32 v133, v134, v135\n"
    "v_mfma_f32_16x16x32_bf16 v[160:163], a[164:167], a[0:3], v[160:163]\n"
    "ds_write_addtid_b32 v210 offset:4608\n"
    "v_cvt_pk_bf16_f32 v203, v206, v207\n"
    "v_mfma_f32_16x16x32_bf16 v[168:171], a[164:167], a[32:35], 0\n"
    "ds_write_addtid_b32 v211 offset:4864\n"
    "v_cvt_pk_bf16_f32 v134, v144, v145\n"
    "v_mfma_f32_16x16x32_bf16 v[168:171], a[160:163], a[60:63], v[168:171]\n"
    "ds_read_b128 a[160:163], v243 offset:38912\n"
    "v_cvt_pk_bf16_f32 v135, v146, v147\n"
    "v_mfma_f32_16x16x32_bf16 v[168:171], a[168:171], a[36:39], v[168:171]\n"
    "ds_read_b128 a[164:167], v240 offset:34816\n"
    "buffer_store_dwordx4 v[132:135], v253, s[8:11], s85 offen offset:3072 sc1\n"
    "v_cvt_pk_bf16_f32 v128, v128, v129\n"
    "v_mfma_f32_16x16x32_bf16 v[168:171], a[172:175], a[40:43], v[168:171]\n"
    "ds_write_addtid_b32 v202 offset:512\n"
    "v_mfma_f32_16x16x32_bf16 v[160:163], a[172:175], a[8:11], v[160:163]\n"
    "ds_write_addtid_b32 v203 offset:768\n"
    "v_mfma_f32_16x16x32_bf16 v[160:163], a[168:171], a[4:7], v[160:163]\n"
    "s_waitcnt lgkmcnt(6)\n"
    "ds_read_b128 a[168:171], v241 offset:34816\n"
    "v_mfma_f32_16x16x32_bf16 v[160:163], a[176:179], a[12:15], v[160:163]\n"
    "ds_read_b128 a[172:175], v240 offset:38912\n"
    "v_mfma_f32_16x16x32_bf16 v[160:163], a[180:183], a[16:19], v[160:163]\n"
    "v_cvt_pk_bf16_f32 v129, v130, v131\n"
    "v_mfma_f32_16x16x32_bf16 v[168:171], a[180:183], a[48:51], v[168:171]\n"
    "s_waitcnt lgkmcnt(2)\n"
    "s_barrier\n"
    "v_mfma_f32_16x16x32_bf16 v[168:171], a[176:179], a[44:47], v[168:171]\n"
    "s_lshl_b32 s45, s89, 8\n"
    "ds_read_b128 a[176:179], v241 offset:38912\n"
    "v_add_u32_e32 v254, s45, v255\n"
    "v_mfma_f32_16x16x32_bf16 v[168:171], a[184:187], a[52:55], v[168:171]\n"
    "ds_read_b128 a[180:183], v242 offset:34816\n"
    "v_cvt_pk_bf16_f32 v130, v140, v141\n"
    "v_mfma_f32_16x16x32_bf16 v[168:171], a[188:191], a[56:59], v[168:171]\n"
    "ds_read2st64_b32 v[204:205], v250 offset0:0 offset1:1\n"
    "v_mfma_f32_16x16x32_bf16 v[160:163], a[188:191], a[24:27], v[160:163]\n"
    "s_lshl_b32 s45, s50, 8\n"
    "ds_read2st64_b32 v[206:207], v250 offset0:2 offset1:3\n"
    "v_add_u32_e32 v255, s45, v255\n"
    "v_mfma_f32_16x16x32_bf16 v[160:163], a[184:187], a[20:23], v[160:163]\n"
    "s_waitcnt lgkmcnt(4)\n"
    "ds_read_b128 a[184:187], v243 offset:34816\n"
    "v_cvt_pk_bf16_f32 v131, v142, v143\n"
    "v_mfma_f32_16x16x32_bf16 v[164:167], a[160:163], a[28:31], 0\n"
    "ds_read_b128 a[188:191], v242 offset:38912\n"
    "buffer_store_dwordx4 v[128:131], v253, s[8:11], s84 offen offset:3072 sc1\n"
    "v_exp_f32_e32 v168, v168\n"
    "v_mfma_f32_16x16x32_bf16 v[164:167], a[164:167], a[0:3], v[164:167]\n"
    "ds_read2st64_b32 v[212:213], v250 offset0:16 offset1:17\n"
    "v_permlane16_swap_b32_e32 v147, v151\n"
    "v_mfma_f32_16x16x32_bf16 v[172:175], a[164:167], a[32:35], 0\n"
    "ds_read2st64_b32 v[214:215], v250 offset0:18 offset1:19\n"
    "v_exp_f32_e32 v169, v169\n"
    "v_mfma_f32_16x16x32_bf16 v[172:175], a[160:163], a[60:63], v[172:175]\n"
    "ds_read_b64_tr_b16 a[160:161], v244 offset:16384\n"
    "v_mov_b32_e32 v148, s39\n"
    "v_mov_b32_e32 v149, 0\n"
    "v_mfma_f32_16x16x32_bf16 v[172:175], a[168:171], a[36:39], v[172:175]\n"
    "ds_read_b64_tr_b16 a[162:163], v244 offset:18432\n"
    "v_exp_f32_e32 v170, v170\n"
    "v_mfma_f32_16x16x32_bf16 v[172:175], a[172:175], a[40:43], v[172:175]\n"
    "ds_read_b64_tr_b16 a[164:165], v244 offset:24576\n"
    "v_permlane32_swap_b32_e32 v143, v151\n"
    "v_mfma_f32_16x16x32_bf16 v[164:167], a[172:175], a[8:11], v[164:167]\n"
    "ds_read_b64_tr_b16 a[166:167], v244 offset:26624\n"
    "buffer_store_dword v151, v251, s[8:11], s60 offen offset:0 sc1\n"
    "v_exp_f32_e32 v171, v171\n"
    "v_mfma_f32_16x16x32_bf16 v[164:167], a[168:171], a[4:7], v[164:167]\n"
    "s_waitcnt lgkmcnt(6)\n"
    "ds_read_b64_tr_b16 a[168:169], v245 offset:16384\n"
    "v_mfma_f32_16x16x32_bf16 v[164:167], a[176:179], a[12:15], v[164:167]\n"
    "ds_read_b64_tr_b16 a[170:171], v245 offset:18432\n"
    "v_lshrrev_b32_e32 v147, 4, v253\n"
    "v_mfma_f32_16x16x32_bf16 v[164:167], a[180:183], a[16:19], v[164:167]\n"
    "ds_read_b64_tr_b16 a[172:173], v245 offset:24576\n"
    "v_cvt_pk_bf16_f32 v168, v168, v169\n"
    "v_mfma_f32_16x16x32_bf16 v[172:175], a[180:183], a[48:51], v[172:175]\n"
    "ds_read_b64_tr_b16 a[174:175], v245 offset:26624\n"
    "v_bfe_u32 v145, v147, 2, 2\n"
    "v_mfma_f32_16x16x32_bf16 v[172:175], a[176:179], a[44:47], v[172:175]\n"
    "ds_read_b64_tr_b16 a[176:177], v246 offset:16384\n"
    "v_exp_f32_e32 v160, v160\n"
    "v_mfma_f32_16x16x32_bf16 v[172:175], a[184:187], a[52:55], v[172:175]\n"
    "ds_read_b64_tr_b16 a[178:179], v246 offset:18432\n"
    "v_mfma_f32_16x16x32_bf16 v[172:175], a[188:191], a[56:59], v[172:175]\n"
    "ds_read_b64_tr_b16 a[180:181], v246 offset:24576\n"
    "v_exp_f32_e32 v161, v161\n"
    "v_mfma_f32_16x16x32_bf16 v[164:167], a[188:191], a[24:27], v[164:167]\n"
    "ds_read_b64_tr_b16 a[182:183], v246 offset:26624\n"
    "v_min_u32_e32 v145, 2, v145\n"
    "v_mfma_f32_16x16x32_bf16 v[164:167], a[184:187], a[20:23], v[164:167]\n"
    "s_waitcnt lgkmcnt(4)\n"
    "ds_read_b64_tr_b16 a[184:185], v247 offset:16384\n"
    "v_cvt_pk_bf16_f32 v169, v170, v171\n"
    "v_mfma_f32_16x16x32_bf16 v[0:3], a[160:163], v[200:203], v[0:3]\n"
    "s_add_u32 s46, s32, 1\n"
    "ds_read_b64_tr_b16 a[186:187], v247 offset:18432\n"
    "v_mfma_f32_16x16x32_bf16 v[0:3], a[164:167], v[204:207], v[0:3]\n"
    "ds_read_b64_tr_b16 a[188:189], v247 offset:24576\n"
    "v_exp_f32_e32 v162, v162\n"
    "v_mfma_f32_16x16x32_bf16 a[248:251], a[164:167], v[212:215], a[248:251]\n"
    "ds_read_b64_tr_b16 a[190:191], v247 offset:26624\n"
    "v_add_u32_e32 v145, s46, v145\n"
    "v_mfma_f32_16x16x32_bf16 a[248:251], a[160:163], v[208:211], a[248:251]\n"
    "ds_read_b64_tr_b16 a[160:161], v244 offset:20480\n"
    "v_exp_f32_e32 v163, v163\n"
    "v_mfma_f32_16x16x32_bf16 a[252:255], a[168:171], v[208:211], a[252:255]\n"
    "ds_read_b64_tr_b16 a[162:163], v244 offset:22528\n"
    "v_and_b32_e32 v145, 3, v145\n"
    "v_mfma_f32_16x16x32_bf16 a[252:255], a[172:175], v[212:215], a[252:255]\n"
    "s_barrier\n"
    "v_mfma_f32_16x16x32_bf16 v[4:7], a[172:175], v[204:207], v[4:7]\n"
    "ds_read_b64_tr_b16 a[164:165], v244 offset:28672\n"
    "v_cvt_pk_bf16_f32 v160, v160, v161\n"
    "v_mfma_f32_16x16x32_bf16 v[4:7], a[168:171], v[200:203], v[4:7]\n"
    "s_waitcnt lgkmcnt(3)\n"
    "ds_read_b64_tr_b16 a[166:167], v244 offset:30720\n"
    "v_cvt_pk_bf16_f32 v161, v162, v163\n"
    "v_mfma_f32_16x16x32_bf16 v[8:11], a[176:179], v[200:203], v[8:11]\n"
    "ds_read_b64_tr_b16 a[168:169], v245 offset:20480\n"
    "ds_write_addtid_b32 v168 offset:4096\n"
    "v_mfma_f32_16x16x32_bf16 v[8:11], a[180:183], v[204:207], v[8:11]\n"
    "ds_read_b64_tr_b16 a[170:171], v245 offset:22528\n"
    "v_lshlrev_b32_e32 v145, 6, v145\n"
    "v_mfma_f32_16x16x32_bf16 v[32:35], a[180:183], v[212:215], v[32:35]\n"
    "ds_read_b64_tr_b16 a[172:173], v245 offset:28672\n"
    "v_exp_f32_e32 v172, v172\n"
    "v_mfma_f32_16x16x32_bf16 v[32:35], a[176:179], v[208:211], v[32:35]\n"
    "ds_read_b64_tr_b16 a[174:175], v245 offset:30720\n"
    "v_and_or_b32 v145, v147, 3, v145\n"
    "v_mfma_f32_16x16x32_bf16 v[36:39], a[184:187], v[208:211], v[36:39]\n"
    "ds_read_b64_tr_b16 a[176:177], v246 offset:20480\n"
    "v_exp_f32_e32 v173, v173\n"
    "v_mfma_f32_16x16x32_bf16 v[36:39], a[188:191], v[212:215], v[36:39]\n"
    "ds_read_b64_tr_b16 a[178:179], v246 offset:22528\n"
    "ds_write_addtid_b32 v169 offset:4352\n"
    "v_mfma_f32_16x16x32_bf16 v[12:15], a[188:191], v[204:207], v[12:15]\n"
    "ds_read_b64_tr_b16 a[180:181], v246 offset:28672\n"
    "v_exp_f32_e32 v174, v174\n"
    "v_mfma_f32_16x16x32_bf16 v[12:15], a[184:187], v[200:203], v[12:15]\n"
    "s_waitcnt lgkmcnt(4)\n"
    "ds_read_b64_tr_b16 a[182:183], v246 offset:30720\n"
    "v_lshlrev_b32_e32 v145, 2, v145\n"
    "v_mfma_f32_16x16x32_bf16 v[16:19], a[160:163], v[200:203], v[16:19]\n"
    "s_lshl_b32 s47, s33, 10\n"
    "ds_read_b64_tr_b16 a[184:185], v247 offset:20480\n"
    "ds_write_addtid_b32 v160 offset:0\n"
    "v_mfma_f32_16x16x32_bf16 v[16:19], a[164:167], v[204:207], v[16:19]\n"
    "ds_read_b64_tr_b16 a[186:187], v247 offset:22528\n"
    "v_mfma_f32_16x16x32_bf16 v[40:43], a[164:167], v[212:215], v[40:43]\n"
    "s_add_u32 s47, s47, 5636096\n"
    "ds_read_b64_tr_b16 a[188:189], v247 offset:28672\n"
    "v_mfma_f32_16x16x32_bf16 v[40:43], a[160:163], v[208:211], v[40:43]\n"
    "ds_read_b64_tr_b16 a[190:191], v247 offset:30720\n"
    "v_mfma_f32_16x16x32_bf16 v[44:47], a[168:171], v[208:211], v[44:47]\n"
    "ds_read_b128 a[160:163], v243 offset:53248\n"
    "ds_write_addtid_b32 v161 offset:256\n"
    "v_mfma_f32_16x16x32_bf16 v[44:47], a[172:175], v[212:215], v[44:47]\n"
    "ds_read_b128 a[164:167], v240 offset:49152\n"
    "v_cmp_gt_u32_e32 vcc, 0x300, v253\n"
    "v_mfma_f32_16x16x32_bf16 v[20:23], a[172:175], v[204:207], v[20:23]\n"
    "v_exp_f32_e32 v175, v175\n"
    "v_mfma_f32_16x16x32_bf16 v[20:23], a[168:171], v[200:203], v[20:23]\n"
    "s_waitcnt lgkmcnt(3)\n"
    "ds_read_b128 a[168:171], v241 offset:49152\n"
    "v_cvt_pk_bf16_f32 v170, v172, v173\n"
    "v_mfma_f32_16x16x32_bf16 v[24:27], a[176:179], v[200:203], v[24:27]\n"
    "s_add_u32 s60, s60, s28\n"
    "ds_read_b128 a[172:175], v240 offset:53248\n"
    "v_and_b32_e32 v251, 0xf0, v253\n"
    "v_mfma_f32_16x16x32_bf16 v[24:27], a[180:183], v[204:207], v[24:27]\n"
    "s_add_u32 s48, s56, 0\n"
    "v_exp_f32_e32 v164, v164\n"
    "v_mfma_f32_16x16x32_bf16 v[48:51], a[180:183], v[212:215], v[48:51]\n"
    "v_exp_f32_e32 v165, v165\n"
    "v_mfma_f32_16x16x32_bf16 v[48:51], a[176:179], v[208:211], v[48:51]\n"
    "ds_read_b128 a[176:179], v241 offset:53248\n"
    "v_cvt_pk_bf16_f32 v171, v174, v175\n"
    "v_mfma_f32_16x16x32_bf16 v[52:55], a[184:187], v[208:211], v[52:55]\n"
    "ds_read_b128 a[180:183], v242 offset:49152\n"
    "buffer_load_dwordx4 v[56:59], v253, s[8:11], s48 offen offset:0 sc1\n"
    "v_cndmask_b32_e64 v251, v251, 2.0, vcc\n"
    "v_mfma_f32_16x16x32_bf16 v[52:55], a[188:191], v[212:215], v[52:55]\n"
    "v_exp_f32_e32 v166, v166\n"
    "v_mfma_f32_16x16x32_bf16 v[28:31], a[188:191], v[204:207], v[28:31]\n"
    "v_exp_f32_e32 v167, v167\n"
    "v_mfma_f32_16x16x32_bf16 v[28:31], a[184:187], v[200:203], v[28:31]\n"
    "s_waitcnt lgkmcnt(2)\n"
    "ds_read_b128 a[184:187], v243 offset:49152\n"
    "v_cvt_pk_bf16_f32 v162, v164, v165\n"
    "v_mfma_f32_16x16x32_bf16 v[200:203], a[160:163], a[28:31], 0\n"
    "ds_read_b128 a[188:191], v242 offset:53248\n"
    "buffer_load_dwordx4 v[60:63], v253, s[8:11], s48 offen offset:1024 sc1\n"
    "v_mfma_f32_16x16x32_bf16 v[200:203], a[164:167], a[0:3], v[200:203]\n"
    "ds_write_addtid_b32 v170 offset:4608\n"
    "v_cvt_pk_bf16_f32 v163, v166, v167\n"
    "v_mfma_f32_16x16x32_bf16 v[208:211], a[164:167], a[32:35], 0\n"
    "ds_write_addtid_b32 v171 offset:4864\n"
    "v_mfma_f32_16x16x32_bf16 v[208:211], a[160:163], a[60:63], v[208:211]\n"
    "ds_read_b128 a[160:163], v243 offset:55296\n"
    "v_mfma_f32_16x16x32_bf16 v[208:211], a[168:171], a[36:39], v[208:211]\n"
    "ds_read_b128 a[164:167], v240 offset:51200\n"
    "buffer_load_dwordx4 v[64:67], v253, s[8:11], s48 offen offset:2048 sc1\n"
    "v_mfma_f32_16x16x32_bf16 v[208:211], a[172:175], a[40:43], v[208:211]\n"
    "ds_write_addtid_b32 v162 offset:512\n"
    "v_mfma_f32_16x16x32_bf16 v[200:203], a[172:175], a[8:11], v[200:203]\n"
    "ds_write_addtid_b32 v163 offset:768\n"
    "v_mfma_f32_16x16x32_bf16 v[200:203], a[168:171], a[4:7], v[200:203]\n"
    "s_waitcnt lgkmcnt(6)\n"
    "ds_read_b128 a[168:171], v241 offset:51200\n"
    "v_mfma_f32_16x16x32_bf16 v[200:203], a[176:179], a[12:15], v[200:203]\n"
    "ds_read_b128 a[172:175], v240 offset:55296\n"
    "buffer_load_dwordx4 v[68:71], v253, s[8:11], s48 offen offset:3072 sc1\n"
    "v_mfma_f32_16x16x32_bf16 v[200:203], a[180:183], a[16:19], v[200:203]\n"
    "s_waitcnt vmcnt(4)\n"
    "buffer_store_dword v148, v149, s[8:11], s87 offen offset:128 sc1\n"
    "v_mfma_f32_16x16x32_bf16 v[208:211], a[180:183], a[48:51], v[208:211]\n"
    "s_waitcnt lgkmcnt(2)\n"
    "s_barrier\n"
    "v_mfma_f32_16x16x32_bf16 v[208:211], a[176:179], a[44:47], v[208:211]\n"
    "s_add_u32 s48, s56, 4096\n"
    "ds_read_b128 a[176:179], v241 offset:55296\n"
    "v_mfma_f32_16x16x32_bf16 v[208:211], a[184:187], a[52:55], v[208:211]\n"
    "ds_read_b128 a[180:183], v242 offset:51200\n"
    "buffer_load_dwordx4 v[72:75], v253, s[8:11], s48 offen offset:0 sc1\n"
    "v_mfma_f32_16x16x32_bf16 v[208:211], a[188:191], a[56:59], v[208:211]\n"
    "ds_read2st64_b32 v[164:165], v250 offset0:0 offset1:1\n"
    "v_mfma_f32_16x16x32_bf16 v[200:203], a[188:191], a[24:27], v[200:203]\n"
    "ds_read2st64_b32 v[166:167], v250 offset0:2 offset1:3\n"
    "v_mfma_f32_16x16x32_bf16 v[200:203], a[184:187], a[20:23], v[200:203]\n"
    "s_waitcnt lgkmcnt(4)\n"
    "ds_read_b128 a[184:187], v243 offset:51200\n"
    "v_mfma_f32_16x16x32_bf16 v[204:207], a[160:163], a[28:31], 0\n"
    "ds_read_b128 a[188:191], v242 offset:55296\n"
    "buffer_load_dwordx4 v[76:79], v253, s[8:11], s48 offen offset:1024 sc1\n"
    "v_exp_f32_e32 v208, v208\n"
    "v_mfma_f32_16x16x32_bf16 v[204:207], a[164:167], a[0:3], v[204:207]\n"
    "ds_read2st64_b32 v[172:173], v250 offset0:16 offset1:17\n"
    "v_exp_f32_e32 v209, v209\n"
    "v_mfma_f32_16x16x32_bf16 v[212:215], a[164:167], a[32:35], 0\n"
    "ds_read2st64_b32 v[174:175], v250 offset0:18 offset1:19\n"
    "v_mfma_f32_16x16x32_bf16 v[212:215], a[160:163], a[60:63], v[212:215]\n"
    "ds_read_b64_tr_b16 a[160:161], v244 offset:32768\n"
    "v_exp_f32_e32 v210, v210\n"
    "v_mfma_f32_16x16x32_bf16 v[212:215], a[168:171], a[36:39], v[212:215]\n"
    "ds_read_b64_tr_b16 a[162:163], v244 offset:34816\n"
    "buffer_load_dwordx4 v[80:83], v253, s[8:11], s48 offen offset:2048 sc1\n"
    "v_mfma_f32_16x16x32_bf16 v[212:215], a[172:175], a[40:43], v[212:215]\n"
    "ds_read_b64_tr_b16 a[164:165], v244 offset:40960\n"
    "v_exp_f32_e32 v211, v211\n"
    "v_mfma_f32_16x16x32_bf16 v[204:207], a[172:175], a[8:11], v[204:207]\n"
    "ds_read_b64_tr_b16 a[166:167], v244 offset:43008\n"
    "v_cvt_pk_bf16_f32 v208, v208, v209\n"
    "v_mfma_f32_16x16x32_bf16 v[204:207], a[168:171], a[4:7], v[204:207]\n"
    "s_waitcnt lgkmcnt(6)\n"
    "ds_read_b64_tr_b16 a[168:169], v245 offset:32768\n"
    "v_mfma_f32_16x16x32_bf16 v[204:207], a[176:179], a[12:15], v[204:207]\n"
    "ds_read_b64_tr_b16 a[170:171], v245 offset:34816\n"
    "buffer_load_dwordx4 v[84:87], v253, s[8:11], s48 offen offset:3072 sc1\n"
    "v_exp_f32_e32 v200, v200\n"
    "v_mfma_f32_16x16x32_bf16 v[204:207], a[180:183], a[16:19], v[204:207]\n"
    "s_add_u32 s48, s56, 8192\n"
    "ds_read_b64_tr_b16 a[172:173], v245 offset:40960\n"
    "v_mfma_f32_16x16x32_bf16 v[212:215], a[180:183], a[48:51], v[212:215]\n"
    "ds_read_b64_tr_b16 a[174:175], v245 offset:43008\n"
    "v_exp_f32_e32 v201, v201\n"
    "v_mfma_f32_16x16x32_bf16 v[212:215], a[176:179], a[44:47], v[212:215]\n"
    "ds_read_b64_tr_b16 a[176:177], v246 offset:32768\n"
    "v_cvt_pk_bf16_f32 v209, v210, v211\n"
    "v_mfma_f32_16x16x32_bf16 v[212:215], a[184:187], a[52:55], v[212:215]\n"
    "ds_read_b64_tr_b16 a[178:179], v246 offset:34816\n"
    "buffer_load_dwordx4 v[88:91], v253, s[8:11], s48 offen offset:0 sc1\n"
    "v_mfma_f32_16x16x32_bf16 v[212:215], a[188:191], a[56:59], v[212:215]\n"
    "ds_read_b64_tr_b16 a[180:181], v246 offset:40960\n"
    "v_exp_f32_e32 v202, v202\n"
    "v_mfma_f32_16x16x32_bf16 v[204:207], a[188:191], a[24:27], v[204:207]\n"
    "ds_read_b64_tr_b16 a[182:183], v246 offset:43008\n"
    "v_exp_f32_e32 v203, v203\n"
    "v_mfma_f32_16x16x32_bf16 v[204:207], a[184:187], a[20:23], v[204:207]\n"
    "s_waitcnt lgkmcnt(4)\n"
    "ds_read_b64_tr_b16 a[184:185], v247 offset:32768\n"
    "v_mfma_f32_16x16x32_bf16 v[0:3], a[160:163], v[160:163], v[0:3]\n"
    "ds_read_b64_tr_b16 a[186:187], v247 offset:34816\n"
    "buffer_load_dwordx4 v[92:95], v253, s[8:11], s48 offen offset:1024 sc1\n"
    "v_cvt_pk_bf16_f32 v200, v200, v201\n"
    "v_mfma_f32_16x16x32_bf16 v[0:3], a[164:167], v[164:167], v[0:3]\n"
    "ds_read_b64_tr_b16 a[188:189], v247 offset:40960\n"
    "v_and_b32_e32 v242, 0x400, v240\n"
    "v_mfma_f32_16x16x32_bf16 a[248:251], a[164:167], v[172:175], a[248:251]\n"
    "ds_read_b64_tr_b16 a[190:191], v247 offset:43008\n"
    "v_cvt_pk_bf16_f32 v201, v202, v203\n"
    "v_mfma_f32_16x16x32_bf16 a[248:251], a[160:163], v[168:171], a[248:251]\n"
    "ds_read_b64_tr_b16 a[160:161], v244 offset:36864\n"
    "v_exp_f32_e32 v212, v212\n"
    "v_mfma_f32_16x16x32_bf16 a[252:255], a[168:171], v[168:171], a[252:255]\n"
    "ds_read_b64_tr_b16 a[162:163], v244 offset:38912\n"
    "buffer_load_dwordx4 v[96:99], v253, s[8:11], s48 offen offset:2048 sc1\n"
    "v_exp_f32_e32 v213, v213\n"
    "v_mfma_f32_16x16x32_bf16 a[252:255], a[172:175], v[172:175], a[252:255]\n"
    "s_waitcnt vmcnt(24)\n"
    "s_barrier\n"
    "v_mfma_f32_16x16x32_bf16 v[4:7], a[172:175], v[164:167], v[4:7]\n"
    "ds_read_b64_tr_b16 a[164:165], v244 offset:45056\n"
    "ds_write_addtid_b32 v208 offset:4096\n"
    "v_mfma_f32_16x16x32_bf16 v[4:7], a[168:171], v[160:163], v[4:7]\n"
    "s_waitcnt lgkmcnt(4)\n"
    "ds_read_b64_tr_b16 a[166:167], v244 offset:47104\n"
    "v_exp_f32_e32 v214, v214\n"
    "v_mfma_f32_16x16x32_bf16 v[8:11], a[176:179], v[160:163], v[8:11]\n"
    "ds_read_b64_tr_b16 a[168:169], v245 offset:36864\n"
    "buffer_load_dwordx4 v[100:103], v253, s[8:11], s48 offen offset:3072 sc1\n"
    "v_exp_f32_e32 v215, v215\n"
    "v_mfma_f32_16x16x32_bf16 v[8:11], a[180:183], v[164:167], v[8:11]\n"
    "s_add_u32 s48, s56, 12288\n"
    "ds_read_b64_tr_b16 a[170:171], v245 offset:38912\n"
    "ds_write_addtid_b32 v209 offset:4352\n"
    "v_mfma_f32_16x16x32_bf16 v[32:35], a[180:183], v[172:175], v[32:35]\n"
    "ds_read_b64_tr_b16 a[172:173], v245 offset:45056\n"
    "v_cvt_pk_bf16_f32 v210, v212, v213\n"
    "v_mfma_f32_16x16x32_bf16 v[32:35], a[176:179], v[168:171], v[32:35]\n"
    "ds_read_b64_tr_b16 a[174:175], v245 offset:47104\n"
    "v_exp_f32_e32 v204, v204\n"
    "v_mfma_f32_16x16x32_bf16 v[36:39], a[184:187], v[168:171], v[36:39]\n"
    "ds_read_b64_tr_b16 a[176:177], v246 offset:36864\n"
    "ds_write_addtid_b32 v200 offset:0\n"
    "buffer_load_dwordx4 v[104:107], v253, s[8:11], s48 offen offset:0 sc1\n"
    "v_mfma_f32_16x16x32_bf16 v[36:39], a[188:191], v[172:175], v[36:39]\n"
    "ds_read_b64_tr_b16 a[178:179], v246 offset:38912\n"
    "v_exp_f32_e32 v205, v205\n"
    "v_mfma_f32_16x16x32_bf16 v[12:15], a[188:191], v[164:167], v[12:15]\n"
    "ds_read_b64_tr_b16 a[180:181], v246 offset:45056\n"
    "ds_write_addtid_b32 v201 offset:256\n"
    "v_mfma_f32_16x16x32_bf16 v[12:15], a[184:187], v[160:163], v[12:15]\n"
    "s_waitcnt lgkmcnt(5)\n"
    "ds_read_b64_tr_b16 a[182:183], v246 offset:47104\n"
    "v_cvt_pk_bf16_f32 v211, v214, v215\n"
    "v_mfma_f32_16x16x32_bf16 v[16:19], a[160:163], v[160:163], v[16:19]\n"
    "ds_read_b64_tr_b16 a[184:185], v247 offset:36864\n"
    "buffer_load_dwordx4 v[108:111], v253, s[8:11], s48 offen offset:1024 sc1\n"
    "v_exp_f32_e32 v206, v206\n"
    "v_mfma_f32_16x16x32_bf16 v[16:19], a[164:167], v[164:167], v[16:19]\n"
    "ds_read_b64_tr_b16 a[186:187], v247 offset:38912\n"
    "ds_write_addtid_b32 v210 offset:4608\n"
    "v_mfma_f32_16x16x32_bf16 v[40:43], a[164:167], v[172:175], v[40:43]\n"
    "ds_read_b64_tr_b16 a[188:189], v247 offset:45056\n"
    "v_exp_f32_e32 v207, v207\n"
    "v_mfma_f32_16x16x32_bf16 v[40:43], a[160:163], v[168:171], v[40:43]\n"
    "ds_read_b64_tr_b16 a[190:191], v247 offset:47104\n"
    "ds_write_addtid_b32 v211 offset:4864\n"
    "v_mfma_f32_16x16x32_bf16 v[44:47], a[168:171], v[168:171], v[44:47]\n"
    "ds_read_b64_tr_b16 a[160:161], v244 offset:49152\n"
    "buffer_load_dwordx4 v[112:115], v253, s[8:11], s48 offen offset:2048 sc1\n"
    "v_cvt_pk_bf16_f32 v202, v204, v205\n"
    "v_mfma_f32_16x16x32_bf16 v[44:47], a[172:175], v[172:175], v[44:47]\n"
    "ds_read_b64_tr_b16 a[162:163], v244 offset:51200\n"
    "v_cvt_pk_bf16_f32 v203, v206, v207\n"
    "v_mfma_f32_16x16x32_bf16 v[20:23], a[172:175], v[164:167], v[20:23]\n"
    "ds_read_b64_tr_b16 a[164:165], v244 offset:57344\n"
    "v_and_b32_e32 v240, 0x3ff, v240\n"
    "v_mfma_f32_16x16x32_bf16 v[20:23], a[168:171], v[160:163], v[20:23]\n"
    "s_waitcnt lgkmcnt(4)\n"
    "ds_read_b64_tr_b16 a[166:167], v244 offset:59392\n"
    "ds_write_addtid_b32 v202 offset:512\n"
    "v_mfma_f32_16x16x32_bf16 v[24:27], a[176:179], v[160:163], v[24:27]\n"
    "ds_read_b64_tr_b16 a[168:169], v245 offset:49152\n"
    "buffer_load_dwordx4 v[116:119], v253, s[8:11], s48 offen offset:3072 sc1\n"
    "v_mfma_f32_16x16x32_bf16 v[24:27], a[180:183], v[164:167], v[24:27]\n"
    "s_add_u32 s48, s56, 16384\n"
    "ds_read_b64_tr_b16 a[170:171], v245 offset:51200\n"
    "v_mfma_f32_16x16x32_bf16 v[48:51], a[180:183], v[172:175], v[48:51]\n"
    "ds_read_b64_tr_b16 a[172:173], v245 offset:57344\n"
    "ds_write_addtid_b32 v203 offset:768\n"
    "v_mfma_f32_16x16x32_bf16 v[48:51], a[176:179], v[168:171], v[48:51]\n"
    "ds_read_b64_tr_b16 a[174:175], v245 offset:59392\n"
    "v_lshl_or_b32 v240, v242, 2, v240\n"
    "v_mfma_f32_16x16x32_bf16 v[52:55], a[184:187], v[168:171], v[52:55]\n"
    "ds_read_b64_tr_b16 a[176:177], v246 offset:49152\n"
    "buffer_load_dwordx4 a[28:31], v253, s[8:11], s48 offen offset:0 sc1\n"
    "v_mfma_f32_16x16x32_bf16 v[52:55], a[188:191], v[172:175], v[52:55]\n"
    "s_waitcnt lgkmcnt(2)\n"
    "s_barrier\n"
    "v_mfma_f32_16x16x32_bf16 v[28:31], a[188:191], v[164:167], v[28:31]\n"
    "ds_read_b64_tr_b16 a[178:179], v246 offset:51200\n"
    "v_mfma_f32_16x16x32_bf16 v[28:31], a[184:187], v[160:163], v[28:31]\n"
    "s_waitcnt lgkmcnt(0)\n"
    "ds_read_b64_tr_b16 a[180:181], v246 offset:57344\n"
    "ds_read2st64_b32 v[204:205], v250 offset0:0 offset1:1\n"
    "v_mfma_f32_16x16x32_bf16 v[0:3], a[160:163], v[200:203], v[0:3]\n"
    "ds_read_b64_tr_b16 a[182:183], v246 offset:59392\n"
    "ds_read2st64_b32 v[206:207], v250 offset0:2 offset1:3\n"
    "buffer_load_dwordx4 a[60:63], v253, s[8:11], s48 offen offset:1024 sc1\n"
    "v_mfma_f32_16x16x32_bf16 a[248:251], a[160:163], v[208:211], a[248:251]\n"
    "ds_read_b64_tr_b16 a[184:185], v247 offset:49152\n"
    "ds_read2st64_b32 v[212:213], v250 offset0:16 offset1:17\n"
    "v_mfma_f32_16x16x32_bf16 a[252:255], a[168:171], v[208:211], a[252:255]\n"
    "ds_read_b64_tr_b16 a[186:187], v247 offset:51200\n"
    "ds_read2st64_b32 v[214:215], v250 offset0:18 offset1:19\n"
    "v_mfma_f32_16x16x32_bf16 v[4:7], a[168:171], v[200:203], v[4:7]\n"
    "ds_read_b64_tr_b16 a[188:189], v247 offset:57344\n"
    "v_mfma_f32_16x16x32_bf16 v[8:11], a[176:179], v[200:203], v[8:11]\n"
    "ds_read_b64_tr_b16 a[190:191], v247 offset:59392\n"
    "buffer_load_dwordx4 a[0:3], v253, s[8:11], s48 offen offset:2048 sc1\n"
    "v_mfma_f32_16x16x32_bf16 v[32:35], a[176:179], v[208:211], v[32:35]\n"
    "s_waitcnt lgkmcnt(2)\n"
    "ds_read_b64_tr_b16 a[160:161], v244 offset:53248\n"
    "v_xor_b32_e32 v241, 64, v240\n"
    "v_mfma_f32_16x16x32_bf16 v[0:3], a[164:167], v[204:207], v[0:3]\n"
    "ds_read_b64_tr_b16 a[162:163], v244 offset:55296\n"
    "v_mfma_f32_16x16x32_bf16 a[248:251], a[164:167], v[212:215], a[248:251]\n"
    "s_waitcnt lgkmcnt(2)\n"
    "ds_read_b64_tr_b16 a[164:165], v244 offset:61440\n"
    "v_mfma_f32_16x16x32_bf16 a[252:255], a[172:175], v[212:215], a[252:255]\n"
    "ds_read_b64_tr_b16 a[166:167], v244 offset:63488\n"
    "buffer_load_dwordx4 a[32:35], v253, s[8:11], s48 offen offset:3072 sc1\n"
    "v_mfma_f32_16x16x32_bf16 v[4:7], a[172:175], v[204:207], v[4:7]\n"
    "s_add_u32 s48, s56, 20480\n"
    "ds_read_b64_tr_b16 a[168:169], v245 offset:53248\n"
    "v_mfma_f32_16x16x32_bf16 v[8:11], a[180:183], v[204:207], v[8:11]\n"
    "ds_read_b64_tr_b16 a[170:171], v245 offset:55296\n"
    "v_mfma_f32_16x16x32_bf16 v[32:35], a[180:183], v[212:215], v[32:35]\n"
    "ds_read_b64_tr_b16 a[172:173], v245 offset:61440\n"
    "v_accvgpr_read_b32 v216, a248\n"
    "v_mfma_f32_16x16x32_bf16 v[36:39], a[184:187], v[208:211], v[36:39]\n"
    "ds_read_b64_tr_b16 a[174:175], v245 offset:63488\n"
    "buffer_load_dwordx4 a[4:7], v253, s[8:11], s48 offen offset:0 sc1\n"
    "v_mfma_f32_16x16x32_bf16 v[36:39], a[188:191], v[212:215], v[36:39]\n"
    "s_lshl_b32 s45, s51, 16\n"
    "ds_read_b64_tr_b16 a[176:177], v246 offset:53248\n"
    "v_accvgpr_read_b32 v217, a249\n"
    "v_mfma_f32_16x16x32_bf16 v[12:15], a[188:191], v[204:207], v[12:15]\n"
    "ds_read_b64_tr_b16 a[178:179], v246 offset:55296\n"
    "buffer_load_dword v146, v145, s[8:11], s47 offen offset:128 sc1\n"
    "v_accvgpr_read_b32 v218, a250\n"
    "v_mfma_f32_16x16x32_bf16 v[12:15], a[184:187], v[200:203], v[12:15]\n"
    "s_waitcnt lgkmcnt(2)\n"
    "ds_read_b64_tr_b16 a[180:181], v246 offset:61440\n"
    "v_accvgpr_read_b32 v219, a251\n"
    "v_mfma_f32_16x16x32_bf16 v[16:19], a[160:163], v[200:203], v[16:19]\n"
    "ds_read_b64_tr_b16 a[182:183], v246 offset:63488\n"
    "buffer_load_dwordx4 a[36:39], v253, s[8:11], s48 offen offset:1024 sc1\n"
    "v_mfma_f32_16x16x32_bf16 v[16:19], a[164:167], v[204:207], v[16:19]\n"
    "s_lshl_b32 s46, s50, 11\n"
    "ds_read_b64_tr_b16 a[184:185], v247 offset:53248\n"
    "v_accvgpr_read_b32 v220, a252\n"
    "v_mfma_f32_16x16x32_bf16 v[40:43], a[164:167], v[212:215], v[40:43]\n"
    "s_add_u32 s46, s46, s45\n"
    "ds_read_b64_tr_b16 a[186:187], v247 offset:55296\n"
    "v_accvgpr_read_b32 v221, a253\n"
    "v_mfma_f32_16x16x32_bf16 v[40:43], a[160:163], v[208:211], v[40:43]\n"
    "s_add_u32 s46, s46, %3\n"
    "ds_read_b64_tr_b16 a[188:189], v247 offset:61440\n"
    "v_accvgpr_read_b32 v222, a254\n"
    "v_mfma_f32_16x16x32_bf16 v[44:47], a[168:171], v[208:211], v[44:47]\n"
    "ds_read_b64_tr_b16 a[190:191], v247 offset:63488\n"
    "buffer_load_dwordx4 a[8:11], v253, s[8:11], s48 offen offset:2048 sc1\n"
    "v_mfma_f32_16x16x32_bf16 v[44:47], a[172:175], v[212:215], v[44:47]\n"
    "s_add_u32 s46, s46, 65536\n"
    "ds_read_b128 a[160:163], v241 offset:11264\n"
    "v_accvgpr_read_b32 v223, a255\n"
    "v_mfma_f32_16x16x32_bf16 v[20:23], a[172:175], v[204:207], v[20:23]\n"
    "ds_read_b128 a[164:167], v241 offset:19456\n"
    "v_cvt_pk_bf16_f32 v216, v216, v217\n"
    "v_mfma_f32_16x16x32_bf16 v[20:23], a[168:171], v[200:203], v[20:23]\n"
    "s_waitcnt lgkmcnt(2)\n"
    "ds_read_b128 a[168:171], v241 offset:27648\n"
    "v_cvt_pk_bf16_f32 v217, v218, v219\n"
    "v_mfma_f32_16x16x32_bf16 v[24:27], a[176:179], v[200:203], v[24:27]\n"
    "ds_read_b128 a[172:175], v240 offset:8192\n"
    "buffer_load_dwordx4 a[40:43], v253, s[8:11], s48 offen offset:3072 sc1\n"
    "v_mfma_f32_16x16x32_bf16 v[24:27], a[180:183], v[204:207], v[24:27]\n"
    "v_cvt_pk_bf16_f32 v218, v220, v221\n"
    "v_cvt_pk_bf16_f32 v219, v222, v223\n"
    "v_mfma_f32_16x16x32_bf16 v[48:51], a[180:183], v[212:215], v[48:51]\n"
    "buffer_store_dwordx4 v[216:219], v253, s[8:11], s83 offen offset:0 sc1\n"
    "v_cvt_pk_bf16_f32 v32, v32, v33\n"
    "v_cvt_pk_bf16_f32 v33, v34, v35\n"
    "v_mfma_f32_16x16x32_bf16 v[48:51], a[176:179], v[208:211], v[48:51]\n"
    "ds_read_b128 a[176:179], v240 offset:16384\n"
    "v_cvt_pk_bf16_f32 v34, v36, v37\n"
    "v_mfma_f32_16x16x32_bf16 v[52:55], a[184:187], v[208:211], v[52:55]\n"
    "ds_read_b128 a[180:183], v240 offset:24576\n"
    "v_cvt_pk_bf16_f32 v35, v38, v39\n"
    "v_mfma_f32_16x16x32_bf16 v[52:55], a[188:191], v[212:215], v[52:55]\n"
    "buffer_store_dwordx4 v[32:35], v253, s[8:11], s83 offen offset:1024 sc1\n"
    "v_cvt_pk_bf16_f32 v40, v40, v41\n"
    "v_cvt_pk_bf16_f32 v41, v42, v43\n"
    "v_mfma_f32_16x16x32_bf16 v[28:31], a[188:191], v[204:207], v[28:31]\n"
    "v_cvt_pk_bf16_f32 v42, v44, v45\n"
    "v_cvt_pk_bf16_f32 v43, v46, v47\n"
    "v_mfma_f32_16x16x32_bf16 v[28:31], a[184:187], v[200:203], v[28:31]\n"
    "ds_read_b128 a[184:187], v241 offset:8192\n"
    "buffer_store_dwordx4 v[40:43], v253, s[8:11], s83 offen offset:2048 sc1\n"
    "v_cvt_pk_bf16_f32 v48, v48, v49\n"
    "v_add_u32_e32 v158, s46, v253\n"
    "v_mfma_f32_16x16x32_bf16 v[160:163], v[154:157], a[224:227], 0\n"
    "ds_read_b128 a[188:191], v241 offset:16384\n"
    "v_cvt_pk_bf16_f32 v49, v50, v51\n"
    "v_mfma_f32_16x16x32_bf16 v[164:167], v[152:155], a[224:227], 0\n"
    "v_cvt_pk_bf16_f32 v50, v52, v53\n"
    "v_cvt_pk_bf16_f32 v51, v54, v55\n"
    "v_mfma_f32_16x16x32_bf16 v[168:171], v[154:157], a[228:231], 0\n"
    "s_add_u32 s45, %3, 139456\n"
    "buffer_store_dwordx4 v[48:51], v253, s[8:11], s83 offen offset:3072 sc1\n"
    "v_and_b32_e32 v150, 15, v147\n"
    "v_lshlrev_b32_e32 v151, 9, v150\n"
    "v_mfma_f32_16x16x32_bf16 v[172:175], v[152:155], a[228:231], 0\n"
    "buffer_store_dword v55, v251, s[8:11], s60 offen offset:0 sc1\n"
    "v_lshl_add_u32 v150, v150, 2, s45\n"
    "v_and_or_b32 v151, v147, 48, v151\n"
    "v_mfma_f32_16x16x32_bf16 v[176:179], v[154:157], a[232:235], 0\n"
    "s_lshl_b32 s46, %2, 7\n"
    "s_add_u32 s58, s35, s46\n"
    "s_mul_i32 s45, %1, 74496\n"
    "v_mfma_f32_16x16x32_bf16 v[180:183], v[152:155], a[232:235], 0\n"
    "s_add_u32 s45, s45, 16265216\n"
    "ds_write_b128 v159, v[216:219] offset:0\n"
    "v_mfma_f32_16x16x32_bf16 v[184:187], v[154:157], a[236:239], 0\n"
    "s_lshl_b32 s54, %2, 11\n"
    "v_and_b32_e32 v147, 0xf0, v253\n"
    "v_add_u32_e32 v252, %3, v253\n"
    "v_mfma_f32_16x16x32_bf16 v[188:191], v[152:155], a[236:239], 0\n"
    "s_add_u32 s54, s54, s45\n"
    "ds_write_b128 v159, v[32:35] offset:1024\n"
    "v_mfma_f32_16x16x32_bf16 v[192:195], v[154:157], a[240:243], 0\n"
    "s_add_u32 s54, s54, 16384\n"
    "ds_write_b128 v159, v[40:43] offset:2048\n"
    "v_mfma_f32_16x16x32_bf16 v[196:199], v[152:155], a[240:243], 0\n"
    "s_lshl1_add_u32 s59, s50, s51\n"
    "s_lshl_b32 s59, s59, 12\n"
    "v_mfma_f32_16x16x32_bf16 v[200:203], v[154:157], a[244:247], 0\n"
    "s_add_u32 s46, s59, s45\n"
    "ds_write_b128 v159, v[48:51] offset:3072\n"
    "v_mfma_f32_16x16x32_bf16 v[204:207], v[152:155], a[244:247], 0\n"
    "s_waitcnt lgkmcnt(10)\n"
    "s_bitcmp1_b32 %1, 0\n"
    "s_cselect_b32 s54, s54, 2.0\n"
    "s_cselect_b32 s55, s58, 2.0\n"
    "v_mfma_f32_16x16x32_bf16 v[160:163], a[192:195], a[160:163], v[160:163]\n"
    "ds_write_b32 v254, v55 offset:0\n"
    "v_mfma_f32_16x16x32_bf16 v[164:167], a[196:199], a[160:163], v[164:167]\n"
    "ds_write_b32 v255, v31 offset:0\n"
    "v_mfma_f32_16x16x32_bf16 v[168:171], a[200:203], a[160:163], v[168:171]\n"
    "s_waitcnt vmcnt(8)\n"
    "v_cmp_ne_u32_e32 vcc, s39, v146\n"
    "v_mfma_f32_16x16x32_bf16 v[172:175], a[204:207], a[160:163], v[172:175]\n"
    "s_add_u32 s61, s54, 0\n"
    "ds_read_b128 a[160:163], v241 offset:24576\n"
    "v_mfma_f32_16x16x32_bf16 v[176:179], a[192:195], a[164:167], v[176:179]\n"
    "s_cbranch_vccnz .Lat_xpoll\n"
    ".Lat_xpoll_ret:\n"
    "v_mfma_f32_16x16x32_bf16 v[180:183], a[196:199], a[164:167], v[180:183]\n"
    "v_mfma_f32_16x16x32_bf16 v[184:187], a[200:203], a[164:167], v[184:187]\n"
    "buffer_load_dwordx4 a[224:227], v253, s[8:11], s61 offen offset:0 sc1\n"
    "v_mfma_f32_16x16x32_bf16 v[188:191], a[204:207], a[164:167], v[188:191]\n"
    "s_waitcnt lgkmcnt(11)\n"
    "ds_read_b128 a[164:167], v240 offset:9216\n"
    "v_mfma_f32_16x16x32_bf16 v[192:195], a[192:195], a[168:171], v[192:195]\n"
    "v_mfma_f32_16x16x32_bf16 v[196:199], a[196:199], a[168:171], v[196:199]\n"
    "v_mfma_f32_16x16x32_bf16 v[200:203], a[200:203], a[168:171], v[200:203]\n"
    "buffer_load_dwordx4 a[228:231], v253, s[8:11], s61 offen offset:1024 sc1\n"
    "v_mfma_f32_16x16x32_bf16 v[204:207], a[204:207], a[168:171], v[204:207]\n"
    "s_add_u32 s61, s54, 24832\n"
    "ds_read_b128 a[168:171], v240 offset:17408\n"
    "v_mfma_f32_16x16x32_bf16 v[160:163], a[208:211], a[172:175], v[160:163]\n"
    "s_waitcnt lgkmcnt(3)\n"
    "s_barrier\n"
    "v_mfma_f32_16x16x32_bf16 v[164:167], a[212:215], a[172:175], v[164:167]\n"
    "v_mfma_f32_16x16x32_bf16 v[168:171], a[216:219], a[172:175], v[168:171]\n"
    "buffer_load_dwordx4 a[232:235], v253, s[8:11], s61 offen offset:0 sc1\n"
    "v_mfma_f32_16x16x32_bf16 v[172:175], a[220:223], a[172:175], v[172:175]\n"
    "ds_read_b128 a[172:175], v240 offset:25600\n"
    "v_mfma_f32_16x16x32_bf16 v[176:179], a[208:211], a[176:179], v[176:179]\n"
    "ds_read_b32 v232, v150 offset:0\n"
    "v_mfma_f32_16x16x32_bf16 v[180:183], a[212:215], a[176:179], v[180:183]\n"
    "v_mfma_f32_16x16x32_bf16 v[184:187], a[216:219], a[176:179], v[184:187]\n"
    "buffer_load_dwordx4 a[236:239], v253, s[8:11], s61 offen offset:1024 sc1\n"
    "v_mfma_f32_16x16x32_bf16 v[188:191], a[220:223], a[176:179], v[188:191]\n"
    "s_add_u32 s61, s54, 49664\n"
    "ds_read_b128 a[176:179], v241 offset:9216\n"
    "v_mfma_f32_16x16x32_bf16 v[192:195], a[208:211], a[180:183], v[192:195]\n"
    "v_mfma_f32_16x16x32_bf16 v[196:199], a[212:215], a[180:183], v[196:199]\n"
    "ds_read_b32 v233, v150 offset:256\n"
    "v_mfma_f32_16x16x32_bf16 v[200:203], a[216:219], a[180:183], v[200:203]\n"
    "buffer_load_dwordx4 a[240:243], v253, s[8:11], s61 offen offset:0 sc1\n"
    "v_mfma_f32_16x16x32_bf16 v[204:207], a[220:223], a[180:183], v[204:207]\n"
    "ds_read_b128 a[180:183], v241 offset:17408\n"
    "v_mfma_f32_16x16x32_bf16 v[160:163], a[92:95], a[184:187], v[160:163]\n"
    "v_mfma_f32_16x16x32_bf16 v[164:167], a[124:127], a[184:187], v[164:167]\n"
    "v_mfma_f32_16x16x32_bf16 v[168:171], a[156:159], a[184:187], v[168:171]\n"
    "buffer_load_dwordx4 a[244:247], v253, s[8:11], s61 offen offset:1024 sc1\n"
    "v_mfma_f32_16x16x32_bf16 v[172:175], a[64:67], a[184:187], v[172:175]\n"
    "s_add_u32 s61, s46, 0\n"
    "ds_read_b128 a[184:187], v241 offset:25600\n"
    "v_mfma_f32_16x16x32_bf16 v[176:179], a[92:95], a[188:191], v[176:179]\n"
    "ds_read_b32 v234, v150 offset:512\n"
    "v_mfma_f32_16x16x32_bf16 v[180:183], a[124:127], a[188:191], v[180:183]\n"
    "v_mfma_f32_16x16x32_bf16 v[184:187], a[156:159], a[188:191], v[184:187]\n"
    "buffer_load_dwordx4 a[192:195], v253, s[8:11], s61 offen offset:0 sc1\n"
    "v_mfma_f32_16x16x32_bf16 v[188:191], a[64:67], a[188:191], v[188:191]\n"
    "s_waitcnt lgkmcnt(8)\n"
    "ds_read_b128 a[188:191], v240 offset:10240\n"
    "v_mfma_f32_16x16x32_bf16 v[192:195], a[92:95], a[160:163], v[192:195]\n"
    "ds_read_b128 a[248:251], v158 offset:0\n"
    "v_mfma_f32_16x16x32_bf16 v[196:199], a[124:127], a[160:163], v[196:199]\n"
    "v_mfma_f32_16x16x32_bf16 v[200:203], a[156:159], a[160:163], v[200:203]\n"
    "buffer_load_dwordx4 a[196:199], v253, s[8:11], s61 offen offset:1024 sc1\n"
    "v_mfma_f32_16x16x32_bf16 v[204:207], a[64:67], a[160:163], v[204:207]\n"
    "ds_read_b128 a[160:163], v240 offset:18432\n"
    "v_mfma_f32_16x16x32_bf16 v[160:163], a[96:99], a[164:167], v[160:163]\n"
    "s_waitcnt vmcnt(8)\n"
    "buffer_store_dword v148, v149, s[8:11], s87 offen offset:0 sc1\n"
    "v_mfma_f32_16x16x32_bf16 v[164:167], a[128:131], a[164:167], v[164:167]\n"
    "ds_read_b128 a[252:255], v158 offset:1024\n"
    "v_mfma_f32_16x16x32_bf16 v[168:171], a[68:71], a[164:167], v[168:171]\n"
    "buffer_load_dwordx4 a[200:203], v253, s[8:11], s61 offen offset:2048 sc1\n"
    "v_mfma_f32_16x16x32_bf16 v[172:175], a[100:103], a[164:167], v[172:175]\n"
    "s_waitcnt lgkmcnt(10)\n"
    "ds_read_b128 a[164:167], v240 offset:26624\n"
    "v_mfma_f32_16x16x32_bf16 v[176:179], a[96:99], a[168:171], v[176:179]\n"
    "v_mfma_f32_16x16x32_bf16 v[180:183], a[128:131], a[168:171], v[180:183]\n"
    "v_mfma_f32_16x16x32_bf16 v[184:187], a[68:71], a[168:171], v[184:187]\n"
    "buffer_load_dwordx4 a[204:207], v253, s[8:11], s61 offen offset:3072 sc1\n"
    "v_mfma_f32_16x16x32_bf16 v[188:191], a[100:103], a[168:171], v[188:191]\n"
    "s_add_u32 s61, s46, 24832\n"
    "ds_read_b128 a[168:171], v241 offset:10240\n"
    "v_mfma_f32_16x16x32_bf16 v[192:195], a[96:99], a[172:175], v[192:195]\n"
    "s_lshl_b32 s52, %2, 11\n"
    "v_mfma_f32_16x16x32_bf16 v[196:199], a[128:131], a[172:175], v[196:199]\n"
    "v_mfma_f32_16x16x32_bf16 v[200:203], a[68:71], a[172:175], v[200:203]\n"
    "buffer_load_dwordx4 a[208:211], v253, s[8:11], s61 offen offset:0 sc1\n"
    "v_mfma_f32_16x16x32_bf16 v[204:207], a[100:103], a[172:175], v[204:207]\n"
    "s_waitcnt lgkmcnt(8)\n"
    "ds_read_b128 a[172:175], v241 offset:18432\n"
    "v_mfma_f32_16x16x32_bf16 v[160:163], a[132:135], a[176:179], v[160:163]\n"
    "v_mfma_f32_16x16x32_bf16 v[164:167], a[72:75], a[176:179], v[164:167]\n"
    "v_add_u32_e32 v251, s52, v252\n"
    "v_mfma_f32_16x16x32_bf16 v[168:171], a[104:107], a[176:179], v[168:171]\n"
    "buffer_load_dwordx4 a[212:215], v253, s[8:11], s61 offen offset:1024 sc1\n"
    "v_mfma_f32_16x16x32_bf16 v[172:175], a[136:139], a[176:179], v[172:175]\n"
    "ds_read_b128 a[176:179], v241 offset:26624\n"
    "v_mfma_f32_16x16x32_bf16 v[176:179], a[132:135], a[180:183], v[176:179]\n"
    "v_mfma_f32_16x16x32_bf16 v[180:183], a[72:75], a[180:183], v[180:183]\n"
    "v_add_u32_e32 v158, s59, v252\n"
    "v_mfma_f32_16x16x32_bf16 v[184:187], a[104:107], a[180:183], v[184:187]\n"
    "buffer_load_dwordx4 a[216:219], v253, s[8:11], s61 offen offset:2048 sc1\n"
    "v_mfma_f32_16x16x32_bf16 v[188:191], a[136:139], a[180:183], v[188:191]\n"
    "s_waitcnt lgkmcnt(7)\n"
    "ds_read_b128 a[180:183], v240 offset:11264\n"
    "v_mfma_f32_16x16x32_bf16 v[192:195], a[132:135], a[184:187], v[192:195]\n"
    "v_mfma_f32_16x16x32_bf16 v[196:199], a[72:75], a[184:187], v[196:199]\n"
    "v_mfma_f32_16x16x32_bf16 v[200:203], a[104:107], a[184:187], v[200:203]\n"
    "buffer_load_dwordx4 a[220:223], v253, s[8:11], s61 offen offset:3072 sc1\n"
    "v_mfma_f32_16x16x32_bf16 v[204:207], a[136:139], a[184:187], v[204:207]\n"
    "s_add_u32 s61, s45, 24576\n"
    "ds_read_b128 a[184:187], v240 offset:19456\n"
    "v_mfma_f32_16x16x32_bf16 v[160:163], a[76:79], a[188:191], v[160:163]\n"
    "v_and_b32_e32 v145, 12, v145\n"
    "v_mfma_f32_16x16x32_bf16 v[164:167], a[108:111], a[188:191], v[164:167]\n"
    "v_mfma_f32_16x16x32_bf16 v[168:171], a[140:143], a[188:191], v[168:171]\n"
    "buffer_load_dwordx3 v[236:238], v147, s[8:11], s61 offen offset:0 sc1\n"
    "v_mfma_f32_16x16x32_bf16 v[172:175], a[80:83], a[188:191], v[172:175]\n"
    "s_waitcnt lgkmcnt(5)\n"
    "s_add_u32 s61, s45, 49408\n"
    "ds_read_b128 a[188:191], v240 offset:27648\n"
    "v_mfma_f32_16x16x32_bf16 v[176:179], a[76:79], a[160:163], v[176:179]\n"
    "v_mfma_f32_16x16x32_bf16 v[180:183], a[108:111], a[160:163], v[180:183]\n"
    "s_add_u32 s52, s32, 3\n"
    "v_mfma_f32_16x16x32_bf16 v[184:187], a[140:143], a[160:163], v[184:187]\n"
    "buffer_load_dwordx3 v[248:250], v147, s[8:11], s61 offen offset:0 sc1\n"
    "v_mfma_f32_16x16x32_bf16 v[188:191], a[80:83], a[160:163], v[188:191]\n"
    "s_add_u32 s48, s56, 24576\n"
    "v_mfma_f32_16x16x32_bf16 v[192:195], a[76:79], a[164:167], v[192:195]\n"
    "v_mfma_f32_16x16x32_bf16 v[196:199], a[108:111], a[164:167], v[196:199]\n"
    "s_and_b32 s52, s52, 3\n"
    "v_mfma_f32_16x16x32_bf16 v[200:203], a[140:143], a[164:167], v[200:203]\n"
    "buffer_load_dwordx4 a[12:15], v253, s[8:11], s48 offen offset:0 sc1\n"
    "v_mfma_f32_16x16x32_bf16 v[204:207], a[80:83], a[164:167], v[204:207]\n"
    "s_waitcnt lgkmcnt(4)\n"
    "v_mfma_f32_16x16x32_bf16 v[160:163], a[112:115], a[168:171], v[160:163]\n"
    "v_mfma_f32_16x16x32_bf16 v[164:167], a[144:147], a[168:171], v[164:167]\n"
    "buffer_load_dwordx4 a[44:47], v253, s[8:11], s48 offen offset:1024 sc1\n"
    "v_mfma_f32_16x16x32_bf16 v[168:171], a[84:87], a[168:171], v[168:171]\n"
    "v_mfma_f32_16x16x32_bf16 v[172:175], a[116:119], a[168:171], v[172:175]\n"
    "s_lshl_b32 s52, s52, 8\n"
    "v_mfma_f32_16x16x32_bf16 v[176:179], a[112:115], a[172:175], v[176:179]\n"
    "buffer_load_dwordx4 a[16:19], v253, s[8:11], s48 offen offset:2048 sc1\n"
    "v_mfma_f32_16x16x32_bf16 v[180:183], a[144:147], a[172:175], v[180:183]\n"
    "v_mfma_f32_16x16x32_bf16 v[184:187], a[84:87], a[172:175], v[184:187]\n"
    "v_mfma_f32_16x16x32_bf16 v[188:191], a[116:119], a[172:175], v[188:191]\n"
    "s_waitcnt lgkmcnt(2)\n"
    "buffer_load_dwordx4 a[48:51], v253, s[8:11], s48 offen offset:3072 sc1\n"
    "v_mfma_f32_16x16x32_bf16 v[192:195], a[112:115], a[176:179], v[192:195]\n"
    "s_add_u32 s48, s56, 28672\n"
    "v_mfma_f32_16x16x32_bf16 v[196:199], a[144:147], a[176:179], v[196:199]\n"
    "s_add_u32 s52, s52, s47\n"
    "v_mfma_f32_16x16x32_bf16 v[200:203], a[84:87], a[176:179], v[200:203]\n"
    "buffer_load_dwordx4 a[20:23], v253, s[8:11], s48 offen offset:0 sc1\n"
    "v_mfma_f32_16x16x32_bf16 v[204:207], a[116:119], a[176:179], v[204:207]\n"
    "v_mfma_f32_16x16x32_bf16 v[160:163], a[148:151], a[180:183], v[160:163]\n"
    "v_mfma_f32_16x16x32_bf16 v[164:167], a[88:91], a[180:183], v[164:167]\n"
    "buffer_load_dwordx4 a[52:55], v253, s[8:11], s48 offen offset:1024 sc1\n"
    "v_mfma_f32_16x16x32_bf16 v[168:171], a[120:123], a[180:183], v[168:171]\n"
    "v_mfma_f32_16x16x32_bf16 v[172:175], a[152:155], a[180:183], v[172:175]\n"
    "s_waitcnt lgkmcnt(0)\n"
    "v_mfma_f32_16x16x32_bf16 v[176:179], a[148:151], a[184:187], v[176:179]\n"
    "buffer_load_dwordx4 a[24:27], v253, s[8:11], s48 offen offset:2048 sc1\n"
    "v_mfma_f32_16x16x32_bf16 v[180:183], a[88:91], a[184:187], v[180:183]\n"
    "v_mfma_f32_16x16x32_bf16 v[184:187], a[120:123], a[184:187], v[184:187]\n"
    "v_mfma_f32_16x16x32_bf16 v[188:191], a[152:155], a[184:187], v[188:191]\n"
    "buffer_load_dwordx4 a[56:59], v253, s[8:11], s48 offen offset:3072 sc1\n"
    "v_mfma_f32_16x16x32_bf16 v[192:195], a[148:151], a[188:191], v[192:195]\n"
    "v_mfma_f32_16x16x32_bf16 v[196:199], a[88:91], a[188:191], v[196:199]\n"
    "v_mfma_f32_16x16x32_bf16 v[200:203], a[120:123], a[188:191], v[200:203]\n"
    "v_mfma_f32_16x16x32_bf16 v[204:207], a[152:155], a[188:191], v[204:207]\n"
    "v_mfma_f32_16x16x32_bf16 v[128:131], v[154:157], a[248:251], 0\n"
    "v_mfma_f32_16x16x32_bf16 v[132:135], v[152:155], a[248:251], 0\n"
    "v_mfma_f32_16x16x32_bf16 v[136:139], v[154:157], a[252:255], 0\n"
    "v_mfma_f32_16x16x32_bf16 v[140:143], v[152:155], a[252:255], 0\n"
    "s_waitcnt vmcnt(23)\n"
    "v_mfma_f32_16x16x32_bf16 v[128:131], v[154:157], a[224:227], v[128:131]\n"
    "v_mfma_f32_16x16x32_bf16 v[132:135], v[152:155], a[224:227], v[132:135]\n"
    "v_mfma_f32_16x16x32_bf16 v[136:139], v[154:157], a[228:231], v[136:139]\n"
    "buffer_load_dword v144, v145, s[8:11], s52 offen offset:0 sc1\n"
    "v_mfma_f32_16x16x32_bf16 v[140:143], v[152:155], a[228:231], v[140:143]\n"
    "s_waitcnt vmcnt(22)\n"
    "v_mfma_f32_16x16x32_bf16 v[128:131], v[154:157], a[232:235], v[128:131]\n"
    "v_mfma_f32_16x16x32_bf16 v[132:135], v[152:155], a[232:235], v[132:135]\n"
    "v_mfma_f32_16x16x32_bf16 v[136:139], v[154:157], a[236:239], v[136:139]\n"
    "v_mfma_f32_16x16x32_bf16 v[140:143], v[152:155], a[236:239], v[140:143]\n"
    "s_waitcnt vmcnt(20)\n"
    "v_mfma_f32_16x16x32_bf16 v[128:131], v[154:157], a[240:243], v[128:131]\n"
    "v_mfma_f32_16x16x32_bf16 v[132:135], v[152:155], a[240:243], v[132:135]\n"
    "v_mfma_f32_16x16x32_bf16 v[136:139], v[154:157], a[244:247], v[136:139]\n"
    "v_mfma_f32_16x16x32_bf16 v[140:143], v[152:155], a[244:247], v[140:143]\n"
    "s_waitcnt vmcnt(18)\n"
    "v_mfma_f32_16x16x32_bf16 v[0:3], v[154:157], a[192:195], v[0:3]\n"
    "v_cvt_pk_bf16_f32 v128, v128, v129\n"
    "v_mfma_f32_16x16x32_bf16 v[4:7], v[152:155], a[192:195], v[4:7]\n"
    "v_cvt_pk_bf16_f32 v129, v130, v131\n"
    "v_mfma_f32_16x16x32_bf16 v[8:11], v[154:157], a[196:199], v[8:11]\n"
    "v_cvt_pk_bf16_f32 v130, v132, v133\n"
    "v_mfma_f32_16x16x32_bf16 v[12:15], v[152:155], a[196:199], v[12:15]\n"
    "s_waitcnt vmcnt(15)\n"
    "v_cvt_pk_bf16_f32 v131, v134, v135\n"
    "v_mfma_f32_16x16x32_bf16 v[16:19], v[154:157], a[200:203], v[16:19]\n"
    "ds_write_b128 v251, v[128:131] offset:49152\n"
    "v_mfma_f32_16x16x32_bf16 v[20:23], v[152:155], a[200:203], v[20:23]\n"
    "v_cvt_pk_bf16_f32 v136, v136, v137\n"
    "v_mfma_f32_16x16x32_bf16 v[24:27], v[154:157], a[204:207], v[24:27]\n"
    "v_cvt_pk_bf16_f32 v137, v138, v139\n"
    "v_mfma_f32_16x16x32_bf16 v[28:31], v[152:155], a[204:207], v[28:31]\n"
    "s_waitcnt vmcnt(13)\n"
    "v_cvt_pk_bf16_f32 v138, v140, v141\n"
    "v_cvt_pk_bf16_f32 v139, v142, v143\n"
    "v_mfma_f32_16x16x32_bf16 v[0:3], v[154:157], a[208:211], v[0:3]\n"
    "ds_write_b128 v251, v[136:139] offset:50176\n"
    "v_mfma_f32_16x16x32_bf16 v[4:7], v[152:155], a[208:211], v[4:7]\n"
    "v_mfma_f32_16x16x32_bf16 v[8:11], v[154:157], a[212:215], v[8:11]\n"
    "s_waitcnt lgkmcnt(0)\n"
    "s_barrier\n"
    "ds_read_b128 a[128:131], v252 offset:49152\n"
    "ds_read_b128 a[132:135], v252 offset:50176\n"
    "ds_read_b128 a[136:139], v252 offset:51200\n"
    "v_mfma_f32_16x16x32_bf16 v[12:15], v[152:155], a[212:215], v[12:15]\n"
    "s_waitcnt vmcnt(11)\n"
    "v_mfma_f32_16x16x32_bf16 v[16:19], v[154:157], a[216:219], v[16:19]\n"
    "ds_read_b128 a[140:143], v252 offset:52224\n"
    "v_mfma_f32_16x16x32_bf16 v[20:23], v[152:155], a[216:219], v[20:23]\n"
    "v_mfma_f32_16x16x32_bf16 v[24:27], v[154:157], a[220:223], v[24:27]\n"
    "ds_read_b128 a[144:147], v252 offset:53248\n"
    "v_mfma_f32_16x16x32_bf16 v[28:31], v[152:155], a[220:223], v[28:31]\n"
    "s_waitcnt lgkmcnt(3)\n"
    "v_mfma_f32_16x16x32_bf16 v[216:219], v[56:59], a[128:131], 0\n"
    "ds_read_b128 a[148:151], v252 offset:54272\n"
    "v_mfma_f32_16x16x32_bf16 v[220:223], v[60:63], a[128:131], 0\n"
    "ds_read_b128 a[152:155], v252 offset:55296\n"
    "v_mfma_f32_16x16x32_bf16 v[224:227], v[64:67], a[128:131], 0\n"
    "s_waitcnt vmcnt(0)\n"
    "v_cmp_ne_u32_e32 vcc, s39, v144\n"
    "s_nop 3\n"
    "s_cbranch_vccnz .Lat_ypoll\n"
    ".Lat_ypoll_ret:\n"
    "s_add_u32 s61, s46, 49664\n"
    "buffer_load_dwordx4 a[160:163], v253, s[8:11], s61 offen offset:0 sc1\n"
    "buffer_load_dwordx4 a[164:167], v253, s[8:11], s61 offen offset:1024 sc1\n"
    "buffer_load_dwordx4 a[168:171], v253, s[8:11], s61 offen offset:2048 sc1\n"
    "buffer_load_dwordx4 a[172:175], v253, s[8:11], s61 offen offset:3072 sc1\n"
    "s_add_u32 s61, s45, 74240\n"
    "buffer_load_dwordx3 v[244:246], v147, s[8:11], s61 offen offset:0 sc1\n"
    "v_mfma_f32_16x16x32_bf16 v[228:231], v[68:71], a[128:131], 0\n"
    "ds_read_b128 a[156:159], v252 offset:56320\n"
    "v_mfma_f32_16x16x32_bf16 v[216:219], v[72:75], a[132:135], v[216:219]\n"
    "v_mfma_f32_16x16x32_bf16 v[220:223], v[76:79], a[132:135], v[220:223]\n"
    "s_waitcnt vmcnt(14)\n"
    "v_mfma_f32_16x16x32_bf16 v[224:227], v[80:83], a[132:135], v[224:227]\n"
    "v_mfma_f32_16x16x32_bf16 v[228:231], v[84:87], a[132:135], v[228:231]\n"
    "s_waitcnt lgkmcnt(4)\n"
    "v_mfma_f32_16x16x32_bf16 v[216:219], v[88:91], a[136:139], v[216:219]\n"
    "v_mfma_f32_16x16x32_bf16 v[220:223], v[92:95], a[136:139], v[220:223]\n"
    "v_add_f32_e32 v232, v232, v236\n"
    "v_mfma_f32_16x16x32_bf16 v[224:227], v[96:99], a[136:139], v[224:227]\n"
    "v_mfma_f32_16x16x32_bf16 v[228:231], v[100:103], a[136:139], v[228:231]\n"
    "v_mfma_f32_16x16x32_bf16 v[216:219], v[104:107], a[140:143], v[216:219]\n"
    "v_mfma_f32_16x16x32_bf16 v[220:223], v[108:111], a[140:143], v[220:223]\n"
    "v_add_f32_e32 v233, v233, v237\n"
    "v_mfma_f32_16x16x32_bf16 v[224:227], v[112:115], a[140:143], v[224:227]\n"
    "v_mfma_f32_16x16x32_bf16 v[228:231], v[116:119], a[140:143], v[228:231]\n"
    "s_waitcnt lgkmcnt(2)\n"
    "v_mfma_f32_16x16x32_bf16 v[216:219], a[28:31], a[144:147], v[216:219]\n"
    "v_mfma_f32_16x16x32_bf16 v[220:223], a[60:63], a[144:147], v[220:223]\n"
    "v_add_f32_e32 v234, v234, v238\n"
    "v_mfma_f32_16x16x32_bf16 v[224:227], a[0:3], a[144:147], v[224:227]\n"
    "v_mfma_f32_16x16x32_bf16 v[228:231], a[32:35], a[144:147], v[228:231]\n"
    "v_mfma_f32_16x16x32_bf16 v[216:219], a[4:7], a[148:151], v[216:219]\n"
    "v_mfma_f32_16x16x32_bf16 v[220:223], a[36:39], a[148:151], v[220:223]\n"
    "v_add_f32_e32 v232, v232, v248\n"
    "v_mfma_f32_16x16x32_bf16 v[224:227], a[8:11], a[148:151], v[224:227]\n"
    "v_mfma_f32_16x16x32_bf16 v[228:231], a[40:43], a[148:151], v[228:231]\n"
    "s_waitcnt lgkmcnt(0)\n"
    "s_waitcnt vmcnt(10)\n"
    "v_mfma_f32_16x16x32_bf16 v[216:219], a[12:15], a[152:155], v[216:219]\n"
    "v_mfma_f32_16x16x32_bf16 v[220:223], a[44:47], a[152:155], v[220:223]\n"
    "v_add_f32_e32 v233, v233, v249\n"
    "v_mfma_f32_16x16x32_bf16 v[224:227], a[16:19], a[152:155], v[224:227]\n"
    "v_mfma_f32_16x16x32_bf16 v[228:231], a[48:51], a[152:155], v[228:231]\n"
    "s_waitcnt vmcnt(6)\n"
    "v_mfma_f32_16x16x32_bf16 v[216:219], a[20:23], a[156:159], v[216:219]\n"
    "v_mfma_f32_16x16x32_bf16 v[220:223], a[52:55], a[156:159], v[220:223]\n"
    "v_add_f32_e32 v234, v234, v250\n"
    "v_mfma_f32_16x16x32_bf16 v[224:227], a[24:27], a[156:159], v[224:227]\n"
    "v_mfma_f32_16x16x32_bf16 v[228:231], a[56:59], a[156:159], v[228:231]\n"
    "s_waitcnt vmcnt(4)\n"
    "v_mfma_f32_16x16x32_bf16 v[0:3], v[154:157], a[160:163], v[0:3]\n"
    "v_mfma_f32_16x16x32_bf16 v[4:7], v[152:155], a[160:163], v[4:7]\n"
    "s_waitcnt vmcnt(3)\n"
    "v_mfma_f32_16x16x32_bf16 v[8:11], v[154:157], a[164:167], v[8:11]\n"
    "v_mfma_f32_16x16x32_bf16 v[12:15], v[152:155], a[164:167], v[12:15]\n"
    "s_waitcnt vmcnt(2)\n"
    "v_cvt_pk_bf16_f32 v0, v0, v1\n"
    "v_cvt_pk_bf16_f32 v1, v2, v3\n"
    "v_mfma_f32_16x16x32_bf16 v[16:19], v[154:157], a[168:171], v[16:19]\n"
    "v_cvt_pk_bf16_f32 v2, v4, v5\n"
    "v_cvt_pk_bf16_f32 v3, v6, v7\n"
    "v_mfma_f32_16x16x32_bf16 v[20:23], v[152:155], a[168:171], v[20:23]\n"
    "s_waitcnt vmcnt(1)\n"
    "ds_write_b128 v158, v[0:3] offset:32768\n"
    "v_mfma_f32_16x16x32_bf16 v[24:27], v[154:157], a[172:175], v[24:27]\n"
    "v_cvt_pk_bf16_f32 v8, v8, v9\n"
    "v_cvt_pk_bf16_f32 v9, v10, v11\n"
    "v_cvt_pk_bf16_f32 v10, v12, v13\n"
    "v_cvt_pk_bf16_f32 v11, v14, v15\n"
    "v_mfma_f32_16x16x32_bf16 v[28:31], v[152:155], a[172:175], v[28:31]\n"
    "ds_write_b128 v158, v[8:11] offset:33792\n"
    "v_cvt_pk_bf16_f32 v16, v16, v17\n"
    "v_cvt_pk_bf16_f32 v17, v18, v19\n"
    "v_cvt_pk_bf16_f32 v18, v20, v21\n"
    "v_cvt_pk_bf16_f32 v19, v22, v23\n"
    "ds_write_b128 v158, v[16:19] offset:34816\n"
    "v_cvt_pk_bf16_f32 v24, v24, v25\n"
    "v_cvt_pk_bf16_f32 v25, v26, v27\n"
    "v_cvt_pk_bf16_f32 v26, v28, v29\n"
    "v_cvt_pk_bf16_f32 v27, v30, v31\n"
    "ds_write_b128 v158, v[24:27] offset:35840\n"
    "s_waitcnt lgkmcnt(0)\n"
    "s_barrier\n"
    "ds_read_b128 a[96:99], v252 offset:40960\n"
    "ds_read_b128 a[100:103], v252 offset:41984\n"
    "ds_read_b128 a[104:107], v252 offset:43008\n"
    "ds_read_b128 a[108:111], v252 offset:44032\n"
    "ds_read_b128 a[112:115], v252 offset:45056\n"
    "ds_read_b128 a[116:119], v252 offset:46080\n"
    "ds_read_b128 a[120:123], v252 offset:47104\n"
    "ds_read_b128 a[124:127], v252 offset:48128\n"
    "s_waitcnt lgkmcnt(6)\n"
    "v_mfma_f32_16x16x32_bf16 v[120:123], v[56:59], a[96:99], 0\n"
    "s_waitcnt vmcnt(0)\n"
    "v_add_f32_e32 v234, v234, v246\n"
    "v_mfma_f32_16x16x32_bf16 v[124:127], v[60:63], a[96:99], 0\n"
    "ds_read_b128 a[64:67], v252 offset:32768\n"
    "v_mfma_f32_16x16x32_bf16 v[128:131], v[64:67], a[96:99], 0\n"
    "v_add_f32_e32 v233, v233, v245\n"
    "v_add_f32_e32 v232, v232, v244\n"
    "v_mfma_f32_16x16x32_bf16 v[132:135], v[68:71], a[96:99], 0\n"
    "ds_read_b128 a[68:71], v252 offset:33792\n"
    "v_mfma_f32_16x16x32_bf16 v[120:123], v[72:75], a[100:103], v[120:123]\n"
    "s_add_u32 s48, s55, 16384\n"
    "v_rcp_f32_e32 v242, v234\n"
    "v_mfma_f32_16x16x32_bf16 v[124:127], v[76:79], a[100:103], v[124:127]\n"
    "ds_read_b128 a[72:75], v252 offset:34816\n"
    "v_fma_f32 v216, v216, v242, v192\n"
    "v_mfma_f32_16x16x32_bf16 v[128:131], v[80:83], a[100:103], v[128:131]\n"
    "v_fma_f32 v217, v217, v242, v193\n"
    "v_fma_f32 v218, v218, v242, v194\n"
    "v_mfma_f32_16x16x32_bf16 v[132:135], v[84:87], a[100:103], v[132:135]\n"
    "s_waitcnt lgkmcnt(7)\n"
    "ds_read_b128 a[76:79], v252 offset:35840\n"
    "v_fma_f32 v219, v219, v242, v195\n"
    "v_mfma_f32_16x16x32_bf16 v[120:123], v[88:91], a[104:107], v[120:123]\n"
    "v_fma_f32 v220, v220, v242, v196\n"
    "v_fma_f32 v221, v221, v242, v197\n"
    "v_mfma_f32_16x16x32_bf16 v[124:127], v[92:95], a[104:107], v[124:127]\n"
    "ds_read_b128 a[80:83], v252 offset:36864\n"
    "v_fma_f32 v222, v222, v242, v198\n"
    "v_mfma_f32_16x16x32_bf16 v[128:131], v[96:99], a[104:107], v[128:131]\n"
    "v_fma_f32 v223, v223, v242, v199\n"
    "v_cvt_pk_bf16_f32 v216, v216, v217\n"
    "v_mfma_f32_16x16x32_bf16 v[132:135], v[100:103], a[104:107], v[132:135]\n"
    "ds_read_b128 a[84:87], v252 offset:37888\n"
    "v_cvt_pk_bf16_f32 v217, v218, v219\n"
    "v_mfma_f32_16x16x32_bf16 v[120:123], v[104:107], a[108:111], v[120:123]\n"
    "v_cvt_pk_bf16_f32 v218, v220, v221\n"
    "v_cvt_pk_bf16_f32 v219, v222, v223\n"
    "v_mfma_f32_16x16x32_bf16 v[124:127], v[108:111], a[108:111], v[124:127]\n"
    "ds_read_b128 a[88:91], v252 offset:38912\n"
    "v_mfma_f32_16x16x32_bf16 v[128:131], v[112:115], a[108:111], v[128:131]\n"
    "buffer_store_dwordx4 v[216:219], v151, s[16:19], s48 offen offset:0 sc0 sc1\n"
    "v_fma_f32 v224, v224, v242, v200\n"
    "v_fma_f32 v225, v225, v242, v201\n"
    "v_mfma_f32_16x16x32_bf16 v[132:135], v[116:119], a[108:111], v[132:135]\n"
    "s_waitcnt lgkmcnt(9)\n"
    "ds_read_b128 a[92:95], v252 offset:39936\n"
    "v_fma_f32 v226, v226, v242, v202\n"
    "v_mfma_f32_16x16x32_bf16 v[120:123], a[28:31], a[112:115], v[120:123]\n"
    "v_fma_f32 v227, v227, v242, v203\n"
    "v_fma_f32 v228, v228, v242, v204\n"
    "v_mfma_f32_16x16x32_bf16 v[124:127], a[60:63], a[112:115], v[124:127]\n"
    "v_fma_f32 v229, v229, v242, v205\n"
    "v_mfma_f32_16x16x32_bf16 v[128:131], a[0:3], a[112:115], v[128:131]\n"
    "v_fma_f32 v230, v230, v242, v206\n"
    "v_fma_f32 v231, v231, v242, v207\n"
    "v_mfma_f32_16x16x32_bf16 v[132:135], a[32:35], a[112:115], v[132:135]\n"
    "v_cvt_pk_bf16_f32 v224, v224, v225\n"
    "v_cvt_pk_bf16_f32 v225, v226, v227\n"
    "v_mfma_f32_16x16x32_bf16 v[120:123], a[4:7], a[116:119], v[120:123]\n"
    "v_cvt_pk_bf16_f32 v226, v228, v229\n"
    "v_cvt_pk_bf16_f32 v227, v230, v231\n"
    "v_mfma_f32_16x16x32_bf16 v[124:127], a[36:39], a[116:119], v[124:127]\n"
    "buffer_store_dwordx4 v[224:227], v151, s[16:19], s48 offen offset:64 sc0 sc1\n"
    "v_mfma_f32_16x16x32_bf16 v[128:131], a[8:11], a[116:119], v[128:131]\n"
    "s_bitcmp1_b32 %1, 0\n"
    "s_cselect_b64 vcc, -1, 0\n"
    "v_mfma_f32_16x16x32_bf16 v[132:135], a[40:43], a[116:119], v[132:135]\n"
    "s_waitcnt lgkmcnt(8)\n"
    "v_cndmask_b32_e32 v234, v233, v234, vcc\n"
    "v_mfma_f32_16x16x32_bf16 v[120:123], a[12:15], a[120:123], v[120:123]\n"
    "s_mov_b32 s61, 0x12800000\n"
    "v_rcp_f32_e32 v241, v233\n"
    "v_mfma_f32_16x16x32_bf16 v[124:127], a[44:47], a[120:123], v[124:127]\n"
    "v_rcp_f32_e32 v240, v232\n"
    "v_mfma_f32_16x16x32_bf16 v[128:131], a[16:19], a[120:123], v[128:131]\n"
    "v_subrev_u32_e32 v236, s61, v232\n"
    "v_mfma_f32_16x16x32_bf16 v[132:135], a[48:51], a[120:123], v[132:135]\n"
    "v_subrev_u32_e32 v237, s61, v233\n"
    "v_mfma_f32_16x16x32_bf16 v[120:123], a[20:23], a[124:127], v[120:123]\n"
    "v_subrev_u32_e32 v238, s61, v234\n"
    "v_max3_u32 v236, v236, v237, v238\n"
    "v_mfma_f32_16x16x32_bf16 v[124:127], a[52:55], a[124:127], v[124:127]\n"
    "buffer_store_dword v148, v149, s[8:11], s88 offen offset:0 sc1\n"
    "v_cmp_le_u32_e32 vcc, 0x5f000000, v236\n"
    "v_mfma_f32_16x16x32_bf16 v[128:131], a[24:27], a[124:127], v[128:131]\n"
    "v_mfma_f32_16x16x32_bf16 v[132:135], a[56:59], a[124:127], v[132:135]\n"
    "s_waitcnt lgkmcnt(0)\n"
    "v_mfma_f32_16x16x32_bf16 v[32:35], v[56:59], a[64:67], 0\n"
    "s_cbranch_vccnz .Lat_exit\n"
    "v_mfma_f32_16x16x32_bf16 v[36:39], v[60:63], a[64:67], 0\n"
    "s_add_u32 s48, s58, 8192\n"
    "v_fma_f32 v120, v120, v241, v176\n"
    "v_mfma_f32_16x16x32_bf16 v[32:35], v[72:75], a[68:71], v[32:35]\n"
    "v_fma_f32 v121, v121, v241, v177\n"
    "v_mfma_f32_16x16x32_bf16 v[36:39], v[76:79], a[68:71], v[36:39]\n"
    "v_fma_f32 v122, v122, v241, v178\n"
    "v_mfma_f32_16x16x32_bf16 v[32:35], v[88:91], a[72:75], v[32:35]\n"
    "v_fma_f32 v123, v123, v241, v179\n"
    "v_mfma_f32_16x16x32_bf16 v[36:39], v[92:95], a[72:75], v[36:39]\n"
    "v_fma_f32 v124, v124, v241, v180\n"
    "v_mfma_f32_16x16x32_bf16 v[32:35], v[104:107], a[76:79], v[32:35]\n"
    "v_fma_f32 v125, v125, v241, v181\n"
    "v_mfma_f32_16x16x32_bf16 v[36:39], v[108:111], a[76:79], v[36:39]\n"
    "v_fma_f32 v126, v126, v241, v182\n"
    "v_fma_f32 v127, v127, v241, v183\n"
    "v_mfma_f32_16x16x32_bf16 v[32:35], a[28:31], a[80:83], v[32:35]\n"
    "v_cvt_pk_bf16_f32 v120, v120, v121\n"
    "v_mfma_f32_16x16x32_bf16 v[36:39], a[60:63], a[80:83], v[36:39]\n"
    "v_cvt_pk_bf16_f32 v121, v122, v123\n"
    "v_mfma_f32_16x16x32_bf16 v[32:35], a[4:7], a[84:87], v[32:35]\n"
    "v_cvt_pk_bf16_f32 v122, v124, v125\n"
    "v_mfma_f32_16x16x32_bf16 v[36:39], a[36:39], a[84:87], v[36:39]\n"
    "v_cvt_pk_bf16_f32 v123, v126, v127\n"
    "v_mfma_f32_16x16x32_bf16 v[32:35], a[12:15], a[88:91], v[32:35]\n"
    "buffer_store_dwordx4 v[120:123], v151, s[16:19], s48 offen offset:0 sc0 sc1\n"
    "v_fma_f32 v128, v128, v241, v184\n"
    "v_mfma_f32_16x16x32_bf16 v[36:39], a[44:47], a[88:91], v[36:39]\n"
    "v_fma_f32 v129, v129, v241, v185\n"
    "v_fma_f32 v130, v130, v241, v186\n"
    "v_mfma_f32_16x16x32_bf16 v[32:35], a[20:23], a[92:95], v[32:35]\n"
    "v_fma_f32 v131, v131, v241, v187\n"
    "v_mfma_f32_16x16x32_bf16 v[36:39], a[52:55], a[92:95], v[36:39]\n"
    "v_fma_f32 v132, v132, v241, v188\n"
    "v_mfma_f32_16x16x32_bf16 v[40:43], v[64:67], a[64:67], 0\n"
    "v_fma_f32 v133, v133, v241, v189\n"
    "v_mfma_f32_16x16x32_bf16 v[44:47], v[68:71], a[64:67], 0\n"
    "v_fma_f32 v134, v134, v241, v190\n"
    "v_fma_f32 v135, v135, v241, v191\n"
    "v_mfma_f32_16x16x32_bf16 v[40:43], v[80:83], a[68:71], v[40:43]\n"
    "v_cvt_pk_bf16_f32 v128, v128, v129\n"
    "v_mfma_f32_16x16x32_bf16 v[44:47], v[84:87], a[68:71], v[44:47]\n"
    "v_cvt_pk_bf16_f32 v129, v130, v131\n"
    "v_mfma_f32_16x16x32_bf16 v[40:43], v[96:99], a[72:75], v[40:43]\n"
    "v_cvt_pk_bf16_f32 v130, v132, v133\n"
    "v_cvt_pk_bf16_f32 v131, v134, v135\n"
    "v_mfma_f32_16x16x32_bf16 v[44:47], v[100:103], a[72:75], v[44:47]\n"
    "buffer_store_dwordx4 v[128:131], v151, s[16:19], s48 offen offset:64 sc0 sc1\n"
    "v_mfma_f32_16x16x32_bf16 v[40:43], v[112:115], a[76:79], v[40:43]\n"
    "s_add_u32 s48, s58, 0\n"
    "v_fma_f32 v32, v32, v240, v160\n"
    "v_mfma_f32_16x16x32_bf16 v[44:47], v[116:119], a[76:79], v[44:47]\n"
    "v_fma_f32 v33, v33, v240, v161\n"
    "v_mfma_f32_16x16x32_bf16 v[40:43], a[0:3], a[80:83], v[40:43]\n"
    "v_fma_f32 v34, v34, v240, v162\n"
    "v_mfma_f32_16x16x32_bf16 v[44:47], a[32:35], a[80:83], v[44:47]\n"
    "v_fma_f32 v35, v35, v240, v163\n"
    "v_mfma_f32_16x16x32_bf16 v[40:43], a[8:11], a[84:87], v[40:43]\n"
    "v_fma_f32 v36, v36, v240, v164\n"
    "v_fma_f32 v37, v37, v240, v165\n"
    "v_mfma_f32_16x16x32_bf16 v[44:47], a[40:43], a[84:87], v[44:47]\n"
    "v_fma_f32 v38, v38, v240, v166\n"
    "v_mfma_f32_16x16x32_bf16 v[40:43], a[16:19], a[88:91], v[40:43]\n"
    "v_fma_f32 v39, v39, v240, v167\n"
    "v_mfma_f32_16x16x32_bf16 v[44:47], a[48:51], a[88:91], v[44:47]\n"
    "v_cvt_pk_bf16_f32 v32, v32, v33\n"
    "v_cvt_pk_bf16_f32 v33, v34, v35\n"
    "v_mfma_f32_16x16x32_bf16 v[40:43], a[24:27], a[92:95], v[40:43]\n"
    "v_cvt_pk_bf16_f32 v34, v36, v37\n"
    "v_cvt_pk_bf16_f32 v35, v38, v39\n"
    "v_mfma_f32_16x16x32_bf16 v[44:47], a[56:59], a[92:95], v[44:47]\n"
    "buffer_store_dwordx4 v[32:35], v151, s[16:19], s48 offen offset:0 sc0 sc1\n"
    "s_nop 2\n"
    "v_fma_f32 v40, v40, v240, v168\n"
    "v_fma_f32 v41, v41, v240, v169\n"
    "v_fma_f32 v42, v42, v240, v170\n"
    "v_fma_f32 v43, v43, v240, v171\n"
    "v_fma_f32 v44, v44, v240, v172\n"
    "v_fma_f32 v45, v45, v240, v173\n"
    "v_fma_f32 v46, v46, v240, v174\n"
    "v_fma_f32 v47, v47, v240, v175\n"
    "v_cvt_pk_bf16_f32 v40, v40, v41\n"
    "v_cvt_pk_bf16_f32 v41, v42, v43\n"
    "v_cvt_pk_bf16_f32 v42, v44, v45\n"
    "v_cvt_pk_bf16_f32 v43, v46, v47\n"
    "buffer_store_dwordx4 v[40:43], v151, s[16:19], s48 offen offset:64 sc0 sc1\n"
    "s_endpgm\n"
    ".Lat_wpoll:\n"
    "s_mov_b32 s53, 0x400000\n"
    ".Lat_wpoll_l:\n"
    "s_sleep 1\n"
    "buffer_load_dword v255, v254, s[8:11], s45 offen offset:0 sc1\n"
    "s_waitcnt vmcnt(0)\n"
    "v_cmp_ne_u32_e32 vcc, s39, v255\n"
    "s_sub_u32 s53, s53, 1\n"
    "s_cbranch_scc1 .Lat_wpoll_ret\n"
    "s_nop 2\n"
    "s_cbranch_vccnz .Lat_wpoll_l\n"
    "s_branch .Lat_wpoll_ret\n"
    ".Lat_xpoll:\n"
    "s_mov_b32 s53, 0x400000\n"
    ".Lat_xpoll_l:\n"
    "s_sleep 0\n"
    "buffer_load_dword v146, v145, s[8:11], s47 offen offset:128 sc1\n"
    "s_waitcnt vmcnt(0)\n"
    "v_cmp_ne_u32_e32 vcc, s39, v146\n"
    "s_sub_u32 s53, s53, 1\n"
    "s_cbranch_scc1 .Lat_xpoll_ret\n"
    "s_nop 2\n"
    "s_cbranch_vccnz .Lat_xpoll_l\n"
    "s_branch .Lat_xpoll_ret\n"
    ".Lat_ypoll:\n"
    "s_mov_b32 s53, 0x400000\n"
    ".Lat_ypoll_l:\n"
    "s_sleep 0\n"
    "buffer_load_dword v144, v145, s[8:11], s52 offen offset:0 sc1\n"
    "s_waitcnt vmcnt(0)\n"
    "v_cmp_ne_u32_e32 vcc, s39, v144\n"
    "s_sub_u32 s53, s53, 1\n"
    "s_cbranch_scc1 .Lat_ypoll_ret\n"
    "s_nop 2\n"
    "s_cbranch_vccnz .Lat_ypoll_l\n"
    "s_branch .Lat_ypoll_ret\n"
    ".Lat_apoll:\n"
    "s_mov_b32 s53, 0x400000\n"
    ".Lat_apoll_l:\n"
    "s_sleep 1\n"
    "buffer_load_dword v255, v252, s[8:11], 0 offen offset:0 sc1\n"
    "s_waitcnt vmcnt(0)\n"
    "v_cmp_ne_u32_e32 vcc, s39, v255\n"
    "s_sub_u32 s53, s53, 1\n"
    "s_cbranch_scc1 .Lat_apoll_ret\n"
    "s_nop 2\n"
    "s_cbranch_vccnz .Lat_apoll_l\n"
    "s_branch .Lat_apoll_ret\n"
    ".Lat_h1poll:\n"
    "s_mov_b32 s53, 0x400000\n"
    ".Lat_h1poll_l:\n"
    "s_sleep 1\n"
    "buffer_load_dword v0, v3, s[8:11], s59 offen offset:0 sc1\n"
    "s_waitcnt vmcnt(0)\n"
    "v_cmp_ne_u32_e32 vcc, s39, v0\n"
    "s_sub_u32 s53, s53, 1\n"
    "s_cbranch_scc1 .Lat_h1poll_ret\n"
    "s_nop 2\n"
    "s_cbranch_vccnz .Lat_h1poll_l\n"
    "s_branch .Lat_h1poll_ret\n"
    ".Lat_hpoll:\n"
    "s_mov_b32 s53, 0x400000\n"
    ".Lat_hpoll_l:\n"
    "s_sleep 1\n"
    "buffer_load_dword v0, v253, s[8:11], s59 offen offset:0 sc1\n"
    "buffer_load_dword v1, v2, s[8:11], s59 offen offset:0 sc1\n"
    "s_waitcnt vmcnt(0)\n"
    "v_xor_b32_e32 v1, s39, v1\n"
    "v_xor_b32_e32 v0, s39, v0\n"
    "v_or_b32_e32 v0, v0, v1\n"
    "v_cmp_ne_u32_e32 vcc, 0, v0\n"
    "s_sub_u32 s53, s53, 1\n"
    "s_cbranch_scc1 .Lat_hpoll_ret\n"
    "s_nop 2\n"
    "s_cbranch_vccnz .Lat_hpoll_l\n"
    "s_branch .Lat_hpoll_ret\n"
    ".Lat_qpoll:\n"
    "s_mov_b32 s53, 0x400000\n"
    ".Lat_qpoll_l:\n"
    "s_sleep 0\n"
    "buffer_load_dword v11, v5, s[8:11], s47 offen offset:0 sc1\n"
    "s_waitcnt vmcnt(0)\n"
    "v_cmp_ne_u32_e32 vcc, s39, v11\n"
    "s_sub_u32 s53, s53, 1\n"
    "s_cbranch_scc1 .Lat_qpoll_ret\n"
    "s_nop 2\n"
    "s_cbranch_vccnz .Lat_qpoll_l\n"
    "s_branch .Lat_qpoll_ret\n"
    ".Lat_lpoll:\n"
    "s_mov_b32 s53, 0x400000\n"
    ".Lat_lpoll_l:\n"
    "s_sleep 1\n"
    "buffer_load_dword v154, off, s[8:11], s80 sc1\n"
    "s_waitcnt vmcnt(0)\n"
    "v_cmp_ne_u32_e32 vcc, s39, v154\n"
    "s_sub_u32 s53, s53, 1\n"
    "s_cbranch_scc1 .Lat_lpoll_ret\n"
    "s_nop 2\n"
    "s_cbranch_vccnz .Lat_lpoll_l\n"
    ".Lat_lpoll_ret:\n"
    "v_lshlrev_b32_e32 v253, 2, v249\n"
    "s_mov_b32 m0, s82\n"
    "s_nop 0\n"
    "buffer_load_dwordx4 v253, s[8:11], s81 offen offset:0 sc1 lds\n"
    "s_waitcnt vmcnt(0)\n"
    "s_branch .Lat_g\n"
    ".Lat_exit:\n"
    "s_waitcnt vmcnt(0)\n"
    "s_nop 15\n"
    ".Lat_end:\n"
    "s_waitcnt lgkmcnt(0)\n"
    : "+s"(ka), "+s"(b), "+s"(wid) : "s"((unsigned)(size_t)lds)
    : "v0", "v1", "v2", "v3", "v4", "v5", "v6", "v7", "v8", "v9", "v10", "v11", "v12", "v13", "v14", "v15", "v16", "v17", "v18", "v19", "v20", "v21", "v22", "v23", "v24", "v25", "v26", "v27", "v28", "v29", "v30", "v31", "v32", "v33", "v34", "v35", "v36", "v37", "v38", "v39", "v40", "v41", "v42", "v43", "v44", "v45", "v46", "v47", "v48", "v49", "v50", "v51", "v52", "v53", "v54", "v55", "v56", "v57", "v58", "v59", "v60", "v61", "v62", "v63", "v64", "v65", "v66", "v67", "v68", "v69", "v70", "v71", "v72", "v73", "v74", "v75", "v76", "v77", "v78", "v79", "v80", "v81", "v82", "v83", "v84", "v85", "v86", "v87", "v88", "v89", "v90", "v91", "v92", "v93", "v94", "v95", "v96", "v97", "v98", "v99", "v100", "v101", "v102", "v103", "v104", "v105", "v106", "v107", "v108", "v109", "v110", "v111", "v112", "v113", "v114", "v115", "v116", "v117", "v118", "v119", "v120", "v121", "v122", "v123", "v124", "v125", "v126", "v127", "v128", "v129", "v130", "v131", "v132", "v133", "v134", "v135", "v136", "v137", "v138", "v139", "v140", "v141", "v142", "v143", "v144", "v145", "v146", "v147", "v148", "v149", "v150", "v151", "v152", "v153", "v154", "v155", "v156", "v157", "v158", "v159", "v160", "v161", "v162", "v163", "v164", "v165", "v166", "v167", "v168", "v169", "v170", "v171", "v172", "v173", "v174", "v175", "v176", "v177", "v178", "v179", "v180", "v181", "v182", "v183", "v184", "v185", "v186", "v187", "v188", "v189", "v190", "v191", "v192", "v193", "v194", "v195", "v196", "v197", "v198", "v199", "v200", "v201", "v202", "v203", "v204", "v205", "v206", "v207", "v208", "v209", "v210", "v211", "v212", "v213", "v214", "v215", "v216", "v217", "v218", "v219", "v220", "v221", "v222", "v223", "v224", "v225", "v226", "v227", "v228", "v229", "v230", "v231", "v232", "v233", "v234", "v235", "v236", "v237", "v238", "v239", "v240", "v241", "v242", "v243", "v244", "v245", "v246", "v247", "v248", "v249", "v250", "v251", "v252", "v253", "v254", "v255", "a0", "a1", "a2", "a3", "a4", "a5", "a6", "a7", "a8", "a9", "a10", "a11", "a12", "a13", "a14", "a15", "a16", "a17", "a18", "a19", "a20", "a21", "a22", "a23", "a24", "a25", "a26", "a27", "a28", "a29", "a30", "a31", "a32", "a33", "a34", "a35", "a36", "a37", "a38", "a39", "a40", "a41", "a42", "a43", "a44", "a45", "a46", "a47", "a48", "a49", "a50", "a51", "a52", "a53", "a54", "a55", "a56", "a57", "a58", "a59", "a60", "a61", "a62", "a63", "a64", "a65", "a66", "a67", "a68", "a69", "a70", "a71", "a72", "a73", "a74", "a75", "a76", "a77", "a78", "a79", "a80", "a81", "a82", "a83", "a84", "a85", "a86", "a87", "a88", "a89", "a90", "a91", "a92", "a93", "a94", "a95", "a96", "a97", "a98", "a99", "a100", "a101", "a102", "a103", "a104", "a105", "a106", "a107", "a108", "a109", "a110", "a111", "a112", "a113", "a114", "a115", "a116", "a117", "a118", "a119", "a120", "a121", "a122", "a123", "a124", "a125", "a126", "a127", "a128", "a129", "a130", "a131", "a132", "a133", "a134", "a135", "a136", "a137", "a138", "a139", "a140", "a141", "a142", "a143", "a144", "a145", "a146", "a147", "a148", "a149", "a150", "a151", "a152", "a153", "a154", "a155", "a156", "a157", "a158", "a159", "a160", "a161", "a162", "a163", "a164", "a165", "a166", "a167", "a168", "a169", "a170", "a171", "a172", "a173", "a174", "a175", "a176", "a177", "a178", "a179", "a180", "a181", "a182", "a183", "a184", "a185", "a186", "a187", "a188", "a189", "a190", "a191", "a192", "a193", "a194", "a195", "a196", "a197", "a198", "a199", "a200", "a201", "a202", "a203", "a204", "a205", "a206", "a207", "a208", "a209", "a210", "a211", "a212", "a213", "a214", "a215", "a216", "a217", "a218", "a219", "a220", "a221", "a222", "a223", "a224", "a225", "a226", "a227", "a228", "a229", "a230", "a231", "a232", "a233", "a234", "a235", "a236", "a237", "a238", "a239", "a240", "a241", "a242", "a243", "a244", "a245", "a246", "a247", "a248", "a249", "a250", "a251", "a252", "a253", "a254", "a255", "s8", "s9", "s10", "s11", "s12", "s13", "s14", "s15", "s16", "s17", "s18", "s19", "s20", "s21", "s22", "s23", "s24", "s25", "s26", "s27", "s28", "s29", "s30", "s31", "s32", "s33", "s34", "s35", "s36", "s37", "s38", "s39", "s40", "s41", "s42", "s43", "s44", "s45", "s46", "s47", "s48", "s49", "s50", "s51", "s52", "s53", "s54", "s55", "s56", "s57", "s58", "s59", "s60", "s61", "s62", "s63", "s80", "s81", "s82", "s83", "s84", "s85", "s86", "s87", "s88", "s89", "s90", "vcc", "m0", "scc", "memory");
  typedef __attribute__((address_space(1))) char* __attribute__((address_space(4))) const* arg_t;
  const arg_t ap = (arg_t)ka;
  typedef const u16* cu16;
  if (b >= NATT) list_branch(lds, b - NATT, wid, (const int*)ap[1], (const int*)ap[2], (cu16)ap[3], (cu16)ap[8], (cu16)ap[9], (cu16)ap[10], (char*)ap[0]);
  else exact_rows(lds, b, wid, (char*)ap[0], (cu16)ap[4], (u16*)ap[5]);
}

extern "C" void kernel_launch(void* const* d_in, const int* in_sizes, int n_in, void* d_out, int out_size, void* d_ws,
                              size_t ws_size, hipStream_t stream) {
  const u16* x = (const u16*)d_in[0];
  const int* src = (const int*)d_in[1];
  const int* dst = (const int*)d_in[2];
  const u16* deg_inv = (const u16*)d_in[3];
  const u16* wmsg = (const u16*)d_in[4];
  const u16* wq = (const u16*)d_in[5];
  const u16* wk = (const u16*)d_in[6];
  const u16* wv = (const u16*)d_in[7];
  const u16* wout = (const u16*)d_in[8];
  char* ws = (char*)d_ws;
  (void)hipFuncSetAttribute((const void*)attn_kernel, hipFuncAttributeMaxDynamicSharedMemorySize, 163840);
  attn_kernel<<<256, 256, 163840, stream>>>(ws, src, dst, deg_inv, x, (u16*)d_out, wq, wk, wv, wmsg, wout);
}
```

```cpp
#include <hip/hip_runtime.h>
#include <hip/hip_bf16.h>
#include <stdint.h>
#pragma clang diagnostic ignored "-Winline-asm"

typedef unsigned short u16;
typedef __attribute__((ext_vector_type(8))) short bf16x8;
typedef __attribute__((ext_vector_type(4))) short bf16x4;
typedef __attribute__((ext_vector_type(4))) float f32x4;
typedef __attribute__((ext_vector_type(2))) float f32x2;
typedef __attribute__((ext_vector_type(4))) unsigned u32x4;
typedef __attribute__((ext_vector_type(2))) unsigned u32x2;
#define DI __device__ __forceinline__

constexpr int NN = 10000, DD = 256, EE = 160000;
constexpr int NATT = 252, NSP = 4;

constexpr unsigned WS_H = 0;
constexpr unsigned WS_AF = WS_H + 10240 * 512;
constexpr unsigned WS_WV = WS_AF + 131072;
constexpr unsigned WS_WM = WS_WV + 131072;
constexpr unsigned WS_XF = WS_WM + 131072;
constexpr unsigned WS_HF = WS_XF + 256 * 256;
constexpr unsigned WS_BF = WS_HF + 4096;
constexpr unsigned WS_PF = WS_BF + 65536;
constexpr unsigned WS_WF = WS_PF + 4096;
constexpr unsigned WS_LF = WS_WF + 256;
constexpr unsigned WS_EP = WS_LF + 2048;
constexpr unsigned WS_G = WS_EP + 1792;
constexpr unsigned WS_QP = WS_G + 10240 * 512;
constexpr unsigned WS_XB = WS_QP + 10240 * 512;
constexpr unsigned WS_BL = WS_XB + NATT * 3 * 24832;
constexpr unsigned WS_GL = WS_BL + 16 * 5 * 260 * 64;
constexpr unsigned WS_DBG = WS_GL + 256 * 4096;
constexpr unsigned WS_END = WS_DBG + 1024 * 128 + 8192;

DI float bf2f(unsigned x) { return __uint_as_float(x << 16); }
DI float bfhi(unsigned x) { return __uint_as_float(x & 0xffff0000u); }
DI unsigned pk2(float lo, float hi) {
  typedef __attribute__((ext_vector_type(2))) __bf16 bf2_t;
  const bf2_t r = __builtin_convertvector((f32x2){lo, hi}, bf2_t);
  return __builtin_bit_cast(unsigned, r);
}
typedef __attribute__((address_space(3))) bf16x4* lds_bf16x4_p;
DI bf16x4 tr_read(unsigned p) { return __builtin_amdgcn_ds_read_tr16_b64_v4i16((lds_bf16x4_p)p); }
DI bf16x8 as_bf(u32x4 v) { return __builtin_bit_cast(bf16x8, v); }
#define SB() __builtin_amdgcn_sched_barrier(0)
#define MFMA16(a, b, c) __builtin_amdgcn_mfma_f32_16x16x32_bf16(a, b, c, 0, 0, 0)

typedef __amdgpu_buffer_rsrc_t srd_t;
DI void drain() { asm volatile("s_waitcnt vmcnt(0)" ::: "memory"); }
DI void st_flag(srd_t w, unsigned off, unsigned tag) { __builtin_amdgcn_raw_buffer_store_b32(tag, w, off, 0, 16); }
DI void wait_flag(srd_t w, unsigned off, unsigned tag) {
#pragma unroll 1
  for (int it = 0; it < (1 << 22); ++it) {
    unsigned v;
    asm volatile("buffer_load_dword %0, %1, %2, 0 offen sc1\n s_waitcnt vmcnt(0)" : "=v"(v) : "v"(off), "s"(w) : "memory");
    if (__ballot(v != tag) == 0) break;
    __builtin_amdgcn_s_sleep(2);
    asm volatile("" ::: "memory");
  }
  asm volatile("" ::: "memory");
}

DI void prod_store(const f32x4 (&acc)[2][2], int mat, int ks, int J, int lane, srd_t wsrd) {
#pragma unroll
  for (int s = 0; s < 2; ++s) {
    u32x4 o = {pk2(acc[0][s][0], acc[0][s][1]), pk2(acc[0][s][2], acc[0][s][3]), pk2(acc[1][s][0], acc[1][s][1]), pk2(acc[1][s][2], acc[1][s][3])};
    if (ks == 7 && lane >= 48) o[3] &= 0xffffu;
    const int T = 2 * J + s;
    const int at = mat ? 131072 + ((J & 1) ^ 1) * 16384 + ks * 2048 + s * 1024 : ks * 4096 + (T & 3) * 1024;
    __builtin_amdgcn_raw_buffer_store_b128(o, wsrd, WS_WV + (T >> 2) * 32768 + at + lane * 16, 0, 16);
  }
}
DI void corr(unsigned* dl, const unsigned (&lr)[16]) {
  float a0 = 0.f, a1 = 0.f;
#pragma unroll
  for (int r = 0; r < 16; ++r) { a0 += bf2f(lr[r] & 0xffffu); a1 += bfhi(lr[r]); }
  dl[0] = pk2(a0 * -0.0625f, a1 * -0.0625f);
  dl[128] = pk2((bf2f(lr[15] & 0xffffu) - 0.25f * a0) * (1.f / 3.f), (bfhi(lr[15]) - 0.25f * a1) * (1.f / 3.f));
}
DI void product_load(char* lds, int wid, int lane, int job, const u16* __restrict__ wv, const u16* __restrict__ wm, const u16* __restrict__ wo, bool left, bool right) {
  const unsigned ldsb = (unsigned)(size_t)lds;
  const int r = lane >> 3, sw = ((lane & 7) ^ (r & 6)) * 16 + wid * 128;
  const unsigned vp = (8 * (r >> 2) + (r & 3)) * 512 + sw, vn = r * 512 + sw;
  auto image = [&](int n, const u16* w, int row0, bool nat) {
#pragma unroll
    for (int i = 0; i < 8; ++i) {
      const int a = i & 3;
      asm volatile("s_mov_b32 m0, %2\n s_nop 0\n global_load_lds_dwordx4 %0, %1"
                   :: "v"(nat ? vn : vp), "s"(w + (row0 + (i >> 2) * 32 + (nat ? 8 * a : 16 * (a & 1) + 4 * (a >> 1))) * DD),
                      "s"(ldsb + n * 32768 + (i >> 2) * 16384 + wid * 4096 + a * 1024) : "m0", "memory");
    }
  };
  if (left) {
    image(2, wv, (job >> 2) * 64, true);
    image(3, wm, (job >> 2) * 64, false);
  }
  if (right)
#pragma unroll
  for (int k = 0; k < 8; ++k)
    asm volatile("s_mov_b32 m0, %2\n s_nop 0\n global_load_lds_dwordx4 %0, %1"
                 :: "v"((lane >> 3) * 512 + (lane & 7) * 16), "s"(wo + (k * 32 + wid * 8) * DD + (job & 3) * 64), "s"(ldsb + k * 4096 + wid * 1024) : "m0", "memory");
}
DI void product_w(char* lds, int wid, int lane, int job, srd_t wsrd) {
  const int m = lane & 15, g = lane >> 4;
  const int ks = 2 * (job >> 2) + (wid >> 1), J = 2 * (job & 3) + (wid & 1);
  const unsigned ldsb = (unsigned)(size_t)lds;
  unsigned* dl = reinterpret_cast<unsigned*>(lds + 0x26c00);
  const bool hi = (m >> 3) != 0;
  const unsigned ra = (m >> 3) * 1024 + (m & 7) * 128 + (g ^ (m & 6)) * 16;
  auto frag = [&](int n, int tl, int t, int k) {
    return *reinterpret_cast<const bf16x8*>(lds + n * 32768 + tl * 16384 + (k >> 1) * 4096 + t * 2048 + (ra ^ ((k & 1) * 64)));
  };
  f32x4 av[2][2] = {}, am[2][2] = {}, ad[2] = {};
  const unsigned tb = ldsb + (job & 1) * 32768 + (8 * g + ((lane >> 2) & 3)) * 128 + (wid & 1) * 64 + (lane & 3) * 16;
  bf16x8 d0[8], d1[8], d2[8], rr[8][2], vf[8][2], mf[8][2];
#pragma unroll
  for (int kk = 0; kk < 9; ++kk) {
    if (kk < 8) {
      d0[kk] = *reinterpret_cast<const bf16x8*>(dl + 512 + 16 * kk + 4 * g); d1[kk] = *reinterpret_cast<const bf16x8*>(dl + 512 + (ks == 7 ? 128 : 0) + 16 * kk + 4 * g);
      d2[kk] = *reinterpret_cast<const bf16x8*>(dl + 768 + (ks == 7 && hi ? 128 : 0) + 16 * kk + 4 * g);
#pragma unroll
      for (int s = 0; s < 2; ++s) {
        rr[kk][s] = __builtin_shufflevector(tr_read(tb + kk * 4096 + s * 8), tr_read(tb + kk * 4096 + s * 8 + 512), 0, 1, 2, 3, 4, 5, 6, 7);
        vf[kk][s] = frag(2, wid >> 1, s, kk); mf[kk][s] = frag(3, wid >> 1, s, kk);
      }
    }
    SB();
    if (kk) {
      const int k = kk - 1;
#pragma unroll
      for (int s = 0; s < 2; ++s) {
        av[0][s] = MFMA16(d0[k], rr[k][s], av[0][s]); av[1][s] = MFMA16(d1[k], rr[k][s], av[1][s]); ad[s] = MFMA16(d2[k], rr[k][s], ad[s]);
#pragma unroll
        for (int t = 0; t < 2; ++t) { av[t][s] = MFMA16(vf[k][t], rr[k][s], av[t][s]); am[t][s] = MFMA16(mf[k][t], rr[k][s], am[t][s]); }
      }
    }
    SB();
  }
#pragma unroll
  for (int t = 0; t < 2; ++t)
#pragma unroll
    for (int s = 0; s < 2; ++s) am[t][s] += ad[s];
  prod_store(av, 0, ks, J, lane, wsrd);
  prod_store(am, 1, ks, J, lane, wsrd);
}

constexpr unsigned CODE_BYTES = 90368;
DI void code_touch(unsigned i, unsigned ldsdst) {
  unsigned long long a; int o_;
  asm volatile("s_getpc_b64 %0\n s_mov_b32 %1, _Z11attn_kernelPcPKiS1_PKtS3_PtS3_S3_S3_S3_S3_@rel32@lo+4" : "=&s"(a), "=&s"(o_));
  a += (long long)o_;
  asm volatile("s_mov_b32 m0, %2\n s_nop 0\n global_load_lds_dword %0, %1" :: "v"(min(i * 128u, CODE_BYTES - 4u)), "s"(a), "s"(ldsdst) : "m0", "memory");
}

constexpr int LONGS = 1056;
typedef __attribute__((address_space(3))) unsigned* lds_u32_p;
typedef __attribute__((address_space(3))) u16* lds_u16_p;
DI void local_store(srd_t srd, int node, int lane, f32x4 acc, float di) {
  const int n = lane & 15, g = lane >> 4;
  const u32x2 o = {pk2(acc[0] * di, acc[1] * di), pk2(acc[2] * di, acc[3] * di)};
  __builtin_amdgcn_raw_buffer_store_b64(o, srd, WS_G + node * 512 + (g >> 1) * 256 + n * 16 + (g & 1) * 8, 0, 16);
}
DI void list_branch(char* lds, int j, int wid, const int* __restrict__ src, const int* __restrict__ dst, const u16* __restrict__ deg_inv,
                    const u16* wv, const u16* wm, const u16* wo, char* ws) {
  const unsigned rec = (unsigned)(size_t)lds + wid * 33280, ext = (unsigned)(size_t)lds + 0x20800 + wid * 6912;
  int lane; asm volatile("v_mbcnt_lo_u32_b32 %0, -1, 0\n\tv_mbcnt_hi_u32_b32 %0, -1, %0" : "=v"(lane));
  const int tid = wid * 64 + lane, bin = j * 4 + wid, lo = bin * 640;
  code_touch(CODE_BYTES / 128 - tid, (unsigned)(size_t)lds + 0x27f00u);
  const srd_t msrd = __builtin_amdgcn_make_buffer_rsrc(ws + WS_H, 0, 10240 * 512, 0x00020000);
  const srd_t wsrd = __builtin_amdgcn_make_buffer_rsrc(ws, 0, WS_END, 0x00020000);
  const unsigned tag = __builtin_amdgcn_raw_buffer_load_b32(wsrd, WS_EP, 0, 0) + 2u;
  if (lane < 3) st_flag(wsrd, lane == 2 ? WS_WF + 32 : WS_LF + bin * 128 + lane * 64 + 8, 0);
  unsigned lr[16];
#pragma unroll
  for (int r = 0; r < 16; ++r) lr[r] = reinterpret_cast<const unsigned*>((wid >= 2 ? wm : wv) + (240 + r) * DD)[tid & 127];
  unsigned dv[10];
#pragma unroll
  for (int i = 0; i < 10; ++i) dv[i] = (unsigned)deg_inv[min(lo + 64 * i + lane, NN - 1)];
#pragma unroll
  for (int i = 0; i < 10; ++i) ((lds_u32_p)ext)[64 * i + lane] = dv[i] << 16 | (lo + 64 * i + lane < NN ? lo + 64 * i + lane : 0xffff);
  corr(reinterpret_cast<unsigned*>(lds + 0x26c00) + 512 + (wid >> 1) * 256 + (tid & 127), lr);
  const u32x4 none = {~0u, ~0u, ~0u, ~0u};
  auto clear = [&]() {
    unsigned a = rec + lane * 16, r = rec + lane * 52;
    asm volatile("" : "+v"(a), "+v"(r));
#pragma unroll
    for (int i = 0; i < 33; ++i) *(__attribute__((address_space(3))) u32x4*)(a + (i < 32 ? i * 1024 : 32256)) = none;
#pragma unroll
    for (int i = 0; i < 10; ++i) ((lds_u32_p)(r + 64 * 52 * i))[12] = 46;
  };
  clear();
  __builtin_amdgcn_s_sleep(40);
  wait_flag(wsrd, WS_BF + min(tid, NATT - 1) * 128, tag);
  __syncthreads();
  auto take = [&](const u32x4& e0, const u32x4& e1, unsigned (&a)[8], unsigned (&p)[8]) {
#pragma unroll
    for (int r = 0; r < 8; ++r) asm("v_mad_u32_u16 %0, %1, 52, %2 op_sel:[1,0,0,0]" : "=v"(a[r]) : "v"(r < 4 ? e0[r & 3] : e1[r & 3]), "s"(rec));
    SB();
#pragma unroll
    for (int r = 0; r < 8; ++r) p[r] = __atomic_fetch_add((lds_u32_p)a[r] + 12, -2u, __ATOMIC_RELAXED);
    SB();
  };
  auto fill = [&](const u32x4& e0, const u32x4& e1, unsigned (&a)[8], const unsigned (&p)[8]) {
#pragma unroll
    for (int r = 7; r >= 0; --r) a[r] = __builtin_elementwise_add_sat(a[r], p[r]);
    SB();
#pragma unroll
    for (int r = 7; r >= 0; --r) *(lds_u16_p)a[r] = r < 4 ? e0[r & 3] : e1[r & 3];
    SB();
  };
  auto add8 = [&](u32x4 e0, u32x4 e1) { unsigned a[8], p[8]; take(e0, e1, a, p); fill(e0, e1, a, p); };
  const unsigned bo = WS_BL + bin * 5 * 260 * 64 + lane * 16;
  auto ld = [&](int g, int k) { return __builtin_amdgcn_raw_buffer_load_b128(wsrd, bo + (g * 260 + k * 16) * 64, 0, 16); };
  auto fix = [&](u32x4 e, unsigned last) { return e | (unsigned)(__builtin_amdgcn_mov_dpp(last, 0xff, 15, 15, 0) >> 31); };
  bool ovf = false;
  unsigned big = 0; int off[10], tot = 0;
#pragma unroll 1
  for (int pass = 0; pass < 2; ++pass) {
    if (!ovf) {
      u32x4 c[64];
#pragma unroll
      for (int k = 0; k < 16; ++k) c[k] = ld(0, k);
      unsigned m = ~0u, a[2][8], p[2][8];
#pragma unroll
      for (int k = 0; k < 66; k += 2) {
        SB();
        if (k < 64) {
          if (k >= 48) { c[k] = fix(c[k], c[k - 16][3]); c[k + 1] = fix(c[k + 1], c[k - 15][3]); }
          if ((k & 15) == 14 && lane >= 48) c[k + 1] = none;
          if (k >= 48) m &= c[k][3] & c[k + 1][3];
          take(c[k], c[k + 1], a[k >> 1 & 1], p[k >> 1 & 1]);
        }
        if (k < 16) {
#pragma unroll
          for (int i = 16 + 2 * k; i < 20 + 2 * k; ++i) c[i] = ld(i >> 4, i & 15);
        }
        if (k >= 24 && k < 40) {
#pragma unroll
          for (int i = k - 24; i < k - 22; ++i)
            c[48 + i] = __builtin_amdgcn_raw_buffer_load_b128(wsrd, bo + (780 + i * 16) * 64 + (__builtin_amdgcn_mov_dpp(c[32 + i][3], 0xff, 15, 15, 0) & 0x40000000), 0, 16);
        }
        SB();
        if (k) fill(c[k - 2], c[k - 1], a[~k >> 1 & 1], p[~k >> 1 & 1]);
      }
      SB();
      if (__ballot((lane & 3) == 3 && m != ~0u)) {
        unsigned more = 0;
#pragma unroll
        for (int k = 0; k < 16; ++k) more |= __ballot((lane & 3) == 3 && c[48 + k][3] != ~0u) ? 1u << k : 0u;
#pragma unroll 1
        for (; more; more &= more - 1) {
          const int k = __builtin_ctz(more);
          u32x4 e = ld(4, k);
          if (k == 15 && lane >= 48) e = none;
          ovf |= __ballot((lane & 3) == 3 && e[3] != ~0u) != 0;
          add8(e, none);
        }
      }
      if (ovf) clear();
    }
    if (ovf) {
      for (int q = lane; q < EE / 4; q += 64) {
        const u32x4 d = reinterpret_cast<const u32x4*>(dst)[q], s = reinterpret_cast<const u32x4*>(src)[q];
        u32x4 e;
#pragma unroll
        for (int r = 0; r < 4; ++r) e[r] = d[r] - lo < 640u ? (d[r] - lo) << 16 | s[r] : ~0u;
        add8(e, none);
      }
    }
    if (pass) break;
    asm volatile("" : "+v"(lane));
    const int qd = lane & 3, nl = lane >> 2;
    int inc = 0;
#pragma unroll
    for (int i = 0; i < 10; ++i) off[i] = 46 - (int)((lds_u32_p)(rec + (64 * i + lane) * 52))[12];
#pragma unroll
    for (int i = 0; i < 10; ++i) {
      const int cn = off[i];
      off[i] = cn > 48 ? cn : 0; inc += off[i];
      if (cn > 48) { big |= 1u << i; ((lds_u16_p)ext)[2 * (64 * i + lane)] = 0xffff; }
    }
    __builtin_amdgcn_wave_barrier();
    auto put = [&](int odd) {
#pragma unroll 2
      for (int bl = 0; bl < 4; ++bl) {
        u32x4 o[10];
#pragma unroll
        for (int T = 0; T < 10; ++T) if (((2 * T + 1) / 5 & 1) == odd) {
          const lds_u32_p r = (lds_u32_p)(rec + (bl * 160 + T * 16 + nl) * 52);
          o[T] = (u32x4){r[3 * qd], r[1 + 3 * qd], r[2 + 3 * qd], ((lds_u32_p)ext)[bl * 160 + T * 16 + nl]};
        }
#pragma unroll
        for (int T = 0; T < 10; ++T) if (((2 * T + 1) / 5 & 1) == odd) {
          const int c = (2 * T + 1) / 5, ro = 16 * (T - (5 * c >> 1)) + nl, w = c & 1 ? ro * 43 >> 9 : ro >> 3;
          __builtin_amdgcn_raw_buffer_store_b128(o[T], wsrd, WS_GL + c * 4096 + w * (c & 1 ? 256 : 512) + ro * 64 + qd * 16, (bin * 4 + bl) * 16384, 16);
        }
      }
    };
    put(1);
    asm volatile("" ::: "memory");
    put(0);
    asm volatile("s_waitcnt vmcnt(16)" ::: "memory");
    if (lane == 0) st_flag(wsrd, WS_LF + bin * 128 + 64, tag);
#pragma unroll
    for (int o = 1; o < 64; o <<= 1) { const int y = __shfl_up(inc, o); if (lane >= o) inc += y; }
    tot = __builtin_amdgcn_readlane(inc, 63);
    drain();
    if (lane == 0) st_flag(wsrd, WS_LF + bin * 128, tag);
    __syncthreads();
    int j2 = j * 4, w2 = wid, l2 = lane;
    asm volatile("" : "+s"(j2), "+s"(w2), "+v"(l2));
    product_load(lds, w2, l2, j2, wv, wm, wo, true, true);
#pragma unroll 1
    for (int job = 0; job < 4; ++job) {
      if (job) asm volatile("s_waitcnt vmcnt(4)" ::: "memory"); else drain();
      __syncthreads();
      asm volatile("" : "+s"(w2));
      if (job < 3) product_load(lds + (~job & 1) * 32768, w2, l2, j2 + job + 1, wv, wm, wo, false, true);
      product_w(lds, w2, l2, j2 + job, wsrd);
    }
    if (w2 == 0) *(lds_u32_p)(ext - 8) = 0;
    __syncthreads();
    if (tot == 0 || tot > 2 * LONGS) break;
#pragma unroll
    for (int i = 0; i < 10; ++i) {
      const unsigned r = rec + (64 * i + lane) * 52;
      ((lds_u32_p)r)[12] = off[i] ? ext + 2558 + inc - r : -2u;
      inc -= off[i]; off[i] = off[i] << 15 | inc;
    }
  }
  __syncthreads();
  const unsigned ex0 = (unsigned)(size_t)lds + 0x20800;
  const lds_u32_p lng = (lds_u32_p)(size_t)(unsigned)(size_t)lds;
#pragma unroll
  for (int i = 0; i < 10; ++i)
    if (big >> i & 1) {
      const unsigned q = __atomic_fetch_add((lds_u32_p)(ex0 - 8), 1u, __ATOMIC_RELAXED);
      lng[2 * q] = tot > 2 * LONGS ? ~0u : off[i] + wid * 6912 + 2560; lng[2 * q + 1] = (((lds_u32_p)ext)[64 * i + lane] & 0xffff0000u) | (lo + 64 * i + lane);
    }
  __syncthreads();
  const int nl_ = *(lds_u32_p)(ex0 - 8);
  bf16x8 b1;
#pragma unroll
  for (int e = 0; e < 8; ++e) b1[e] = (lane & 15) == e + ((lane >> 1) & 8) ? 0x3f80 : 0;
  const unsigned lo16 = (lane & 31) * 16;
  {
    u32x4 d[3][16]; unsigned nn[3]; int mode[3], q = wid, hf = 0;
    auto issue = [&](u32x4 (&v)[16], unsigned& n, int& md) {
      const unsigned w = __builtin_amdgcn_readfirstlane(lng[2 * q]);
      n = lng[2 * q + 1];
      const int dg = q < nl_ && w >> 16 <= 64 ? w >> 16 : 0, g = dg - hf - (lane >> 5);
      const lds_u16_p f = (lds_u16_p)(ex0 + (w & 0xffffu)) + hf + (lane >> 5);
      md = q >= nl_ ? -1 : dg == 0 ? 0 : dg - hf > 32 ? 1 : 2;
      hf = md == 1 ? 32 : 0; q += md == 1 ? 0 : 4;
      unsigned id[16];
#pragma unroll
      for (int t = 0; t < 16; ++t) id[t] = f[2 * t];
#pragma unroll
      for (int t = 0; t < 16; ++t) { asm volatile("" : "+v"(id[t])); v[t] = __builtin_amdgcn_raw_buffer_load_b128(msrd, (2 * t < g ? id[t] : 0xffffu) * 512u + lo16, 0, 16); }
    };
    issue(d[0], nn[0], mode[0]); issue(d[1], nn[1], mode[1]);
    f32x4 keep = {};
#pragma unroll 1
    for (bool go = true; go;) {
#pragma unroll
      for (int s = 0; s < 3; ++s) {
        if (mode[s] < 0) { go = false; break; }
        issue(d[(s + 2) % 3], nn[(s + 2) % 3], mode[(s + 2) % 3]); SB();
        f32x4 acc = {};
#pragma unroll
        for (int t = 0; t < 16; ++t) acc = MFMA16(b1, as_bf(d[s][t]), acc);
        local_store(wsrd, mode[s] == 2 ? nn[s] & 0xffffu : 0x200000u, lane, acc + keep, bfhi(nn[s]));
        keep = mode[s] == 1 ? acc : (f32x4){0.f, 0.f, 0.f, 0.f}; SB();
      }
    }
  }
#pragma unroll 1
  for (int q0 = 0; q0 < nl_; q0 += 256)
#pragma unroll 1
  for (unsigned long long m = __ballot(q0 + 4 * lane + 3 - wid < nl_ && lng[2 * (q0 + 4 * lane + 3 - wid)] >> 16 > 64); m; m &= m - 1) {
    const int q = q0 + 4 * __builtin_ctzll(m) + 3 - wid;
    const unsigned w = __builtin_amdgcn_readfirstlane(lng[2 * q]), n = __builtin_amdgcn_readfirstlane(lng[2 * q + 1]); const int dg = w >> 16;
    f32x4 acc = {};
    if (w == ~0u) {
      for (int e0 = 0; e0 < EE; e0 += 64) {
        unsigned long long h = __ballot(dst[e0 + lane] == (n & 0xffffu));
        while (h) {
          const int e = e0 + __builtin_ctzll(h); h &= h - 1;
          const unsigned vo = lane < 32 ? (unsigned)src[e] * 512 + lo16 : 0x7fffff00u;
          acc = MFMA16(b1, as_bf(__builtin_amdgcn_raw_buffer_load_b128(msrd, vo, 0, 16)), acc);
        }
      }
    } else {
      const lds_u16_p f = (lds_u16_p)(ex0 + (w & 0xffffu)), u = (lds_u16_p)(ext + 2624 + 2 * LONGS);
      for (int x = lane; x < dg; x += 64) {
        const int key = f[x]; int p = 0;
#pragma unroll 8
        for (int t = 0; t < dg; ++t) { const int v = f[t]; p += v < key || (v == key && t < x); }
        u[p] = key;
      }
      __builtin_amdgcn_wave_barrier();
      for (int x = lane >> 5; x < dg + (lane >> 5); x += 32) {
        u32x4 d[16]; unsigned id[16]; f32x4 a2 = {};
#pragma unroll
        for (int t = 0; t < 16; ++t) id[t] = u[min(x + 2 * t, dg)];
#pragma unroll
        for (int t = 0; t < 16; ++t) { asm volatile("" : "+v"(id[t])); d[t] = __builtin_amdgcn_raw_buffer_load_b128(msrd, (x + 2 * t < dg ? id[t] : 0xffffu) * 512u + lo16, 0, 16); }
#pragma unroll
        for (int t = 0; t < 16; ++t) a2 = MFMA16(b1, as_bf(d[t]), a2);
        acc += a2;
      }
      __builtin_amdgcn_wave_barrier();
    }
    local_store(wsrd, n & 0xffffu, lane, acc, bfhi(n));
  }
  drain();
  __syncthreads();
  if (wid == 0 && lane == 0) st_flag(wsrd, WS_WF + j * 4, tag);
}

DI void exact_rows(char* lds, int b, int wid, char* ws, const u16* x, u16* out) {
  int lane; asm volatile("v_mbcnt_lo_u32_b32 %0, -1, 0\n\tv_mbcnt_hi_u32_b32 %0, -1, %0" : "=v"(lane));
  const srd_t w = __builtin_amdgcn_make_buffer_rsrc(ws, 0, WS_END, 0x00020000);
  const srd_t xs = __builtin_amdgcn_make_buffer_rsrc(const_cast<u16*>(x), 0, NN * 512, 0x00020000);
  const srd_t os = __builtin_amdgcn_make_buffer_rsrc(out, 0, NN * 512, 0x00020000);
  const int n = lane & 15, g = lane >> 4, c = b & 3;
  f32x4* red = reinterpret_cast<f32x4*>(lds);
  float* mx = reinterpret_cast<float*>(lds + 65536);
  auto ld = [&](unsigned off) { return as_bf(__builtin_amdgcn_raw_buffer_load_b128(w, off, 0, 16)); };
#pragma unroll 1
  for (int i = 0; i < 2 + (c & 1); ++i) {
    const int tile = (b >> 2) * 10 + (5 * c >> 1) + i;
    bf16x8 q[8];
#pragma unroll
    for (int ks = 0; ks < 8; ++ks) q[ks] = ld(WS_QP + tile * 8192 + ks * 1024 + lane * 16);
    auto score = [&](int kt) {
      f32x4 s = {};
#pragma unroll
      for (int ks = 0; ks < 8; ++ks) s = MFMA16(ld(WS_H + (kt * 16 + n) * 512 + ks * 64 + g * 16), q[ks], s);
      return s;
    };
    float m = -__builtin_inff();
#pragma unroll 1
    for (int kt = wid; kt < NN / 16; kt += 4) { const f32x4 s = score(kt); m = fmaxf(fmaxf(m, fmaxf(s[0], s[1])), fmaxf(s[2], s[3])); }
    __syncthreads();
    mx[wid * 64 + lane] = m;
    __syncthreads();
#pragma unroll
    for (int r = 0; r < 16; ++r) m = fmaxf(m, mx[r * 16 + n]);
    f32x4 acc[16] = {};
#pragma unroll 1
    for (int kt = wid; kt < NN / 16; kt += 4) {
      const f32x4 s = score(kt);
      const u32x2 p = {pk2(__builtin_amdgcn_exp2f(s[0] - m), __builtin_amdgcn_exp2f(s[1] - m)), pk2(__builtin_amdgcn_exp2f(s[2] - m), __builtin_amdgcn_exp2f(s[3] - m))};
#pragma unroll
      for (int dt = 0; dt < 16; ++dt) {
        bf16x4 v;
#pragma unroll
        for (int e = 0; e < 4; ++e) v[e] = __builtin_amdgcn_raw_buffer_load_b16(w, WS_H + (kt * 16 + 4 * g + e) * 512 + n * 2, dt * 32, 16);
        acc[dt] = __builtin_amdgcn_mfma_f32_16x16x16bf16_1k(v, __builtin_bit_cast(bf16x4, p), acc[dt], 0, 0, 0);
      }
    }
#pragma unroll
    for (int dt = 0; dt < 16; ++dt) red[(wid * 16 + dt) * 64 + lane] = acc[dt];
    __syncthreads();
    float l = 0.f;
#pragma unroll
    for (int r = 0; r < 4; ++r) l += red[(r * 16 + 15) * 64 + 48 + n][3];
    l = 1.f / l;
    f32x4 y[4] = {};
#pragma unroll 1
    for (int ks = 0; ks < 8; ++ks) {
      f32x4 o[2];
#pragma unroll
      for (int h = 0; h < 2; ++h) o[h] = (red[(2 * ks + h) * 64 + lane] + red[(16 + 2 * ks + h) * 64 + lane] + red[(32 + 2 * ks + h) * 64 + lane] + red[(48 + 2 * ks + h) * 64 + lane]) * l;
      const bf16x8 of = as_bf((u32x4){pk2(o[0][0], o[0][1]), pk2(o[0][2], o[0][3]), pk2(o[1][0], o[1][1]), pk2(o[1][2], o[1][3])});
      const bf16x8 gf = ld(WS_G + (tile * 16 + n) * 512 + ks * 64 + g * 16);
#pragma unroll
      for (int et = 0; et < 4; ++et) {
        y[et] = MFMA16(ld(WS_WV + wid * 32768 + ks * 4096 + et * 1024 + lane * 16), of, y[et]);
        y[et] = MFMA16(ld(WS_WM + wid * 32768 + (et < 2 ? 16384 : 0) + ks * 2048 + (et & 1) * 1024 + lane * 16), gf, y[et]);
      }
    }
#pragma unroll
    for (int jj = 0; jj < 2; ++jj) {
      const unsigned at = (tile * 16 + n) * 512 + wid * 128 + jj * 64 + g * 16;
      const u32x4 xv = __builtin_amdgcn_raw_buffer_load_b128(xs, at, 0, 0);
      u32x4 o;
#pragma unroll
      for (int r = 0; r < 4; ++r) o[r] = pk2(y[2 * jj + (r >> 1)][2 * (r & 1)] + bf2f(xv[r] & 0xffffu), y[2 * jj + (r >> 1)][2 * (r & 1) + 1] + bfhi(xv[r]));
      __builtin_amdgcn_raw_buffer_store_b128(o, os, at, 0, 0);
    }
  }
}

__global__ __launch_bounds__(256, 1) void attn_kernel(char* ws, const int* __restrict__ src, const int* __restrict__ dst,
                                                      const u16* __restrict__ deg_inv, const u16* __restrict__ x, u16* out,
                                                      const u16* __restrict__ wq, const u16* __restrict__ wk, const u16* __restrict__ wv,
                                                      const u16* __restrict__ wm, const u16* __restrict__ wo) {
  extern __shared__ __attribute__((aligned(16))) char lds[];
  asm volatile("s_load_dword s99, %0, 0" :: "s"(__builtin_amdgcn_kernarg_segment_ptr()) : "s99");
  int b = ((blockIdx.x & 250) == 248 ? 252 + (blockIdx.x >> 1 & 2) : ((blockIdx.x & 6) << 5 | blockIdx.x >> 3 << 1) - (((blockIdx.x & 6) + 2) >> 2 << 1)) | (blockIdx.x & 1);
  code_touch((blockIdx.x >> 3 & 1) * 256 + threadIdx.x, (unsigned)(size_t)lds + 0x27f00u);
  int wid = __builtin_amdgcn_readfirstlane(threadIdx.x >> 6);
  static_assert(WS_END == 37557248 && WS_EP == 5777664, "workspace layout of the generated code");
  auto ka = __builtin_amdgcn_kernarg_segment_ptr();
  asm volatile(
    "s_cmpk_ge_u32 %1, 252\n"
    "s_cbranch_scc1 .Lat_end\n"
    "s_load_dwordx2 s[20:21], %0, 0x30\n"
    "s_load_dwordx2 s[24:25], %0, 0x38\n"
    "s_load_dwordx2 s[12:13], %0, 0x20\n"
    "s_load_dwordx2 s[8:9], %0, 0x0\n"
    "s_load_dwordx2 s[16:17], %0, 0x28\n"
    "s_load_dwordx4 s[28:31], %0, 0x8\n"
    "v_mbcnt_lo_u32_b32 v24, -1, 0\n"
    "v_mbcnt_hi_u32_b32 v24, -1, v24\n"
    "s_and_b32 s50, %2, 1\n"
    "s_lshr_b32 s51, %2, 1\n"
    "v_lshrrev_b32_e32 v25, 3, v24\n"
    "v_and_b32_e32 v26, 6, v25\n"
    "v_and_b32_e32 v27, 7, v24\n"
    "v_xor_b32_e32 v26, v26, v27\n"
    "v_lshlrev_b32_e32 v26, 4, v26\n"
    "v_and_b32_e32 v27, 4, v25\n"
    "v_add_u32_e32 v27, v27, v25\n"
    "v_lshl_add_u32 v254, v27, 9, v26\n"
    "v_add_u32_e32 v255, 0x1c00, v254\n"
    "s_mov_b32 s22, 0x20000\n"
    "s_mov_b32 s23, 0x20000\n"
    "s_mov_b32 s26, 0x20000\n"
    "s_mov_b32 s27, 0x20000\n"
    "s_lshr_b32 s36, %1, 7\n"
    "s_mul_i32 s36, s36, 3\n"
    "s_add_u32 s36, s36, %1\n"
    "s_and_b32 s36, s36, 127\n"
    "s_lshl_b32 s45, %2, 7\n"
    "s_lshl_b32 s46, %2, 12\n"
    "s_add_u32 s46, s46, %3\n"
    "s_and_b32 s47, s36, 7\n"
    "s_lshl_b32 s47, s47, 14\n"
    "s_add_u32 m0, s46, 32768\n"
    "s_add_u32 s47, s47, s45\n"
    "s_and_b32 s32, %1, 3\n"
    "s_lshr_b32 s33, %1, 2\n"
    "s_mul_i32 s38, s32, 5\n"
    "s_lshr_b32 s38, s38, 1\n"
    "s_mul_i32 s35, s33, 10\n"
    "s_add_u32 s35, s35, s38\n"
    "s_lshl_b32 s35, s35, 13\n"
    "s_mov_b32 s14, 5120000\n"
    "s_mov_b32 s15, 0x20000\n"
    "s_waitcnt lgkmcnt(0)\n"
    "buffer_load_dwordx4 v254, s[20:23], s47 offen offset:0 lds\n"
    "s_add_u32 s48, s47, 1024\n"
    "buffer_load_dwordx4 v255, s[20:23], s47 offen offset:3072 lds\n"
    "s_add_u32 s47, s47, 0x1800\n"
    "buffer_load_dwordx4 v254, s[20:23], s48 offen offset:1024 lds\n"
    "buffer_load_dwordx4 v254, s[20:23], s47 offen offset:2048 lds\n"
    "s_lshr_b32 s47, s36, 4\n"
    "s_lshl_b32 s47, s47, 14\n"
    "s_and_b32 s48, s36, 8\n"
    "s_lshl_b32 s48, s48, 8\n"
    "s_add_u32 s47, s47, s48\n"
    "s_add_u32 m0, s46, 49152\n"
    "s_add_u32 s47, s47, s45\n"
    "buffer_load_dwordx4 v254, s[24:27], s47 offen offset:0 lds\n"
    "buffer_load_dwordx4 v255, s[24:27], s47 offen offset:1024 lds\n"
    "s_add_u32 s47, s45, 120832\n"
    "buffer_load_dwordx4 v254, s[24:27], s47 offen offset:2048 lds\n"
    "s_add_u32 s47, s47, 1024\n"
    "buffer_load_dwordx4 v254, s[24:27], s47 offen offset:3072 lds\n"
    "s_mul_i32 s46, %1, 159\n"
    "s_lshl_b32 s45, s50, 6\n"
    "v_add_u32_e32 v111, s45, v24\n"
    "v_add_u32_e32 v108, s46, v111\n"
    "v_min_u32_e32 v108, 39999, v108\n"
    "v_lshlrev_b32_e32 v108, 4, v108\n"
    "s_lshl1_add_u32 s47, s46, 256\n"
    "v_add_u32_e32 v109, s47, v111\n"
    "v_min_u32_e32 v109, 79999, v109\n"
    "v_lshlrev_b32_e32 v109, 3, v109\n"
    "s_cmp_lg_u32 s51, 0\n"
    "s_cselect_b64 exec, -1, 1\n"
    "global_load_dwordx4 v[96:99], v108, s[30:31]\n"
    "global_load_dwordx2 v[92:93], v109, s[30:31]\n"
    "global_load_dwordx4 v[100:103], v108, s[28:29]\n"
    "global_load_dwordx2 v[94:95], v109, s[28:29]\n"
    "s_mov_b64 exec, -1\n"
    "s_lshl_b32 s45, %2, 6\n"
    "v_add_u32_e32 v108, s45, v24\n"
    "s_sub_u32 s59, 40000, s46\n"
    "s_lshl1_add_u32 s61, s59, -256\n"
    "s_min_i32 s61, s61, 62\n"
    "s_lshl_b32 s45, %2, 2\n"
    "s_add_u32 s54, s35, 0x2000\n"
    "s_add_u32 s55, s35, 0x4000\n"
    "v_lshlrev_b32_e32 v253, 4, v24\n"
    "v_lshrrev_b32_e32 v28, 4, v24\n"
    "v_add_u32_e32 v28, s45, v28\n"
    "v_and_b32_e32 v104, 0xf0, v253\n"
    "v_lshl_add_u32 v104, v28, 9, v104\n"
    "buffer_load_dwordx4 v[0:3], v104, s[12:15], s35 offen offset:0\n"
    "buffer_load_dwordx4 v[4:7], v104, s[12:15], s35 offen offset:256\n"
    "s_bitcmp1_b32 %1, 0\n"
    "s_cselect_b32 s60, s55, 2.0\n"
    "buffer_load_dwordx4 v[8:11], v104, s[12:15], s54 offen offset:0\n"
    "buffer_load_dwordx4 v[12:15], v104, s[12:15], s54 offen offset:256\n"
    "buffer_load_dwordx4 v[16:19], v104, s[12:15], s55 offen offset:0\n"
    "buffer_load_dwordx4 v[20:23], v104, s[12:15], s55 offen offset:256\n"
    "s_mov_b32 s10, 37557248\n"
    "s_mov_b32 s11, 0x20000\n"
    "s_mov_b32 s37, 5777664\n"
    "s_buffer_load_dword s39, s[8:11], s37\n"
    "s_bfe_u32 s37, %1, 0x20001\n"
    "v_and_b32_e32 v27, 1, v25\n"
    "v_lshl_add_u32 v248, v27, 9, v26\n"
    "v_lshrrev_b32_e32 v27, 1, v25\n"
    "v_lshl_add_u32 v248, v27, 12, v248\n"
    "s_mul_i32 s40, s32, 39\n"
    "s_and_b32 s34, s32, 1\n"
    "s_add_u32 s40, s40, s34\n"
    "s_lshl1_add_u32 s40, s40, s51\n"
    "s_lshl_b32 s40, s40, 14\n"
    "s_lshl_b32 s47, s50, 8\n"
    "s_add_u32 s40, s40, s47\n"
    "s_add_u32 s41, s40, 128\n"
    "s_xor_b32 s34, s34, 1\n"
    "s_lshl_b32 s34, s34, 15\n"
    "s_xor_b32 s43, s50, s51\n"
    "s_lshl3_add_u32 s43, s50, s43\n"
    "s_lshl_b32 s43, s43, 13\n"
    "s_add_u32 s47, %3, 32768\n"
    "s_add_u32 s43, s43, s47\n"
    "v_bfe_u32 v25, v24, 2, 3\n"
    "v_lshlrev_b32_e32 v244, 7, v25\n"
    "v_lshrrev_b32_e32 v26, 5, v24\n"
    "v_lshl_add_u32 v244, v26, 10, v244\n"
    "v_and_b32_e32 v25, 6, v25\n"
    "v_bfe_u32 v26, v24, 1, 1\n"
    "v_xor_b32_e32 v25, v25, v26\n"
    "v_lshl_add_u32 v244, v25, 4, v244\n"
    "v_and_b32_e32 v26, 1, v24\n"
    "v_lshl_add_u32 v244, v26, 3, v244\n"
    "s_lshl_b32 s48, s51, 16\n"
    "s_add_u32 s48, s48, s47\n"
    "v_add_u32_e32 v244, s48, v244\n"
    "v_xor_b32_e32 v245, 32, v244\n"
    "v_xor_b32_e32 v246, 64, v244\n"
    "v_xor_b32_e32 v247, 0x60, v244\n"
    "v_and_b32_e32 v25, 7, v24\n"
    "v_lshlrev_b32_e32 v27, 7, v25\n"
    "v_bfe_u32 v26, v24, 3, 1\n"
    "v_lshl_add_u32 v27, v26, 10, v27\n"
    "v_and_b32_e32 v25, 6, v24\n"
    "v_lshrrev_b32_e32 v26, 4, v24\n"
    "v_xor_b32_e32 v25, v25, v26\n"
    "v_lshl_add_u32 v27, v25, 4, v27\n"
    "s_lshl_b32 s48, s51, 13\n"
    "s_add_u32 s48, s48, s47\n"
    "v_add_u32_e32 v240, s48, v27\n"
    "v_xor_b32_e32 v241, 64, v240\n"
    "v_and_b32_e32 v29, 15, v24\n"
    "s_cmp_ge_u32 s36, 112\n"
    "s_cselect_b32 s47, 8, 16\n"
    "v_cmp_le_u32_e32 vcc, s47, v29\n"
    "v_mov_b32_e32 v64, 0xbd80bd80\n"
    "v_mov_b32_e32 v65, 0xbdabbdab\n"
    "v_mov_b32_e32 v66, 0x392b392b\n"
    "v_cndmask_b32_e32 v64, v64, v65, vcc\n"
    "v_cndmask_b32_e32 v66, 0, v66, vcc\n"
    "s_cselect_b32 s47, 56, 64\n"
    "v_cmp_le_u32_e32 vcc, s47, v24\n"
    "v_mov_b32_e32 v65, 0x3e80bdab\n"
    "v_mov_b32_e32 v67, 0x392b\n"
    "v_cndmask_b32_e32 v65, v64, v65, vcc\n"
    "v_cndmask_b32_e32 v67, v66, v67, vcc\n"
    "s_cmp_ge_u32 s36, 120\n"
    "s_cselect_b32 s47, 15, 16\n"
    "v_cmp_ne_u32_e32 vcc, s47, v29\n"
    "v_mov_b32_e32 v68, 0x3db8aa3b\n"
    "s_lshr_b32 s47, s36, 5\n"
    "v_cndmask_b32_e32 v68, 0, v68, vcc\n"
    "s_lshl_b32 s47, s47, 15\n"
    "s_and_b32 s48, s36, 7\n"
    "s_lshl_b32 s48, s48, 11\n"
    "s_add_u32 s47, s47, s48\n"
    "s_and_b32 s48, s36, 8\n"
    "s_lshl_b32 s48, s48, 7\n"
    "s_add_u32 s47, s47, s48\n"
    "s_and_b32 s48, s36, 16\n"
    "s_lshl_b32 s48, s48, 10\n"
    "s_add_u32 s47, s47, s48\n"
    "s_add_u32 s52, s47, 5242880\n"
    "s_lshl_b32 s48, s50, 9\n"
    "s_add_u32 s52, s52, s48\n"
    "s_lshl_b32 s48, s50, 11\n"
    "v_add_u32_e32 v72, s48, v240\n"
    "v_add_u32_e32 v73, s48, v241\n"
    "v_and_b32_e32 v74, 0x1f0, v253\n"
    "v_lshrrev_b32_e32 v29, 5, v24\n"
    "v_lshl_add_u32 v74, v29, 3, v74\n"
    "s_lshl_b32 s53, s36, 1\n"
    "s_add_u32 s53, s53, s50\n"
    "s_lshl_b32 s53, s53, 4\n"
    "s_add_u32 s53, s53, 5771264\n"
    "s_cmp_eq_u32 s51, 0\n"
    "s_cselect_b32 s52, s52, 2.0\n"
    "s_cselect_b32 s53, s53, 2.0\n"
    "s_lshl_b32 s47, %1, 4\n"
    "s_add_u32 s47, s47, 5701632\n"
    "s_lshl_b32 s48, %1, 7\n"
    "s_add_u32 s48, s48, 5705728\n"
    "s_cmp_eq_u32 s50, 0\n"
    "s_cselect_b32 s47, s47, s48\n"
    "s_cmp_eq_u32 s51, 0\n"
    "s_cselect_b32 s47, s53, s47\n"
    "buffer_store_dword v24, off, s[8:11], s47 offset:8 sc1\n"
    "v_lshrrev_b32_e32 v109, 2, v108\n"
    "v_mul_u32_u24_e32 v109, 0x4100, v109\n"
    "v_and_b32_e32 v110, 48, v253\n"
    "v_or_b32_e32 v109, v109, v110\n"
    "v_add_u32_e32 v110, 0x104000, v109\n"
    "v_cmp_gt_u32_e32 vcc, 64, v108\n"
    "s_add_u32 s44, %3, 24576\n"
    "v_lshl_add_u32 v108, v108, 4, s44\n"
    "v_cndmask_b32_e32 v110, 2.0, v110, vcc\n"
    "v_mov_b32_e32 v32, -1\n"
    "v_mov_b32_e32 v33, -1\n"
    "v_mov_b32_e32 v34, -1\n"
    "v_mov_b32_e32 v35, -1\n"
    "ds_write_b128 v108, v[32:35] offset:0\n"
    "ds_write_b128 v108, v[32:35] offset:1280\n"
    "v_and_b32_e32 v29, 6, v28\n"
    "v_xor_b32_e32 v29, v29, v24\n"
    "v_and_b32_e32 v29, 7, v29\n"
    "v_lshlrev_b32_e32 v105, 7, v28\n"
    "v_lshl_add_u32 v105, v29, 4, v105\n"
    "v_bfe_u32 v29, v24, 3, 1\n"
    "v_subrev_u32_e32 v29, s37, v29\n"
    "v_and_b32_e32 v29, 3, v29\n"
    "v_lshl_add_u32 v105, v29, 11, v105\n"
    "s_add_u32 s45, %3, 65536\n"
    "v_add_u32_e32 v105, s45, v105\n"
    "v_xor_b32_e32 v106, 0x1000, v105\n"
    "v_and_b32_e32 v29, 15, v24\n"
    "v_cmp_eq_u32_e32 vcc, 15, v29\n"
    "v_mov_b32_e32 v107, 0xffff\n"
    "s_mov_b32 s58, 0x3f803f80\n"
    "s_mov_b32 s57, 0x43c00000\n"
    "v_cndmask_b32_e32 v107, -1, v107, vcc\n"
    "s_mov_b32 s56, 0xc3c00000\n"
    "s_mov_b32 s62, 0xc000c000\n"
    "s_mov_b32 s63, 0xc000c000\n"
    "v_add_u32_e32 v250, s45, v27\n"
    "v_xor_b32_e32 v251, 64, v250\n"
    "s_lshl_b32 s47, %2, 10\n"
    "s_add_u32 s47, s47, 5771264\n"
    "v_lshl_add_u32 v252, v24, 4, s47\n"
    "s_waitcnt vmcnt(11)\n"
    "s_waitcnt lgkmcnt(0)\n"
    "s_barrier\n"
    "s_add_u32 s39, s39, 2\n"
    "s_cmp_lg_u32 s51, 0\n"
    "s_cbranch_scc1 .Lat_bin\n"
    "ds_read_b128 v[112:115], v72 offset:0\n"
    "ds_read_b128 v[208:211], v240 offset:18432\n"
    "ds_read_b128 v[176:179], v240 offset:16384\n"
    "ds_read_b128 v[116:119], v73 offset:0\n"
    "ds_read_b128 v[212:215], v241 offset:18432\n"
    "ds_read_b128 v[180:183], v241 offset:16384\n"
    "ds_read_b128 v[120:123], v72 offset:4096\n"
    "ds_read_b128 v[216:219], v240 offset:22528\n"
    "ds_read_b128 v[184:187], v240 offset:20480\n"
    "ds_read_b128 v[124:127], v73 offset:4096\n"
    "ds_read_b128 v[220:223], v241 offset:22528\n"
    "ds_read_b128 v[188:191], v241 offset:20480\n"
    "ds_read_b128 v[128:131], v72 offset:8192\n"
    "ds_read_b128 v[224:227], v240 offset:26624\n"
    "ds_read_b128 v[192:195], v240 offset:24576\n"
    "s_waitcnt lgkmcnt(12)\n"
    "v_mfma_f32_16x16x32_bf16 v[56:59], v[208:211], v[112:115], 0\n"
    "ds_read_b128 v[132:135], v73 offset:8192\n"
    "ds_read_b128 v[228:231], v241 offset:26624\n"
    "v_mfma_f32_16x16x32_bf16 v[48:51], v[112:115], v[176:179], 0\n"
    "ds_read_b128 v[196:199], v241 offset:24576\n"
    "s_waitcnt lgkmcnt(12)\n"
    "v_mfma_f32_16x16x32_bf16 v[56:59], v[212:215], v[116:119], v[56:59]\n"
    "ds_read_b128 v[136:139], v72 offset:12288\n"
    "ds_read_b128 v[232:235], v240 offset:30720\n"
    "v_mfma_f32_16x16x32_bf16 v[48:51], v[116:119], v[180:183], v[48:51]\n"
    "ds_read_b128 v[200:203], v240 offset:28672\n"
    "s_waitcnt lgkmcnt(12)\n"
    "v_mfma_f32_16x16x32_bf16 v[56:59], v[216:219], v[120:123], v[56:59]\n"
    "ds_read_b128 v[140:143], v73 offset:12288\n"
    "ds_read_b128 v[236:239], v241 offset:30720\n"
    "v_mfma_f32_16x16x32_bf16 v[48:51], v[120:123], v[184:187], v[48:51]\n"
    "ds_read_b128 v[204:207], v241 offset:28672\n"
    "s_waitcnt lgkmcnt(12)\n"
    "v_mfma_f32_16x16x32_bf16 v[56:59], v[220:223], v[124:127], v[56:59]\n"
    "v_mfma_f32_16x16x32_bf16 v[48:51], v[124:127], v[188:191], v[48:51]\n"
    "s_waitcnt lgkmcnt(9)\n"
    "v_mfma_f32_16x16x32_bf16 v[56:59], v[224:227], v[128:131], v[56:59]\n"
    "v_mfma_f32_16x16x32_bf16 v[48:51], v[128:131], v[192:195], v[48:51]\n"
    "s_waitcnt lgkmcnt(6)\n"
    "v_mfma_f32_16x16x32_bf16 v[56:59], v[228:231], v[132:135], v[56:59]\n"
    "v_mfma_f32_16x16x32_bf16 v[48:51], v[132:135], v[196:199], v[48:51]\n"
    "s_waitcnt lgkmcnt(3)\n"
    "v_mfma_f32_16x16x32_bf16 v[56:59], v[232:235], v[136:139], v[56:59]\n"
    "v_mfma_f32_16x16x32_bf16 v[48:51], v[136:139], v[200:203], v[48:51]\n"
    "s_waitcnt lgkmcnt(0)\n"
    "v_mfma_f32_16x16x32_bf16 v[56:59], v[236:239], v[140:143], v[56:59]\n"
    "v_mfma_f32_16x16x32_bf16 v[48:51], v[140:143], v[204:207], v[48:51]\n"
    "s_nop 3\n"
    "v_cvt_pk_bf16_f32 v56, v56, v57\n"
    "v_cvt_pk_bf16_f32 v57, v58, v59\n"
    "s_nop 1\n"
    "v_mfma_f32_16x16x16_bf16 v[48:51], v[56:57], v[64:65], v[48:51]\n"
    "v_mfma_f32_16x16x16_bf16 v[48:51], v[56:57], v[66:67], v[48:51]\n"
    "s_nop 7\n"
    "v_mul_f32_e32 v48, v48, v68\n"
    "v_mul_f32_e32 v49, v49, v68\n"
    "v_mul_f32_e32 v50, v50, v68\n"
    "v_mul_f32_e32 v51, v51, v68\n"
    "v_cvt_pk_bf16_f32 v48, v48, v49\n"
    "v_cvt_pk_bf16_f32 v49, v50, v51\n"
    "s_branch .Lat_noj2\n"
    ".Lat_bin:\n"
    "s_movk_i32 s45, -640\n"
    "s_movk_i32 s48, 80\n"
    "v_mov_b32_e32 v60, 1\n"
    "v_cmp_gt_u32_e32 vcc, s59, v111\n"
    "v_cmp_gt_i32_e64 s[46:47], s61, v111\n"
    "s_waitcnt vmcnt(9)\n"
    "v_mul_u32_u24_e32 v48, 0x6667, v96\n"
    "v_mul_u32_u24_e32 v49, 0x6667, v97\n"
    "v_mul_u32_u24_e32 v50, 0x6667, v98\n"
    "v_mul_u32_u24_e32 v51, 0x6667, v99\n"
    "v_mul_u32_u24_e32 v52, 0x6667, v92\n"
    "v_mul_u32_u24_e32 v53, 0x6667, v93\n"
    "v_lshrrev_b32_e32 v48, 24, v48\n"
    "v_lshrrev_b32_e32 v49, 24, v49\n"
    "v_lshrrev_b32_e32 v50, 24, v50\n"
    "v_lshrrev_b32_e32 v51, 24, v51\n"
    "v_lshrrev_b32_e32 v52, 24, v52\n"
    "v_lshrrev_b32_e32 v53, 24, v53\n"
    "v_min_u32_e32 v48, 15, v48\n"
    "v_min_u32_e32 v49, 15, v49\n"
    "v_min_u32_e32 v50, 15, v50\n"
    "v_min_u32_e32 v51, 15, v51\n"
    "v_min_u32_e32 v52, 15, v52\n"
    "v_min_u32_e32 v53, 15, v53\n"
    "v_lshl_add_u32 v54, v48, 2, s44\n"
    "v_lshl_add_u32 v55, v49, 2, s44\n"
    "v_lshl_add_u32 v56, v50, 2, s44\n"
    "v_lshl_add_u32 v57, v51, 2, s44\n"
    "v_lshl_add_u32 v58, v52, 2, s44\n"
    "v_lshl_add_u32 v59, v53, 2, s44\n"
    "s_mov_b64 exec, vcc\n"
    "ds_add_rtn_u32 v54, v54, v60 offset:5248\n"
    "ds_add_rtn_u32 v55, v55, v60 offset:5248\n"
    "ds_add_rtn_u32 v56, v56, v60 offset:5248\n"
    "ds_add_rtn_u32 v57, v57, v60 offset:5248\n"
    "s_mov_b64 exec, s[46:47]\n"
    "ds_add_rtn_u32 v58, v58, v60 offset:5248\n"
    "ds_add_rtn_u32 v59, v59, v60 offset:5248\n"
    "s_mov_b64 exec, -1\n"
    "s_waitcnt vmcnt(7)\n"
    "v_mad_i32_i24 v96, v48, s45, v96\n"
    "v_mad_i32_i24 v97, v49, s45, v97\n"
    "v_mad_i32_i24 v98, v50, s45, v98\n"
    "v_mad_i32_i24 v99, v51, s45, v99\n"
    "v_mad_i32_i24 v92, v52, s45, v92\n"
    "v_mad_i32_i24 v93, v53, s45, v93\n"
    "v_lshl_or_b32 v96, v96, 16, v100\n"
    "v_lshl_or_b32 v97, v97, 16, v101\n"
    "v_lshl_or_b32 v98, v98, 16, v102\n"
    "v_lshl_or_b32 v99, v99, 16, v103\n"
    "v_lshl_or_b32 v92, v92, 16, v94\n"
    "v_lshl_or_b32 v93, v93, 16, v95\n"
    "s_mov_b64 exec, vcc\n"
    "s_waitcnt lgkmcnt(0)\n"
    "v_min_i32_e32 v54, 78, v54\n"
    "v_min_i32_e32 v55, 78, v55\n"
    "v_min_i32_e32 v56, 78, v56\n"
    "v_min_i32_e32 v57, 78, v57\n"
    "v_mad_u32_u24 v48, v48, s48, v54\n"
    "v_mad_u32_u24 v49, v49, s48, v55\n"
    "v_mad_u32_u24 v50, v50, s48, v56\n"
    "v_mad_u32_u24 v51, v51, s48, v57\n"
    "v_lshl_add_u32 v48, v48, 2, s44\n"
    "v_lshl_add_u32 v49, v49, 2, s44\n"
    "v_lshl_add_u32 v50, v50, 2, s44\n"
    "v_lshl_add_u32 v51, v51, 2, s44\n"
    "ds_write_b32 v48, v96 offset:4\n"
    "ds_write_b32 v49, v97 offset:4\n"
    "ds_write_b32 v50, v98 offset:4\n"
    "ds_write_b32 v51, v99 offset:4\n"
    "s_mov_b64 exec, s[46:47]\n"
    "v_min_i32_e32 v58, 78, v58\n"
    "v_min_i32_e32 v59, 78, v59\n"
    "v_mad_u32_u24 v52, v52, s48, v58\n"
    "v_mad_u32_u24 v53, v53, s48, v59\n"
    "v_lshl_add_u32 v52, v52, 2, s44\n"
    "v_lshl_add_u32 v53, v53, 2, s44\n"
    "ds_write_b32 v52, v92 offset:4\n"
    "ds_write_b32 v53, v93 offset:4\n"
    "s_mov_b64 exec, -1\n"
    ".Lat_noj2:\n"
    "buffer_store_dwordx2 v[48:49], v74, s[8:11], s52 offen offset:0 sc1\n"
    "v_mov_b32_e32 v112, s39\n"
    "v_mov_b32_e32 v113, 0\n"
    "s_waitcnt vmcnt(2)\n"
    "v_dot2_f32_bf16 v24, v0, s58, 0\n"
    "v_dot2_f32_bf16 v32, v8, s58, 0\n"
    "v_dot2_f32_bf16 v40, v16, s58, 0\n"
    "v_dot2_f32_bf16 v25, v4, s58, 0\n"
    "v_dot2_f32_bf16 v33, v12, s58, 0\n"
    "v_dot2_f32_bf16 v41, v20, s58, 0\n"
    "v_dot2_f32_bf16 v26, v0, v0, 0\n"
    "v_dot2_f32_bf16 v34, v8, v8, 0\n"
    "v_dot2_f32_bf16 v42, v16, v16, 0\n"
    "v_dot2_f32_bf16 v27, v4, v4, 0\n"
    "v_dot2_f32_bf16 v35, v12, v12, 0\n"
    "v_dot2_f32_bf16 v43, v20, v20, 0\n"
    "v_dot2_f32_bf16 v24, v1, s58, v24\n"
    "v_dot2_f32_bf16 v32, v9, s58, v32\n"
    "v_dot2_f32_bf16 v40, v17, s58, v40\n"
    "v_dot2_f32_bf16 v25, v5, s58, v25\n"
    "v_dot2_f32_bf16 v33, v13, s58, v33\n"
    "v_dot2_f32_bf16 v41, v21, s58, v41\n"
    "v_dot2_f32_bf16 v26, v1, v1, v26\n"
    "v_dot2_f32_bf16 v34, v9, v9, v34\n"
    "v_dot2_f32_bf16 v42, v17, v17, v42\n"
    "v_dot2_f32_bf16 v27, v5, v5, v27\n"
    "v_dot2_f32_bf16 v35, v13, v13, v35\n"
    "v_dot2_f32_bf16 v43, v21, v21, v43\n"
    "v_dot2_f32_bf16 v24, v2, s58, v24\n"
    "v_dot2_f32_bf16 v32, v10, s58, v32\n"
    "v_dot2_f32_bf16 v40, v18, s58, v40\n"
    "v_dot2_f32_bf16 v25, v6, s58, v25\n"
    "v_dot2_f32_bf16 v33, v14, s58, v33\n"
    "v_dot2_f32_bf16 v41, v22, s58, v41\n"
    "v_dot2_f32_bf16 v26, v2, v2, v26\n"
    "v_dot2_f32_bf16 v34, v10, v10, v34\n"
    "v_dot2_f32_bf16 v42, v18, v18, v42\n"
    "v_dot2_f32_bf16 v27, v6, v6, v27\n"
    "v_dot2_f32_bf16 v35, v14, v14, v35\n"
    "v_dot2_f32_bf16 v43, v22, v22, v43\n"
    "v_dot2_f32_bf16 v24, v3, s58, v24\n"
    "v_dot2_f32_bf16 v32, v11, s58, v32\n"
    "v_dot2_f32_bf16 v40, v19, s58, v40\n"
    "v_dot2_f32_bf16 v25, v7, s58, v25\n"
    "v_dot2_f32_bf16 v33, v15, s58, v33\n"
    "v_dot2_f32_bf16 v41, v23, s58, v41\n"
    "v_dot2_f32_bf16 v26, v3, v3, v26\n"
    "v_dot2_f32_bf16 v34, v11, v11, v34\n"
    "v_dot2_f32_bf16 v42, v19, v19, v42\n"
    "v_dot2_f32_bf16 v27, v7, v7, v27\n"
    "v_dot2_f32_bf16 v35, v15, v15, v35\n"
    "v_dot2_f32_bf16 v43, v23, v23, v43\n"
    "v_and_b32_e32 v30, 0xffff0000, v7\n"
    "v_and_b32_e32 v38, 0xffff0000, v15\n"
    "v_and_b32_e32 v46, 0xffff0000, v23\n"
    "v_add_f32_e32 v24, v24, v25\n"
    "v_add_f32_e32 v32, v32, v33\n"
    "v_add_f32_e32 v40, v40, v41\n"
    "v_add_f32_e32 v26, v26, v27\n"
    "v_add_f32_e32 v34, v34, v35\n"
    "v_add_f32_e32 v42, v42, v43\n"
    "v_add_f32_dpp v25, v25, v25 quad_perm:[1,0,3,2] row_mask:0xf bank_mask:0xf bound_ctrl:1\n"
    "v_lshlrev_b32_e32 v48, 16, v0\n"
    "v_add_f32_dpp v33, v33, v33 quad_perm:[1,0,3,2] row_mask:0xf bank_mask:0xf bound_ctrl:1\n"
    "v_and_b32_e32 v49, 0xffff0000, v0\n"
    "v_add_f32_dpp v41, v41, v41 quad_perm:[1,0,3,2] row_mask:0xf bank_mask:0xf bound_ctrl:1\n"
    "v_lshlrev_b32_e32 v50, 16, v1\n"
    "v_mov_b32_dpp v30, v30 quad_perm:[3,3,3,3] row_mask:0xf bank_mask:0xf bound_ctrl:1\n"
    "v_and_b32_e32 v51, 0xffff0000, v1\n"
    "v_mov_b32_dpp v38, v38 quad_perm:[3,3,3,3] row_mask:0xf bank_mask:0xf bound_ctrl:1\n"
    "v_lshlrev_b32_e32 v52, 16, v2\n"
    "v_mov_b32_dpp v46, v46 quad_perm:[3,3,3,3] row_mask:0xf bank_mask:0xf bound_ctrl:1\n"
    "v_and_b32_e32 v53, 0xffff0000, v2\n"
    "v_add_f32_dpp v24, v24, v24 quad_perm:[1,0,3,2] row_mask:0xf bank_mask:0xf bound_ctrl:1\n"
    "v_lshlrev_b32_e32 v54, 16, v3\n"
    "v_add_f32_dpp v32, v32, v32 quad_perm:[1,0,3,2] row_mask:0xf bank_mask:0xf bound_ctrl:1\n"
    "v_and_b32_e32 v55, 0xffff0000, v3\n"
    "v_add_f32_dpp v40, v40, v40 quad_perm:[1,0,3,2] row_mask:0xf bank_mask:0xf bound_ctrl:1\n"
    "v_lshlrev_b32_e32 v56, 16, v4\n"
    "v_add_f32_dpp v26, v26, v26 quad_perm:[1,0,3,2] row_mask:0xf bank_mask:0xf bound_ctrl:1\n"
    "v_and_b32_e32 v57, 0xffff0000, v4\n"
    "v_add_f32_dpp v34, v34, v34 quad_perm:[1,0,3,2] row_mask:0xf bank_mask:0xf bound_ctrl:1\n"
    "v_lshlrev_b32_e32 v58, 16, v5\n"
    "s_waitcnt vmcnt(0)\n"
    "buffer_store_dword v112, v113, s[8:11], s53 offen offset:0 sc1\n"
    "v_add_f32_dpp v42, v42, v42 quad_perm:[1,0,3,2] row_mask:0xf bank_mask:0xf bound_ctrl:1\n"
    "v_and_b32_e32 v59, 0xffff0000, v5\n"
    "v_add_f32_dpp v24, v24, v24 quad_perm:[2,3,0,1] row_mask:0xf bank_mask:0xf bound_ctrl:1\n"
    "v_lshlrev_b32_e32 v60, 16, v6\n"
    "v_add_f32_dpp v32, v32, v32 quad_perm:[2,3,0,1] row_mask:0xf bank_mask:0xf bound_ctrl:1\n"
    "v_and_b32_e32 v61, 0xffff0000, v6\n"
    "s_waitcnt lgkmcnt(0)\n"
    "s_barrier\n"
    "ds_read_b128 v[96:99], v108 offset:0\n"
    "ds_read_b128 v[100:103], v108 offset:4096\n"
    "s_lshl_b32 s45, %1, 6\n"
    "s_add_u32 s45, s45, 35038208\n"
    "v_add_f32_dpp v40, v40, v40 quad_perm:[2,3,0,1] row_mask:0xf bank_mask:0xf bound_ctrl:1\n"
    "v_lshlrev_b32_e32 v62, 16, v7\n"
    "v_add_f32_dpp v26, v26, v26 quad_perm:[2,3,0,1] row_mask:0xf bank_mask:0xf bound_ctrl:1\n"
    "v_and_b32_e32 v63, 0xffff0000, v7\n"
    "v_add_f32_dpp v34, v34, v34 quad_perm:[2,3,0,1] row_mask:0xf bank_mask:0xf bound_ctrl:1\n"
    "v_lshlrev_b32_e32 v64, 16, v8\n"
    "v_add_f32_dpp v42, v42, v42 quad_perm:[2,3,0,1] row_mask:0xf bank_mask:0xf bound_ctrl:1\n"
    "v_and_b32_e32 v65, 0xffff0000, v8\n"
    "v_add_f32_dpp v24, v24, v24 row_half_mirror row_mask:0xf bank_mask:0xf bound_ctrl:1\n"
    "v_lshlrev_b32_e32 v66, 16, v9\n"
    "v_add_f32_dpp v32, v32, v32 row_half_mirror row_mask:0xf bank_mask:0xf bound_ctrl:1\n"
    "v_and_b32_e32 v67, 0xffff0000, v9\n"
    "v_add_f32_dpp v40, v40, v40 row_half_mirror row_mask:0xf bank_mask:0xf bound_ctrl:1\n"
    "v_lshlrev_b32_e32 v68, 16, v10\n"
    "v_add_f32_dpp v26, v26, v26 row_half_mirror row_mask:0xf bank_mask:0xf bound_ctrl:1\n"
    "v_and_b32_e32 v69, 0xffff0000, v10\n"
    "v_add_f32_dpp v34, v34, v34 row_half_mirror row_mask:0xf bank_mask:0xf bound_ctrl:1\n"
    "v_lshlrev_b32_e32 v70, 16, v11\n"
    "v_add_f32_dpp v42, v42, v42 row_half_mirror row_mask:0xf bank_mask:0xf bound_ctrl:1\n"
    "v_and_b32_e32 v71, 0xffff0000, v11\n"
    "v_add_f32_dpp v24, v24, v24 row_mirror row_mask:0xf bank_mask:0xf bound_ctrl:1\n"
    "v_lshlrev_b32_e32 v72, 16, v12\n"
    "v_add_f32_dpp v32, v32, v32 row_mirror row_mask:0xf bank_mask:0xf bound_ctrl:1\n"
    "v_and_b32_e32 v73, 0xffff0000, v12\n"
    "v_add_f32_dpp v40, v40, v40 row_mirror row_mask:0xf bank_mask:0xf bound_ctrl:1\n"
    "v_lshlrev_b32_e32 v74, 16, v13\n"
    "v_add_f32_dpp v26, v26, v26 row_mirror row_mask:0xf bank_mask:0xf bound_ctrl:1\n"
    "v_and_b32_e32 v75, 0xffff0000, v13\n"
    "v_add_f32_dpp v34, v34, v34 row_mirror row_mask:0xf bank_mask:0xf bound_ctrl:1\n"
    "v_lshlrev_b32_e32 v76, 16, v14\n"
    "s_waitcnt lgkmcnt(0)\n"
    "buffer_store_dwordx4 v[96:99], v109, s[8:11], s45 offen offset:0 sc1\n"
    "buffer_store_dwordx4 v[100:103], v110, s[8:11], s45 offen offset:0 sc1\n"
    "v_add_f32_dpp v42, v42, v42 row_mirror row_mask:0xf bank_mask:0xf bound_ctrl:1\n"
    "v_and_b32_e32 v77, 0xffff0000, v14\n"
    "v_mul_f32_e32 v24, 0x3b800000, v24\n"
    "v_lshlrev_b32_e32 v78, 16, v15\n"
    "v_mul_f32_e32 v32, 0x3b800000, v32\n"
    "v_and_b32_e32 v79, 0xffff0000, v15\n"
    "v_mul_f32_e32 v40, 0x3b800000, v40\n"
    "v_lshlrev_b32_e32 v80, 16, v16\n"
    "v_mul_f32_e32 v25, 0.25, v25\n"
    "v_and_b32_e32 v81, 0xffff0000, v16\n"
    "v_mul_f32_e32 v33, 0.25, v33\n"
    "v_lshlrev_b32_e32 v82, 16, v17\n"
    "v_mul_f32_e32 v41, 0.25, v41\n"
    "v_and_b32_e32 v83, 0xffff0000, v17\n"
    "v_mul_f32_e64 v27, v24, -v24\n"
    "v_lshlrev_b32_e32 v84, 16, v18\n"
    "v_mul_f32_e64 v35, v32, -v32\n"
    "v_and_b32_e32 v85, 0xffff0000, v18\n"
    "v_mul_f32_e64 v43, v40, -v40\n"
    "v_lshlrev_b32_e32 v86, 16, v19\n"
    "v_sub_f32_e32 v25, v25, v30\n"
    "v_and_b32_e32 v87, 0xffff0000, v19\n"
    "v_sub_f32_e32 v33, v33, v38\n"
    "v_lshlrev_b32_e32 v88, 16, v20\n"
    "v_sub_f32_e32 v41, v41, v46\n"
    "v_and_b32_e32 v89, 0xffff0000, v20\n"
    "v_fmac_f32_e32 v27, 0x3b800000, v26\n"
    "v_lshlrev_b32_e32 v90, 16, v21\n"
    "v_fmac_f32_e32 v35, 0x3b800000, v34\n"
    "v_and_b32_e32 v91, 0xffff0000, v21\n"
    "v_fmac_f32_e32 v43, 0x3b800000, v42\n"
    "v_lshlrev_b32_e32 v92, 16, v22\n"
    "v_mul_f32_e32 v25, 0x3eaaaaab, v25\n"
    "v_and_b32_e32 v93, 0xffff0000, v22\n"
    "v_mul_f32_e32 v33, 0x3eaaaaab, v33\n"
    "v_lshlrev_b32_e32 v94, 16, v23\n"
    "v_mul_f32_e32 v41, 0x3eaaaaab, v41\n"
    "v_and_b32_e32 v95, 0xffff0000, v23\n"
    "v_max_f32_e32 v27, 0, v27\n"
    "v_max_f32_e32 v35, 0, v35\n"
    "v_max_f32_e32 v43, 0, v43\n"
    "v_cndmask_b32_e64 v25, v24, v25, s[62:63]\n"
    "v_cndmask_b32_e64 v33, v32, v33, s[62:63]\n"
    "v_cndmask_b32_e64 v41, v40, v41, s[62:63]\n"
    "v_add_f32_e32 v27, 0x3727c5ac, v27\n"
    "v_add_f32_e32 v35, 0x3727c5ac, v35\n"
    "v_add_f32_e32 v43, 0x3727c5ac, v43\n"
    "v_rsq_f32_e32 v28, v27\n"
    "v_rsq_f32_e32 v36, v35\n"
    "v_rsq_f32_e32 v44, v43\n"
    "v_fma_f32 v29, -v24, v28, s57\n"
    "v_fma_f32 v37, -v32, v36, s57\n"
    "v_fma_f32 v45, -v40, v44, s57\n"
    "v_fma_f32 v31, -v25, v28, s57\n"
    "v_fma_f32 v39, -v33, v36, s57\n"
    "v_fma_f32 v47, -v41, v44, s57\n"
    "v_sub_f32_e32 v24, v29, v31\n"
    "v_sub_f32_e32 v32, v37, v39\n"
    "v_sub_f32_e32 v40, v45, v47\n"
    "v_pk_fma_f32 v[48:49], v[48:49], v[28:29], v[28:29] op_sel:[0,0,1] op_sel_hi:[1,0,1]\n"
    "v_pk_fma_f32 v[50:51], v[50:51], v[28:29], v[28:29] op_sel:[0,0,1] op_sel_hi:[1,0,1]\n"
    "v_pk_fma_f32 v[52:53], v[52:53], v[28:29], v[28:29] op_sel:[0,0,1] op_sel_hi:[1,0,1]\n"
    "v_pk_fma_f32 v[54:55], v[54:55], v[28:29], v[28:29] op_sel:[0,0,1] op_sel_hi:[1,0,1]\n"
    "v_pk_fma_f32 v[56:57], v[56:57], v[28:29], v[30:31] op_sel:[0,0,1] op_sel_hi:[1,0,1]\n"
    "v_pk_fma_f32 v[58:59], v[58:59], v[28:29], v[30:31] op_sel:[0,0,1] op_sel_hi:[1,0,1]\n"
    "v_pk_fma_f32 v[60:61], v[60:61], v[28:29], v[30:31] op_sel:[0,0,1] op_sel_hi:[1,0,1]\n"
    "v_pk_fma_f32 v[62:63], v[62:63], v[28:29], v[30:31] op_sel:[0,0,1] op_sel_hi:[1,0,1]\n"
    "v_pk_add_f32 v[48:49], v[48:49], s[56:57] op_sel_hi:[1,0]\n"
    "v_pk_add_f32 v[50:51], v[50:51], s[56:57] op_sel_hi:[1,0]\n"
    "v_pk_add_f32 v[52:53], v[52:53], s[56:57] op_sel_hi:[1,0]\n"
    "v_pk_add_f32 v[54:55], v[54:55], s[56:57] op_sel_hi:[1,0]\n"
    "v_pk_add_f32 v[56:57], v[56:57], s[56:57] op_sel_hi:[1,0]\n"
    "v_pk_add_f32 v[58:59], v[58:59], s[56:57] op_sel_hi:[1,0]\n"
    "v_pk_add_f32 v[60:61], v[60:61], s[56:57] op_sel_hi:[1,0]\n"
    "v_pk_add_f32 v[62:63], v[62:63], s[56:57] op_sel_hi:[1,0]\n"
    "v_cvt_pk_bf16_f32 v0, v48, v49\n"
    "v_cvt_pk_bf16_f32 v1, v50, v51\n"
    "v_cvt_pk_bf16_f32 v2, v52, v53\n"
    "v_cvt_pk_bf16_f32 v3, v54, v55\n"
    "v_cvt_pk_bf16_f32 v4, v56, v57\n"
    "v_cvt_pk_bf16_f32 v5, v58, v59\n"
    "v_cvt_pk_bf16_f32 v6, v60, v61\n"
    "v_cvt_pk_bf16_f32 v7, v62, v63\n"
    "buffer_store_dwordx4 v[0:3], v104, s[8:11], s35 offen offset:0 sc1\n"
    "v_bfi_b32 v7, v107, v7, 1.0\n"
    "ds_write_b128 v105, v[0:3] offset:0\n"
    "buffer_store_dwordx4 v[4:7], v104, s[8:11], s35 offen offset:256 sc1\n"
    "v_pk_fma_f32 v[64:65], v[64:65], v[36:37], v[36:37] op_sel:[0,0,1] op_sel_hi:[1,0,1]\n"
    "v_pk_fma_f32 v[66:67], v[66:67], v[36:37], v[36:37] op_sel:[0,0,1] op_sel_hi:[1,0,1]\n"
    "v_pk_fma_f32 v[68:69], v[68:69], v[36:37], v[36:37] op_sel:[0,0,1] op_sel_hi:[1,0,1]\n"
    "v_pk_fma_f32 v[70:71], v[70:71], v[36:37], v[36:37] op_sel:[0,0,1] op_sel_hi:[1,0,1]\n"
    "v_pk_fma_f32 v[72:73], v[72:73], v[36:37], v[38:39] op_sel:[0,0,1] op_sel_hi:[1,0,1]\n"
    "v_pk_fma_f32 v[74:75], v[74:75], v[36:37], v[38:39] op_sel:[0,0,1] op_sel_hi:[1,0,1]\n"
    "v_pk_fma_f32 v[76:77], v[76:77], v[36:37], v[38:39] op_sel:[0,0,1] op_sel_hi:[1,0,1]\n"
    "v_pk_fma_f32 v[78:79], v[78:79], v[36:37], v[38:39] op_sel:[0,0,1] op_sel_hi:[1,0,1]\n"
    "v_pk_add_f32 v[64:65], v[64:65], s[56:57] op_sel_hi:[1,0]\n"
    "v_pk_add_f32 v[66:67], v[66:67], s[56:57] op_sel_hi:[1,0]\n"
    "v_pk_add_f32 v[68:69], v[68:69], s[56:57] op_sel_hi:[1,0]\n"
    "v_pk_add_f32 v[70:71], v[70:71], s[56:57] op_sel_hi:[1,0]\n"
    "v_pk_add_f32 v[72:73], v[72:73], s[56:57] op_sel_hi:[1,0]\n"
    "v_pk_add_f32 v[74:75], v[74:75], s[56:57] op_sel_hi:[1,0]\n"
    "v_pk_add_f32 v[76:77], v[76:77], s[56:57] op_sel_hi:[1,0]\n"
    "v_pk_add_f32 v[78:79], v[78:79], s[56:57] op_sel_hi:[1,0]\n"
    "v_cvt_pk_bf16_f32 v8, v64, v65\n"
    "v_cvt_pk_bf16_f32 v9, v66, v67\n"
    "v_cvt_pk_bf16_f32 v10, v68, v69\n"
    "v_cvt_pk_bf16_f32 v11, v70, v71\n"
    "v_cvt_pk_bf16_f32 v12, v72, v73\n"
    "v_cvt_pk_bf16_f32 v13, v74, v75\n"
    "v_cvt_pk_bf16_f32 v14, v76, v77\n"
    "v_cvt_pk_bf16_f32 v15, v78, v79\n"
    "buffer_store_dwordx4 v[8:11], v104, s[8:11], s54 offen offset:0 sc1\n"
    "v_bfi_b32 v15, v107, v15, 1.0\n"
    "ds_write_b128 v105, v[8:11] offset:8192\n"
    "buffer_store_dwordx4 v[12:15], v104, s[8:11], s54 offen offset:256 sc1\n"
    "v_pk_fma_f32 v[80:81], v[80:81], v[44:45], v[44:45] op_sel:[0,0,1] op_sel_hi:[1,0,1]\n"
    "v_pk_fma_f32 v[82:83], v[82:83], v[44:45], v[44:45] op_sel:[0,0,1] op_sel_hi:[1,0,1]\n"
    "v_pk_fma_f32 v[84:85], v[84:85], v[44:45], v[44:45] op_sel:[0,0,1] op_sel_hi:[1,0,1]\n"
    "v_pk_fma_f32 v[86:87], v[86:87], v[44:45], v[44:45] op_sel:[0,0,1] op_sel_hi:[1,0,1]\n"
    "v_pk_fma_f32 v[88:89], v[88:89], v[44:45], v[46:47] op_sel:[0,0,1] op_sel_hi:[1,0,1]\n"
    "v_pk_fma_f32 v[90:91], v[90:91], v[44:45], v[46:47] op_sel:[0,0,1] op_sel_hi:[1,0,1]\n"
    "v_pk_fma_f32 v[92:93], v[92:93], v[44:45], v[46:47] op_sel:[0,0,1] op_sel_hi:[1,0,1]\n"
    "v_pk_fma_f32 v[94:95], v[94:95], v[44:45], v[46:47] op_sel:[0,0,1] op_sel_hi:[1,0,1]\n"
    "v_pk_add_f32 v[80:81], v[80:81], s[56:57] op_sel_hi:[1,0]\n"
    "v_pk_add_f32 v[82:83], v[82:83], s[56:57] op_sel_hi:[1,0]\n"
    "v_pk_add_f32 v[84:85], v[84:85], s[56:57] op_sel_hi:[1,0]\n"
    "v_pk_add_f32 v[86:87], v[86:87], s[56:57] op_sel_hi:[1,0]\n"
    "v_pk_add_f32 v[88:89], v[88:89], s[56:57] op_sel_hi:[1,0]\n"
    "v_pk_add_f32 v[90:91], v[90:91], s[56:57] op_sel_hi:[1,0]\n"
    "v_pk_add_f32 v[92:93], v[92:93], s[56:57] op_sel_hi:[1,0]\n"
    "v_pk_add_f32 v[94:95], v[94:95], s[56:57] op_sel_hi:[1,0]\n"
    "v_cvt_pk_bf16_f32 v16, v80, v81\n"
    "v_cvt_pk_bf16_f32 v17, v82, v83\n"
    "v_cvt_pk_bf16_f32 v18, v84, v85\n"
    "v_cvt_pk_bf16_f32 v19, v86, v87\n"
    "v_cvt_pk_bf16_f32 v20, v88, v89\n"
    "v_cvt_pk_bf16_f32 v21, v90, v91\n"
    "v_cvt_pk_bf16_f32 v22, v92, v93\n"
    "v_cvt_pk_bf16_f32 v23, v94, v95\n"
    "buffer_store_dwordx4 v[16:19], v104, s[8:11], s60 offen offset:0 sc1\n"
    "v_bfi_b32 v23, v107, v23, 1.0\n"
    "ds_write_b128 v105, v[16:19] offset:16384\n"
    "buffer_store_dwordx4 v[20:23], v104, s[8:11], s60 offen offset:256 sc1\n"
    "v_pk_add_f32 v[56:57], v[56:57], v[24:25] op_sel_hi:[1,0]\n"
    "v_pk_add_f32 v[58:59], v[58:59], v[24:25] op_sel_hi:[1,0]\n"
    "v_pk_add_f32 v[60:61], v[60:61], v[24:25] op_sel_hi:[1,0]\n"
    "v_pk_add_f32 v[62:63], v[62:63], v[24:25] op_sel_hi:[1,0]\n"
    "v_cvt_pk_bf16_f32 v56, v56, v57\n"
    "v_cvt_pk_bf16_f32 v57, v58, v59\n"
    "v_cvt_pk_bf16_f32 v58, v60, v61\n"
    "v_cvt_pk_bf16_f32 v59, v62, v63\n"
    "ds_write_b128 v106, v[56:59] offset:0\n"
    "v_pk_add_f32 v[72:73], v[72:73], v[32:33] op_sel_hi:[1,0]\n"
    "v_pk_add_f32 v[74:75], v[74:75], v[32:33] op_sel_hi:[1,0]\n"
    "v_pk_add_f32 v[76:77], v[76:77], v[32:33] op_sel_hi:[1,0]\n"
    "v_pk_add_f32 v[78:79], v[78:79], v[32:33] op_sel_hi:[1,0]\n"
    "v_cvt_pk_bf16_f32 v72, v72, v73\n"
    "v_cvt_pk_bf16_f32 v73, v74, v75\n"
    "v_cvt_pk_bf16_f32 v74, v76, v77\n"
    "v_cvt_pk_bf16_f32 v75, v78, v79\n"
    "ds_write_b128 v106, v[72:75] offset:8192\n"
    "v_pk_add_f32 v[88:89], v[88:89], v[40:41] op_sel_hi:[1,0]\n"
    "v_pk_add_f32 v[90:91], v[90:91], v[40:41] op_sel_hi:[1,0]\n"
    "v_pk_add_f32 v[92:93], v[92:93], v[40:41] op_sel_hi:[1,0]\n"
    "v_pk_add_f32 v[94:95], v[94:95], v[40:41] op_sel_hi:[1,0]\n"
    "v_cvt_pk_bf16_f32 v88, v88, v89\n"
    "v_cvt_pk_bf16_f32 v89, v90, v91\n"
    "v_cvt_pk_bf16_f32 v90, v92, v93\n"
    "v_cvt_pk_bf16_f32 v91, v94, v95\n"
    "ds_write_b128 v106, v[88:91] offset:16384\n"
    "s_waitcnt lgkmcnt(0)\n"
    "s_barrier\n"
    "ds_read_b128 a[0:3], v250 offset:0\n"
    "ds_read_b128 a[32:35], v250 offset:8192\n"
    "ds_read_b128 a[64:67], v250 offset:16384\n"
    "ds_read_b128 a[4:7], v251 offset:0\n"
    "ds_read_b128 a[36:39], v251 offset:8192\n"
    "ds_read_b128 a[68:71], v251 offset:16384\n"
    "ds_read_b128 a[8:11], v250 offset:2048\n"
    "ds_read_b128 a[40:43], v250 offset:10240\n"
    "ds_read_b128 a[72:75], v250 offset:18432\n"
    "ds_read_b128 a[12:15], v251 offset:2048\n"
    "ds_read_b128 a[44:47], v251 offset:10240\n"
    "ds_read_b128 a[76:79], v251 offset:18432\n"
    "buffer_load_dword v255, v252, s[8:11], 0 offen offset:0 sc1\n"
    "ds_read_b128 a[16:19], v250 offset:4096\n"
    "ds_read_b128 a[48:51], v250 offset:12288\n"
    "ds_read_b128 a[80:83], v250 offset:20480\n"
    "ds_read_b128 a[20:23], v251 offset:4096\n"
    "ds_read_b128 a[52:55], v251 offset:12288\n"
    "ds_read_b128 a[84:87], v251 offset:20480\n"
    "ds_read_b128 a[24:27], v250 offset:6144\n"
    "ds_read_b128 a[56:59], v250 offset:14336\n"
    "ds_read_b128 a[88:91], v250 offset:22528\n"
    "ds_read_b128 a[28:31], v251 offset:6144\n"
    "ds_read_b128 a[60:63], v251 offset:14336\n"
    "ds_read_b128 a[92:95], v251 offset:22528\n"
    "s_lshl_b32 s52, %2, 15\n"
    "s_add_u32 s52, s52, 5242880\n"
    "s_lshl_b32 s61, s37, 12\n"
    "s_lshl_b32 s46, %1, 4\n"
    "s_lshl_b32 s47, %1, 7\n"
    "s_add_u32 s47, s47, 4096\n"
    "s_cmp_eq_u32 s50, 0\n"
    "s_cselect_b32 s46, s46, s47\n"
    "s_add_u32 s46, s46, 5701632\n"
    "s_cmp_eq_u32 s51, 0\n"
    "s_cselect_b32 s46, s46, 2.0\n"
    "v_mov_b32_e32 v40, s39\n"
    "v_mov_b32_e32 v41, 0\n"
    "s_waitcnt vmcnt(1)\n"
    "s_waitcnt lgkmcnt(0)\n"
    "s_barrier\n"
    "buffer_store_dword v40, v41, s[8:11], s46 offen offset:0 sc1\n"
    "s_waitcnt vmcnt(1)\n"
    "v_cmp_ne_u32_e32 vcc, s39, v255\n"
    "s_nop 4\n"
    "s_cbranch_vccnz .Lat_apoll\n"
    ".Lat_apoll_ret:\n"
    "s_add_u32 s47, s52, s61\n"
    "buffer_load_dwordx4 v[112:115], v253, s[8:11], s47 offen offset:0 sc1\n"
    "buffer_load_dwordx4 v[116:119], v253, s[8:11], s47 offen offset:1024 sc1\n"
    "buffer_load_dwordx4 v[128:131], v253, s[8:11], s47 offen offset:2048 sc1\n"
    "s_add_u32 s61, s61, 0x1000\n"
    "buffer_load_dwordx4 v[132:135], v253, s[8:11], s47 offen offset:3072 sc1\n"
    "s_bitset0_b32 s61, 14\n"
    "s_add_u32 s47, s52, s61\n"
    "buffer_load_dwordx4 v[144:147], v253, s[8:11], s47 offen offset:0 sc1\n"
    "buffer_load_dwordx4 v[148:151], v253, s[8:11], s47 offen offset:1024 sc1\n"
    "buffer_load_dwordx4 v[160:163], v253, s[8:11], s47 offen offset:2048 sc1\n"
    "s_add_u32 s61, s61, 0x1000\n"
    "buffer_load_dwordx4 v[164:167], v253, s[8:11], s47 offen offset:3072 sc1\n"
    "s_bitset0_b32 s61, 14\n"
    "s_add_u32 s47, s52, s61\n"
    "buffer_load_dwordx4 v[176:179], v253, s[8:11], s47 offen offset:0 sc1\n"
    "buffer_load_dwordx4 v[180:183], v253, s[8:11], s47 offen offset:1024 sc1\n"
    "buffer_load_dwordx4 v[192:195], v253, s[8:11], s47 offen offset:2048 sc1\n"
    "s_add_u32 s61, s61, 0x1000\n"
    "s_lshl_b32 s59, s32, 4\n"
    "s_lshr_b32 s48, s32, 1\n"
    "s_sub_u32 s59, s59, s48\n"
    "s_lshl_b32 s59, s59, 6\n"
    "s_sub_u32 s48, 3984, s59\n"
    "v_cmp_le_u32_e32 vcc, 0x40, v253\n"
    "v_mov_b32_e32 v0, s48\n"
    "s_cmp_eq_u32 s32, 2\n"
    "s_cselect_b32 s48, 1024, 0\n"
    "v_mov_b32_e32 v2, s48\n"
    "s_add_u32 s59, s59, 5701632\n"
    "v_cndmask_b32_e32 v3, v253, v0, vcc\n"
    "s_lshl_b32 s45, %2, 11\n"
    "s_add_u32 s48, s45, %3\n"
    "v_add_u32_e32 v9, s48, v253\n"
    "s_add_u32 s45, s45, 11022336\n"
    "v_add_u32_e32 v8, s45, v253\n"
    "v_lshrrev_b32_e32 v13, 7, v253\n"
    "v_and_b32_e32 v12, 0x70, v254\n"
    "v_lshl_add_u32 v12, v13, 9, v12\n"
    "v_add_u32_e32 v13, 0xc00, v12\n"
    "v_add_u32_e32 v14, 0x1800, v12\n"
    "v_add_u32_e32 v15, 0x2400, v12\n"
    "s_lshl_b32 s57, s50, 8\n"
    "s_add_u32 s57, s57, 5111808\n"
    "s_add_u32 s62, s43, s34\n"
    "s_xor_b32 s63, s34, 0xc000\n"
    "s_add_u32 s63, s63, s43\n"
    "s_waitcnt vmcnt(9)\n"
    "v_mfma_f32_16x16x32_bf16 v[24:27], v[112:115], a[0:3], 0\n"
    "buffer_load_dwordx4 v[196:199], v253, s[8:11], s47 offen offset:3072 sc1\n"
    "v_mfma_f32_16x16x32_bf16 v[28:31], v[116:119], a[0:3], 0\n"
    "s_bitset0_b32 s61, 14\n"
    "s_mov_b32 s18, 5120000\n"
    "s_mov_b32 s19, 0x20000\n"
    "v_mfma_f32_16x16x32_bf16 v[48:51], v[112:115], a[32:35], 0\n"
    "s_add_u32 s47, s52, s61\n"
    "v_xor_b32_e32 v242, 0x12000, v240\n"
    "v_xor_b32_e32 v243, 0x12000, v241\n"
    "v_mfma_f32_16x16x32_bf16 v[52:55], v[116:119], a[32:35], 0\n"
    "buffer_load_dwordx4 v[208:211], v253, s[8:11], s47 offen offset:0 sc1\n"
    "v_mfma_f32_16x16x32_bf16 v[64:67], v[112:115], a[64:67], 0\n"
    "s_cmp_eq_u32 %2, 3\n"
    "s_cselect_b32 s88, 0x2ff, -1\n"
    "v_mov_b32_e32 v10, 0xc2980000\n"
    "v_mfma_f32_16x16x32_bf16 v[68:71], v[116:119], a[64:67], 0\n"
    "s_waitcnt vmcnt(9)\n"
    "v_cmp_lt_u32_e32 vcc, s88, v253\n"
    "s_nop 1\n"
    "v_cndmask_b32_e32 v10, 0, v10, vcc\n"
    "v_mfma_f32_16x16x32_bf16 v[24:27], v[128:131], a[4:7], v[24:27]\n"
    "buffer_load_dwordx4 v[212:215], v253, s[8:11], s47 offen offset:1024 sc1\n"
    "v_mfma_f32_16x16x32_bf16 v[28:31], v[132:135], a[4:7], v[28:31]\n"
    "s_add_u32 s52, s52, 0x4000\n"
    "v_lshrrev_b32_e32 v5, 4, v253\n"
    "v_min_u32_e32 v5, 2, v5\n"
    "v_mfma_f32_16x16x32_bf16 v[48:51], v[128:131], a[36:39], v[48:51]\n"
    "s_add_u32 s88, s32, 1\n"
    "v_add_u32_e32 v5, s88, v5\n"
    "v_mov_b32_e32 v80, 0\n"
    "v_mfma_f32_16x16x32_bf16 v[52:55], v[132:135], a[36:39], v[52:55]\n"
    "buffer_load_dwordx4 v[224:227], v253, s[8:11], s47 offen offset:2048 sc1\n"
    "v_mfma_f32_16x16x32_bf16 v[64:67], v[128:131], a[68:71], v[64:67]\n"
    "s_add_u32 s61, s61, 0x1000\n"
    "v_and_b32_e32 v5, 3, v5\n"
    "v_lshlrev_b32_e32 v5, 7, v5\n"
    "v_mfma_f32_16x16x32_bf16 v[68:71], v[132:135], a[68:71], v[68:71]\n"
    "s_waitcnt vmcnt(9)\n"
    "s_lshl_b32 s58, %1, 7\n"
    "s_add_u32 s58, s58, 5738432\n"
    "s_cmp_eq_u32 %2, 2\n"
    "s_cselect_b32 s58, s58, 2.0\n"
    "v_mfma_f32_16x16x32_bf16 v[24:27], v[144:147], a[8:11], v[24:27]\n"
    "buffer_load_dwordx4 v[228:231], v253, s[8:11], s47 offen offset:3072 sc1\n"
    "v_mfma_f32_16x16x32_bf16 v[28:31], v[148:151], a[8:11], v[28:31]\n"
    "s_bitset0_b32 s61, 14\n"
    "s_lshl_b32 s88, s50, 13\n"
    "s_add_u32 s88, s88, %3\n"
    "v_mfma_f32_16x16x32_bf16 v[48:51], v[144:147], a[40:43], v[48:51]\n"
    "s_add_u32 s47, s52, s61\n"
    "v_add_u32_e32 v6, s88, v253\n"
    "v_mov_b32_e32 v81, 0\n"
    "v_mfma_f32_16x16x32_bf16 v[52:55], v[148:151], a[40:43], v[52:55]\n"
    "buffer_load_dwordx4 v[120:123], v253, s[8:11], s47 offen offset:0 sc1\n"
    "v_mfma_f32_16x16x32_bf16 v[64:67], v[144:147], a[72:75], v[64:67]\n"
    "s_mul_i32 s88, %2, 1024\n"
    "v_add_u32_e32 v7, %3, v253\n"
    "s_add_u32 s88, s88, %3\n"
    "v_mfma_f32_16x16x32_bf16 v[68:71], v[148:151], a[72:75], v[68:71]\n"
    "s_waitcnt vmcnt(9)\n"
    "v_lshrrev_b32_e32 v250, 2, v253\n"
    "v_add_u32_e32 v249, s88, v250\n"
    "v_mfma_f32_16x16x32_bf16 v[24:27], v[160:163], a[12:15], v[24:27]\n"
    "buffer_load_dwordx4 v[124:127], v253, s[8:11], s47 offen offset:1024 sc1\n"
    "v_mfma_f32_16x16x32_bf16 v[28:31], v[164:167], a[12:15], v[28:31]\n"
    "s_mov_b32 s49, s88\n"
    "s_lshl_b32 s81, s88, 2\n"
    "s_lshl2_add_u32 s90, %1, %2\n"
    "v_mfma_f32_16x16x32_bf16 v[48:51], v[160:163], a[44:47], v[48:51]\n"
    "s_lshl_b32 s90, s90, 10\n"
    "s_sub_u32 s81, s90, s81\n"
    "s_add_u32 s81, s81, 36369408\n"
    "v_mfma_f32_16x16x32_bf16 v[52:55], v[164:167], a[44:47], v[52:55]\n"
    "buffer_load_dwordx4 v[136:139], v253, s[8:11], s47 offen offset:2048 sc1\n"
    "v_mfma_f32_16x16x32_bf16 v[64:67], v[160:163], a[76:79], v[64:67]\n"
    "s_add_u32 s61, s61, 0x1000\n"
    "s_xor_b32 s88, %2, 2\n"
    "s_mul_i32 s88, s88, 1024\n"
    "v_mfma_f32_16x16x32_bf16 v[68:71], v[164:167], a[76:79], v[68:71]\n"
    "s_waitcnt vmcnt(9)\n"
    "s_add_u32 s88, s88, %3\n"
    "v_add_u32_e32 v250, s88, v250\n"
    "v_mfma_f32_16x16x32_bf16 v[24:27], v[176:179], a[16:19], v[24:27]\n"
    "buffer_load_dwordx4 v[140:143], v253, s[8:11], s47 offen offset:3072 sc1\n"
    "v_mfma_f32_16x16x32_bf16 v[28:31], v[180:183], a[16:19], v[28:31]\n"
    "s_bitset0_b32 s61, 14\n"
    "s_lshl_b32 s82, %2, 10\n"
    "v_and_b32_e32 v251, 0x1f0, v253\n"
    "v_mfma_f32_16x16x32_bf16 v[48:51], v[176:179], a[48:51], v[48:51]\n"
    "s_add_u32 s47, s52, s61\n"
    "s_add_u32 s82, s82, %3\n"
    "s_add_u32 s82, s82, 20480\n"
    "v_mfma_f32_16x16x32_bf16 v[52:55], v[180:183], a[48:51], v[52:55]\n"
    "buffer_load_dwordx4 v[152:155], v253, s[8:11], s47 offen offset:0 sc1\n"
    "v_mfma_f32_16x16x32_bf16 v[64:67], v[176:179], a[80:83], v[64:67]\n"
    "buffer_load_dword v0, v3, s[8:11], s59 offen offset:0 sc1\n"
    "v_mfma_f32_16x16x32_bf16 v[68:71], v[180:183], a[80:83], v[68:71]\n"
    "s_waitcnt vmcnt(10)\n"
    "v_lshrrev_b32_e32 v252, 9, v253\n"
    "v_lshl_add_u32 v252, v252, 4, s82\n"
    "v_mfma_f32_16x16x32_bf16 v[24:27], v[192:195], a[20:23], v[24:27]\n"
    "buffer_load_dwordx4 v[156:159], v253, s[8:11], s47 offen offset:1024 sc1\n"
    "v_mfma_f32_16x16x32_bf16 v[28:31], v[196:199], a[20:23], v[28:31]\n"
    "s_lshr_b32 s80, %1, 4\n"
    "s_lshl_b32 s80, s80, 7\n"
    "s_add_u32 s80, s80, 5775616\n"
    "v_mfma_f32_16x16x32_bf16 v[48:51], v[192:195], a[52:55], v[48:51]\n"
    "s_and_b32 s88, s32, 1\n"
    "s_lshl_b32 s88, s88, 6\n"
    "s_add_u32 s80, s80, s88\n"
    "v_mfma_f32_16x16x32_bf16 v[52:55], v[196:199], a[52:55], v[52:55]\n"
    "buffer_load_dwordx4 v[168:171], v253, s[8:11], s47 offen offset:2048 sc1\n"
    "v_mfma_f32_16x16x32_bf16 v[64:67], v[192:195], a[84:87], v[64:67]\n"
    "s_add_u32 s61, s61, 0x1000\n"
    "s_mov_b64 s[20:21], s[8:9]\n"
    "s_mov_b32 s22, 5242880\n"
    "v_mfma_f32_16x16x32_bf16 v[68:71], v[196:199], a[84:87], v[68:71]\n"
    "s_waitcnt vmcnt(10)\n"
    "s_add_u32 s24, s8, 5779456\n"
    "s_addc_u32 s25, s9, 0\n"
    "v_mfma_f32_16x16x32_bf16 v[24:27], v[208:211], a[24:27], v[24:27]\n"
    "buffer_load_dwordx4 v[172:175], v253, s[8:11], s47 offen offset:3072 sc1\n"
    "v_mfma_f32_16x16x32_bf16 v[28:31], v[212:215], a[24:27], v[28:31]\n"
    "s_bitset0_b32 s61, 14\n"
    "s_mov_b32 s26, 5242880\n"
    "v_mov_b32_e32 v82, 0\n"
    "v_mfma_f32_16x16x32_bf16 v[48:51], v[208:211], a[56:59], v[48:51]\n"
    "s_add_u32 s47, s52, s61\n"
    "s_movk_i32 s42, 512\n"
    "s_mov_b32 s37, 0\n"
    "v_mfma_f32_16x16x32_bf16 v[52:55], v[212:215], a[56:59], v[52:55]\n"
    "buffer_load_dwordx4 v[184:187], v253, s[8:11], s47 offen offset:0 sc1\n"
    "v_mfma_f32_16x16x32_bf16 v[64:67], v[208:211], a[88:91], v[64:67]\n"
    "s_add_u32 s38, s38, s50\n"
    "v_bfe_u32 v16, v253, 4, 2\n"
    "v_bfe_u32 v17, v253, 6, 2\n"
    "v_mov_b32_e32 v18, 0x3f80\n"
    "v_mfma_f32_16x16x32_bf16 v[68:71], v[212:215], a[88:91], v[68:71]\n"
    "s_waitcnt vmcnt(10)\n"
    "v_cmp_eq_u32_e32 vcc, 3, v16\n"
    "s_nop 1\n"
    "v_cndmask_b32_e64 v18, v18, 1.0, vcc\n"
    "v_cndmask_b32_e64 v16, v16, 0, vcc\n"
    "v_mfma_f32_16x16x32_bf16 v[24:27], v[224:227], a[28:31], v[24:27]\n"
    "buffer_load_dwordx4 v[188:191], v253, s[8:11], s47 offen offset:1024 sc1\n"
    "v_mfma_f32_16x16x32_bf16 v[28:31], v[228:231], a[28:31], v[28:31]\n"
    "v_or_b32_e32 v16, 12, v16\n"
    "v_mul_u32_u24_e32 v16, 0x1111, v16\n"
    "v_mfma_f32_16x16x32_bf16 v[48:51], v[224:227], a[60:63], v[48:51]\n"
    "v_cmp_eq_u32_e32 vcc, 0, v17\n"
    "s_nop 1\n"
    "v_cndmask_b32_e32 v19, 0, v18, vcc\n"
    "v_mfma_f32_16x16x32_bf16 v[52:55], v[228:231], a[60:63], v[52:55]\n"
    "buffer_load_dwordx4 v[200:203], v253, s[8:11], s47 offen offset:2048 sc1\n"
    "v_mfma_f32_16x16x32_bf16 v[64:67], v[224:227], a[92:95], v[64:67]\n"
    "s_add_u32 s61, s61, 0x1000\n"
    "v_cvt_pk_bf16_f32 v24, v24, v25\n"
    "v_accvgpr_write_b32 a192, v19\n"
    "v_mfma_f32_16x16x32_bf16 v[68:71], v[228:231], a[92:95], v[68:71]\n"
    "s_waitcnt vmcnt(10)\n"
    "v_cvt_pk_bf16_f32 v25, v26, v27\n"
    "v_cmp_eq_u32_e32 vcc, 1, v17\n"
    "s_nop 1\n"
    "v_cndmask_b32_e32 v19, 0, v18, vcc\n"
    "v_mfma_f32_16x16x32_bf16 v[32:35], v[120:123], a[0:3], 0\n"
    "buffer_load_dwordx4 v[204:207], v253, s[8:11], s47 offen offset:3072 sc1\n"
    "v_mfma_f32_16x16x32_bf16 v[36:39], v[124:127], a[0:3], 0\n"
    "s_bitset0_b32 s61, 14\n"
    "v_cvt_pk_bf16_f32 v26, v28, v29\n"
    "v_accvgpr_write_b32 a193, v19\n"
    "v_mfma_f32_16x16x32_bf16 v[56:59], v[120:123], a[32:35], 0\n"
    "s_add_u32 s47, s52, s61\n"
    "v_cvt_pk_bf16_f32 v27, v30, v31\n"
    "v_cmp_eq_u32_e32 vcc, 2, v17\n"
    "s_nop 1\n"
    "v_cndmask_b32_e32 v19, 0, v18, vcc\n"
    "v_mfma_f32_16x16x32_bf16 v[60:63], v[124:127], a[32:35], 0\n"
    "buffer_load_dwordx4 v[216:219], v253, s[8:11], s47 offen offset:0 sc1\n"
    "v_mfma_f32_16x16x32_bf16 v[72:75], v[120:123], a[64:67], 0\n"
    "buffer_store_dwordx4 v[24:27], v8, s[8:11], s35 offen offset:0 sc1\n"
    "v_cvt_pk_bf16_f32 v48, v48, v49\n"
    "v_accvgpr_write_b32 a194, v19\n"
    "v_mfma_f32_16x16x32_bf16 v[76:79], v[124:127], a[64:67], 0\n"
    "s_waitcnt vmcnt(11)\n"
    "v_cvt_pk_bf16_f32 v49, v50, v51\n"
    "v_cmp_eq_u32_e32 vcc, 3, v17\n"
    "s_nop 1\n"
    "v_cndmask_b32_e32 v19, 0, v18, vcc\n"
    "v_mfma_f32_16x16x32_bf16 v[32:35], v[136:139], a[4:7], v[32:35]\n"
    "buffer_load_dwordx4 v[220:223], v253, s[8:11], s47 offen offset:1024 sc1\n"
    "v_mfma_f32_16x16x32_bf16 v[36:39], v[140:143], a[4:7], v[36:39]\n"
    "s_add_u32 s52, s52, 0x4000\n"
    "v_cvt_pk_bf16_f32 v50, v52, v53\n"
    "v_accvgpr_write_b32 a195, v19\n"
    "v_mfma_f32_16x16x32_bf16 v[56:59], v[136:139], a[36:39], v[56:59]\n"
    "s_add_u32 s28, s38, 2\n"
    "v_cvt_pk_bf16_f32 v51, v54, v55\n"
    "v_mov_b32_e32 v83, 0\n"
    "v_mfma_f32_16x16x32_bf16 v[60:63], v[140:143], a[36:39], v[60:63]\n"
    "s_waitcnt vmcnt(10)\n"
    "buffer_load_dwordx4 v[232:235], v253, s[8:11], s47 offen offset:2048 sc1\n"
    "buffer_store_dwordx4 v[48:51], v8, s[8:11], s54 offen offset:0 sc1\n"
    "v_cmp_ne_u32_e32 vcc, s39, v0\n"
    "v_mfma_f32_16x16x32_bf16 v[72:75], v[136:139], a[68:71], v[72:75]\n"
    "s_add_u32 s61, s61, 0x1000\n"
    "v_cvt_pk_bf16_f32 v64, v64, v65\n"
    "v_mov_b32_e32 v84, 0\n"
    "v_mfma_f32_16x16x32_bf16 v[76:79], v[140:143], a[68:71], v[76:79]\n"
    "s_waitcnt vmcnt(11)\n"
    "v_cvt_pk_bf16_f32 v65, v66, v67\n"
    "v_mov_b32_e32 v85, 0\n"
    "v_mfma_f32_16x16x32_bf16 v[32:35], v[152:155], a[8:11], v[32:35]\n"
    "buffer_load_dwordx4 v[236:239], v253, s[8:11], s47 offen offset:3072 sc1\n"
    "v_mfma_f32_16x16x32_bf16 v[36:39], v[156:159], a[8:11], v[36:39]\n"
    "s_bitset0_b32 s61, 14\n"
    "s_cbranch_vccnz .Lat_h1poll\n"
    ".Lat_h1poll_ret:\n"
    "v_mfma_f32_16x16x32_bf16 v[56:59], v[152:155], a[40:43], v[56:59]\n"
    "s_sub_u32 s88, s28, 10\n"
    "v_cvt_pk_bf16_f32 v66, v68, v69\n"
    "v_cvt_pk_bf16_f32 v67, v70, v71\n"
    "v_mfma_f32_16x16x32_bf16 v[60:63], v[156:159], a[40:43], v[60:63]\n"
    "buffer_store_dwordx4 v[64:67], v8, s[8:11], s60 offen offset:0 sc1\n"
    "v_mov_b32_e32 v86, 0\n"
    "v_mov_b32_e32 v87, 0\n"
    "v_mfma_f32_16x16x32_bf16 v[72:75], v[152:155], a[72:75], v[72:75]\n"
    "s_min_u32 s28, s28, s88\n"
    "s_lshl_b32 s88, s28, 0\n"
    "v_mov_b32_e32 v88, 0\n"
    "v_mfma_f32_16x16x32_bf16 v[76:79], v[156:159], a[72:75], v[76:79]\n"
    "s_waitcnt vmcnt(11)\n"
    "s_or_b32 s36, s88, s88\n"
    "v_mov_b32_e32 v89, 0\n"
    "v_mfma_f32_16x16x32_bf16 v[32:35], v[168:171], a[12:15], v[32:35]\n"
    "s_lshl1_add_u32 s88, s28, 1\n"
    "s_mul_i32 s88, s88, 52\n"
    "v_mov_b32_e32 v90, 0\n"
    "v_mfma_f32_16x16x32_bf16 v[36:39], v[172:175], a[12:15], v[36:39]\n"
    "s_lshr_b32 s88, s88, 8\n"
    "s_mul_i32 s90, s88, 5\n"
    "v_mov_b32_e32 v91, 0\n"
    "v_mfma_f32_16x16x32_bf16 v[56:59], v[168:171], a[44:47], v[56:59]\n"
    "s_lshr_b32 s90, s90, 1\n"
    "ds_write_b128 v9, v[24:27] offset:0\n"
    "v_mfma_f32_16x16x32_bf16 v[60:63], v[172:175], a[44:47], v[60:63]\n"
    "s_sub_u32 s28, s28, s90\n"
    "s_sub_u32 s90, s32, s88\n"
    "v_mov_b32_e32 v92, 0\n"
    "v_mfma_f32_16x16x32_bf16 v[72:75], v[168:171], a[76:79], v[72:75]\n"
    "s_sub_u32 s90, s90, 1\n"
    "s_and_b32 s90, s90, 3\n"
    "v_mov_b32_e32 v93, 0\n"
    "v_mfma_f32_16x16x32_bf16 v[76:79], v[172:175], a[76:79], v[76:79]\n"
    "s_waitcnt vmcnt(9)\n"
    "s_lshl2_add_u32 s88, s33, s88\n"
    "v_mov_b32_e32 v94, 0\n"
    "v_mfma_f32_16x16x32_bf16 v[32:35], v[184:187], a[16:19], v[32:35]\n"
    "s_mul_i32 s88, s88, 3\n"
    "v_mov_b32_e32 v95, 0\n"
    "v_mfma_f32_16x16x32_bf16 v[36:39], v[188:191], a[16:19], v[36:39]\n"
    "s_add_u32 s88, s88, s90\n"
    "v_mov_b32_e32 v96, 0\n"
    "v_mfma_f32_16x16x32_bf16 v[56:59], v[184:187], a[48:51], v[56:59]\n"
    "s_mul_i32 s88, s88, 24832\n"
    "s_add_u32 s88, s88, 16265216\n"
    "v_mfma_f32_16x16x32_bf16 v[60:63], v[188:191], a[48:51], v[60:63]\n"
    "s_cmp_eq_u32 s90, 3\n"
    "s_cselect_b32 s88, 2.0, s88\n"
    "s_cselect_b32 s89, 2, 3\n"
    "v_mfma_f32_16x16x32_bf16 v[72:75], v[184:187], a[80:83], v[72:75]\n"
    "s_lshl1_add_u32 s83, s28, s51\n"
    "v_mov_b32_e32 v97, 0\n"
    "v_mfma_f32_16x16x32_bf16 v[76:79], v[188:191], a[80:83], v[76:79]\n"
    "s_waitcnt vmcnt(7)\n"
    "ds_write_b128 v9, v[48:51] offset:8192\n"
    "v_mfma_f32_16x16x32_bf16 v[32:35], v[200:203], a[20:23], v[32:35]\n"
    "s_lshl_b32 s83, s83, 12\n"
    "v_mov_b32_e32 v98, 0\n"
    "v_mfma_f32_16x16x32_bf16 v[36:39], v[204:207], a[20:23], v[36:39]\n"
    "s_add_u32 s83, s83, s88\n"
    "v_mov_b32_e32 v99, 0\n"
    "v_mfma_f32_16x16x32_bf16 v[56:59], v[200:203], a[52:55], v[56:59]\n"
    "s_lshl2_add_u32 s28, s28, s88\n"
    "v_mov_b32_e32 v100, 0\n"
    "v_mfma_f32_16x16x32_bf16 v[60:63], v[204:207], a[52:55], v[60:63]\n"
    "s_add_u32 s29, s38, 4\n"
    "v_mov_b32_e32 v101, 0\n"
    "v_mfma_f32_16x16x32_bf16 v[72:75], v[200:203], a[84:87], v[72:75]\n"
    "s_sub_u32 s88, s29, 10\n"
    "v_mov_b32_e32 v102, 0\n"
    "v_mfma_f32_16x16x32_bf16 v[76:79], v[204:207], a[84:87], v[76:79]\n"
    "s_waitcnt vmcnt(4)\n"
    "s_min_u32 s29, s29, s88\n"
    "v_mfma_f32_16x16x32_bf16 v[32:35], v[216:219], a[24:27], v[32:35]\n"
    "s_lshl_b32 s88, s29, 4\n"
    "v_mov_b32_e32 v103, 0\n"
    "v_mfma_f32_16x16x32_bf16 v[36:39], v[220:223], a[24:27], v[36:39]\n"
    "s_or_b32 s36, s88, s36\n"
    "v_mov_b32_e32 v104, 0\n"
    "v_mfma_f32_16x16x32_bf16 v[56:59], v[216:219], a[56:59], v[56:59]\n"
    "s_lshl1_add_u32 s88, s29, 1\n"
    "ds_write_b128 v9, v[64:67] offset:16384\n"
    "v_mfma_f32_16x16x32_bf16 v[60:63], v[220:223], a[56:59], v[60:63]\n"
    "s_mul_i32 s88, s88, 52\n"
    "v_mov_b32_e32 v105, 0\n"
    "v_mfma_f32_16x16x32_bf16 v[72:75], v[216:219], a[88:91], v[72:75]\n"
    "s_lshr_b32 s88, s88, 8\n"
    "v_mov_b32_e32 v106, 0\n"
    "v_mfma_f32_16x16x32_bf16 v[76:79], v[220:223], a[88:91], v[76:79]\n"
    "s_waitcnt vmcnt(1)\n"
    "s_mul_i32 s90, s88, 5\n"
    "v_mfma_f32_16x16x32_bf16 v[32:35], v[232:235], a[28:31], v[32:35]\n"
    "s_lshr_b32 s90, s90, 1\n"
    "v_mov_b32_e32 v107, 0\n"
    "v_mfma_f32_16x16x32_bf16 v[36:39], v[236:239], a[28:31], v[36:39]\n"
    "s_sub_u32 s29, s29, s90\n"
    "v_mov_b32_e32 v108, 0\n"
    "v_mfma_f32_16x16x32_bf16 v[56:59], v[232:235], a[60:63], v[56:59]\n"
    "s_sub_u32 s90, s32, s88\n"
    "v_mov_b32_e32 v109, 0\n"
    "v_mfma_f32_16x16x32_bf16 v[60:63], v[236:239], a[60:63], v[60:63]\n"
    "s_sub_u32 s90, s90, 1\n"
    "v_mov_b32_e32 v110, 0\n"
    "v_mfma_f32_16x16x32_bf16 v[72:75], v[232:235], a[92:95], v[72:75]\n"
    "s_and_b32 s90, s90, 3\n"
    "v_mov_b32_e32 v111, 0\n"
    "v_mfma_f32_16x16x32_bf16 v[76:79], v[236:239], a[92:95], v[76:79]\n"
    "s_lshl2_add_u32 s88, s33, s88\n"
    "s_mul_i32 s88, s88, 3\n"
    "v_cvt_pk_bf16_f32 v32, v32, v33\n"
    "v_cvt_pk_bf16_f32 v33, v34, v35\n"
    "v_cvt_pk_bf16_f32 v34, v36, v37\n"
    "v_cvt_pk_bf16_f32 v35, v38, v39\n"
    "v_or_b32_e32 v35, v10, v35\n"
    "ds_write_b128 v9, v[32:35] offset:1024\n"
    "buffer_store_dwordx4 v[32:35], v8, s[8:11], s35 offen offset:1024 sc1\n"
    "v_cvt_pk_bf16_f32 v56, v56, v57\n"
    "v_cvt_pk_bf16_f32 v57, v58, v59\n"
    "v_cvt_pk_bf16_f32 v58, v60, v61\n"
    "v_cvt_pk_bf16_f32 v59, v62, v63\n"
    "v_or_b32_e32 v59, v10, v59\n"
    "ds_write_b128 v9, v[56:59] offset:9216\n"
    "buffer_store_dwordx4 v[56:59], v8, s[8:11], s54 offen offset:1024 sc1\n"
    "v_cvt_pk_bf16_f32 v72, v72, v73\n"
    "v_cvt_pk_bf16_f32 v73, v74, v75\n"
    "v_cvt_pk_bf16_f32 v74, v76, v77\n"
    "v_cvt_pk_bf16_f32 v75, v78, v79\n"
    "v_or_b32_e32 v75, v10, v75\n"
    "ds_write_b128 v9, v[72:75] offset:17408\n"
    "buffer_store_dwordx4 v[72:75], v8, s[8:11], s60 offen offset:1024 sc1\n"
    "v_mov_b32_e32 v112, 0\n"
    "v_mov_b32_e32 v113, 0\n"
    "v_mov_b32_e32 v114, 0\n"
    "s_mov_b32 m0, s63\n"
    "v_mov_b32_e32 v115, 0\n"
    "v_mov_b32_e32 v116, 0\n"
    "v_mov_b32_e32 v117, 0\n"
    "buffer_load_dwordx4 v254, s[8:11], s57 offen offset:0 sc1 lds\n"
    "v_mov_b32_e32 v118, 0\n"
    "v_mov_b32_e32 v119, 0\n"
    "v_mov_b32_e32 v120, 0\n"
    "s_sub_u32 m0, s63, 1024\n"
    "v_mov_b32_e32 v121, 0\n"
    "v_mov_b32_e32 v122, 0\n"
    "v_mov_b32_e32 v123, 0\n"
    "buffer_load_dwordx4 v254, s[8:11], s57 offen offset:2048 sc1 lds\n"
    "v_mov_b32_e32 v124, 0\n"
    "v_mov_b32_e32 v125, 0\n"
    "v_mov_b32_e32 v126, 0\n"
    "s_add_u32 m0, s63, 3968\n"
    "v_mov_b32_e32 v127, 0\n"
    "v_mov_b32_e32 v128, 0\n"
    "v_mov_b32_e32 v129, 0\n"
    "buffer_load_dwordx4 v254, s[8:11], s57 offen offset:128 sc1 lds\n"
    "v_mov_b32_e32 v130, 0\n"
    "v_mov_b32_e32 v131, 0\n"
    "v_mov_b32_e32 v132, 0\n"
    "s_add_u32 m0, s63, 2944\n"
    "v_mov_b32_e32 v133, 0\n"
    "v_mov_b32_e32 v134, 0\n"
    "v_mov_b32_e32 v135, 0\n"
    "buffer_load_dwordx4 v254, s[8:11], s57 offen offset:2176 sc1 lds\n"
    "v_mov_b32_e32 v136, 0\n"
    "v_mov_b32_e32 v137, 0\n"
    "v_mov_b32_e32 v138, 0\n"
    "s_add_u32 m0, s62, 0\n"
    "v_mov_b32_e32 v139, 0\n"
    "v_mov_b32_e32 v140, 0\n"
    "v_mov_b32_e32 v141, 0\n"
    "buffer_load_dwordx4 v12, s[8:11], s40 offen offset:0 sc1 lds\n"
    "v_mov_b32_e32 v142, 0\n"
    "v_mov_b32_e32 v143, 0\n"
    "v_mov_b32_e32 v144, 0\n"
    "buffer_load_dwordx4 v13, s[8:11], s40 offen offset:1024 sc1 lds\n"
    "v_mov_b32_e32 v145, 0\n"
    "v_mov_b32_e32 v146, 0\n"
    "v_mov_b32_e32 v147, 0\n"
    "buffer_load_dwordx4 v14, s[8:11], s40 offen offset:2048 sc1 lds\n"
    "v_mov_b32_e32 v148, 0\n"
    "v_mov_b32_e32 v149, 0\n"
    "v_mov_b32_e32 v150, 0\n"
    "buffer_load_dwordx4 v15, s[8:11], s40 offen offset:3072 sc1 lds\n"
    "v_mov_b32_e32 v151, 0\n"
    "s_xor_b32 s45, s34, 0xc000\n"
    "v_add_u32_e32 v204, s45, v240\n"
    "s_add_u32 s40, s40, 0x8000\n"
    "v_add_u32_e32 v205, s45, v241\n"
    "v_add_u32_e32 v206, s45, v242\n"
    "v_add_u32_e32 v207, s45, v243\n"
    "s_add_u32 m0, s62, 4096\n"
    "v_add_u32_e32 v208, s45, v244\n"
    "v_add_u32_e32 v209, s45, v245\n"
    "v_add_u32_e32 v210, s45, v246\n"
    "buffer_load_dwordx4 v12, s[8:11], s41 offen offset:0 sc1 lds\n"
    "buffer_load_dwordx4 v13, s[8:11], s41 offen offset:1024 sc1 lds\n"
    "buffer_load_dwordx4 v14, s[8:11], s41 offen offset:2048 sc1 lds\n"
    "buffer_load_dwordx4 v15, s[8:11], s41 offen offset:3072 sc1 lds\n"
    "s_add_u32 s41, s41, 0x8000\n"
    "buffer_load_dword v0, v253, s[8:11], s59 offen offset:0 sc1\n"
    "buffer_load_dword v1, v2, s[8:11], s59 offen offset:0 sc1\n"
    "v_add_u32_e32 v211, s45, v247\n"
    "v_add_u32_e32 v200, s34, v240\n"
    "v_add_u32_e32 v201, s34, v241\n"
    "v_add_u32_e32 v202, s34, v242\n"
    "v_add_u32_e32 v203, s34, v243\n"
    "v_mov_b32_e32 v152, v16\n"
    "v_and_b32_e32 v153, 0xf0, v253\n"
    "v_lshrrev_b32_e32 v154, 9, v253\n"
    "v_lshl_add_u32 v153, v154, 8, v153\n"
    "v_bfe_u32 v154, v253, 8, 1\n"
    "v_lshl_add_u32 v153, v154, 3, v153\n"
    "v_mov_b32_e32 v156, 0\n"
    "v_mov_b32_e32 v157, 0\n"
    "v_mov_b32_e32 v158, 0\n"
    "v_mov_b32_e32 v159, 0\n"
    "v_mov_b32_e32 v56, 0\n"
    "v_mov_b32_e32 v57, 0\n"
    "v_mov_b32_e32 v58, 0\n"
    "v_mov_b32_e32 v59, 0\n"
    "v_mov_b32_e32 v60, 0\n"
    "v_mov_b32_e32 v61, 0\n"
    "v_mov_b32_e32 v62, 0\n"
    "v_mov_b32_e32 v63, 0\n"
    "v_mov_b32_e32 v64, 0\n"
    "v_mov_b32_e32 v65, 0\n"
    "v_mov_b32_e32 v66, 0\n"
    "v_mov_b32_e32 v67, 0\n"
    "v_mov_b32_e32 v68, 0\n"
    "v_mov_b32_e32 v69, 0\n"
    "v_mov_b32_e32 v70, 0\n"
    "v_mov_b32_e32 v71, 0\n"
    "v_mov_b32_e32 v72, 0\n"
    "v_mov_b32_e32 v73, 0\n"
    "v_mov_b32_e32 v74, 0\n"
    "v_mov_b32_e32 v75, 0\n"
    "v_mov_b32_e32 v76, 0\n"
    "v_mov_b32_e32 v77, 0\n"
    "v_mov_b32_e32 v78, 0\n"
    "v_mov_b32_e32 v79, 0\n"
    "s_add_u32 s88, s88, s90\n"
    "s_mul_i32 s88, s88, 24832\n"
    "s_add_u32 s88, s88, 16265216\n"
    "s_lshl1_add_u32 s84, s29, s51\n"
    "s_lshl_b32 s84, s84, 12\n"
    "s_add_u32 s84, s84, s88\n"
    "s_lshl2_add_u32 s29, s29, s88\n"
    "s_add_u32 s30, s38, 6\n"
    "s_sub_u32 s88, s30, 10\n"
    "s_min_u32 s30, s30, s88\n"
    "s_lshl_b32 s88, s30, 8\n"
    "s_or_b32 s36, s88, s36\n"
    "s_lshl1_add_u32 s88, s30, 1\n"
    "s_mul_i32 s88, s88, 52\n"
    "s_lshr_b32 s88, s88, 8\n"
    "s_mul_i32 s90, s88, 5\n"
    "s_lshr_b32 s90, s90, 1\n"
    "s_sub_u32 s30, s30, s90\n"
    "s_sub_u32 s90, s32, s88\n"
    "s_sub_u32 s90, s90, 1\n"
    "s_and_b32 s90, s90, 3\n"
    "s_lshl2_add_u32 s88, s33, s88\n"
    "s_mul_i32 s88, s88, 3\n"
    "s_add_u32 s88, s88, s90\n"
    "s_mul_i32 s88, s88, 24832\n"
    "s_add_u32 s88, s88, 16265216\n"
    "s_lshl1_add_u32 s85, s30, s51\n"
    "s_lshl_b32 s85, s85, 12\n"
    "s_add_u32 s85, s85, s88\n"
    "s_lshl2_add_u32 s30, s30, s88\n"
    "s_add_u32 s31, s38, 8\n"
    "s_sub_u32 s88, s31, 10\n"
    "s_min_u32 s31, s31, s88\n"
    "s_lshl_b32 s88, s31, 12\n"
    "s_or_b32 s36, s88, s36\n"
    "s_lshl1_add_u32 s88, s31, 1\n"
    "s_mul_i32 s88, s88, 52\n"
    "s_lshr_b32 s88, s88, 8\n"
    "s_mul_i32 s90, s88, 5\n"
    "s_lshr_b32 s90, s90, 1\n"
    "s_sub_u32 s31, s31, s90\n"
    "s_sub_u32 s90, s32, s88\n"
    "s_sub_u32 s90, s90, 1\n"
    "s_and_b32 s90, s90, 3\n"
    "s_lshl2_add_u32 s88, s33, s88\n"
    "s_mul_i32 s88, s88, 3\n"
    "s_add_u32 s88, s88, s90\n"
    "s_mul_i32 s88, s88, 24832\n"
    "s_add_u32 s88, s88, 16265216\n"
    "s_lshl1_add_u32 s86, s31, s51\n"
    "s_lshl_b32 s86, s86, 12\n"
    "s_add_u32 s86, s86, s88\n"
    "s_lshl2_add_u32 s31, s31, s88\n"
    "s_lshl_b32 s87, %1, 6\n"
    "s_add_u32 s87, s87, %2\n"
    "s_lshl2_add_u32 s87, s87, 5636096\n"
    "s_or_b32 s88, %1, %2\n"
    "s_cselect_b32 s88, 2.0, 5777664\n"
    "s_lshl_b32 s47, s33, 9\n"
    "s_add_u32 s47, s47, 5738496\n"
    "v_lshlrev_b32_e32 v9, 3, v253\n"
    "s_cmp_eq_u32 s50, 0\n"
    "s_cselect_b32 s46, s35, s54\n"
    "s_cselect_b32 s45, s60, 2.0\n"
    "s_cmp_eq_u32 s51, 0\n"
    "s_cselect_b32 s46, s46, s45\n"
    "s_add_u32 s46, s46, 11022336\n"
    "s_waitcnt vmcnt(14)\n"
    "s_waitcnt lgkmcnt(0)\n"
    "s_barrier\n"
    "buffer_store_dword v40, v41, s[8:11], s58 offen offset:64 sc1\n"
    "buffer_load_dword v228, v9, s[8:11], s46 offen offset:0 sc1\n"
    "ds_read_b128 a[0:3], v6 offset:0\n"
    "ds_read_b128 a[4:7], v6 offset:1024\n"
    "ds_read_b128 a[8:11], v6 offset:2048\n"
    "ds_read_b128 a[12:15], v6 offset:3072\n"
    "ds_read_b128 a[16:19], v6 offset:4096\n"
    "ds_read_b128 a[20:23], v6 offset:5120\n"
    "ds_read_b128 a[24:27], v6 offset:6144\n"
    "ds_read_b128 a[28:31], v6 offset:7168\n"
    "s_waitcnt vmcnt(4)\n"
    "s_barrier\n"
    "ds_read_b128 a[160:163], v204 offset:0\n"
    "ds_read_b128 a[164:167], v205 offset:0\n"
    "ds_read_b128 a[168:171], v204 offset:4096\n"
    "ds_read_b128 a[172:175], v205 offset:4096\n"
    "ds_read_b128 a[176:179], v206 offset:0\n"
    "ds_read_b128 a[180:183], v207 offset:0\n"
    "ds_read_b128 a[184:187], v206 offset:4096\n"
    "ds_read_b128 a[188:191], v207 offset:4096\n"
    "s_add_u32 m0, s62, 16384\n"
    "s_nop 0\n"
    "buffer_load_dwordx4 v12, s[8:11], s40 offen offset:0 sc1 lds\n"
    "buffer_load_dwordx4 v13, s[8:11], s40 offen offset:1024 sc1 lds\n"
    "buffer_load_dwordx4 v14, s[8:11], s40 offen offset:2048 sc1 lds\n"
    "buffer_load_dwordx4 v15, s[8:11], s40 offen offset:3072 sc1 lds\n"
    "s_add_u32 s40, s40, 0x8000\n"
    "s_add_u32 m0, s62, 20480\n"
    "s_nop 0\n"
    "buffer_load_dwordx4 v12, s[8:11], s41 offen offset:0 sc1 lds\n"
    "buffer_load_dwordx4 v13, s[8:11], s41 offen offset:1024 sc1 lds\n"
    "buffer_load_dwordx4 v14, s[8:11], s41 offen offset:2048 sc1 lds\n"
    "buffer_load_dwordx4 v15, s[8:11], s41 offen offset:3072 sc1 lds\n"
    "s_add_u32 s41, s41, 0x8000\n"
    "s_waitcnt lgkmcnt(4)\n"
    "v_mfma_f32_16x16x32_bf16 v[220:223], a[160:163], a[0:3], 0\n"
    "ds_read_b128 a[32:35], v7 offset:16384\n"
    "ds_read_b128 a[36:39], v7 offset:17408\n"
    "v_mfma_f32_16x16x32_bf16 v[220:223], a[164:167], a[4:7], v[220:223]\n"
    "ds_read_b128 a[40:43], v7 offset:18432\n"
    "ds_read_b128 a[44:47], v7 offset:19456\n"
    "v_mfma_f32_16x16x32_bf16 v[220:223], a[168:171], a[8:11], v[220:223]\n"
    "ds_read_b128 a[48:51], v7 offset:20480\n"
    "ds_read_b128 a[52:55], v7 offset:21504\n"
    "v_mfma_f32_16x16x32_bf16 v[220:223], a[172:175], a[12:15], v[220:223]\n"
    "ds_read_b128 a[56:59], v7 offset:22528\n"
    "ds_read_b128 a[60:63], v7 offset:23552\n"
    "s_waitcnt lgkmcnt(8)\n"
    "v_mfma_f32_16x16x32_bf16 v[220:223], a[176:179], a[16:19], v[220:223]\n"
    "ds_read_b64_tr_b16 a[232:233], v208 offset:0\n"
    "ds_read_b64_tr_b16 a[234:235], v209 offset:0\n"
    "v_mfma_f32_16x16x32_bf16 v[220:223], a[180:183], a[20:23], v[220:223]\n"
    "ds_read_b64_tr_b16 a[236:237], v210 offset:0\n"
    "ds_read_b64_tr_b16 a[238:239], v211 offset:0\n"
    "v_mfma_f32_16x16x32_bf16 v[220:223], a[184:187], a[24:27], v[220:223]\n"
    "ds_read_b64_tr_b16 a[240:241], v208 offset:4096\n"
    "ds_read_b64_tr_b16 a[242:243], v209 offset:4096\n"
    "v_mfma_f32_16x16x32_bf16 v[220:223], a[188:191], a[28:31], v[220:223]\n"
    "buffer_load_dword v11, v5, s[8:11], s47 offen offset:0 sc1\n"
    "ds_read_b64_tr_b16 a[244:245], v210 offset:4096\n"
    "s_waitcnt lgkmcnt(11)\n"
    "v_mfma_f32_16x16x32_bf16 v[224:227], a[160:163], a[32:35], 0\n"
    "ds_read_b64_tr_b16 a[246:247], v211 offset:4096\n"
    "ds_read_b128 a[200:203], v203 offset:6144\n"
    "v_mfma_f32_16x16x32_bf16 v[224:227], a[164:167], a[36:39], v[224:227]\n"
    "ds_read_b128 a[204:207], v200 offset:2048\n"
    "ds_read_b128 a[208:211], v201 offset:2048\n"
    "v_mfma_f32_16x16x32_bf16 v[224:227], a[168:171], a[40:43], v[224:227]\n"
    "ds_read_b128 a[212:215], v200 offset:6144\n"
    "ds_read_b128 a[216:219], v201 offset:6144\n"
    "v_mfma_f32_16x16x32_bf16 v[224:227], a[172:175], a[44:47], v[224:227]\n"
    "ds_read_b128 a[220:223], v202 offset:2048\n"
    "ds_read_b128 a[224:227], v203 offset:2048\n"
    "s_waitcnt lgkmcnt(15)\n"
    "v_mfma_f32_16x16x32_bf16 v[224:227], a[176:179], a[48:51], v[224:227]\n"
    "ds_read_b128 a[228:231], v202 offset:6144\n"
    "v_mfma_f32_16x16x32_bf16 v[224:227], a[180:183], a[52:55], v[224:227]\n"
    "v_mfma_f32_16x16x32_bf16 v[224:227], a[184:187], a[56:59], v[224:227]\n"
    "v_mfma_f32_16x16x32_bf16 v[224:227], a[188:191], a[60:63], v[224:227]\n"
    "s_andn2_b32 s61, s32, s50\n"
    "s_bfe_i32 s61, s61, 0x10000\n"
    "ds_read_b128 a[160:163], v203 offset:4096\n"
    "ds_read_b128 a[164:167], v200 offset:0\n"
    "ds_read_b128 a[168:171], v201 offset:0\n"
    "ds_read_b128 a[172:175], v200 offset:4096\n"
    "ds_read_b128 a[176:179], v201 offset:4096\n"
    "ds_read_b128 a[180:183], v202 offset:0\n"
    "ds_read_b128 a[184:187], v203 offset:0\n"
    "ds_read_b128 a[188:191], v202 offset:4096\n"
    "s_and_b32 s57, s63, 0xffffdfff\n"
    "s_sub_u32 s57, s57, 16384\n"
    "v_add_u32_e32 v216, s57, v253\n"
    "s_lshl_b32 s45, s51, 12\n"
    "s_add_u32 s57, s57, s45\n"
    "s_mul_i32 s48, s33, 81920\n"
    "s_add_u32 s48, s48, s45\n"
    "s_add_u32 s48, s48, 11022336\n"
    "v_exp_f32_e32 v220, v220\n"
    "v_exp_f32_e32 v221, v221\n"
    "v_exp_f32_e32 v222, v222\n"
    "v_exp_f32_e32 v223, v223\n"
    "v_exp_f32_e32 v224, v224\n"
    "v_exp_f32_e32 v225, v225\n"
    "v_exp_f32_e32 v226, v226\n"
    "v_exp_f32_e32 v227, v227\n"
    "v_cvt_pk_bf16_f32 v220, v220, v221\n"
    "v_cvt_pk_bf16_f32 v221, v222, v223\n"
    "v_cvt_pk_bf16_f32 v222, v224, v225\n"
    "v_cvt_pk_bf16_f32 v223, v226, v227\n"
    "v_and_b32_e32 v222, s61, v222\n"
    "v_and_b32_e32 v223, s61, v223\n"
    "s_waitcnt vmcnt(11)\n"
    "s_waitcnt lgkmcnt(0)\n"
    "s_barrier\n"
    "v_xor_b32_e32 v1, s39, v1\n"
    "v_xor_b32_e32 v0, s39, v0\n"
    "v_or_b32_e32 v0, v0, v1\n"
    "v_cmp_ne_u32_e32 vcc, 0, v0\n"
    "s_nop 3\n"
    "s_cbranch_vccnz .Lat_hpoll\n"
    ".Lat_hpoll_ret:\n"
    "s_waitcnt vmcnt(0)\n"
    "v_cmp_ne_u32_e32 vcc, s39, v11\n"
    "s_nop 3\n"
    "s_cbranch_vccnz .Lat_qpoll\n"
    ".Lat_qpoll_ret:\n"
    "s_cmp_eq_u32 s50, 0\n"
    "s_cbranch_scc1 .Lat_qx\n"
    "s_bfe_u32 s45, s36, 0x40000\n"
    "s_add_u32 m0, s57, 0\n"
    "s_lshl_b32 s45, s45, 13\n"
    "s_add_u32 s45, s45, s48\n"
    "buffer_load_dwordx4 v253, s[8:11], s45 offen offset:0 sc1 lds\n"
    "buffer_load_dwordx4 v253, s[8:11], s45 offen offset:1024 sc1 lds\n"
    "buffer_load_dwordx4 v253, s[8:11], s45 offen offset:2048 sc1 lds\n"
    "buffer_load_dwordx4 v253, s[8:11], s45 offen offset:3072 sc1 lds\n"
    ".Lat_qx:\n"
    "s_bfe_u32 s45, s36, 0x40004\n"
    "s_add_u32 m0, s57, 8192\n"
    "s_lshl_b32 s45, s45, 13\n"
    "s_add_u32 s45, s45, s48\n"
    "buffer_load_dwordx4 v253, s[8:11], s45 offen offset:0 sc1 lds\n"
    "buffer_load_dwordx4 v253, s[8:11], s45 offen offset:1024 sc1 lds\n"
    "buffer_load_dwordx4 v253, s[8:11], s45 offen offset:2048 sc1 lds\n"
    "buffer_load_dwordx4 v253, s[8:11], s45 offen offset:3072 sc1 lds\n"
    "v_mfma_f32_16x16x16_bf16 v[52:55], a[246:247], v[222:223], 0\n"
    "v_mfma_f32_16x16x16_bf16 v[48:51], a[244:245], v[222:223], 0\n"
    "s_bfe_u32 s45, s36, 0x40008\n"
    "s_add_u32 m0, s57, 16384\n"
    "s_lshl_b32 s45, s45, 13\n"
    "s_add_u32 s45, s45, s48\n"
    "v_mfma_f32_16x16x16_bf16 v[44:47], a[242:243], v[222:223], 0\n"
    "buffer_load_dwordx4 v253, s[8:11], s45 offen offset:0 sc1 lds\n"
    "v_mfma_f32_16x16x16_bf16 v[40:43], a[240:241], v[222:223], 0\n"
    "v_mfma_f32_16x16x16_bf16 v[36:39], a[238:239], v[222:223], 0\n"
    "buffer_load_dwordx4 v253, s[8:11], s45 offen offset:1024 sc1 lds\n"
    "v_mfma_f32_16x16x16_bf16 v[32:35], a[236:237], v[222:223], 0\n"
    "v_mfma_f32_16x16x16_bf16 a[252:255], a[234:235], v[222:223], 0\n"
    "buffer_load_dwordx4 v253, s[8:11], s45 offen offset:2048 sc1 lds\n"
    "v_mfma_f32_16x16x16_bf16 a[248:251], a[232:233], v[222:223], 0\n"
    "v_mfma_f32_16x16x16_bf16 v[28:31], a[246:247], v[220:221], 0\n"
    "buffer_load_dwordx4 v253, s[8:11], s45 offen offset:3072 sc1 lds\n"
    "v_mfma_f32_16x16x16_bf16 v[24:27], a[244:245], v[220:221], 0\n"
    "s_bfe_u32 s45, s36, 0x4000c\n"
    "s_add_u32 m0, s57, 24576\n"
    "s_lshl_b32 s45, s45, 13\n"
    "s_add_u32 s45, s45, s48\n"
    "v_mfma_f32_16x16x16_bf16 v[20:23], a[242:243], v[220:221], 0\n"
    "buffer_load_dwordx4 v253, s[8:11], s45 offen offset:0 sc1 lds\n"
    "v_mfma_f32_16x16x16_bf16 v[16:19], a[240:241], v[220:221], 0\n"
    "v_mfma_f32_16x16x16_bf16 v[12:15], a[238:239], v[220:221], 0\n"
    "buffer_load_dwordx4 v253, s[8:11], s45 offen offset:1024 sc1 lds\n"
    "v_mfma_f32_16x16x16_bf16 v[8:11], a[236:237], v[220:221], 0\n"
    "v_mfma_f32_16x16x16_bf16 v[4:7], a[234:235], v[220:221], 0\n"
    "buffer_load_dwordx4 v253, s[8:11], s45 offen offset:2048 sc1 lds\n"
    "v_mfma_f32_16x16x16_bf16 v[0:3], a[232:233], v[220:221], 0\n"
    "v_mfma_f32_16x16x32_bf16 v[160:163], a[160:163], a[28:31], 0\n"
    "buffer_load_dwordx4 v253, s[8:11], s45 offen offset:3072 sc1 lds\n"
    "v_mfma_f32_16x16x32_bf16 v[160:163], a[164:167], a[0:3], v[160:163]\n"
    "s_mov_b32 m0, s49\n"
    "v_mfma_f32_16x16x32_bf16 v[160:163], a[168:171], a[4:7], v[160:163]\n"
    "v_mfma_f32_16x16x32_bf16 v[160:163], a[172:175], a[8:11], v[160:163]\n"
    "v_mfma_f32_16x16x32_bf16 v[160:163], a[176:179], a[12:15], v[160:163]\n"
    "v_mfma_f32_16x16x32_bf16 v[160:163], a[180:183], a[16:19], v[160:163]\n"
    "v_mfma_f32_16x16x32_bf16 v[160:163], a[184:187], a[20:23], v[160:163]\n"
    "v_mfma_f32_16x16x32_bf16 v[160:163], a[188:191], a[24:27], v[160:163]\n"
    "v_mfma_f32_16x16x32_bf16 v[164:167], a[200:203], a[28:31], 0\n"
    "v_mfma_f32_16x16x32_bf16 v[164:167], a[204:207], a[0:3], v[164:167]\n"
    "v_mfma_f32_16x16x32_bf16 v[164:167], a[208:211], a[4:7], v[164:167]\n"
    "v_exp_f32_e32 v160, v160\n"
    "v_mfma_f32_16x16x32_bf16 v[164:167], a[212:215], a[8:11], v[164:167]\n"
    "v_exp_f32_e32 v161, v161\n"
    "v_mfma_f32_16x16x32_bf16 v[164:167], a[216:219], a[12:15], v[164:167]\n"
    "v_exp_f32_e32 v162, v162\n"
    "v_mfma_f32_16x16x32_bf16 v[164:167], a[220:223], a[16:19], v[164:167]\n"
    "v_exp_f32_e32 v163, v163\n"
    "v_mfma_f32_16x16x32_bf16 v[164:167], a[224:227], a[20:23], v[164:167]\n"
    "v_cvt_pk_bf16_f32 v160, v160, v161\n"
    "v_mfma_f32_16x16x32_bf16 v[164:167], a[228:231], a[24:27], v[164:167]\n"
    "s_waitcnt vmcnt(8)\n"
    "s_barrier\n"
    "s_cmp_eq_u32 s50, 0\n"
    "s_cbranch_scc1 .Lat_qy\n"
    "ds_read_b128 a[60:63], v216 offset:7168\n"
    "ds_read_b128 a[32:35], v216 offset:0\n"
    "ds_read_b128 a[36:39], v216 offset:1024\n"
    "ds_read_b128 a[40:43], v216 offset:2048\n"
    "ds_read_b128 a[44:47], v216 offset:3072\n"
    "ds_read_b128 a[48:51], v216 offset:4096\n"
    "ds_read_b128 a[52:55], v216 offset:5120\n"
    "ds_read_b128 a[56:59], v216 offset:6144\n"
    ".Lat_qy:\n"
    "ds_read_b128 a[92:95], v216 offset:15360\n"
    "ds_read_b128 a[64:67], v216 offset:8192\n"
    "s_waitcnt lgkmcnt(8)\n"
    "v_mfma_f32_16x16x32_bf16 v[168:171], a[160:163], a[60:63], 0\n"
    "v_cvt_pk_bf16_f32 v161, v162, v163\n"
    "v_mfma_f32_16x16x32_bf16 v[168:171], a[164:167], a[32:35], v[168:171]\n"
    "s_waitcnt lgkmcnt(6)\n"
    "ds_read_b128 a[68:71], v216 offset:9216\n"
    "v_mfma_f32_16x16x32_bf16 v[168:171], a[168:171], a[36:39], v[168:171]\n"
    "ds_read_b128 a[72:75], v216 offset:10240\n"
    "v_mfma_f32_16x16x32_bf16 v[168:171], a[172:175], a[40:43], v[168:171]\n"
    "s_waitcnt lgkmcnt(6)\n"
    "ds_read_b128 a[76:79], v216 offset:11264\n"
    "ds_write_addtid_b32 v160 offset:0\n"
    "v_mfma_f32_16x16x32_bf16 v[168:171], a[176:179], a[44:47], v[168:171]\n"
    "v_exp_f32_e32 v164, v164\n"
    "v_mfma_f32_16x16x32_bf16 v[168:171], a[180:183], a[48:51], v[168:171]\n"
    "s_waitcnt lgkmcnt(6)\n"
    "ds_read_b128 a[80:83], v216 offset:12288\n"
    "v_mfma_f32_16x16x32_bf16 v[168:171], a[184:187], a[52:55], v[168:171]\n"
    "ds_read_b128 a[84:87], v216 offset:13312\n"
    "v_mfma_f32_16x16x32_bf16 v[168:171], a[188:191], a[56:59], v[168:171]\n"
    "ds_read_b128 a[88:91], v216 offset:14336\n"
    "ds_write_addtid_b32 v161 offset:256\n"
    "v_mfma_f32_16x16x32_bf16 v[172:175], a[200:203], a[60:63], 0\n"
    "v_exp_f32_e32 v165, v165\n"
    "v_mfma_f32_16x16x32_bf16 v[172:175], a[204:207], a[32:35], v[172:175]\n"
    "v_exp_f32_e32 v166, v166\n"
    "v_mfma_f32_16x16x32_bf16 v[172:175], a[208:211], a[36:39], v[172:175]\n"
    "v_exp_f32_e32 v167, v167\n"
    "v_mfma_f32_16x16x32_bf16 v[172:175], a[212:215], a[40:43], v[172:175]\n"
    "v_cvt_pk_bf16_f32 v162, v164, v165\n"
    "v_cvt_pk_bf16_f32 v163, v166, v167\n"
    "v_mfma_f32_16x16x32_bf16 v[172:175], a[216:219], a[44:47], v[172:175]\n"
    "ds_write_addtid_b32 v162 offset:512\n"
    "v_exp_f32_e32 v168, v168\n"
    "v_mfma_f32_16x16x32_bf16 v[172:175], a[220:223], a[48:51], v[172:175]\n"
    "v_exp_f32_e32 v169, v169\n"
    "v_mfma_f32_16x16x32_bf16 v[172:175], a[224:227], a[52:55], v[172:175]\n"
    "v_exp_f32_e32 v170, v170\n"
    "v_mfma_f32_16x16x32_bf16 v[172:175], a[228:231], a[56:59], v[172:175]\n"
    "s_waitcnt lgkmcnt(9)\n"
    "v_exp_f32_e32 v171, v171\n"
    "v_mfma_f32_16x16x32_bf16 v[176:179], a[160:163], a[92:95], 0\n"
    "ds_write_addtid_b32 v163 offset:768\n"
    "v_cvt_pk_bf16_f32 v168, v168, v169\n"
    "v_mfma_f32_16x16x32_bf16 v[176:179], a[164:167], a[64:67], v[176:179]\n"
    "s_waitcnt lgkmcnt(8)\n"
    "ds_write_addtid_b32 v168 offset:4096\n"
    "v_cvt_pk_bf16_f32 v169, v170, v171\n"
    "v_mfma_f32_16x16x32_bf16 v[176:179], a[168:171], a[68:71], v[176:179]\n"
    "v_exp_f32_e32 v172, v172\n"
    "v_mfma_f32_16x16x32_bf16 v[176:179], a[172:175], a[72:75], v[176:179]\n"
    "s_waitcnt lgkmcnt(6)\n"
    "v_exp_f32_e32 v173, v173\n"
    "v_mfma_f32_16x16x32_bf16 v[176:179], a[176:179], a[76:79], v[176:179]\n"
    "v_exp_f32_e32 v174, v174\n"
    "v_mfma_f32_16x16x32_bf16 v[176:179], a[180:183], a[80:83], v[176:179]\n"
    "s_waitcnt vmcnt(0)\n"
    "s_barrier\n"
    "s_waitcnt lgkmcnt(4)\n"
    "v_mfma_f32_16x16x32_bf16 v[176:179], a[184:187], a[84:87], v[176:179]\n"
    "ds_write_addtid_b32 v169 offset:4352\n"
    "v_exp_f32_e32 v175, v175\n"
    "v_mfma_f32_16x16x32_bf16 v[176:179], a[188:191], a[88:91], v[176:179]\n"
    "ds_read_b128 a[124:127], v216 offset:23552\n"
    "v_cvt_pk_bf16_f32 v170, v172, v173\n"
    "v_mfma_f32_16x16x32_bf16 v[180:183], a[200:203], a[92:95], 0\n"
    "ds_read_b128 a[96:99], v216 offset:16384\n"
    "v_cvt_pk_bf16_f32 v171, v174, v175\n"
    "v_mfma_f32_16x16x32_bf16 v[180:183], a[204:207], a[64:67], v[180:183]\n"
    "ds_read_b128 a[100:103], v216 offset:17408\n"
    "v_exp_f32_e32 v176, v176\n"
    "v_mfma_f32_16x16x32_bf16 v[180:183], a[208:211], a[68:71], v[180:183]\n"
    "ds_read_b128 a[104:107], v216 offset:18432\n"
    "ds_write_addtid_b32 v170 offset:4608\n"
    "v_mfma_f32_16x16x32_bf16 v[180:183], a[212:215], a[72:75], v[180:183]\n"
    "ds_read_b128 a[108:111], v216 offset:19456\n"
    "v_exp_f32_e32 v177, v177\n"
    "v_mfma_f32_16x16x32_bf16 v[180:183], a[216:219], a[76:79], v[180:183]\n"
    "ds_read_b128 a[112:115], v216 offset:20480\n"
    "v_exp_f32_e32 v178, v178\n"
    "v_mfma_f32_16x16x32_bf16 v[180:183], a[220:223], a[80:83], v[180:183]\n"
    "ds_read_b128 a[116:119], v216 offset:21504\n"
    "ds_write_addtid_b32 v171 offset:4864\n"
    "v_mfma_f32_16x16x32_bf16 v[180:183], a[224:227], a[84:87], v[180:183]\n"
    "ds_read_b128 a[120:123], v216 offset:22528\n"
    "v_mfma_f32_16x16x32_bf16 v[180:183], a[228:231], a[88:91], v[180:183]\n"
    "s_waitcnt lgkmcnt(8)\n"
    "s_sub_u32 m0, s63, 16384\n"
    "v_exp_f32_e32 v179, v179\n"
    "v_mfma_f32_16x16x32_bf16 v[184:187], a[160:163], a[124:127], 0\n"
    "ds_read_b128 a[156:159], v216 offset:31744\n"
    "buffer_load_dwordx4 v248, s[8:11], s40 offen offset:0 sc1 lds\n"
    "v_cvt_pk_bf16_f32 v176, v176, v177\n"
    "v_mfma_f32_16x16x32_bf16 v[184:187], a[164:167], a[96:99], v[184:187]\n"
    "s_waitcnt lgkmcnt(7)\n"
    "ds_read_b128 a[128:131], v216 offset:24576\n"
    "v_cvt_pk_bf16_f32 v177, v178, v179\n"
    "v_mfma_f32_16x16x32_bf16 v[184:187], a[168:171], a[100:103], v[184:187]\n"
    "ds_read_b128 a[132:135], v216 offset:25600\n"
    "v_mfma_f32_16x16x32_bf16 v[184:187], a[172:175], a[104:107], v[184:187]\n"
    "s_waitcnt lgkmcnt(6)\n"
    "ds_read_b128 a[136:139], v216 offset:26624\n"
    "v_mfma_f32_16x16x32_bf16 v[184:187], a[176:179], a[108:111], v[184:187]\n"
    "ds_read_b128 a[140:143], v216 offset:27648\n"
    "buffer_load_dwordx4 v248, s[8:11], s40 offen offset:1024 sc1 lds\n"
    "v_mfma_f32_16x16x32_bf16 v[184:187], a[180:183], a[112:115], v[184:187]\n"
    "s_waitcnt lgkmcnt(5)\n"
    "ds_read_b128 a[144:147], v216 offset:28672\n"
    "v_mfma_f32_16x16x32_bf16 v[184:187], a[184:187], a[116:119], v[184:187]\n"
    "ds_read_b128 a[148:151], v216 offset:29696\n"
    "v_mfma_f32_16x16x32_bf16 v[184:187], a[188:191], a[120:123], v[184:187]\n"
    "ds_read_b128 a[152:155], v216 offset:30720\n"
    "v_mfma_f32_16x16x32_bf16 v[188:191], a[200:203], a[124:127], 0\n"
    "buffer_load_dwordx4 v248, s[8:11], s40 offen offset:2048 sc1 lds\n"
    "v_exp_f32_e32 v180, v180\n"
    "v_mfma_f32_16x16x32_bf16 v[188:191], a[204:207], a[96:99], v[188:191]\n"
    "ds_write_b32 v249, v176 offset:8192\n"
    "v_exp_f32_e32 v181, v181\n"
    "v_mfma_f32_16x16x32_bf16 v[188:191], a[208:211], a[100:103], v[188:191]\n"
    "ds_write_b32 v249, v177 offset:8448\n"
    "v_exp_f32_e32 v182, v182\n"
    "v_mfma_f32_16x16x32_bf16 v[188:191], a[212:215], a[104:107], v[188:191]\n"
    "v_exp_f32_e32 v183, v183\n"
    "v_mfma_f32_16x16x32_bf16 v[188:191], a[216:219], a[108:111], v[188:191]\n"
    "buffer_load_dwordx4 v248, s[8:11], s40 offen offset:3072 sc1 lds\n"
    "v_cvt_pk_bf16_f32 v178, v180, v181\n"
    "v_cvt_pk_bf16_f32 v179, v182, v183\n"
    "v_mfma_f32_16x16x32_bf16 v[188:191], a[220:223], a[112:115], v[188:191]\n"
    "s_add_u32 s40, s40, 0x8000\n"
    "ds_write_b32 v249, v178 offset:8704\n"
    "v_exp_f32_e32 v184, v184\n"
    "v_mfma_f32_16x16x32_bf16 v[188:191], a[224:227], a[116:119], v[188:191]\n"
    "s_sub_u32 m0, s63, 12288\n"
    "ds_write_b32 v249, v179 offset:8960\n"
    "v_exp_f32_e32 v185, v185\n"
    "v_mfma_f32_16x16x32_bf16 v[188:191], a[228:231], a[120:123], v[188:191]\n"
    "s_waitcnt lgkmcnt(10)\n"
    "v_exp_f32_e32 v186, v186\n"
    "v_mfma_f32_16x16x32_bf16 v[192:195], a[160:163], a[156:159], 0\n"
    "buffer_load_dwordx4 v248, s[8:11], s41 offen offset:0 sc1 lds\n"
    "v_exp_f32_e32 v187, v187\n"
    "v_mfma_f32_16x16x32_bf16 v[192:195], a[164:167], a[128:131], v[192:195]\n"
    "s_waitcnt lgkmcnt(8)\n"
    "ds_read_b128 a[160:163], v203 offset:20480\n"
    "v_cvt_pk_bf16_f32 v184, v184, v185\n"
    "v_mfma_f32_16x16x32_bf16 v[192:195], a[168:171], a[132:135], v[192:195]\n"
    "ds_read_b128 a[164:167], v200 offset:16384\n"
    "v_cvt_pk_bf16_f32 v185, v186, v187\n"
    "v_mfma_f32_16x16x32_bf16 v[192:195], a[172:175], a[136:139], v[192:195]\n"
    "s_waitcnt lgkmcnt(8)\n"
    "ds_read_b128 a[168:171], v201 offset:16384\n"
    "v_exp_f32_e32 v188, v188\n"
    "v_mfma_f32_16x16x32_bf16 v[192:195], a[176:179], a[140:143], v[192:195]\n"
    "ds_read_b128 a[172:175], v200 offset:20480\n"
    "buffer_load_dwordx4 v248, s[8:11], s41 offen offset:1024 sc1 lds\n"
    "v_exp_f32_e32 v189, v189\n"
    "v_mfma_f32_16x16x32_bf16 v[192:195], a[180:183], a[144:147], v[192:195]\n"
    "s_waitcnt lgkmcnt(8)\n"
    "ds_read_b128 a[176:179], v201 offset:20480\n"
    "v_exp_f32_e32 v190, v190\n"
    "v_mfma_f32_16x16x32_bf16 v[192:195], a[184:187], a[148:151], v[192:195]\n"
    "ds_read_b128 a[180:183], v202 offset:16384\n"
    "v_mfma_f32_16x16x32_bf16 v[192:195], a[188:191], a[152:155], v[192:195]\n"
    "ds_read_b128 a[184:187], v203 offset:16384\n"
    "v_mfma_f32_16x16x32_bf16 v[196:199], a[200:203], a[156:159], 0\n"
    "ds_read_b128 a[188:191], v202 offset:20480\n"
    "buffer_load_dwordx4 v248, s[8:11], s41 offen offset:2048 sc1 lds\n"
    "v_mfma_f32_16x16x32_bf16 v[196:199], a[204:207], a[128:131], v[196:199]\n"
    "ds_write_b32 v249, v184 offset:12288\n"
    "v_exp_f32_e32 v191, v191\n"
    "v_mfma_f32_16x16x32_bf16 v[196:199], a[208:211], a[132:135], v[196:199]\n"
    "ds_write_b32 v249, v185 offset:12544\n"
    "v_cvt_pk_bf16_f32 v186, v188, v189\n"
    "v_cvt_pk_bf16_f32 v187, v190, v191\n"
    "v_mfma_f32_16x16x32_bf16 v[196:199], a[212:215], a[136:139], v[196:199]\n"
    "ds_write_b32 v249, v186 offset:12800\n"
    "v_exp_f32_e32 v192, v192\n"
    "v_mfma_f32_16x16x32_bf16 v[196:199], a[216:219], a[140:143], v[196:199]\n"
    "buffer_load_dwordx4 v248, s[8:11], s41 offen offset:3072 sc1 lds\n"
    "v_exp_f32_e32 v193, v193\n"
    "v_mfma_f32_16x16x32_bf16 v[196:199], a[220:223], a[144:147], v[196:199]\n"
    "s_add_u32 s41, s41, 0x8000\n"
    "ds_write_b32 v249, v187 offset:13056\n"
    "v_exp_f32_e32 v194, v194\n"
    "v_mfma_f32_16x16x32_bf16 v[196:199], a[224:227], a[148:151], v[196:199]\n"
    "v_exp_f32_e32 v195, v195\n"
    "v_mfma_f32_16x16x32_bf16 v[196:199], a[228:231], a[152:155], v[196:199]\n"
    "v_cvt_pk_bf16_f32 v192, v192, v193\n"
    "ds_write_b32 v249, v192 offset:16384\n"
    "v_cvt_pk_bf16_f32 v193, v194, v195\n"
    "ds_write_b32 v249, v193 offset:16640\n"
    "s_cmp_lg_u32 s34, 0\n"
    "s_cselect_b32 s44, 2, 1\n"
    "s_waitcnt lgkmcnt(4)\n"
    "s_nop 0\n"
    "v_exp_f32_e32 v196, v196\n"
    "v_exp_f32_e32 v197, v197\n"
    "v_exp_f32_e32 v198, v198\n"
    "v_exp_f32_e32 v199, v199\n"
    "v_cvt_pk_bf16_f32 v194, v196, v197\n"
    "v_cvt_pk_bf16_f32 v195, v198, v199\n"
    "ds_write_b32 v249, v194 offset:16896\n"
    "ds_write_b32 v249, v195 offset:17152\n"
    "s_cbranch_scc1 .Lat_p2e\n"
    ".p2align 6\n"
    ".Lat_p:\n"
    "v_mfma_f32_16x16x32_bf16 v[200:203], a[160:163], a[28:31], 0\n"
    "v_mfma_f32_16x16x32_bf16 v[200:203], a[164:167], a[0:3], v[200:203]\n"
    "v_mfma_f32_16x16x32_bf16 v[208:211], a[164:167], a[32:35], 0\n"
    "v_mfma_f32_16x16x32_bf16 v[208:211], a[160:163], a[60:63], v[208:211]\n"
    "v_mfma_f32_16x16x32_bf16 v[216:219], a[160:163], a[92:95], 0\n"
    "v_mfma_f32_16x16x32_bf16 v[216:219], a[164:167], a[64:67], v[216:219]\n"
    "s_waitcnt lgkmcnt(0)\n"
    "v_mfma_f32_16x16x32_bf16 v[224:227], a[164:167], a[96:99], 0\n"
    "s_barrier\n"
    "v_mfma_f32_16x16x32_bf16 v[224:227], a[160:163], a[124:127], v[224:227]\n"
    "s_add_u32 m0, s43, 49152\n"
    "ds_read2st64_b32 v[164:165], v250 offset0:0 offset1:1\n"
    "v_mfma_f32_16x16x32_bf16 v[232:235], a[160:163], a[156:159], 0\n"
    "ds_read_b128 a[160:163], v243 offset:22528\n"
    "v_mfma_f32_16x16x32_bf16 v[232:235], a[164:167], a[128:131], v[232:235]\n"
    "ds_read_b128 a[164:167], v240 offset:18432\n"
    "v_mfma_f32_16x16x32_bf16 v[232:235], a[168:171], a[132:135], v[232:235]\n"
    "buffer_load_dwordx4 v248, s[8:11], s40 offen offset:0 sc1 lds\n"
    "v_mfma_f32_16x16x32_bf16 v[232:235], a[172:175], a[136:139], v[232:235]\n"
    "v_mfma_f32_16x16x32_bf16 v[224:227], a[172:175], a[104:107], v[224:227]\n"
    "v_mfma_f32_16x16x32_bf16 v[224:227], a[168:171], a[100:103], v[224:227]\n"
    "ds_read2st64_b32 v[166:167], v250 offset0:2 offset1:3\n"
    "v_mfma_f32_16x16x32_bf16 v[216:219], a[168:171], a[68:71], v[216:219]\n"
    "v_mfma_f32_16x16x32_bf16 v[216:219], a[172:175], a[72:75], v[216:219]\n"
    "buffer_load_dwordx4 v248, s[8:11], s40 offen offset:1024 sc1 lds\n"
    "v_mfma_f32_16x16x32_bf16 v[208:211], a[172:175], a[40:43], v[208:211]\n"
    "v_mfma_f32_16x16x32_bf16 v[208:211], a[168:171], a[36:39], v[208:211]\n"
    "ds_read2st64_b32 v[172:173], v250 offset0:16 offset1:17\n"
    "v_mfma_f32_16x16x32_bf16 v[200:203], a[168:171], a[4:7], v[200:203]\n"
    "ds_read_b128 a[168:171], v241 offset:18432\n"
    "v_mfma_f32_16x16x32_bf16 v[200:203], a[172:175], a[8:11], v[200:203]\n"
    "ds_read_b128 a[172:175], v240 offset:22528\n"
    "v_mfma_f32_16x16x32_bf16 v[200:203], a[176:179], a[12:15], v[200:203]\n"
    "buffer_load_dwordx4 v248, s[8:11], s40 offen offset:2048 sc1 lds\n"
    "v_mfma_f32_16x16x32_bf16 v[200:203], a[180:183], a[16:19], v[200:203]\n"
    "ds_read2st64_b32 v[174:175], v250 offset0:18 offset1:19\n"
    "v_mfma_f32_16x16x32_bf16 v[208:211], a[180:183], a[48:51], v[208:211]\n"
    "v_mfma_f32_16x16x32_bf16 v[208:211], a[176:179], a[44:47], v[208:211]\n"
    "v_mfma_f32_16x16x32_bf16 v[216:219], a[176:179], a[76:79], v[216:219]\n"
    "ds_read2st64_b32 v[180:181], v250 offset0:32 offset1:33\n"
    "v_mfma_f32_16x16x32_bf16 v[216:219], a[180:183], a[80:83], v[216:219]\n"
    "buffer_load_dwordx4 v248, s[8:11], s40 offen offset:3072 sc1 lds\n"
    "v_mfma_f32_16x16x32_bf16 v[224:227], a[180:183], a[112:115], v[224:227]\n"
    "s_add_u32 m0, s43, 53248\n"
    "v_mfma_f32_16x16x32_bf16 v[224:227], a[176:179], a[108:111], v[224:227]\n"
    "s_add_u32 s40, s40, 0x8000\n"
    "ds_read2st64_b32 v[182:183], v250 offset0:34 offset1:35\n"
    "v_mfma_f32_16x16x32_bf16 v[232:235], a[176:179], a[140:143], v[232:235]\n"
    "ds_read_b128 a[176:179], v241 offset:22528\n"
    "v_mfma_f32_16x16x32_bf16 v[232:235], a[180:183], a[144:147], v[232:235]\n"
    "ds_read_b128 a[180:183], v242 offset:18432\n"
    "v_mfma_f32_16x16x32_bf16 v[232:235], a[184:187], a[148:151], v[232:235]\n"
    "buffer_load_dwordx4 v248, s[8:11], s41 offen offset:0 sc1 lds\n"
    "v_mfma_f32_16x16x32_bf16 v[232:235], a[188:191], a[152:155], v[232:235]\n"
    "v_mfma_f32_16x16x32_bf16 v[224:227], a[188:191], a[120:123], v[224:227]\n"
    "ds_read2st64_b32 v[188:189], v250 offset0:48 offset1:49\n"
    "v_mfma_f32_16x16x32_bf16 v[224:227], a[184:187], a[116:119], v[224:227]\n"
    "v_mfma_f32_16x16x32_bf16 v[216:219], a[184:187], a[84:87], v[216:219]\n"
    "v_exp_f32_e32 v232, v232\n"
    "v_mfma_f32_16x16x32_bf16 v[216:219], a[188:191], a[88:91], v[216:219]\n"
    "buffer_load_dwordx4 v248, s[8:11], s41 offen offset:1024 sc1 lds\n"
    "v_mfma_f32_16x16x32_bf16 v[208:211], a[188:191], a[56:59], v[208:211]\n"
    "ds_read2st64_b32 v[190:191], v250 offset0:50 offset1:51\n"
    "v_mfma_f32_16x16x32_bf16 v[208:211], a[184:187], a[52:55], v[208:211]\n"
    "v_exp_f32_e32 v233, v233\n"
    "v_mfma_f32_16x16x32_bf16 v[200:203], a[184:187], a[20:23], v[200:203]\n"
    "ds_read_b128 a[184:187], v243 offset:18432\n"
    "v_mfma_f32_16x16x32_bf16 v[200:203], a[188:191], a[24:27], v[200:203]\n"
    "s_waitcnt lgkmcnt(8)\n"
    "ds_read_b128 a[188:191], v242 offset:22528\n"
    "v_mfma_f32_16x16x32_bf16 v[204:207], a[160:163], a[28:31], 0\n"
    "buffer_load_dwordx4 v248, s[8:11], s41 offen offset:2048 sc1 lds\n"
    "v_mfma_f32_16x16x32_bf16 v[204:207], a[164:167], a[0:3], v[204:207]\n"
    "ds_read2st64_b32 v[196:197], v250 offset0:64 offset1:65\n"
    "v_mfma_f32_16x16x32_bf16 v[212:215], a[164:167], a[32:35], 0\n"
    "v_exp_f32_e32 v234, v234\n"
    "v_mfma_f32_16x16x32_bf16 v[212:215], a[160:163], a[60:63], v[212:215]\n"
    "ds_read2st64_b32 v[198:199], v250 offset0:66 offset1:67\n"
    "v_mfma_f32_16x16x32_bf16 v[220:223], a[160:163], a[92:95], 0\n"
    "v_exp_f32_e32 v235, v235\n"
    "v_mfma_f32_16x16x32_bf16 v[220:223], a[164:167], a[64:67], v[220:223]\n"
    "buffer_load_dwordx4 v248, s[8:11], s41 offen offset:3072 sc1 lds\n"
    "v_mfma_f32_16x16x32_bf16 v[228:231], a[164:167], a[96:99], 0\n"
    "s_add_u32 s41, s41, 0x8000\n"
    "v_cvt_pk_bf16_f32 v232, v232, v233\n"
    "v_mfma_f32_16x16x32_bf16 v[228:231], a[160:163], a[124:127], v[228:231]\n"
    "v_exp_f32_e32 v224, v224\n"
    "v_mfma_f32_16x16x32_bf16 v[236:239], a[160:163], a[156:159], 0\n"
    "ds_read_b64_tr_b16 a[160:161], v244 offset:0\n"
    "v_mfma_f32_16x16x32_bf16 v[236:239], a[164:167], a[128:131], v[236:239]\n"
    "ds_read_b64_tr_b16 a[162:163], v244 offset:2048\n"
    "v_mfma_f32_16x16x32_bf16 v[236:239], a[168:171], a[132:135], v[236:239]\n"
    "ds_read_b64_tr_b16 a[164:165], v244 offset:8192\n"
    "v_mfma_f32_16x16x32_bf16 v[236:239], a[172:175], a[136:139], v[236:239]\n"
    "ds_read_b64_tr_b16 a[166:167], v244 offset:10240\n"
    "v_mfma_f32_16x16x32_bf16 v[228:231], a[172:175], a[104:107], v[228:231]\n"
    "v_exp_f32_e32 v225, v225\n"
    "v_mfma_f32_16x16x32_bf16 v[228:231], a[168:171], a[100:103], v[228:231]\n"
    "v_cvt_pk_bf16_f32 v233, v234, v235\n"
    "v_mfma_f32_16x16x32_bf16 v[220:223], a[168:171], a[68:71], v[220:223]\n"
    "v_exp_f32_e32 v226, v226\n"
    "v_mfma_f32_16x16x32_bf16 v[220:223], a[172:175], a[72:75], v[220:223]\n"
    "v_exp_f32_e32 v227, v227\n"
    "v_mfma_f32_16x16x32_bf16 v[212:215], a[172:175], a[40:43], v[212:215]\n"
    "v_cvt_pk_bf16_f32 v224, v224, v225\n"
    "v_mfma_f32_16x16x32_bf16 v[212:215], a[168:171], a[36:39], v[212:215]\n"
    "v_exp_f32_e32 v216, v216\n"
    "v_mfma_f32_16x16x32_bf16 v[204:207], a[168:171], a[4:7], v[204:207]\n"
    "ds_read_b64_tr_b16 a[168:169], v245 offset:0\n"
    "v_mfma_f32_16x16x32_bf16 v[204:207], a[172:175], a[8:11], v[204:207]\n"
    "s_waitcnt lgkmcnt(7)\n"
    "ds_read_b64_tr_b16 a[170:171], v245 offset:2048\n"
    "v_mfma_f32_16x16x32_bf16 v[204:207], a[176:179], a[12:15], v[204:207]\n"
    "ds_read_b64_tr_b16 a[172:173], v245 offset:8192\n"
    "v_mfma_f32_16x16x32_bf16 v[204:207], a[180:183], a[16:19], v[204:207]\n"
    "ds_read_b64_tr_b16 a[174:175], v245 offset:10240\n"
    "v_mfma_f32_16x16x32_bf16 v[212:215], a[180:183], a[48:51], v[212:215]\n"
    "v_exp_f32_e32 v217, v217\n"
    "v_mfma_f32_16x16x32_bf16 v[212:215], a[176:179], a[44:47], v[212:215]\n"
    "v_cvt_pk_bf16_f32 v225, v226, v227\n"
    "v_mfma_f32_16x16x32_bf16 v[220:223], a[176:179], a[76:79], v[220:223]\n"
    "v_exp_f32_e32 v218, v218\n"
    "v_mfma_f32_16x16x32_bf16 v[220:223], a[180:183], a[80:83], v[220:223]\n"
    "v_exp_f32_e32 v219, v219\n"
    "v_mfma_f32_16x16x32_bf16 v[228:231], a[180:183], a[112:115], v[228:231]\n"
    "v_cvt_pk_bf16_f32 v216, v216, v217\n"
    "v_mfma_f32_16x16x32_bf16 v[228:231], a[176:179], a[108:111], v[228:231]\n"
    "v_exp_f32_e32 v208, v208\n"
    "v_mfma_f32_16x16x32_bf16 v[236:239], a[176:179], a[140:143], v[236:239]\n"
    "ds_read_b64_tr_b16 a[176:177], v246 offset:0\n"
    "v_mfma_f32_16x16x32_bf16 v[236:239], a[180:183], a[144:147], v[236:239]\n"
    "ds_read_b64_tr_b16 a[178:179], v246 offset:2048\n"
    "v_mfma_f32_16x16x32_bf16 v[236:239], a[184:187], a[148:151], v[236:239]\n"
    "ds_read_b64_tr_b16 a[180:181], v246 offset:8192\n"
    "v_mfma_f32_16x16x32_bf16 v[236:239], a[188:191], a[152:155], v[236:239]\n"
    "ds_read_b64_tr_b16 a[182:183], v246 offset:10240\n"
    "v_mfma_f32_16x16x32_bf16 v[228:231], a[188:191], a[120:123], v[228:231]\n"
    "v_exp_f32_e32 v209, v209\n"
    "v_mfma_f32_16x16x32_bf16 v[228:231], a[184:187], a[116:119], v[228:231]\n"
    "v_cvt_pk_bf16_f32 v217, v218, v219\n"
    "v_mfma_f32_16x16x32_bf16 v[220:223], a[184:187], a[84:87], v[220:223]\n"
    "v_exp_f32_e32 v210, v210\n"
    "v_mfma_f32_16x16x32_bf16 v[220:223], a[188:191], a[88:91], v[220:223]\n"
    "v_exp_f32_e32 v211, v211\n"
    "v_mfma_f32_16x16x32_bf16 v[212:215], a[188:191], a[56:59], v[212:215]\n"
    "v_cvt_pk_bf16_f32 v208, v208, v209\n"
    "v_mfma_f32_16x16x32_bf16 v[212:215], a[184:187], a[52:55], v[212:215]\n"
    "v_exp_f32_e32 v200, v200\n"
    "v_mfma_f32_16x16x32_bf16 v[204:207], a[184:187], a[20:23], v[204:207]\n"
    "ds_read_b64_tr_b16 a[184:185], v247 offset:0\n"
    "v_mfma_f32_16x16x32_bf16 v[204:207], a[188:191], a[24:27], v[204:207]\n"
    "s_waitcnt lgkmcnt(5)\n"
    "ds_read_b64_tr_b16 a[186:187], v247 offset:2048\n"
    "v_mfma_f32_16x16x32_bf16 v[0:3], a[160:163], v[160:163], v[0:3]\n"
    "ds_read_b64_tr_b16 a[188:189], v247 offset:8192\n"
    "v_mfma_f32_16x16x32_bf16 v[0:3], a[164:167], v[164:167], v[0:3]\n"
    "ds_read_b64_tr_b16 a[190:191], v247 offset:10240\n"
    "v_mfma_f32_16x16x32_bf16 a[248:251], a[164:167], v[172:175], a[248:251]\n"
    "v_exp_f32_e32 v201, v201\n"
    "v_mfma_f32_16x16x32_bf16 a[248:251], a[160:163], v[168:171], a[248:251]\n"
    "v_cvt_pk_bf16_f32 v209, v210, v211\n"
    "v_mfma_f32_16x16x32_bf16 v[56:59], a[160:163], v[176:179], v[56:59]\n"
    "v_exp_f32_e32 v202, v202\n"
    "v_mfma_f32_16x16x32_bf16 v[56:59], a[164:167], v[180:183], v[56:59]\n"
    "v_exp_f32_e32 v203, v203\n"
    "v_mfma_f32_16x16x32_bf16 v[60:63], a[164:167], v[188:191], v[60:63]\n"
    "v_cvt_pk_bf16_f32 v200, v200, v201\n"
    "v_mfma_f32_16x16x32_bf16 v[60:63], a[160:163], v[184:187], v[60:63]\n"
    "v_cvt_pk_bf16_f32 v201, v202, v203\n"
    "v_mfma_f32_16x16x32_bf16 v[64:67], a[160:163], v[192:195], v[64:67]\n"
    "ds_read_b64_tr_b16 a[160:161], v244 offset:4096\n"
    "v_mfma_f32_16x16x32_bf16 v[64:67], a[164:167], v[196:199], v[64:67]\n"
    "ds_read_b64_tr_b16 a[162:163], v244 offset:6144\n"
    "v_mfma_f32_16x16x32_bf16 v[76:79], a[168:171], v[192:195], v[76:79]\n"
    "ds_read_b64_tr_b16 a[164:165], v244 offset:12288\n"
    "v_mfma_f32_16x16x32_bf16 v[76:79], a[172:175], v[196:199], v[76:79]\n"
    "ds_read_b64_tr_b16 a[166:167], v244 offset:14336\n"
    "v_mfma_f32_16x16x32_bf16 v[72:75], a[172:175], v[188:191], v[72:75]\n"
    "v_exp_f32_e32 v236, v236\n"
    "v_mfma_f32_16x16x32_bf16 v[72:75], a[168:171], v[184:187], v[72:75]\n"
    "v_exp_f32_e32 v237, v237\n"
    "v_mfma_f32_16x16x32_bf16 v[68:71], a[168:171], v[176:179], v[68:71]\n"
    "v_exp_f32_e32 v238, v238\n"
    "v_mfma_f32_16x16x32_bf16 v[68:71], a[172:175], v[180:183], v[68:71]\n"
    "v_exp_f32_e32 v239, v239\n"
    "v_mfma_f32_16x16x32_bf16 a[252:255], a[172:175], v[172:175], a[252:255]\n"
    "v_cvt_pk_bf16_f32 v234, v236, v237\n"
    "v_mfma_f32_16x16x32_bf16 a[252:255], a[168:171], v[168:171], a[252:255]\n"
    "v_exp_f32_e32 v228, v228\n"
    "v_mfma_f32_16x16x32_bf16 v[4:7], a[168:171], v[160:163], v[4:7]\n"
    "ds_read_b64_tr_b16 a[168:169], v245 offset:4096\n"
    "v_mfma_f32_16x16x32_bf16 v[4:7], a[172:175], v[164:167], v[4:7]\n"
    "s_waitcnt lgkmcnt(5)\n"
    "ds_read_b64_tr_b16 a[170:171], v245 offset:6144\n"
    "v_mfma_f32_16x16x32_bf16 v[8:11], a[176:179], v[160:163], v[8:11]\n"
    "ds_read_b64_tr_b16 a[172:173], v245 offset:12288\n"
    "v_mfma_f32_16x16x32_bf16 v[8:11], a[180:183], v[164:167], v[8:11]\n"
    "ds_read_b64_tr_b16 a[174:175], v245 offset:14336\n"
    "v_mfma_f32_16x16x32_bf16 v[32:35], a[180:183], v[172:175], v[32:35]\n"
    "v_exp_f32_e32 v229, v229\n"
    "v_mfma_f32_16x16x32_bf16 v[32:35], a[176:179], v[168:171], v[32:35]\n"
    "v_cvt_pk_bf16_f32 v235, v238, v239\n"
    "v_mfma_f32_16x16x32_bf16 v[80:83], a[176:179], v[176:179], v[80:83]\n"
    "v_exp_f32_e32 v230, v230\n"
    "v_mfma_f32_16x16x32_bf16 v[80:83], a[180:183], v[180:183], v[80:83]\n"
    "v_exp_f32_e32 v231, v231\n"
    "v_mfma_f32_16x16x32_bf16 v[84:87], a[180:183], v[188:191], v[84:87]\n"
    "v_cvt_pk_bf16_f32 v226, v228, v229\n"
    "v_mfma_f32_16x16x32_bf16 v[84:87], a[176:179], v[184:187], v[84:87]\n"
    "v_exp_f32_e32 v220, v220\n"
    "v_mfma_f32_16x16x32_bf16 v[88:91], a[176:179], v[192:195], v[88:91]\n"
    "ds_read_b64_tr_b16 a[176:177], v246 offset:4096\n"
    "v_mfma_f32_16x16x32_bf16 v[88:91], a[180:183], v[196:199], v[88:91]\n"
    "ds_read_b64_tr_b16 a[178:179], v246 offset:6144\n"
    "v_mfma_f32_16x16x32_bf16 v[100:103], a[184:187], v[192:195], v[100:103]\n"
    "ds_read_b64_tr_b16 a[180:181], v246 offset:12288\n"
    "v_mfma_f32_16x16x32_bf16 v[100:103], a[188:191], v[196:199], v[100:103]\n"
    "ds_read_b64_tr_b16 a[182:183], v246 offset:14336\n"
    "v_mfma_f32_16x16x32_bf16 v[96:99], a[188:191], v[188:191], v[96:99]\n"
    "v_exp_f32_e32 v221, v221\n"
    "v_mfma_f32_16x16x32_bf16 v[96:99], a[184:187], v[184:187], v[96:99]\n"
    "v_cvt_pk_bf16_f32 v227, v230, v231\n"
    "v_mfma_f32_16x16x32_bf16 v[92:95], a[184:187], v[176:179], v[92:95]\n"
    "v_exp_f32_e32 v222, v222\n"
    "v_mfma_f32_16x16x32_bf16 v[92:95], a[188:191], v[180:183], v[92:95]\n"
    "v_exp_f32_e32 v223, v223\n"
    "v_mfma_f32_16x16x32_bf16 v[36:39], a[188:191], v[172:175], v[36:39]\n"
    "v_cvt_pk_bf16_f32 v218, v220, v221\n"
    "v_mfma_f32_16x16x32_bf16 v[36:39], a[184:187], v[168:171], v[36:39]\n"
    "v_exp_f32_e32 v212, v212\n"
    "v_mfma_f32_16x16x32_bf16 v[12:15], a[184:187], v[160:163], v[12:15]\n"
    "s_mov_b32 m0, s49\n"
    "ds_read_b64_tr_b16 a[184:185], v247 offset:4096\n"
    "v_mfma_f32_16x16x32_bf16 v[12:15], a[188:191], v[164:167], v[12:15]\n"
    "s_waitcnt lgkmcnt(5)\n"
    "ds_read_b64_tr_b16 a[186:187], v247 offset:6144\n"
    "v_mfma_f32_16x16x32_bf16 v[16:19], a[160:163], v[160:163], v[16:19]\n"
    "ds_read_b64_tr_b16 a[188:189], v247 offset:12288\n"
    "v_mfma_f32_16x16x32_bf16 v[16:19], a[164:167], v[164:167], v[16:19]\n"
    "ds_read_b64_tr_b16 a[190:191], v247 offset:14336\n"
    "v_mfma_f32_16x16x32_bf16 v[40:43], a[164:167], v[172:175], v[40:43]\n"
    "v_exp_f32_e32 v213, v213\n"
    "v_mfma_f32_16x16x32_bf16 v[40:43], a[160:163], v[168:171], v[40:43]\n"
    "v_cvt_pk_bf16_f32 v219, v222, v223\n"
    "v_mfma_f32_16x16x32_bf16 v[104:107], a[160:163], v[176:179], v[104:107]\n"
    "v_exp_f32_e32 v214, v214\n"
    "v_mfma_f32_16x16x32_bf16 v[104:107], a[164:167], v[180:183], v[104:107]\n"
    "s_waitcnt vmcnt(8)\n"
    "v_mfma_f32_16x16x32_bf16 v[108:111], a[164:167], v[188:191], v[108:111]\n"
    "s_barrier\n"
    "v_mfma_f32_16x16x32_bf16 v[108:111], a[160:163], v[184:187], v[108:111]\n"
    "v_exp_f32_e32 v215, v215\n"
    "v_mfma_f32_16x16x32_bf16 v[112:115], a[160:163], v[192:195], v[112:115]\n"
    "ds_read_b128 a[160:163], v243 offset:36864\n"
    "v_mfma_f32_16x16x32_bf16 v[112:115], a[164:167], v[196:199], v[112:115]\n"
    "ds_read_b128 a[164:167], v240 offset:32768\n"
    "v_mfma_f32_16x16x32_bf16 v[124:127], a[168:171], v[192:195], v[124:127]\n"
    "ds_write_addtid_b32 v232 offset:16384\n"
    "v_cvt_pk_bf16_f32 v210, v212, v213\n"
    "v_mfma_f32_16x16x32_bf16 v[124:127], a[172:175], v[196:199], v[124:127]\n"
    "v_exp_f32_e32 v204, v204\n"
    "v_mfma_f32_16x16x32_bf16 v[120:123], a[172:175], v[188:191], v[120:123]\n"
    "v_exp_f32_e32 v205, v205\n"
    "v_mfma_f32_16x16x32_bf16 v[120:123], a[168:171], v[184:187], v[120:123]\n"
    "ds_write_addtid_b32 v233 offset:16640\n"
    "v_cvt_pk_bf16_f32 v211, v214, v215\n"
    "v_mfma_f32_16x16x32_bf16 v[116:119], a[168:171], v[176:179], v[116:119]\n"
    "v_exp_f32_e32 v206, v206\n"
    "v_mfma_f32_16x16x32_bf16 v[116:119], a[172:175], v[180:183], v[116:119]\n"
    "v_exp_f32_e32 v207, v207\n"
    "v_mfma_f32_16x16x32_bf16 v[44:47], a[172:175], v[172:175], v[44:47]\n"
    "ds_write_addtid_b32 v224 offset:12288\n"
    "v_cvt_pk_bf16_f32 v202, v204, v205\n"
    "v_mfma_f32_16x16x32_bf16 v[44:47], a[168:171], v[168:171], v[44:47]\n"
    "ds_write_addtid_b32 v225 offset:12544\n"
    "v_cvt_pk_bf16_f32 v203, v206, v207\n"
    "v_mfma_f32_16x16x32_bf16 v[20:23], a[168:171], v[160:163], v[20:23]\n"
    "ds_read_b128 a[168:171], v241 offset:32768\n"
    "v_mfma_f32_16x16x32_bf16 v[20:23], a[172:175], v[164:167], v[20:23]\n"
    "s_waitcnt lgkmcnt(7)\n"
    "ds_read_b128 a[172:175], v240 offset:36864\n"
    "v_mfma_f32_16x16x32_bf16 v[24:27], a[176:179], v[160:163], v[24:27]\n"
    "ds_write_addtid_b32 v216 offset:8192\n"
    "v_mfma_f32_16x16x32_bf16 v[24:27], a[180:183], v[164:167], v[24:27]\n"
    "ds_write_addtid_b32 v217 offset:8448\n"
    "v_mfma_f32_16x16x32_bf16 v[48:51], a[180:183], v[172:175], v[48:51]\n"
    "ds_write_addtid_b32 v208 offset:4096\n"
    "v_mfma_f32_16x16x32_bf16 v[48:51], a[176:179], v[168:171], v[48:51]\n"
    "ds_write_addtid_b32 v209 offset:4352\n"
    "v_mfma_f32_16x16x32_bf16 v[128:131], a[176:179], v[176:179], v[128:131]\n"
    "ds_write_addtid_b32 v200 offset:0\n"
    "v_mfma_f32_16x16x32_bf16 v[128:131], a[180:183], v[180:183], v[128:131]\n"
    "ds_write_addtid_b32 v201 offset:256\n"
    "v_mfma_f32_16x16x32_bf16 v[132:135], a[180:183], v[188:191], v[132:135]\n"
    "ds_write_addtid_b32 v234 offset:16896\n"
    "v_mfma_f32_16x16x32_bf16 v[132:135], a[176:179], v[184:187], v[132:135]\n"
    "ds_write_addtid_b32 v235 offset:17152\n"
    "v_mfma_f32_16x16x32_bf16 v[136:139], a[176:179], v[192:195], v[136:139]\n"
    "ds_read_b128 a[176:179], v241 offset:36864\n"
    "v_mfma_f32_16x16x32_bf16 v[136:139], a[180:183], v[196:199], v[136:139]\n"
    "ds_read_b128 a[180:183], v242 offset:32768\n"
    "v_mfma_f32_16x16x32_bf16 v[148:151], a[184:187], v[192:195], v[148:151]\n"
    "ds_write_addtid_b32 v226 offset:12800\n"
    "v_mfma_f32_16x16x32_bf16 v[148:151], a[188:191], v[196:199], v[148:151]\n"
    "ds_write_addtid_b32 v227 offset:13056\n"
    "v_mfma_f32_16x16x32_bf16 v[144:147], a[188:191], v[188:191], v[144:147]\n"
    "ds_write_addtid_b32 v218 offset:8704\n"
    "v_mfma_f32_16x16x32_bf16 v[144:147], a[184:187], v[184:187], v[144:147]\n"
    "ds_write_addtid_b32 v219 offset:8960\n"
    "v_mfma_f32_16x16x32_bf16 v[140:143], a[184:187], v[176:179], v[140:143]\n"
    "ds_write_addtid_b32 v210 offset:4608\n"
    "v_mfma_f32_16x16x32_bf16 v[140:143], a[188:191], v[180:183], v[140:143]\n"
    "ds_write_addtid_b32 v211 offset:4864\n"
    "v_mfma_f32_16x16x32_bf16 v[52:55], a[188:191], v[172:175], v[52:55]\n"
    "ds_write_addtid_b32 v202 offset:512\n"
    "v_mfma_f32_16x16x32_bf16 v[52:55], a[184:187], v[168:171], v[52:55]\n"
    "ds_write_addtid_b32 v203 offset:768\n"
    "v_mfma_f32_16x16x32_bf16 v[28:31], a[184:187], v[160:163], v[28:31]\n"
    "ds_read_b128 a[184:187], v243 offset:32768\n"
    "v_mfma_f32_16x16x32_bf16 v[28:31], a[188:191], v[164:167], v[28:31]\n"
    "s_waitcnt lgkmcnt(15)\n"
    "ds_read_b128 a[188:191], v242 offset:36864\n"
    "v_mfma_f32_16x16x32_bf16 v[160:163], a[160:163], a[28:31], 0\n"
    "v_mfma_f32_16x16x32_bf16 v[160:163], a[164:167], a[0:3], v[160:163]\n"
    "v_mfma_f32_16x16x32_bf16 v[168:171], a[164:167], a[32:35], 0\n"
    "v_mfma_f32_16x16x32_bf16 v[168:171], a[160:163], a[60:63], v[168:171]\n"
    "v_mfma_f32_16x16x32_bf16 v[176:179], a[160:163], a[92:95], 0\n"
    "v_mfma_f32_16x16x32_bf16 v[176:179], a[164:167], a[64:67], v[176:179]\n"
    "s_waitcnt lgkmcnt(0)\n"
    "v_mfma_f32_16x16x32_bf16 v[184:187], a[164:167], a[96:99], 0\n"
    "s_barrier\n"
    "v_mfma_f32_16x16x32_bf16 v[184:187], a[160:163], a[124:127], v[184:187]\n"
    "s_add_u32 m0, s43, 0\n"
    "ds_read2st64_b32 v[204:205], v250 offset0:0 offset1:1\n"
    "v_mfma_f32_16x16x32_bf16 v[192:195], a[160:163], a[156:159], 0\n"
    "ds_read_b128 a[160:163], v243 offset:38912\n"
    "v_mfma_f32_16x16x32_bf16 v[192:195], a[164:167], a[128:131], v[192:195]\n"
    "ds_read_b128 a[164:167], v240 offset:34816\n"
    "v_mfma_f32_16x16x32_bf16 v[192:195], a[168:171], a[132:135], v[192:195]\n"
    "buffer_load_dwordx4 v248, s[8:11], s40 offen offset:0 sc1 lds\n"
    "v_mfma_f32_16x16x32_bf16 v[192:195], a[172:175], a[136:139], v[192:195]\n"
    "v_mfma_f32_16x16x32_bf16 v[184:187], a[172:175], a[104:107], v[184:187]\n"
    "v_mfma_f32_16x16x32_bf16 v[184:187], a[168:171], a[100:103], v[184:187]\n"
    "ds_read2st64_b32 v[206:207], v250 offset0:2 offset1:3\n"
    "v_mfma_f32_16x16x32_bf16 v[176:179], a[168:171], a[68:71], v[176:179]\n"
    "v_mfma_f32_16x16x32_bf16 v[176:179], a[172:175], a[72:75], v[176:179]\n"
    "buffer_load_dwordx4 v248, s[8:11], s40 offen offset:1024 sc1 lds\n"
    "v_mfma_f32_16x16x32_bf16 v[168:171], a[172:175], a[40:43], v[168:171]\n"
    "v_mfma_f32_16x16x32_bf16 v[168:171], a[168:171], a[36:39], v[168:171]\n"
    "ds_read2st64_b32 v[212:213], v250 offset0:16 offset1:17\n"
    "v_mfma_f32_16x16x32_bf16 v[160:163], a[168:171], a[4:7], v[160:163]\n"
    "ds_read_b128 a[168:171], v241 offset:34816\n"
    "v_mfma_f32_16x16x32_bf16 v[160:163], a[172:175], a[8:11], v[160:163]\n"
    "ds_read_b128 a[172:175], v240 offset:38912\n"
    "v_mfma_f32_16x16x32_bf16 v[160:163], a[176:179], a[12:15], v[160:163]\n"
    "buffer_load_dwordx4 v248, s[8:11], s40 offen offset:2048 sc1 lds\n"
    "v_mfma_f32_16x16x32_bf16 v[160:163], a[180:183], a[16:19], v[160:163]\n"
    "ds_read2st64_b32 v[214:215], v250 offset0:18 offset1:19\n"
    "v_mfma_f32_16x16x32_bf16 v[168:171], a[180:183], a[48:51], v[168:171]\n"
    "v_mfma_f32_16x16x32_bf16 v[168:171], a[176:179], a[44:47], v[168:171]\n"
    "v_mfma_f32_16x16x32_bf16 v[176:179], a[176:179], a[76:79], v[176:179]\n"
    "ds_read2st64_b32 v[220:221], v250 offset0:32 offset1:33\n"
    "v_mfma_f32_16x16x32_bf16 v[176:179], a[180:183], a[80:83], v[176:179]\n"
    "buffer_load_dwordx4 v248, s[8:11], s40 offen offset:3072 sc1 lds\n"
    "v_mfma_f32_16x16x32_bf16 v[184:187], a[180:183], a[112:115], v[184:187]\n"
    "s_add_u32 m0, s43, 4096\n"
    "v_mfma_f32_16x16x32_bf16 v[184:187], a[176:179], a[108:111], v[184:187]\n"
    "s_add_u32 s40, s40, 0x8000\n"
    "ds_read2st64_b32 v[222:223], v250 offset0:34 offset1:35\n"
    "v_mfma_f32_16x16x32_bf16 v[192:195], a[176:179], a[140:143], v[192:195]\n"
    "ds_read_b128 a[176:179], v241 offset:38912\n"
    "v_mfma_f32_16x16x32_bf16 v[192:195], a[180:183], a[144:147], v[192:195]\n"
    "ds_read_b128 a[180:183], v242 offset:34816\n"
    "v_mfma_f32_16x16x32_bf16 v[192:195], a[184:187], a[148:151], v[192:195]\n"
    "buffer_load_dwordx4 v248, s[8:11], s41 offen offset:0 sc1 lds\n"
    "v_mfma_f32_16x16x32_bf16 v[192:195], a[188:191], a[152:155], v[192:195]\n"
    "v_mfma_f32_16x16x32_bf16 v[184:187], a[188:191], a[120:123], v[184:187]\n"
    "ds_read2st64_b32 v[228:229], v250 offset0:48 offset1:49\n"
    "v_mfma_f32_16x16x32_bf16 v[184:187], a[184:187], a[116:119], v[184:187]\n"
    "v_mfma_f32_16x16x32_bf16 v[176:179], a[184:187], a[84:87], v[176:179]\n"
    "v_exp_f32_e32 v192, v192\n"
    "v_mfma_f32_16x16x32_bf16 v[176:179], a[188:191], a[88:91], v[176:179]\n"
    "buffer_load_dwordx4 v248, s[8:11], s41 offen offset:1024 sc1 lds\n"
    "v_mfma_f32_16x16x32_bf16 v[168:171], a[188:191], a[56:59], v[168:171]\n"
    "ds_read2st64_b32 v[230:231], v250 offset0:50 offset1:51\n"
    "v_mfma_f32_16x16x32_bf16 v[168:171], a[184:187], a[52:55], v[168:171]\n"
    "v_exp_f32_e32 v193, v193\n"
    "v_mfma_f32_16x16x32_bf16 v[160:163], a[184:187], a[20:23], v[160:163]\n"
    "ds_read_b128 a[184:187], v243 offset:34816\n"
    "v_mfma_f32_16x16x32_bf16 v[160:163], a[188:191], a[24:27], v[160:163]\n"
    "s_waitcnt lgkmcnt(8)\n"
    "ds_read_b128 a[188:191], v242 offset:38912\n"
    "v_mfma_f32_16x16x32_bf16 v[164:167], a[160:163], a[28:31], 0\n"
    "buffer_load_dwordx4 v248, s[8:11], s41 offen offset:2048 sc1 lds\n"
    "v_mfma_f32_16x16x32_bf16 v[164:167], a[164:167], a[0:3], v[164:167]\n"
    "ds_read2st64_b32 v[236:237], v250 offset0:64 offset1:65\n"
    "v_mfma_f32_16x16x32_bf16 v[172:175], a[164:167], a[32:35], 0\n"
    "v_exp_f32_e32 v194, v194\n"
    "v_mfma_f32_16x16x32_bf16 v[172:175], a[160:163], a[60:63], v[172:175]\n"
    "ds_read2st64_b32 v[238:239], v250 offset0:66 offset1:67\n"
    "v_mfma_f32_16x16x32_bf16 v[180:183], a[160:163], a[92:95], 0\n"
    "v_exp_f32_e32 v195, v195\n"
    "v_mfma_f32_16x16x32_bf16 v[180:183], a[164:167], a[64:67], v[180:183]\n"
    "buffer_load_dwordx4 v248, s[8:11], s41 offen offset:3072 sc1 lds\n"
    "v_mfma_f32_16x16x32_bf16 v[188:191], a[164:167], a[96:99], 0\n"
    "s_add_u32 s41, s41, 0x8000\n"
    "v_cvt_pk_bf16_f32 v192, v192, v193\n"
    "v_mfma_f32_16x16x32_bf16 v[188:191], a[160:163], a[124:127], v[188:191]\n"
    "v_exp_f32_e32 v184, v184\n"
    "v_mfma_f32_16x16x32_bf16 v[196:199], a[160:163], a[156:159], 0\n"
    "ds_read_b64_tr_b16 a[160:161], v244 offset:16384\n"
    "v_mfma_f32_16x16x32_bf16 v[196:199], a[164:167], a[128:131], v[196:199]\n"
    "ds_read_b64_tr_b16 a[162:163], v244 offset:18432\n"
    "v_mfma_f32_16x16x32_bf16 v[196:199], a[168:171], a[132:135], v[196:199]\n"
    "ds_read_b64_tr_b16 a[164:165], v244 offset:24576\n"
    "v_mfma_f32_16x16x32_bf16 v[196:199], a[172:175], a[136:139], v[196:199]\n"
    "ds_read_b64_tr_b16 a[166:167], v244 offset:26624\n"
    "v_mfma_f32_16x16x32_bf16 v[188:191], a[172:175], a[104:107], v[188:191]\n"
    "v_exp_f32_e32 v185, v185\n"
    "v_mfma_f32_16x16x32_bf16 v[188:191], a[168:171], a[100:103], v[188:191]\n"
    "v_cvt_pk_bf16_f32 v193, v194, v195\n"
    "v_mfma_f32_16x16x32_bf16 v[180:183], a[168:171], a[68:71], v[180:183]\n"
    "v_exp_f32_e32 v186, v186\n"
    "v_mfma_f32_16x16x32_bf16 v[180:183], a[172:175], a[72:75], v[180:183]\n"
    "v_exp_f32_e32 v187, v187\n"
    "v_mfma_f32_16x16x32_bf16 v[172:175], a[172:175], a[40:43], v[172:175]\n"
    "v_cvt_pk_bf16_f32 v184, v184, v185\n"
    "v_mfma_f32_16x16x32_bf16 v[172:175], a[168:171], a[36:39], v[172:175]\n"
    "v_exp_f32_e32 v176, v176\n"
    "v_mfma_f32_16x16x32_bf16 v[164:167], a[168:171], a[4:7], v[164:167]\n"
    "ds_read_b64_tr_b16 a[168:169], v245 offset:16384\n"
    "v_mfma_f32_16x16x32_bf16 v[164:167], a[172:175], a[8:11], v[164:167]\n"
    "s_waitcnt lgkmcnt(7)\n"
    "ds_read_b64_tr_b16 a[170:171], v245 offset:18432\n"
    "v_mfma_f32_16x16x32_bf16 v[164:167], a[176:179], a[12:15], v[164:167]\n"
    "ds_read_b64_tr_b16 a[172:173], v245 offset:24576\n"
    "v_mfma_f32_16x16x32_bf16 v[164:167], a[180:183], a[16:19], v[164:167]\n"
    "ds_read_b64_tr_b16 a[174:175], v245 offset:26624\n"
    "v_mfma_f32_16x16x32_bf16 v[172:175], a[180:183], a[48:51], v[172:175]\n"
    "v_exp_f32_e32 v177, v177\n"
    "v_mfma_f32_16x16x32_bf16 v[172:175], a[176:179], a[44:47], v[172:175]\n"
    "v_cvt_pk_bf16_f32 v185, v186, v187\n"
    "v_mfma_f32_16x16x32_bf16 v[180:183], a[176:179], a[76:79], v[180:183]\n"
    "v_exp_f32_e32 v178, v178\n"
    "v_mfma_f32_16x16x32_bf16 v[180:183], a[180:183], a[80:83], v[180:183]\n"
    "v_exp_f32_e32 v179, v179\n"
    "v_mfma_f32_16x16x32_bf16 v[188:191], a[180:183], a[112:115], v[188:191]\n"
    "v_cvt_pk_bf16_f32 v176, v176, v177\n"
    "v_mfma_f32_16x16x32_bf16 v[188:191], a[176:179], a[108:111], v[188:191]\n"
    "v_exp_f32_e32 v168, v168\n"
    "v_mfma_f32_16x16x32_bf16 v[196:199], a[176:179], a[140:143], v[196:199]\n"
    "ds_read_b64_tr_b16 a[176:177], v246 offset:16384\n"
    "v_mfma_f32_16x16x32_bf16 v[196:199], a[180:183], a[144:147], v[196:199]\n"
    "ds_read_b64_tr_b16 a[178:179], v246 offset:18432\n"
    "v_mfma_f32_16x16x32_bf16 v[196:199], a[184:187], a[148:151], v[196:199]\n"
    "ds_read_b64_tr_b16 a[180:181], v246 offset:24576\n"
    "v_mfma_f32_16x16x32_bf16 v[196:199], a[188:191], a[152:155], v[196:199]\n"
    "ds_read_b64_tr_b16 a[182:183], v246 offset:26624\n"
    "v_mfma_f32_16x16x32_bf16 v[188:191], a[188:191], a[120:123], v[188:191]\n"
    "v_exp_f32_e32 v169, v169\n"
    "v_mfma_f32_16x16x32_bf16 v[188:191], a[184:187], a[116:119], v[188:191]\n"
    "v_cvt_pk_bf16_f32 v177, v178, v179\n"
    "v_mfma_f32_16x16x32_bf16 v[180:183], a[184:187], a[84:87], v[180:183]\n"
    "v_exp_f32_e32 v170, v170\n"
    "v_mfma_f32_16x16x32_bf16 v[180:183], a[188:191], a[88:91], v[180:183]\n"
    "v_exp_f32_e32 v171, v171\n"
    "v_mfma_f32_16x16x32_bf16 v[172:175], a[188:191], a[56:59], v[172:175]\n"
    "v_cvt_pk_bf16_f32 v168, v168, v169\n"
    "v_mfma_f32_16x16x32_bf16 v[172:175], a[184:187], a[52:55], v[172:175]\n"
    "v_exp_f32_e32 v160, v160\n"
    "v_mfma_f32_16x16x32_bf16 v[164:167], a[184:187], a[20:23], v[164:167]\n"
    "ds_read_b64_tr_b16 a[184:185], v247 offset:16384\n"
    "v_mfma_f32_16x16x32_bf16 v[164:167], a[188:191], a[24:27], v[164:167]\n"
    "s_waitcnt lgkmcnt(5)\n"
    "ds_read_b64_tr_b16 a[186:187], v247 offset:18432\n"
    "v_mfma_f32_16x16x32_bf16 v[0:3], a[160:163], v[200:203], v[0:3]\n"
    "ds_read_b64_tr_b16 a[188:189], v247 offset:24576\n"
    "v_mfma_f32_16x16x32_bf16 v[0:3], a[164:167], v[204:207], v[0:3]\n"
    "ds_read_b64_tr_b16 a[190:191], v247 offset:26624\n"
    "v_mfma_f32_16x16x32_bf16 a[248:251], a[164:167], v[212:215], a[248:251]\n"
    "v_exp_f32_e32 v161, v161\n"
    "v_mfma_f32_16x16x32_bf16 a[248:251], a[160:163], v[208:211], a[248:251]\n"
    "v_cvt_pk_bf16_f32 v169, v170, v171\n"
    "v_mfma_f32_16x16x32_bf16 v[56:59], a[160:163], v[216:219], v[56:59]\n"
    "v_exp_f32_e32 v162, v162\n"
    "v_mfma_f32_16x16x32_bf16 v[56:59], a[164:167], v[220:223], v[56:59]\n"
    "v_exp_f32_e32 v163, v163\n"
    "v_mfma_f32_16x16x32_bf16 v[60:63], a[164:167], v[228:231], v[60:63]\n"
    "v_cvt_pk_bf16_f32 v160, v160, v161\n"
    "v_mfma_f32_16x16x32_bf16 v[60:63], a[160:163], v[224:227], v[60:63]\n"
    "v_cvt_pk_bf16_f32 v161, v162, v163\n"
    "v_mfma_f32_16x16x32_bf16 v[64:67], a[160:163], v[232:235], v[64:67]\n"
    "ds_read_b64_tr_b16 a[160:161], v244 offset:20480\n"
    "v_mfma_f32_16x16x32_bf16 v[64:67], a[164:167], v[236:239], v[64:67]\n"
    "ds_read_b64_tr_b16 a[162:163], v244 offset:22528\n"
    "v_mfma_f32_16x16x32_bf16 v[76:79], a[168:171], v[232:235], v[76:79]\n"
    "ds_read_b64_tr_b16 a[164:165], v244 offset:28672\n"
    "v_mfma_f32_16x16x32_bf16 v[76:79], a[172:175], v[236:239], v[76:79]\n"
    "ds_read_b64_tr_b16 a[166:167], v244 offset:30720\n"
    "v_mfma_f32_16x16x32_bf16 v[72:75], a[172:175], v[228:231], v[72:75]\n"
    "v_exp_f32_e32 v196, v196\n"
    "v_mfma_f32_16x16x32_bf16 v[72:75], a[168:171], v[224:227], v[72:75]\n"
    "v_exp_f32_e32 v197, v197\n"
    "v_mfma_f32_16x16x32_bf16 v[68:71], a[168:171], v[216:219], v[68:71]\n"
    "v_exp_f32_e32 v198, v198\n"
    "v_mfma_f32_16x16x32_bf16 v[68:71], a[172:175], v[220:223], v[68:71]\n"
    "v_exp_f32_e32 v199, v199\n"
    "v_mfma_f32_16x16x32_bf16 a[252:255], a[172:175], v[212:215], a[252:255]\n"
    "v_cvt_pk_bf16_f32 v194, v196, v197\n"
    "v_mfma_f32_16x16x32_bf16 a[252:255], a[168:171], v[208:211], a[252:255]\n"
    "v_exp_f32_e32 v188, v188\n"
    "v_mfma_f32_16x16x32_bf16 v[4:7], a[168:171], v[200:203], v[4:7]\n"
    "ds_read_b64_tr_b16 a[168:169], v245 offset:20480\n"
    "v_mfma_f32_16x16x32_bf16 v[4:7], a[172:175], v[204:207], v[4:7]\n"
    "s_waitcnt lgkmcnt(5)\n"
    "ds_read_b64_tr_b16 a[170:171], v245 offset:22528\n"
    "v_mfma_f32_16x16x32_bf16 v[8:11], a[176:179], v[200:203], v[8:11]\n"
    "ds_read_b64_tr_b16 a[172:173], v245 offset:28672\n"
    "v_mfma_f32_16x16x32_bf16 v[8:11], a[180:183], v[204:207], v[8:11]\n"
    "ds_read_b64_tr_b16 a[174:175], v245 offset:30720\n"
    "v_mfma_f32_16x16x32_bf16 v[32:35], a[180:183], v[212:215], v[32:35]\n"
    "v_exp_f32_e32 v189, v189\n"
    "v_mfma_f32_16x16x32_bf16 v[32:35], a[176:179], v[208:211], v[32:35]\n"
    "v_cvt_pk_bf16_f32 v195, v198, v199\n"
    "v_mfma_f32_16x16x32_bf16 v[80:83], a[176:179], v[216:219], v[80:83]\n"
    "v_exp_f32_e32 v190, v190\n"
    "v_mfma_f32_16x16x32_bf16 v[80:83], a[180:183], v[220:223], v[80:83]\n"
    "v_exp_f32_e32 v191, v191\n"
    "v_mfma_f32_16x16x32_bf16 v[84:87], a[180:183], v[228:231], v[84:87]\n"
    "v_cvt_pk_bf16_f32 v186, v188, v189\n"
    "v_mfma_f32_16x16x32_bf16 v[84:87], a[176:179], v[224:227], v[84:87]\n"
    "v_exp_f32_e32 v180, v180\n"
    "v_mfma_f32_16x16x32_bf16 v[88:91], a[176:179], v[232:235], v[88:91]\n"
    "ds_read_b64_tr_b16 a[176:177], v246 offset:20480\n"
    "v_mfma_f32_16x16x32_bf16 v[88:91], a[180:183], v[236:239], v[88:91]\n"
    "ds_read_b64_tr_b16 a[178:179], v246 offset:22528\n"
    "v_mfma_f32_16x16x32_bf16 v[100:103], a[184:187], v[232:235], v[100:103]\n"
    "ds_read_b64_tr_b16 a[180:181], v246 offset:28672\n"
    "v_mfma_f32_16x16x32_bf16 v[100:103], a[188:191], v[236:239], v[100:103]\n"
    "ds_read_b64_tr_b16 a[182:183], v246 offset:30720\n"
    "v_mfma_f32_16x16x32_bf16 v[96:99], a[188:191], v[228:231], v[96:99]\n"
    "v_exp_f32_e32 v181, v181\n"
    "v_mfma_f32_16x16x32_bf16 v[96:99], a[184:187], v[224:227], v[96:99]\n"
    "v_cvt_pk_bf16_f32 v187, v190, v191\n"
    "v_mfma_f32_16x16x32_bf16 v[92:95], a[184:187], v[216:219], v[92:95]\n"
    "v_exp_f32_e32 v182, v182\n"
    "v_mfma_f32_16x16x32_bf16 v[92:95], a[188:191], v[220:223], v[92:95]\n"
    "v_exp_f32_e32 v183, v183\n"
    "v_mfma_f32_16x16x32_bf16 v[36:39], a[188:191], v[212:215], v[36:39]\n"
    "v_cvt_pk_bf16_f32 v178, v180, v181\n"
    "v_mfma_f32_16x16x32_bf16 v[36:39], a[184:187], v[208:211], v[36:39]\n"
    "v_exp_f32_e32 v172, v172\n"
    "v_mfma_f32_16x16x32_bf16 v[12:15], a[184:187], v[200:203], v[12:15]\n"
    "s_mov_b32 m0, s49\n"
    "ds_read_b64_tr_b16 a[184:185], v247 offset:20480\n"
    "v_mfma_f32_16x16x32_bf16 v[12:15], a[188:191], v[204:207], v[12:15]\n"
    "s_waitcnt lgkmcnt(5)\n"
    "ds_read_b64_tr_b16 a[186:187], v247 offset:22528\n"
    "v_mfma_f32_16x16x32_bf16 v[16:19], a[160:163], v[200:203], v[16:19]\n"
    "ds_read_b64_tr_b16 a[188:189], v247 offset:28672\n"
    "v_mfma_f32_16x16x32_bf16 v[16:19], a[164:167], v[204:207], v[16:19]\n"
    "ds_read_b64_tr_b16 a[190:191], v247 offset:30720\n"
    "v_mfma_f32_16x16x32_bf16 v[40:43], a[164:167], v[212:215], v[40:43]\n"
    "v_exp_f32_e32 v173, v173\n"
    "v_mfma_f32_16x16x32_bf16 v[40:43], a[160:163], v[208:211], v[40:43]\n"
    "v_cvt_pk_bf16_f32 v179, v182, v183\n"
    "v_mfma_f32_16x16x32_bf16 v[104:107], a[160:163], v[216:219], v[104:107]\n"
    "v_exp_f32_e32 v174, v174\n"
    "v_mfma_f32_16x16x32_bf16 v[104:107], a[164:167], v[220:223], v[104:107]\n"
    "s_waitcnt vmcnt(8)\n"
    "v_mfma_f32_16x16x32_bf16 v[108:111], a[164:167], v[228:231], v[108:111]\n"
    "s_barrier\n"
    "v_mfma_f32_16x16x32_bf16 v[108:111], a[160:163], v[224:227], v[108:111]\n"
    "v_exp_f32_e32 v175, v175\n"
    "v_mfma_f32_16x16x32_bf16 v[112:115], a[160:163], v[232:235], v[112:115]\n"
    "ds_read_b128 a[160:163], v243 offset:53248\n"
    "v_mfma_f32_16x16x32_bf16 v[112:115], a[164:167], v[236:239], v[112:115]\n"
    "ds_read_b128 a[164:167], v240 offset:49152\n"
    "v_mfma_f32_16x16x32_bf16 v[124:127], a[168:171], v[232:235], v[124:127]\n"
    "ds_write_addtid_b32 v192 offset:16384\n"
    "v_cvt_pk_bf16_f32 v170, v172, v173\n"
    "v_mfma_f32_16x16x32_bf16 v[124:127], a[172:175], v[236:239], v[124:127]\n"
    "v_exp_f32_e32 v164, v164\n"
    "v_mfma_f32_16x16x32_bf16 v[120:123], a[172:175], v[228:231], v[120:123]\n"
    "v_exp_f32_e32 v165, v165\n"
    "v_mfma_f32_16x16x32_bf16 v[120:123], a[168:171], v[224:227], v[120:123]\n"
    "ds_write_addtid_b32 v193 offset:16640\n"
    "v_cvt_pk_bf16_f32 v171, v174, v175\n"
    "v_mfma_f32_16x16x32_bf16 v[116:119], a[168:171], v[216:219], v[116:119]\n"
    "v_exp_f32_e32 v166, v166\n"
    "v_mfma_f32_16x16x32_bf16 v[116:119], a[172:175], v[220:223], v[116:119]\n"
    "v_exp_f32_e32 v167, v167\n"
    "v_mfma_f32_16x16x32_bf16 v[44:47], a[172:175], v[212:215], v[44:47]\n"
    "ds_write_addtid_b32 v184 offset:12288\n"
    "v_cvt_pk_bf16_f32 v162, v164, v165\n"
    "v_mfma_f32_16x16x32_bf16 v[44:47], a[168:171], v[208:211], v[44:47]\n"
    "ds_write_addtid_b32 v185 offset:12544\n"
    "v_cvt_pk_bf16_f32 v163, v166, v167\n"
    "v_mfma_f32_16x16x32_bf16 v[20:23], a[168:171], v[200:203], v[20:23]\n"
    "ds_read_b128 a[168:171], v241 offset:49152\n"
    "v_mfma_f32_16x16x32_bf16 v[20:23], a[172:175], v[204:207], v[20:23]\n"
    "s_waitcnt lgkmcnt(7)\n"
    "ds_read_b128 a[172:175], v240 offset:53248\n"
    "v_mfma_f32_16x16x32_bf16 v[24:27], a[176:179], v[200:203], v[24:27]\n"
    "ds_write_addtid_b32 v176 offset:8192\n"
    "v_mfma_f32_16x16x32_bf16 v[24:27], a[180:183], v[204:207], v[24:27]\n"
    "ds_write_addtid_b32 v177 offset:8448\n"
    "v_mfma_f32_16x16x32_bf16 v[48:51], a[180:183], v[212:215], v[48:51]\n"
    "ds_write_addtid_b32 v168 offset:4096\n"
    "v_mfma_f32_16x16x32_bf16 v[48:51], a[176:179], v[208:211], v[48:51]\n"
    "ds_write_addtid_b32 v169 offset:4352\n"
    "v_mfma_f32_16x16x32_bf16 v[128:131], a[176:179], v[216:219], v[128:131]\n"
    "ds_write_addtid_b32 v160 offset:0\n"
    "v_mfma_f32_16x16x32_bf16 v[128:131], a[180:183], v[220:223], v[128:131]\n"
    "ds_write_addtid_b32 v161 offset:256\n"
    "v_mfma_f32_16x16x32_bf16 v[132:135], a[180:183], v[228:231], v[132:135]\n"
    "ds_write_addtid_b32 v194 offset:16896\n"
    "v_mfma_f32_16x16x32_bf16 v[132:135], a[176:179], v[224:227], v[132:135]\n"
    "ds_write_addtid_b32 v195 offset:17152\n"
    "v_mfma_f32_16x16x32_bf16 v[136:139], a[176:179], v[232:235], v[136:139]\n"
    "ds_read_b128 a[176:179], v241 offset:53248\n"
    "v_mfma_f32_16x16x32_bf16 v[136:139], a[180:183], v[236:239], v[136:139]\n"
    "ds_read_b128 a[180:183], v242 offset:49152\n"
    "v_mfma_f32_16x16x32_bf16 v[148:151], a[184:187], v[232:235], v[148:151]\n"
    "ds_write_addtid_b32 v186 offset:12800\n"
    "v_mfma_f32_16x16x32_bf16 v[148:151], a[188:191], v[236:239], v[148:151]\n"
    "ds_write_addtid_b32 v187 offset:13056\n"
    "v_mfma_f32_16x16x32_bf16 v[144:147], a[188:191], v[228:231], v[144:147]\n"
    "ds_write_addtid_b32 v178 offset:8704\n"
    "v_mfma_f32_16x16x32_bf16 v[144:147], a[184:187], v[224:227], v[144:147]\n"
    "ds_write_addtid_b32 v179 offset:8960\n"
    "v_mfma_f32_16x16x32_bf16 v[140:143], a[184:187], v[216:219], v[140:143]\n"
    "ds_write_addtid_b32 v170 offset:4608\n"
    "v_mfma_f32_16x16x32_bf16 v[140:143], a[188:191], v[220:223], v[140:143]\n"
    "ds_write_addtid_b32 v171 offset:4864\n"
    "v_mfma_f32_16x16x32_bf16 v[52:55], a[188:191], v[212:215], v[52:55]\n"
    "ds_write_addtid_b32 v162 offset:512\n"
    "v_mfma_f32_16x16x32_bf16 v[52:55], a[184:187], v[208:211], v[52:55]\n"
    "ds_write_addtid_b32 v163 offset:768\n"
    "v_mfma_f32_16x16x32_bf16 v[28:31], a[184:187], v[200:203], v[28:31]\n"
    "ds_read_b128 a[184:187], v243 offset:49152\n"
    "v_mfma_f32_16x16x32_bf16 v[28:31], a[188:191], v[204:207], v[28:31]\n"
    "s_waitcnt lgkmcnt(15)\n"
    "ds_read_b128 a[188:191], v242 offset:53248\n"
    ".Lat_p2e:\n"
    "v_mfma_f32_16x16x32_bf16 v[200:203], a[160:163], a[28:31], 0\n"
    "v_mfma_f32_16x16x32_bf16 v[200:203], a[164:167], a[0:3], v[200:203]\n"
    "v_mfma_f32_16x16x32_bf16 v[208:211], a[164:167], a[32:35], 0\n"
    "v_mfma_f32_16x16x32_bf16 v[208:211], a[160:163], a[60:63], v[208:211]\n"
    "v_mfma_f32_16x16x32_bf16 v[216:219], a[160:163], a[92:95], 0\n"
    "v_mfma_f32_16x16x32_bf16 v[216:219], a[164:167], a[64:67], v[216:219]\n"
    "s_waitcnt lgkmcnt(0)\n"
    "v_mfma_f32_16x16x32_bf16 v[224:227], a[164:167], a[96:99], 0\n"
    "s_barrier\n"
    "v_mfma_f32_16x16x32_bf16 v[224:227], a[160:163], a[124:127], v[224:227]\n"
    "s_add_u32 m0, s43, 16384\n"
    "ds_read2st64_b32 v[164:165], v250 offset0:0 offset1:1\n"
    "v_mfma_f32_16x16x32_bf16 v[232:235], a[160:163], a[156:159], 0\n"
    "ds_read_b128 a[160:163], v243 offset:55296\n"
    "v_mfma_f32_16x16x32_bf16 v[232:235], a[164:167], a[128:131], v[232:235]\n"
    "ds_read_b128 a[164:167], v240 offset:51200\n"
    "v_mfma_f32_16x16x32_bf16 v[232:235], a[168:171], a[132:135], v[232:235]\n"
    "buffer_load_dwordx4 v248, s[8:11], s40 offen offset:0 sc1 lds\n"
    "v_mfma_f32_16x16x32_bf16 v[232:235], a[172:175], a[136:139], v[232:235]\n"
    "v_mfma_f32_16x16x32_bf16 v[224:227], a[172:175], a[104:107], v[224:227]\n"
    "v_mfma_f32_16x16x32_bf16 v[224:227], a[168:171], a[100:103], v[224:227]\n"
    "ds_read2st64_b32 v[166:167], v250 offset0:2 offset1:3\n"
    "v_mfma_f32_16x16x32_bf16 v[216:219], a[168:171], a[68:71], v[216:219]\n"
    "v_mfma_f32_16x16x32_bf16 v[216:219], a[172:175], a[72:75], v[216:219]\n"
    "buffer_load_dwordx4 v248, s[8:11], s40 offen offset:1024 sc1 lds\n"
    "v_mfma_f32_16x16x32_bf16 v[208:211], a[172:175], a[40:43], v[208:211]\n"
    "v_mfma_f32_16x16x32_bf16 v[208:211], a[168:171], a[36:39], v[208:211]\n"
    "ds_read2st64_b32 v[172:173], v250 offset0:16 offset1:17\n"
    "v_mfma_f32_16x16x32_bf16 v[200:203], a[168:171], a[4:7], v[200:203]\n"
    "ds_read_b128 a[168:171], v241 offset:51200\n"
    "v_mfma_f32_16x16x32_bf16 v[200:203], a[172:175], a[8:11], v[200:203]\n"
    "ds_read_b128 a[172:175], v240 offset:55296\n"
    "v_mfma_f32_16x16x32_bf16 v[200:203], a[176:179], a[12:15], v[200:203]\n"
    "buffer_load_dwordx4 v248, s[8:11], s40 offen offset:2048 sc1 lds\n"
    "v_mfma_f32_16x16x32_bf16 v[200:203], a[180:183], a[16:19], v[200:203]\n"
    "ds_read2st64_b32 v[174:175], v250 offset0:18 offset1:19\n"
    "v_mfma_f32_16x16x32_bf16 v[208:211], a[180:183], a[48:51], v[208:211]\n"
    "v_mfma_f32_16x16x32_bf16 v[208:211], a[176:179], a[44:47], v[208:211]\n"
    "v_mfma_f32_16x16x32_bf16 v[216:219], a[176:179], a[76:79], v[216:219]\n"
    "ds_read2st64_b32 v[180:181], v250 offset0:32 offset1:33\n"
    "v_mfma_f32_16x16x32_bf16 v[216:219], a[180:183], a[80:83], v[216:219]\n"
    "buffer_load_dwordx4 v248, s[8:11], s40 offen offset:3072 sc1 lds\n"
    "v_mfma_f32_16x16x32_bf16 v[224:227], a[180:183], a[112:115], v[224:227]\n"
    "s_add_u32 m0, s43, 20480\n"
    "v_mfma_f32_16x16x32_bf16 v[224:227], a[176:179], a[108:111], v[224:227]\n"
    "s_add_u32 s40, s40, 0x8000\n"
    "ds_read2st64_b32 v[182:183], v250 offset0:34 offset1:35\n"
    "v_mfma_f32_16x16x32_bf16 v[232:235], a[176:179], a[140:143], v[232:235]\n"
    "ds_read_b128 a[176:179], v241 offset:55296\n"
    "v_mfma_f32_16x16x32_bf16 v[232:235], a[180:183], a[144:147], v[232:235]\n"
    "ds_read_b128 a[180:183], v242 offset:51200\n"
    "v_mfma_f32_16x16x32_bf16 v[232:235], a[184:187], a[148:151], v[232:235]\n"
    "buffer_load_dwordx4 v248, s[8:11], s41 offen offset:0 sc1 lds\n"
    "v_mfma_f32_16x16x32_bf16 v[232:235], a[188:191], a[152:155], v[232:235]\n"
    "v_mfma_f32_16x16x32_bf16 v[224:227], a[188:191], a[120:123], v[224:227]\n"
    "ds_read2st64_b32 v[188:189], v250 offset0:48 offset1:49\n"
    "v_mfma_f32_16x16x32_bf16 v[224:227], a[184:187], a[116:119], v[224:227]\n"
    "v_mfma_f32_16x16x32_bf16 v[216:219], a[184:187], a[84:87], v[216:219]\n"
    "v_exp_f32_e32 v232, v232\n"
    "v_mfma_f32_16x16x32_bf16 v[216:219], a[188:191], a[88:91], v[216:219]\n"
    "buffer_load_dwordx4 v248, s[8:11], s41 offen offset:1024 sc1 lds\n"
    "v_mfma_f32_16x16x32_bf16 v[208:211], a[188:191], a[56:59], v[208:211]\n"
    "ds_read2st64_b32 v[190:191], v250 offset0:50 offset1:51\n"
    "v_mfma_f32_16x16x32_bf16 v[208:211], a[184:187], a[52:55], v[208:211]\n"
    "v_exp_f32_e32 v233, v233\n"
    "v_mfma_f32_16x16x32_bf16 v[200:203], a[184:187], a[20:23], v[200:203]\n"
    "ds_read_b128 a[184:187], v243 offset:51200\n"
    "v_mfma_f32_16x16x32_bf16 v[200:203], a[188:191], a[24:27], v[200:203]\n"
    "s_waitcnt lgkmcnt(8)\n"
    "ds_read_b128 a[188:191], v242 offset:55296\n"
    "v_mfma_f32_16x16x32_bf16 v[204:207], a[160:163], a[28:31], 0\n"
    "buffer_load_dwordx4 v248, s[8:11], s41 offen offset:2048 sc1 lds\n"
    "v_mfma_f32_16x16x32_bf16 v[204:207], a[164:167], a[0:3], v[204:207]\n"
    "ds_read2st64_b32 v[196:197], v250 offset0:64 offset1:65\n"
    "v_mfma_f32_16x16x32_bf16 v[212:215], a[164:167], a[32:35], 0\n"
    "v_exp_f32_e32 v234, v234\n"
    "v_mfma_f32_16x16x32_bf16 v[212:215], a[160:163], a[60:63], v[212:215]\n"
    "ds_read2st64_b32 v[198:199], v250 offset0:66 offset1:67\n"
    "v_mfma_f32_16x16x32_bf16 v[220:223], a[160:163], a[92:95], 0\n"
    "v_exp_f32_e32 v235, v235\n"
    "v_mfma_f32_16x16x32_bf16 v[220:223], a[164:167], a[64:67], v[220:223]\n"
    "buffer_load_dwordx4 v248, s[8:11], s41 offen offset:3072 sc1 lds\n"
    "v_cvt_pk_bf16_f32 v232, v232, v233\n"
    "v_mfma_f32_16x16x32_bf16 v[228:231], a[164:167], a[96:99], 0\n"
    "s_add_u32 s41, s41, 0x8000\n"
    "v_exp_f32_e32 v224, v224\n"
    "v_mfma_f32_16x16x32_bf16 v[228:231], a[160:163], a[124:127], v[228:231]\n"
    "v_exp_f32_e32 v225, v225\n"
    "v_mfma_f32_16x16x32_bf16 v[236:239], a[160:163], a[156:159], 0\n"
    "ds_read_b64_tr_b16 a[160:161], v244 offset:32768\n"
    "v_cvt_pk_bf16_f32 v233, v234, v235\n"
    "v_mfma_f32_16x16x32_bf16 v[236:239], a[164:167], a[128:131], v[236:239]\n"
    "ds_read_b64_tr_b16 a[162:163], v244 offset:34816\n"
    "v_mfma_f32_16x16x32_bf16 v[236:239], a[168:171], a[132:135], v[236:239]\n"
    "ds_read_b64_tr_b16 a[164:165], v244 offset:40960\n"
    "v_mfma_f32_16x16x32_bf16 v[236:239], a[172:175], a[136:139], v[236:239]\n"
    "ds_read_b64_tr_b16 a[166:167], v244 offset:43008\n"
    "v_mfma_f32_16x16x32_bf16 v[228:231], a[172:175], a[104:107], v[228:231]\n"
    "v_exp_f32_e32 v226, v226\n"
    "v_mfma_f32_16x16x32_bf16 v[228:231], a[168:171], a[100:103], v[228:231]\n"
    "v_exp_f32_e32 v227, v227\n"
    "v_mfma_f32_16x16x32_bf16 v[220:223], a[168:171], a[68:71], v[220:223]\n"
    "v_cvt_pk_bf16_f32 v224, v224, v225\n"
    "v_mfma_f32_16x16x32_bf16 v[220:223], a[172:175], a[72:75], v[220:223]\n"
    "v_exp_f32_e32 v216, v216\n"
    "v_mfma_f32_16x16x32_bf16 v[212:215], a[172:175], a[40:43], v[212:215]\n"
    "v_exp_f32_e32 v217, v217\n"
    "v_mfma_f32_16x16x32_bf16 v[212:215], a[168:171], a[36:39], v[212:215]\n"
    "v_cvt_pk_bf16_f32 v225, v226, v227\n"
    "v_mfma_f32_16x16x32_bf16 v[204:207], a[168:171], a[4:7], v[204:207]\n"
    "ds_read_b64_tr_b16 a[168:169], v245 offset:32768\n"
    "v_mfma_f32_16x16x32_bf16 v[204:207], a[172:175], a[8:11], v[204:207]\n"
    "s_waitcnt lgkmcnt(7)\n"
    "ds_read_b64_tr_b16 a[170:171], v245 offset:34816\n"
    "v_mfma_f32_16x16x32_bf16 v[204:207], a[176:179], a[12:15], v[204:207]\n"
    "ds_read_b64_tr_b16 a[172:173], v245 offset:40960\n"
    "v_mfma_f32_16x16x32_bf16 v[204:207], a[180:183], a[16:19], v[204:207]\n"
    "ds_read_b64_tr_b16 a[174:175], v245 offset:43008\n"
    "v_mfma_f32_16x16x32_bf16 v[212:215], a[180:183], a[48:51], v[212:215]\n"
    "v_exp_f32_e32 v218, v218\n"
    "v_mfma_f32_16x16x32_bf16 v[212:215], a[176:179], a[44:47], v[212:215]\n"
    "v_exp_f32_e32 v219, v219\n"
    "v_mfma_f32_16x16x32_bf16 v[220:223], a[176:179], a[76:79], v[220:223]\n"
    "v_cvt_pk_bf16_f32 v216, v216, v217\n"
    "v_mfma_f32_16x16x32_bf16 v[220:223], a[180:183], a[80:83], v[220:223]\n"
    "v_exp_f32_e32 v208, v208\n"
    "v_mfma_f32_16x16x32_bf16 v[228:231], a[180:183], a[112:115], v[228:231]\n"
    "v_exp_f32_e32 v209, v209\n"
    "v_mfma_f32_16x16x32_bf16 v[228:231], a[176:179], a[108:111], v[228:231]\n"
    "v_cvt_pk_bf16_f32 v217, v218, v219\n"
    "v_mfma_f32_16x16x32_bf16 v[236:239], a[176:179], a[140:143], v[236:239]\n"
    "ds_read_b64_tr_b16 a[176:177], v246 offset:32768\n"
    "v_mfma_f32_16x16x32_bf16 v[236:239], a[180:183], a[144:147], v[236:239]\n"
    "ds_read_b64_tr_b16 a[178:179], v246 offset:34816\n"
    "v_mfma_f32_16x16x32_bf16 v[236:239], a[184:187], a[148:151], v[236:239]\n"
    "ds_read_b64_tr_b16 a[180:181], v246 offset:40960\n"
    "v_mfma_f32_16x16x32_bf16 v[236:239], a[188:191], a[152:155], v[236:239]\n"
    "ds_read_b64_tr_b16 a[182:183], v246 offset:43008\n"
    "v_mfma_f32_16x16x32_bf16 v[228:231], a[188:191], a[120:123], v[228:231]\n"
    "v_exp_f32_e32 v210, v210\n"
    "v_mfma_f32_16x16x32_bf16 v[228:231], a[184:187], a[116:119], v[228:231]\n"
    "v_exp_f32_e32 v211, v211\n"
    "v_mfma_f32_16x16x32_bf16 v[220:223], a[184:187], a[84:87], v[220:223]\n"
    "v_cvt_pk_bf16_f32 v208, v208, v209\n"
    "v_mfma_f32_16x16x32_bf16 v[220:223], a[188:191], a[88:91], v[220:223]\n"
    "v_exp_f32_e32 v200, v200\n"
    "v_mfma_f32_16x16x32_bf16 v[212:215], a[188:191], a[56:59], v[212:215]\n"
    "v_exp_f32_e32 v201, v201\n"
    "v_mfma_f32_16x16x32_bf16 v[212:215], a[184:187], a[52:55], v[212:215]\n"
    "v_cvt_pk_bf16_f32 v209, v210, v211\n"
    "v_mfma_f32_16x16x32_bf16 v[204:207], a[184:187], a[20:23], v[204:207]\n"
    "ds_read_b64_tr_b16 a[184:185], v247 offset:32768\n"
    "v_mfma_f32_16x16x32_bf16 v[204:207], a[188:191], a[24:27], v[204:207]\n"
    "s_waitcnt lgkmcnt(5)\n"
    "ds_read_b64_tr_b16 a[186:187], v247 offset:34816\n"
    "v_mfma_f32_16x16x32_bf16 v[0:3], a[160:163], v[160:163], v[0:3]\n"
    "ds_read_b64_tr_b16 a[188:189], v247 offset:40960\n"
    "v_mfma_f32_16x16x32_bf16 v[0:3], a[164:167], v[164:167], v[0:3]\n"
    "ds_read_b64_tr_b16 a[190:191], v247 offset:43008\n"
    "v_mfma_f32_16x16x32_bf16 a[248:251], a[164:167], v[172:175], a[248:251]\n"
    "v_exp_f32_e32 v202, v202\n"
    "v_mfma_f32_16x16x32_bf16 a[248:251], a[160:163], v[168:171], a[248:251]\n"
    "v_exp_f32_e32 v203, v203\n"
    "v_mfma_f32_16x16x32_bf16 v[56:59], a[160:163], v[176:179], v[56:59]\n"
    "v_cvt_pk_bf16_f32 v200, v200, v201\n"
    "v_mfma_f32_16x16x32_bf16 v[56:59], a[164:167], v[180:183], v[56:59]\n"
    "v_cvt_pk_bf16_f32 v201, v202, v203\n"
    "v_mfma_f32_16x16x32_bf16 v[60:63], a[164:167], v[188:191], v[60:63]\n"
    "v_exp_f32_e32 v236, v236\n"
    "v_mfma_f32_16x16x32_bf16 v[60:63], a[160:163], v[184:187], v[60:63]\n"
    "v_exp_f32_e32 v237, v237\n"
    "v_mfma_f32_16x16x32_bf16 v[64:67], a[160:163], v[192:195], v[64:67]\n"
    "ds_read_b64_tr_b16 a[160:161], v244 offset:36864\n"
    "v_mfma_f32_16x16x32_bf16 v[64:67], a[164:167], v[196:199], v[64:67]\n"
    "ds_read_b64_tr_b16 a[162:163], v244 offset:38912\n"
    "v_mfma_f32_16x16x32_bf16 v[76:79], a[168:171], v[192:195], v[76:79]\n"
    "ds_read_b64_tr_b16 a[164:165], v244 offset:45056\n"
    "v_mfma_f32_16x16x32_bf16 v[76:79], a[172:175], v[196:199], v[76:79]\n"
    "ds_read_b64_tr_b16 a[166:167], v244 offset:47104\n"
    "v_mfma_f32_16x16x32_bf16 v[72:75], a[172:175], v[188:191], v[72:75]\n"
    "v_exp_f32_e32 v238, v238\n"
    "v_mfma_f32_16x16x32_bf16 v[72:75], a[168:171], v[184:187], v[72:75]\n"
    "v_exp_f32_e32 v239, v239\n"
    "v_mfma_f32_16x16x32_bf16 v[68:71], a[168:171], v[176:179], v[68:71]\n"
    "v_cvt_pk_bf16_f32 v234, v236, v237\n"
    "v_mfma_f32_16x16x32_bf16 v[68:71], a[172:175], v[180:183], v[68:71]\n"
    "v_exp_f32_e32 v228, v228\n"
    "v_mfma_f32_16x16x32_bf16 a[252:255], a[172:175], v[172:175], a[252:255]\n"
    "v_exp_f32_e32 v229, v229\n"
    "v_mfma_f32_16x16x32_bf16 a[252:255], a[168:171], v[168:171], a[252:255]\n"
    "v_cvt_pk_bf16_f32 v235, v238, v239\n"
    "v_mfma_f32_16x16x32_bf16 v[4:7], a[168:171], v[160:163], v[4:7]\n"
    "ds_read_b64_tr_b16 a[168:169], v245 offset:36864\n"
    "v_mfma_f32_16x16x32_bf16 v[4:7], a[172:175], v[164:167], v[4:7]\n"
    "s_waitcnt lgkmcnt(5)\n"
    "ds_read_b64_tr_b16 a[170:171], v245 offset:38912\n"
    "v_mfma_f32_16x16x32_bf16 v[8:11], a[176:179], v[160:163], v[8:11]\n"
    "ds_read_b64_tr_b16 a[172:173], v245 offset:45056\n"
    "v_mfma_f32_16x16x32_bf16 v[8:11], a[180:183], v[164:167], v[8:11]\n"
    "ds_read_b64_tr_b16 a[174:175], v245 offset:47104\n"
    "v_mfma_f32_16x16x32_bf16 v[32:35], a[180:183], v[172:175], v[32:35]\n"
    "s_or_b32 s45, s44, s37\n"
    "s_cselect_b32 s46, 2.0, s81\n"
    "s_cselect_b32 s45, 2.0, s80\n"
    "v_mfma_f32_16x16x32_bf16 v[32:35], a[176:179], v[168:171], v[32:35]\n"
    "v_exp_f32_e32 v230, v230\n"
    "v_mfma_f32_16x16x32_bf16 v[80:83], a[176:179], v[176:179], v[80:83]\n"
    "v_exp_f32_e32 v231, v231\n"
    "v_mfma_f32_16x16x32_bf16 v[80:83], a[180:183], v[180:183], v[80:83]\n"
    "v_cvt_pk_bf16_f32 v226, v228, v229\n"
    "v_mfma_f32_16x16x32_bf16 v[84:87], a[180:183], v[188:191], v[84:87]\n"
    "v_exp_f32_e32 v220, v220\n"
    "v_mfma_f32_16x16x32_bf16 v[84:87], a[176:179], v[184:187], v[84:87]\n"
    "v_exp_f32_e32 v221, v221\n"
    "v_mfma_f32_16x16x32_bf16 v[88:91], a[176:179], v[192:195], v[88:91]\n"
    "ds_read_b64_tr_b16 a[176:177], v246 offset:36864\n"
    "v_mfma_f32_16x16x32_bf16 v[88:91], a[180:183], v[196:199], v[88:91]\n"
    "ds_read_b64_tr_b16 a[178:179], v246 offset:38912\n"
    "v_mfma_f32_16x16x32_bf16 v[100:103], a[184:187], v[192:195], v[100:103]\n"
    "ds_read_b64_tr_b16 a[180:181], v246 offset:45056\n"
    "v_mfma_f32_16x16x32_bf16 v[100:103], a[188:191], v[196:199], v[100:103]\n"
    "ds_read_b64_tr_b16 a[182:183], v246 offset:47104\n"
    "v_mfma_f32_16x16x32_bf16 v[96:99], a[188:191], v[188:191], v[96:99]\n"
    "v_cvt_pk_bf16_f32 v227, v230, v231\n"
    "v_mfma_f32_16x16x32_bf16 v[96:99], a[184:187], v[184:187], v[96:99]\n"
    "v_exp_f32_e32 v222, v222\n"
    "v_mfma_f32_16x16x32_bf16 v[92:95], a[184:187], v[176:179], v[92:95]\n"
    "v_exp_f32_e32 v223, v223\n"
    "v_mfma_f32_16x16x32_bf16 v[92:95], a[188:191], v[180:183], v[92:95]\n"
    "v_cvt_pk_bf16_f32 v218, v220, v221\n"
    "v_mfma_f32_16x16x32_bf16 v[36:39], a[188:191], v[172:175], v[36:39]\n"
    "v_exp_f32_e32 v212, v212\n"
    "v_mfma_f32_16x16x32_bf16 v[36:39], a[184:187], v[168:171], v[36:39]\n"
    "v_exp_f32_e32 v213, v213\n"
    "v_mfma_f32_16x16x32_bf16 v[12:15], a[184:187], v[160:163], v[12:15]\n"
    "s_mov_b32 m0, s49\n"
    "ds_read_b64_tr_b16 a[184:185], v247 offset:36864\n"
    "v_mfma_f32_16x16x32_bf16 v[12:15], a[188:191], v[164:167], v[12:15]\n"
    "s_waitcnt lgkmcnt(5)\n"
    "ds_read_b64_tr_b16 a[186:187], v247 offset:38912\n"
    "v_mfma_f32_16x16x32_bf16 v[16:19], a[160:163], v[160:163], v[16:19]\n"
    "ds_read_b64_tr_b16 a[188:189], v247 offset:45056\n"
    "v_mfma_f32_16x16x32_bf16 v[16:19], a[164:167], v[164:167], v[16:19]\n"
    "ds_read_b64_tr_b16 a[190:191], v247 offset:47104\n"
    "v_mfma_f32_16x16x32_bf16 v[40:43], a[164:167], v[172:175], v[40:43]\n"
    "buffer_load_dword v154, off, s[8:11], s45 sc1\n"
    "v_mfma_f32_16x16x32_bf16 v[40:43], a[160:163], v[168:171], v[40:43]\n"
    "v_cvt_pk_bf16_f32 v219, v222, v223\n"
    "v_mfma_f32_16x16x32_bf16 v[104:107], a[160:163], v[176:179], v[104:107]\n"
    "v_exp_f32_e32 v214, v214\n"
    "v_mfma_f32_16x16x32_bf16 v[104:107], a[164:167], v[180:183], v[104:107]\n"
    "s_waitcnt vmcnt(9)\n"
    "v_mfma_f32_16x16x32_bf16 v[108:111], a[164:167], v[188:191], v[108:111]\n"
    "s_barrier\n"
    "v_mfma_f32_16x16x32_bf16 v[108:111], a[160:163], v[184:187], v[108:111]\n"
    "v_exp_f32_e32 v215, v215\n"
    "v_mfma_f32_16x16x32_bf16 v[112:115], a[160:163], v[192:195], v[112:115]\n"
    "ds_read_b128 a[160:163], v243 offset:4096\n"
    "v_mfma_f32_16x16x32_bf16 v[112:115], a[164:167], v[196:199], v[112:115]\n"
    "ds_read_b128 a[164:167], v240 offset:0\n"
    "v_mfma_f32_16x16x32_bf16 v[124:127], a[168:171], v[192:195], v[124:127]\n"
    "ds_write_addtid_b32 v232 offset:16384\n"
    "v_cvt_pk_bf16_f32 v210, v212, v213\n"
    "v_mfma_f32_16x16x32_bf16 v[124:127], a[172:175], v[196:199], v[124:127]\n"
    "v_exp_f32_e32 v204, v204\n"
    "v_mfma_f32_16x16x32_bf16 v[120:123], a[172:175], v[188:191], v[120:123]\n"
    "v_exp_f32_e32 v205, v205\n"
    "v_mfma_f32_16x16x32_bf16 v[120:123], a[168:171], v[184:187], v[120:123]\n"
    "ds_write_addtid_b32 v233 offset:16640\n"
    "v_cvt_pk_bf16_f32 v211, v214, v215\n"
    "v_mfma_f32_16x16x32_bf16 v[116:119], a[168:171], v[176:179], v[116:119]\n"
    "v_exp_f32_e32 v206, v206\n"
    "v_mfma_f32_16x16x32_bf16 v[116:119], a[172:175], v[180:183], v[116:119]\n"
    "v_exp_f32_e32 v207, v207\n"
    "v_mfma_f32_16x16x32_bf16 v[44:47], a[172:175], v[172:175], v[44:47]\n"
    "ds_write_addtid_b32 v224 offset:12288\n"
    "v_cvt_pk_bf16_f32 v202, v204, v205\n"
    "v_mfma_f32_16x16x32_bf16 v[44:47], a[168:171], v[168:171], v[44:47]\n"
    "ds_write_addtid_b32 v225 offset:12544\n"
    "v_cvt_pk_bf16_f32 v203, v206, v207\n"
    "v_mfma_f32_16x16x32_bf16 v[20:23], a[168:171], v[160:163], v[20:23]\n"
    "ds_read_b128 a[168:171], v241 offset:0\n"
    "v_mfma_f32_16x16x32_bf16 v[20:23], a[172:175], v[164:167], v[20:23]\n"
    "s_waitcnt lgkmcnt(7)\n"
    "ds_read_b128 a[172:175], v240 offset:4096\n"
    "v_mfma_f32_16x16x32_bf16 v[24:27], a[176:179], v[160:163], v[24:27]\n"
    "ds_write_addtid_b32 v216 offset:8192\n"
    "v_mfma_f32_16x16x32_bf16 v[24:27], a[180:183], v[164:167], v[24:27]\n"
    "ds_write_addtid_b32 v217 offset:8448\n"
    "v_mfma_f32_16x16x32_bf16 v[48:51], a[180:183], v[172:175], v[48:51]\n"
    "ds_write_addtid_b32 v208 offset:4096\n"
    "v_mfma_f32_16x16x32_bf16 v[48:51], a[176:179], v[168:171], v[48:51]\n"
    "ds_write_addtid_b32 v209 offset:4352\n"
    "v_mfma_f32_16x16x32_bf16 v[128:131], a[176:179], v[176:179], v[128:131]\n"
    "ds_write_addtid_b32 v200 offset:0\n"
    "v_mfma_f32_16x16x32_bf16 v[128:131], a[180:183], v[180:183], v[128:131]\n"
    "ds_write_addtid_b32 v201 offset:256\n"
    "v_mfma_f32_16x16x32_bf16 v[132:135], a[180:183], v[188:191], v[132:135]\n"
    "ds_write_addtid_b32 v234 offset:16896\n"
    "v_mfma_f32_16x16x32_bf16 v[132:135], a[176:179], v[184:187], v[132:135]\n"
    "ds_write_addtid_b32 v235 offset:17152\n"
    "v_mfma_f32_16x16x32_bf16 v[136:139], a[176:179], v[192:195], v[136:139]\n"
    "ds_read_b128 a[176:179], v241 offset:4096\n"
    "v_mfma_f32_16x16x32_bf16 v[136:139], a[180:183], v[196:199], v[136:139]\n"
    "ds_read_b128 a[180:183], v242 offset:0\n"
    "v_mfma_f32_16x16x32_bf16 v[148:151], a[184:187], v[192:195], v[148:151]\n"
    "ds_write_addtid_b32 v226 offset:12800\n"
    "v_mfma_f32_16x16x32_bf16 v[148:151], a[188:191], v[196:199], v[148:151]\n"
    "ds_write_addtid_b32 v227 offset:13056\n"
    "v_mfma_f32_16x16x32_bf16 v[144:147], a[188:191], v[188:191], v[144:147]\n"
    "ds_write_addtid_b32 v218 offset:8704\n"
    "v_mfma_f32_16x16x32_bf16 v[144:147], a[184:187], v[184:187], v[144:147]\n"
    "ds_write_addtid_b32 v219 offset:8960\n"
    "v_mfma_f32_16x16x32_bf16 v[140:143], a[184:187], v[176:179], v[140:143]\n"
    "ds_write_addtid_b32 v210 offset:4608\n"
    "v_mfma_f32_16x16x32_bf16 v[140:143], a[188:191], v[180:183], v[140:143]\n"
    "ds_write_addtid_b32 v211 offset:4864\n"
    "v_mfma_f32_16x16x32_bf16 v[52:55], a[188:191], v[172:175], v[52:55]\n"
    "ds_write_addtid_b32 v202 offset:512\n"
    "v_mfma_f32_16x16x32_bf16 v[52:55], a[184:187], v[168:171], v[52:55]\n"
    "ds_write_addtid_b32 v203 offset:768\n"
    "v_mfma_f32_16x16x32_bf16 v[28:31], a[184:187], v[160:163], v[28:31]\n"
    "ds_read_b128 a[184:187], v243 offset:0\n"
    "v_mfma_f32_16x16x32_bf16 v[28:31], a[188:191], v[164:167], v[28:31]\n"
    "s_waitcnt lgkmcnt(15)\n"
    "ds_read_b128 a[188:191], v242 offset:4096\n"
    "v_mfma_f32_16x16x32_bf16 v[160:163], a[160:163], a[28:31], 0\n"
    "v_lshlrev_b32_e32 v253, 2, v249\n"
    "v_mfma_f32_16x16x32_bf16 v[160:163], a[164:167], a[0:3], v[160:163]\n"
    "v_mfma_f32_16x16x32_bf16 v[168:171], a[164:167], a[32:35], 0\n"
    "v_mfma_f32_16x16x32_bf16 v[168:171], a[160:163], a[60:63], v[168:171]\n"
    "v_mfma_f32_16x16x32_bf16 v[176:179], a[160:163], a[92:95], 0\n"
    "v_mfma_f32_16x16x32_bf16 v[176:179], a[164:167], a[64:67], v[176:179]\n"
    "s_waitcnt lgkmcnt(0)\n"
    "v_mfma_f32_16x16x32_bf16 v[184:187], a[164:167], a[96:99], 0\n"
    "s_barrier\n"
    "v_mfma_f32_16x16x32_bf16 v[184:187], a[160:163], a[124:127], v[184:187]\n"
    "s_add_u32 m0, s43, 32768\n"
    "ds_read2st64_b32 v[204:205], v250 offset0:0 offset1:1\n"
    "v_mfma_f32_16x16x32_bf16 v[192:195], a[160:163], a[156:159], 0\n"
    "ds_read_b128 a[160:163], v243 offset:6144\n"
    "v_mfma_f32_16x16x32_bf16 v[192:195], a[164:167], a[128:131], v[192:195]\n"
    "ds_read_b128 a[164:167], v240 offset:2048\n"
    "v_mfma_f32_16x16x32_bf16 v[192:195], a[168:171], a[132:135], v[192:195]\n"
    "buffer_load_dwordx4 v248, s[8:11], s40 offen offset:0 sc1 lds\n"
    "v_mfma_f32_16x16x32_bf16 v[192:195], a[172:175], a[136:139], v[192:195]\n"
    "v_mfma_f32_16x16x32_bf16 v[184:187], a[172:175], a[104:107], v[184:187]\n"
    "v_mfma_f32_16x16x32_bf16 v[184:187], a[168:171], a[100:103], v[184:187]\n"
    "ds_read2st64_b32 v[206:207], v250 offset0:2 offset1:3\n"
    "v_mfma_f32_16x16x32_bf16 v[176:179], a[168:171], a[68:71], v[176:179]\n"
    "v_mfma_f32_16x16x32_bf16 v[176:179], a[172:175], a[72:75], v[176:179]\n"
    "buffer_load_dwordx4 v248, s[8:11], s40 offen offset:1024 sc1 lds\n"
    "v_mfma_f32_16x16x32_bf16 v[168:171], a[172:175], a[40:43], v[168:171]\n"
    "v_mfma_f32_16x16x32_bf16 v[168:171], a[168:171], a[36:39], v[168:171]\n"
    "ds_read2st64_b32 v[212:213], v250 offset0:16 offset1:17\n"
    "v_mfma_f32_16x16x32_bf16 v[160:163], a[168:171], a[4:7], v[160:163]\n"
    "ds_read_b128 a[168:171], v241 offset:2048\n"
    "v_mfma_f32_16x16x32_bf16 v[160:163], a[172:175], a[8:11], v[160:163]\n"
    "ds_read_b128 a[172:175], v240 offset:6144\n"
    "v_mfma_f32_16x16x32_bf16 v[160:163], a[176:179], a[12:15], v[160:163]\n"
    "buffer_load_dwordx4 v248, s[8:11], s40 offen offset:2048 sc1 lds\n"
    "v_mfma_f32_16x16x32_bf16 v[160:163], a[180:183], a[16:19], v[160:163]\n"
    "ds_read2st64_b32 v[214:215], v250 offset0:18 offset1:19\n"
    "v_mfma_f32_16x16x32_bf16 v[168:171], a[180:183], a[48:51], v[168:171]\n"
    "v_mfma_f32_16x16x32_bf16 v[168:171], a[176:179], a[44:47], v[168:171]\n"
    "v_mfma_f32_16x16x32_bf16 v[176:179], a[176:179], a[76:79], v[176:179]\n"
    "ds_read2st64_b32 v[220:221], v250 offset0:32 offset1:33\n"
    "v_mfma_f32_16x16x32_bf16 v[176:179], a[180:183], a[80:83], v[176:179]\n"
    "buffer_load_dwordx4 v248, s[8:11], s40 offen offset:3072 sc1 lds\n"
    "v_mfma_f32_16x16x32_bf16 v[184:187], a[180:183], a[112:115], v[184:187]\n"
    "s_add_u32 m0, s43, 36864\n"
    "v_mfma_f32_16x16x32_bf16 v[184:187], a[176:179], a[108:111], v[184:187]\n"
    "s_add_u32 s40, s40, 0x8000\n"
    "ds_read2st64_b32 v[222:223], v250 offset0:34 offset1:35\n"
    "v_mfma_f32_16x16x32_bf16 v[192:195], a[176:179], a[140:143], v[192:195]\n"
    "ds_read_b128 a[176:179], v241 offset:6144\n"
    "v_mfma_f32_16x16x32_bf16 v[192:195], a[180:183], a[144:147], v[192:195]\n"
    "ds_read_b128 a[180:183], v242 offset:2048\n"
    "v_mfma_f32_16x16x32_bf16 v[192:195], a[184:187], a[148:151], v[192:195]\n"
    "buffer_load_dwordx4 v248, s[8:11], s41 offen offset:0 sc1 lds\n"
    "v_mfma_f32_16x16x32_bf16 v[192:195], a[188:191], a[152:155], v[192:195]\n"
    "v_mfma_f32_16x16x32_bf16 v[184:187], a[188:191], a[120:123], v[184:187]\n"
    "ds_read2st64_b32 v[228:229], v250 offset0:48 offset1:49\n"
    "v_mfma_f32_16x16x32_bf16 v[184:187], a[184:187], a[116:119], v[184:187]\n"
    "v_mfma_f32_16x16x32_bf16 v[176:179], a[184:187], a[84:87], v[176:179]\n"
    "v_exp_f32_e32 v192, v192\n"
    "v_mfma_f32_16x16x32_bf16 v[176:179], a[188:191], a[88:91], v[176:179]\n"
    "buffer_load_dwordx4 v248, s[8:11], s41 offen offset:1024 sc1 lds\n"
    "v_mfma_f32_16x16x32_bf16 v[168:171], a[188:191], a[56:59], v[168:171]\n"
    "ds_read2st64_b32 v[230:231], v250 offset0:50 offset1:51\n"
    "v_mfma_f32_16x16x32_bf16 v[168:171], a[184:187], a[52:55], v[168:171]\n"
    "v_exp_f32_e32 v193, v193\n"
    "v_mfma_f32_16x16x32_bf16 v[160:163], a[184:187], a[20:23], v[160:163]\n"
    "ds_read_b128 a[184:187], v243 offset:2048\n"
    "v_mfma_f32_16x16x32_bf16 v[160:163], a[188:191], a[24:27], v[160:163]\n"
    "s_waitcnt lgkmcnt(8)\n"
    "ds_read_b128 a[188:191], v242 offset:6144\n"
    "v_mfma_f32_16x16x32_bf16 v[164:167], a[160:163], a[28:31], 0\n"
    "buffer_load_dwordx4 v248, s[8:11], s41 offen offset:2048 sc1 lds\n"
    "v_mfma_f32_16x16x32_bf16 v[164:167], a[164:167], a[0:3], v[164:167]\n"
    "ds_read2st64_b32 v[236:237], v250 offset0:64 offset1:65\n"
    "v_mfma_f32_16x16x32_bf16 v[172:175], a[164:167], a[32:35], 0\n"
    "v_exp_f32_e32 v194, v194\n"
    "v_mfma_f32_16x16x32_bf16 v[172:175], a[160:163], a[60:63], v[172:175]\n"
    "ds_read2st64_b32 v[238:239], v250 offset0:66 offset1:67\n"
    "v_mfma_f32_16x16x32_bf16 v[180:183], a[160:163], a[92:95], 0\n"
    "v_exp_f32_e32 v195, v195\n"
    "v_mfma_f32_16x16x32_bf16 v[180:183], a[164:167], a[64:67], v[180:183]\n"
    "buffer_load_dwordx4 v248, s[8:11], s41 offen offset:3072 sc1 lds\n"
    "v_cvt_pk_bf16_f32 v192, v192, v193\n"
    "v_mfma_f32_16x16x32_bf16 v[188:191], a[164:167], a[96:99], 0\n"
    "s_add_u32 s41, s41, 0x8000\n"
    "v_exp_f32_e32 v184, v184\n"
    "v_mfma_f32_16x16x32_bf16 v[188:191], a[160:163], a[124:127], v[188:191]\n"
    "v_exp_f32_e32 v185, v185\n"
    "v_mfma_f32_16x16x32_bf16 v[196:199], a[160:163], a[156:159], 0\n"
    "ds_read_b64_tr_b16 a[160:161], v244 offset:49152\n"
    "v_mfma_f32_16x16x32_bf16 v[196:199], a[164:167], a[128:131], v[196:199]\n"
    "ds_read_b64_tr_b16 a[162:163], v244 offset:51200\n"
    "v_mfma_f32_16x16x32_bf16 v[196:199], a[168:171], a[132:135], v[196:199]\n"
    "ds_read_b64_tr_b16 a[164:165], v244 offset:57344\n"
    "v_mfma_f32_16x16x32_bf16 v[196:199], a[172:175], a[136:139], v[196:199]\n"
    "ds_read_b64_tr_b16 a[166:167], v244 offset:59392\n"
    "v_mfma_f32_16x16x32_bf16 v[188:191], a[172:175], a[104:107], v[188:191]\n"
    "v_cvt_pk_bf16_f32 v193, v194, v195\n"
    "v_mfma_f32_16x16x32_bf16 v[188:191], a[168:171], a[100:103], v[188:191]\n"
    "v_exp_f32_e32 v186, v186\n"
    "v_mfma_f32_16x16x32_bf16 v[180:183], a[168:171], a[68:71], v[180:183]\n"
    "v_exp_f32_e32 v187, v187\n"
    "v_mfma_f32_16x16x32_bf16 v[180:183], a[172:175], a[72:75], v[180:183]\n"
    "v_cvt_pk_bf16_f32 v184, v184, v185\n"
    "v_mfma_f32_16x16x32_bf16 v[172:175], a[172:175], a[40:43], v[172:175]\n"
    "v_exp_f32_e32 v176, v176\n"
    "v_mfma_f32_16x16x32_bf16 v[172:175], a[168:171], a[36:39], v[172:175]\n"
    "v_exp_f32_e32 v177, v177\n"
    "v_mfma_f32_16x16x32_bf16 v[164:167], a[168:171], a[4:7], v[164:167]\n"
    "ds_read_b64_tr_b16 a[168:169], v245 offset:49152\n"
    "v_mfma_f32_16x16x32_bf16 v[164:167], a[172:175], a[8:11], v[164:167]\n"
    "s_waitcnt lgkmcnt(7)\n"
    "ds_read_b64_tr_b16 a[170:171], v245 offset:51200\n"
    "v_mfma_f32_16x16x32_bf16 v[164:167], a[176:179], a[12:15], v[164:167]\n"
    "s_mov_b32 m0, s82\n"
    "ds_read_b64_tr_b16 a[172:173], v245 offset:57344\n"
    "v_mfma_f32_16x16x32_bf16 v[164:167], a[180:183], a[16:19], v[164:167]\n"
    "s_waitcnt vmcnt(8)\n"
    "ds_read_b64_tr_b16 a[174:175], v245 offset:59392\n"
    "v_mfma_f32_16x16x32_bf16 v[172:175], a[180:183], a[48:51], v[172:175]\n"
    "buffer_load_dwordx4 v253, s[8:11], s46 offen offset:0 sc1 lds\n"
    "v_mfma_f32_16x16x32_bf16 v[172:175], a[176:179], a[44:47], v[172:175]\n"
    "v_cvt_pk_bf16_f32 v185, v186, v187\n"
    "v_mfma_f32_16x16x32_bf16 v[180:183], a[176:179], a[76:79], v[180:183]\n"
    "v_exp_f32_e32 v178, v178\n"
    "v_mfma_f32_16x16x32_bf16 v[180:183], a[180:183], a[80:83], v[180:183]\n"
    "v_exp_f32_e32 v179, v179\n"
    "v_mfma_f32_16x16x32_bf16 v[188:191], a[180:183], a[112:115], v[188:191]\n"
    "v_cvt_pk_bf16_f32 v176, v176, v177\n"
    "v_mfma_f32_16x16x32_bf16 v[188:191], a[176:179], a[108:111], v[188:191]\n"
    "v_exp_f32_e32 v168, v168\n"
    "v_mfma_f32_16x16x32_bf16 v[196:199], a[176:179], a[140:143], v[196:199]\n"
    "ds_read_b64_tr_b16 a[176:177], v246 offset:49152\n"
    "v_mfma_f32_16x16x32_bf16 v[196:199], a[180:183], a[144:147], v[196:199]\n"
    "ds_read_b64_tr_b16 a[178:179], v246 offset:51200\n"
    "v_mfma_f32_16x16x32_bf16 v[196:199], a[184:187], a[148:151], v[196:199]\n"
    "ds_read_b64_tr_b16 a[180:181], v246 offset:57344\n"
    "v_mfma_f32_16x16x32_bf16 v[196:199], a[188:191], a[152:155], v[196:199]\n"
    "ds_read_b64_tr_b16 a[182:183], v246 offset:59392\n"
    "v_mfma_f32_16x16x32_bf16 v[188:191], a[188:191], a[120:123], v[188:191]\n"
    "v_exp_f32_e32 v169, v169\n"
    "v_mfma_f32_16x16x32_bf16 v[188:191], a[184:187], a[116:119], v[188:191]\n"
    "v_cvt_pk_bf16_f32 v177, v178, v179\n"
    "v_mfma_f32_16x16x32_bf16 v[180:183], a[184:187], a[84:87], v[180:183]\n"
    "v_exp_f32_e32 v170, v170\n"
    "v_mfma_f32_16x16x32_bf16 v[180:183], a[188:191], a[88:91], v[180:183]\n"
    "v_exp_f32_e32 v171, v171\n"
    "v_mfma_f32_16x16x32_bf16 v[172:175], a[188:191], a[56:59], v[172:175]\n"
    "v_cvt_pk_bf16_f32 v168, v168, v169\n"
    "v_mfma_f32_16x16x32_bf16 v[172:175], a[184:187], a[52:55], v[172:175]\n"
    "v_exp_f32_e32 v160, v160\n"
    "v_mfma_f32_16x16x32_bf16 v[164:167], a[184:187], a[20:23], v[164:167]\n"
    "ds_read_b64_tr_b16 a[184:185], v247 offset:49152\n"
    "v_mfma_f32_16x16x32_bf16 v[164:167], a[188:191], a[24:27], v[164:167]\n"
    "s_waitcnt lgkmcnt(5)\n"
    "ds_read_b64_tr_b16 a[186:187], v247 offset:51200\n"
    "v_mfma_f32_16x16x32_bf16 v[0:3], a[160:163], v[200:203], v[0:3]\n"
    "ds_read_b64_tr_b16 a[188:189], v247 offset:57344\n"
    "v_mfma_f32_16x16x32_bf16 v[0:3], a[164:167], v[204:207], v[0:3]\n"
    "ds_read_b64_tr_b16 a[190:191], v247 offset:59392\n"
    "v_mfma_f32_16x16x32_bf16 a[248:251], a[164:167], v[212:215], a[248:251]\n"
    "v_exp_f32_e32 v161, v161\n"
    "v_mfma_f32_16x16x32_bf16 a[248:251], a[160:163], v[208:211], a[248:251]\n"
    "v_cvt_pk_bf16_f32 v169, v170, v171\n"
    "v_mfma_f32_16x16x32_bf16 v[56:59], a[160:163], v[216:219], v[56:59]\n"
    "v_exp_f32_e32 v162, v162\n"
    "v_mfma_f32_16x16x32_bf16 v[56:59], a[164:167], v[220:223], v[56:59]\n"
    "v_exp_f32_e32 v163, v163\n"
    "v_mfma_f32_16x16x32_bf16 v[60:63], a[164:167], v[228:231], v[60:63]\n"
    "v_cvt_pk_bf16_f32 v160, v160, v161\n"
    "v_mfma_f32_16x16x32_bf16 v[60:63], a[160:163], v[224:227], v[60:63]\n"
    "v_cvt_pk_bf16_f32 v161, v162, v163\n"
    "v_mfma_f32_16x16x32_bf16 v[64:67], a[160:163], v[232:235], v[64:67]\n"
    "ds_read_b64_tr_b16 a[160:161], v244 offset:53248\n"
    "v_mfma_f32_16x16x32_bf16 v[64:67], a[164:167], v[236:239], v[64:67]\n"
    "ds_read_b64_tr_b16 a[162:163], v244 offset:55296\n"
    "v_mfma_f32_16x16x32_bf16 v[76:79], a[168:171], v[232:235], v[76:79]\n"
    "ds_read_b64_tr_b16 a[164:165], v244 offset:61440\n"
    "v_mfma_f32_16x16x32_bf16 v[76:79], a[172:175], v[236:239], v[76:79]\n"
    "ds_read_b64_tr_b16 a[166:167], v244 offset:63488\n"
    "v_mfma_f32_16x16x32_bf16 v[72:75], a[172:175], v[228:231], v[72:75]\n"
    "v_exp_f32_e32 v196, v196\n"
    "v_mfma_f32_16x16x32_bf16 v[72:75], a[168:171], v[224:227], v[72:75]\n"
    "v_exp_f32_e32 v197, v197\n"
    "v_mfma_f32_16x16x32_bf16 v[68:71], a[168:171], v[216:219], v[68:71]\n"
    "v_exp_f32_e32 v198, v198\n"
    "v_mfma_f32_16x16x32_bf16 v[68:71], a[172:175], v[220:223], v[68:71]\n"
    "v_exp_f32_e32 v199, v199\n"
    "v_mfma_f32_16x16x32_bf16 a[252:255], a[172:175], v[212:215], a[252:255]\n"
    "v_cvt_pk_bf16_f32 v194, v196, v197\n"
    "v_mfma_f32_16x16x32_bf16 a[252:255], a[168:171], v[208:211], a[252:255]\n"
    "v_exp_f32_e32 v188, v188\n"
    "v_mfma_f32_16x16x32_bf16 v[4:7], a[168:171], v[200:203], v[4:7]\n"
    "ds_read_b64_tr_b16 a[168:169], v245 offset:53248\n"
    "v_mfma_f32_16x16x32_bf16 v[4:7], a[172:175], v[204:207], v[4:7]\n"
    "s_waitcnt lgkmcnt(5)\n"
    "ds_read_b64_tr_b16 a[170:171], v245 offset:55296\n"
    "v_mfma_f32_16x16x32_bf16 v[8:11], a[176:179], v[200:203], v[8:11]\n"
    "ds_read_b64_tr_b16 a[172:173], v245 offset:61440\n"
    "v_mfma_f32_16x16x32_bf16 v[8:11], a[180:183], v[204:207], v[8:11]\n"
    "ds_read_b64_tr_b16 a[174:175], v245 offset:63488\n"
    "v_mfma_f32_16x16x32_bf16 v[32:35], a[180:183], v[212:215], v[32:35]\n"
    "v_exp_f32_e32 v189, v189\n"
    "v_mfma_f32_16x16x32_bf16 v[32:35], a[176:179], v[208:211], v[32:35]\n"
    "v_cvt_pk_bf16_f32 v195, v198, v199\n"
    "v_mfma_f32_16x16x32_bf16 v[80:83], a[176:179], v[216:219], v[80:83]\n"
    "v_exp_f32_e32 v190, v190\n"
    "v_mfma_f32_16x16x32_bf16 v[80:83], a[180:183], v[220:223], v[80:83]\n"
    "v_exp_f32_e32 v191, v191\n"
    "v_mfma_f32_16x16x32_bf16 v[84:87], a[180:183], v[228:231], v[84:87]\n"
    "v_cvt_pk_bf16_f32 v186, v188, v189\n"
    "v_mfma_f32_16x16x32_bf16 v[84:87], a[176:179], v[224:227], v[84:87]\n"
    "v_exp_f32_e32 v180, v180\n"
    "v_mfma_f32_16x16x32_bf16 v[88:91], a[176:179], v[232:235], v[88:91]\n"
    "ds_read_b64_tr_b16 a[176:177], v246 offset:53248\n"
    "v_mfma_f32_16x16x32_bf16 v[88:91], a[180:183], v[236:239], v[88:91]\n"
    "ds_read_b64_tr_b16 a[178:179], v246 offset:55296\n"
    "v_mfma_f32_16x16x32_bf16 v[100:103], a[184:187], v[232:235], v[100:103]\n"
    "ds_read_b64_tr_b16 a[180:181], v246 offset:61440\n"
    "v_mfma_f32_16x16x32_bf16 v[100:103], a[188:191], v[236:239], v[100:103]\n"
    "ds_read_b64_tr_b16 a[182:183], v246 offset:63488\n"
    "v_mfma_f32_16x16x32_bf16 v[96:99], a[188:191], v[228:231], v[96:99]\n"
    "v_exp_f32_e32 v181, v181\n"
    "v_mfma_f32_16x16x32_bf16 v[96:99], a[184:187], v[224:227], v[96:99]\n"
    "v_cvt_pk_bf16_f32 v187, v190, v191\n"
    "v_mfma_f32_16x16x32_bf16 v[92:95], a[184:187], v[216:219], v[92:95]\n"
    "v_exp_f32_e32 v182, v182\n"
    "v_mfma_f32_16x16x32_bf16 v[92:95], a[188:191], v[220:223], v[92:95]\n"
    "v_exp_f32_e32 v183, v183\n"
    "v_mfma_f32_16x16x32_bf16 v[36:39], a[188:191], v[212:215], v[36:39]\n"
    "v_cvt_pk_bf16_f32 v178, v180, v181\n"
    "v_mfma_f32_16x16x32_bf16 v[36:39], a[184:187], v[208:211], v[36:39]\n"
    "v_exp_f32_e32 v172, v172\n"
    "v_mfma_f32_16x16x32_bf16 v[12:15], a[184:187], v[200:203], v[12:15]\n"
    "s_mov_b32 m0, s49\n"
    "ds_read_b64_tr_b16 a[184:185], v247 offset:53248\n"
    "v_mfma_f32_16x16x32_bf16 v[12:15], a[188:191], v[204:207], v[12:15]\n"
    "s_waitcnt lgkmcnt(5)\n"
    "ds_read_b64_tr_b16 a[186:187], v247 offset:55296\n"
    "v_mfma_f32_16x16x32_bf16 v[16:19], a[160:163], v[200:203], v[16:19]\n"
    "ds_read_b64_tr_b16 a[188:189], v247 offset:61440\n"
    "v_mfma_f32_16x16x32_bf16 v[16:19], a[164:167], v[204:207], v[16:19]\n"
    "ds_read_b64_tr_b16 a[190:191], v247 offset:63488\n"
    "v_mfma_f32_16x16x32_bf16 v[40:43], a[164:167], v[212:215], v[40:43]\n"
    "v_exp_f32_e32 v173, v173\n"
    "v_mfma_f32_16x16x32_bf16 v[40:43], a[160:163], v[208:211], v[40:43]\n"
    "v_cvt_pk_bf16_f32 v179, v182, v183\n"
    "v_mfma_f32_16x16x32_bf16 v[104:107], a[160:163], v[216:219], v[104:107]\n"
    "v_exp_f32_e32 v174, v174\n"
    "v_mfma_f32_16x16x32_bf16 v[104:107], a[164:167], v[220:223], v[104:107]\n"
    "s_waitcnt vmcnt(9)\n"
    "v_mfma_f32_16x16x32_bf16 v[108:111], a[164:167], v[228:231], v[108:111]\n"
    "s_barrier\n"
    "v_mfma_f32_16x16x32_bf16 v[108:111], a[160:163], v[224:227], v[108:111]\n"
    "v_exp_f32_e32 v175, v175\n"
    "v_mfma_f32_16x16x32_bf16 v[112:115], a[160:163], v[232:235], v[112:115]\n"
    "ds_read_b128 a[160:163], v243 offset:20480\n"
    "v_mfma_f32_16x16x32_bf16 v[112:115], a[164:167], v[236:239], v[112:115]\n"
    "ds_read_b128 a[164:167], v240 offset:16384\n"
    "v_mfma_f32_16x16x32_bf16 v[124:127], a[168:171], v[232:235], v[124:127]\n"
    "ds_write_addtid_b32 v192 offset:16384\n"
    "v_cvt_pk_bf16_f32 v170, v172, v173\n"
    "v_mfma_f32_16x16x32_bf16 v[124:127], a[172:175], v[236:239], v[124:127]\n"
    "v_exp_f32_e32 v164, v164\n"
    "v_mfma_f32_16x16x32_bf16 v[120:123], a[172:175], v[228:231], v[120:123]\n"
    "v_exp_f32_e32 v165, v165\n"
    "v_mfma_f32_16x16x32_bf16 v[120:123], a[168:171], v[224:227], v[120:123]\n"
    "ds_write_addtid_b32 v193 offset:16640\n"
    "v_cvt_pk_bf16_f32 v171, v174, v175\n"
    "v_mfma_f32_16x16x32_bf16 v[116:119], a[168:171], v[216:219], v[116:119]\n"
    "v_exp_f32_e32 v166, v166\n"
    "v_mfma_f32_16x16x32_bf16 v[116:119], a[172:175], v[220:223], v[116:119]\n"
    "v_exp_f32_e32 v167, v167\n"
    "v_mfma_f32_16x16x32_bf16 v[44:47], a[172:175], v[212:215], v[44:47]\n"
    "ds_write_addtid_b32 v184 offset:12288\n"
    "v_cvt_pk_bf16_f32 v162, v164, v165\n"
    "v_mfma_f32_16x16x32_bf16 v[44:47], a[168:171], v[208:211], v[44:47]\n"
    "ds_write_addtid_b32 v185 offset:12544\n"
    "v_cvt_pk_bf16_f32 v163, v166, v167\n"
    "v_mfma_f32_16x16x32_bf16 v[20:23], a[168:171], v[200:203], v[20:23]\n"
    "ds_read_b128 a[168:171], v241 offset:16384\n"
    "v_mfma_f32_16x16x32_bf16 v[20:23], a[172:175], v[204:207], v[20:23]\n"
    "s_waitcnt lgkmcnt(7)\n"
    "ds_read_b128 a[172:175], v240 offset:20480\n"
    "v_mfma_f32_16x16x32_bf16 v[24:27], a[176:179], v[200:203], v[24:27]\n"
    "ds_write_addtid_b32 v176 offset:8192\n"
    "v_mfma_f32_16x16x32_bf16 v[24:27], a[180:183], v[204:207], v[24:27]\n"
    "ds_write_addtid_b32 v177 offset:8448\n"
    "v_mfma_f32_16x16x32_bf16 v[48:51], a[180:183], v[212:215], v[48:51]\n"
    "ds_write_addtid_b32 v168 offset:4096\n"
    "v_mfma_f32_16x16x32_bf16 v[48:51], a[176:179], v[208:211], v[48:51]\n"
    "ds_write_addtid_b32 v169 offset:4352\n"
    "v_mfma_f32_16x16x32_bf16 v[128:131], a[176:179], v[216:219], v[128:131]\n"
    "ds_write_addtid_b32 v160 offset:0\n"
    "v_mfma_f32_16x16x32_bf16 v[128:131], a[180:183], v[220:223], v[128:131]\n"
    "ds_write_addtid_b32 v161 offset:256\n"
    "v_mfma_f32_16x16x32_bf16 v[132:135], a[180:183], v[228:231], v[132:135]\n"
    "ds_write_addtid_b32 v194 offset:16896\n"
    "v_mfma_f32_16x16x32_bf16 v[132:135], a[176:179], v[224:227], v[132:135]\n"
    "ds_write_addtid_b32 v195 offset:17152\n"
    "v_mfma_f32_16x16x32_bf16 v[136:139], a[176:179], v[232:235], v[136:139]\n"
    "ds_read_b128 a[176:179], v241 offset:20480\n"
    "v_mfma_f32_16x16x32_bf16 v[136:139], a[180:183], v[236:239], v[136:139]\n"
    "ds_read_b128 a[180:183], v242 offset:16384\n"
    "v_mfma_f32_16x16x32_bf16 v[148:151], a[184:187], v[232:235], v[148:151]\n"
    "ds_write_addtid_b32 v186 offset:12800\n"
    "v_mfma_f32_16x16x32_bf16 v[148:151], a[188:191], v[236:239], v[148:151]\n"
    "ds_write_addtid_b32 v187 offset:13056\n"
    "v_mfma_f32_16x16x32_bf16 v[144:147], a[188:191], v[228:231], v[144:147]\n"
    "ds_write_addtid_b32 v178 offset:8704\n"
    "v_mfma_f32_16x16x32_bf16 v[144:147], a[184:187], v[224:227], v[144:147]\n"
    "ds_write_addtid_b32 v179 offset:8960\n"
    "v_mfma_f32_16x16x32_bf16 v[140:143], a[184:187], v[216:219], v[140:143]\n"
    "s_sub_u32 s44, s44, 1\n"
    "ds_write_addtid_b32 v170 offset:4608\n"
    "v_mfma_f32_16x16x32_bf16 v[140:143], a[188:191], v[220:223], v[140:143]\n"
    "ds_write_addtid_b32 v171 offset:4864\n"
    "v_mfma_f32_16x16x32_bf16 v[52:55], a[188:191], v[212:215], v[52:55]\n"
    "ds_write_addtid_b32 v162 offset:512\n"
    "v_mfma_f32_16x16x32_bf16 v[52:55], a[184:187], v[208:211], v[52:55]\n"
    "ds_write_addtid_b32 v163 offset:768\n"
    "v_mfma_f32_16x16x32_bf16 v[28:31], a[184:187], v[200:203], v[28:31]\n"
    "ds_read_b128 a[184:187], v243 offset:16384\n"
    "v_mfma_f32_16x16x32_bf16 v[28:31], a[188:191], v[204:207], v[28:31]\n"
    "s_waitcnt lgkmcnt(15)\n"
    "ds_read_b128 a[188:191], v242 offset:20480\n"
    "s_cbranch_scc0 .Lat_p\n"
    "s_cmp_lg_u32 s37, 0\n"
    "s_cbranch_scc1 .Lat_tail\n"
    "s_waitcnt vmcnt(0)\n"
    "v_cmp_ne_u32_e32 vcc, s39, v154\n"
    "s_mov_b32 s37, 1\n"
    "s_bitcmp1_b32 %1, 0\n"
    "s_cselect_b32 s44, 5, 3\n"
    "s_nop 1\n"
    "s_cbranch_vccnz .Lat_lpoll\n"
    ".p2align 4\n"
    ".Lat_g:\n"
    "v_mfma_f32_16x16x32_bf16 v[200:203], a[160:163], a[28:31], 0\n"
    "v_mfma_f32_16x16x32_bf16 v[200:203], a[164:167], a[0:3], v[200:203]\n"
    "v_mfma_f32_16x16x32_bf16 v[208:211], a[164:167], a[32:35], 0\n"
    "v_mfma_f32_16x16x32_bf16 v[208:211], a[160:163], a[60:63], v[208:211]\n"
    "v_mfma_f32_16x16x32_bf16 v[216:219], a[160:163], a[92:95], 0\n"
    "v_mfma_f32_16x16x32_bf16 v[216:219], a[164:167], a[64:67], v[216:219]\n"
    "s_waitcnt lgkmcnt(0)\n"
    "v_mfma_f32_16x16x32_bf16 v[224:227], a[164:167], a[96:99], 0\n"
    "s_barrier\n"
    "v_mfma_f32_16x16x32_bf16 v[224:227], a[160:163], a[124:127], v[224:227]\n"
    "s_add_u32 m0, s43, 49152\n"
    "ds_read2st64_b32 v[164:165], v250 offset0:0 offset1:1\n"
    "v_mfma_f32_16x16x32_bf16 v[232:235], a[160:163], a[156:159], 0\n"
    "ds_read_b128 a[160:163], v243 offset:22528\n"
    "v_mfma_f32_16x16x32_bf16 v[232:235], a[164:167], a[128:131], v[232:235]\n"
    "ds_read_b128 a[164:167], v240 offset:18432\n"
    "v_mfma_f32_16x16x32_bf16 v[232:235], a[168:171], a[132:135], v[232:235]\n"
    "buffer_load_dwordx4 v248, s[8:11], s40 offen offset:0 sc1 lds\n"
    "v_mfma_f32_16x16x32_bf16 v[232:235], a[172:175], a[136:139], v[232:235]\n"
    "v_mfma_f32_16x16x32_bf16 v[224:227], a[172:175], a[104:107], v[224:227]\n"
    "v_mfma_f32_16x16x32_bf16 v[224:227], a[168:171], a[100:103], v[224:227]\n"
    "ds_read2st64_b32 v[166:167], v250 offset0:2 offset1:3\n"
    "v_mfma_f32_16x16x32_bf16 v[216:219], a[168:171], a[68:71], v[216:219]\n"
    "buffer_load_dwordx4 v248, s[8:11], s40 offen offset:1024 sc1 lds\n"
    "v_mfma_f32_16x16x32_bf16 v[216:219], a[172:175], a[72:75], v[216:219]\n"
    "v_mfma_f32_16x16x32_bf16 v[208:211], a[172:175], a[40:43], v[208:211]\n"
    "v_mfma_f32_16x16x32_bf16 v[208:211], a[168:171], a[36:39], v[208:211]\n"
    "ds_read2st64_b32 v[172:173], v250 offset0:16 offset1:17\n"
    "v_mfma_f32_16x16x32_bf16 v[200:203], a[168:171], a[4:7], v[200:203]\n"
    "ds_read_b128 a[168:171], v241 offset:18432\n"
    "buffer_load_dwordx4 v248, s[8:11], s40 offen offset:2048 sc1 lds\n"
    "v_mfma_f32_16x16x32_bf16 v[200:203], a[172:175], a[8:11], v[200:203]\n"
    "ds_read_b128 a[172:175], v240 offset:22528\n"
    "v_mfma_f32_16x16x32_bf16 v[200:203], a[176:179], a[12:15], v[200:203]\n"
    "ds_read2st64_b32 v[174:175], v250 offset0:18 offset1:19\n"
    "v_mfma_f32_16x16x32_bf16 v[200:203], a[180:183], a[16:19], v[200:203]\n"
    "v_mfma_f32_16x16x32_bf16 v[208:211], a[180:183], a[48:51], v[208:211]\n"
    "buffer_load_dwordx4 v248, s[8:11], s40 offen offset:3072 sc1 lds\n"
    "v_mfma_f32_16x16x32_bf16 v[208:211], a[176:179], a[44:47], v[208:211]\n"
    "s_add_u32 m0, s43, 53248\n"
    "v_mfma_f32_16x16x32_bf16 v[216:219], a[176:179], a[76:79], v[216:219]\n"
    "s_add_u32 s40, s40, 0x8000\n"
    "ds_read2st64_b32 v[180:181], v250 offset0:32 offset1:33\n"
    "v_mfma_f32_16x16x32_bf16 v[216:219], a[180:183], a[80:83], v[216:219]\n"
    "v_mfma_f32_16x16x32_bf16 v[224:227], a[180:183], a[112:115], v[224:227]\n"
    "buffer_load_dwordx4 v248, s[8:11], s41 offen offset:0 sc1 lds\n"
    "v_mfma_f32_16x16x32_bf16 v[224:227], a[176:179], a[108:111], v[224:227]\n"
    "ds_read2st64_b32 v[182:183], v250 offset0:34 offset1:35\n"
    "v_mfma_f32_16x16x32_bf16 v[232:235], a[176:179], a[140:143], v[232:235]\n"
    "ds_read_b128 a[176:179], v241 offset:22528\n"
    "v_mfma_f32_16x16x32_bf16 v[232:235], a[180:183], a[144:147], v[232:235]\n"
    "ds_read_b128 a[180:183], v242 offset:18432\n"
    "v_mfma_f32_16x16x32_bf16 v[232:235], a[184:187], a[148:151], v[232:235]\n"
    "buffer_load_dwordx4 v248, s[8:11], s41 offen offset:1024 sc1 lds\n"
    "v_mfma_f32_16x16x32_bf16 v[232:235], a[188:191], a[152:155], v[232:235]\n"
    "v_mfma_f32_16x16x32_bf16 v[224:227], a[188:191], a[120:123], v[224:227]\n"
    "ds_read2st64_b32 v[188:189], v250 offset0:48 offset1:49\n"
    "v_mfma_f32_16x16x32_bf16 v[224:227], a[184:187], a[116:119], v[224:227]\n"
    "v_mfma_f32_16x16x32_bf16 v[216:219], a[184:187], a[84:87], v[216:219]\n"
    "buffer_load_dwordx4 v248, s[8:11], s41 offen offset:2048 sc1 lds\n"
    "v_mfma_f32_16x16x32_bf16 v[216:219], a[188:191], a[88:91], v[216:219]\n"
    "ds_read2st64_b32 v[190:191], v250 offset0:50 offset1:51\n"
    "v_mfma_f32_16x16x32_bf16 v[208:211], a[188:191], a[56:59], v[208:211]\n"
    "ds_read_b64 v[254:255], v252 offset:0\n"
    "v_mfma_f32_16x16x32_bf16 v[208:211], a[184:187], a[52:55], v[208:211]\n"
    "ds_read2st64_b32 v[196:197], v250 offset0:64 offset1:65\n"
    "v_mfma_f32_16x16x32_bf16 v[200:203], a[184:187], a[20:23], v[200:203]\n"
    "ds_read_b128 a[184:187], v243 offset:18432\n"
    "buffer_load_dwordx4 v248, s[8:11], s41 offen offset:3072 sc1 lds\n"
    "v_mfma_f32_16x16x32_bf16 v[200:203], a[188:191], a[24:27], v[200:203]\n"
    "s_waitcnt lgkmcnt(15)\n"
    "s_add_u32 s41, s41, 0x8000\n"
    "ds_read_b128 a[188:191], v242 offset:22528\n"
    "v_mfma_f32_16x16x32_bf16 v[204:207], a[160:163], a[28:31], 0\n"
    "s_waitcnt lgkmcnt(3)\n"
    "v_mad_u32_u16 v253, v254, s42, v251 op_sel:[0,0,0,0]\n"
    "v_mfma_f32_16x16x32_bf16 v[204:207], a[164:167], a[0:3], v[204:207]\n"
    "buffer_load_dwordx4 a[200:203], v253, s[20:23], 0 offen offset:0 sc1\n"
    "v_mfma_f32_16x16x32_bf16 v[212:215], a[164:167], a[32:35], 0\n"
    "v_exp_f32_e32 v232, v232\n"
    "v_mfma_f32_16x16x32_bf16 v[212:215], a[160:163], a[60:63], v[212:215]\n"
    "ds_read2st64_b32 v[198:199], v250 offset0:66 offset1:67\n"
    "v_mfma_f32_16x16x32_bf16 v[220:223], a[160:163], a[92:95], 0\n"
    "v_mad_u32_u16 v253, v254, s42, v251 op_sel:[1,0,0,0]\n"
    "v_mfma_f32_16x16x32_bf16 v[220:223], a[164:167], a[64:67], v[220:223]\n"
    "buffer_load_dwordx4 a[204:207], v253, s[20:23], 0 offen offset:0 sc1\n"
    "v_mfma_f32_16x16x32_bf16 v[228:231], a[164:167], a[96:99], 0\n"
    "v_exp_f32_e32 v233, v233\n"
    "v_mfma_f32_16x16x32_bf16 v[228:231], a[160:163], a[124:127], v[228:231]\n"
    "v_exp_f32_e32 v234, v234\n"
    "v_mfma_f32_16x16x32_bf16 v[236:239], a[160:163], a[156:159], 0\n"
    "ds_read_b64_tr_b16 a[160:161], v244 offset:0\n"
    "v_mad_u32_u16 v253, v255, s42, v251 op_sel:[0,0,0,0]\n"
    "v_mfma_f32_16x16x32_bf16 v[236:239], a[164:167], a[128:131], v[236:239]\n"
    "ds_read_b64_tr_b16 a[162:163], v244 offset:2048\n"
    "buffer_load_dwordx4 a[208:211], v253, s[20:23], 0 offen offset:0 sc1\n"
    "v_mfma_f32_16x16x32_bf16 v[236:239], a[168:171], a[132:135], v[236:239]\n"
    "ds_read_b64_tr_b16 a[164:165], v244 offset:8192\n"
    "v_mfma_f32_16x16x32_bf16 v[236:239], a[172:175], a[136:139], v[236:239]\n"
    "ds_read_b64_tr_b16 a[166:167], v244 offset:10240\n"
    "v_mfma_f32_16x16x32_bf16 v[228:231], a[172:175], a[104:107], v[228:231]\n"
    "v_mad_u32_u16 v253, v255, s42, v251 op_sel:[1,0,0,0]\n"
    "v_mfma_f32_16x16x32_bf16 v[228:231], a[168:171], a[100:103], v[228:231]\n"
    "buffer_load_dwordx4 a[212:215], v253, s[20:23], 0 offen offset:0 sc1\n"
    "ds_read_b64 v[254:255], v252 offset:8\n"
    "v_mfma_f32_16x16x32_bf16 v[220:223], a[168:171], a[68:71], v[220:223]\n"
    "v_exp_f32_e32 v235, v235\n"
    "v_mfma_f32_16x16x32_bf16 v[220:223], a[172:175], a[72:75], v[220:223]\n"
    "v_cvt_pk_bf16_f32 v232, v232, v233\n"
    "v_mfma_f32_16x16x32_bf16 v[212:215], a[172:175], a[40:43], v[212:215]\n"
    "v_exp_f32_e32 v224, v224\n"
    "v_mfma_f32_16x16x32_bf16 v[212:215], a[168:171], a[36:39], v[212:215]\n"
    "v_exp_f32_e32 v225, v225\n"
    "v_mfma_f32_16x16x32_bf16 v[204:207], a[168:171], a[4:7], v[204:207]\n"
    "ds_read_b64_tr_b16 a[168:169], v245 offset:0\n"
    "v_cvt_pk_bf16_f32 v233, v234, v235\n"
    "v_mfma_f32_16x16x32_bf16 v[204:207], a[172:175], a[8:11], v[204:207]\n"
    "ds_read_b64_tr_b16 a[170:171], v245 offset:2048\n"
    "v_mfma_f32_16x16x32_bf16 v[204:207], a[176:179], a[12:15], v[204:207]\n"
    "s_waitcnt lgkmcnt(2)\n"
    "ds_read_b64_tr_b16 a[172:173], v245 offset:8192\n"
    "v_mad_u32_u16 v253, v254, s42, v251 op_sel:[0,0,0,0]\n"
    "v_mfma_f32_16x16x32_bf16 v[204:207], a[180:183], a[16:19], v[204:207]\n"
    "ds_read_b64_tr_b16 a[174:175], v245 offset:10240\n"
    "buffer_load_dwordx4 a[216:219], v253, s[20:23], 0 offen offset:0 sc1\n"
    "v_mfma_f32_16x16x32_bf16 v[212:215], a[180:183], a[48:51], v[212:215]\n"
    "v_exp_f32_e32 v226, v226\n"
    "v_mfma_f32_16x16x32_bf16 v[212:215], a[176:179], a[44:47], v[212:215]\n"
    "v_exp_f32_e32 v227, v227\n"
    "v_mfma_f32_16x16x32_bf16 v[220:223], a[176:179], a[76:79], v[220:223]\n"
    "v_mad_u32_u16 v253, v254, s42, v251 op_sel:[1,0,0,0]\n"
    "v_cvt_pk_bf16_f32 v224, v224, v225\n"
    "v_mfma_f32_16x16x32_bf16 v[220:223], a[180:183], a[80:83], v[220:223]\n"
    "buffer_load_dwordx4 a[220:223], v253, s[20:23], 0 offen offset:0 sc1\n"
    "v_mfma_f32_16x16x32_bf16 v[228:231], a[180:183], a[112:115], v[228:231]\n"
    "v_exp_f32_e32 v216, v216\n"
    "v_mfma_f32_16x16x32_bf16 v[228:231], a[176:179], a[108:111], v[228:231]\n"
    "v_exp_f32_e32 v217, v217\n"
    "v_mfma_f32_16x16x32_bf16 v[236:239], a[176:179], a[140:143], v[236:239]\n"
    "ds_read_b64_tr_b16 a[176:177], v246 offset:0\n"
    "v_cvt_pk_bf16_f32 v225, v226, v227\n"
    "v_mfma_f32_16x16x32_bf16 v[236:239], a[180:183], a[144:147], v[236:239]\n"
    "ds_read_b64_tr_b16 a[178:179], v246 offset:2048\n"
    "v_mfma_f32_16x16x32_bf16 v[236:239], a[184:187], a[148:151], v[236:239]\n"
    "ds_read_b64_tr_b16 a[180:181], v246 offset:8192\n"
    "v_mfma_f32_16x16x32_bf16 v[236:239], a[188:191], a[152:155], v[236:239]\n"
    "ds_read_b64_tr_b16 a[182:183], v246 offset:10240\n"
    "v_mfma_f32_16x16x32_bf16 v[228:231], a[188:191], a[120:123], v[228:231]\n"
    "ds_read_b64 v[254:255], v252 offset:32\n"
    "v_mfma_f32_16x16x32_bf16 v[228:231], a[184:187], a[116:119], v[228:231]\n"
    "v_exp_f32_e32 v218, v218\n"
    "v_mfma_f32_16x16x32_bf16 v[220:223], a[184:187], a[84:87], v[220:223]\n"
    "v_exp_f32_e32 v219, v219\n"
    "v_mfma_f32_16x16x32_bf16 v[220:223], a[188:191], a[88:91], v[220:223]\n"
    "v_cvt_pk_bf16_f32 v216, v216, v217\n"
    "v_mfma_f32_16x16x32_bf16 v[212:215], a[188:191], a[56:59], v[212:215]\n"
    "s_waitcnt lgkmcnt(0)\n"
    "v_mad_u32_u16 v253, v254, s42, v251 op_sel:[0,0,0,0]\n"
    "v_mfma_f32_16x16x32_bf16 v[212:215], a[184:187], a[52:55], v[212:215]\n"
    "buffer_load_dwordx4 a[224:227], v253, s[20:23], 0 offen offset:0 sc1\n"
    "v_mfma_f32_16x16x32_bf16 v[204:207], a[184:187], a[20:23], v[204:207]\n"
    "ds_read_b64_tr_b16 a[184:185], v247 offset:0\n"
    "v_mfma_f32_16x16x32_bf16 v[204:207], a[188:191], a[24:27], v[204:207]\n"
    "ds_read_b64_tr_b16 a[186:187], v247 offset:2048\n"
    "v_mfma_f32_16x16x32_bf16 v[0:3], a[160:163], v[160:163], v[0:3]\n"
    "ds_read_b64_tr_b16 a[188:189], v247 offset:8192\n"
    "v_mad_u32_u16 v253, v254, s42, v251 op_sel:[1,0,0,0]\n"
    "v_mfma_f32_16x16x32_bf16 v[0:3], a[164:167], v[164:167], v[0:3]\n"
    "ds_read_b64_tr_b16 a[190:191], v247 offset:10240\n"
    "buffer_load_dwordx4 a[228:231], v253, s[20:23], 0 offen offset:0 sc1\n"
    "v_mfma_f32_16x16x32_bf16 a[248:251], a[164:167], v[172:175], a[248:251]\n"
    "v_exp_f32_e32 v208, v208\n"
    "v_mfma_f32_16x16x32_bf16 a[248:251], a[160:163], v[168:171], a[248:251]\n"
    "v_exp_f32_e32 v209, v209\n"
    "v_mfma_f32_16x16x32_bf16 v[56:59], a[160:163], v[176:179], v[56:59]\n"
    "v_mad_u32_u16 v253, v255, s42, v251 op_sel:[0,0,0,0]\n"
    "v_cvt_pk_bf16_f32 v217, v218, v219\n"
    "v_mfma_f32_16x16x32_bf16 v[56:59], a[164:167], v[180:183], v[56:59]\n"
    "buffer_load_dwordx4 a[232:235], v253, s[20:23], 0 offen offset:0 sc1\n"
    "v_mfma_f32_16x16x32_bf16 v[60:63], a[164:167], v[188:191], v[60:63]\n"
    "v_exp_f32_e32 v210, v210\n"
    "v_mfma_f32_16x16x32_bf16 v[60:63], a[160:163], v[184:187], v[60:63]\n"
    "v_exp_f32_e32 v211, v211\n"
    "v_mfma_f32_16x16x32_bf16 v[64:67], a[160:163], v[192:195], v[64:67]\n"
    "ds_read_b64_tr_b16 a[160:161], v244 offset:4096\n"
    "v_mad_u32_u16 v253, v255, s42, v251 op_sel:[1,0,0,0]\n"
    "v_mfma_f32_16x16x32_bf16 v[64:67], a[164:167], v[196:199], v[64:67]\n"
    "ds_read_b64_tr_b16 a[162:163], v244 offset:6144\n"
    "buffer_load_dwordx4 a[236:239], v253, s[20:23], 0 offen offset:0 sc1\n"
    "v_mfma_f32_16x16x32_bf16 v[76:79], a[168:171], v[192:195], v[76:79]\n"
    "ds_read_b64_tr_b16 a[164:165], v244 offset:12288\n"
    "v_cvt_pk_bf16_f32 v208, v208, v209\n"
    "v_mfma_f32_16x16x32_bf16 v[76:79], a[172:175], v[196:199], v[76:79]\n"
    "ds_read_b64_tr_b16 a[166:167], v244 offset:14336\n"
    "v_mfma_f32_16x16x32_bf16 v[72:75], a[172:175], v[188:191], v[72:75]\n"
    "ds_read_b64 v[254:255], v252 offset:40\n"
    "v_mfma_f32_16x16x32_bf16 v[72:75], a[168:171], v[184:187], v[72:75]\n"
    "v_exp_f32_e32 v200, v200\n"
    "v_mfma_f32_16x16x32_bf16 v[68:71], a[168:171], v[176:179], v[68:71]\n"
    "v_exp_f32_e32 v201, v201\n"
    "v_mfma_f32_16x16x32_bf16 v[68:71], a[172:175], v[180:183], v[68:71]\n"
    "v_cvt_pk_bf16_f32 v209, v210, v211\n"
    "v_mfma_f32_16x16x32_bf16 a[252:255], a[172:175], v[172:175], a[252:255]\n"
    "v_exp_f32_e32 v202, v202\n"
    "v_mfma_f32_16x16x32_bf16 a[252:255], a[168:171], v[168:171], a[252:255]\n"
    "v_exp_f32_e32 v203, v203\n"
    "v_mfma_f32_16x16x32_bf16 v[4:7], a[168:171], v[160:163], v[4:7]\n"
    "s_waitcnt lgkmcnt(0)\n"
    "ds_read_b64_tr_b16 a[168:169], v245 offset:4096\n"
    "v_mad_u32_u16 v253, v254, s42, v251 op_sel:[0,0,0,0]\n"
    "v_mfma_f32_16x16x32_bf16 v[4:7], a[172:175], v[164:167], v[4:7]\n"
    "ds_read_b64_tr_b16 a[170:171], v245 offset:6144\n"
    "v_cvt_pk_bf16_f32 v200, v200, v201\n"
    "v_mfma_f32_16x16x32_bf16 v[8:11], a[176:179], v[160:163], v[8:11]\n"
    "ds_read_b64_tr_b16 a[172:173], v245 offset:12288\n"
    "buffer_load_dwordx4 a[240:243], v253, s[20:23], 0 offen offset:0 sc1\n"
    "v_mfma_f32_16x16x32_bf16 v[8:11], a[180:183], v[164:167], v[8:11]\n"
    "ds_read_b64_tr_b16 a[174:175], v245 offset:14336\n"
    "v_cvt_pk_bf16_f32 v201, v202, v203\n"
    "v_mfma_f32_16x16x32_bf16 v[32:35], a[180:183], v[172:175], v[32:35]\n"
    "v_exp_f32_e32 v236, v236\n"
    "v_mfma_f32_16x16x32_bf16 v[32:35], a[176:179], v[168:171], v[32:35]\n"
    "v_mad_u32_u16 v253, v254, s42, v251 op_sel:[1,0,0,0]\n"
    "v_mfma_f32_16x16x32_bf16 v[80:83], a[176:179], v[176:179], v[80:83]\n"
    "buffer_load_dwordx4 a[244:247], v253, s[20:23], 0 offen offset:0 sc1\n"
    "v_mfma_f32_16x16x32_bf16 v[80:83], a[180:183], v[180:183], v[80:83]\n"
    "v_exp_f32_e32 v237, v237\n"
    "v_mfma_f32_16x16x32_bf16 v[84:87], a[180:183], v[188:191], v[84:87]\n"
    "v_exp_f32_e32 v238, v238\n"
    "v_mfma_f32_16x16x32_bf16 v[84:87], a[176:179], v[184:187], v[84:87]\n"
    "v_exp_f32_e32 v239, v239\n"
    "v_mfma_f32_16x16x32_bf16 v[88:91], a[176:179], v[192:195], v[88:91]\n"
    "ds_read_b64_tr_b16 a[176:177], v246 offset:4096\n"
    "v_cvt_pk_bf16_f32 v234, v236, v237\n"
    "v_mfma_f32_16x16x32_bf16 v[88:91], a[180:183], v[196:199], v[88:91]\n"
    "ds_read_b64_tr_b16 a[178:179], v246 offset:6144\n"
    "v_exp_f32_e32 v228, v228\n"
    "v_mfma_f32_16x16x32_bf16 v[100:103], a[184:187], v[192:195], v[100:103]\n"
    "ds_read_b64_tr_b16 a[180:181], v246 offset:12288\n"
    "v_exp_f32_e32 v229, v229\n"
    "v_mfma_f32_16x16x32_bf16 v[100:103], a[188:191], v[196:199], v[100:103]\n"
    "ds_read_b64_tr_b16 a[182:183], v246 offset:14336\n"
    "v_cvt_pk_bf16_f32 v235, v238, v239\n"
    "v_mfma_f32_16x16x32_bf16 v[96:99], a[188:191], v[188:191], v[96:99]\n"
    "v_exp_f32_e32 v230, v230\n"
    "v_mfma_f32_16x16x32_bf16 v[96:99], a[184:187], v[184:187], v[96:99]\n"
    "v_exp_f32_e32 v231, v231\n"
    "v_cvt_pk_bf16_f32 v226, v228, v229\n"
    "v_mfma_f32_16x16x32_bf16 v[92:95], a[184:187], v[176:179], v[92:95]\n"
    "v_exp_f32_e32 v220, v220\n"
    "v_mfma_f32_16x16x32_bf16 v[92:95], a[188:191], v[180:183], v[92:95]\n"
    "v_exp_f32_e32 v221, v221\n"
    "v_mfma_f32_16x16x32_bf16 v[36:39], a[188:191], v[172:175], v[36:39]\n"
    "v_cvt_pk_bf16_f32 v227, v230, v231\n"
    "v_mfma_f32_16x16x32_bf16 v[36:39], a[184:187], v[168:171], v[36:39]\n"
    "v_exp_f32_e32 v222, v222\n"
    "v_mfma_f32_16x16x32_bf16 v[12:15], a[184:187], v[160:163], v[12:15]\n"
    "s_mov_b32 m0, s49\n"
    "ds_read_b64_tr_b16 a[184:185], v247 offset:4096\n"
    "v_mfma_f32_16x16x32_bf16 v[12:15], a[188:191], v[164:167], v[12:15]\n"
    "s_waitcnt lgkmcnt(5)\n"
    "ds_read_b64_tr_b16 a[186:187], v247 offset:6144\n"
    "v_mfma_f32_16x16x32_bf16 v[16:19], a[160:163], v[160:163], v[16:19]\n"
    "ds_read_b64_tr_b16 a[188:189], v247 offset:12288\n"
    "v_mfma_f32_16x16x32_bf16 v[16:19], a[164:167], v[164:167], v[16:19]\n"
    "ds_read_b64_tr_b16 a[190:191], v247 offset:14336\n"
    "v_mfma_f32_16x16x32_bf16 v[40:43], a[164:167], v[172:175], v[40:43]\n"
    "v_exp_f32_e32 v223, v223\n"
    "v_mfma_f32_16x16x32_bf16 v[40:43], a[160:163], v[168:171], v[40:43]\n"
    "v_cvt_pk_bf16_f32 v218, v220, v221\n"
    "v_mfma_f32_16x16x32_bf16 v[104:107], a[160:163], v[176:179], v[104:107]\n"
    "v_exp_f32_e32 v212, v212\n"
    "v_mfma_f32_16x16x32_bf16 v[104:107], a[164:167], v[180:183], v[104:107]\n"
    "s_waitcnt vmcnt(20)\n"
    "v_mfma_f32_16x16x32_bf16 v[108:111], a[164:167], v[188:191], v[108:111]\n"
    "s_barrier\n"
    "v_mfma_f32_16x16x32_bf16 v[108:111], a[160:163], v[184:187], v[108:111]\n"
    "v_exp_f32_e32 v213, v213\n"
    "v_mfma_f32_16x16x32_bf16 v[112:115], a[160:163], v[192:195], v[112:115]\n"
    "ds_read_b128 a[160:163], v243 offset:36864\n"
    "v_cvt_pk_bf16_f32 v219, v222, v223\n"
    "v_mfma_f32_16x16x32_bf16 v[112:115], a[164:167], v[196:199], v[112:115]\n"
    "ds_read_b128 a[164:167], v240 offset:32768\n"
    "ds_write_addtid_b32 v232 offset:16384\n"
    "v_mfma_f32_16x16x32_bf16 v[124:127], a[168:171], v[192:195], v[124:127]\n"
    "v_exp_f32_e32 v214, v214\n"
    "v_mfma_f32_16x16x32_bf16 v[124:127], a[172:175], v[196:199], v[124:127]\n"
    "v_exp_f32_e32 v215, v215\n"
    "v_mfma_f32_16x16x32_bf16 v[120:123], a[172:175], v[188:191], v[120:123]\n"
    "ds_write_addtid_b32 v233 offset:16640\n"
    "v_cvt_pk_bf16_f32 v210, v212, v213\n"
    "v_mfma_f32_16x16x32_bf16 v[120:123], a[168:171], v[184:187], v[120:123]\n"
    "v_exp_f32_e32 v204, v204\n"
    "v_mfma_f32_16x16x32_bf16 v[116:119], a[168:171], v[176:179], v[116:119]\n"
    "v_exp_f32_e32 v205, v205\n"
    "v_mfma_f32_16x16x32_bf16 v[116:119], a[172:175], v[180:183], v[116:119]\n"
    "ds_write_addtid_b32 v224 offset:12288\n"
    "v_cvt_pk_bf16_f32 v211, v214, v215\n"
    "v_mfma_f32_16x16x32_bf16 v[44:47], a[172:175], v[172:175], v[44:47]\n"
    "v_exp_f32_e32 v206, v206\n"
    "v_mfma_f32_16x16x32_bf16 v[44:47], a[168:171], v[168:171], v[44:47]\n"
    "v_exp_f32_e32 v207, v207\n"
    "v_mfma_f32_16x16x32_bf16 v[20:23], a[168:171], v[160:163], v[20:23]\n"
    "ds_read_b128 a[168:171], v241 offset:32768\n"
    "v_cvt_pk_bf16_f32 v202, v204, v205\n"
    "v_mfma_f32_16x16x32_bf16 v[20:23], a[172:175], v[164:167], v[20:23]\n"
    "s_waitcnt lgkmcnt(6)\n"
    "ds_read_b128 a[172:175], v240 offset:36864\n"
    "v_cvt_pk_bf16_f32 v203, v206, v207\n"
    "v_mfma_f32_16x16x32_bf16 v[24:27], a[176:179], v[160:163], v[24:27]\n"
    "ds_write_addtid_b32 v225 offset:12544\n"
    "ds_write_addtid_b32 v216 offset:8192\n"
    "v_mfma_f32_16x16x32_bf16 v[24:27], a[180:183], v[164:167], v[24:27]\n"
    "ds_write_addtid_b32 v217 offset:8448\n"
    "v_mfma_f32_16x16x32_bf16 v[48:51], a[180:183], v[172:175], v[48:51]\n"
    "ds_write_addtid_b32 v208 offset:4096\n"
    "v_mfma_f32_16x16x32_bf16 v[48:51], a[176:179], v[168:171], v[48:51]\n"
    "ds_write_addtid_b32 v209 offset:4352\n"
    "v_mfma_f32_16x16x32_bf16 v[128:131], a[176:179], v[176:179], v[128:131]\n"
    "ds_write_addtid_b32 v200 offset:0\n"
    "v_mfma_f32_16x16x32_bf16 v[128:131], a[180:183], v[180:183], v[128:131]\n"
    "ds_write_addtid_b32 v201 offset:256\n"
    "v_mfma_f32_16x16x32_bf16 v[132:135], a[180:183], v[188:191], v[132:135]\n"
    "ds_write_addtid_b32 v234 offset:16896\n"
    "v_mfma_f32_16x16x32_bf16 v[132:135], a[176:179], v[184:187], v[132:135]\n"
    "ds_write_addtid_b32 v235 offset:17152\n"
    "v_mfma_f32_16x16x32_bf16 v[136:139], a[176:179], v[192:195], v[136:139]\n"
    "ds_read_b128 a[176:179], v241 offset:36864\n"
    "v_mfma_f32_16x16x32_bf16 v[136:139], a[180:183], v[196:199], v[136:139]\n"
    "ds_read_b128 a[180:183], v242 offset:32768\n"
    "v_mfma_f32_16x16x32_bf16 v[148:151], a[184:187], v[192:195], v[148:151]\n"
    "ds_write_addtid_b32 v226 offset:12800\n"
    "v_mfma_f32_16x16x32_bf16 v[148:151], a[188:191], v[196:199], v[148:151]\n"
    "ds_write_addtid_b32 v227 offset:13056\n"
    "v_mfma_f32_16x16x32_bf16 v[144:147], a[188:191], v[188:191], v[144:147]\n"
    "ds_write_addtid_b32 v218 offset:8704\n"
    "v_mfma_f32_16x16x32_bf16 v[144:147], a[184:187], v[184:187], v[144:147]\n"
    "ds_write_addtid_b32 v219 offset:8960\n"
    "v_mfma_f32_16x16x32_bf16 v[140:143], a[184:187], v[176:179], v[140:143]\n"
    "ds_write_addtid_b32 v210 offset:4608\n"
    "v_mfma_f32_16x16x32_bf16 v[140:143], a[188:191], v[180:183], v[140:143]\n"
    "ds_write_addtid_b32 v211 offset:4864\n"
    "v_mfma_f32_16x16x32_bf16 v[52:55], a[188:191], v[172:175], v[52:55]\n"
    "ds_write_addtid_b32 v202 offset:512\n"
    "v_mfma_f32_16x16x32_bf16 v[52:55], a[184:187], v[168:171], v[52:55]\n"
    "ds_write_addtid_b32 v203 offset:768\n"
    "v_mfma_f32_16x16x32_bf16 v[28:31], a[184:187], v[160:163], v[28:31]\n"
    "ds_read_b128 a[184:187], v243 offset:32768\n"
    "v_mfma_f32_16x16x32_bf16 v[28:31], a[188:191], v[164:167], v[28:31]\n"
    "s_waitcnt lgkmcnt(15)\n"
    "ds_read_b128 a[188:191], v242 offset:36864\n"
    "v_mfma_f32_16x16x32_bf16 v[160:163], a[160:163], a[28:31], 0\n"
    "v_mfma_f32_16x16x32_bf16 v[160:163], a[164:167], a[0:3], v[160:163]\n"
    "v_mfma_f32_16x16x32_bf16 v[168:171], a[164:167], a[32:35], 0\n"
    "v_mfma_f32_16x16x32_bf16 v[168:171], a[160:163], a[60:63], v[168:171]\n"
    "v_mfma_f32_16x16x32_bf16 v[176:179], a[160:163], a[92:95], 0\n"
    "v_mfma_f32_16x16x32_bf16 v[176:179], a[164:167], a[64:67], v[176:179]\n"
    "s_waitcnt lgkmcnt(0)\n"
    "v_mfma_f32_16x16x32_bf16 v[184:187], a[164:167], a[96:99], 0\n"
    "s_barrier\n"
    "v_mfma_f32_16x16x32_bf16 v[184:187], a[160:163], a[124:127], v[184:187]\n"
    "s_add_u32 m0, s43, 0\n"
    "v_mfma_f32_16x16x32_bf16 v[192:195], a[160:163], a[156:159], 0\n"
    "ds_read_b128 a[160:163], v243 offset:38912\n"
    "v_mfma_f32_16x16x32_bf16 v[192:195], a[164:167], a[128:131], v[192:195]\n"
    "ds_read_b128 a[164:167], v240 offset:34816\n"
    "v_mfma_f32_16x16x32_bf16 v[192:195], a[168:171], a[132:135], v[192:195]\n"
    "buffer_load_dwordx4 v248, s[8:11], s40 offen offset:0 sc1 lds\n"
    "v_mfma_f32_16x16x32_bf16 v[192:195], a[172:175], a[136:139], v[192:195]\n"
    "ds_read2st64_b32 v[204:205], v250 offset0:0 offset1:1\n"
    "v_mfma_f32_16x16x32_bf16 v[184:187], a[172:175], a[104:107], v[184:187]\n"
    "s_waitcnt vmcnt(11)\n"
    "v_smfmac_f32_16x16x64_bf16 v[156:159], a[192:195], a[200:207], v152\n"
    "v_mfma_f32_16x16x32_bf16 v[184:187], a[168:171], a[100:103], v[184:187]\n"
    "ds_read2st64_b32 v[206:207], v250 offset0:2 offset1:3\n"
    "v_mfma_f32_16x16x32_bf16 v[176:179], a[168:171], a[68:71], v[176:179]\n"
    "buffer_load_dwordx4 v248, s[8:11], s40 offen offset:1024 sc1 lds\n"
    "v_mfma_f32_16x16x32_bf16 v[176:179], a[172:175], a[72:75], v[176:179]\n"
    "v_mfma_f32_16x16x32_bf16 v[168:171], a[172:175], a[40:43], v[168:171]\n"
    "s_waitcnt vmcnt(10)\n"
    "v_smfmac_f32_16x16x64_bf16 v[156:159], a[192:195], a[208:215], v152\n"
    "ds_read2st64_b32 v[212:213], v250 offset0:16 offset1:17\n"
    "v_mfma_f32_16x16x32_bf16 v[168:171], a[168:171], a[36:39], v[168:171]\n"
    "v_mfma_f32_16x16x32_bf16 v[160:163], a[168:171], a[4:7], v[160:163]\n"
    "ds_read_b128 a[168:171], v241 offset:34816\n"
    "buffer_load_dwordx4 v248, s[8:11], s40 offen offset:2048 sc1 lds\n"
    "v_mfma_f32_16x16x32_bf16 v[160:163], a[172:175], a[8:11], v[160:163]\n"
    "ds_read_b128 a[172:175], v240 offset:38912\n"
    "v_mfma_f32_16x16x32_bf16 v[160:163], a[176:179], a[12:15], v[160:163]\n"
    "s_waitcnt vmcnt(9)\n"
    "ds_read2st64_b32 v[214:215], v250 offset0:18 offset1:19\n"
    "v_smfmac_f32_16x16x64_bf16 v[156:159], a[192:195], a[216:223], v152\n"
    "v_mfma_f32_16x16x32_bf16 v[160:163], a[180:183], a[16:19], v[160:163]\n"
    "v_mfma_f32_16x16x32_bf16 v[168:171], a[180:183], a[48:51], v[168:171]\n"
    "buffer_load_dwordx4 v248, s[8:11], s40 offen offset:3072 sc1 lds\n"
    "v_mfma_f32_16x16x32_bf16 v[168:171], a[176:179], a[44:47], v[168:171]\n"
    "s_add_u32 m0, s43, 4096\n"
    "v_mfma_f32_16x16x32_bf16 v[176:179], a[176:179], a[76:79], v[176:179]\n"
    "s_add_u32 s40, s40, 0x8000\n"
    "ds_read2st64_b32 v[220:221], v250 offset0:32 offset1:33\n"
    "v_mfma_f32_16x16x32_bf16 v[176:179], a[180:183], a[80:83], v[176:179]\n"
    "v_mfma_f32_16x16x32_bf16 v[184:187], a[180:183], a[112:115], v[184:187]\n"
    "v_mfma_f32_16x16x32_bf16 v[184:187], a[176:179], a[108:111], v[184:187]\n"
    "buffer_load_dwordx4 v248, s[8:11], s41 offen offset:0 sc1 lds\n"
    "v_mfma_f32_16x16x32_bf16 v[192:195], a[176:179], a[140:143], v[192:195]\n"
    "ds_read_b128 a[176:179], v241 offset:38912\n"
    "v_mfma_f32_16x16x32_bf16 v[192:195], a[180:183], a[144:147], v[192:195]\n"
    "ds_read_b128 a[180:183], v242 offset:34816\n"
    "v_mfma_f32_16x16x32_bf16 v[192:195], a[184:187], a[148:151], v[192:195]\n"
    "ds_read2st64_b32 v[222:223], v250 offset0:34 offset1:35\n"
    "v_mfma_f32_16x16x32_bf16 v[192:195], a[188:191], a[152:155], v[192:195]\n"
    "v_mfma_f32_16x16x32_bf16 v[184:187], a[188:191], a[120:123], v[184:187]\n"
    "buffer_load_dwordx4 v248, s[8:11], s41 offen offset:1024 sc1 lds\n"
    "v_mfma_f32_16x16x32_bf16 v[184:187], a[184:187], a[116:119], v[184:187]\n"
    "ds_read2st64_b32 v[228:229], v250 offset0:48 offset1:49\n"
    "v_mfma_f32_16x16x32_bf16 v[176:179], a[184:187], a[84:87], v[176:179]\n"
    "s_waitcnt vmcnt(10)\n"
    "v_exp_f32_e32 v192, v192\n"
    "v_smfmac_f32_16x16x64_bf16 v[156:159], a[192:195], a[224:231], v152\n"
    "v_exp_f32_e32 v193, v193\n"
    "v_mfma_f32_16x16x32_bf16 v[176:179], a[188:191], a[88:91], v[176:179]\n"
    "v_exp_f32_e32 v194, v194\n"
    "v_mfma_f32_16x16x32_bf16 v[168:171], a[188:191], a[56:59], v[168:171]\n"
    "buffer_load_dwordx4 v248, s[8:11], s41 offen offset:2048 sc1 lds\n"
    "v_mfma_f32_16x16x32_bf16 v[168:171], a[184:187], a[52:55], v[168:171]\n"
    "ds_read2st64_b32 v[230:231], v250 offset0:50 offset1:51\n"
    "v_mfma_f32_16x16x32_bf16 v[160:163], a[184:187], a[20:23], v[160:163]\n"
    "s_waitcnt vmcnt(9)\n"
    "ds_read_b128 a[184:187], v243 offset:34816\n"
    "v_smfmac_f32_16x16x64_bf16 v[156:159], a[192:195], a[232:239], v152\n"
    "ds_read2st64_b32 v[236:237], v250 offset0:64 offset1:65\n"
    "v_mfma_f32_16x16x32_bf16 v[160:163], a[188:191], a[24:27], v[160:163]\n"
    "s_waitcnt lgkmcnt(9)\n"
    "ds_read_b128 a[188:191], v242 offset:38912\n"
    "v_mfma_f32_16x16x32_bf16 v[164:167], a[160:163], a[28:31], 0\n"
    "buffer_load_dwordx4 v248, s[8:11], s41 offen offset:3072 sc1 lds\n"
    "v_mfma_f32_16x16x32_bf16 v[164:167], a[164:167], a[0:3], v[164:167]\n"
    "s_add_u32 s41, s41, 0x8000\n"
    "v_exp_f32_e32 v195, v195\n"
    "v_mfma_f32_16x16x32_bf16 v[172:175], a[164:167], a[32:35], 0\n"
    "s_waitcnt vmcnt(8)\n"
    "v_cvt_pk_bf16_f32 v192, v192, v193\n"
    "v_smfmac_f32_16x16x64_bf16 v[156:159], a[192:195], a[240:247], v152\n"
    "v_exp_f32_e32 v184, v184\n"
    "v_mfma_f32_16x16x32_bf16 v[172:175], a[160:163], a[60:63], v[172:175]\n"
    "ds_read2st64_b32 v[238:239], v250 offset0:66 offset1:67\n"
    "v_mfma_f32_16x16x32_bf16 v[180:183], a[160:163], a[92:95], 0\n"
    "v_exp_f32_e32 v185, v185\n"
    "v_mfma_f32_16x16x32_bf16 v[180:183], a[164:167], a[64:67], v[180:183]\n"
    "v_cvt_pk_bf16_f32 v193, v194, v195\n"
    "v_mfma_f32_16x16x32_bf16 v[188:191], a[164:167], a[96:99], 0\n"
    "v_exp_f32_e32 v186, v186\n"
    "v_mfma_f32_16x16x32_bf16 v[188:191], a[160:163], a[124:127], v[188:191]\n"
    "v_exp_f32_e32 v187, v187\n"
    "v_mfma_f32_16x16x32_bf16 v[196:199], a[160:163], a[156:159], 0\n"
    "ds_read_b64_tr_b16 a[160:161], v244 offset:16384\n"
    "v_and_b32_e32 v253, 0xffff0000, v255\n"
    "v_mfma_f32_16x16x32_bf16 v[196:199], a[164:167], a[128:131], v[196:199]\n"
    "ds_read_b64_tr_b16 a[162:163], v244 offset:18432\n"
    "v_mfma_f32_16x16x32_bf16 v[196:199], a[168:171], a[132:135], v[196:199]\n"
    "ds_read_b64_tr_b16 a[164:165], v244 offset:24576\n"
    "v_mfma_f32_16x16x32_bf16 v[196:199], a[172:175], a[136:139], v[196:199]\n"
    "ds_read_b64_tr_b16 a[166:167], v244 offset:26624\n"
    "v_mfma_f32_16x16x32_bf16 v[188:191], a[172:175], a[104:107], v[188:191]\n"
    "v_cvt_pk_bf16_f32 v184, v184, v185\n"
    "v_mfma_f32_16x16x32_bf16 v[188:191], a[168:171], a[100:103], v[188:191]\n"
    "v_exp_f32_e32 v176, v176\n"
    "v_mfma_f32_16x16x32_bf16 v[180:183], a[168:171], a[68:71], v[180:183]\n"
    "v_exp_f32_e32 v177, v177\n"
    "v_mfma_f32_16x16x32_bf16 v[180:183], a[172:175], a[72:75], v[180:183]\n"
    "v_cvt_pk_bf16_f32 v185, v186, v187\n"
    "v_mul_f32_e32 v156, v253, v156\n"
    "v_mfma_f32_16x16x32_bf16 v[172:175], a[172:175], a[40:43], v[172:175]\n"
    "v_exp_f32_e32 v178, v178\n"
    "v_mfma_f32_16x16x32_bf16 v[172:175], a[168:171], a[36:39], v[172:175]\n"
    "v_exp_f32_e32 v179, v179\n"
    "v_mfma_f32_16x16x32_bf16 v[164:167], a[168:171], a[4:7], v[164:167]\n"
    "ds_read_b64_tr_b16 a[168:169], v245 offset:16384\n"
    "v_mfma_f32_16x16x32_bf16 v[164:167], a[172:175], a[8:11], v[164:167]\n"
    "s_waitcnt lgkmcnt(6)\n"
    "ds_read_b64_tr_b16 a[170:171], v245 offset:18432\n"
    "v_mfma_f32_16x16x32_bf16 v[164:167], a[176:179], a[12:15], v[164:167]\n"
    "ds_read_b64_tr_b16 a[172:173], v245 offset:24576\n"
    "v_mfma_f32_16x16x32_bf16 v[164:167], a[180:183], a[16:19], v[164:167]\n"
    "ds_read_b64_tr_b16 a[174:175], v245 offset:26624\n"
    "v_mfma_f32_16x16x32_bf16 v[172:175], a[180:183], a[48:51], v[172:175]\n"
    "v_cvt_pk_bf16_f32 v176, v176, v177\n"
    "v_mfma_f32_16x16x32_bf16 v[172:175], a[176:179], a[44:47], v[172:175]\n"
    "v_exp_f32_e32 v168, v168\n"
    "v_mfma_f32_16x16x32_bf16 v[180:183], a[176:179], a[76:79], v[180:183]\n"
    "v_mul_f32_e32 v157, v253, v157\n"
    "v_mfma_f32_16x16x32_bf16 v[180:183], a[180:183], a[80:83], v[180:183]\n"
    "v_exp_f32_e32 v169, v169\n"
    "v_mfma_f32_16x16x32_bf16 v[188:191], a[180:183], a[112:115], v[188:191]\n"
    "v_cvt_pk_bf16_f32 v177, v178, v179\n"
    "v_mfma_f32_16x16x32_bf16 v[188:191], a[176:179], a[108:111], v[188:191]\n"
    "v_exp_f32_e32 v170, v170\n"
    "v_mfma_f32_16x16x32_bf16 v[196:199], a[176:179], a[140:143], v[196:199]\n"
    "ds_read_b64_tr_b16 a[176:177], v246 offset:16384\n"
    "v_mul_f32_e32 v158, v253, v158\n"
    "v_mfma_f32_16x16x32_bf16 v[196:199], a[180:183], a[144:147], v[196:199]\n"
    "ds_read_b64_tr_b16 a[178:179], v246 offset:18432\n"
    "v_mfma_f32_16x16x32_bf16 v[196:199], a[184:187], a[148:151], v[196:199]\n"
    "ds_read_b64_tr_b16 a[180:181], v246 offset:24576\n"
    "v_mfma_f32_16x16x32_bf16 v[196:199], a[188:191], a[152:155], v[196:199]\n"
    "ds_read_b64_tr_b16 a[182:183], v246 offset:26624\n"
    "v_mfma_f32_16x16x32_bf16 v[188:191], a[188:191], a[120:123], v[188:191]\n"
    "v_exp_f32_e32 v171, v171\n"
    "v_mfma_f32_16x16x32_bf16 v[188:191], a[184:187], a[116:119], v[188:191]\n"
    "v_cvt_pk_bf16_f32 v168, v168, v169\n"
    "v_mul_f32_e32 v159, v253, v159\n"
    "v_mfma_f32_16x16x32_bf16 v[180:183], a[184:187], a[84:87], v[180:183]\n"
    "v_exp_f32_e32 v160, v160\n"
    "v_mfma_f32_16x16x32_bf16 v[180:183], a[188:191], a[88:91], v[180:183]\n"
    "v_exp_f32_e32 v161, v161\n"
    "v_mfma_f32_16x16x32_bf16 v[172:175], a[188:191], a[56:59], v[172:175]\n"
    "v_cvt_pk_bf16_f32 v169, v170, v171\n"
    "v_mfma_f32_16x16x32_bf16 v[172:175], a[184:187], a[52:55], v[172:175]\n"
    "v_exp_f32_e32 v162, v162\n"
    "v_mfma_f32_16x16x32_bf16 v[164:167], a[184:187], a[20:23], v[164:167]\n"
    "ds_read_b64_tr_b16 a[184:185], v247 offset:16384\n"
    "v_mfma_f32_16x16x32_bf16 v[164:167], a[188:191], a[24:27], v[164:167]\n"
    "s_waitcnt lgkmcnt(5)\n"
    "ds_read_b64_tr_b16 a[186:187], v247 offset:18432\n"
    "v_mfma_f32_16x16x32_bf16 v[0:3], a[160:163], v[200:203], v[0:3]\n"
    "ds_read_b64_tr_b16 a[188:189], v247 offset:24576\n"
    "v_mad_u32_u16 v253, v255, s42, v153 op_sel:[0,0,0,0]\n"
    "v_mfma_f32_16x16x32_bf16 v[0:3], a[164:167], v[204:207], v[0:3]\n"
    "ds_read_b64_tr_b16 a[190:191], v247 offset:26624\n"
    "v_mfma_f32_16x16x32_bf16 a[248:251], a[164:167], v[212:215], a[248:251]\n"
    "v_exp_f32_e32 v163, v163\n"
    "v_mfma_f32_16x16x32_bf16 a[248:251], a[160:163], v[208:211], a[248:251]\n"
    "v_cvt_pk_bf16_f32 v160, v160, v161\n"
    "v_mfma_f32_16x16x32_bf16 v[56:59], a[160:163], v[216:219], v[56:59]\n"
    "v_cvt_pk_bf16_f32 v154, v156, v157\n"
    "v_mfma_f32_16x16x32_bf16 v[56:59], a[164:167], v[220:223], v[56:59]\n"
    "v_cvt_pk_bf16_f32 v161, v162, v163\n"
    "v_mfma_f32_16x16x32_bf16 v[60:63], a[164:167], v[228:231], v[60:63]\n"
    "v_exp_f32_e32 v196, v196\n"
    "v_mfma_f32_16x16x32_bf16 v[60:63], a[160:163], v[224:227], v[60:63]\n"
    "v_exp_f32_e32 v197, v197\n"
    "v_mfma_f32_16x16x32_bf16 v[64:67], a[160:163], v[232:235], v[64:67]\n"
    "ds_read_b64_tr_b16 a[160:161], v244 offset:20480\n"
    "v_mfma_f32_16x16x32_bf16 v[64:67], a[164:167], v[236:239], v[64:67]\n"
    "ds_read_b64_tr_b16 a[162:163], v244 offset:22528\n"
    "v_mfma_f32_16x16x32_bf16 v[76:79], a[168:171], v[232:235], v[76:79]\n"
    "ds_read_b64_tr_b16 a[164:165], v244 offset:28672\n"
    "v_mfma_f32_16x16x32_bf16 v[76:79], a[172:175], v[236:239], v[76:79]\n"
    "ds_read_b64_tr_b16 a[166:167], v244 offset:30720\n"
    "v_mfma_f32_16x16x32_bf16 v[72:75], a[172:175], v[228:231], v[72:75]\n"
    "v_exp_f32_e32 v198, v198\n"
    "v_mfma_f32_16x16x32_bf16 v[72:75], a[168:171], v[224:227], v[72:75]\n"
    "v_exp_f32_e32 v199, v199\n"
    "v_mfma_f32_16x16x32_bf16 v[68:71], a[168:171], v[216:219], v[68:71]\n"
    "v_cvt_pk_bf16_f32 v194, v196, v197\n"
    "v_cvt_pk_bf16_f32 v155, v158, v159\n"
    "v_mfma_f32_16x16x32_bf16 v[68:71], a[172:175], v[220:223], v[68:71]\n"
    "v_exp_f32_e32 v188, v188\n"
    "v_mfma_f32_16x16x32_bf16 a[252:255], a[172:175], v[212:215], a[252:255]\n"
    "v_exp_f32_e32 v189, v189\n"
    "v_mfma_f32_16x16x32_bf16 a[252:255], a[168:171], v[208:211], a[252:255]\n"
    "v_cvt_pk_bf16_f32 v195, v198, v199\n"
    "v_mfma_f32_16x16x32_bf16 v[4:7], a[168:171], v[200:203], v[4:7]\n"
    "ds_read_b64_tr_b16 a[168:169], v245 offset:20480\n"
    "v_mfma_f32_16x16x32_bf16 v[4:7], a[172:175], v[204:207], v[4:7]\n"
    "s_waitcnt lgkmcnt(5)\n"
    "ds_read_b64_tr_b16 a[170:171], v245 offset:22528\n"
    "v_mfma_f32_16x16x32_bf16 v[8:11], a[176:179], v[200:203], v[8:11]\n"
    "ds_read_b64_tr_b16 a[172:173], v245 offset:28672\n"
    "v_mov_b32_e32 v156, 0\n"
    "v_mfma_f32_16x16x32_bf16 v[8:11], a[180:183], v[204:207], v[8:11]\n"
    "ds_read_b64_tr_b16 a[174:175], v245 offset:30720\n"
    "v_mfma_f32_16x16x32_bf16 v[32:35], a[180:183], v[212:215], v[32:35]\n"
    "v_exp_f32_e32 v190, v190\n"
    "v_mfma_f32_16x16x32_bf16 v[32:35], a[176:179], v[208:211], v[32:35]\n"
    "v_exp_f32_e32 v191, v191\n"
    "v_mfma_f32_16x16x32_bf16 v[80:83], a[176:179], v[216:219], v[80:83]\n"
    "v_mov_b32_e32 v157, 0\n"
    "v_mfma_f32_16x16x32_bf16 v[80:83], a[180:183], v[220:223], v[80:83]\n"
    "v_cvt_pk_bf16_f32 v186, v188, v189\n"
    "v_mfma_f32_16x16x32_bf16 v[84:87], a[180:183], v[228:231], v[84:87]\n"
    "v_exp_f32_e32 v180, v180\n"
    "v_mfma_f32_16x16x32_bf16 v[84:87], a[176:179], v[224:227], v[84:87]\n"
    "v_exp_f32_e32 v181, v181\n"
    "v_mfma_f32_16x16x32_bf16 v[88:91], a[176:179], v[232:235], v[88:91]\n"
    "ds_read_b64_tr_b16 a[176:177], v246 offset:20480\n"
    "v_mfma_f32_16x16x32_bf16 v[88:91], a[180:183], v[236:239], v[88:91]\n"
    "ds_read_b64_tr_b16 a[178:179], v246 offset:22528\n"
    "v_mfma_f32_16x16x32_bf16 v[100:103], a[184:187], v[232:235], v[100:103]\n"
    "ds_read_b64_tr_b16 a[180:181], v246 offset:28672\n"
    "v_mfma_f32_16x16x32_bf16 v[100:103], a[188:191], v[236:239], v[100:103]\n"
    "ds_read_b64_tr_b16 a[182:183], v246 offset:30720\n"
    "v_mfma_f32_16x16x32_bf16 v[96:99], a[188:191], v[228:231], v[96:99]\n"
    "v_cvt_pk_bf16_f32 v187, v190, v191\n"
    "v_mov_b32_e32 v158, 0\n"
    "v_mfma_f32_16x16x32_bf16 v[96:99], a[184:187], v[224:227], v[96:99]\n"
    "v_exp_f32_e32 v182, v182\n"
    "v_mfma_f32_16x16x32_bf16 v[92:95], a[184:187], v[216:219], v[92:95]\n"
    "v_exp_f32_e32 v183, v183\n"
    "v_mfma_f32_16x16x32_bf16 v[92:95], a[188:191], v[220:223], v[92:95]\n"
    "v_cvt_pk_bf16_f32 v178, v180, v181\n"
    "v_mfma_f32_16x16x32_bf16 v[36:39], a[188:191], v[212:215], v[36:39]\n"
    "v_exp_f32_e32 v172, v172\n"
    "v_mfma_f32_16x16x32_bf16 v[36:39], a[184:187], v[208:211], v[36:39]\n"
    "v_exp_f32_e32 v173, v173\n"
    "v_mfma_f32_16x16x32_bf16 v[12:15], a[184:187], v[200:203], v[12:15]\n"
    "s_mov_b32 m0, s49\n"
    "ds_read_b64_tr_b16 a[184:185], v247 offset:20480\n"
    "v_mfma_f32_16x16x32_bf16 v[12:15], a[188:191], v[204:207], v[12:15]\n"
    "s_waitcnt lgkmcnt(5)\n"
    "ds_read_b64_tr_b16 a[186:187], v247 offset:22528\n"
    "v_mov_b32_e32 v159, 0\n"
    "v_mfma_f32_16x16x32_bf16 v[16:19], a[160:163], v[200:203], v[16:19]\n"
    "ds_read_b64_tr_b16 a[188:189], v247 offset:28672\n"
    "v_mfma_f32_16x16x32_bf16 v[16:19], a[164:167], v[204:207], v[16:19]\n"
    "ds_read_b64_tr_b16 a[190:191], v247 offset:30720\n"
    "v_mfma_f32_16x16x32_bf16 v[40:43], a[164:167], v[212:215], v[40:43]\n"
    "buffer_store_dwordx2 v[154:155], v253, s[24:27], 0 offen offset:0 sc1\n"
    "v_cvt_pk_bf16_f32 v179, v182, v183\n"
    "v_mfma_f32_16x16x32_bf16 v[40:43], a[160:163], v[208:211], v[40:43]\n"
    "v_exp_f32_e32 v174, v174\n"
    "v_mfma_f32_16x16x32_bf16 v[104:107], a[160:163], v[216:219], v[104:107]\n"
    "v_exp_f32_e32 v175, v175\n"
    "v_mfma_f32_16x16x32_bf16 v[104:107], a[164:167], v[220:223], v[104:107]\n"
    "s_waitcnt vmcnt(9)\n"
    "v_mfma_f32_16x16x32_bf16 v[108:111], a[164:167], v[228:231], v[108:111]\n"
    "s_barrier\n"
    "v_mfma_f32_16x16x32_bf16 v[108:111], a[160:163], v[224:227], v[108:111]\n"
    "ds_write_addtid_b32 v192 offset:16384\n"
    "v_cvt_pk_bf16_f32 v170, v172, v173\n"
    "v_mfma_f32_16x16x32_bf16 v[112:115], a[160:163], v[232:235], v[112:115]\n"
    "ds_read_b128 a[160:163], v243 offset:53248\n"
    "v_mfma_f32_16x16x32_bf16 v[112:115], a[164:167], v[236:239], v[112:115]\n"
    "ds_read_b128 a[164:167], v240 offset:49152\n"
    "v_mfma_f32_16x16x32_bf16 v[124:127], a[168:171], v[232:235], v[124:127]\n"
    "v_exp_f32_e32 v164, v164\n"
    "v_mfma_f32_16x16x32_bf16 v[124:127], a[172:175], v[236:239], v[124:127]\n"
    "v_exp_f32_e32 v165, v165\n"
    "v_mfma_f32_16x16x32_bf16 v[120:123], a[172:175], v[228:231], v[120:123]\n"
    "ds_write_addtid_b32 v193 offset:16640\n"
    "v_cvt_pk_bf16_f32 v171, v174, v175\n"
    "v_mfma_f32_16x16x32_bf16 v[120:123], a[168:171], v[224:227], v[120:123]\n"
    "ds_write_addtid_b32 v184 offset:12288\n"
    "v_mfma_f32_16x16x32_bf16 v[116:119], a[168:171], v[216:219], v[116:119]\n"
    "v_exp_f32_e32 v166, v166\n"
    "v_mfma_f32_16x16x32_bf16 v[116:119], a[172:175], v[220:223], v[116:119]\n"
    "v_exp_f32_e32 v167, v167\n"
    "v_mfma_f32_16x16x32_bf16 v[44:47], a[172:175], v[212:215], v[44:47]\n"
    "ds_write_addtid_b32 v185 offset:12544\n"
    "v_cvt_pk_bf16_f32 v162, v164, v165\n"
    "v_mfma_f32_16x16x32_bf16 v[44:47], a[168:171], v[208:211], v[44:47]\n"
    "v_cvt_pk_bf16_f32 v163, v166, v167\n"
    "v_mfma_f32_16x16x32_bf16 v[20:23], a[168:171], v[200:203], v[20:23]\n"
    "ds_read_b128 a[168:171], v241 offset:49152\n"
    "v_mfma_f32_16x16x32_bf16 v[20:23], a[172:175], v[204:207], v[20:23]\n"
    "s_waitcnt lgkmcnt(7)\n"
    "ds_read_b128 a[172:175], v240 offset:53248\n"
    "v_mfma_f32_16x16x32_bf16 v[24:27], a[176:179], v[200:203], v[24:27]\n"
    "ds_write_addtid_b32 v176 offset:8192\n"
    "v_mfma_f32_16x16x32_bf16 v[24:27], a[180:183], v[204:207], v[24:27]\n"
    "ds_write_addtid_b32 v177 offset:8448\n"
    "v_mfma_f32_16x16x32_bf16 v[48:51], a[180:183], v[212:215], v[48:51]\n"
    "ds_write_addtid_b32 v168 offset:4096\n"
    "v_mfma_f32_16x16x32_bf16 v[48:51], a[176:179], v[208:211], v[48:51]\n"
    "ds_write_addtid_b32 v169 offset:4352\n"
    "v_mfma_f32_16x16x32_bf16 v[128:131], a[176:179], v[216:219], v[128:131]\n"
    "ds_write_addtid_b32 v160 offset:0\n"
    "v_mfma_f32_16x16x32_bf16 v[128:131], a[180:183], v[220:223], v[128:131]\n"
    "ds_write_addtid_b32 v161 offset:256\n"
    "v_mfma_f32_16x16x32_bf16 v[132:135], a[180:183], v[228:231], v[132:135]\n"
    "ds_write_addtid_b32 v194 offset:16896\n"
    "v_mfma_f32_16x16x32_bf16 v[132:135], a[176:179], v[224:227], v[132:135]\n"
    "ds_write_addtid_b32 v195 offset:17152\n"
    "v_mfma_f32_16x16x32_bf16 v[136:139], a[176:179], v[232:235], v[136:139]\n"
    "ds_read_b128 a[176:179], v241 offset:53248\n"
    "v_mfma_f32_16x16x32_bf16 v[136:139], a[180:183], v[236:239], v[136:139]\n"
    "ds_read_b128 a[180:183], v242 offset:49152\n"
    "v_mfma_f32_16x16x32_bf16 v[148:151], a[184:187], v[232:235], v[148:151]\n"
    "ds_write_addtid_b32 v186 offset:12800\n"
    "v_mfma_f32_16x16x32_bf16 v[148:151], a[188:191], v[236:239], v[148:151]\n"
    "ds_write_addtid_b32 v187 offset:13056\n"
    "v_mfma_f32_16x16x32_bf16 v[144:147], a[188:191], v[228:231], v[144:147]\n"
    "ds_write_addtid_b32 v178 offset:8704\n"
    "v_mfma_f32_16x16x32_bf16 v[144:147], a[184:187], v[224:227], v[144:147]\n"
    "ds_write_addtid_b32 v179 offset:8960\n"
    "v_mfma_f32_16x16x32_bf16 v[140:143], a[184:187], v[216:219], v[140:143]\n"
    "ds_write_addtid_b32 v170 offset:4608\n"
    "v_mfma_f32_16x16x32_bf16 v[140:143], a[188:191], v[220:223], v[140:143]\n"
    "ds_write_addtid_b32 v171 offset:4864\n"
    "v_mfma_f32_16x16x32_bf16 v[52:55], a[188:191], v[212:215], v[52:55]\n"
    "ds_write_addtid_b32 v162 offset:512\n"
    "v_mfma_f32_16x16x32_bf16 v[52:55], a[184:187], v[208:211], v[52:55]\n"
    "ds_write_addtid_b32 v163 offset:768\n"
    "v_mfma_f32_16x16x32_bf16 v[28:31], a[184:187], v[200:203], v[28:31]\n"
    "ds_read_b128 a[184:187], v243 offset:49152\n"
    "v_mfma_f32_16x16x32_bf16 v[28:31], a[188:191], v[204:207], v[28:31]\n"
    "s_waitcnt lgkmcnt(15)\n"
    "ds_read_b128 a[188:191], v242 offset:53248\n"
    "v_mfma_f32_16x16x32_bf16 v[200:203], a[160:163], a[28:31], 0\n"
    "v_mfma_f32_16x16x32_bf16 v[200:203], a[164:167], a[0:3], v[200:203]\n"
    "v_mfma_f32_16x16x32_bf16 v[208:211], a[164:167], a[32:35], 0\n"
    "v_mfma_f32_16x16x32_bf16 v[208:211], a[160:163], a[60:63], v[208:211]\n"
    "v_mfma_f32_16x16x32_bf16 v[216:219], a[160:163], a[92:95], 0\n"
    "v_mfma_f32_16x16x32_bf16 v[216:219], a[164:167], a[64:67], v[216:219]\n"
    "s_waitcnt lgkmcnt(0)\n"
    "v_mfma_f32_16x16x32_bf16 v[224:227], a[164:167], a[96:99], 0\n"
    "s_barrier\n"
    "v_mfma_f32_16x16x32_bf16 v[224:227], a[160:163], a[124:127], v[224:227]\n"
    "s_add_u32 m0, s43, 16384\n"
    "ds_read2st64_b32 v[164:165], v250 offset0:0 offset1:1\n"
    "v_mfma_f32_16x16x32_bf16 v[232:235], a[160:163], a[156:159], 0\n"
    "ds_read_b128 a[160:163], v243 offset:55296\n"
    "v_mfma_f32_16x16x32_bf16 v[232:235], a[164:167], a[128:131], v[232:235]\n"
    "ds_read_b128 a[164:167], v240 offset:51200\n"
    "v_mfma_f32_16x16x32_bf16 v[232:235], a[168:171], a[132:135], v[232:235]\n"
    "buffer_load_dwordx4 v248, s[8:11], s40 offen offset:0 sc1 lds\n"
    "v_mfma_f32_16x16x32_bf16 v[232:235], a[172:175], a[136:139], v[232:235]\n"
    "v_mfma_f32_16x16x32_bf16 v[224:227], a[172:175], a[104:107], v[224:227]\n"
    "v_mfma_f32_16x16x32_bf16 v[224:227], a[168:171], a[100:103], v[224:227]\n"
    "ds_read2st64_b32 v[166:167], v250 offset0:2 offset1:3\n"
    "v_mfma_f32_16x16x32_bf16 v[216:219], a[168:171], a[68:71], v[216:219]\n"
    "buffer_load_dwordx4 v248, s[8:11], s40 offen offset:1024 sc1 lds\n"
    "v_mfma_f32_16x16x32_bf16 v[216:219], a[172:175], a[72:75], v[216:219]\n"
    "v_mfma_f32_16x16x32_bf16 v[208:211], a[172:175], a[40:43], v[208:211]\n"
    "v_mfma_f32_16x16x32_bf16 v[208:211], a[168:171], a[36:39], v[208:211]\n"
    "ds_read2st64_b32 v[172:173], v250 offset0:16 offset1:17\n"
    "v_mfma_f32_16x16x32_bf16 v[200:203], a[168:171], a[4:7], v[200:203]\n"
    "ds_read_b128 a[168:171], v241 offset:51200\n"
    "buffer_load_dwordx4 v248, s[8:11], s40 offen offset:2048 sc1 lds\n"
    "v_mfma_f32_16x16x32_bf16 v[200:203], a[172:175], a[8:11], v[200:203]\n"
    "ds_read_b128 a[172:175], v240 offset:55296\n"
    "v_mfma_f32_16x16x32_bf16 v[200:203], a[176:179], a[12:15], v[200:203]\n"
    "ds_read2st64_b32 v[174:175], v250 offset0:18 offset1:19\n"
    "v_mfma_f32_16x16x32_bf16 v[200:203], a[180:183], a[16:19], v[200:203]\n"
    "v_mfma_f32_16x16x32_bf16 v[208:211], a[180:183], a[48:51], v[208:211]\n"
    "buffer_load_dwordx4 v248, s[8:11], s40 offen offset:3072 sc1 lds\n"
    "v_mfma_f32_16x16x32_bf16 v[208:211], a[176:179], a[44:47], v[208:211]\n"
    "s_add_u32 m0, s43, 20480\n"
    "v_mfma_f32_16x16x32_bf16 v[216:219], a[176:179], a[76:79], v[216:219]\n"
    "s_add_u32 s40, s40, 0x8000\n"
    "ds_read2st64_b32 v[180:181], v250 offset0:32 offset1:33\n"
    "v_mfma_f32_16x16x32_bf16 v[216:219], a[180:183], a[80:83], v[216:219]\n"
    "v_mfma_f32_16x16x32_bf16 v[224:227], a[180:183], a[112:115], v[224:227]\n"
    "buffer_load_dwordx4 v248, s[8:11], s41 offen offset:0 sc1 lds\n"
    "v_mfma_f32_16x16x32_bf16 v[224:227], a[176:179], a[108:111], v[224:227]\n"
    "ds_read2st64_b32 v[182:183], v250 offset0:34 offset1:35\n"
    "v_mfma_f32_16x16x32_bf16 v[232:235], a[176:179], a[140:143], v[232:235]\n"
    "ds_read_b128 a[176:179], v241 offset:55296\n"
    "v_mfma_f32_16x16x32_bf16 v[232:235], a[180:183], a[144:147], v[232:235]\n"
    "ds_read_b128 a[180:183], v242 offset:51200\n"
    "v_mfma_f32_16x16x32_bf16 v[232:235], a[184:187], a[148:151], v[232:235]\n"
    "buffer_load_dwordx4 v248, s[8:11], s41 offen offset:1024 sc1 lds\n"
    "v_mfma_f32_16x16x32_bf16 v[232:235], a[188:191], a[152:155], v[232:235]\n"
    "v_mfma_f32_16x16x32_bf16 v[224:227], a[188:191], a[120:123], v[224:227]\n"
    "ds_read2st64_b32 v[188:189], v250 offset0:48 offset1:49\n"
    "v_mfma_f32_16x16x32_bf16 v[224:227], a[184:187], a[116:119], v[224:227]\n"
    "v_mfma_f32_16x16x32_bf16 v[216:219], a[184:187], a[84:87], v[216:219]\n"
    "buffer_load_dwordx4 v248, s[8:11], s41 offen offset:2048 sc1 lds\n"
    "v_mfma_f32_16x16x32_bf16 v[216:219], a[188:191], a[88:91], v[216:219]\n"
    "ds_read2st64_b32 v[190:191], v250 offset0:50 offset1:51\n"
    "v_mfma_f32_16x16x32_bf16 v[208:211], a[188:191], a[56:59], v[208:211]\n"
    "ds_read_b64 v[254:255], v252 offset:64\n"
    "v_mfma_f32_16x16x32_bf16 v[208:211], a[184:187], a[52:55], v[208:211]\n"
    "ds_read2st64_b32 v[196:197], v250 offset0:64 offset1:65\n"
    "v_mfma_f32_16x16x32_bf16 v[200:203], a[184:187], a[20:23], v[200:203]\n"
    "ds_read_b128 a[184:187], v243 offset:51200\n"
    "buffer_load_dwordx4 v248, s[8:11], s41 offen offset:3072 sc1 lds\n"
    "v_mfma_f32_16x16x32_bf16 v[200:203], a[188:191], a[24:27], v[200:203]\n"
    "s_waitcnt lgkmcnt(15)\n"
    "s_add_u32 s41, s41, 0x8000\n"
    "ds_read_b128 a[188:191], v242 offset:55296\n"
    "v_mfma_f32_16x16x32_bf16 v[204:207], a[160:163], a[28:31], 0\n"
    "s_waitcnt lgkmcnt(3)\n"
    "v_mad_u32_u16 v253, v254, s42, v251 op_sel:[0,0,0,0]\n"
    "v_mfma_f32_16x16x32_bf16 v[204:207], a[164:167], a[0:3], v[204:207]\n"
    "buffer_load_dwordx4 a[200:203], v253, s[20:23], 0 offen offset:0 sc1\n"
    "v_mfma_f32_16x16x32_bf16 v[212:215], a[164:167], a[32:35], 0\n"
    "v_exp_f32_e32 v232, v232\n"
    "v_mfma_f32_16x16x32_bf16 v[212:215], a[160:163], a[60:63], v[212:215]\n"
    "ds_read2st64_b32 v[198:199], v250 offset0:66 offset1:67\n"
    "v_mfma_f32_16x16x32_bf16 v[220:223], a[160:163], a[92:95], 0\n"
    "v_mad_u32_u16 v253, v254, s42, v251 op_sel:[1,0,0,0]\n"
    "v_mfma_f32_16x16x32_bf16 v[220:223], a[164:167], a[64:67], v[220:223]\n"
    "buffer_load_dwordx4 a[204:207], v253, s[20:23], 0 offen offset:0 sc1\n"
    "v_mfma_f32_16x16x32_bf16 v[228:231], a[164:167], a[96:99], 0\n"
    "v_exp_f32_e32 v233, v233\n"
    "v_mfma_f32_16x16x32_bf16 v[228:231], a[160:163], a[124:127], v[228:231]\n"
    "v_exp_f32_e32 v234, v234\n"
    "v_mfma_f32_16x16x32_bf16 v[236:239], a[160:163], a[156:159], 0\n"
    "ds_read_b64_tr_b16 a[160:161], v244 offset:32768\n"
    "v_mad_u32_u16 v253, v255, s42, v251 op_sel:[0,0,0,0]\n"
    "v_mfma_f32_16x16x32_bf16 v[236:239], a[164:167], a[128:131], v[236:239]\n"
    "ds_read_b64_tr_b16 a[162:163], v244 offset:34816\n"
    "buffer_load_dwordx4 a[208:211], v253, s[20:23], 0 offen offset:0 sc1\n"
    "v_mfma_f32_16x16x32_bf16 v[236:239], a[168:171], a[132:135], v[236:239]\n"
    "ds_read_b64_tr_b16 a[164:165], v244 offset:40960\n"
    "v_mfma_f32_16x16x32_bf16 v[236:239], a[172:175], a[136:139], v[236:239]\n"
    "ds_read_b64_tr_b16 a[166:167], v244 offset:43008\n"
    "v_mfma_f32_16x16x32_bf16 v[228:231], a[172:175], a[104:107], v[228:231]\n"
    "v_mad_u32_u16 v253, v255, s42, v251 op_sel:[1,0,0,0]\n"
    "v_mfma_f32_16x16x32_bf16 v[228:231], a[168:171], a[100:103], v[228:231]\n"
    "buffer_load_dwordx4 a[212:215], v253, s[20:23], 0 offen offset:0 sc1\n"
    "ds_read_b64 v[254:255], v252 offset:72\n"
    "v_mfma_f32_16x16x32_bf16 v[220:223], a[168:171], a[68:71], v[220:223]\n"
    "v_exp_f32_e32 v235, v235\n"
    "v_mfma_f32_16x16x32_bf16 v[220:223], a[172:175], a[72:75], v[220:223]\n"
    "v_cvt_pk_bf16_f32 v232, v232, v233\n"
    "v_mfma_f32_16x16x32_bf16 v[212:215], a[172:175], a[40:43], v[212:215]\n"
    "v_exp_f32_e32 v224, v224\n"
    "v_mfma_f32_16x16x32_bf16 v[212:215], a[168:171], a[36:39], v[212:215]\n"
    "v_exp_f32_e32 v225, v225\n"
    "v_mfma_f32_16x16x32_bf16 v[204:207], a[168:171], a[4:7], v[204:207]\n"
    "ds_read_b64_tr_b16 a[168:169], v245 offset:32768\n"
    "v_cvt_pk_bf16_f32 v233, v234, v235\n"
    "v_mfma_f32_16x16x32_bf16 v[204:207], a[172:175], a[8:11], v[204:207]\n"
    "ds_read_b64_tr_b16 a[170:171], v245 offset:34816\n"
    "v_mfma_f32_16x16x32_bf16 v[204:207], a[176:179], a[12:15], v[204:207]\n"
    "s_waitcnt lgkmcnt(2)\n"
    "ds_read_b64_tr_b16 a[172:173], v245 offset:40960\n"
    "v_mad_u32_u16 v253, v254, s42, v251 op_sel:[0,0,0,0]\n"
    "v_mfma_f32_16x16x32_bf16 v[204:207], a[180:183], a[16:19], v[204:207]\n"
    "ds_read_b64_tr_b16 a[174:175], v245 offset:43008\n"
    "buffer_load_dwordx4 a[216:219], v253, s[20:23], 0 offen offset:0 sc1\n"
    "v_mfma_f32_16x16x32_bf16 v[212:215], a[180:183], a[48:51], v[212:215]\n"
    "v_exp_f32_e32 v226, v226\n"
    "v_mfma_f32_16x16x32_bf16 v[212:215], a[176:179], a[44:47], v[212:215]\n"
    "v_exp_f32_e32 v227, v227\n"
    "v_mfma_f32_16x16x32_bf16 v[220:223], a[176:179], a[76:79], v[220:223]\n"
    "v_mad_u32_u16 v253, v254, s42, v251 op_sel:[1,0,0,0]\n"
    "v_cvt_pk_bf16_f32 v224, v224, v225\n"
    "v_mfma_f32_16x16x32_bf16 v[220:223], a[180:183], a[80:83], v[220:223]\n"
    "buffer_load_dwordx4 a[220:223], v253, s[20:23], 0 offen offset:0 sc1\n"
    "v_mfma_f32_16x16x32_bf16 v[228:231], a[180:183], a[112:115], v[228:231]\n"
    "v_exp_f32_e32 v216, v216\n"
    "v_mfma_f32_16x16x32_bf16 v[228:231], a[176:179], a[108:111], v[228:231]\n"
    "v_exp_f32_e32 v217, v217\n"
    "v_mfma_f32_16x16x32_bf16 v[236:239], a[176:179], a[140:143], v[236:239]\n"
    "ds_read_b64_tr_b16 a[176:177], v246 offset:32768\n"
    "v_cvt_pk_bf16_f32 v225, v226, v227\n"
    "v_mfma_f32_16x16x32_bf16 v[236:239], a[180:183], a[144:147], v[236:239]\n"
    "ds_read_b64_tr_b16 a[178:179], v246 offset:34816\n"
    "v_mfma_f32_16x16x32_bf16 v[236:239], a[184:187], a[148:151], v[236:239]\n"
    "ds_read_b64_tr_b16 a[180:181], v246 offset:40960\n"
    "v_mfma_f32_16x16x32_bf16 v[236:239], a[188:191], a[152:155], v[236:239]\n"
    "ds_read_b64_tr_b16 a[182:183], v246 offset:43008\n"
    "v_mfma_f32_16x16x32_bf16 v[228:231], a[188:191], a[120:123], v[228:231]\n"
    "ds_read_b64 v[254:255], v252 offset:96\n"
    "v_mfma_f32_16x16x32_bf16 v[228:231], a[184:187], a[116:119], v[228:231]\n"
    "v_exp_f32_e32 v218, v218\n"
    "v_mfma_f32_16x16x32_bf16 v[220:223], a[184:187], a[84:87], v[220:223]\n"
    "v_exp_f32_e32 v219, v219\n"
    "v_mfma_f32_16x16x32_bf16 v[220:223], a[188:191], a[88:91], v[220:223]\n"
    "v_cvt_pk_bf16_f32 v216, v216, v217\n"
    "v_mfma_f32_16x16x32_bf16 v[212:215], a[188:191], a[56:59], v[212:215]\n"
    "s_waitcnt lgkmcnt(0)\n"
    "v_mad_u32_u16 v253, v254, s42, v251 op_sel:[0,0,0,0]\n"
    "v_mfma_f32_16x16x32_bf16 v[212:215], a[184:187], a[52:55], v[212:215]\n"
    "buffer_load_dwordx4 a[224:227], v253, s[20:23], 0 offen offset:0 sc1\n"
    "v_mfma_f32_16x16x32_bf16 v[204:207], a[184:187], a[20:23], v[204:207]\n"
    "ds_read_b64_tr_b16 a[184:185], v247 offset:32768\n"
    "v_mfma_f32_16x16x32_bf16 v[204:207], a[188:191], a[24:27], v[204:207]\n"
    "ds_read_b64_tr_b16 a[186:187], v247 offset:34816\n"
    "v_mfma_f32_16x16x32_bf16 v[0:3], a[160:163], v[160:163], v[0:3]\n"
    "ds_read_b64_tr_b16 a[188:189], v247 offset:40960\n"
    "v_mad_u32_u16 v253, v254, s42, v251 op_sel:[1,0,0,0]\n"
    "v_mfma_f32_16x16x32_bf16 v[0:3], a[164:167], v[164:167], v[0:3]\n"
    "ds_read_b64_tr_b16 a[190:191], v247 offset:43008\n"
    "buffer_load_dwordx4 a[228:231], v253, s[20:23], 0 offen offset:0 sc1\n"
    "v_mfma_f32_16x16x32_bf16 a[248:251], a[164:167], v[172:175], a[248:251]\n"
    "v_exp_f32_e32 v208, v208\n"
    "v_mfma_f32_16x16x32_bf16 a[248:251], a[160:163], v[168:171], a[248:251]\n"
    "v_exp_f32_e32 v209, v209\n"
    "v_mfma_f32_16x16x32_bf16 v[56:59], a[160:163], v[176:179], v[56:59]\n"
    "v_mad_u32_u16 v253, v255, s42, v251 op_sel:[0,0,0,0]\n"
    "v_cvt_pk_bf16_f32 v217, v218, v219\n"
    "v_mfma_f32_16x16x32_bf16 v[56:59], a[164:167], v[180:183], v[56:59]\n"
    "buffer_load_dwordx4 a[232:235], v253, s[20:23], 0 offen offset:0 sc1\n"
    "v_mfma_f32_16x16x32_bf16 v[60:63], a[164:167], v[188:191], v[60:63]\n"
    "v_exp_f32_e32 v210, v210\n"
    "v_mfma_f32_16x16x32_bf16 v[60:63], a[160:163], v[184:187], v[60:63]\n"
    "v_exp_f32_e32 v211, v211\n"
    "v_mfma_f32_16x16x32_bf16 v[64:67], a[160:163], v[192:195], v[64:67]\n"
    "ds_read_b64_tr_b16 a[160:161], v244 offset:36864\n"
    "v_mad_u32_u16 v253, v255, s42, v251 op_sel:[1,0,0,0]\n"
    "v_mfma_f32_16x16x32_bf16 v[64:67], a[164:167], v[196:199], v[64:67]\n"
    "ds_read_b64_tr_b16 a[162:163], v244 offset:38912\n"
    "buffer_load_dwordx4 a[236:239], v253, s[20:23], 0 offen offset:0 sc1\n"
    "v_mfma_f32_16x16x32_bf16 v[76:79], a[168:171], v[192:195], v[76:79]\n"
    "ds_read_b64_tr_b16 a[164:165], v244 offset:45056\n"
    "v_cvt_pk_bf16_f32 v208, v208, v209\n"
    "v_mfma_f32_16x16x32_bf16 v[76:79], a[172:175], v[196:199], v[76:79]\n"
    "ds_read_b64_tr_b16 a[166:167], v244 offset:47104\n"
    "v_mfma_f32_16x16x32_bf16 v[72:75], a[172:175], v[188:191], v[72:75]\n"
    "ds_read_b64 v[254:255], v252 offset:104\n"
    "v_mfma_f32_16x16x32_bf16 v[72:75], a[168:171], v[184:187], v[72:75]\n"
    "v_exp_f32_e32 v200, v200\n"
    "v_mfma_f32_16x16x32_bf16 v[68:71], a[168:171], v[176:179], v[68:71]\n"
    "v_exp_f32_e32 v201, v201\n"
    "v_mfma_f32_16x16x32_bf16 v[68:71], a[172:175], v[180:183], v[68:71]\n"
    "v_cvt_pk_bf16_f32 v209, v210, v211\n"
    "v_mfma_f32_16x16x32_bf16 a[252:255], a[172:175], v[172:175], a[252:255]\n"
    "v_exp_f32_e32 v202, v202\n"
    "v_mfma_f32_16x16x32_bf16 a[252:255], a[168:171], v[168:171], a[252:255]\n"
    "v_exp_f32_e32 v203, v203\n"
    "v_mfma_f32_16x16x32_bf16 v[4:7], a[168:171], v[160:163], v[4:7]\n"
    "s_waitcnt lgkmcnt(0)\n"
    "ds_read_b64_tr_b16 a[168:169], v245 offset:36864\n"
    "v_mad_u32_u16 v253, v254, s42, v251 op_sel:[0,0,0,0]\n"
    "v_mfma_f32_16x16x32_bf16 v[4:7], a[172:175], v[164:167], v[4:7]\n"
    "ds_read_b64_tr_b16 a[170:171], v245 offset:38912\n"
    "v_cvt_pk_bf16_f32 v200, v200, v201\n"
    "v_mfma_f32_16x16x32_bf16 v[8:11], a[176:179], v[160:163], v[8:11]\n"
    "ds_read_b64_tr_b16 a[172:173], v245 offset:45056\n"
    "buffer_load_dwordx4 a[240:243], v253, s[20:23], 0 offen offset:0 sc1\n"
    "v_mfma_f32_16x16x32_bf16 v[8:11], a[180:183], v[164:167], v[8:11]\n"
    "ds_read_b64_tr_b16 a[174:175], v245 offset:47104\n"
    "v_cvt_pk_bf16_f32 v201, v202, v203\n"
    "v_mfma_f32_16x16x32_bf16 v[32:35], a[180:183], v[172:175], v[32:35]\n"
    "v_exp_f32_e32 v236, v236\n"
    "v_mfma_f32_16x16x32_bf16 v[32:35], a[176:179], v[168:171], v[32:35]\n"
    "v_mad_u32_u16 v253, v254, s42, v251 op_sel:[1,0,0,0]\n"
    "v_mfma_f32_16x16x32_bf16 v[80:83], a[176:179], v[176:179], v[80:83]\n"
    "buffer_load_dwordx4 a[244:247], v253, s[20:23], 0 offen offset:0 sc1\n"
    "v_mfma_f32_16x16x32_bf16 v[80:83], a[180:183], v[180:183], v[80:83]\n"
    "v_exp_f32_e32 v237, v237\n"
    "v_mfma_f32_16x16x32_bf16 v[84:87], a[180:183], v[188:191], v[84:87]\n"
    "v_exp_f32_e32 v238, v238\n"
    "v_mfma_f32_16x16x32_bf16 v[84:87], a[176:179], v[184:187], v[84:87]\n"
    "v_exp_f32_e32 v239, v239\n"
    "v_mfma_f32_16x16x32_bf16 v[88:91], a[176:179], v[192:195], v[88:91]\n"
    "ds_read_b64_tr_b16 a[176:177], v246 offset:36864\n"
    "v_cvt_pk_bf16_f32 v234, v236, v237\n"
    "v_mfma_f32_16x16x32_bf16 v[88:91], a[180:183], v[196:199], v[88:91]\n"
    "ds_read_b64_tr_b16 a[178:179], v246 offset:38912\n"
    "v_exp_f32_e32 v228, v228\n"
    "v_mfma_f32_16x16x32_bf16 v[100:103], a[184:187], v[192:195], v[100:103]\n"
    "ds_read_b64_tr_b16 a[180:181], v246 offset:45056\n"
    "v_exp_f32_e32 v229, v229\n"
    "v_mfma_f32_16x16x32_bf16 v[100:103], a[188:191], v[196:199], v[100:103]\n"
    "ds_read_b64_tr_b16 a[182:183], v246 offset:47104\n"
    "v_cvt_pk_bf16_f32 v235, v238, v239\n"
    "v_mfma_f32_16x16x32_bf16 v[96:99], a[188:191], v[188:191], v[96:99]\n"
    "v_exp_f32_e32 v230, v230\n"
    "v_mfma_f32_16x16x32_bf16 v[96:99], a[184:187], v[184:187], v[96:99]\n"
    "v_exp_f32_e32 v231, v231\n"
    "v_cvt_pk_bf16_f32 v226, v228, v229\n"
    "v_mfma_f32_16x16x32_bf16 v[92:95], a[184:187], v[176:179], v[92:95]\n"
    "v_exp_f32_e32 v220, v220\n"
    "v_mfma_f32_16x16x32_bf16 v[92:95], a[188:191], v[180:183], v[92:95]\n"
    "v_exp_f32_e32 v221, v221\n"
    "v_mfma_f32_16x16x32_bf16 v[36:39], a[188:191], v[172:175], v[36:39]\n"
    "v_cvt_pk_bf16_f32 v227, v230, v231\n"
    "v_mfma_f32_16x16x32_bf16 v[36:39], a[184:187], v[168:171], v[36:39]\n"
    "v_exp_f32_e32 v222, v222\n"
    "v_mfma_f32_16x16x32_bf16 v[12:15], a[184:187], v[160:163], v[12:15]\n"
    "s_mov_b32 m0, s49\n"
    "ds_read_b64_tr_b16 a[184:185], v247 offset:36864\n"
    "v_mfma_f32_16x16x32_bf16 v[12:15], a[188:191], v[164:167], v[12:15]\n"
    "s_waitcnt lgkmcnt(5)\n"
    "ds_read_b64_tr_b16 a[186:187], v247 offset:38912\n"
    "v_mfma_f32_16x16x32_bf16 v[16:19], a[160:163], v[160:163], v[16:19]\n"
    "ds_read_b64_tr_b16 a[188:189], v247 offset:45056\n"
    "v_mfma_f32_16x16x32_bf16 v[16:19], a[164:167], v[164:167], v[16:19]\n"
    "ds_read_b64_tr_b16 a[190:191], v247 offset:47104\n"
    "v_mfma_f32_16x16x32_bf16 v[40:43], a[164:167], v[172:175], v[40:43]\n"
    "v_exp_f32_e32 v223, v223\n"
    "v_mfma_f32_16x16x32_bf16 v[40:43], a[160:163], v[168:171], v[40:43]\n"
    "v_cvt_pk_bf16_f32 v218, v220, v221\n"
    "v_mfma_f32_16x16x32_bf16 v[104:107], a[160:163], v[176:179], v[104:107]\n"
    "v_exp_f32_e32 v212, v212\n"
    "v_mfma_f32_16x16x32_bf16 v[104:107], a[164:167], v[180:183], v[104:107]\n"
    "s_waitcnt vmcnt(20)\n"
    "v_mfma_f32_16x16x32_bf16 v[108:111], a[164:167], v[188:191], v[108:111]\n"
    "s_barrier\n"
    "v_mfma_f32_16x16x32_bf16 v[108:111], a[160:163], v[184:187], v[108:111]\n"
    "v_exp_f32_e32 v213, v213\n"
    "v_mfma_f32_16x16x32_bf16 v[112:115], a[160:163], v[192:195], v[112:115]\n"
    "ds_read_b128 a[160:163], v243 offset:4096\n"
    "v_cvt_pk_bf16_f32 v219, v222, v223\n"
    "v_mfma_f32_16x16x32_bf16 v[112:115], a[164:167], v[196:199], v[112:115]\n"
    "ds_read_b128 a[164:167], v240 offset:0\n"
    "ds_write_addtid_b32 v232 offset:16384\n"
    "v_mfma_f32_16x16x32_bf16 v[124:127], a[168:171], v[192:195], v[124:127]\n"
    "v_exp_f32_e32 v214, v214\n"
    "v_mfma_f32_16x16x32_bf16 v[124:127], a[172:175], v[196:199], v[124:127]\n"
    "v_exp_f32_e32 v215, v215\n"
    "v_mfma_f32_16x16x32_bf16 v[120:123], a[172:175], v[188:191], v[120:123]\n"
    "ds_write_addtid_b32 v233 offset:16640\n"
    "v_cvt_pk_bf16_f32 v210, v212, v213\n"
    "v_mfma_f32_16x16x32_bf16 v[120:123], a[168:171], v[184:187], v[120:123]\n"
    "v_exp_f32_e32 v204, v204\n"
    "v_mfma_f32_16x16x32_bf16 v[116:119], a[168:171], v[176:179], v[116:119]\n"
    "v_exp_f32_e32 v205, v205\n"
    "v_mfma_f32_16x16x32_bf16 v[116:119], a[172:175], v[180:183], v[116:119]\n"
    "ds_write_addtid_b32 v224 offset:12288\n"
    "v_cvt_pk_bf16_f32 v211, v214, v215\n"
    "v_mfma_f32_16x16x32_bf16 v[44:47], a[172:175], v[172:175], v[44:47]\n"
    "v_exp_f32_e32 v206, v206\n"
    "v_mfma_f32_16x16x32_bf16 v[44:47], a[168:171], v[168:171], v[44:47]\n"
    "v_exp_f32_e32 v207, v207\n"
    "v_mfma_f32_16x16x32_bf16 v[20:23], a[168:171], v[160:163], v[20:23]\n"
    "ds_read_b128 a[168:171], v241 offset:0\n"
    "v_cvt_pk_bf16_f32 v202, v204, v205\n"
    "v_mfma_f32_16x16x32_bf16 v[20:23], a[172:175], v[164:167], v[20:23]\n"
    "s_waitcnt lgkmcnt(6)\n"
    "ds_read_b128 a[172:175], v240 offset:4096\n"
    "v_cvt_pk_bf16_f32 v203, v206, v207\n"
    "v_mfma_f32_16x16x32_bf16 v[24:27], a[176:179], v[160:163], v[24:27]\n"
    "ds_write_addtid_b32 v225 offset:12544\n"
    "ds_write_addtid_b32 v216 offset:8192\n"
    "v_mfma_f32_16x16x32_bf16 v[24:27], a[180:183], v[164:167], v[24:27]\n"
    "ds_write_addtid_b32 v217 offset:8448\n"
    "v_mfma_f32_16x16x32_bf16 v[48:51], a[180:183], v[172:175], v[48:51]\n"
    "ds_write_addtid_b32 v208 offset:4096\n"
    "v_mfma_f32_16x16x32_bf16 v[48:51], a[176:179], v[168:171], v[48:51]\n"
    "ds_write_addtid_b32 v209 offset:4352\n"
    "v_mfma_f32_16x16x32_bf16 v[128:131], a[176:179], v[176:179], v[128:131]\n"
    "ds_write_addtid_b32 v200 offset:0\n"
    "v_mfma_f32_16x16x32_bf16 v[128:131], a[180:183], v[180:183], v[128:131]\n"
    "ds_write_addtid_b32 v201 offset:256\n"
    "v_mfma_f32_16x16x32_bf16 v[132:135], a[180:183], v[188:191], v[132:135]\n"
    "ds_write_addtid_b32 v234 offset:16896\n"
    "v_mfma_f32_16x16x32_bf16 v[132:135], a[176:179], v[184:187], v[132:135]\n"
    "ds_write_addtid_b32 v235 offset:17152\n"
    "v_mfma_f32_16x16x32_bf16 v[136:139], a[176:179], v[192:195], v[136:139]\n"
    "ds_read_b128 a[176:179], v241 offset:4096\n"
    "v_mfma_f32_16x16x32_bf16 v[136:139], a[180:183], v[196:199], v[136:139]\n"
    "ds_read_b128 a[180:183], v242 offset:0\n"
    "v_mfma_f32_16x16x32_bf16 v[148:151], a[184:187], v[192:195], v[148:151]\n"
    "ds_write_addtid_b32 v226 offset:12800\n"
    "v_mfma_f32_16x16x32_bf16 v[148:151], a[188:191], v[196:199], v[148:151]\n"
    "ds_write_addtid_b32 v227 offset:13056\n"
    "v_mfma_f32_16x16x32_bf16 v[144:147], a[188:191], v[188:191], v[144:147]\n"
    "ds_write_addtid_b32 v218 offset:8704\n"
    "v_mfma_f32_16x16x32_bf16 v[144:147], a[184:187], v[184:187], v[144:147]\n"
    "ds_write_addtid_b32 v219 offset:8960\n"
    "v_mfma_f32_16x16x32_bf16 v[140:143], a[184:187], v[176:179], v[140:143]\n"
    "ds_write_addtid_b32 v210 offset:4608\n"
    "v_mfma_f32_16x16x32_bf16 v[140:143], a[188:191], v[180:183], v[140:143]\n"
    "ds_write_addtid_b32 v211 offset:4864\n"
    "v_mfma_f32_16x16x32_bf16 v[52:55], a[188:191], v[172:175], v[52:55]\n"
    "ds_write_addtid_b32 v202 offset:512\n"
    "v_mfma_f32_16x16x32_bf16 v[52:55], a[184:187], v[168:171], v[52:55]\n"
    "ds_write_addtid_b32 v203 offset:768\n"
    "v_mfma_f32_16x16x32_bf16 v[28:31], a[184:187], v[160:163], v[28:31]\n"
    "ds_read_b128 a[184:187], v243 offset:0\n"
    "v_mfma_f32_16x16x32_bf16 v[28:31], a[188:191], v[164:167], v[28:31]\n"
    "s_waitcnt lgkmcnt(15)\n"
    "ds_read_b128 a[188:191], v242 offset:4096\n"
    "v_mfma_f32_16x16x32_bf16 v[160:163], a[160:163], a[28:31], 0\n"
    "v_mfma_f32_16x16x32_bf16 v[160:163], a[164:167], a[0:3], v[160:163]\n"
    "v_mfma_f32_16x16x32_bf16 v[168:171], a[164:167], a[32:35], 0\n"
    "v_mfma_f32_16x16x32_bf16 v[168:171], a[160:163], a[60:63], v[168:171]\n"
    "v_mfma_f32_16x16x32_bf16 v[176:179], a[160:163], a[92:95], 0\n"
    "v_mfma_f32_16x16x32_bf16 v[176:179], a[164:167], a[64:67], v[176:179]\n"
    "s_waitcnt lgkmcnt(0)\n"
    "v_mfma_f32_16x16x32_bf16 v[184:187], a[164:167], a[96:99], 0\n"
    "s_barrier\n"
    "v_mfma_f32_16x16x32_bf16 v[184:187], a[160:163], a[124:127], v[184:187]\n"
    "s_add_u32 m0, s43, 32768\n"
    "v_mfma_f32_16x16x32_bf16 v[192:195], a[160:163], a[156:159], 0\n"
    "ds_read_b128 a[160:163], v243 offset:6144\n"
    "v_mfma_f32_16x16x32_bf16 v[192:195], a[164:167], a[128:131], v[192:195]\n"
    "ds_read_b128 a[164:167], v240 offset:2048\n"
    "v_mfma_f32_16x16x32_bf16 v[192:195], a[168:171], a[132:135], v[192:195]\n"
    "buffer_load_dwordx4 v248, s[8:11], s40 offen offset:0 sc1 lds\n"
    "v_mfma_f32_16x16x32_bf16 v[192:195], a[172:175], a[136:139], v[192:195]\n"
    "ds_read2st64_b32 v[204:205], v250 offset0:0 offset1:1\n"
    "v_mfma_f32_16x16x32_bf16 v[184:187], a[172:175], a[104:107], v[184:187]\n"
    "s_waitcnt vmcnt(11)\n"
    "v_smfmac_f32_16x16x64_bf16 v[156:159], a[192:195], a[200:207], v152\n"
    "v_mfma_f32_16x16x32_bf16 v[184:187], a[168:171], a[100:103], v[184:187]\n"
    "ds_read2st64_b32 v[206:207], v250 offset0:2 offset1:3\n"
    "v_mfma_f32_16x16x32_bf16 v[176:179], a[168:171], a[68:71], v[176:179]\n"
    "buffer_load_dwordx4 v248, s[8:11], s40 offen offset:1024 sc1 lds\n"
    "v_mfma_f32_16x16x32_bf16 v[176:179], a[172:175], a[72:75], v[176:179]\n"
    "v_mfma_f32_16x16x32_bf16 v[168:171], a[172:175], a[40:43], v[168:171]\n"
    "s_waitcnt vmcnt(10)\n"
    "v_smfmac_f32_16x16x64_bf16 v[156:159], a[192:195], a[208:215], v152\n"
    "ds_read2st64_b32 v[212:213], v250 offset0:16 offset1:17\n"
    "v_mfma_f32_16x16x32_bf16 v[168:171], a[168:171], a[36:39], v[168:171]\n"
    "v_mfma_f32_16x16x32_bf16 v[160:163], a[168:171], a[4:7], v[160:163]\n"
    "ds_read_b128 a[168:171], v241 offset:2048\n"
    "buffer_load_dwordx4 v248, s[8:11], s40 offen offset:2048 sc1 lds\n"
    "v_mfma_f32_16x16x32_bf16 v[160:163], a[172:175], a[8:11], v[160:163]\n"
    "ds_read_b128 a[172:175], v240 offset:6144\n"
    "v_mfma_f32_16x16x32_bf16 v[160:163], a[176:179], a[12:15], v[160:163]\n"
    "s_waitcnt vmcnt(9)\n"
    "ds_read2st64_b32 v[214:215], v250 offset0:18 offset1:19\n"
    "v_smfmac_f32_16x16x64_bf16 v[156:159], a[192:195], a[216:223], v152\n"
    "v_mfma_f32_16x16x32_bf16 v[160:163], a[180:183], a[16:19], v[160:163]\n"
    "v_mfma_f32_16x16x32_bf16 v[168:171], a[180:183], a[48:51], v[168:171]\n"
    "buffer_load_dwordx4 v248, s[8:11], s40 offen offset:3072 sc1 lds\n"
    "v_mfma_f32_16x16x32_bf16 v[168:171], a[176:179], a[44:47], v[168:171]\n"
    "s_add_u32 m0, s43, 36864\n"
    "v_mfma_f32_16x16x32_bf16 v[176:179], a[176:179], a[76:79], v[176:179]\n"
    "s_add_u32 s40, s40, 0x8000\n"
    "ds_read2st64_b32 v[220:221], v250 offset0:32 offset1:33\n"
    "v_mfma_f32_16x16x32_bf16 v[176:179], a[180:183], a[80:83], v[176:179]\n"
    "v_mfma_f32_16x16x32_bf16 v[184:187], a[180:183], a[112:115], v[184:187]\n"
    "v_mfma_f32_16x16x32_bf16 v[184:187], a[176:179], a[108:111], v[184:187]\n"
    "buffer_load_dwordx4 v248, s[8:11], s41 offen offset:0 sc1 lds\n"
    "v_mfma_f32_16x16x32_bf16 v[192:195], a[176:179], a[140:143], v[192:195]\n"
    "ds_read_b128 a[176:179], v241 offset:6144\n"
    "v_mfma_f32_16x16x32_bf16 v[192:195], a[180:183], a[144:147], v[192:195]\n"
    "ds_read_b128 a[180:183], v242 offset:2048\n"
    "v_mfma_f32_16x16x32_bf16 v[192:195], a[184:187], a[148:151], v[192:195]\n"
    "ds_read2st64_b32 v[222:223], v250 offset0:34 offset1:35\n"
    "v_mfma_f32_16x16x32_bf16 v[192:195], a[188:191], a[152:155], v[192:195]\n"
    "v_mfma_f32_16x16x32_bf16 v[184:187], a[188:191], a[120:123], v[184:187]\n"
    "buffer_load_dwordx4 v248, s[8:11], s41 offen offset:1024 sc1 lds\n"
    "v_mfma_f32_16x16x32_bf16 v[184:187], a[184:187], a[116:119], v[184:187]\n"
    "ds_read2st64_b32 v[228:229], v250 offset0:48 offset1:49\n"
    "v_mfma_f32_16x16x32_bf16 v[176:179], a[184:187], a[84:87], v[176:179]\n"
    "s_waitcnt vmcnt(10)\n"
    "v_exp_f32_e32 v192, v192\n"
    "v_smfmac_f32_16x16x64_bf16 v[156:159], a[192:195], a[224:231], v152\n"
    "v_exp_f32_e32 v193, v193\n"
    "v_mfma_f32_16x16x32_bf16 v[176:179], a[188:191], a[88:91], v[176:179]\n"
    "v_exp_f32_e32 v194, v194\n"
    "v_mfma_f32_16x16x32_bf16 v[168:171], a[188:191], a[56:59], v[168:171]\n"
    "buffer_load_dwordx4 v248, s[8:11], s41 offen offset:2048 sc1 lds\n"
    "v_mfma_f32_16x16x32_bf16 v[168:171], a[184:187], a[52:55], v[168:171]\n"
    "ds_read2st64_b32 v[230:231], v250 offset0:50 offset1:51\n"
    "v_mfma_f32_16x16x32_bf16 v[160:163], a[184:187], a[20:23], v[160:163]\n"
    "s_waitcnt vmcnt(9)\n"
    "ds_read_b128 a[184:187], v243 offset:2048\n"
    "v_smfmac_f32_16x16x64_bf16 v[156:159], a[192:195], a[232:239], v152\n"
    "ds_read2st64_b32 v[236:237], v250 offset0:64 offset1:65\n"
    "v_mfma_f32_16x16x32_bf16 v[160:163], a[188:191], a[24:27], v[160:163]\n"
    "s_waitcnt lgkmcnt(9)\n"
    "ds_read_b128 a[188:191], v242 offset:6144\n"
    "v_mfma_f32_16x16x32_bf16 v[164:167], a[160:163], a[28:31], 0\n"
    "buffer_load_dwordx4 v248, s[8:11], s41 offen offset:3072 sc1 lds\n"
    "v_mfma_f32_16x16x32_bf16 v[164:167], a[164:167], a[0:3], v[164:167]\n"
    "s_add_u32 s41, s41, 0x8000\n"
    "v_exp_f32_e32 v195, v195\n"
    "v_mfma_f32_16x16x32_bf16 v[172:175], a[164:167], a[32:35], 0\n"
    "s_waitcnt vmcnt(8)\n"
    "v_cvt_pk_bf16_f32 v192, v192, v193\n"
    "v_smfmac_f32_16x16x64_bf16 v[156:159], a[192:195], a[240:247], v152\n"
    "v_exp_f32_e32 v184, v184\n"
    "v_mfma_f32_16x16x32_bf16 v[172:175], a[160:163], a[60:63], v[172:175]\n"
    "ds_read2st64_b32 v[238:239], v250 offset0:66 offset1:67\n"
    "v_mfma_f32_16x16x32_bf16 v[180:183], a[160:163], a[92:95], 0\n"
    "v_exp_f32_e32 v185, v185\n"
    "v_mfma_f32_16x16x32_bf16 v[180:183], a[164:167], a[64:67], v[180:183]\n"
    "v_cvt_pk_bf16_f32 v193, v194, v195\n"
    "v_mfma_f32_16x16x32_bf16 v[188:191], a[164:167], a[96:99], 0\n"
    "v_exp_f32_e32 v186, v186\n"
    "v_mfma_f32_16x16x32_bf16 v[188:191], a[160:163], a[124:127], v[188:191]\n"
    "v_exp_f32_e32 v187, v187\n"
    "v_mfma_f32_16x16x32_bf16 v[196:199], a[160:163], a[156:159], 0\n"
    "ds_read_b64_tr_b16 a[160:161], v244 offset:49152\n"
    "v_and_b32_e32 v253, 0xffff0000, v255\n"
    "v_mfma_f32_16x16x32_bf16 v[196:199], a[164:167], a[128:131], v[196:199]\n"
    "ds_read_b64_tr_b16 a[162:163], v244 offset:51200\n"
    "v_mfma_f32_16x16x32_bf16 v[196:199], a[168:171], a[132:135], v[196:199]\n"
    "ds_read_b64_tr_b16 a[164:165], v244 offset:57344\n"
    "v_mfma_f32_16x16x32_bf16 v[196:199], a[172:175], a[136:139], v[196:199]\n"
    "ds_read_b64_tr_b16 a[166:167], v244 offset:59392\n"
    "v_mfma_f32_16x16x32_bf16 v[188:191], a[172:175], a[104:107], v[188:191]\n"
    "v_cvt_pk_bf16_f32 v184, v184, v185\n"
    "v_mfma_f32_16x16x32_bf16 v[188:191], a[168:171], a[100:103], v[188:191]\n"
    "v_exp_f32_e32 v176, v176\n"
    "v_mfma_f32_16x16x32_bf16 v[180:183], a[168:171], a[68:71], v[180:183]\n"
    "v_exp_f32_e32 v177, v177\n"
    "v_mfma_f32_16x16x32_bf16 v[180:183], a[172:175], a[72:75], v[180:183]\n"
    "v_cvt_pk_bf16_f32 v185, v186, v187\n"
    "v_mul_f32_e32 v156, v253, v156\n"
    "v_mfma_f32_16x16x32_bf16 v[172:175], a[172:175], a[40:43], v[172:175]\n"
    "v_exp_f32_e32 v178, v178\n"
    "v_mfma_f32_16x16x32_bf16 v[172:175], a[168:171], a[36:39], v[172:175]\n"
    "v_exp_f32_e32 v179, v179\n"
    "v_mfma_f32_16x16x32_bf16 v[164:167], a[168:171], a[4:7], v[164:167]\n"
    "ds_read_b64_tr_b16 a[168:169], v245 offset:49152\n"
    "v_mfma_f32_16x16x32_bf16 v[164:167], a[172:175], a[8:11], v[164:167]\n"
    "s_waitcnt lgkmcnt(6)\n"
    "ds_read_b64_tr_b16 a[170:171], v245 offset:51200\n"
    "v_mfma_f32_16x16x32_bf16 v[164:167], a[176:179], a[12:15], v[164:167]\n"
    "ds_read_b64_tr_b16 a[172:173], v245 offset:57344\n"
    "v_mul_f32_e32 v157, v253, v157\n"
    "v_mfma_f32_16x16x32_bf16 v[164:167], a[180:183], a[16:19], v[164:167]\n"
    "ds_read_b64_tr_b16 a[174:175], v245 offset:59392\n"
    "v_mfma_f32_16x16x32_bf16 v[172:175], a[180:183], a[48:51], v[172:175]\n"
    "v_cvt_pk_bf16_f32 v176, v176, v177\n"
    "v_mfma_f32_16x16x32_bf16 v[172:175], a[176:179], a[44:47], v[172:175]\n"
    "v_exp_f32_e32 v168, v168\n"
    "v_mfma_f32_16x16x32_bf16 v[180:183], a[176:179], a[76:79], v[180:183]\n"
    "v_mul_f32_e32 v158, v253, v158\n"
    "v_mfma_f32_16x16x32_bf16 v[180:183], a[180:183], a[80:83], v[180:183]\n"
    "v_exp_f32_e32 v169, v169\n"
    "v_mfma_f32_16x16x32_bf16 v[188:191], a[180:183], a[112:115], v[188:191]\n"
    "v_cvt_pk_bf16_f32 v177, v178, v179\n"
    "v_mfma_f32_16x16x32_bf16 v[188:191], a[176:179], a[108:111], v[188:191]\n"
    "v_exp_f32_e32 v170, v170\n"
    "v_mfma_f32_16x16x32_bf16 v[196:199], a[176:179], a[140:143], v[196:199]\n"
    "ds_read_b64_tr_b16 a[176:177], v246 offset:49152\n"
    "v_mfma_f32_16x16x32_bf16 v[196:199], a[180:183], a[144:147], v[196:199]\n"
    "ds_read_b64_tr_b16 a[178:179], v246 offset:51200\n"
    "v_mfma_f32_16x16x32_bf16 v[196:199], a[184:187], a[148:151], v[196:199]\n"
    "ds_read_b64_tr_b16 a[180:181], v246 offset:57344\n"
    "v_mfma_f32_16x16x32_bf16 v[196:199], a[188:191], a[152:155], v[196:199]\n"
    "ds_read_b64_tr_b16 a[182:183], v246 offset:59392\n"
    "v_mul_f32_e32 v159, v253, v159\n"
    "v_mfma_f32_16x16x32_bf16 v[188:191], a[188:191], a[120:123], v[188:191]\n"
    "v_exp_f32_e32 v171, v171\n"
    "v_mfma_f32_16x16x32_bf16 v[188:191], a[184:187], a[116:119], v[188:191]\n"
    "v_cvt_pk_bf16_f32 v168, v168, v169\n"
    "v_mfma_f32_16x16x32_bf16 v[180:183], a[184:187], a[84:87], v[180:183]\n"
    "v_exp_f32_e32 v160, v160\n"
    "v_mfma_f32_16x16x32_bf16 v[180:183], a[188:191], a[88:91], v[180:183]\n"
    "v_exp_f32_e32 v161, v161\n"
    "v_mfma_f32_16x16x32_bf16 v[172:175], a[188:191], a[56:59], v[172:175]\n"
    "v_cvt_pk_bf16_f32 v169, v170, v171\n"
    "v_mfma_f32_16x16x32_bf16 v[172:175], a[184:187], a[52:55], v[172:175]\n"
    "v_exp_f32_e32 v162, v162\n"
    "v_mfma_f32_16x16x32_bf16 v[164:167], a[184:187], a[20:23], v[164:167]\n"
    "ds_read_b64_tr_b16 a[184:185], v247 offset:49152\n"
    "v_mad_u32_u16 v253, v255, s42, v153 op_sel:[0,0,0,0]\n"
    "v_mfma_f32_16x16x32_bf16 v[164:167], a[188:191], a[24:27], v[164:167]\n"
    "s_waitcnt lgkmcnt(5)\n"
    "ds_read_b64_tr_b16 a[186:187], v247 offset:51200\n"
    "v_mfma_f32_16x16x32_bf16 v[0:3], a[160:163], v[200:203], v[0:3]\n"
    "ds_read_b64_tr_b16 a[188:189], v247 offset:57344\n"
    "v_mfma_f32_16x16x32_bf16 v[0:3], a[164:167], v[204:207], v[0:3]\n"
    "ds_read_b64_tr_b16 a[190:191], v247 offset:59392\n"
    "v_mfma_f32_16x16x32_bf16 a[248:251], a[164:167], v[212:215], a[248:251]\n"
    "v_exp_f32_e32 v163, v163\n"
    "v_mfma_f32_16x16x32_bf16 a[248:251], a[160:163], v[208:211], a[248:251]\n"
    "v_cvt_pk_bf16_f32 v160, v160, v161\n"
    "v_mfma_f32_16x16x32_bf16 v[56:59], a[160:163], v[216:219], v[56:59]\n"
    "v_cvt_pk_bf16_f32 v154, v156, v157\n"
    "v_mfma_f32_16x16x32_bf16 v[56:59], a[164:167], v[220:223], v[56:59]\n"
    "v_cvt_pk_bf16_f32 v161, v162, v163\n"
    "v_mfma_f32_16x16x32_bf16 v[60:63], a[164:167], v[228:231], v[60:63]\n"
    "v_exp_f32_e32 v196, v196\n"
    "v_mfma_f32_16x16x32_bf16 v[60:63], a[160:163], v[224:227], v[60:63]\n"
    "v_exp_f32_e32 v197, v197\n"
    "v_mfma_f32_16x16x32_bf16 v[64:67], a[160:163], v[232:235], v[64:67]\n"
    "ds_read_b64_tr_b16 a[160:161], v244 offset:53248\n"
    "v_mfma_f32_16x16x32_bf16 v[64:67], a[164:167], v[236:239], v[64:67]\n"
    "ds_read_b64_tr_b16 a[162:163], v244 offset:55296\n"
    "v_cvt_pk_bf16_f32 v155, v158, v159\n"
    "v_mfma_f32_16x16x32_bf16 v[76:79], a[168:171], v[232:235], v[76:79]\n"
    "ds_read_b64_tr_b16 a[164:165], v244 offset:61440\n"
    "v_mfma_f32_16x16x32_bf16 v[76:79], a[172:175], v[236:239], v[76:79]\n"
    "ds_read_b64_tr_b16 a[166:167], v244 offset:63488\n"
    "v_mfma_f32_16x16x32_bf16 v[72:75], a[172:175], v[228:231], v[72:75]\n"
    "v_exp_f32_e32 v198, v198\n"
    "v_mfma_f32_16x16x32_bf16 v[72:75], a[168:171], v[224:227], v[72:75]\n"
    "v_exp_f32_e32 v199, v199\n"
    "v_mfma_f32_16x16x32_bf16 v[68:71], a[168:171], v[216:219], v[68:71]\n"
    "v_cvt_pk_bf16_f32 v194, v196, v197\n"
    "v_mov_b32_e32 v156, 0\n"
    "v_mfma_f32_16x16x32_bf16 v[68:71], a[172:175], v[220:223], v[68:71]\n"
    "v_exp_f32_e32 v188, v188\n"
    "v_mfma_f32_16x16x32_bf16 a[252:255], a[172:175], v[212:215], a[252:255]\n"
    "v_exp_f32_e32 v189, v189\n"
    "v_mfma_f32_16x16x32_bf16 a[252:255], a[168:171], v[208:211], a[252:255]\n"
    "v_cvt_pk_bf16_f32 v195, v198, v199\n"
    "v_mfma_f32_16x16x32_bf16 v[4:7], a[168:171], v[200:203], v[4:7]\n"
    "ds_read_b64_tr_b16 a[168:169], v245 offset:53248\n"
    "v_mfma_f32_16x16x32_bf16 v[4:7], a[172:175], v[204:207], v[4:7]\n"
    "s_waitcnt lgkmcnt(5)\n"
    "ds_read_b64_tr_b16 a[170:171], v245 offset:55296\n"
    "v_mfma_f32_16x16x32_bf16 v[8:11], a[176:179], v[200:203], v[8:11]\n"
    "ds_read_b64_tr_b16 a[172:173], v245 offset:61440\n"
    "v_mfma_f32_16x16x32_bf16 v[8:11], a[180:183], v[204:207], v[8:11]\n"
    "ds_read_b64_tr_b16 a[174:175], v245 offset:63488\n"
    "v_mov_b32_e32 v157, 0\n"
    "v_mfma_f32_16x16x32_bf16 v[32:35], a[180:183], v[212:215], v[32:35]\n"
    "v_exp_f32_e32 v190, v190\n"
    "v_mfma_f32_16x16x32_bf16 v[32:35], a[176:179], v[208:211], v[32:35]\n"
    "v_exp_f32_e32 v191, v191\n"
    "v_mfma_f32_16x16x32_bf16 v[80:83], a[176:179], v[216:219], v[80:83]\n"
    "v_mov_b32_e32 v158, 0\n"
    "v_mfma_f32_16x16x32_bf16 v[80:83], a[180:183], v[220:223], v[80:83]\n"
    "v_cvt_pk_bf16_f32 v186, v188, v189\n"
    "v_mfma_f32_16x16x32_bf16 v[84:87], a[180:183], v[228:231], v[84:87]\n"
    "v_exp_f32_e32 v180, v180\n"
    "v_mfma_f32_16x16x32_bf16 v[84:87], a[176:179], v[224:227], v[84:87]\n"
    "v_exp_f32_e32 v181, v181\n"
    "v_mfma_f32_16x16x32_bf16 v[88:91], a[176:179], v[232:235], v[88:91]\n"
    "ds_read_b64_tr_b16 a[176:177], v246 offset:53248\n"
    "v_mfma_f32_16x16x32_bf16 v[88:91], a[180:183], v[236:239], v[88:91]\n"
    "ds_read_b64_tr_b16 a[178:179], v246 offset:55296\n"
    "v_mfma_f32_16x16x32_bf16 v[100:103], a[184:187], v[232:235], v[100:103]\n"
    "ds_read_b64_tr_b16 a[180:181], v246 offset:61440\n"
    "v_mfma_f32_16x16x32_bf16 v[100:103], a[188:191], v[236:239], v[100:103]\n"
    "ds_read_b64_tr_b16 a[182:183], v246 offset:63488\n"
    "v_mfma_f32_16x16x32_bf16 v[96:99], a[188:191], v[228:231], v[96:99]\n"
    "v_cvt_pk_bf16_f32 v187, v190, v191\n"
    "v_mov_b32_e32 v159, 0\n"
    "v_mfma_f32_16x16x32_bf16 v[96:99], a[184:187], v[224:227], v[96:99]\n"
    "v_exp_f32_e32 v182, v182\n"
    "v_mfma_f32_16x16x32_bf16 v[92:95], a[184:187], v[216:219], v[92:95]\n"
    "v_exp_f32_e32 v183, v183\n"
    "v_mfma_f32_16x16x32_bf16 v[92:95], a[188:191], v[220:223], v[92:95]\n"
    "v_cvt_pk_bf16_f32 v178, v180, v181\n"
    "v_mfma_f32_16x16x32_bf16 v[36:39], a[188:191], v[212:215], v[36:39]\n"
    "v_exp_f32_e32 v172, v172\n"
    "v_mfma_f32_16x16x32_bf16 v[36:39], a[184:187], v[208:211], v[36:39]\n"
    "v_exp_f32_e32 v173, v173\n"
    "v_mfma_f32_16x16x32_bf16 v[12:15], a[184:187], v[200:203], v[12:15]\n"
    "s_mov_b32 m0, s49\n"
    "ds_read_b64_tr_b16 a[184:185], v247 offset:53248\n"
    "v_mfma_f32_16x16x32_bf16 v[12:15], a[188:191], v[204:207], v[12:15]\n"
    "s_waitcnt lgkmcnt(5)\n"
    "ds_read_b64_tr_b16 a[186:187], v247 offset:55296\n"
    "v_add_u32_e32 v252, 0x80, v252\n"
    "v_mfma_f32_16x16x32_bf16 v[16:19], a[160:163], v[200:203], v[16:19]\n"
    "ds_read_b64_tr_b16 a[188:189], v247 offset:61440\n"
    "v_mfma_f32_16x16x32_bf16 v[16:19], a[164:167], v[204:207], v[16:19]\n"
    "ds_read_b64_tr_b16 a[190:191], v247 offset:63488\n"
    "v_mfma_f32_16x16x32_bf16 v[40:43], a[164:167], v[212:215], v[40:43]\n"
    "buffer_store_dwordx2 v[154:155], v253, s[24:27], 0 offen offset:0 sc1\n"
    "v_cvt_pk_bf16_f32 v179, v182, v183\n"
    "v_mfma_f32_16x16x32_bf16 v[40:43], a[160:163], v[208:211], v[40:43]\n"
    "v_exp_f32_e32 v174, v174\n"
    "v_mfma_f32_16x16x32_bf16 v[104:107], a[160:163], v[216:219], v[104:107]\n"
    "v_exp_f32_e32 v175, v175\n"
    "v_mfma_f32_16x16x32_bf16 v[104:107], a[164:167], v[220:223], v[104:107]\n"
    "s_waitcnt vmcnt(9)\n"
    "v_mfma_f32_16x16x32_bf16 v[108:111], a[164:167], v[228:231], v[108:111]\n"
    "s_barrier\n"
    "v_mfma_f32_16x16x32_bf16 v[108:111], a[160:163], v[224:227], v[108:111]\n"
    "ds_write_addtid_b32 v192 offset:16384\n"
    "v_cvt_pk_bf16_f32 v170, v172, v173\n"
    "v_mfma_f32_16x16x32_bf16 v[112:115], a[160:163], v[232:235], v[112:115]\n"
    "ds_read_b128 a[160:163], v243 offset:20480\n"
    "v_mfma_f32_16x16x32_bf16 v[112:115], a[164:167], v[236:239], v[112:115]\n"
    "ds_read_b128 a[164:167], v240 offset:16384\n"
    "v_mfma_f32_16x16x32_bf16 v[124:127], a[168:171], v[232:235], v[124:127]\n"
    "v_exp_f32_e32 v164, v164\n"
    "v_mfma_f32_16x16x32_bf16 v[124:127], a[172:175], v[236:239], v[124:127]\n"
    "v_exp_f32_e32 v165, v165\n"
    "v_mfma_f32_16x16x32_bf16 v[120:123], a[172:175], v[228:231], v[120:123]\n"
    "ds_write_addtid_b32 v193 offset:16640\n"
    "v_cvt_pk_bf16_f32 v171, v174, v175\n"
    "v_mfma_f32_16x16x32_bf16 v[120:123], a[168:171], v[224:227], v[120:123]\n"
    "ds_write_addtid_b32 v184 offset:12288\n"
    "v_mfma_f32_16x16x32_bf16 v[116:119], a[168:171], v[216:219], v[116:119]\n"
    "v_exp_f32_e32 v166, v166\n"
    "v_mfma_f32_16x16x32_bf16 v[116:119], a[172:175], v[220:223], v[116:119]\n"
    "v_exp_f32_e32 v167, v167\n"
    "v_mfma_f32_16x16x32_bf16 v[44:47], a[172:175], v[212:215], v[44:47]\n"
    "ds_write_addtid_b32 v185 offset:12544\n"
    "v_cvt_pk_bf16_f32 v162, v164, v165\n"
    "v_mfma_f32_16x16x32_bf16 v[44:47], a[168:171], v[208:211], v[44:47]\n"
    "v_cvt_pk_bf16_f32 v163, v166, v167\n"
    "v_mfma_f32_16x16x32_bf16 v[20:23], a[168:171], v[200:203], v[20:23]\n"
    "ds_read_b128 a[168:171], v241 offset:16384\n"
    "v_mfma_f32_16x16x32_bf16 v[20:23], a[172:175], v[204:207], v[20:23]\n"
    "s_waitcnt lgkmcnt(7)\n"
    "ds_read_b128 a[172:175], v240 offset:20480\n"
    "v_mfma_f32_16x16x32_bf16 v[24:27], a[176:179], v[200:203], v[24:27]\n"
    "ds_write_addtid_b32 v176 offset:8192\n"
    "v_mfma_f32_16x16x32_bf16 v[24:27], a[180:183], v[204:207], v[24:27]\n"
    "ds_write_addtid_b32 v177 offset:8448\n"
    "v_mfma_f32_16x16x32_bf16 v[48:51], a[180:183], v[212:215], v[48:51]\n"
    "ds_write_addtid_b32 v168 offset:4096\n"
    "v_mfma_f32_16x16x32_bf16 v[48:51], a[176:179], v[208:211], v[48:51]\n"
    "ds_write_addtid_b32 v169 offset:4352\n"
    "v_mfma_f32_16x16x32_bf16 v[128:131], a[176:179], v[216:219], v[128:131]\n"
    "ds_write_addtid_b32 v160 offset:0\n"
    "v_mfma_f32_16x16x32_bf16 v[128:131], a[180:183], v[220:223], v[128:131]\n"
    "ds_write_addtid_b32 v161 offset:256\n"
    "v_mfma_f32_16x16x32_bf16 v[132:135], a[180:183], v[228:231], v[132:135]\n"
    "ds_write_addtid_b32 v194 offset:16896\n"
    "v_mfma_f32_16x16x32_bf16 v[132:135], a[176:179], v[224:227], v[132:135]\n"
    "ds_write_addtid_b32 v195 offset:17152\n"
    "v_mfma_f32_16x16x32_bf16 v[136:139], a[176:179], v[232:235], v[136:139]\n"
    "ds_read_b128 a[176:179], v241 offset:20480\n"
    "v_mfma_f32_16x16x32_bf16 v[136:139], a[180:183], v[236:239], v[136:139]\n"
    "ds_read_b128 a[180:183], v242 offset:16384\n"
    "v_mfma_f32_16x16x32_bf16 v[148:151], a[184:187], v[232:235], v[148:151]\n"
    "ds_write_addtid_b32 v186 offset:12800\n"
    "v_mfma_f32_16x16x32_bf16 v[148:151], a[188:191], v[236:239], v[148:151]\n"
    "ds_write_addtid_b32 v187 offset:13056\n"
    "v_mfma_f32_16x16x32_bf16 v[144:147], a[188:191], v[228:231], v[144:147]\n"
    "ds_write_addtid_b32 v178 offset:8704\n"
    "v_mfma_f32_16x16x32_bf16 v[144:147], a[184:187], v[224:227], v[144:147]\n"
    "ds_write_addtid_b32 v179 offset:8960\n"
    "v_mfma_f32_16x16x32_bf16 v[140:143], a[184:187], v[216:219], v[140:143]\n"
    "s_sub_u32 s44, s44, 1\n"
    "ds_write_addtid_b32 v170 offset:4608\n"
    "v_mfma_f32_16x16x32_bf16 v[140:143], a[188:191], v[220:223], v[140:143]\n"
    "ds_write_addtid_b32 v171 offset:4864\n"
    "v_mfma_f32_16x16x32_bf16 v[52:55], a[188:191], v[212:215], v[52:55]\n"
    "ds_write_addtid_b32 v162 offset:512\n"
    "v_mfma_f32_16x16x32_bf16 v[52:55], a[184:187], v[208:211], v[52:55]\n"
    "ds_write_addtid_b32 v163 offset:768\n"
    "v_mfma_f32_16x16x32_bf16 v[28:31], a[184:187], v[200:203], v[28:31]\n"
    "ds_read_b128 a[184:187], v243 offset:16384\n"
    "v_mfma_f32_16x16x32_bf16 v[28:31], a[188:191], v[204:207], v[28:31]\n"
    "s_waitcnt lgkmcnt(15)\n"
    "ds_read_b128 a[188:191], v242 offset:20480\n"
    "s_cbranch_scc0 .Lat_g\n"
    "s_mov_b32 s44, 1\n"
    "s_bitcmp0_b32 %1, 0\n"
    "s_cbranch_scc1 .Lat_p\n"
    ".Lat_tail:\n"
    "v_mfma_f32_16x16x32_bf16 v[216:219], a[160:163], a[92:95], 0\n"
    "s_mov_b32 s45, 5775360\n"
    "v_mbcnt_lo_u32_b32 v253, -1, 0\n"
    "v_mfma_f32_16x16x32_bf16 v[216:219], a[164:167], a[64:67], v[216:219]\n"
    "s_add_u32 m0, s43, 49152\n"
    "v_mbcnt_hi_u32_b32 v253, -1, v253\n"
    "v_mfma_f32_16x16x32_bf16 v[224:227], a[164:167], a[96:99], 0\n"
    "buffer_load_dwordx4 v248, s[8:11], s40 offen offset:0 sc1 lds\n"
    "v_mfma_f32_16x16x32_bf16 v[224:227], a[160:163], a[124:127], v[224:227]\n"
    "s_lshl_b32 s56, %2, 15\n"
    "v_and_b32_e32 v254, 3, v253\n"
    "v_mfma_f32_16x16x32_bf16 v[232:235], a[160:163], a[156:159], 0\n"
    "ds_read_b128 a[160:163], v243 offset:22528\n"
    "v_mfma_f32_16x16x32_bf16 v[232:235], a[164:167], a[128:131], v[232:235]\n"
    "ds_read_b128 a[164:167], v240 offset:18432\n"
    "v_mfma_f32_16x16x32_bf16 v[232:235], a[168:171], a[132:135], v[232:235]\n"
    "s_waitcnt lgkmcnt(0)\n"
    "s_barrier\n"
    "v_mfma_f32_16x16x32_bf16 v[232:235], a[172:175], a[136:139], v[232:235]\n"
    "buffer_load_dwordx4 v248, s[8:11], s40 offen offset:1024 sc1 lds\n"
    "v_mfma_f32_16x16x32_bf16 v[224:227], a[172:175], a[104:107], v[224:227]\n"
    "s_add_u32 s57, s56, 5505024\n"
    "v_lshlrev_b32_e32 v254, 2, v254\n"
    "v_mfma_f32_16x16x32_bf16 v[224:227], a[168:171], a[100:103], v[224:227]\n"
    "s_add_u32 s56, s56, 5373952\n"
    "ds_read2st64_b32 v[164:165], v250 offset0:0 offset1:1\n"
    "v_and_b32_e32 v158, 15, v253\n"
    "v_mfma_f32_16x16x32_bf16 v[216:219], a[168:171], a[68:71], v[216:219]\n"
    "ds_read_b128 a[168:171], v241 offset:18432\n"
    "v_mfma_f32_16x16x32_bf16 v[216:219], a[172:175], a[72:75], v[216:219]\n"
    "ds_read_b128 a[172:175], v240 offset:22528\n"
    "v_mfma_f32_16x16x32_bf16 v[216:219], a[176:179], a[76:79], v[216:219]\n"
    "buffer_load_dwordx4 v248, s[8:11], s40 offen offset:2048 sc1 lds\n"
    "ds_read2st64_b32 v[166:167], v250 offset0:2 offset1:3\n"
    "v_mfma_f32_16x16x32_bf16 v[216:219], a[180:183], a[80:83], v[216:219]\n"
    "s_lshl_b32 s47, %2, 7\n"
    "v_lshlrev_b32_e32 v158, 9, v158\n"
    "v_mfma_f32_16x16x32_bf16 v[224:227], a[180:183], a[112:115], v[224:227]\n"
    "s_add_u32 s47, s47, s35\n"
    "v_and_or_b32 v158, v253, 48, v158\n"
    "v_mfma_f32_16x16x32_bf16 v[224:227], a[176:179], a[108:111], v[224:227]\n"
    "ds_read2st64_b32 v[172:173], v250 offset0:16 offset1:17\n"
    "v_bfe_u32 v252, v253, 4, 2\n"
    "v_mfma_f32_16x16x32_bf16 v[232:235], a[176:179], a[140:143], v[232:235]\n"
    "ds_read_b128 a[176:179], v241 offset:22528\n"
    "buffer_load_dwordx4 v248, s[8:11], s40 offen offset:3072 sc1 lds\n"
    "v_mfma_f32_16x16x32_bf16 v[232:235], a[180:183], a[144:147], v[232:235]\n"
    "s_add_u32 s40, s40, 32768\n"
    "ds_read_b128 a[180:183], v242 offset:18432\n"
    "v_mfma_f32_16x16x32_bf16 v[232:235], a[184:187], a[148:151], v[232:235]\n"
    "s_add_u32 m0, s43, 53248\n"
    "v_lshlrev_b32_e32 v252, 1, v252\n"
    "v_mfma_f32_16x16x32_bf16 v[232:235], a[188:191], a[152:155], v[232:235]\n"
    "ds_read2st64_b32 v[174:175], v250 offset0:18 offset1:19\n"
    "v_xor_b32_e32 v252, v252, v253\n"
    "v_mfma_f32_16x16x32_bf16 v[224:227], a[188:191], a[120:123], v[224:227]\n"
    "buffer_load_dwordx4 v248, s[8:11], s41 offen offset:0 sc1 lds\n"
    "v_mfma_f32_16x16x32_bf16 v[224:227], a[184:187], a[116:119], v[224:227]\n"
    "v_and_b32_e32 v252, 7, v252\n"
    "v_mfma_f32_16x16x32_bf16 v[216:219], a[184:187], a[84:87], v[216:219]\n"
    "ds_read_b128 a[184:187], v243 offset:18432\n"
    "v_mfma_f32_16x16x32_bf16 v[216:219], a[188:191], a[88:91], v[216:219]\n"
    "ds_read_b128 a[188:191], v242 offset:22528\n"
    "v_mfma_f32_16x16x32_bf16 v[220:223], a[160:163], a[92:95], 0\n"
    "buffer_load_dwordx4 v248, s[8:11], s41 offen offset:1024 sc1 lds\n"
    "ds_read2st64_b32 v[180:181], v250 offset0:32 offset1:33\n"
    "v_mfma_f32_16x16x32_bf16 v[220:223], a[164:167], a[64:67], v[220:223]\n"
    "v_lshlrev_b32_e32 v252, 4, v252\n"
    "v_mfma_f32_16x16x32_bf16 v[228:231], a[164:167], a[96:99], 0\n"
    "ds_read2st64_b32 v[182:183], v250 offset0:34 offset1:35\n"
    "v_lshrrev_b32_e32 v255, 3, v253\n"
    "v_mfma_f32_16x16x32_bf16 v[228:231], a[160:163], a[124:127], v[228:231]\n"
    "ds_read2st64_b32 v[188:189], v250 offset0:48 offset1:49\n"
    "v_lshl_add_u32 v252, v255, 9, v252\n"
    "v_mfma_f32_16x16x32_bf16 v[236:239], a[160:163], a[156:159], 0\n"
    "ds_read_b64_tr_b16 a[160:161], v244 offset:0\n"
    "buffer_load_dwordx4 v248, s[8:11], s41 offen offset:2048 sc1 lds\n"
    "v_mfma_f32_16x16x32_bf16 v[236:239], a[164:167], a[128:131], v[236:239]\n"
    "s_waitcnt lgkmcnt(11)\n"
    "ds_read_b64_tr_b16 a[162:163], v244 offset:2048\n"
    "v_mfma_f32_16x16x32_bf16 v[236:239], a[168:171], a[132:135], v[236:239]\n"
    "ds_read_b64_tr_b16 a[164:165], v244 offset:8192\n"
    "v_mfma_f32_16x16x32_bf16 v[236:239], a[172:175], a[136:139], v[236:239]\n"
    "ds_read_b64_tr_b16 a[166:167], v244 offset:10240\n"
    "v_mfma_f32_16x16x32_bf16 v[228:231], a[172:175], a[104:107], v[228:231]\n"
    "buffer_load_dwordx4 v248, s[8:11], s41 offen offset:3072 sc1 lds\n"
    "ds_read2st64_b32 v[190:191], v250 offset0:50 offset1:51\n"
    "v_mfma_f32_16x16x32_bf16 v[228:231], a[168:171], a[100:103], v[228:231]\n"
    "s_add_u32 s41, s41, 32768\n"
    "s_add_u32 s61, s47, 0\n"
    "v_lshlrev_b32_e32 v253, 4, v253\n"
    "v_mfma_f32_16x16x32_bf16 v[220:223], a[168:171], a[68:71], v[220:223]\n"
    "s_mov_b32 m0, s49\n"
    "ds_read_b64_tr_b16 a[168:169], v245 offset:0\n"
    "v_mfma_f32_16x16x32_bf16 v[220:223], a[172:175], a[72:75], v[220:223]\n"
    "s_waitcnt lgkmcnt(12)\n"
    "ds_read_b64_tr_b16 a[170:171], v245 offset:2048\n"
    "v_mfma_f32_16x16x32_bf16 v[220:223], a[176:179], a[76:79], v[220:223]\n"
    "ds_read_b64_tr_b16 a[172:173], v245 offset:8192\n"
    "buffer_load_dwordx4 a[224:227], v158, s[12:15], s61 offen offset:0\n"
    "v_mfma_f32_16x16x32_bf16 v[220:223], a[180:183], a[80:83], v[220:223]\n"
    "ds_read_b64_tr_b16 a[174:175], v245 offset:10240\n"
    "v_mfma_f32_16x16x32_bf16 v[228:231], a[180:183], a[112:115], v[228:231]\n"
    "ds_read2st64_b32 v[196:197], v250 offset0:64 offset1:65\n"
    "v_mfma_f32_16x16x32_bf16 v[228:231], a[176:179], a[108:111], v[228:231]\n"
    "ds_read2st64_b32 v[198:199], v250 offset0:66 offset1:67\n"
    "v_mfma_f32_16x16x32_bf16 v[236:239], a[176:179], a[140:143], v[236:239]\n"
    "ds_read_b64_tr_b16 a[176:177], v246 offset:0\n"
    "v_mfma_f32_16x16x32_bf16 v[236:239], a[180:183], a[144:147], v[236:239]\n"
    "s_waitcnt lgkmcnt(15)\n"
    "ds_read_b64_tr_b16 a[178:179], v246 offset:2048\n"
    "v_mfma_f32_16x16x32_bf16 v[236:239], a[184:187], a[148:151], v[236:239]\n"
    "ds_read_b64_tr_b16 a[180:181], v246 offset:8192\n"
    "v_mfma_f32_16x16x32_bf16 v[236:239], a[188:191], a[152:155], v[236:239]\n"
    "ds_read_b64_tr_b16 a[182:183], v246 offset:10240\n"
    "v_mfma_f32_16x16x32_bf16 v[228:231], a[188:191], a[120:123], v[228:231]\n"
    "v_exp_f32_e32 v232, v232\n"
    "v_mfma_f32_16x16x32_bf16 v[228:231], a[184:187], a[116:119], v[228:231]\n"
    "v_exp_f32_e32 v233, v233\n"
    "v_mfma_f32_16x16x32_bf16 v[220:223], a[184:187], a[84:87], v[220:223]\n"
    "ds_read_b64_tr_b16 a[184:185], v247 offset:0\n"
    "v_mfma_f32_16x16x32_bf16 v[220:223], a[188:191], a[88:91], v[220:223]\n"
    "s_waitcnt lgkmcnt(5)\n"
    "ds_read_b64_tr_b16 a[186:187], v247 offset:2048\n"
    "v_mfma_f32_16x16x32_bf16 v[0:3], a[160:163], v[160:163], v[0:3]\n"
    "ds_read_b64_tr_b16 a[188:189], v247 offset:8192\n"
    "buffer_load_dwordx4 a[228:231], v158, s[12:15], s61 offen offset:64\n"
    "v_mfma_f32_16x16x32_bf16 v[0:3], a[164:167], v[164:167], v[0:3]\n"
    "s_add_u32 s61, s47, 8192\n"
    "ds_read_b64_tr_b16 a[190:191], v247 offset:10240\n"
    "v_mfma_f32_16x16x32_bf16 a[248:251], a[164:167], v[172:175], a[248:251]\n"
    "v_exp_f32_e32 v234, v234\n"
    "v_mfma_f32_16x16x32_bf16 a[248:251], a[160:163], v[168:171], a[248:251]\n"
    "v_exp_f32_e32 v235, v235\n"
    "v_mfma_f32_16x16x32_bf16 v[56:59], a[160:163], v[176:179], v[56:59]\n"
    "v_cvt_pk_bf16_f32 v232, v232, v233\n"
    "v_mfma_f32_16x16x32_bf16 v[56:59], a[164:167], v[180:183], v[56:59]\n"
    "v_exp_f32_e32 v224, v224\n"
    "v_mfma_f32_16x16x32_bf16 v[60:63], a[164:167], v[188:191], v[60:63]\n"
    "v_exp_f32_e32 v225, v225\n"
    "v_mfma_f32_16x16x32_bf16 v[60:63], a[160:163], v[184:187], v[60:63]\n"
    "v_cvt_pk_bf16_f32 v233, v234, v235\n"
    "v_mfma_f32_16x16x32_bf16 v[64:67], a[160:163], v[192:195], v[64:67]\n"
    "ds_read_b64_tr_b16 a[160:161], v244 offset:4096\n"
    "v_mfma_f32_16x16x32_bf16 v[64:67], a[164:167], v[196:199], v[64:67]\n"
    "ds_read_b64_tr_b16 a[162:163], v244 offset:6144\n"
    "v_mfma_f32_16x16x32_bf16 v[76:79], a[168:171], v[192:195], v[76:79]\n"
    "ds_read_b64_tr_b16 a[164:165], v244 offset:12288\n"
    "v_mfma_f32_16x16x32_bf16 v[76:79], a[172:175], v[196:199], v[76:79]\n"
    "ds_read_b64_tr_b16 a[166:167], v244 offset:14336\n"
    "v_mfma_f32_16x16x32_bf16 v[72:75], a[172:175], v[188:191], v[72:75]\n"
    "buffer_load_dwordx4 a[232:235], v158, s[12:15], s61 offen offset:0\n"
    "v_mfma_f32_16x16x32_bf16 v[72:75], a[168:171], v[184:187], v[72:75]\n"
    "v_exp_f32_e32 v226, v226\n"
    "v_mfma_f32_16x16x32_bf16 v[68:71], a[168:171], v[176:179], v[68:71]\n"
    "s_waitcnt vmcnt(11)\n"
    "s_barrier\n"
    "v_mfma_f32_16x16x32_bf16 v[68:71], a[172:175], v[180:183], v[68:71]\n"
    "v_exp_f32_e32 v227, v227\n"
    "v_mfma_f32_16x16x32_bf16 a[252:255], a[172:175], v[172:175], a[252:255]\n"
    "v_cvt_pk_bf16_f32 v224, v224, v225\n"
    "v_mfma_f32_16x16x32_bf16 a[252:255], a[168:171], v[168:171], a[252:255]\n"
    "v_exp_f32_e32 v216, v216\n"
    "v_mfma_f32_16x16x32_bf16 v[4:7], a[168:171], v[160:163], v[4:7]\n"
    "ds_read_b64_tr_b16 a[168:169], v245 offset:4096\n"
    "v_mfma_f32_16x16x32_bf16 v[4:7], a[172:175], v[164:167], v[4:7]\n"
    "s_waitcnt lgkmcnt(9)\n"
    "ds_read_b64_tr_b16 a[170:171], v245 offset:6144\n"
    "v_mfma_f32_16x16x32_bf16 v[8:11], a[176:179], v[160:163], v[8:11]\n"
    "ds_read_b64_tr_b16 a[172:173], v245 offset:12288\n"
    "v_mfma_f32_16x16x32_bf16 v[8:11], a[180:183], v[164:167], v[8:11]\n"
    "ds_read_b64_tr_b16 a[174:175], v245 offset:14336\n"
    "v_mfma_f32_16x16x32_bf16 v[32:35], a[180:183], v[172:175], v[32:35]\n"
    "v_exp_f32_e32 v217, v217\n"
    "v_mfma_f32_16x16x32_bf16 v[32:35], a[176:179], v[168:171], v[32:35]\n"
    "v_cvt_pk_bf16_f32 v225, v226, v227\n"
    "v_mfma_f32_16x16x32_bf16 v[80:83], a[176:179], v[176:179], v[80:83]\n"
    "buffer_load_dwordx4 a[236:239], v158, s[12:15], s61 offen offset:64\n"
    "v_mfma_f32_16x16x32_bf16 v[80:83], a[180:183], v[180:183], v[80:83]\n"
    "s_add_u32 s61, s47, 16384\n"
    "v_exp_f32_e32 v218, v218\n"
    "v_mfma_f32_16x16x32_bf16 v[84:87], a[180:183], v[188:191], v[84:87]\n"
    "v_exp_f32_e32 v219, v219\n"
    "v_mfma_f32_16x16x32_bf16 v[84:87], a[176:179], v[184:187], v[84:87]\n"
    "v_cvt_pk_bf16_f32 v216, v216, v217\n"
    "v_mfma_f32_16x16x32_bf16 v[88:91], a[176:179], v[192:195], v[88:91]\n"
    "ds_read_b64_tr_b16 a[176:177], v246 offset:4096\n"
    "v_mfma_f32_16x16x32_bf16 v[88:91], a[180:183], v[196:199], v[88:91]\n"
    "s_waitcnt lgkmcnt(9)\n"
    "ds_read_b64_tr_b16 a[178:179], v246 offset:6144\n"
    "v_mfma_f32_16x16x32_bf16 v[100:103], a[184:187], v[192:195], v[100:103]\n"
    "ds_read_b64_tr_b16 a[180:181], v246 offset:12288\n"
    "v_mfma_f32_16x16x32_bf16 v[100:103], a[188:191], v[196:199], v[100:103]\n"
    "ds_read_b64_tr_b16 a[182:183], v246 offset:14336\n"
    "v_mfma_f32_16x16x32_bf16 v[96:99], a[188:191], v[188:191], v[96:99]\n"
    "v_cvt_pk_bf16_f32 v217, v218, v219\n"
    "v_mfma_f32_16x16x32_bf16 v[96:99], a[184:187], v[184:187], v[96:99]\n"
    "v_exp_f32_e32 v236, v236\n"
    "v_mfma_f32_16x16x32_bf16 v[92:95], a[184:187], v[176:179], v[92:95]\n"
    "v_exp_f32_e32 v237, v237\n"
    "v_mfma_f32_16x16x32_bf16 v[92:95], a[188:191], v[180:183], v[92:95]\n"
    "v_exp_f32_e32 v238, v238\n"
    "v_mfma_f32_16x16x32_bf16 v[36:39], a[188:191], v[172:175], v[36:39]\n"
    "buffer_load_dwordx4 a[240:243], v158, s[12:15], s61 offen offset:0\n"
    "v_mfma_f32_16x16x32_bf16 v[36:39], a[184:187], v[168:171], v[36:39]\n"
    "v_exp_f32_e32 v239, v239\n"
    "v_mfma_f32_16x16x32_bf16 v[12:15], a[184:187], v[160:163], v[12:15]\n"
    "ds_read_b64_tr_b16 a[184:185], v247 offset:4096\n"
    "v_mfma_f32_16x16x32_bf16 v[12:15], a[188:191], v[164:167], v[12:15]\n"
    "s_waitcnt lgkmcnt(9)\n"
    "ds_read_b64_tr_b16 a[186:187], v247 offset:6144\n"
    "v_mfma_f32_16x16x32_bf16 v[16:19], a[160:163], v[160:163], v[16:19]\n"
    "ds_read_b64_tr_b16 a[188:189], v247 offset:12288\n"
    "v_mfma_f32_16x16x32_bf16 v[16:19], a[164:167], v[164:167], v[16:19]\n"
    "ds_read_b64_tr_b16 a[190:191], v247 offset:14336\n"
    "v_mfma_f32_16x16x32_bf16 v[40:43], a[164:167], v[172:175], v[40:43]\n"
    "v_cvt_pk_bf16_f32 v234, v236, v237\n"
    "v_mfma_f32_16x16x32_bf16 v[40:43], a[160:163], v[168:171], v[40:43]\n"
    "v_exp_f32_e32 v228, v228\n"
    "v_mfma_f32_16x16x32_bf16 v[104:107], a[160:163], v[176:179], v[104:107]\n"
    "ds_write_addtid_b32 v232 offset:16384\n"
    "v_mfma_f32_16x16x32_bf16 v[104:107], a[164:167], v[180:183], v[104:107]\n"
    "ds_write_addtid_b32 v233 offset:16640\n"
    "v_mfma_f32_16x16x32_bf16 v[108:111], a[164:167], v[188:191], v[108:111]\n"
    "v_exp_f32_e32 v229, v229\n"
    "v_mfma_f32_16x16x32_bf16 v[108:111], a[160:163], v[184:187], v[108:111]\n"
    "ds_write_addtid_b32 v224 offset:12288\n"
    "v_mfma_f32_16x16x32_bf16 v[112:115], a[160:163], v[192:195], v[112:115]\n"
    "ds_read_b128 a[160:163], v243 offset:36864\n"
    "buffer_load_dwordx4 a[244:247], v158, s[12:15], s61 offen offset:64\n"
    "v_mfma_f32_16x16x32_bf16 v[112:115], a[164:167], v[196:199], v[112:115]\n"
    "s_waitcnt lgkmcnt(12)\n"
    "ds_read_b128 a[164:167], v240 offset:32768\n"
    "v_mfma_f32_16x16x32_bf16 v[124:127], a[168:171], v[192:195], v[124:127]\n"
    "v_cvt_pk_bf16_f32 v235, v238, v239\n"
    "v_mfma_f32_16x16x32_bf16 v[124:127], a[172:175], v[196:199], v[124:127]\n"
    "ds_write_addtid_b32 v225 offset:12544\n"
    "v_mfma_f32_16x16x32_bf16 v[120:123], a[172:175], v[188:191], v[120:123]\n"
    "v_exp_f32_e32 v230, v230\n"
    "v_mfma_f32_16x16x32_bf16 v[120:123], a[168:171], v[184:187], v[120:123]\n"
    "ds_write_addtid_b32 v216 offset:8192\n"
    "v_mfma_f32_16x16x32_bf16 v[116:119], a[168:171], v[176:179], v[116:119]\n"
    "ds_write_addtid_b32 v217 offset:8448\n"
    "v_mfma_f32_16x16x32_bf16 v[116:119], a[172:175], v[180:183], v[116:119]\n"
    "v_exp_f32_e32 v231, v231\n"
    "v_mfma_f32_16x16x32_bf16 v[44:47], a[172:175], v[172:175], v[44:47]\n"
    "ds_write_addtid_b32 v234 offset:16896\n"
    "v_mfma_f32_16x16x32_bf16 v[44:47], a[168:171], v[168:171], v[44:47]\n"
    "v_cvt_pk_bf16_f32 v226, v228, v229\n"
    "v_mfma_f32_16x16x32_bf16 v[20:23], a[168:171], v[160:163], v[20:23]\n"
    "ds_read_b128 a[168:171], v241 offset:32768\n"
    "v_mfma_f32_16x16x32_bf16 v[20:23], a[172:175], v[164:167], v[20:23]\n"
    "s_waitcnt lgkmcnt(14)\n"
    "ds_read_b128 a[172:175], v240 offset:36864\n"
    "v_mfma_f32_16x16x32_bf16 v[24:27], a[176:179], v[160:163], v[24:27]\n"
    "v_mfma_f32_16x16x32_bf16 v[24:27], a[180:183], v[164:167], v[24:27]\n"
    "v_exp_f32_e32 v220, v220\n"
    "v_mfma_f32_16x16x32_bf16 v[48:51], a[180:183], v[172:175], v[48:51]\n"
    "v_mfma_f32_16x16x32_bf16 v[48:51], a[176:179], v[168:171], v[48:51]\n"
    "ds_write_addtid_b32 v235 offset:17152\n"
    "v_mfma_f32_16x16x32_bf16 v[128:131], a[176:179], v[176:179], v[128:131]\n"
    "v_exp_f32_e32 v221, v221\n"
    "v_mfma_f32_16x16x32_bf16 v[128:131], a[180:183], v[180:183], v[128:131]\n"
    "v_mfma_f32_16x16x32_bf16 v[132:135], a[180:183], v[188:191], v[132:135]\n"
    "ds_write_addtid_b32 v226 offset:12800\n"
    "v_mfma_f32_16x16x32_bf16 v[132:135], a[176:179], v[184:187], v[132:135]\n"
    "v_cvt_pk_bf16_f32 v227, v230, v231\n"
    "v_mfma_f32_16x16x32_bf16 v[136:139], a[176:179], v[192:195], v[136:139]\n"
    "ds_read_b128 a[176:179], v241 offset:36864\n"
    "v_mfma_f32_16x16x32_bf16 v[136:139], a[180:183], v[196:199], v[136:139]\n"
    "s_waitcnt lgkmcnt(14)\n"
    "ds_read_b128 a[180:183], v242 offset:32768\n"
    "v_mfma_f32_16x16x32_bf16 v[148:151], a[184:187], v[192:195], v[148:151]\n"
    "v_exp_f32_e32 v222, v222\n"
    "v_mfma_f32_16x16x32_bf16 v[148:151], a[188:191], v[196:199], v[148:151]\n"
    "v_mfma_f32_16x16x32_bf16 v[144:147], a[188:191], v[188:191], v[144:147]\n"
    "ds_write_addtid_b32 v227 offset:13056\n"
    "v_mfma_f32_16x16x32_bf16 v[144:147], a[184:187], v[184:187], v[144:147]\n"
    "v_exp_f32_e32 v223, v223\n"
    "v_mfma_f32_16x16x32_bf16 v[140:143], a[184:187], v[176:179], v[140:143]\n"
    "v_mfma_f32_16x16x32_bf16 v[140:143], a[188:191], v[180:183], v[140:143]\n"
    "v_mfma_f32_16x16x32_bf16 v[52:55], a[188:191], v[172:175], v[52:55]\n"
    "v_cvt_pk_bf16_f32 v218, v220, v221\n"
    "v_mfma_f32_16x16x32_bf16 v[52:55], a[184:187], v[168:171], v[52:55]\n"
    "v_cvt_pk_bf16_f32 v219, v222, v223\n"
    "v_mfma_f32_16x16x32_bf16 v[28:31], a[184:187], v[160:163], v[28:31]\n"
    "ds_read_b128 a[184:187], v243 offset:32768\n"
    "v_mfma_f32_16x16x32_bf16 v[28:31], a[188:191], v[164:167], v[28:31]\n"
    "s_waitcnt lgkmcnt(12)\n"
    "ds_read_b128 a[188:191], v242 offset:36864\n"
    "v_mfma_f32_16x16x32_bf16 v[176:179], a[160:163], a[92:95], 0\n"
    "ds_write_addtid_b32 v218 offset:8704\n"
    "v_mfma_f32_16x16x32_bf16 v[176:179], a[164:167], a[64:67], v[176:179]\n"
    "v_mfma_f32_16x16x32_bf16 v[184:187], a[164:167], a[96:99], 0\n"
    "v_mfma_f32_16x16x32_bf16 v[184:187], a[160:163], a[124:127], v[184:187]\n"
    "ds_write_addtid_b32 v219 offset:8960\n"
    "v_mfma_f32_16x16x32_bf16 v[192:195], a[160:163], a[156:159], 0\n"
    "ds_read_b128 a[160:163], v243 offset:38912\n"
    "v_mfma_f32_16x16x32_bf16 v[192:195], a[164:167], a[128:131], v[192:195]\n"
    "s_waitcnt lgkmcnt(10)\n"
    "ds_read_b128 a[164:167], v240 offset:34816\n"
    "v_mfma_f32_16x16x32_bf16 v[192:195], a[168:171], a[132:135], v[192:195]\n"
    "v_mfma_f32_16x16x32_bf16 v[192:195], a[172:175], a[136:139], v[192:195]\n"
    "v_mfma_f32_16x16x32_bf16 v[184:187], a[172:175], a[104:107], v[184:187]\n"
    "s_waitcnt lgkmcnt(2)\n"
    "s_barrier\n"
    "v_mfma_f32_16x16x32_bf16 v[184:187], a[168:171], a[100:103], v[184:187]\n"
    "s_add_u32 m0, s43, 0\n"
    "v_mfma_f32_16x16x32_bf16 v[176:179], a[168:171], a[68:71], v[176:179]\n"
    "ds_read_b128 a[168:171], v241 offset:34816\n"
    "buffer_load_dwordx4 v248, s[8:11], s40 offen offset:0 sc1 lds\n"
    "v_mfma_f32_16x16x32_bf16 v[176:179], a[172:175], a[72:75], v[176:179]\n"
    "ds_read_b128 a[172:175], v240 offset:38912\n"
    "v_mfma_f32_16x16x32_bf16 v[176:179], a[176:179], a[76:79], v[176:179]\n"
    "ds_read2st64_b32 v[220:221], v250 offset0:32 offset1:33\n"
    "v_mfma_f32_16x16x32_bf16 v[176:179], a[180:183], a[80:83], v[176:179]\n"
    "v_mfma_f32_16x16x32_bf16 v[184:187], a[180:183], a[112:115], v[184:187]\n"
    "buffer_load_dwordx4 v248, s[8:11], s40 offen offset:1024 sc1 lds\n"
    "v_mfma_f32_16x16x32_bf16 v[184:187], a[176:179], a[108:111], v[184:187]\n"
    "v_mfma_f32_16x16x32_bf16 v[192:195], a[176:179], a[140:143], v[192:195]\n"
    "ds_read_b128 a[176:179], v241 offset:38912\n"
    "v_mfma_f32_16x16x32_bf16 v[192:195], a[180:183], a[144:147], v[192:195]\n"
    "ds_read_b128 a[180:183], v242 offset:34816\n"
    "v_mfma_f32_16x16x32_bf16 v[192:195], a[184:187], a[148:151], v[192:195]\n"
    "buffer_load_dwordx4 v248, s[8:11], s40 offen offset:2048 sc1 lds\n"
    "v_mfma_f32_16x16x32_bf16 v[192:195], a[188:191], a[152:155], v[192:195]\n"
    "ds_read2st64_b32 v[222:223], v250 offset0:34 offset1:35\n"
    "v_mfma_f32_16x16x32_bf16 v[184:187], a[188:191], a[120:123], v[184:187]\n"
    "v_mfma_f32_16x16x32_bf16 v[184:187], a[184:187], a[116:119], v[184:187]\n"
    "v_exp_f32_e32 v192, v192\n"
    "v_mfma_f32_16x16x32_bf16 v[176:179], a[184:187], a[84:87], v[176:179]\n"
    "ds_read_b128 a[184:187], v243 offset:34816\n"
    "buffer_load_dwordx4 v248, s[8:11], s40 offen offset:3072 sc1 lds\n"
    "v_mfma_f32_16x16x32_bf16 v[176:179], a[188:191], a[88:91], v[176:179]\n"
    "s_waitcnt lgkmcnt(7)\n"
    "s_add_u32 s40, s40, 32768\n"
    "ds_read_b128 a[188:191], v242 offset:38912\n"
    "v_mfma_f32_16x16x32_bf16 v[180:183], a[160:163], a[92:95], 0\n"
    "s_add_u32 m0, s43, 4096\n"
    "ds_read2st64_b32 v[228:229], v250 offset0:48 offset1:49\n"
    "v_mfma_f32_16x16x32_bf16 v[180:183], a[164:167], a[64:67], v[180:183]\n"
    "v_exp_f32_e32 v193, v193\n"
    "v_mfma_f32_16x16x32_bf16 v[188:191], a[164:167], a[96:99], 0\n"
    "buffer_load_dwordx4 v248, s[8:11], s41 offen offset:0 sc1 lds\n"
    "v_exp_f32_e32 v194, v194\n"
    "v_mfma_f32_16x16x32_bf16 v[188:191], a[160:163], a[124:127], v[188:191]\n"
    "ds_read2st64_b32 v[230:231], v250 offset0:50 offset1:51\n"
    "v_mfma_f32_16x16x32_bf16 v[196:199], a[160:163], a[156:159], 0\n"
    "ds_read_b64_tr_b16 a[160:161], v244 offset:16384\n"
    "v_mfma_f32_16x16x32_bf16 v[196:199], a[164:167], a[128:131], v[196:199]\n"
    "s_waitcnt lgkmcnt(9)\n"
    "ds_read_b64_tr_b16 a[162:163], v244 offset:18432\n"
    "v_mfma_f32_16x16x32_bf16 v[196:199], a[168:171], a[132:135], v[196:199]\n"
    "ds_read_b64_tr_b16 a[164:165], v244 offset:24576\n"
    "buffer_load_dwordx4 v248, s[8:11], s41 offen offset:1024 sc1 lds\n"
    "v_mfma_f32_16x16x32_bf16 v[196:199], a[172:175], a[136:139], v[196:199]\n"
    "ds_read_b64_tr_b16 a[166:167], v244 offset:26624\n"
    "v_mfma_f32_16x16x32_bf16 v[188:191], a[172:175], a[104:107], v[188:191]\n"
    "v_exp_f32_e32 v195, v195\n"
    "v_mfma_f32_16x16x32_bf16 v[188:191], a[168:171], a[100:103], v[188:191]\n"
    "ds_read2st64_b32 v[236:237], v250 offset0:64 offset1:65\n"
    "v_cvt_pk_bf16_f32 v192, v192, v193\n"
    "v_mfma_f32_16x16x32_bf16 v[180:183], a[168:171], a[68:71], v[180:183]\n"
    "ds_read_b64_tr_b16 a[168:169], v245 offset:16384\n"
    "buffer_load_dwordx4 v248, s[8:11], s41 offen offset:2048 sc1 lds\n"
    "v_mfma_f32_16x16x32_bf16 v[180:183], a[172:175], a[72:75], v[180:183]\n"
    "s_waitcnt lgkmcnt(11)\n"
    "ds_read_b64_tr_b16 a[170:171], v245 offset:18432\n"
    "v_mfma_f32_16x16x32_bf16 v[180:183], a[176:179], a[76:79], v[180:183]\n"
    "ds_read_b64_tr_b16 a[172:173], v245 offset:24576\n"
    "v_mfma_f32_16x16x32_bf16 v[180:183], a[180:183], a[80:83], v[180:183]\n"
    "ds_read_b64_tr_b16 a[174:175], v245 offset:26624\n"
    "v_mfma_f32_16x16x32_bf16 v[188:191], a[180:183], a[112:115], v[188:191]\n"
    "buffer_load_dwordx4 v248, s[8:11], s41 offen offset:3072 sc1 lds\n"
    "v_mfma_f32_16x16x32_bf16 v[188:191], a[176:179], a[108:111], v[188:191]\n"
    "s_add_u32 s41, s41, 32768\n"
    "ds_read2st64_b32 v[238:239], v250 offset0:66 offset1:67\n"
    "v_mfma_f32_16x16x32_bf16 v[196:199], a[176:179], a[140:143], v[196:199]\n"
    "s_mov_b32 m0, s49\n"
    "ds_read_b64_tr_b16 a[176:177], v246 offset:16384\n"
    "v_mfma_f32_16x16x32_bf16 v[196:199], a[180:183], a[144:147], v[196:199]\n"
    "s_waitcnt lgkmcnt(13)\n"
    "ds_read_b64_tr_b16 a[178:179], v246 offset:18432\n"
    "v_mfma_f32_16x16x32_bf16 v[196:199], a[184:187], a[148:151], v[196:199]\n"
    "ds_read_b64_tr_b16 a[180:181], v246 offset:24576\n"
    "v_mfma_f32_16x16x32_bf16 v[196:199], a[188:191], a[152:155], v[196:199]\n"
    "ds_read_b64_tr_b16 a[182:183], v246 offset:26624\n"
    "v_mfma_f32_16x16x32_bf16 v[188:191], a[188:191], a[120:123], v[188:191]\n"
    "v_exp_f32_e32 v184, v184\n"
    "v_mfma_f32_16x16x32_bf16 v[188:191], a[184:187], a[116:119], v[188:191]\n"
    "v_exp_f32_e32 v185, v185\n"
    "v_mfma_f32_16x16x32_bf16 v[180:183], a[184:187], a[84:87], v[180:183]\n"
    "ds_read_b64_tr_b16 a[184:185], v247 offset:16384\n"
    "v_cvt_pk_bf16_f32 v193, v194, v195\n"
    "v_mfma_f32_16x16x32_bf16 v[180:183], a[188:191], a[88:91], v[180:183]\n"
    "s_waitcnt lgkmcnt(5)\n"
    "ds_read_b64_tr_b16 a[186:187], v247 offset:18432\n"
    "v_mfma_f32_16x16x32_bf16 v[56:59], a[160:163], v[216:219], v[56:59]\n"
    "ds_read_b64_tr_b16 a[188:189], v247 offset:24576\n"
    "v_mfma_f32_16x16x32_bf16 v[56:59], a[164:167], v[220:223], v[56:59]\n"
    "ds_read_b64_tr_b16 a[190:191], v247 offset:26624\n"
    "v_mfma_f32_16x16x32_bf16 v[60:63], a[164:167], v[228:231], v[60:63]\n"
    "v_exp_f32_e32 v186, v186\n"
    "v_mfma_f32_16x16x32_bf16 v[60:63], a[160:163], v[224:227], v[60:63]\n"
    "v_exp_f32_e32 v187, v187\n"
    "v_mfma_f32_16x16x32_bf16 v[64:67], a[160:163], v[232:235], v[64:67]\n"
    "ds_read_b64_tr_b16 a[160:161], v244 offset:20480\n"
    "v_cvt_pk_bf16_f32 v184, v184, v185\n"
    "v_mfma_f32_16x16x32_bf16 v[64:67], a[164:167], v[236:239], v[64:67]\n"
    "ds_read_b64_tr_b16 a[162:163], v244 offset:22528\n"
    "v_mfma_f32_16x16x32_bf16 v[76:79], a[168:171], v[232:235], v[76:79]\n"
    "ds_read_b64_tr_b16 a[164:165], v244 offset:28672\n"
    "v_mfma_f32_16x16x32_bf16 v[76:79], a[172:175], v[236:239], v[76:79]\n"
    "ds_read_b64_tr_b16 a[166:167], v244 offset:30720\n"
    "v_mfma_f32_16x16x32_bf16 v[72:75], a[172:175], v[228:231], v[72:75]\n"
    "v_exp_f32_e32 v176, v176\n"
    "v_mfma_f32_16x16x32_bf16 v[72:75], a[168:171], v[224:227], v[72:75]\n"
    "v_exp_f32_e32 v177, v177\n"
    "v_mfma_f32_16x16x32_bf16 v[68:71], a[168:171], v[216:219], v[68:71]\n"
    "ds_read_b64_tr_b16 a[168:169], v245 offset:20480\n"
    "v_cvt_pk_bf16_f32 v185, v186, v187\n"
    "v_mfma_f32_16x16x32_bf16 v[68:71], a[172:175], v[220:223], v[68:71]\n"
    "s_waitcnt lgkmcnt(9)\n"
    "ds_read_b64_tr_b16 a[170:171], v245 offset:22528\n"
    "v_mfma_f32_16x16x32_bf16 v[80:83], a[176:179], v[216:219], v[80:83]\n"
    "ds_read_b64_tr_b16 a[172:173], v245 offset:28672\n"
    "v_mfma_f32_16x16x32_bf16 v[80:83], a[180:183], v[220:223], v[80:83]\n"
    "ds_read_b64_tr_b16 a[174:175], v245 offset:30720\n"
    "v_mfma_f32_16x16x32_bf16 v[84:87], a[180:183], v[228:231], v[84:87]\n"
    "v_exp_f32_e32 v178, v178\n"
    "v_mfma_f32_16x16x32_bf16 v[84:87], a[176:179], v[224:227], v[84:87]\n"
    "v_exp_f32_e32 v179, v179\n"
    "v_mfma_f32_16x16x32_bf16 v[88:91], a[176:179], v[232:235], v[88:91]\n"
    "s_waitcnt vmcnt(14)\n"
    "s_barrier\n"
    "v_mfma_f32_16x16x32_bf16 v[88:91], a[180:183], v[236:239], v[88:91]\n"
    "s_waitcnt lgkmcnt(8)\n"
    "ds_read_b64_tr_b16 a[176:177], v246 offset:20480\n"
    "v_cvt_pk_bf16_f32 v176, v176, v177\n"
    "v_mfma_f32_16x16x32_bf16 v[100:103], a[184:187], v[232:235], v[100:103]\n"
    "ds_read_b64_tr_b16 a[178:179], v246 offset:22528\n"
    "v_cvt_pk_bf16_f32 v177, v178, v179\n"
    "v_mfma_f32_16x16x32_bf16 v[100:103], a[188:191], v[236:239], v[100:103]\n"
    "ds_read_b64_tr_b16 a[180:181], v246 offset:28672\n"
    "v_mfma_f32_16x16x32_bf16 v[96:99], a[188:191], v[228:231], v[96:99]\n"
    "ds_read_b64_tr_b16 a[182:183], v246 offset:30720\n"
    "v_mfma_f32_16x16x32_bf16 v[96:99], a[184:187], v[224:227], v[96:99]\n"
    "v_exp_f32_e32 v196, v196\n"
    "v_mfma_f32_16x16x32_bf16 v[92:95], a[184:187], v[216:219], v[92:95]\n"
    "ds_read_b64_tr_b16 a[184:185], v247 offset:20480\n"
    "ds_write_addtid_b32 v192 offset:16384\n"
    "v_mfma_f32_16x16x32_bf16 v[92:95], a[188:191], v[220:223], v[92:95]\n"
    "s_waitcnt lgkmcnt(10)\n"
    "ds_read_b64_tr_b16 a[186:187], v247 offset:22528\n"
    "v_mfma_f32_16x16x32_bf16 v[104:107], a[160:163], v[216:219], v[104:107]\n"
    "ds_read_b64_tr_b16 a[188:189], v247 offset:28672\n"
    "ds_write_addtid_b32 v193 offset:16640\n"
    "v_mfma_f32_16x16x32_bf16 v[104:107], a[164:167], v[220:223], v[104:107]\n"
    "ds_read_b64_tr_b16 a[190:191], v247 offset:30720\n"
    "v_mfma_f32_16x16x32_bf16 v[108:111], a[164:167], v[228:231], v[108:111]\n"
    "v_exp_f32_e32 v197, v197\n"
    "v_mfma_f32_16x16x32_bf16 v[108:111], a[160:163], v[224:227], v[108:111]\n"
    "v_exp_f32_e32 v198, v198\n"
    "v_mfma_f32_16x16x32_bf16 v[112:115], a[160:163], v[232:235], v[112:115]\n"
    "ds_read_b128 a[160:163], v243 offset:53248\n"
    "ds_write_addtid_b32 v184 offset:12288\n"
    "v_mfma_f32_16x16x32_bf16 v[112:115], a[164:167], v[236:239], v[112:115]\n"
    "s_waitcnt lgkmcnt(12)\n"
    "ds_read_b128 a[164:167], v240 offset:49152\n"
    "ds_write_addtid_b32 v185 offset:12544\n"
    "v_mfma_f32_16x16x32_bf16 v[124:127], a[168:171], v[232:235], v[124:127]\n"
    "v_exp_f32_e32 v199, v199\n"
    "v_mfma_f32_16x16x32_bf16 v[124:127], a[172:175], v[236:239], v[124:127]\n"
    "ds_write_addtid_b32 v176 offset:8192\n"
    "v_cvt_pk_bf16_f32 v194, v196, v197\n"
    "v_mfma_f32_16x16x32_bf16 v[120:123], a[172:175], v[228:231], v[120:123]\n"
    "v_exp_f32_e32 v188, v188\n"
    "v_mfma_f32_16x16x32_bf16 v[120:123], a[168:171], v[224:227], v[120:123]\n"
    "v_exp_f32_e32 v189, v189\n"
    "v_mfma_f32_16x16x32_bf16 v[116:119], a[168:171], v[216:219], v[116:119]\n"
    "ds_read_b128 a[168:171], v241 offset:49152\n"
    "v_cvt_pk_bf16_f32 v195, v198, v199\n"
    "v_mfma_f32_16x16x32_bf16 v[116:119], a[172:175], v[220:223], v[116:119]\n"
    "s_waitcnt lgkmcnt(12)\n"
    "ds_read_b128 a[172:175], v240 offset:53248\n"
    "ds_write_addtid_b32 v177 offset:8448\n"
    "v_mfma_f32_16x16x32_bf16 v[128:131], a[176:179], v[216:219], v[128:131]\n"
    "v_exp_f32_e32 v190, v190\n"
    "v_mfma_f32_16x16x32_bf16 v[128:131], a[180:183], v[220:223], v[128:131]\n"
    "v_exp_f32_e32 v191, v191\n"
    "v_mfma_f32_16x16x32_bf16 v[132:135], a[180:183], v[228:231], v[132:135]\n"
    "ds_write_addtid_b32 v194 offset:16896\n"
    "v_cvt_pk_bf16_f32 v186, v188, v189\n"
    "v_mfma_f32_16x16x32_bf16 v[132:135], a[176:179], v[224:227], v[132:135]\n"
    "v_exp_f32_e32 v180, v180\n"
    "v_mfma_f32_16x16x32_bf16 v[136:139], a[176:179], v[232:235], v[136:139]\n"
    "ds_read_b128 a[176:179], v241 offset:53248\n"
    "v_mfma_f32_16x16x32_bf16 v[136:139], a[180:183], v[236:239], v[136:139]\n"
    "s_waitcnt lgkmcnt(10)\n"
    "ds_read_b128 a[180:183], v242 offset:49152\n"
    "v_mfma_f32_16x16x32_bf16 v[148:151], a[184:187], v[232:235], v[148:151]\n"
    "v_exp_f32_e32 v181, v181\n"
    "v_mfma_f32_16x16x32_bf16 v[148:151], a[188:191], v[236:239], v[148:151]\n"
    "ds_write_addtid_b32 v195 offset:17152\n"
    "v_cvt_pk_bf16_f32 v187, v190, v191\n"
    "v_mfma_f32_16x16x32_bf16 v[144:147], a[188:191], v[228:231], v[144:147]\n"
    "v_exp_f32_e32 v182, v182\n"
    "v_mfma_f32_16x16x32_bf16 v[144:147], a[184:187], v[224:227], v[144:147]\n"
    "v_exp_f32_e32 v183, v183\n"
    "v_mfma_f32_16x16x32_bf16 v[140:143], a[184:187], v[216:219], v[140:143]\n"
    "ds_read_b128 a[184:187], v243 offset:49152\n"
    "v_cvt_pk_bf16_f32 v178, v180, v181\n"
    "v_mfma_f32_16x16x32_bf16 v[140:143], a[188:191], v[220:223], v[140:143]\n"
    "s_waitcnt lgkmcnt(10)\n"
    "ds_read_b128 a[188:191], v242 offset:53248\n"
    "v_cvt_pk_bf16_f32 v179, v182, v183\n"
    "v_mfma_f32_16x16x32_bf16 v[216:219], a[160:163], a[92:95], 0\n"
    "ds_write_addtid_b32 v186 offset:12800\n"
    "v_mfma_f32_16x16x32_bf16 v[216:219], a[164:167], a[64:67], v[216:219]\n"
    "ds_write_addtid_b32 v187 offset:13056\n"
    "v_mfma_f32_16x16x32_bf16 v[224:227], a[164:167], a[96:99], 0\n"
    "ds_write_addtid_b32 v178 offset:8704\n"
    "v_mfma_f32_16x16x32_bf16 v[224:227], a[160:163], a[124:127], v[224:227]\n"
    "ds_write_addtid_b32 v179 offset:8960\n"
    "v_mfma_f32_16x16x32_bf16 v[232:235], a[160:163], a[156:159], 0\n"
    "ds_read_b128 a[160:163], v243 offset:55296\n"
    "v_mfma_f32_16x16x32_bf16 v[232:235], a[164:167], a[128:131], v[232:235]\n"
    "s_waitcnt lgkmcnt(12)\n"
    "ds_read_b128 a[164:167], v240 offset:51200\n"
    "v_mfma_f32_16x16x32_bf16 v[232:235], a[168:171], a[132:135], v[232:235]\n"
    "v_mfma_f32_16x16x32_bf16 v[232:235], a[172:175], a[136:139], v[232:235]\n"
    "v_mfma_f32_16x16x32_bf16 v[224:227], a[172:175], a[104:107], v[224:227]\n"
    "s_waitcnt lgkmcnt(2)\n"
    "s_barrier\n"
    "v_mfma_f32_16x16x32_bf16 v[224:227], a[168:171], a[100:103], v[224:227]\n"
    "s_add_u32 m0, s43, 16384\n"
    "v_mfma_f32_16x16x32_bf16 v[216:219], a[168:171], a[68:71], v[216:219]\n"
    "ds_read_b128 a[168:171], v241 offset:51200\n"
    "buffer_load_dwordx4 v248, s[8:11], s40 offen offset:0 sc1 lds\n"
    "v_mfma_f32_16x16x32_bf16 v[216:219], a[172:175], a[72:75], v[216:219]\n"
    "ds_read_b128 a[172:175], v240 offset:55296\n"
    "v_mfma_f32_16x16x32_bf16 v[216:219], a[176:179], a[76:79], v[216:219]\n"
    "ds_read2st64_b32 v[180:181], v250 offset0:32 offset1:33\n"
    "v_mfma_f32_16x16x32_bf16 v[216:219], a[180:183], a[80:83], v[216:219]\n"
    "v_mfma_f32_16x16x32_bf16 v[224:227], a[180:183], a[112:115], v[224:227]\n"
    "buffer_load_dwordx4 v248, s[8:11], s40 offen offset:1024 sc1 lds\n"
    "v_mfma_f32_16x16x32_bf16 v[224:227], a[176:179], a[108:111], v[224:227]\n"
    "v_mfma_f32_16x16x32_bf16 v[232:235], a[176:179], a[140:143], v[232:235]\n"
    "ds_read_b128 a[176:179], v241 offset:55296\n"
    "v_mfma_f32_16x16x32_bf16 v[232:235], a[180:183], a[144:147], v[232:235]\n"
    "ds_read_b128 a[180:183], v242 offset:51200\n"
    "v_mfma_f32_16x16x32_bf16 v[232:235], a[184:187], a[148:151], v[232:235]\n"
    "buffer_load_dwordx4 v248, s[8:11], s40 offen offset:2048 sc1 lds\n"
    "v_mfma_f32_16x16x32_bf16 v[232:235], a[188:191], a[152:155], v[232:235]\n"
    "ds_read2st64_b32 v[182:183], v250 offset0:34 offset1:35\n"
    "v_mfma_f32_16x16x32_bf16 v[224:227], a[188:191], a[120:123], v[224:227]\n"
    "v_mfma_f32_16x16x32_bf16 v[224:227], a[184:187], a[116:119], v[224:227]\n"
    "v_exp_f32_e32 v232, v232\n"
    "v_mfma_f32_16x16x32_bf16 v[216:219], a[184:187], a[84:87], v[216:219]\n"
    "ds_read_b128 a[184:187], v243 offset:51200\n"
    "buffer_load_dwordx4 v248, s[8:11], s40 offen offset:3072 sc1 lds\n"
    "v_mfma_f32_16x16x32_bf16 v[216:219], a[188:191], a[88:91], v[216:219]\n"
    "s_waitcnt lgkmcnt(7)\n"
    "s_add_u32 s40, s40, -131072\n"
    "ds_read_b128 a[188:191], v242 offset:55296\n"
    "v_mfma_f32_16x16x32_bf16 v[220:223], a[160:163], a[92:95], 0\n"
    "s_add_u32 m0, s43, 20480\n"
    "ds_read2st64_b32 v[188:189], v250 offset0:48 offset1:49\n"
    "v_mfma_f32_16x16x32_bf16 v[220:223], a[164:167], a[64:67], v[220:223]\n"
    "v_exp_f32_e32 v233, v233\n"
    "v_mfma_f32_16x16x32_bf16 v[228:231], a[164:167], a[96:99], 0\n"
    "buffer_load_dwordx4 v248, s[8:11], s41 offen offset:0 sc1 lds\n"
    "v_exp_f32_e32 v234, v234\n"
    "v_mfma_f32_16x16x32_bf16 v[228:231], a[160:163], a[124:127], v[228:231]\n"
    "ds_read2st64_b32 v[190:191], v250 offset0:50 offset1:51\n"
    "v_mfma_f32_16x16x32_bf16 v[236:239], a[160:163], a[156:159], 0\n"
    "ds_read_b64_tr_b16 a[160:161], v244 offset:32768\n"
    "v_mfma_f32_16x16x32_bf16 v[236:239], a[164:167], a[128:131], v[236:239]\n"
    "s_waitcnt lgkmcnt(9)\n"
    "ds_read_b64_tr_b16 a[162:163], v244 offset:34816\n"
    "v_mfma_f32_16x16x32_bf16 v[236:239], a[168:171], a[132:135], v[236:239]\n"
    "ds_read_b64_tr_b16 a[164:165], v244 offset:40960\n"
    "buffer_load_dwordx4 v248, s[8:11], s41 offen offset:1024 sc1 lds\n"
    "v_mfma_f32_16x16x32_bf16 v[236:239], a[172:175], a[136:139], v[236:239]\n"
    "ds_read_b64_tr_b16 a[166:167], v244 offset:43008\n"
    "v_mfma_f32_16x16x32_bf16 v[228:231], a[172:175], a[104:107], v[228:231]\n"
    "v_exp_f32_e32 v235, v235\n"
    "v_mfma_f32_16x16x32_bf16 v[228:231], a[168:171], a[100:103], v[228:231]\n"
    "ds_read2st64_b32 v[196:197], v250 offset0:64 offset1:65\n"
    "v_cvt_pk_bf16_f32 v232, v232, v233\n"
    "v_mfma_f32_16x16x32_bf16 v[220:223], a[168:171], a[68:71], v[220:223]\n"
    "ds_read_b64_tr_b16 a[168:169], v245 offset:32768\n"
    "buffer_load_dwordx4 v248, s[8:11], s41 offen offset:2048 sc1 lds\n"
    "v_mfma_f32_16x16x32_bf16 v[220:223], a[172:175], a[72:75], v[220:223]\n"
    "s_waitcnt lgkmcnt(11)\n"
    "ds_read_b64_tr_b16 a[170:171], v245 offset:34816\n"
    "v_mfma_f32_16x16x32_bf16 v[220:223], a[176:179], a[76:79], v[220:223]\n"
    "ds_read_b64_tr_b16 a[172:173], v245 offset:40960\n"
    "v_mfma_f32_16x16x32_bf16 v[220:223], a[180:183], a[80:83], v[220:223]\n"
    "ds_read_b64_tr_b16 a[174:175], v245 offset:43008\n"
    "v_mfma_f32_16x16x32_bf16 v[228:231], a[180:183], a[112:115], v[228:231]\n"
    "buffer_load_dwordx4 v248, s[8:11], s41 offen offset:3072 sc1 lds\n"
    "v_mfma_f32_16x16x32_bf16 v[228:231], a[176:179], a[108:111], v[228:231]\n"
    "s_add_u32 s41, s41, -131072\n"
    "ds_read2st64_b32 v[198:199], v250 offset0:66 offset1:67\n"
    "v_mfma_f32_16x16x32_bf16 v[236:239], a[176:179], a[140:143], v[236:239]\n"
    "s_mov_b32 m0, s49\n"
    "ds_read_b64_tr_b16 a[176:177], v246 offset:32768\n"
    "v_mfma_f32_16x16x32_bf16 v[236:239], a[180:183], a[144:147], v[236:239]\n"
    "s_waitcnt lgkmcnt(13)\n"
    "ds_read_b64_tr_b16 a[178:179], v246 offset:34816\n"
    "v_mfma_f32_16x16x32_bf16 v[236:239], a[184:187], a[148:151], v[236:239]\n"
    "ds_read_b64_tr_b16 a[180:181], v246 offset:40960\n"
    "buffer_load_dword v255, v254, s[8:11], s45 offen offset:0 sc1\n"
    "v_mfma_f32_16x16x32_bf16 v[236:239], a[188:191], a[152:155], v[236:239]\n"
    "ds_read_b64_tr_b16 a[182:183], v246 offset:43008\n"
    "v_mfma_f32_16x16x32_bf16 v[228:231], a[188:191], a[120:123], v[228:231]\n"
    "v_exp_f32_e32 v224, v224\n"
    "v_mfma_f32_16x16x32_bf16 v[228:231], a[184:187], a[116:119], v[228:231]\n"
    "v_exp_f32_e32 v225, v225\n"
    "v_mfma_f32_16x16x32_bf16 v[220:223], a[184:187], a[84:87], v[220:223]\n"
    "ds_read_b64_tr_b16 a[184:185], v247 offset:32768\n"
    "v_cvt_pk_bf16_f32 v233, v234, v235\n"
    "v_mfma_f32_16x16x32_bf16 v[220:223], a[188:191], a[88:91], v[220:223]\n"
    "s_waitcnt lgkmcnt(5)\n"
    "ds_read_b64_tr_b16 a[186:187], v247 offset:34816\n"
    "v_mfma_f32_16x16x32_bf16 v[56:59], a[160:163], v[176:179], v[56:59]\n"
    "ds_read_b64_tr_b16 a[188:189], v247 offset:40960\n"
    "v_mfma_f32_16x16x32_bf16 v[56:59], a[164:167], v[180:183], v[56:59]\n"
    "ds_read_b64_tr_b16 a[190:191], v247 offset:43008\n"
    "v_mfma_f32_16x16x32_bf16 v[60:63], a[164:167], v[188:191], v[60:63]\n"
    "v_exp_f32_e32 v226, v226\n"
    "v_mfma_f32_16x16x32_bf16 v[60:63], a[160:163], v[184:187], v[60:63]\n"
    "v_exp_f32_e32 v227, v227\n"
    "v_mfma_f32_16x16x32_bf16 v[64:67], a[160:163], v[192:195], v[64:67]\n"
    "ds_read_b64_tr_b16 a[160:161], v244 offset:36864\n"
    "v_cvt_pk_bf16_f32 v224, v224, v225\n"
    "v_mfma_f32_16x16x32_bf16 v[64:67], a[164:167], v[196:199], v[64:67]\n"
    "ds_read_b64_tr_b16 a[162:163], v244 offset:38912\n"
    "v_mfma_f32_16x16x32_bf16 v[76:79], a[168:171], v[192:195], v[76:79]\n"
    "ds_read_b64_tr_b16 a[164:165], v244 offset:45056\n"
    "v_mfma_f32_16x16x32_bf16 v[76:79], a[172:175], v[196:199], v[76:79]\n"
    "ds_read_b64_tr_b16 a[166:167], v244 offset:47104\n"
    "v_mfma_f32_16x16x32_bf16 v[72:75], a[172:175], v[188:191], v[72:75]\n"
    "v_exp_f32_e32 v216, v216\n"
    "v_mfma_f32_16x16x32_bf16 v[72:75], a[168:171], v[184:187], v[72:75]\n"
    "v_exp_f32_e32 v217, v217\n"
    "v_mfma_f32_16x16x32_bf16 v[68:71], a[168:171], v[176:179], v[68:71]\n"
    "ds_read_b64_tr_b16 a[168:169], v245 offset:36864\n"
    "v_cvt_pk_bf16_f32 v225, v226, v227\n"
    "v_mfma_f32_16x16x32_bf16 v[68:71], a[172:175], v[180:183], v[68:71]\n"
    "s_waitcnt lgkmcnt(9)\n"
    "ds_read_b64_tr_b16 a[170:171], v245 offset:38912\n"
    "v_mfma_f32_16x16x32_bf16 v[80:83], a[176:179], v[176:179], v[80:83]\n"
    "ds_read_b64_tr_b16 a[172:173], v245 offset:45056\n"
    "v_mfma_f32_16x16x32_bf16 v[80:83], a[180:183], v[180:183], v[80:83]\n"
    "ds_read_b64_tr_b16 a[174:175], v245 offset:47104\n"
    "v_mfma_f32_16x16x32_bf16 v[84:87], a[180:183], v[188:191], v[84:87]\n"
    "v_exp_f32_e32 v218, v218\n"
    "v_mfma_f32_16x16x32_bf16 v[84:87], a[176:179], v[184:187], v[84:87]\n"
    "v_exp_f32_e32 v219, v219\n"
    "v_mfma_f32_16x16x32_bf16 v[88:91], a[176:179], v[192:195], v[88:91]\n"
    "s_waitcnt vmcnt(9)\n"
    "s_barrier\n"
    "v_mfma_f32_16x16x32_bf16 v[88:91], a[180:183], v[196:199], v[88:91]\n"
    "s_waitcnt lgkmcnt(8)\n"
    "ds_read_b64_tr_b16 a[176:177], v246 offset:36864\n"
    "v_cvt_pk_bf16_f32 v216, v216, v217\n"
    "v_mfma_f32_16x16x32_bf16 v[100:103], a[184:187], v[192:195], v[100:103]\n"
    "ds_read_b64_tr_b16 a[178:179], v246 offset:38912\n"
    "v_cvt_pk_bf16_f32 v217, v218, v219\n"
    "v_mfma_f32_16x16x32_bf16 v[100:103], a[188:191], v[196:199], v[100:103]\n"
    "ds_read_b64_tr_b16 a[180:181], v246 offset:45056\n"
    "v_mfma_f32_16x16x32_bf16 v[96:99], a[188:191], v[188:191], v[96:99]\n"
    "ds_read_b64_tr_b16 a[182:183], v246 offset:47104\n"
    "v_mfma_f32_16x16x32_bf16 v[96:99], a[184:187], v[184:187], v[96:99]\n"
    "v_exp_f32_e32 v236, v236\n"
    "v_mfma_f32_16x16x32_bf16 v[92:95], a[184:187], v[176:179], v[92:95]\n"
    "ds_read_b64_tr_b16 a[184:185], v247 offset:36864\n"
    "ds_write_addtid_b32 v232 offset:16384\n"
    "v_mfma_f32_16x16x32_bf16 v[92:95], a[188:191], v[180:183], v[92:95]\n"
    "s_waitcnt lgkmcnt(10)\n"
    "ds_read_b64_tr_b16 a[186:187], v247 offset:38912\n"
    "v_mfma_f32_16x16x32_bf16 v[104:107], a[160:163], v[176:179], v[104:107]\n"
    "ds_read_b64_tr_b16 a[188:189], v247 offset:45056\n"
    "ds_write_addtid_b32 v233 offset:16640\n"
    "v_mfma_f32_16x16x32_bf16 v[104:107], a[164:167], v[180:183], v[104:107]\n"
    "ds_read_b64_tr_b16 a[190:191], v247 offset:47104\n"
    "v_mfma_f32_16x16x32_bf16 v[108:111], a[164:167], v[188:191], v[108:111]\n"
    "v_exp_f32_e32 v237, v237\n"
    "v_mfma_f32_16x16x32_bf16 v[108:111], a[160:163], v[184:187], v[108:111]\n"
    "v_exp_f32_e32 v238, v238\n"
    "v_mfma_f32_16x16x32_bf16 v[112:115], a[160:163], v[192:195], v[112:115]\n"
    "ds_read_b128 a[160:163], v243 offset:4096\n"
    "ds_write_addtid_b32 v224 offset:12288\n"
    "v_mfma_f32_16x16x32_bf16 v[112:115], a[164:167], v[196:199], v[112:115]\n"
    "s_waitcnt lgkmcnt(12)\n"
    "ds_read_b128 a[164:167], v240 offset:0\n"
    "ds_write_addtid_b32 v225 offset:12544\n"
    "v_mfma_f32_16x16x32_bf16 v[124:127], a[168:171], v[192:195], v[124:127]\n"
    "v_exp_f32_e32 v239, v239\n"
    "v_mfma_f32_16x16x32_bf16 v[124:127], a[172:175], v[196:199], v[124:127]\n"
    "ds_write_addtid_b32 v216 offset:8192\n"
    "v_cvt_pk_bf16_f32 v234, v236, v237\n"
    "v_mfma_f32_16x16x32_bf16 v[120:123], a[172:175], v[188:191], v[120:123]\n"
    "v_exp_f32_e32 v228, v228\n"
    "v_mfma_f32_16x16x32_bf16 v[120:123], a[168:171], v[184:187], v[120:123]\n"
    "v_exp_f32_e32 v229, v229\n"
    "v_mfma_f32_16x16x32_bf16 v[116:119], a[168:171], v[176:179], v[116:119]\n"
    "ds_read_b128 a[168:171], v241 offset:0\n"
    "v_cvt_pk_bf16_f32 v235, v238, v239\n"
    "v_mfma_f32_16x16x32_bf16 v[116:119], a[172:175], v[180:183], v[116:119]\n"
    "s_waitcnt lgkmcnt(12)\n"
    "ds_read_b128 a[172:175], v240 offset:4096\n"
    "ds_write_addtid_b32 v217 offset:8448\n"
    "v_mfma_f32_16x16x32_bf16 v[128:131], a[176:179], v[176:179], v[128:131]\n"
    "v_exp_f32_e32 v230, v230\n"
    "v_mfma_f32_16x16x32_bf16 v[128:131], a[180:183], v[180:183], v[128:131]\n"
    "v_exp_f32_e32 v231, v231\n"
    "v_mfma_f32_16x16x32_bf16 v[132:135], a[180:183], v[188:191], v[132:135]\n"
    "ds_write_addtid_b32 v234 offset:16896\n"
    "v_cvt_pk_bf16_f32 v226, v228, v229\n"
    "v_mfma_f32_16x16x32_bf16 v[132:135], a[176:179], v[184:187], v[132:135]\n"
    "v_exp_f32_e32 v220, v220\n"
    "v_mfma_f32_16x16x32_bf16 v[136:139], a[176:179], v[192:195], v[136:139]\n"
    "ds_read_b128 a[176:179], v241 offset:4096\n"
    "v_mfma_f32_16x16x32_bf16 v[136:139], a[180:183], v[196:199], v[136:139]\n"
    "s_waitcnt lgkmcnt(10)\n"
    "ds_read_b128 a[180:183], v242 offset:0\n"
    "ds_write_addtid_b32 v235 offset:17152\n"
    "v_mfma_f32_16x16x32_bf16 v[148:151], a[184:187], v[192:195], v[148:151]\n"
    "v_exp_f32_e32 v221, v221\n"
    "v_mfma_f32_16x16x32_bf16 v[148:151], a[188:191], v[196:199], v[148:151]\n"
    "ds_write_addtid_b32 v226 offset:12800\n"
    "v_cvt_pk_bf16_f32 v227, v230, v231\n"
    "v_mfma_f32_16x16x32_bf16 v[144:147], a[188:191], v[188:191], v[144:147]\n"
    "v_exp_f32_e32 v222, v222\n"
    "v_mfma_f32_16x16x32_bf16 v[144:147], a[184:187], v[184:187], v[144:147]\n"
    "v_exp_f32_e32 v223, v223\n"
    "v_mfma_f32_16x16x32_bf16 v[140:143], a[184:187], v[176:179], v[140:143]\n"
    "ds_read_b128 a[184:187], v243 offset:0\n"
    "v_cvt_pk_bf16_f32 v218, v220, v221\n"
    "v_mfma_f32_16x16x32_bf16 v[140:143], a[188:191], v[180:183], v[140:143]\n"
    "s_waitcnt lgkmcnt(11)\n"
    "ds_read_b128 a[188:191], v242 offset:4096\n"
    "v_cvt_pk_bf16_f32 v219, v222, v223\n"
    "v_mfma_f32_16x16x32_bf16 v[176:179], a[160:163], a[92:95], 0\n"
    "s_waitcnt vmcnt(0)\n"
    "ds_write_addtid_b32 v227 offset:13056\n"
    "v_cmp_ne_u32_e32 vcc, s39, v255\n"
    "v_mfma_f32_16x16x32_bf16 v[176:179], a[164:167], a[64:67], v[176:179]\n"
    "ds_write_addtid_b32 v218 offset:8704\n"
    "v_mfma_f32_16x16x32_bf16 v[184:187], a[164:167], a[96:99], 0\n"
    "s_cbranch_vccnz .Lat_wpoll\n"
    ".Lat_wpoll_ret:\n"
    "v_mfma_f32_16x16x32_bf16 v[184:187], a[160:163], a[124:127], v[184:187]\n"
    "ds_write_addtid_b32 v219 offset:8960\n"
    "v_mfma_f32_16x16x32_bf16 v[192:195], a[160:163], a[156:159], 0\n"
    "ds_read_b128 a[160:163], v243 offset:6144\n"
    "v_mfma_f32_16x16x32_bf16 v[192:195], a[164:167], a[128:131], v[192:195]\n"
    "s_waitcnt lgkmcnt(12)\n"
    "ds_read_b128 a[164:167], v240 offset:2048\n"
    "v_mfma_f32_16x16x32_bf16 v[192:195], a[168:171], a[132:135], v[192:195]\n"
    "v_mfma_f32_16x16x32_bf16 v[192:195], a[172:175], a[136:139], v[192:195]\n"
    "v_mfma_f32_16x16x32_bf16 v[184:187], a[172:175], a[104:107], v[184:187]\n"
    "s_waitcnt lgkmcnt(2)\n"
    "s_barrier\n"
    "v_mfma_f32_16x16x32_bf16 v[184:187], a[168:171], a[100:103], v[184:187]\n"
    "s_add_u32 s61, s47, 5791744\n"
    "v_mfma_f32_16x16x32_bf16 v[176:179], a[168:171], a[68:71], v[176:179]\n"
    "s_add_u32 m0, s49, 20480\n"
    "ds_read_b128 a[168:171], v241 offset:2048\n"
    "v_mfma_f32_16x16x32_bf16 v[176:179], a[172:175], a[72:75], v[176:179]\n"
    "ds_read_b128 a[172:175], v240 offset:6144\n"
    "v_mfma_f32_16x16x32_bf16 v[176:179], a[176:179], a[76:79], v[176:179]\n"
    "buffer_load_dwordx4 v252, s[8:11], s61 offen offset:0 sc1 lds\n"
    "v_mfma_f32_16x16x32_bf16 v[176:179], a[180:183], a[80:83], v[176:179]\n"
    "s_add_u32 s61, s47, 5795840\n"
    "ds_read2st64_b32 v[220:221], v250 offset0:32 offset1:33\n"
    "v_mfma_f32_16x16x32_bf16 v[184:187], a[180:183], a[112:115], v[184:187]\n"
    "s_add_u32 m0, s49, 24576\n"
    "v_mfma_f32_16x16x32_bf16 v[184:187], a[176:179], a[108:111], v[184:187]\n"
    "v_mfma_f32_16x16x32_bf16 v[192:195], a[176:179], a[140:143], v[192:195]\n"
    "ds_read_b128 a[176:179], v241 offset:6144\n"
    "buffer_load_dwordx4 v252, s[8:11], s61 offen offset:0 sc1 lds\n"
    "v_mfma_f32_16x16x32_bf16 v[192:195], a[180:183], a[144:147], v[192:195]\n"
    "s_add_u32 s61, s47, 5799936\n"
    "ds_read_b128 a[180:183], v242 offset:2048\n"
    "v_mfma_f32_16x16x32_bf16 v[192:195], a[184:187], a[148:151], v[192:195]\n"
    "s_add_u32 m0, s49, 28672\n"
    "ds_read2st64_b32 v[222:223], v250 offset0:34 offset1:35\n"
    "v_mfma_f32_16x16x32_bf16 v[192:195], a[188:191], a[152:155], v[192:195]\n"
    "v_mfma_f32_16x16x32_bf16 v[184:187], a[188:191], a[120:123], v[184:187]\n"
    "buffer_load_dwordx4 v252, s[8:11], s61 offen offset:0 sc1 lds\n"
    "v_mfma_f32_16x16x32_bf16 v[184:187], a[184:187], a[116:119], v[184:187]\n"
    "s_mov_b32 m0, s49\n"
    "ds_read2st64_b32 v[228:229], v250 offset0:48 offset1:49\n"
    "v_mfma_f32_16x16x32_bf16 v[176:179], a[184:187], a[84:87], v[176:179]\n"
    "s_add_u32 s48, s57, 30720\n"
    "ds_read_b128 a[184:187], v243 offset:2048\n"
    "v_exp_f32_e32 v192, v192\n"
    "v_mfma_f32_16x16x32_bf16 v[176:179], a[188:191], a[88:91], v[176:179]\n"
    "s_waitcnt lgkmcnt(8)\n"
    "ds_read_b128 a[188:191], v242 offset:6144\n"
    "v_mfma_f32_16x16x32_bf16 v[180:183], a[160:163], a[92:95], 0\n"
    "buffer_load_dwordx4 a[192:195], v253, s[8:11], s48 offen offset:0 sc1\n"
    "v_mfma_f32_16x16x32_bf16 v[180:183], a[164:167], a[64:67], v[180:183]\n"
    "v_exp_f32_e32 v193, v193\n"
    "v_mfma_f32_16x16x32_bf16 v[188:191], a[164:167], a[96:99], 0\n"
    "v_exp_f32_e32 v194, v194\n"
    "v_mfma_f32_16x16x32_bf16 v[188:191], a[160:163], a[124:127], v[188:191]\n"
    "ds_read2st64_b32 v[230:231], v250 offset0:50 offset1:51\n"
    "v_exp_f32_e32 v195, v195\n"
    "v_mfma_f32_16x16x32_bf16 v[196:199], a[160:163], a[156:159], 0\n"
    "ds_read_b64_tr_b16 a[160:161], v244 offset:49152\n"
    "buffer_load_dwordx4 a[196:199], v253, s[8:11], s48 offen offset:1024 sc1\n"
    "v_mfma_f32_16x16x32_bf16 v[196:199], a[164:167], a[128:131], v[196:199]\n"
    "s_waitcnt lgkmcnt(9)\n"
    "s_add_u32 s48, s57, 14336\n"
    "ds_read_b64_tr_b16 a[162:163], v244 offset:51200\n"
    "v_mfma_f32_16x16x32_bf16 v[196:199], a[168:171], a[132:135], v[196:199]\n"
    "ds_read_b64_tr_b16 a[164:165], v244 offset:57344\n"
    "v_cvt_pk_bf16_f32 v192, v192, v193\n"
    "v_mfma_f32_16x16x32_bf16 v[196:199], a[172:175], a[136:139], v[196:199]\n"
    "ds_read_b64_tr_b16 a[166:167], v244 offset:59392\n"
    "v_mfma_f32_16x16x32_bf16 v[188:191], a[172:175], a[104:107], v[188:191]\n"
    "buffer_load_dwordx4 a[200:203], v253, s[8:11], s48 offen offset:0 sc1\n"
    "v_mfma_f32_16x16x32_bf16 v[188:191], a[168:171], a[100:103], v[188:191]\n"
    "ds_read2st64_b32 v[236:237], v250 offset0:64 offset1:65\n"
    "v_mfma_f32_16x16x32_bf16 v[180:183], a[168:171], a[68:71], v[180:183]\n"
    "ds_read_b64_tr_b16 a[168:169], v245 offset:49152\n"
    "v_mfma_f32_16x16x32_bf16 v[180:183], a[172:175], a[72:75], v[180:183]\n"
    "s_waitcnt lgkmcnt(11)\n"
    "ds_read_b64_tr_b16 a[170:171], v245 offset:51200\n"
    "v_mfma_f32_16x16x32_bf16 v[180:183], a[176:179], a[76:79], v[180:183]\n"
    "ds_read_b64_tr_b16 a[172:173], v245 offset:57344\n"
    "buffer_load_dwordx4 a[204:207], v253, s[8:11], s48 offen offset:1024 sc1\n"
    "v_mfma_f32_16x16x32_bf16 v[180:183], a[180:183], a[80:83], v[180:183]\n"
    "s_add_u32 s48, s57, 16384\n"
    "ds_read_b64_tr_b16 a[174:175], v245 offset:59392\n"
    "v_mfma_f32_16x16x32_bf16 v[188:191], a[180:183], a[112:115], v[188:191]\n"
    "ds_read2st64_b32 v[238:239], v250 offset0:66 offset1:67\n"
    "v_mfma_f32_16x16x32_bf16 v[188:191], a[176:179], a[108:111], v[188:191]\n"
    "v_exp_f32_e32 v184, v184\n"
    "v_mfma_f32_16x16x32_bf16 v[196:199], a[176:179], a[140:143], v[196:199]\n"
    "ds_read_b64_tr_b16 a[176:177], v246 offset:49152\n"
    "buffer_load_dwordx4 a[208:211], v253, s[8:11], s48 offen offset:0 sc1\n"
    "v_mfma_f32_16x16x32_bf16 v[196:199], a[180:183], a[144:147], v[196:199]\n"
    "s_waitcnt lgkmcnt(12)\n"
    "ds_read_b64_tr_b16 a[178:179], v246 offset:51200\n"
    "v_mfma_f32_16x16x32_bf16 v[196:199], a[184:187], a[148:151], v[196:199]\n"
    "ds_read_b64_tr_b16 a[180:181], v246 offset:57344\n"
    "v_mfma_f32_16x16x32_bf16 v[196:199], a[188:191], a[152:155], v[196:199]\n"
    "ds_read_b64_tr_b16 a[182:183], v246 offset:59392\n"
    "v_mfma_f32_16x16x32_bf16 v[188:191], a[188:191], a[120:123], v[188:191]\n"
    "buffer_load_dwordx4 a[212:215], v253, s[8:11], s48 offen offset:1024 sc1\n"
    "v_mfma_f32_16x16x32_bf16 v[188:191], a[184:187], a[116:119], v[188:191]\n"
    "s_add_u32 s48, s57, 0\n"
    "v_exp_f32_e32 v185, v185\n"
    "v_mfma_f32_16x16x32_bf16 v[180:183], a[184:187], a[84:87], v[180:183]\n"
    "ds_read_b64_tr_b16 a[184:185], v247 offset:49152\n"
    "v_cvt_pk_bf16_f32 v193, v194, v195\n"
    "v_mfma_f32_16x16x32_bf16 v[180:183], a[188:191], a[88:91], v[180:183]\n"
    "s_waitcnt lgkmcnt(5)\n"
    "ds_read_b64_tr_b16 a[186:187], v247 offset:51200\n"
    "v_mfma_f32_16x16x32_bf16 v[56:59], a[160:163], v[216:219], v[56:59]\n"
    "ds_read_b64_tr_b16 a[188:189], v247 offset:57344\n"
    "buffer_load_dwordx4 a[216:219], v253, s[8:11], s48 offen offset:0 sc1\n"
    "v_mfma_f32_16x16x32_bf16 v[56:59], a[164:167], v[220:223], v[56:59]\n"
    "ds_read_b64_tr_b16 a[190:191], v247 offset:59392\n"
    "v_mfma_f32_16x16x32_bf16 v[60:63], a[164:167], v[228:231], v[60:63]\n"
    "v_exp_f32_e32 v186, v186\n"
    "v_mfma_f32_16x16x32_bf16 v[60:63], a[160:163], v[224:227], v[60:63]\n"
    "v_exp_f32_e32 v187, v187\n"
    "v_mfma_f32_16x16x32_bf16 v[64:67], a[160:163], v[232:235], v[64:67]\n"
    "ds_read_b64_tr_b16 a[160:161], v244 offset:53248\n"
    "buffer_load_dwordx4 a[220:223], v253, s[8:11], s48 offen offset:1024 sc1\n"
    "v_mfma_f32_16x16x32_bf16 v[64:67], a[164:167], v[236:239], v[64:67]\n"
    "ds_read_b64_tr_b16 a[162:163], v244 offset:55296\n"
    "v_cvt_pk_bf16_f32 v184, v184, v185\n"
    "v_mfma_f32_16x16x32_bf16 v[76:79], a[168:171], v[232:235], v[76:79]\n"
    "ds_read_b64_tr_b16 a[164:165], v244 offset:61440\n"
    "v_exp_f32_e32 v176, v176\n"
    "v_mfma_f32_16x16x32_bf16 v[76:79], a[172:175], v[236:239], v[76:79]\n"
    "s_add_u32 s61, s28, 24576\n"
    "ds_read_b64_tr_b16 a[166:167], v244 offset:63488\n"
    "v_mfma_f32_16x16x32_bf16 v[72:75], a[172:175], v[228:231], v[72:75]\n"
    "v_and_b32_e32 v254, 0x80, v253\n"
    "v_mfma_f32_16x16x32_bf16 v[72:75], a[168:171], v[224:227], v[72:75]\n"
    "v_exp_f32_e32 v177, v177\n"
    "v_mfma_f32_16x16x32_bf16 v[68:71], a[168:171], v[216:219], v[68:71]\n"
    "ds_read_b64_tr_b16 a[168:169], v245 offset:53248\n"
    "buffer_load_dword v249, v254, s[8:11], s61 offen offset:0 sc1\n"
    "v_mfma_f32_16x16x32_bf16 v[68:71], a[172:175], v[220:223], v[68:71]\n"
    "s_waitcnt lgkmcnt(9)\n"
    "s_add_u32 s61, s29, 24576\n"
    "ds_read_b64_tr_b16 a[170:171], v245 offset:55296\n"
    "v_mfma_f32_16x16x32_bf16 v[80:83], a[176:179], v[216:219], v[80:83]\n"
    "ds_read_b64_tr_b16 a[172:173], v245 offset:61440\n"
    "v_cvt_pk_bf16_f32 v185, v186, v187\n"
    "v_mfma_f32_16x16x32_bf16 v[80:83], a[180:183], v[220:223], v[80:83]\n"
    "ds_read_b64_tr_b16 a[174:175], v245 offset:63488\n"
    "buffer_load_dword v249, v254, s[8:11], s61 offen offset:0 sc1\n"
    "v_mfma_f32_16x16x32_bf16 v[84:87], a[180:183], v[228:231], v[84:87]\n"
    "s_add_u32 s61, s30, 24576\n"
    "v_exp_f32_e32 v178, v178\n"
    "v_mfma_f32_16x16x32_bf16 v[84:87], a[176:179], v[224:227], v[84:87]\n"
    "v_exp_f32_e32 v179, v179\n"
    "v_mfma_f32_16x16x32_bf16 v[88:91], a[176:179], v[232:235], v[88:91]\n"
    "s_waitcnt vmcnt(14)\n"
    "s_barrier\n"
    "v_mfma_f32_16x16x32_bf16 v[88:91], a[180:183], v[236:239], v[88:91]\n"
    "s_waitcnt lgkmcnt(8)\n"
    "ds_read_b64_tr_b16 a[176:177], v246 offset:53248\n"
    "buffer_load_dword v249, v254, s[8:11], s61 offen offset:0 sc1\n"
    "v_mfma_f32_16x16x32_bf16 v[100:103], a[184:187], v[232:235], v[100:103]\n"
    "s_add_u32 s61, s31, 24576\n"
    "ds_read_b64_tr_b16 a[178:179], v246 offset:55296\n"
    "v_cvt_pk_bf16_f32 v176, v176, v177\n"
    "v_mfma_f32_16x16x32_bf16 v[100:103], a[188:191], v[236:239], v[100:103]\n"
    "ds_read_b64_tr_b16 a[180:181], v246 offset:61440\n"
    "v_cvt_pk_bf16_f32 v177, v178, v179\n"
    "v_mfma_f32_16x16x32_bf16 v[96:99], a[188:191], v[228:231], v[96:99]\n"
    "ds_read_b64_tr_b16 a[182:183], v246 offset:63488\n"
    "buffer_load_dword v249, v254, s[8:11], s61 offen offset:0 sc1\n"
    "v_mfma_f32_16x16x32_bf16 v[96:99], a[184:187], v[224:227], v[96:99]\n"
    "v_exp_f32_e32 v196, v196\n"
    "v_mfma_f32_16x16x32_bf16 v[92:95], a[184:187], v[216:219], v[92:95]\n"
    "ds_read_b64_tr_b16 a[184:185], v247 offset:53248\n"
    "ds_write_addtid_b32 v192 offset:16384\n"
    "v_mfma_f32_16x16x32_bf16 v[92:95], a[188:191], v[220:223], v[92:95]\n"
    "s_waitcnt lgkmcnt(10)\n"
    "ds_read_b64_tr_b16 a[186:187], v247 offset:55296\n"
    "ds_write_addtid_b32 v193 offset:16640\n"
    "v_mfma_f32_16x16x32_bf16 v[104:107], a[160:163], v[216:219], v[104:107]\n"
    "ds_read_b64_tr_b16 a[188:189], v247 offset:61440\n"
    "buffer_load_dword v249, v254, s[8:11], s87 offen offset:0 sc1\n"
    "v_mfma_f32_16x16x32_bf16 v[104:107], a[164:167], v[220:223], v[104:107]\n"
    "ds_read_b64_tr_b16 a[190:191], v247 offset:63488\n"
    "ds_write_addtid_b32 v184 offset:12288\n"
    "v_mfma_f32_16x16x32_bf16 v[108:111], a[164:167], v[228:231], v[108:111]\n"
    "v_exp_f32_e32 v197, v197\n"
    "v_mfma_f32_16x16x32_bf16 v[108:111], a[160:163], v[224:227], v[108:111]\n"
    "v_exp_f32_e32 v198, v198\n"
    "v_mfma_f32_16x16x32_bf16 v[112:115], a[160:163], v[232:235], v[112:115]\n"
    "ds_read_b128 a[160:163], v243 offset:20480\n"
    "ds_write_addtid_b32 v185 offset:12544\n"
    "v_mfma_f32_16x16x32_bf16 v[112:115], a[164:167], v[236:239], v[112:115]\n"
    "s_waitcnt lgkmcnt(13)\n"
    "ds_read_b128 a[164:167], v240 offset:16384\n"
    "v_mfma_f32_16x16x32_bf16 v[124:127], a[168:171], v[232:235], v[124:127]\n"
    "v_exp_f32_e32 v199, v199\n"
    "v_mfma_f32_16x16x32_bf16 v[124:127], a[172:175], v[236:239], v[124:127]\n"
    "ds_write_addtid_b32 v176 offset:8192\n"
    "v_cvt_pk_bf16_f32 v194, v196, v197\n"
    "v_mfma_f32_16x16x32_bf16 v[120:123], a[172:175], v[228:231], v[120:123]\n"
    "v_exp_f32_e32 v188, v188\n"
    "v_mfma_f32_16x16x32_bf16 v[120:123], a[168:171], v[224:227], v[120:123]\n"
    "v_exp_f32_e32 v189, v189\n"
    "v_mfma_f32_16x16x32_bf16 v[116:119], a[168:171], v[216:219], v[116:119]\n"
    "ds_read_b128 a[168:171], v241 offset:16384\n"
    "v_cvt_pk_bf16_f32 v195, v198, v199\n"
    "v_mfma_f32_16x16x32_bf16 v[116:119], a[172:175], v[220:223], v[116:119]\n"
    "s_waitcnt lgkmcnt(12)\n"
    "ds_read_b128 a[172:175], v240 offset:20480\n"
    "ds_write_addtid_b32 v177 offset:8448\n"
    "v_mfma_f32_16x16x32_bf16 v[128:131], a[176:179], v[216:219], v[128:131]\n"
    "v_exp_f32_e32 v190, v190\n"
    "v_mfma_f32_16x16x32_bf16 v[128:131], a[180:183], v[220:223], v[128:131]\n"
    "v_exp_f32_e32 v191, v191\n"
    "v_mfma_f32_16x16x32_bf16 v[132:135], a[180:183], v[228:231], v[132:135]\n"
    "ds_write_addtid_b32 v194 offset:16896\n"
    "v_cvt_pk_bf16_f32 v186, v188, v189\n"
    "v_mfma_f32_16x16x32_bf16 v[132:135], a[176:179], v[224:227], v[132:135]\n"
    "v_exp_f32_e32 v180, v180\n"
    "v_mfma_f32_16x16x32_bf16 v[136:139], a[176:179], v[232:235], v[136:139]\n"
    "ds_read_b128 a[176:179], v241 offset:20480\n"
    "v_mfma_f32_16x16x32_bf16 v[136:139], a[180:183], v[236:239], v[136:139]\n"
    "s_waitcnt lgkmcnt(10)\n"
    "ds_read_b128 a[180:183], v242 offset:16384\n"
    "v_mfma_f32_16x16x32_bf16 v[148:151], a[184:187], v[232:235], v[148:151]\n"
    "v_exp_f32_e32 v181, v181\n"
    "v_mfma_f32_16x16x32_bf16 v[148:151], a[188:191], v[236:239], v[148:151]\n"
    "ds_write_addtid_b32 v195 offset:17152\n"
    "v_cvt_pk_bf16_f32 v187, v190, v191\n"
    "v_mfma_f32_16x16x32_bf16 v[144:147], a[188:191], v[228:231], v[144:147]\n"
    "v_exp_f32_e32 v182, v182\n"
    "v_mfma_f32_16x16x32_bf16 v[144:147], a[184:187], v[224:227], v[144:147]\n"
    "v_exp_f32_e32 v183, v183\n"
    "v_mfma_f32_16x16x32_bf16 v[140:143], a[184:187], v[216:219], v[140:143]\n"
    "ds_read_b128 a[184:187], v243 offset:16384\n"
    "v_cvt_pk_bf16_f32 v178, v180, v181\n"
    "v_mfma_f32_16x16x32_bf16 v[140:143], a[188:191], v[220:223], v[140:143]\n"
    "s_waitcnt lgkmcnt(9)\n"
    "ds_read_b128 a[188:191], v242 offset:20480\n"
    "v_cvt_pk_bf16_f32 v179, v182, v183\n"
    "v_mfma_f32_16x16x32_bf16 v[216:219], a[160:163], a[92:95], 0\n"
    "ds_write_addtid_b32 v186 offset:12800\n"
    "v_mfma_f32_16x16x32_bf16 v[216:219], a[164:167], a[64:67], v[216:219]\n"
    "ds_write_addtid_b32 v187 offset:13056\n"
    "v_mfma_f32_16x16x32_bf16 v[224:227], a[164:167], a[96:99], 0\n"
    "ds_write_addtid_b32 v178 offset:8704\n"
    "v_mfma_f32_16x16x32_bf16 v[224:227], a[160:163], a[124:127], v[224:227]\n"
    "ds_write_addtid_b32 v179 offset:8960\n"
    "v_mfma_f32_16x16x32_bf16 v[232:235], a[160:163], a[156:159], 0\n"
    "ds_read_b128 a[160:163], v243 offset:22528\n"
    "v_mfma_f32_16x16x32_bf16 v[232:235], a[164:167], a[128:131], v[232:235]\n"
    "s_waitcnt lgkmcnt(12)\n"
    "ds_read_b128 a[164:167], v240 offset:18432\n"
    "v_mfma_f32_16x16x32_bf16 v[232:235], a[168:171], a[132:135], v[232:235]\n"
    "v_mfma_f32_16x16x32_bf16 v[232:235], a[172:175], a[136:139], v[232:235]\n"
    "v_mfma_f32_16x16x32_bf16 v[224:227], a[172:175], a[104:107], v[224:227]\n"
    "s_waitcnt lgkmcnt(2)\n"
    "s_barrier\n"
    "v_mfma_f32_16x16x32_bf16 v[224:227], a[168:171], a[100:103], v[224:227]\n"
    "v_mfma_f32_16x16x32_bf16 v[216:219], a[168:171], a[68:71], v[216:219]\n"
    "ds_read_b128 a[168:171], v241 offset:18432\n"
    "v_mfma_f32_16x16x32_bf16 v[216:219], a[172:175], a[72:75], v[216:219]\n"
    "ds_read_b128 a[172:175], v240 offset:22528\n"
    "v_mfma_f32_16x16x32_bf16 v[216:219], a[176:179], a[76:79], v[216:219]\n"
    "ds_read2st64_b32 v[180:181], v250 offset0:32 offset1:33\n"
    "v_mfma_f32_16x16x32_bf16 v[216:219], a[180:183], a[80:83], v[216:219]\n"
    "v_mfma_f32_16x16x32_bf16 v[224:227], a[180:183], a[112:115], v[224:227]\n"
    "v_mfma_f32_16x16x32_bf16 v[224:227], a[176:179], a[108:111], v[224:227]\n"
    "v_mfma_f32_16x16x32_bf16 v[232:235], a[176:179], a[140:143], v[232:235]\n"
    "ds_read_b128 a[176:179], v241 offset:22528\n"
    "v_mfma_f32_16x16x32_bf16 v[232:235], a[180:183], a[144:147], v[232:235]\n"
    "ds_read_b128 a[180:183], v242 offset:18432\n"
    "v_mfma_f32_16x16x32_bf16 v[232:235], a[184:187], a[148:151], v[232:235]\n"
    "ds_read2st64_b32 v[182:183], v250 offset0:34 offset1:35\n"
    "v_mfma_f32_16x16x32_bf16 v[232:235], a[188:191], a[152:155], v[232:235]\n"
    "v_mfma_f32_16x16x32_bf16 v[224:227], a[188:191], a[120:123], v[224:227]\n"
    "v_mfma_f32_16x16x32_bf16 v[224:227], a[184:187], a[116:119], v[224:227]\n"
    "ds_read2st64_b32 v[188:189], v250 offset0:48 offset1:49\n"
    "v_mfma_f32_16x16x32_bf16 v[216:219], a[184:187], a[84:87], v[216:219]\n"
    "ds_read_b128 a[184:187], v243 offset:18432\n"
    "v_mfma_f32_16x16x32_bf16 v[216:219], a[188:191], a[88:91], v[216:219]\n"
    "s_waitcnt lgkmcnt(8)\n"
    "ds_read_b128 a[188:191], v242 offset:22528\n"
    "v_mfma_f32_16x16x32_bf16 v[220:223], a[160:163], a[92:95], 0\n"
    "v_exp_f32_e32 v232, v232\n"
    "v_mfma_f32_16x16x32_bf16 v[220:223], a[164:167], a[64:67], v[220:223]\n"
    "v_exp_f32_e32 v233, v233\n"
    "v_mfma_f32_16x16x32_bf16 v[228:231], a[164:167], a[96:99], 0\n"
    "s_add_u32 s48, s57, 18432\n"
    "v_exp_f32_e32 v234, v234\n"
    "v_mfma_f32_16x16x32_bf16 v[228:231], a[160:163], a[124:127], v[228:231]\n"
    "ds_read2st64_b32 v[190:191], v250 offset0:50 offset1:51\n"
    "v_mfma_f32_16x16x32_bf16 v[236:239], a[160:163], a[156:159], 0\n"
    "ds_read_b64_tr_b16 a[160:161], v244 offset:0\n"
    "v_exp_f32_e32 v235, v235\n"
    "v_mfma_f32_16x16x32_bf16 v[236:239], a[164:167], a[128:131], v[236:239]\n"
    "s_waitcnt lgkmcnt(9)\n"
    "ds_read_b64_tr_b16 a[162:163], v244 offset:2048\n"
    "v_mfma_f32_16x16x32_bf16 v[236:239], a[168:171], a[132:135], v[236:239]\n"
    "ds_read_b64_tr_b16 a[164:165], v244 offset:8192\n"
    "buffer_load_dwordx4 a[92:95], v253, s[8:11], s48 offen offset:0 sc1\n"
    "v_mfma_f32_16x16x32_bf16 v[236:239], a[172:175], a[136:139], v[236:239]\n"
    "ds_read_b64_tr_b16 a[166:167], v244 offset:10240\n"
    "v_cvt_pk_bf16_f32 v232, v232, v233\n"
    "v_mfma_f32_16x16x32_bf16 v[228:231], a[172:175], a[104:107], v[228:231]\n"
    "ds_read2st64_b32 v[196:197], v250 offset0:64 offset1:65\n"
    "v_mfma_f32_16x16x32_bf16 v[228:231], a[168:171], a[100:103], v[228:231]\n"
    "v_exp_f32_e32 v224, v224\n"
    "v_mfma_f32_16x16x32_bf16 v[220:223], a[168:171], a[68:71], v[220:223]\n"
    "ds_read_b64_tr_b16 a[168:169], v245 offset:0\n"
    "buffer_load_dwordx4 a[124:127], v253, s[8:11], s48 offen offset:1024 sc1\n"
    "v_mfma_f32_16x16x32_bf16 v[220:223], a[172:175], a[72:75], v[220:223]\n"
    "s_waitcnt lgkmcnt(11)\n"
    "s_add_u32 s48, s57, 2048\n"
    "ds_read_b64_tr_b16 a[170:171], v245 offset:2048\n"
    "v_mfma_f32_16x16x32_bf16 v[220:223], a[176:179], a[76:79], v[220:223]\n"
    "ds_read_b64_tr_b16 a[172:173], v245 offset:8192\n"
    "v_exp_f32_e32 v225, v225\n"
    "v_mfma_f32_16x16x32_bf16 v[220:223], a[180:183], a[80:83], v[220:223]\n"
    "ds_read_b64_tr_b16 a[174:175], v245 offset:10240\n"
    "v_cvt_pk_bf16_f32 v233, v234, v235\n"
    "v_mfma_f32_16x16x32_bf16 v[228:231], a[180:183], a[112:115], v[228:231]\n"
    "buffer_load_dwordx4 a[156:159], v253, s[8:11], s48 offen offset:0 sc1\n"
    "v_mfma_f32_16x16x32_bf16 v[228:231], a[176:179], a[108:111], v[228:231]\n"
    "ds_read2st64_b32 v[198:199], v250 offset0:66 offset1:67\n"
    "v_exp_f32_e32 v226, v226\n"
    "v_mfma_f32_16x16x32_bf16 v[236:239], a[176:179], a[140:143], v[236:239]\n"
    "ds_read_b64_tr_b16 a[176:177], v246 offset:0\n"
    "v_mfma_f32_16x16x32_bf16 v[236:239], a[180:183], a[144:147], v[236:239]\n"
    "s_waitcnt lgkmcnt(12)\n"
    "ds_read_b64_tr_b16 a[178:179], v246 offset:2048\n"
    "v_exp_f32_e32 v227, v227\n"
    "v_mfma_f32_16x16x32_bf16 v[236:239], a[184:187], a[148:151], v[236:239]\n"
    "ds_read_b64_tr_b16 a[180:181], v246 offset:8192\n"
    "buffer_load_dwordx4 a[64:67], v253, s[8:11], s48 offen offset:1024 sc1\n"
    "v_mfma_f32_16x16x32_bf16 v[236:239], a[188:191], a[152:155], v[236:239]\n"
    "s_add_u32 s48, s57, 20480\n"
    "ds_read_b64_tr_b16 a[182:183], v246 offset:10240\n"
    "v_cvt_pk_bf16_f32 v224, v224, v225\n"
    "v_mfma_f32_16x16x32_bf16 v[228:231], a[188:191], a[120:123], v[228:231]\n"
    "v_exp_f32_e32 v216, v216\n"
    "v_mfma_f32_16x16x32_bf16 v[228:231], a[184:187], a[116:119], v[228:231]\n"
    "v_exp_f32_e32 v217, v217\n"
    "v_mfma_f32_16x16x32_bf16 v[220:223], a[184:187], a[84:87], v[220:223]\n"
    "ds_read_b64_tr_b16 a[184:185], v247 offset:0\n"
    "buffer_load_dwordx4 a[96:99], v253, s[8:11], s48 offen offset:0 sc1\n"
    "v_mfma_f32_16x16x32_bf16 v[220:223], a[188:191], a[88:91], v[220:223]\n"
    "s_waitcnt lgkmcnt(5)\n"
    "ds_read_b64_tr_b16 a[186:187], v247 offset:2048\n"
    "v_mfma_f32_16x16x32_bf16 v[56:59], a[160:163], v[176:179], v[56:59]\n"
    "ds_read_b64_tr_b16 a[188:189], v247 offset:8192\n"
    "v_mfma_f32_16x16x32_bf16 v[56:59], a[164:167], v[180:183], v[56:59]\n"
    "ds_read_b64_tr_b16 a[190:191], v247 offset:10240\n"
    "v_mfma_f32_16x16x32_bf16 v[60:63], a[164:167], v[188:191], v[60:63]\n"
    "buffer_load_dwordx4 a[128:131], v253, s[8:11], s48 offen offset:1024 sc1\n"
    "v_cvt_pk_bf16_f32 v225, v226, v227\n"
    "v_mfma_f32_16x16x32_bf16 v[60:63], a[160:163], v[184:187], v[60:63]\n"
    "s_add_u32 s48, s57, 4096\n"
    "v_exp_f32_e32 v218, v218\n"
    "v_mfma_f32_16x16x32_bf16 v[64:67], a[160:163], v[192:195], v[64:67]\n"
    "ds_read_b64_tr_b16 a[160:161], v244 offset:4096\n"
    "v_mfma_f32_16x16x32_bf16 v[64:67], a[164:167], v[196:199], v[64:67]\n"
    "ds_read_b64_tr_b16 a[162:163], v244 offset:6144\n"
    "v_mfma_f32_16x16x32_bf16 v[76:79], a[168:171], v[192:195], v[76:79]\n"
    "ds_read_b64_tr_b16 a[164:165], v244 offset:12288\n"
    "buffer_load_dwordx4 a[68:71], v253, s[8:11], s48 offen offset:0 sc1\n"
    "v_mfma_f32_16x16x32_bf16 v[76:79], a[172:175], v[196:199], v[76:79]\n"
    "ds_read_b64_tr_b16 a[166:167], v244 offset:14336\n"
    "v_mfma_f32_16x16x32_bf16 v[72:75], a[172:175], v[188:191], v[72:75]\n"
    "s_barrier\n"
    "v_mfma_f32_16x16x32_bf16 v[72:75], a[168:171], v[184:187], v[72:75]\n"
    "v_exp_f32_e32 v219, v219\n"
    "v_mfma_f32_16x16x32_bf16 v[68:71], a[168:171], v[176:179], v[68:71]\n"
    "ds_read_b64_tr_b16 a[168:169], v245 offset:4096\n"
    "buffer_load_dwordx4 a[100:103], v253, s[8:11], s48 offen offset:1024 sc1\n"
    "v_mfma_f32_16x16x32_bf16 v[68:71], a[172:175], v[180:183], v[68:71]\n"
    "s_waitcnt lgkmcnt(9)\n"
    "s_add_u32 s48, s57, 22528\n"
    "ds_read_b64_tr_b16 a[170:171], v245 offset:6144\n"
    "v_mfma_f32_16x16x32_bf16 v[80:83], a[176:179], v[176:179], v[80:83]\n"
    "ds_read_b64_tr_b16 a[172:173], v245 offset:12288\n"
    "v_cvt_pk_bf16_f32 v216, v216, v217\n"
    "v_mfma_f32_16x16x32_bf16 v[80:83], a[180:183], v[180:183], v[80:83]\n"
    "ds_read_b64_tr_b16 a[174:175], v245 offset:14336\n"
    "v_cvt_pk_bf16_f32 v217, v218, v219\n"
    "v_mfma_f32_16x16x32_bf16 v[84:87], a[180:183], v[188:191], v[84:87]\n"
    "buffer_load_dwordx4 a[132:135], v253, s[8:11], s48 offen offset:0 sc1\n"
    "ds_write_addtid_b32 v232 offset:16384\n"
    "v_mfma_f32_16x16x32_bf16 v[84:87], a[176:179], v[184:187], v[84:87]\n"
    "v_exp_f32_e32 v236, v236\n"
    "v_mfma_f32_16x16x32_bf16 v[88:91], a[176:179], v[192:195], v[88:91]\n"
    "ds_read_b64_tr_b16 a[176:177], v246 offset:4096\n"
    "ds_write_addtid_b32 v233 offset:16640\n"
    "v_mfma_f32_16x16x32_bf16 v[88:91], a[180:183], v[196:199], v[88:91]\n"
    "s_waitcnt lgkmcnt(11)\n"
    "ds_read_b64_tr_b16 a[178:179], v246 offset:6144\n"
    "v_mfma_f32_16x16x32_bf16 v[100:103], a[184:187], v[192:195], v[100:103]\n"
    "ds_read_b64_tr_b16 a[180:181], v246 offset:12288\n"
    "buffer_load_dwordx4 a[72:75], v253, s[8:11], s48 offen offset:1024 sc1\n"
    "v_mfma_f32_16x16x32_bf16 v[100:103], a[188:191], v[196:199], v[100:103]\n"
    "s_add_u32 s48, s57, 6144\n"
    "ds_read_b64_tr_b16 a[182:183], v246 offset:14336\n"
    "ds_write_addtid_b32 v224 offset:12288\n"
    "v_mfma_f32_16x16x32_bf16 v[96:99], a[188:191], v[188:191], v[96:99]\n"
    "v_exp_f32_e32 v237, v237\n"
    "v_mfma_f32_16x16x32_bf16 v[96:99], a[184:187], v[184:187], v[96:99]\n"
    "v_exp_f32_e32 v238, v238\n"
    "v_mfma_f32_16x16x32_bf16 v[92:95], a[184:187], v[176:179], v[92:95]\n"
    "ds_read_b64_tr_b16 a[184:185], v247 offset:4096\n"
    "buffer_load_dwordx4 a[104:107], v253, s[8:11], s48 offen offset:0 sc1\n"
    "v_mfma_f32_16x16x32_bf16 v[92:95], a[188:191], v[180:183], v[92:95]\n"
    "s_waitcnt lgkmcnt(12)\n"
    "ds_read_b64_tr_b16 a[186:187], v247 offset:6144\n"
    "ds_write_addtid_b32 v225 offset:12544\n"
    "v_mfma_f32_16x16x32_bf16 v[104:107], a[160:163], v[176:179], v[104:107]\n"
    "ds_read_b64_tr_b16 a[188:189], v247 offset:12288\n"
    "v_mfma_f32_16x16x32_bf16 v[104:107], a[164:167], v[180:183], v[104:107]\n"
    "ds_read_b64_tr_b16 a[190:191], v247 offset:14336\n"
    "v_mfma_f32_16x16x32_bf16 v[108:111], a[164:167], v[188:191], v[108:111]\n"
    "buffer_load_dwordx4 a[136:139], v253, s[8:11], s48 offen offset:1024 sc1\n"
    "ds_write_addtid_b32 v216 offset:8192\n"
    "v_mfma_f32_16x16x32_bf16 v[108:111], a[160:163], v[184:187], v[108:111]\n"
    "s_add_u32 s48, s57, 24576\n"
    "v_exp_f32_e32 v239, v239\n"
    "v_mfma_f32_16x16x32_bf16 v[112:115], a[160:163], v[192:195], v[112:115]\n"
    "ds_read_b128 a[160:163], v243 offset:36864\n"
    "v_cvt_pk_bf16_f32 v234, v236, v237\n"
    "v_mfma_f32_16x16x32_bf16 v[112:115], a[164:167], v[196:199], v[112:115]\n"
    "s_waitcnt lgkmcnt(14)\n"
    "ds_read_b128 a[164:167], v240 offset:32768\n"
    "v_mfma_f32_16x16x32_bf16 v[124:127], a[168:171], v[192:195], v[124:127]\n"
    "buffer_load_dwordx4 a[76:79], v253, s[8:11], s48 offen offset:0 sc1\n"
    "ds_write_addtid_b32 v217 offset:8448\n"
    "v_mfma_f32_16x16x32_bf16 v[124:127], a[172:175], v[196:199], v[124:127]\n"
    "v_exp_f32_e32 v228, v228\n"
    "v_mfma_f32_16x16x32_bf16 v[120:123], a[172:175], v[188:191], v[120:123]\n"
    "v_exp_f32_e32 v229, v229\n"
    "v_mfma_f32_16x16x32_bf16 v[120:123], a[168:171], v[184:187], v[120:123]\n"
    "ds_write_addtid_b32 v234 offset:16896\n"
    "v_cvt_pk_bf16_f32 v235, v238, v239\n"
    "v_mfma_f32_16x16x32_bf16 v[116:119], a[168:171], v[176:179], v[116:119]\n"
    "ds_read_b128 a[168:171], v241 offset:32768\n"
    "buffer_load_dwordx4 a[108:111], v253, s[8:11], s48 offen offset:1024 sc1\n"
    "v_mfma_f32_16x16x32_bf16 v[116:119], a[172:175], v[180:183], v[116:119]\n"
    "s_waitcnt lgkmcnt(12)\n"
    "s_add_u32 s48, s57, 8192\n"
    "ds_read_b128 a[172:175], v240 offset:36864\n"
    "v_mfma_f32_16x16x32_bf16 v[128:131], a[176:179], v[176:179], v[128:131]\n"
    "v_exp_f32_e32 v230, v230\n"
    "v_mfma_f32_16x16x32_bf16 v[128:131], a[180:183], v[180:183], v[128:131]\n"
    "v_exp_f32_e32 v231, v231\n"
    "v_mfma_f32_16x16x32_bf16 v[132:135], a[180:183], v[188:191], v[132:135]\n"
    "buffer_load_dwordx4 a[140:143], v253, s[8:11], s48 offen offset:0 sc1\n"
    "v_cvt_pk_bf16_f32 v226, v228, v229\n"
    "v_mfma_f32_16x16x32_bf16 v[132:135], a[176:179], v[184:187], v[132:135]\n"
    "v_exp_f32_e32 v220, v220\n"
    "v_mfma_f32_16x16x32_bf16 v[136:139], a[176:179], v[192:195], v[136:139]\n"
    "ds_read_b128 a[176:179], v241 offset:36864\n"
    "v_mfma_f32_16x16x32_bf16 v[136:139], a[180:183], v[196:199], v[136:139]\n"
    "s_waitcnt lgkmcnt(8)\n"
    "ds_read_b128 a[180:183], v242 offset:32768\n"
    "v_mfma_f32_16x16x32_bf16 v[148:151], a[184:187], v[192:195], v[148:151]\n"
    "buffer_load_dwordx4 a[80:83], v253, s[8:11], s48 offen offset:1024 sc1\n"
    "ds_write_addtid_b32 v235 offset:17152\n"
    "v_mfma_f32_16x16x32_bf16 v[148:151], a[188:191], v[196:199], v[148:151]\n"
    "s_add_u32 s48, s57, 26624\n"
    "v_exp_f32_e32 v221, v221\n"
    "v_mfma_f32_16x16x32_bf16 v[144:147], a[188:191], v[188:191], v[144:147]\n"
    "ds_write_addtid_b32 v226 offset:12800\n"
    "v_cvt_pk_bf16_f32 v227, v230, v231\n"
    "v_mfma_f32_16x16x32_bf16 v[144:147], a[184:187], v[184:187], v[144:147]\n"
    "v_exp_f32_e32 v222, v222\n"
    "v_mfma_f32_16x16x32_bf16 v[140:143], a[184:187], v[176:179], v[140:143]\n"
    "ds_read_b128 a[184:187], v243 offset:32768\n"
    "buffer_load_dwordx4 a[112:115], v253, s[8:11], s48 offen offset:0 sc1\n"
    "v_mfma_f32_16x16x32_bf16 v[140:143], a[188:191], v[180:183], v[140:143]\n"
    "s_waitcnt lgkmcnt(5)\n"
    "ds_read_b128 a[188:191], v242 offset:36864\n"
    "v_mfma_f32_16x16x32_bf16 v[160:163], a[160:163], a[28:31], 0\n"
    "v_exp_f32_e32 v223, v223\n"
    "v_mfma_f32_16x16x32_bf16 v[160:163], a[164:167], a[0:3], v[160:163]\n"
    "v_cvt_pk_bf16_f32 v218, v220, v221\n"
    "v_cvt_pk_bf16_f32 v219, v222, v223\n"
    "v_mfma_f32_16x16x32_bf16 v[168:171], a[164:167], a[32:35], 0\n"
    "buffer_load_dwordx4 a[144:147], v253, s[8:11], s48 offen offset:1024 sc1\n"
    "ds_write_addtid_b32 v227 offset:13056\n"
    "v_mfma_f32_16x16x32_bf16 v[168:171], a[160:163], a[60:63], v[168:171]\n"
    "s_add_u32 s48, s57, 10240\n"
    "ds_read_b128 a[160:163], v243 offset:38912\n"
    "v_mfma_f32_16x16x32_bf16 v[168:171], a[168:171], a[36:39], v[168:171]\n"
    "ds_read_b128 a[164:167], v240 offset:34816\n"
    "v_mfma_f32_16x16x32_bf16 v[168:171], a[172:175], a[40:43], v[168:171]\n"
    "ds_write_addtid_b32 v218 offset:8704\n"
    "v_mfma_f32_16x16x32_bf16 v[160:163], a[172:175], a[8:11], v[160:163]\n"
    "buffer_load_dwordx4 a[84:87], v253, s[8:11], s48 offen offset:0 sc1\n"
    "ds_write_addtid_b32 v219 offset:8960\n"
    "v_mfma_f32_16x16x32_bf16 v[160:163], a[168:171], a[4:7], v[160:163]\n"
    "s_waitcnt lgkmcnt(5)\n"
    "ds_read_b128 a[168:171], v241 offset:34816\n"
    "v_mfma_f32_16x16x32_bf16 v[160:163], a[176:179], a[12:15], v[160:163]\n"
    "ds_read_b128 a[172:175], v240 offset:38912\n"
    "v_mfma_f32_16x16x32_bf16 v[160:163], a[180:183], a[16:19], v[160:163]\n"
    "v_mfma_f32_16x16x32_bf16 v[168:171], a[180:183], a[48:51], v[168:171]\n"
    "buffer_load_dwordx4 a[116:119], v253, s[8:11], s48 offen offset:1024 sc1\n"
    "v_mfma_f32_16x16x32_bf16 v[168:171], a[176:179], a[44:47], v[168:171]\n"
    "s_add_u32 s48, s57, 28672\n"
    "ds_read_b128 a[176:179], v241 offset:38912\n"
    "v_mfma_f32_16x16x32_bf16 v[168:171], a[184:187], a[52:55], v[168:171]\n"
    "s_waitcnt lgkmcnt(3)\n"
    "s_barrier\n"
    "v_mfma_f32_16x16x32_bf16 v[168:171], a[188:191], a[56:59], v[168:171]\n"
    "ds_read_b128 a[180:183], v242 offset:34816\n"
    "v_mfma_f32_16x16x32_bf16 v[160:163], a[188:191], a[24:27], v[160:163]\n"
    "buffer_load_dwordx4 a[148:151], v253, s[8:11], s48 offen offset:0 sc1\n"
    "ds_read2st64_b32 v[220:221], v250 offset0:32 offset1:33\n"
    "v_mfma_f32_16x16x32_bf16 v[160:163], a[184:187], a[20:23], v[160:163]\n"
    "s_waitcnt lgkmcnt(3)\n"
    "ds_read_b128 a[184:187], v243 offset:34816\n"
    "v_mfma_f32_16x16x32_bf16 v[164:167], a[160:163], a[28:31], 0\n"
    "ds_read_b128 a[188:191], v242 offset:38912\n"
    "v_mfma_f32_16x16x32_bf16 v[164:167], a[164:167], a[0:3], v[164:167]\n"
    "ds_read2st64_b32 v[222:223], v250 offset0:34 offset1:35\n"
    "v_mfma_f32_16x16x32_bf16 v[172:175], a[164:167], a[32:35], 0\n"
    "buffer_load_dwordx4 a[88:91], v253, s[8:11], s48 offen offset:1024 sc1\n"
    "ds_read2st64_b32 v[228:229], v250 offset0:48 offset1:49\n"
    "v_mfma_f32_16x16x32_bf16 v[172:175], a[160:163], a[60:63], v[172:175]\n"
    "s_add_u32 s48, s57, 12288\n"
    "ds_read_b64_tr_b16 a[160:161], v244 offset:16384\n"
    "ds_read2st64_b32 v[230:231], v250 offset0:50 offset1:51\n"
    "v_mfma_f32_16x16x32_bf16 v[172:175], a[168:171], a[36:39], v[172:175]\n"
    "ds_read_b64_tr_b16 a[162:163], v244 offset:18432\n"
    "v_mfma_f32_16x16x32_bf16 v[172:175], a[172:175], a[40:43], v[172:175]\n"
    "ds_read_b64_tr_b16 a[164:165], v244 offset:24576\n"
    "ds_read2st64_b32 v[236:237], v250 offset0:64 offset1:65\n"
    "v_mfma_f32_16x16x32_bf16 v[164:167], a[172:175], a[8:11], v[164:167]\n"
    "ds_read_b64_tr_b16 a[166:167], v244 offset:26624\n"
    "buffer_load_dwordx4 a[120:123], v253, s[8:11], s48 offen offset:0 sc1\n"
    "v_mfma_f32_16x16x32_bf16 v[164:167], a[168:171], a[4:7], v[164:167]\n"
    "s_waitcnt lgkmcnt(8)\n"
    "ds_read_b64_tr_b16 a[168:169], v245 offset:16384\n"
    "ds_read2st64_b32 v[238:239], v250 offset0:66 offset1:67\n"
    "v_mfma_f32_16x16x32_bf16 v[164:167], a[176:179], a[12:15], v[164:167]\n"
    "ds_read_b64_tr_b16 a[170:171], v245 offset:18432\n"
    "v_mfma_f32_16x16x32_bf16 v[164:167], a[180:183], a[16:19], v[164:167]\n"
    "ds_read_b64_tr_b16 a[172:173], v245 offset:24576\n"
    "v_mfma_f32_16x16x32_bf16 v[172:175], a[180:183], a[48:51], v[172:175]\n"
    "ds_read_b64_tr_b16 a[174:175], v245 offset:26624\n"
    "buffer_load_dwordx4 a[152:155], v253, s[8:11], s48 offen offset:1024 sc1\n"
    "v_mfma_f32_16x16x32_bf16 v[172:175], a[176:179], a[44:47], v[172:175]\n"
    "ds_read_b64_tr_b16 a[176:177], v246 offset:16384\n"
    "v_mfma_f32_16x16x32_bf16 v[172:175], a[184:187], a[52:55], v[172:175]\n"
    "s_waitcnt lgkmcnt(4)\n"
    "s_barrier\n"
    "v_mfma_f32_16x16x32_bf16 v[172:175], a[188:191], a[56:59], v[172:175]\n"
    "s_add_u32 s61, s47, 5779456\n"
    "ds_read_b64_tr_b16 a[178:179], v246 offset:18432\n"
    "v_mfma_f32_16x16x32_bf16 v[164:167], a[188:191], a[24:27], v[164:167]\n"
    "s_add_u32 m0, s49, 8192\n"
    "ds_read_b64_tr_b16 a[180:181], v246 offset:24576\n"
    "v_mfma_f32_16x16x32_bf16 v[164:167], a[184:187], a[20:23], v[164:167]\n"
    "ds_read_b64_tr_b16 a[182:183], v246 offset:26624\n"
    "buffer_load_dwordx4 v252, s[8:11], s61 offen offset:0 sc1 lds\n"
    "v_mfma_f32_16x16x32_bf16 v[56:59], a[160:163], v[216:219], v[56:59]\n"
    "s_add_u32 s61, s47, 5783552\n"
    "ds_read_b64_tr_b16 a[184:185], v247 offset:16384\n"
    "v_mfma_f32_16x16x32_bf16 v[56:59], a[164:167], v[220:223], v[56:59]\n"
    "s_add_u32 m0, s49, 12288\n"
    "ds_read_b64_tr_b16 a[186:187], v247 offset:18432\n"
    "v_mfma_f32_16x16x32_bf16 v[60:63], a[164:167], v[228:231], v[60:63]\n"
    "ds_read_b64_tr_b16 a[188:189], v247 offset:24576\n"
    "buffer_load_dwordx4 v252, s[8:11], s61 offen offset:0 sc1 lds\n"
    "v_mfma_f32_16x16x32_bf16 v[60:63], a[160:163], v[224:227], v[60:63]\n"
    "s_add_u32 s61, s47, 5787648\n"
    "ds_read_b64_tr_b16 a[190:191], v247 offset:26624\n"
    "v_mfma_f32_16x16x32_bf16 v[64:67], a[160:163], v[232:235], v[64:67]\n"
    "s_add_u32 m0, s49, 16384\n"
    "ds_read_b64_tr_b16 a[160:161], v244 offset:20480\n"
    "v_lshrrev_b32_e32 v158, 4, v253\n"
    "v_mfma_f32_16x16x32_bf16 v[64:67], a[164:167], v[236:239], v[64:67]\n"
    "s_waitcnt lgkmcnt(9)\n"
    "ds_read_b64_tr_b16 a[162:163], v244 offset:22528\n"
    "buffer_load_dwordx4 v252, s[8:11], s61 offen offset:0 sc1 lds\n"
    "v_mfma_f32_16x16x32_bf16 v[76:79], a[168:171], v[232:235], v[76:79]\n"
    "s_mov_b32 m0, s49\n"
    "ds_read_b64_tr_b16 a[164:165], v244 offset:28672\n"
    "v_lshrrev_b32_e32 v159, 4, v158\n"
    "v_mfma_f32_16x16x32_bf16 v[76:79], a[172:175], v[236:239], v[76:79]\n"
    "ds_read_b64_tr_b16 a[166:167], v244 offset:30720\n"
    "v_cvt_pk_bf16_f32 v64, v64, v65\n"
    "v_mfma_f32_16x16x32_bf16 v[72:75], a[172:175], v[228:231], v[72:75]\n"
    "v_exp_f32_e32 v168, v168\n"
    "v_mfma_f32_16x16x32_bf16 v[72:75], a[168:171], v[224:227], v[72:75]\n"
    "v_exp_f32_e32 v169, v169\n"
    "v_mfma_f32_16x16x32_bf16 v[68:71], a[168:171], v[216:219], v[68:71]\n"
    "ds_read_b64_tr_b16 a[168:169], v245 offset:20480\n"
    "v_cvt_pk_bf16_f32 v65, v66, v67\n"
    "v_mfma_f32_16x16x32_bf16 v[68:71], a[172:175], v[220:223], v[68:71]\n"
    "s_waitcnt lgkmcnt(9)\n"
    "ds_read_b64_tr_b16 a[170:171], v245 offset:22528\n"
    "v_cvt_pk_bf16_f32 v66, v76, v77\n"
    "v_mfma_f32_16x16x32_bf16 v[80:83], a[176:179], v[216:219], v[80:83]\n"
    "ds_read_b64_tr_b16 a[172:173], v245 offset:28672\n"
    "v_cvt_pk_bf16_f32 v67, v78, v79\n"
    "v_mfma_f32_16x16x32_bf16 v[80:83], a[180:183], v[220:223], v[80:83]\n"
    "ds_read_b64_tr_b16 a[174:175], v245 offset:30720\n"
    "v_cvt_pk_bf16_f32 v60, v60, v61\n"
    "v_mfma_f32_16x16x32_bf16 v[84:87], a[180:183], v[228:231], v[84:87]\n"
    "v_exp_f32_e32 v170, v170\n"
    "v_mfma_f32_16x16x32_bf16 v[84:87], a[176:179], v[224:227], v[84:87]\n"
    "v_exp_f32_e32 v171, v171\n"
    "v_mfma_f32_16x16x32_bf16 v[88:91], a[176:179], v[232:235], v[88:91]\n"
    "ds_read_b64_tr_b16 a[176:177], v246 offset:20480\n"
    "buffer_store_dwordx4 v[64:67], v253, s[8:11], s86 offen offset:0 sc1\n"
    "v_cvt_pk_bf16_f32 v168, v168, v169\n"
    "v_cvt_pk_bf16_f32 v61, v62, v63\n"
    "v_mfma_f32_16x16x32_bf16 v[88:91], a[180:183], v[236:239], v[88:91]\n"
    "s_waitcnt lgkmcnt(9)\n"
    "ds_read_b64_tr_b16 a[178:179], v246 offset:22528\n"
    "v_cvt_pk_bf16_f32 v62, v72, v73\n"
    "v_mfma_f32_16x16x32_bf16 v[100:103], a[184:187], v[232:235], v[100:103]\n"
    "ds_read_b64_tr_b16 a[180:181], v246 offset:28672\n"
    "v_cvt_pk_bf16_f32 v63, v74, v75\n"
    "v_mfma_f32_16x16x32_bf16 v[100:103], a[188:191], v[236:239], v[100:103]\n"
    "ds_read_b64_tr_b16 a[182:183], v246 offset:30720\n"
    "v_cvt_pk_bf16_f32 v56, v56, v57\n"
    "v_mfma_f32_16x16x32_bf16 v[96:99], a[188:191], v[228:231], v[96:99]\n"
    "v_exp_f32_e32 v160, v160\n"
    "v_mfma_f32_16x16x32_bf16 v[96:99], a[184:187], v[224:227], v[96:99]\n"
    "v_exp_f32_e32 v161, v161\n"
    "v_mfma_f32_16x16x32_bf16 v[92:95], a[184:187], v[216:219], v[92:95]\n"
    "ds_read_b64_tr_b16 a[184:185], v247 offset:20480\n"
    "v_cvt_pk_bf16_f32 v169, v170, v171\n"
    "v_mfma_f32_16x16x32_bf16 v[92:95], a[188:191], v[220:223], v[92:95]\n"
    "s_waitcnt lgkmcnt(9)\n"
    "ds_read_b64_tr_b16 a[186:187], v247 offset:22528\n"
    "v_cvt_pk_bf16_f32 v57, v58, v59\n"
    "v_mfma_f32_16x16x32_bf16 v[104:107], a[160:163], v[216:219], v[104:107]\n"
    "ds_read_b64_tr_b16 a[188:189], v247 offset:28672\n"
    "v_cvt_pk_bf16_f32 v58, v68, v69\n"
    "v_mfma_f32_16x16x32_bf16 v[104:107], a[164:167], v[220:223], v[104:107]\n"
    "ds_read_b64_tr_b16 a[190:191], v247 offset:30720\n"
    "v_cvt_pk_bf16_f32 v59, v70, v71\n"
    "v_mfma_f32_16x16x32_bf16 v[108:111], a[164:167], v[228:231], v[108:111]\n"
    "buffer_store_dwordx4 v[60:63], v253, s[8:11], s85 offen offset:0 sc1\n"
    "v_exp_f32_e32 v162, v162\n"
    "v_mfma_f32_16x16x32_bf16 v[108:111], a[160:163], v[224:227], v[108:111]\n"
    "v_exp_f32_e32 v163, v163\n"
    "v_mfma_f32_16x16x32_bf16 v[112:115], a[160:163], v[232:235], v[112:115]\n"
    "ds_read_b128 a[160:163], v243 offset:53248\n"
    "v_cvt_pk_bf16_f32 v160, v160, v161\n"
    "v_mfma_f32_16x16x32_bf16 v[112:115], a[164:167], v[236:239], v[112:115]\n"
    "s_waitcnt lgkmcnt(9)\n"
    "ds_read_b128 a[164:167], v240 offset:49152\n"
    "v_cvt_pk_bf16_f32 v161, v162, v163\n"
    "v_mfma_f32_16x16x32_bf16 v[124:127], a[168:171], v[232:235], v[124:127]\n"
    "v_exp_f32_e32 v172, v172\n"
    "v_mfma_f32_16x16x32_bf16 v[124:127], a[172:175], v[236:239], v[124:127]\n"
    "v_exp_f32_e32 v173, v173\n"
    "v_mfma_f32_16x16x32_bf16 v[120:123], a[172:175], v[228:231], v[120:123]\n"
    "v_exp_f32_e32 v174, v174\n"
    "v_mfma_f32_16x16x32_bf16 v[120:123], a[168:171], v[224:227], v[120:123]\n"
    "v_exp_f32_e32 v175, v175\n"
    "v_mfma_f32_16x16x32_bf16 v[116:119], a[168:171], v[216:219], v[116:119]\n"
    "ds_read_b128 a[168:171], v241 offset:49152\n"
    "ds_write_addtid_b32 v168 offset:4096\n"
    "v_mfma_f32_16x16x32_bf16 v[116:119], a[172:175], v[220:223], v[116:119]\n"
    "s_waitcnt lgkmcnt(8)\n"
    "ds_read_b128 a[172:175], v240 offset:53248\n"
    "v_cvt_pk_bf16_f32 v88, v88, v89\n"
    "v_mfma_f32_16x16x32_bf16 v[128:131], a[176:179], v[216:219], v[128:131]\n"
    "ds_write_addtid_b32 v169 offset:4352\n"
    "v_cvt_pk_bf16_f32 v170, v172, v173\n"
    "v_mfma_f32_16x16x32_bf16 v[128:131], a[180:183], v[220:223], v[128:131]\n"
    "buffer_store_dwordx4 v[56:59], v253, s[8:11], s84 offen offset:0 sc1\n"
    "v_exp_f32_e32 v164, v164\n"
    "v_mfma_f32_16x16x32_bf16 v[132:135], a[180:183], v[228:231], v[132:135]\n"
    "v_exp_f32_e32 v165, v165\n"
    "v_mfma_f32_16x16x32_bf16 v[132:135], a[176:179], v[224:227], v[132:135]\n"
    "ds_write_addtid_b32 v160 offset:0\n"
    "v_cvt_pk_bf16_f32 v171, v174, v175\n"
    "v_mfma_f32_16x16x32_bf16 v[136:139], a[176:179], v[232:235], v[136:139]\n"
    "ds_read_b128 a[176:179], v241 offset:53248\n"
    "v_cvt_pk_bf16_f32 v89, v90, v91\n"
    "v_mfma_f32_16x16x32_bf16 v[136:139], a[180:183], v[236:239], v[136:139]\n"
    "s_waitcnt lgkmcnt(8)\n"
    "ds_read_b128 a[180:183], v242 offset:49152\n"
    "v_cvt_pk_bf16_f32 v90, v100, v101\n"
    "v_mfma_f32_16x16x32_bf16 v[148:151], a[184:187], v[232:235], v[148:151]\n"
    "v_exp_f32_e32 v166, v166\n"
    "v_mfma_f32_16x16x32_bf16 v[148:151], a[188:191], v[236:239], v[148:151]\n"
    "v_exp_f32_e32 v167, v167\n"
    "v_mfma_f32_16x16x32_bf16 v[144:147], a[188:191], v[228:231], v[144:147]\n"
    "ds_write_addtid_b32 v161 offset:256\n"
    "v_cvt_pk_bf16_f32 v162, v164, v165\n"
    "v_mfma_f32_16x16x32_bf16 v[144:147], a[184:187], v[224:227], v[144:147]\n"
    "ds_write_addtid_b32 v170 offset:4608\n"
    "v_cvt_pk_bf16_f32 v163, v166, v167\n"
    "v_cvt_pk_bf16_f32 v91, v102, v103\n"
    "v_mfma_f32_16x16x32_bf16 v[140:143], a[184:187], v[216:219], v[140:143]\n"
    "ds_read_b128 a[184:187], v243 offset:49152\n"
    "buffer_store_dwordx4 v[88:91], v253, s[8:11], s86 offen offset:1024 sc1\n"
    "v_cvt_pk_bf16_f32 v84, v84, v85\n"
    "v_bfe_u32 v255, v158, 2, 2\n"
    "v_mfma_f32_16x16x32_bf16 v[140:143], a[188:191], v[220:223], v[140:143]\n"
    "s_waitcnt lgkmcnt(7)\n"
    "ds_read_b128 a[188:191], v242 offset:53248\n"
    "v_and_b32_e32 v158, 3, v158\n"
    "v_mfma_f32_16x16x32_bf16 v[200:203], a[160:163], a[28:31], 0\n"
    "ds_write_addtid_b32 v171 offset:4864\n"
    "v_xor_b32_e32 v159, v159, v255\n"
    "v_mfma_f32_16x16x32_bf16 v[200:203], a[164:167], a[0:3], v[200:203]\n"
    "ds_write_addtid_b32 v162 offset:512\n"
    "v_lshl_or_b32 v158, v159, 2, v158\n"
    "v_mfma_f32_16x16x32_bf16 v[208:211], a[164:167], a[32:35], 0\n"
    "ds_write_addtid_b32 v163 offset:768\n"
    "v_mov_b32_e32 v159, 0x3f80\n"
    "v_mfma_f32_16x16x32_bf16 v[208:211], a[160:163], a[60:63], v[208:211]\n"
    "ds_read_b128 a[160:163], v243 offset:55296\n"
    "v_mov_b32_e32 v152, 0\n"
    "v_mfma_f32_16x16x32_bf16 v[208:211], a[168:171], a[36:39], v[208:211]\n"
    "ds_read_b128 a[164:167], v240 offset:51200\n"
    "v_mov_b32_e32 v153, 0\n"
    "v_mfma_f32_16x16x32_bf16 v[208:211], a[172:175], a[40:43], v[208:211]\n"
    "v_cvt_pk_bf16_f32 v85, v86, v87\n"
    "v_mov_b32_e32 v156, 0\n"
    "v_mfma_f32_16x16x32_bf16 v[200:203], a[172:175], a[8:11], v[200:203]\n"
    "s_waitcnt lgkmcnt(2)\n"
    "s_barrier\n"
    "v_mfma_f32_16x16x32_bf16 v[200:203], a[168:171], a[4:7], v[200:203]\n"
    "ds_read_b128 a[168:171], v241 offset:51200\n"
    "v_mov_b32_e32 v157, 0\n"
    "v_mfma_f32_16x16x32_bf16 v[200:203], a[176:179], a[12:15], v[200:203]\n"
    "ds_read_b128 a[172:175], v240 offset:55296\n"
    "v_mfma_f32_16x16x32_bf16 v[200:203], a[180:183], a[16:19], v[200:203]\n"
    "ds_read2st64_b32 v[164:165], v250 offset0:0 offset1:1\n"
    "v_cmp_eq_u32_e32 vcc, 0, v158\n"
    "v_mfma_f32_16x16x32_bf16 v[208:211], a[180:183], a[48:51], v[208:211]\n"
    "v_cvt_pk_bf16_f32 v86, v96, v97\n"
    "v_cndmask_b32_e32 v154, 0, v159, vcc\n"
    "v_mfma_f32_16x16x32_bf16 v[208:211], a[176:179], a[44:47], v[208:211]\n"
    "ds_read_b128 a[176:179], v241 offset:55296\n"
    "v_cmp_eq_u32_e32 vcc, 1, v158\n"
    "v_mfma_f32_16x16x32_bf16 v[208:211], a[184:187], a[52:55], v[208:211]\n"
    "ds_read_b128 a[180:183], v242 offset:51200\n"
    "v_cndmask_b32_e64 v154, v154, 1.0, vcc\n"
    "v_mfma_f32_16x16x32_bf16 v[208:211], a[188:191], a[56:59], v[208:211]\n"
    "v_cvt_pk_bf16_f32 v87, v98, v99\n"
    "v_cmp_eq_u32_e32 vcc, 2, v158\n"
    "v_mfma_f32_16x16x32_bf16 v[200:203], a[188:191], a[24:27], v[200:203]\n"
    "ds_read2st64_b32 v[166:167], v250 offset0:2 offset1:3\n"
    "v_cndmask_b32_e32 v155, 0, v159, vcc\n"
    "v_mfma_f32_16x16x32_bf16 v[200:203], a[184:187], a[20:23], v[200:203]\n"
    "s_waitcnt lgkmcnt(4)\n"
    "ds_read_b128 a[184:187], v243 offset:51200\n"
    "v_cmp_eq_u32_e32 vcc, 3, v158\n"
    "v_mfma_f32_16x16x32_bf16 v[204:207], a[160:163], a[28:31], 0\n"
    "ds_read_b128 a[188:191], v242 offset:55296\n"
    "v_exp_f32_e32 v208, v208\n"
    "v_mfma_f32_16x16x32_bf16 v[204:207], a[164:167], a[0:3], v[204:207]\n"
    "ds_read2st64_b32 v[172:173], v250 offset0:16 offset1:17\n"
    "v_cndmask_b32_e64 v155, v155, 1.0, vcc\n"
    "v_mfma_f32_16x16x32_bf16 v[212:215], a[164:167], a[32:35], 0\n"
    "ds_read2st64_b32 v[174:175], v250 offset0:18 offset1:19\n"
    "v_exp_f32_e32 v209, v209\n"
    "v_mfma_f32_16x16x32_bf16 v[212:215], a[160:163], a[60:63], v[212:215]\n"
    "ds_read_b64_tr_b16 a[160:161], v244 offset:32768\n"
    "v_and_b32_e32 v251, 0xf0, v253\n"
    "v_mfma_f32_16x16x32_bf16 v[212:215], a[168:171], a[36:39], v[212:215]\n"
    "ds_read_b64_tr_b16 a[162:163], v244 offset:34816\n"
    "v_exp_f32_e32 v210, v210\n"
    "v_mfma_f32_16x16x32_bf16 v[212:215], a[172:175], a[40:43], v[212:215]\n"
    "ds_read_b64_tr_b16 a[164:165], v244 offset:40960\n"
    "v_mov_b32_e32 v254, 2.0\n"
    "v_mfma_f32_16x16x32_bf16 v[204:207], a[172:175], a[8:11], v[204:207]\n"
    "ds_read_b64_tr_b16 a[166:167], v244 offset:43008\n"
    "v_exp_f32_e32 v211, v211\n"
    "v_mfma_f32_16x16x32_bf16 v[204:207], a[168:171], a[4:7], v[204:207]\n"
    "s_waitcnt lgkmcnt(6)\n"
    "ds_read_b64_tr_b16 a[168:169], v245 offset:32768\n"
    "v_cmp_le_u32_e32 vcc, 0x100, v253\n"
    "v_mov_b32_e32 v255, s29\n"
    "v_mfma_f32_16x16x32_bf16 v[204:207], a[176:179], a[12:15], v[204:207]\n"
    "ds_read_b64_tr_b16 a[170:171], v245 offset:34816\n"
    "v_cndmask_b32_e32 v254, v254, v255, vcc\n"
    "v_mfma_f32_16x16x32_bf16 v[204:207], a[180:183], a[16:19], v[204:207]\n"
    "ds_read_b64_tr_b16 a[172:173], v245 offset:40960\n"
    "v_cvt_pk_bf16_f32 v208, v208, v209\n"
    "v_mfma_f32_16x16x32_bf16 v[212:215], a[180:183], a[48:51], v[212:215]\n"
    "ds_read_b64_tr_b16 a[174:175], v245 offset:43008\n"
    "v_cmp_le_u32_e32 vcc, 0x200, v253\n"
    "v_mov_b32_e32 v255, s30\n"
    "v_mfma_f32_16x16x32_bf16 v[212:215], a[176:179], a[44:47], v[212:215]\n"
    "ds_read_b64_tr_b16 a[176:177], v246 offset:32768\n"
    "v_exp_f32_e32 v200, v200\n"
    "v_mfma_f32_16x16x32_bf16 v[212:215], a[184:187], a[52:55], v[212:215]\n"
    "ds_read_b64_tr_b16 a[178:179], v246 offset:34816\n"
    "v_cvt_pk_bf16_f32 v80, v80, v81\n"
    "v_mfma_f32_16x16x32_bf16 v[212:215], a[188:191], a[56:59], v[212:215]\n"
    "ds_read_b64_tr_b16 a[180:181], v246 offset:40960\n"
    "v_exp_f32_e32 v201, v201\n"
    "v_mfma_f32_16x16x32_bf16 v[204:207], a[188:191], a[24:27], v[204:207]\n"
    "ds_read_b64_tr_b16 a[182:183], v246 offset:43008\n"
    "v_cndmask_b32_e32 v254, v254, v255, vcc\n"
    "v_mfma_f32_16x16x32_bf16 v[204:207], a[184:187], a[20:23], v[204:207]\n"
    "s_waitcnt lgkmcnt(4)\n"
    "ds_read_b64_tr_b16 a[184:185], v247 offset:32768\n"
    "v_cvt_pk_bf16_f32 v209, v210, v211\n"
    "v_mfma_f32_16x16x32_bf16 v[0:3], a[160:163], v[160:163], v[0:3]\n"
    "ds_read_b64_tr_b16 a[186:187], v247 offset:34816\n"
    "v_cvt_pk_bf16_f32 v81, v82, v83\n"
    "v_mfma_f32_16x16x32_bf16 v[0:3], a[164:167], v[164:167], v[0:3]\n"
    "ds_read_b64_tr_b16 a[188:189], v247 offset:40960\n"
    "v_exp_f32_e32 v202, v202\n"
    "v_mfma_f32_16x16x32_bf16 a[248:251], a[164:167], v[172:175], a[248:251]\n"
    "ds_read_b64_tr_b16 a[190:191], v247 offset:43008\n"
    "v_cvt_pk_bf16_f32 v82, v92, v93\n"
    "v_mfma_f32_16x16x32_bf16 a[248:251], a[160:163], v[168:171], a[248:251]\n"
    "ds_read_b64_tr_b16 a[160:161], v244 offset:36864\n"
    "v_exp_f32_e32 v203, v203\n"
    "v_mfma_f32_16x16x32_bf16 a[252:255], a[168:171], v[168:171], a[252:255]\n"
    "ds_read_b64_tr_b16 a[162:163], v244 offset:38912\n"
    "v_cvt_pk_bf16_f32 v83, v94, v95\n"
    "v_mfma_f32_16x16x32_bf16 a[252:255], a[172:175], v[172:175], a[252:255]\n"
    "s_barrier\n"
    "v_mfma_f32_16x16x32_bf16 v[4:7], a[172:175], v[164:167], v[4:7]\n"
    "ds_read_b64_tr_b16 a[164:165], v244 offset:45056\n"
    "v_cvt_pk_bf16_f32 v200, v200, v201\n"
    "v_mfma_f32_16x16x32_bf16 v[4:7], a[168:171], v[160:163], v[4:7]\n"
    "s_waitcnt lgkmcnt(3)\n"
    "ds_read_b64_tr_b16 a[166:167], v244 offset:47104\n"
    "v_cvt_pk_bf16_f32 v201, v202, v203\n"
    "v_mfma_f32_16x16x32_bf16 v[8:11], a[176:179], v[160:163], v[8:11]\n"
    "ds_read_b64_tr_b16 a[168:169], v245 offset:36864\n"
    "ds_write_addtid_b32 v208 offset:4096\n"
    "v_mfma_f32_16x16x32_bf16 v[8:11], a[180:183], v[164:167], v[8:11]\n"
    "ds_read_b64_tr_b16 a[170:171], v245 offset:38912\n"
    "v_cvt_pk_bf16_f32 v112, v112, v113\n"
    "v_mfma_f32_16x16x32_bf16 v[32:35], a[180:183], v[172:175], v[32:35]\n"
    "ds_read_b64_tr_b16 a[172:173], v245 offset:45056\n"
    "buffer_store_dwordx4 v[84:87], v253, s[8:11], s85 offen offset:1024 sc1\n"
    "v_exp_f32_e32 v212, v212\n"
    "v_mfma_f32_16x16x32_bf16 v[32:35], a[176:179], v[168:171], v[32:35]\n"
    "ds_read_b64_tr_b16 a[174:175], v245 offset:47104\n"
    "v_cvt_pk_bf16_f32 v113, v114, v115\n"
    "v_mfma_f32_16x16x32_bf16 v[36:39], a[184:187], v[168:171], v[36:39]\n"
    "ds_read_b64_tr_b16 a[176:177], v246 offset:36864\n"
    "v_exp_f32_e32 v213, v213\n"
    "v_mfma_f32_16x16x32_bf16 v[36:39], a[188:191], v[172:175], v[36:39]\n"
    "ds_read_b64_tr_b16 a[178:179], v246 offset:38912\n"
    "ds_write_addtid_b32 v209 offset:4352\n"
    "v_mfma_f32_16x16x32_bf16 v[12:15], a[188:191], v[164:167], v[12:15]\n"
    "ds_read_b64_tr_b16 a[180:181], v246 offset:45056\n"
    "buffer_store_dwordx4 v[80:83], v253, s[8:11], s84 offen offset:1024 sc1\n"
    "v_exp_f32_e32 v214, v214\n"
    "v_mfma_f32_16x16x32_bf16 v[12:15], a[184:187], v[160:163], v[12:15]\n"
    "s_waitcnt lgkmcnt(4)\n"
    "ds_read_b64_tr_b16 a[182:183], v246 offset:47104\n"
    "v_cvt_pk_bf16_f32 v114, v124, v125\n"
    "v_mfma_f32_16x16x32_bf16 v[16:19], a[160:163], v[160:163], v[16:19]\n"
    "ds_read_b64_tr_b16 a[184:185], v247 offset:36864\n"
    "ds_write_addtid_b32 v200 offset:0\n"
    "v_mfma_f32_16x16x32_bf16 v[16:19], a[164:167], v[164:167], v[16:19]\n"
    "ds_read_b64_tr_b16 a[186:187], v247 offset:38912\n"
    "v_cvt_pk_bf16_f32 v115, v126, v127\n"
    "v_mfma_f32_16x16x32_bf16 v[40:43], a[164:167], v[172:175], v[40:43]\n"
    "ds_read_b64_tr_b16 a[188:189], v247 offset:45056\n"
    "buffer_store_dwordx4 v[112:115], v253, s[8:11], s86 offen offset:2048 sc1\n"
    "v_cvt_pk_bf16_f32 v108, v108, v109\n"
    "v_cmp_le_u32_e32 vcc, 0x300, v253\n"
    "v_mov_b32_e32 v255, s31\n"
    "v_mfma_f32_16x16x32_bf16 v[40:43], a[160:163], v[168:171], v[40:43]\n"
    "ds_read_b64_tr_b16 a[190:191], v247 offset:47104\n"
    "v_cvt_pk_bf16_f32 v109, v110, v111\n"
    "v_mfma_f32_16x16x32_bf16 v[44:47], a[168:171], v[168:171], v[44:47]\n"
    "ds_read_b128 a[160:163], v243 offset:4096\n"
    "ds_write_addtid_b32 v201 offset:256\n"
    "v_mfma_f32_16x16x32_bf16 v[44:47], a[172:175], v[172:175], v[44:47]\n"
    "ds_read_b128 a[164:167], v240 offset:0\n"
    "v_cvt_pk_bf16_f32 v110, v120, v121\n"
    "v_mfma_f32_16x16x32_bf16 v[20:23], a[172:175], v[164:167], v[20:23]\n"
    "v_exp_f32_e32 v215, v215\n"
    "v_mfma_f32_16x16x32_bf16 v[20:23], a[168:171], v[160:163], v[20:23]\n"
    "s_waitcnt lgkmcnt(3)\n"
    "ds_read_b128 a[168:171], v241 offset:0\n"
    "v_cvt_pk_bf16_f32 v210, v212, v213\n"
    "v_mfma_f32_16x16x32_bf16 v[24:27], a[176:179], v[160:163], v[24:27]\n"
    "ds_read_b128 a[172:175], v240 offset:4096\n"
    "v_cvt_pk_bf16_f32 v111, v122, v123\n"
    "v_mfma_f32_16x16x32_bf16 v[24:27], a[180:183], v[164:167], v[24:27]\n"
    "buffer_store_dwordx4 v[108:111], v253, s[8:11], s85 offen offset:2048 sc1\n"
    "v_exp_f32_e32 v204, v204\n"
    "v_mfma_f32_16x16x32_bf16 v[48:51], a[180:183], v[172:175], v[48:51]\n"
    "v_exp_f32_e32 v205, v205\n"
    "v_mfma_f32_16x16x32_bf16 v[48:51], a[176:179], v[168:171], v[48:51]\n"
    "ds_read_b128 a[176:179], v241 offset:4096\n"
    "v_cvt_pk_bf16_f32 v211, v214, v215\n"
    "v_mfma_f32_16x16x32_bf16 v[52:55], a[184:187], v[168:171], v[52:55]\n"
    "ds_read_b128 a[180:183], v242 offset:0\n"
    "v_cndmask_b32_e32 v254, v254, v255, vcc\n"
    "v_mfma_f32_16x16x32_bf16 v[52:55], a[188:191], v[172:175], v[52:55]\n"
    "v_exp_f32_e32 v206, v206\n"
    "v_mfma_f32_16x16x32_bf16 v[28:31], a[188:191], v[164:167], v[28:31]\n"
    "v_exp_f32_e32 v207, v207\n"
    "v_mfma_f32_16x16x32_bf16 v[28:31], a[184:187], v[160:163], v[28:31]\n"
    "s_waitcnt lgkmcnt(2)\n"
    "ds_read_b128 a[184:187], v243 offset:0\n"
    "v_cvt_pk_bf16_f32 v202, v204, v205\n"
    "v_mfma_f32_16x16x32_bf16 v[160:163], a[160:163], a[28:31], 0\n"
    "s_cmp_eq_u32 s51, 0\n"
    "s_cselect_b32 s60, 2.0, 24576\n"
    "ds_read_b128 a[188:191], v242 offset:4096\n"
    "v_mfma_f32_16x16x32_bf16 v[160:163], a[164:167], a[0:3], v[160:163]\n"
    "ds_write_addtid_b32 v210 offset:4608\n"
    "v_cvt_pk_bf16_f32 v203, v206, v207\n"
    "v_mfma_f32_16x16x32_bf16 v[168:171], a[164:167], a[32:35], 0\n"
    "ds_write_addtid_b32 v211 offset:4864\n"
    "v_add_u32_e32 v251, v251, v254\n"
    "v_mfma_f32_16x16x32_bf16 v[168:171], a[160:163], a[60:63], v[168:171]\n"
    "s_lshl_b32 s45, s51, 16\n"
    "s_add_u32 s45, s45, %3\n"
    "s_add_u32 s45, s45, 73728\n"
    "ds_read_b128 a[160:163], v243 offset:6144\n"
    "v_mfma_f32_16x16x32_bf16 v[168:171], a[168:171], a[36:39], v[168:171]\n"
    "s_lshl_b32 s46, s89, 12\n"
    "s_add_u32 s46, s46, s45\n"
    "ds_read_b128 a[164:167], v240 offset:2048\n"
    "v_add_u32_e32 v159, s46, v253\n"
    "v_mfma_f32_16x16x32_bf16 v[168:171], a[172:175], a[40:43], v[168:171]\n"
    "ds_write_addtid_b32 v202 offset:512\n"
    "v_cvt_pk_bf16_f32 v104, v104, v105\n"
    "v_mfma_f32_16x16x32_bf16 v[160:163], a[172:175], a[8:11], v[160:163]\n"
    "s_add_u32 s45, s45, 16384\n"
    "ds_write_addtid_b32 v203 offset:768\n"
    "v_lshrrev_b32_e32 v255, 2, v253\n"
    "v_mfma_f32_16x16x32_bf16 v[160:163], a[168:171], a[4:7], v[160:163]\n"
    "s_waitcnt lgkmcnt(6)\n"
    "ds_read_b128 a[168:171], v241 offset:2048\n"
    "v_mfma_f32_16x16x32_bf16 v[160:163], a[176:179], a[12:15], v[160:163]\n"
    "ds_read_b128 a[172:175], v240 offset:6144\n"
    "v_cvt_pk_bf16_f32 v105, v106, v107\n"
    "v_mfma_f32_16x16x32_bf16 v[160:163], a[180:183], a[16:19], v[160:163]\n"
    "v_cvt_pk_bf16_f32 v106, v116, v117\n"
    "v_add_u32_e32 v255, s45, v255\n"
    "v_mfma_f32_16x16x32_bf16 v[168:171], a[180:183], a[48:51], v[168:171]\n"
    "s_waitcnt lgkmcnt(2)\n"
    "s_barrier\n"
    "v_mfma_f32_16x16x32_bf16 v[168:171], a[176:179], a[44:47], v[168:171]\n"
    "s_add_u32 m0, s43, 32768\n"
    "ds_read_b128 a[176:179], v241 offset:6144\n"
    "v_cvt_pk_bf16_f32 v107, v118, v119\n"
    "v_mfma_f32_16x16x32_bf16 v[168:171], a[184:187], a[52:55], v[168:171]\n"
    "ds_read_b128 a[180:183], v242 offset:2048\n"
    "buffer_load_dwordx4 v248, s[8:11], s40 offen offset:0 sc1 lds\n"
    "v_mfma_f32_16x16x32_bf16 v[168:171], a[188:191], a[56:59], v[168:171]\n"
    "ds_read2st64_b32 v[204:205], v250 offset0:0 offset1:1\n"
    "v_cvt_pk_bf16_f32 v136, v136, v137\n"
    "v_mfma_f32_16x16x32_bf16 v[160:163], a[188:191], a[24:27], v[160:163]\n"
    "ds_read2st64_b32 v[206:207], v250 offset0:2 offset1:3\n"
    "v_cvt_pk_bf16_f32 v137, v138, v139\n"
    "v_mfma_f32_16x16x32_bf16 v[160:163], a[184:187], a[20:23], v[160:163]\n"
    "s_waitcnt lgkmcnt(4)\n"
    "ds_read_b128 a[184:187], v243 offset:2048\n"
    "buffer_load_dwordx4 v248, s[8:11], s40 offen offset:1024 sc1 lds\n"
    "v_mfma_f32_16x16x32_bf16 v[164:167], a[160:163], a[28:31], 0\n"
    "ds_read_b128 a[188:191], v242 offset:6144\n"
    "v_exp_f32_e32 v168, v168\n"
    "v_mfma_f32_16x16x32_bf16 v[164:167], a[164:167], a[0:3], v[164:167]\n"
    "ds_read2st64_b32 v[212:213], v250 offset0:16 offset1:17\n"
    "v_cvt_pk_bf16_f32 v138, v148, v149\n"
    "v_mfma_f32_16x16x32_bf16 v[172:175], a[164:167], a[32:35], 0\n"
    "buffer_load_dwordx4 v248, s[8:11], s40 offen offset:2048 sc1 lds\n"
    "ds_read2st64_b32 v[214:215], v250 offset0:18 offset1:19\n"
    "v_mfma_f32_16x16x32_bf16 v[172:175], a[160:163], a[60:63], v[172:175]\n"
    "ds_read_b64_tr_b16 a[160:161], v244 offset:49152\n"
    "v_exp_f32_e32 v169, v169\n"
    "v_mfma_f32_16x16x32_bf16 v[172:175], a[168:171], a[36:39], v[172:175]\n"
    "ds_read_b64_tr_b16 a[162:163], v244 offset:51200\n"
    "v_exp_f32_e32 v170, v170\n"
    "v_mfma_f32_16x16x32_bf16 v[172:175], a[172:175], a[40:43], v[172:175]\n"
    "ds_read_b64_tr_b16 a[164:165], v244 offset:57344\n"
    "buffer_load_dwordx4 v248, s[8:11], s40 offen offset:3072 sc1 lds\n"
    "v_mfma_f32_16x16x32_bf16 v[164:167], a[172:175], a[8:11], v[164:167]\n"
    "s_add_u32 s40, s40, 32768\n"
    "ds_read_b64_tr_b16 a[166:167], v244 offset:59392\n"
    "v_exp_f32_e32 v171, v171\n"
    "v_mfma_f32_16x16x32_bf16 v[164:167], a[168:171], a[4:7], v[164:167]\n"
    "s_waitcnt lgkmcnt(6)\n"
    "s_add_u32 m0, s43, 36864\n"
    "ds_read_b64_tr_b16 a[168:169], v245 offset:49152\n"
    "v_mfma_f32_16x16x32_bf16 v[164:167], a[176:179], a[12:15], v[164:167]\n"
    "ds_read_b64_tr_b16 a[170:171], v245 offset:51200\n"
    "buffer_load_dwordx4 v248, s[8:11], s41 offen offset:0 sc1 lds\n"
    "v_mfma_f32_16x16x32_bf16 v[164:167], a[180:183], a[16:19], v[164:167]\n"
    "ds_read_b64_tr_b16 a[172:173], v245 offset:57344\n"
    "v_cvt_pk_bf16_f32 v168, v168, v169\n"
    "v_mfma_f32_16x16x32_bf16 v[172:175], a[180:183], a[48:51], v[172:175]\n"
    "ds_read_b64_tr_b16 a[174:175], v245 offset:59392\n"
    "v_exp_f32_e32 v160, v160\n"
    "v_mfma_f32_16x16x32_bf16 v[172:175], a[176:179], a[44:47], v[172:175]\n"
    "ds_read_b64_tr_b16 a[176:177], v246 offset:49152\n"
    "buffer_load_dwordx4 v248, s[8:11], s41 offen offset:1024 sc1 lds\n"
    "v_mfma_f32_16x16x32_bf16 v[172:175], a[184:187], a[52:55], v[172:175]\n"
    "ds_read_b64_tr_b16 a[178:179], v246 offset:51200\n"
    "v_cvt_pk_bf16_f32 v139, v150, v151\n"
    "v_mfma_f32_16x16x32_bf16 v[172:175], a[188:191], a[56:59], v[172:175]\n"
    "ds_read_b64_tr_b16 a[180:181], v246 offset:57344\n"
    "v_exp_f32_e32 v161, v161\n"
    "v_mfma_f32_16x16x32_bf16 v[164:167], a[188:191], a[24:27], v[164:167]\n"
    "ds_read_b64_tr_b16 a[182:183], v246 offset:59392\n"
    "buffer_load_dwordx4 v248, s[8:11], s41 offen offset:2048 sc1 lds\n"
    "v_mfma_f32_16x16x32_bf16 v[164:167], a[184:187], a[20:23], v[164:167]\n"
    "s_waitcnt lgkmcnt(4)\n"
    "ds_read_b64_tr_b16 a[184:185], v247 offset:49152\n"
    "v_cvt_pk_bf16_f32 v169, v170, v171\n"
    "v_mfma_f32_16x16x32_bf16 v[0:3], a[160:163], v[200:203], v[0:3]\n"
    "ds_read_b64_tr_b16 a[186:187], v247 offset:51200\n"
    "v_cvt_pk_bf16_f32 v132, v132, v133\n"
    "v_mfma_f32_16x16x32_bf16 v[0:3], a[164:167], v[204:207], v[0:3]\n"
    "ds_read_b64_tr_b16 a[188:189], v247 offset:57344\n"
    "buffer_load_dwordx4 v248, s[8:11], s41 offen offset:3072 sc1 lds\n"
    "v_mfma_f32_16x16x32_bf16 a[248:251], a[164:167], v[212:215], a[248:251]\n"
    "s_add_u32 s41, s41, 32768\n"
    "ds_read_b64_tr_b16 a[190:191], v247 offset:59392\n"
    "v_exp_f32_e32 v162, v162\n"
    "v_mfma_f32_16x16x32_bf16 a[248:251], a[160:163], v[208:211], a[248:251]\n"
    "s_mov_b32 m0, s49\n"
    "ds_read_b64_tr_b16 a[160:161], v244 offset:53248\n"
    "v_exp_f32_e32 v163, v163\n"
    "v_mfma_f32_16x16x32_bf16 a[252:255], a[168:171], v[208:211], a[252:255]\n"
    "ds_read_b64_tr_b16 a[162:163], v244 offset:55296\n"
    "v_cvt_pk_bf16_f32 v133, v134, v135\n"
    "v_mfma_f32_16x16x32_bf16 a[252:255], a[172:175], v[212:215], a[252:255]\n"
    "s_barrier\n"
    "v_mfma_f32_16x16x32_bf16 v[4:7], a[172:175], v[204:207], v[4:7]\n"
    "ds_read_b64_tr_b16 a[164:165], v244 offset:61440\n"
    "v_cvt_pk_bf16_f32 v160, v160, v161\n"
    "v_mfma_f32_16x16x32_bf16 v[4:7], a[168:171], v[200:203], v[4:7]\n"
    "s_waitcnt lgkmcnt(3)\n"
    "ds_read_b64_tr_b16 a[166:167], v244 offset:63488\n"
    "v_cvt_pk_bf16_f32 v161, v162, v163\n"
    "v_mfma_f32_16x16x32_bf16 v[8:11], a[176:179], v[200:203], v[8:11]\n"
    "ds_read_b64_tr_b16 a[168:169], v245 offset:53248\n"
    "v_cvt_pk_bf16_f32 v134, v144, v145\n"
    "v_mfma_f32_16x16x32_bf16 v[8:11], a[180:183], v[204:207], v[8:11]\n"
    "ds_read_b64_tr_b16 a[170:171], v245 offset:55296\n"
    "v_cvt_pk_bf16_f32 v135, v146, v147\n"
    "v_mfma_f32_16x16x32_bf16 v[32:35], a[180:183], v[212:215], v[32:35]\n"
    "ds_read_b64_tr_b16 a[172:173], v245 offset:61440\n"
    "buffer_store_dwordx4 v[104:107], v253, s[8:11], s84 offen offset:2048 sc1\n"
    "v_exp_f32_e32 v172, v172\n"
    "v_mfma_f32_16x16x32_bf16 v[32:35], a[176:179], v[208:211], v[32:35]\n"
    "s_lshl_b32 s45, s89, 8\n"
    "ds_read_b64_tr_b16 a[174:175], v245 offset:63488\n"
    "v_add_u32_e32 v254, s45, v255\n"
    "v_mfma_f32_16x16x32_bf16 v[36:39], a[184:187], v[208:211], v[36:39]\n"
    "ds_read_b64_tr_b16 a[176:177], v246 offset:53248\n"
    "v_exp_f32_e32 v173, v173\n"
    "v_mfma_f32_16x16x32_bf16 v[36:39], a[188:191], v[212:215], v[36:39]\n"
    "ds_read_b64_tr_b16 a[178:179], v246 offset:55296\n"
    "v_cvt_pk_bf16_f32 v128, v128, v129\n"
    "v_mfma_f32_16x16x32_bf16 v[12:15], a[188:191], v[204:207], v[12:15]\n"
    "ds_read_b64_tr_b16 a[180:181], v246 offset:61440\n"
    "buffer_store_dwordx4 v[136:139], v253, s[8:11], s86 offen offset:3072 sc1\n"
    "v_exp_f32_e32 v174, v174\n"
    "v_mfma_f32_16x16x32_bf16 v[12:15], a[184:187], v[200:203], v[12:15]\n"
    "s_waitcnt lgkmcnt(3)\n"
    "ds_read_b64_tr_b16 a[182:183], v246 offset:63488\n"
    "v_mfma_f32_16x16x32_bf16 v[16:19], a[160:163], v[200:203], v[16:19]\n"
    "s_lshl_b32 s45, s50, 8\n"
    "ds_read_b64_tr_b16 a[184:185], v247 offset:53248\n"
    "v_add_u32_e32 v255, s45, v255\n"
    "v_mfma_f32_16x16x32_bf16 v[16:19], a[164:167], v[204:207], v[16:19]\n"
    "ds_read_b64_tr_b16 a[186:187], v247 offset:55296\n"
    "v_cvt_pk_bf16_f32 v129, v130, v131\n"
    "v_mfma_f32_16x16x32_bf16 v[40:43], a[164:167], v[212:215], v[40:43]\n"
    "ds_read_b64_tr_b16 a[188:189], v247 offset:61440\n"
    "buffer_store_dwordx4 v[132:135], v253, s[8:11], s85 offen offset:3072 sc1\n"
    "v_cvt_pk_bf16_f32 v130, v140, v141\n"
    "v_mfma_f32_16x16x32_bf16 v[40:43], a[160:163], v[208:211], v[40:43]\n"
    "ds_read_b64_tr_b16 a[190:191], v247 offset:63488\n"
    "v_cvt_pk_bf16_f32 v131, v142, v143\n"
    "v_mfma_f32_16x16x32_bf16 v[44:47], a[168:171], v[208:211], v[44:47]\n"
    "ds_read_b128 a[160:163], v243 offset:20480\n"
    "ds_write_addtid_b32 v168 offset:4096\n"
    "v_mfma_f32_16x16x32_bf16 v[44:47], a[172:175], v[212:215], v[44:47]\n"
    "ds_read_b128 a[164:167], v240 offset:16384\n"
    "ds_write_addtid_b32 v169 offset:4352\n"
    "v_mfma_f32_16x16x32_bf16 v[20:23], a[172:175], v[204:207], v[20:23]\n"
    "v_exp_f32_e32 v175, v175\n"
    "v_mfma_f32_16x16x32_bf16 v[20:23], a[168:171], v[200:203], v[20:23]\n"
    "s_waitcnt lgkmcnt(4)\n"
    "ds_read_b128 a[168:171], v241 offset:16384\n"
    "v_cvt_pk_bf16_f32 v170, v172, v173\n"
    "v_mfma_f32_16x16x32_bf16 v[24:27], a[176:179], v[200:203], v[24:27]\n"
    "ds_read_b128 a[172:175], v240 offset:20480\n"
    "ds_write_addtid_b32 v160 offset:0\n"
    "v_mfma_f32_16x16x32_bf16 v[24:27], a[180:183], v[204:207], v[24:27]\n"
    "buffer_store_dwordx4 v[128:131], v253, s[8:11], s84 offen offset:3072 sc1\n"
    "v_exp_f32_e32 v164, v164\n"
    "v_mfma_f32_16x16x32_bf16 v[48:51], a[180:183], v[212:215], v[48:51]\n"
    "v_exp_f32_e32 v165, v165\n"
    "v_permlane16_swap_b32_e32 v147, v151\n"
    "v_mfma_f32_16x16x32_bf16 v[48:51], a[176:179], v[208:211], v[48:51]\n"
    "ds_read_b128 a[176:179], v241 offset:20480\n"
    "v_cvt_pk_bf16_f32 v171, v174, v175\n"
    "v_mov_b32_e32 v148, s39\n"
    "v_mfma_f32_16x16x32_bf16 v[52:55], a[184:187], v[208:211], v[52:55]\n"
    "ds_read_b128 a[180:183], v242 offset:16384\n"
    "ds_write_addtid_b32 v161 offset:256\n"
    "v_mfma_f32_16x16x32_bf16 v[52:55], a[188:191], v[212:215], v[52:55]\n"
    "v_exp_f32_e32 v166, v166\n"
    "v_mov_b32_e32 v149, 0\n"
    "v_mfma_f32_16x16x32_bf16 v[28:31], a[188:191], v[204:207], v[28:31]\n"
    "v_exp_f32_e32 v167, v167\n"
    "v_permlane32_swap_b32_e32 v143, v151\n"
    "v_mfma_f32_16x16x32_bf16 v[28:31], a[184:187], v[200:203], v[28:31]\n"
    "s_waitcnt lgkmcnt(4)\n"
    "ds_read_b128 a[184:187], v243 offset:16384\n"
    "v_cvt_pk_bf16_f32 v162, v164, v165\n"
    "v_mfma_f32_16x16x32_bf16 v[200:203], a[160:163], a[28:31], 0\n"
    "ds_read_b128 a[188:191], v242 offset:20480\n"
    "v_mfma_f32_16x16x32_bf16 v[200:203], a[164:167], a[0:3], v[200:203]\n"
    "ds_write_addtid_b32 v170 offset:4608\n"
    "buffer_store_dword v151, v251, s[8:11], s60 offen offset:0 sc1\n"
    "v_cvt_pk_bf16_f32 v163, v166, v167\n"
    "v_mfma_f32_16x16x32_bf16 v[208:211], a[164:167], a[32:35], 0\n"
    "ds_write_addtid_b32 v171 offset:4864\n"
    "v_mfma_f32_16x16x32_bf16 v[208:211], a[160:163], a[60:63], v[208:211]\n"
    "ds_read_b128 a[160:163], v243 offset:22528\n"
    "v_lshrrev_b32_e32 v147, 4, v253\n"
    "v_mfma_f32_16x16x32_bf16 v[208:211], a[168:171], a[36:39], v[208:211]\n"
    "ds_read_b128 a[164:167], v240 offset:18432\n"
    "v_mfma_f32_16x16x32_bf16 v[208:211], a[172:175], a[40:43], v[208:211]\n"
    "ds_write_addtid_b32 v162 offset:512\n"
    "v_mfma_f32_16x16x32_bf16 v[200:203], a[172:175], a[8:11], v[200:203]\n"
    "ds_write_addtid_b32 v163 offset:768\n"
    "v_bfe_u32 v145, v147, 2, 2\n"
    "v_mfma_f32_16x16x32_bf16 v[200:203], a[168:171], a[4:7], v[200:203]\n"
    "s_waitcnt lgkmcnt(6)\n"
    "ds_read_b128 a[168:171], v241 offset:18432\n"
    "v_mfma_f32_16x16x32_bf16 v[200:203], a[176:179], a[12:15], v[200:203]\n"
    "ds_read_b128 a[172:175], v240 offset:22528\n"
    "v_mfma_f32_16x16x32_bf16 v[200:203], a[180:183], a[16:19], v[200:203]\n"
    "v_min_u32_e32 v145, 2, v145\n"
    "v_mfma_f32_16x16x32_bf16 v[208:211], a[180:183], a[48:51], v[208:211]\n"
    "s_waitcnt lgkmcnt(2)\n"
    "s_barrier\n"
    "v_mfma_f32_16x16x32_bf16 v[208:211], a[176:179], a[44:47], v[208:211]\n"
    "s_add_u32 s46, s32, 1\n"
    "ds_read_b128 a[176:179], v241 offset:22528\n"
    "v_mfma_f32_16x16x32_bf16 v[208:211], a[184:187], a[52:55], v[208:211]\n"
    "ds_read_b128 a[180:183], v242 offset:18432\n"
    "v_mfma_f32_16x16x32_bf16 v[208:211], a[188:191], a[56:59], v[208:211]\n"
    "ds_read2st64_b32 v[164:165], v250 offset0:0 offset1:1\n"
    "v_mfma_f32_16x16x32_bf16 v[200:203], a[188:191], a[24:27], v[200:203]\n"
    "ds_read2st64_b32 v[166:167], v250 offset0:2 offset1:3\n"
    "v_add_u32_e32 v145, s46, v145\n"
    "v_mfma_f32_16x16x32_bf16 v[200:203], a[184:187], a[20:23], v[200:203]\n"
    "s_waitcnt lgkmcnt(4)\n"
    "ds_read_b128 a[184:187], v243 offset:18432\n"
    "v_mfma_f32_16x16x32_bf16 v[204:207], a[160:163], a[28:31], 0\n"
    "ds_read_b128 a[188:191], v242 offset:22528\n"
    "v_exp_f32_e32 v208, v208\n"
    "v_mfma_f32_16x16x32_bf16 v[204:207], a[164:167], a[0:3], v[204:207]\n"
    "ds_read2st64_b32 v[172:173], v250 offset0:16 offset1:17\n"
    "v_and_b32_e32 v145, 3, v145\n"
    "v_mfma_f32_16x16x32_bf16 v[212:215], a[164:167], a[32:35], 0\n"
    "ds_read2st64_b32 v[174:175], v250 offset0:18 offset1:19\n"
    "v_exp_f32_e32 v209, v209\n"
    "v_mfma_f32_16x16x32_bf16 v[212:215], a[160:163], a[60:63], v[212:215]\n"
    "ds_read_b64_tr_b16 a[160:161], v244 offset:0\n"
    "v_lshlrev_b32_e32 v145, 6, v145\n"
    "v_mfma_f32_16x16x32_bf16 v[212:215], a[168:171], a[36:39], v[212:215]\n"
    "ds_read_b64_tr_b16 a[162:163], v244 offset:2048\n"
    "v_exp_f32_e32 v210, v210\n"
    "v_mfma_f32_16x16x32_bf16 v[212:215], a[172:175], a[40:43], v[212:215]\n"
    "ds_read_b64_tr_b16 a[164:165], v244 offset:8192\n"
    "v_and_or_b32 v145, v147, 3, v145\n"
    "v_mfma_f32_16x16x32_bf16 v[204:207], a[172:175], a[8:11], v[204:207]\n"
    "ds_read_b64_tr_b16 a[166:167], v244 offset:10240\n"
    "v_exp_f32_e32 v211, v211\n"
    "v_mfma_f32_16x16x32_bf16 v[204:207], a[168:171], a[4:7], v[204:207]\n"
    "s_waitcnt lgkmcnt(6)\n"
    "ds_read_b64_tr_b16 a[168:169], v245 offset:0\n"
    "v_mfma_f32_16x16x32_bf16 v[204:207], a[176:179], a[12:15], v[204:207]\n"
    "s_add_u32 s48, s56, 0\n"
    "ds_read_b64_tr_b16 a[170:171], v245 offset:2048\n"
    "v_lshlrev_b32_e32 v145, 2, v145\n"
    "v_mfma_f32_16x16x32_bf16 v[204:207], a[180:183], a[16:19], v[204:207]\n"
    "ds_read_b64_tr_b16 a[172:173], v245 offset:8192\n"
    "v_cvt_pk_bf16_f32 v208, v208, v209\n"
    "v_mfma_f32_16x16x32_bf16 v[212:215], a[180:183], a[48:51], v[212:215]\n"
    "ds_read_b64_tr_b16 a[174:175], v245 offset:10240\n"
    "v_mfma_f32_16x16x32_bf16 v[212:215], a[176:179], a[44:47], v[212:215]\n"
    "s_lshl_b32 s47, s33, 10\n"
    "ds_read_b64_tr_b16 a[176:177], v246 offset:0\n"
    "v_exp_f32_e32 v200, v200\n"
    "v_mfma_f32_16x16x32_bf16 v[212:215], a[184:187], a[52:55], v[212:215]\n"
    "ds_read_b64_tr_b16 a[178:179], v246 offset:2048\n"
    "buffer_load_dwordx4 v[56:59], v253, s[8:11], s48 offen offset:0 sc1\n"
    "v_mfma_f32_16x16x32_bf16 v[212:215], a[188:191], a[56:59], v[212:215]\n"
    "s_add_u32 s47, s47, 5636096\n"
    "ds_read_b64_tr_b16 a[180:181], v246 offset:8192\n"
    "v_exp_f32_e32 v201, v201\n"
    "v_mfma_f32_16x16x32_bf16 v[204:207], a[188:191], a[24:27], v[204:207]\n"
    "ds_read_b64_tr_b16 a[182:183], v246 offset:10240\n"
    "v_mfma_f32_16x16x32_bf16 v[204:207], a[184:187], a[20:23], v[204:207]\n"
    "s_waitcnt lgkmcnt(4)\n"
    "ds_read_b64_tr_b16 a[184:185], v247 offset:0\n"
    "v_cvt_pk_bf16_f32 v209, v210, v211\n"
    "v_mfma_f32_16x16x32_bf16 v[0:3], a[160:163], v[160:163], v[0:3]\n"
    "ds_read_b64_tr_b16 a[186:187], v247 offset:2048\n"
    "buffer_load_dwordx4 v[60:63], v253, s[8:11], s48 offen offset:1024 sc1\n"
    "v_cmp_gt_u32_e32 vcc, 0x300, v253\n"
    "v_mfma_f32_16x16x32_bf16 v[0:3], a[164:167], v[164:167], v[0:3]\n"
    "ds_read_b64_tr_b16 a[188:189], v247 offset:8192\n"
    "v_exp_f32_e32 v202, v202\n"
    "v_mfma_f32_16x16x32_bf16 a[248:251], a[164:167], v[172:175], a[248:251]\n"
    "s_add_u32 s60, s60, s28\n"
    "ds_read_b64_tr_b16 a[190:191], v247 offset:10240\n"
    "v_and_b32_e32 v251, 0xf0, v253\n"
    "v_mfma_f32_16x16x32_bf16 a[248:251], a[160:163], v[168:171], a[248:251]\n"
    "ds_read_b64_tr_b16 a[160:161], v244 offset:4096\n"
    "v_exp_f32_e32 v203, v203\n"
    "v_mfma_f32_16x16x32_bf16 a[252:255], a[168:171], v[168:171], a[252:255]\n"
    "ds_read_b64_tr_b16 a[162:163], v244 offset:6144\n"
    "buffer_load_dwordx4 v[64:67], v253, s[8:11], s48 offen offset:2048 sc1\n"
    "v_cndmask_b32_e64 v251, v251, 2.0, vcc\n"
    "v_mfma_f32_16x16x32_bf16 a[252:255], a[172:175], v[172:175], a[252:255]\n"
    "s_waitcnt vmcnt(8)\n"
    "s_barrier\n"
    "v_mfma_f32_16x16x32_bf16 v[4:7], a[172:175], v[164:167], v[4:7]\n"
    "ds_read_b64_tr_b16 a[164:165], v244 offset:12288\n"
    "v_cvt_pk_bf16_f32 v200, v200, v201\n"
    "v_mfma_f32_16x16x32_bf16 v[4:7], a[168:171], v[160:163], v[4:7]\n"
    "s_waitcnt lgkmcnt(3)\n"
    "ds_read_b64_tr_b16 a[166:167], v244 offset:14336\n"
    "v_cvt_pk_bf16_f32 v201, v202, v203\n"
    "v_mfma_f32_16x16x32_bf16 v[8:11], a[176:179], v[160:163], v[8:11]\n"
    "ds_read_b64_tr_b16 a[168:169], v245 offset:4096\n"
    "ds_write_addtid_b32 v208 offset:4096\n"
    "buffer_load_dwordx4 v[68:71], v253, s[8:11], s48 offen offset:3072 sc1\n"
    "v_mfma_f32_16x16x32_bf16 v[8:11], a[180:183], v[164:167], v[8:11]\n"
    "s_add_u32 s48, s56, 4096\n"
    "ds_read_b64_tr_b16 a[170:171], v245 offset:6144\n"
    "v_mfma_f32_16x16x32_bf16 v[32:35], a[180:183], v[172:175], v[32:35]\n"
    "ds_read_b64_tr_b16 a[172:173], v245 offset:12288\n"
    "v_exp_f32_e32 v212, v212\n"
    "v_mfma_f32_16x16x32_bf16 v[32:35], a[176:179], v[168:171], v[32:35]\n"
    "ds_read_b64_tr_b16 a[174:175], v245 offset:14336\n"
    "v_mfma_f32_16x16x32_bf16 v[36:39], a[184:187], v[168:171], v[36:39]\n"
    "ds_read_b64_tr_b16 a[176:177], v246 offset:4096\n"
    "buffer_load_dwordx4 v[72:75], v253, s[8:11], s48 offen offset:0 sc1\n"
    "v_exp_f32_e32 v213, v213\n"
    "v_mfma_f32_16x16x32_bf16 v[36:39], a[188:191], v[172:175], v[36:39]\n"
    "ds_read_b64_tr_b16 a[178:179], v246 offset:6144\n"
    "ds_write_addtid_b32 v209 offset:4352\n"
    "v_mfma_f32_16x16x32_bf16 v[12:15], a[188:191], v[164:167], v[12:15]\n"
    "ds_read_b64_tr_b16 a[180:181], v246 offset:12288\n"
    "v_exp_f32_e32 v214, v214\n"
    "v_mfma_f32_16x16x32_bf16 v[12:15], a[184:187], v[160:163], v[12:15]\n"
    "s_waitcnt lgkmcnt(4)\n"
    "ds_read_b64_tr_b16 a[182:183], v246 offset:14336\n"
    "v_mfma_f32_16x16x32_bf16 v[16:19], a[160:163], v[160:163], v[16:19]\n"
    "ds_read_b64_tr_b16 a[184:185], v247 offset:4096\n"
    "ds_write_addtid_b32 v200 offset:0\n"
    "buffer_load_dwordx4 v[76:79], v253, s[8:11], s48 offen offset:1024 sc1\n"
    "v_mfma_f32_16x16x32_bf16 v[16:19], a[164:167], v[164:167], v[16:19]\n"
    "ds_read_b64_tr_b16 a[186:187], v247 offset:6144\n"
    "v_mfma_f32_16x16x32_bf16 v[40:43], a[164:167], v[172:175], v[40:43]\n"
    "ds_read_b64_tr_b16 a[188:189], v247 offset:12288\n"
    "v_mfma_f32_16x16x32_bf16 v[40:43], a[160:163], v[168:171], v[40:43]\n"
    "ds_read_b64_tr_b16 a[190:191], v247 offset:14336\n"
    "v_mfma_f32_16x16x32_bf16 v[44:47], a[168:171], v[168:171], v[44:47]\n"
    "ds_read_b128 a[160:163], v243 offset:36864\n"
    "ds_write_addtid_b32 v201 offset:256\n"
    "buffer_load_dwordx4 v[80:83], v253, s[8:11], s48 offen offset:2048 sc1\n"
    "v_mfma_f32_16x16x32_bf16 v[44:47], a[172:175], v[172:175], v[44:47]\n"
    "ds_read_b128 a[164:167], v240 offset:32768\n"
    "v_mfma_f32_16x16x32_bf16 v[20:23], a[172:175], v[164:167], v[20:23]\n"
    "v_exp_f32_e32 v215, v215\n"
    "v_mfma_f32_16x16x32_bf16 v[20:23], a[168:171], v[160:163], v[20:23]\n"
    "s_waitcnt lgkmcnt(3)\n"
    "ds_read_b128 a[168:171], v241 offset:32768\n"
    "v_cvt_pk_bf16_f32 v210, v212, v213\n"
    "v_mfma_f32_16x16x32_bf16 v[24:27], a[176:179], v[160:163], v[24:27]\n"
    "ds_read_b128 a[172:175], v240 offset:36864\n"
    "buffer_load_dwordx4 v[84:87], v253, s[8:11], s48 offen offset:3072 sc1\n"
    "v_mfma_f32_16x16x32_bf16 v[24:27], a[180:183], v[164:167], v[24:27]\n"
    "s_add_u32 s48, s56, 8192\n"
    "v_exp_f32_e32 v204, v204\n"
    "v_mfma_f32_16x16x32_bf16 v[48:51], a[180:183], v[172:175], v[48:51]\n"
    "s_waitcnt vmcnt(8)\n"
    "buffer_store_dword v148, v149, s[8:11], s87 offen offset:128 sc1\n"
    "v_exp_f32_e32 v205, v205\n"
    "v_mfma_f32_16x16x32_bf16 v[48:51], a[176:179], v[168:171], v[48:51]\n"
    "ds_read_b128 a[176:179], v241 offset:36864\n"
    "v_cvt_pk_bf16_f32 v211, v214, v215\n"
    "v_mfma_f32_16x16x32_bf16 v[52:55], a[184:187], v[168:171], v[52:55]\n"
    "ds_read_b128 a[180:183], v242 offset:32768\n"
    "buffer_load_dwordx4 v[88:91], v253, s[8:11], s48 offen offset:0 sc1\n"
    "v_mfma_f32_16x16x32_bf16 v[52:55], a[188:191], v[172:175], v[52:55]\n"
    "v_exp_f32_e32 v206, v206\n"
    "v_mfma_f32_16x16x32_bf16 v[28:31], a[188:191], v[164:167], v[28:31]\n"
    "v_exp_f32_e32 v207, v207\n"
    "v_mfma_f32_16x16x32_bf16 v[28:31], a[184:187], v[160:163], v[28:31]\n"
    "s_waitcnt lgkmcnt(2)\n"
    "ds_read_b128 a[184:187], v243 offset:32768\n"
    "v_cvt_pk_bf16_f32 v202, v204, v205\n"
    "v_mfma_f32_16x16x32_bf16 v[160:163], a[160:163], a[28:31], 0\n"
    "ds_read_b128 a[188:191], v242 offset:36864\n"
    "buffer_load_dwordx4 v[92:95], v253, s[8:11], s48 offen offset:1024 sc1\n"
    "v_mfma_f32_16x16x32_bf16 v[160:163], a[164:167], a[0:3], v[160:163]\n"
    "ds_write_addtid_b32 v210 offset:4608\n"
    "v_cvt_pk_bf16_f32 v203, v206, v207\n"
    "v_mfma_f32_16x16x32_bf16 v[168:171], a[164:167], a[32:35], 0\n"
    "ds_write_addtid_b32 v211 offset:4864\n"
    "v_mfma_f32_16x16x32_bf16 v[168:171], a[160:163], a[60:63], v[168:171]\n"
    "ds_read_b128 a[160:163], v243 offset:38912\n"
    "v_mfma_f32_16x16x32_bf16 v[168:171], a[168:171], a[36:39], v[168:171]\n"
    "ds_read_b128 a[164:167], v240 offset:34816\n"
    "buffer_load_dwordx4 v[96:99], v253, s[8:11], s48 offen offset:2048 sc1\n"
    "v_mfma_f32_16x16x32_bf16 v[168:171], a[172:175], a[40:43], v[168:171]\n"
    "ds_write_addtid_b32 v202 offset:512\n"
    "v_mfma_f32_16x16x32_bf16 v[160:163], a[172:175], a[8:11], v[160:163]\n"
    "ds_write_addtid_b32 v203 offset:768\n"
    "v_mfma_f32_16x16x32_bf16 v[160:163], a[168:171], a[4:7], v[160:163]\n"
    "s_waitcnt lgkmcnt(6)\n"
    "ds_read_b128 a[168:171], v241 offset:34816\n"
    "v_mfma_f32_16x16x32_bf16 v[160:163], a[176:179], a[12:15], v[160:163]\n"
    "ds_read_b128 a[172:175], v240 offset:38912\n"
    "buffer_load_dwordx4 v[100:103], v253, s[8:11], s48 offen offset:3072 sc1\n"
    "v_mfma_f32_16x16x32_bf16 v[160:163], a[180:183], a[16:19], v[160:163]\n"
    "s_add_u32 s48, s56, 12288\n"
    "v_mfma_f32_16x16x32_bf16 v[168:171], a[180:183], a[48:51], v[168:171]\n"
    "s_waitcnt lgkmcnt(2)\n"
    "s_barrier\n"
    "v_mfma_f32_16x16x32_bf16 v[168:171], a[176:179], a[44:47], v[168:171]\n"
    "ds_read_b128 a[176:179], v241 offset:38912\n"
    "v_mfma_f32_16x16x32_bf16 v[168:171], a[184:187], a[52:55], v[168:171]\n"
    "ds_read_b128 a[180:183], v242 offset:34816\n"
    "buffer_load_dwordx4 v[104:107], v253, s[8:11], s48 offen offset:0 sc1\n"
    "v_mfma_f32_16x16x32_bf16 v[168:171], a[188:191], a[56:59], v[168:171]\n"
    "ds_read2st64_b32 v[204:205], v250 offset0:0 offset1:1\n"
    "v_mfma_f32_16x16x32_bf16 v[160:163], a[188:191], a[24:27], v[160:163]\n"
    "ds_read2st64_b32 v[206:207], v250 offset0:2 offset1:3\n"
    "v_mfma_f32_16x16x32_bf16 v[160:163], a[184:187], a[20:23], v[160:163]\n"
    "s_waitcnt lgkmcnt(4)\n"
    "ds_read_b128 a[184:187], v243 offset:34816\n"
    "v_mfma_f32_16x16x32_bf16 v[164:167], a[160:163], a[28:31], 0\n"
    "ds_read_b128 a[188:191], v242 offset:38912\n"
    "buffer_load_dwordx4 v[108:111], v253, s[8:11], s48 offen offset:1024 sc1\n"
    "v_exp_f32_e32 v168, v168\n"
    "v_mfma_f32_16x16x32_bf16 v[164:167], a[164:167], a[0:3], v[164:167]\n"
    "ds_read2st64_b32 v[212:213], v250 offset0:16 offset1:17\n"
    "v_exp_f32_e32 v169, v169\n"
    "v_mfma_f32_16x16x32_bf16 v[172:175], a[164:167], a[32:35], 0\n"
    "ds_read2st64_b32 v[214:215], v250 offset0:18 offset1:19\n"
    "v_mfma_f32_16x16x32_bf16 v[172:175], a[160:163], a[60:63], v[172:175]\n"
    "ds_read_b64_tr_b16 a[160:161], v244 offset:16384\n"
    "v_exp_f32_e32 v170, v170\n"
    "v_mfma_f32_16x16x32_bf16 v[172:175], a[168:171], a[36:39], v[172:175]\n"
    "ds_read_b64_tr_b16 a[162:163], v244 offset:18432\n"
    "buffer_load_dwordx4 v[112:115], v253, s[8:11], s48 offen offset:2048 sc1\n"
    "v_mfma_f32_16x16x32_bf16 v[172:175], a[172:175], a[40:43], v[172:175]\n"
    "ds_read_b64_tr_b16 a[164:165], v244 offset:24576\n"
    "v_exp_f32_e32 v171, v171\n"
    "v_mfma_f32_16x16x32_bf16 v[164:167], a[172:175], a[8:11], v[164:167]\n"
    "ds_read_b64_tr_b16 a[166:167], v244 offset:26624\n"
    "v_cvt_pk_bf16_f32 v168, v168, v169\n"
    "v_mfma_f32_16x16x32_bf16 v[164:167], a[168:171], a[4:7], v[164:167]\n"
    "s_waitcnt lgkmcnt(6)\n"
    "ds_read_b64_tr_b16 a[168:169], v245 offset:16384\n"
    "v_mfma_f32_16x16x32_bf16 v[164:167], a[176:179], a[12:15], v[164:167]\n"
    "ds_read_b64_tr_b16 a[170:171], v245 offset:18432\n"
    "buffer_load_dwordx4 v[116:119], v253, s[8:11], s48 offen offset:3072 sc1\n"
    "v_exp_f32_e32 v160, v160\n"
    "v_mfma_f32_16x16x32_bf16 v[164:167], a[180:183], a[16:19], v[164:167]\n"
    "s_add_u32 s48, s56, 16384\n"
    "ds_read_b64_tr_b16 a[172:173], v245 offset:24576\n"
    "v_mfma_f32_16x16x32_bf16 v[172:175], a[180:183], a[48:51], v[172:175]\n"
    "ds_read_b64_tr_b16 a[174:175], v245 offset:26624\n"
    "v_exp_f32_e32 v161, v161\n"
    "v_mfma_f32_16x16x32_bf16 v[172:175], a[176:179], a[44:47], v[172:175]\n"
    "ds_read_b64_tr_b16 a[176:177], v246 offset:16384\n"
    "v_cvt_pk_bf16_f32 v169, v170, v171\n"
    "v_mfma_f32_16x16x32_bf16 v[172:175], a[184:187], a[52:55], v[172:175]\n"
    "ds_read_b64_tr_b16 a[178:179], v246 offset:18432\n"
    "buffer_load_dwordx4 a[28:31], v253, s[8:11], s48 offen offset:0 sc1\n"
    "v_mfma_f32_16x16x32_bf16 v[172:175], a[188:191], a[56:59], v[172:175]\n"
    "ds_read_b64_tr_b16 a[180:181], v246 offset:24576\n"
    "v_exp_f32_e32 v162, v162\n"
    "v_mfma_f32_16x16x32_bf16 v[164:167], a[188:191], a[24:27], v[164:167]\n"
    "ds_read_b64_tr_b16 a[182:183], v246 offset:26624\n"
    "v_exp_f32_e32 v163, v163\n"
    "v_mfma_f32_16x16x32_bf16 v[164:167], a[184:187], a[20:23], v[164:167]\n"
    "s_waitcnt lgkmcnt(4)\n"
    "ds_read_b64_tr_b16 a[184:185], v247 offset:16384\n"
    "v_mfma_f32_16x16x32_bf16 v[0:3], a[160:163], v[200:203], v[0:3]\n"
    "ds_read_b64_tr_b16 a[186:187], v247 offset:18432\n"
    "buffer_load_dwordx4 a[60:63], v253, s[8:11], s48 offen offset:1024 sc1\n"
    "v_cvt_pk_bf16_f32 v160, v160, v161\n"
    "v_mfma_f32_16x16x32_bf16 v[0:3], a[164:167], v[204:207], v[0:3]\n"
    "ds_read_b64_tr_b16 a[188:189], v247 offset:24576\n"
    "v_and_b32_e32 v242, 0x400, v240\n"
    "v_mfma_f32_16x16x32_bf16 a[248:251], a[164:167], v[212:215], a[248:251]\n"
    "ds_read_b64_tr_b16 a[190:191], v247 offset:26624\n"
    "v_cvt_pk_bf16_f32 v161, v162, v163\n"
    "v_mfma_f32_16x16x32_bf16 a[248:251], a[160:163], v[208:211], a[248:251]\n"
    "ds_read_b64_tr_b16 a[160:161], v244 offset:20480\n"
    "v_exp_f32_e32 v172, v172\n"
    "v_mfma_f32_16x16x32_bf16 a[252:255], a[168:171], v[208:211], a[252:255]\n"
    "ds_read_b64_tr_b16 a[162:163], v244 offset:22528\n"
    "buffer_load_dwordx4 a[0:3], v253, s[8:11], s48 offen offset:2048 sc1\n"
    "v_exp_f32_e32 v173, v173\n"
    "v_mfma_f32_16x16x32_bf16 a[252:255], a[172:175], v[212:215], a[252:255]\n"
    "s_waitcnt vmcnt(41)\n"
    "s_barrier\n"
    "v_mfma_f32_16x16x32_bf16 v[4:7], a[172:175], v[204:207], v[4:7]\n"
    "ds_read_b64_tr_b16 a[164:165], v244 offset:28672\n"
    "ds_write_addtid_b32 v168 offset:4096\n"
    "v_mfma_f32_16x16x32_bf16 v[4:7], a[168:171], v[200:203], v[4:7]\n"
    "s_waitcnt lgkmcnt(4)\n"
    "ds_read_b64_tr_b16 a[166:167], v244 offset:30720\n"
    "v_exp_f32_e32 v174, v174\n"
    "v_mfma_f32_16x16x32_bf16 v[8:11], a[176:179], v[200:203], v[8:11]\n"
    "ds_read_b64_tr_b16 a[168:169], v245 offset:20480\n"
    "buffer_load_dwordx4 a[32:35], v253, s[8:11], s48 offen offset:3072 sc1\n"
    "v_exp_f32_e32 v175, v175\n"
    "v_mfma_f32_16x16x32_bf16 v[8:11], a[180:183], v[204:207], v[8:11]\n"
    "s_add_u32 s48, s56, 20480\n"
    "ds_read_b64_tr_b16 a[170:171], v245 offset:22528\n"
    "ds_write_addtid_b32 v169 offset:4352\n"
    "v_mfma_f32_16x16x32_bf16 v[32:35], a[180:183], v[212:215], v[32:35]\n"
    "ds_read_b64_tr_b16 a[172:173], v245 offset:28672\n"
    "v_cvt_pk_bf16_f32 v170, v172, v173\n"
    "v_mfma_f32_16x16x32_bf16 v[32:35], a[176:179], v[208:211], v[32:35]\n"
    "ds_read_b64_tr_b16 a[174:175], v245 offset:30720\n"
    "v_exp_f32_e32 v164, v164\n"
    "v_mfma_f32_16x16x32_bf16 v[36:39], a[184:187], v[208:211], v[36:39]\n"
    "ds_read_b64_tr_b16 a[176:177], v246 offset:20480\n"
    "ds_write_addtid_b32 v160 offset:0\n"
    "buffer_load_dwordx4 a[4:7], v253, s[8:11], s48 offen offset:0 sc1\n"
    "v_mfma_f32_16x16x32_bf16 v[36:39], a[188:191], v[212:215], v[36:39]\n"
    "ds_read_b64_tr_b16 a[178:179], v246 offset:22528\n"
    "v_exp_f32_e32 v165, v165\n"
    "v_mfma_f32_16x16x32_bf16 v[12:15], a[188:191], v[204:207], v[12:15]\n"
    "ds_read_b64_tr_b16 a[180:181], v246 offset:28672\n"
    "ds_write_addtid_b32 v161 offset:256\n"
    "v_mfma_f32_16x16x32_bf16 v[12:15], a[184:187], v[200:203], v[12:15]\n"
    "s_waitcnt lgkmcnt(5)\n"
    "ds_read_b64_tr_b16 a[182:183], v246 offset:30720\n"
    "v_cvt_pk_bf16_f32 v171, v174, v175\n"
    "v_mfma_f32_16x16x32_bf16 v[16:19], a[160:163], v[200:203], v[16:19]\n"
    "ds_read_b64_tr_b16 a[184:185], v247 offset:20480\n"
    "buffer_load_dwordx4 a[36:39], v253, s[8:11], s48 offen offset:1024 sc1\n"
    "v_exp_f32_e32 v166, v166\n"
    "v_mfma_f32_16x16x32_bf16 v[16:19], a[164:167], v[204:207], v[16:19]\n"
    "ds_read_b64_tr_b16 a[186:187], v247 offset:22528\n"
    "ds_write_addtid_b32 v170 offset:4608\n"
    "v_mfma_f32_16x16x32_bf16 v[40:43], a[164:167], v[212:215], v[40:43]\n"
    "ds_read_b64_tr_b16 a[188:189], v247 offset:28672\n"
    "v_exp_f32_e32 v167, v167\n"
    "v_mfma_f32_16x16x32_bf16 v[40:43], a[160:163], v[208:211], v[40:43]\n"
    "ds_read_b64_tr_b16 a[190:191], v247 offset:30720\n"
    "ds_write_addtid_b32 v171 offset:4864\n"
    "v_mfma_f32_16x16x32_bf16 v[44:47], a[168:171], v[208:211], v[44:47]\n"
    "ds_read_b64_tr_b16 a[160:161], v244 offset:32768\n"
    "buffer_load_dwordx4 a[8:11], v253, s[8:11], s48 offen offset:2048 sc1\n"
    "v_cvt_pk_bf16_f32 v162, v164, v165\n"
    "v_mfma_f32_16x16x32_bf16 v[44:47], a[172:175], v[212:215], v[44:47]\n"
    "ds_read_b64_tr_b16 a[162:163], v244 offset:34816\n"
    "v_cvt_pk_bf16_f32 v163, v166, v167\n"
    "v_mfma_f32_16x16x32_bf16 v[20:23], a[172:175], v[204:207], v[20:23]\n"
    "ds_read_b64_tr_b16 a[164:165], v244 offset:40960\n"
    "v_and_b32_e32 v240, 0x3ff, v240\n"
    "v_mfma_f32_16x16x32_bf16 v[20:23], a[168:171], v[200:203], v[20:23]\n"
    "s_waitcnt lgkmcnt(4)\n"
    "ds_read_b64_tr_b16 a[166:167], v244 offset:43008\n"
    "ds_write_addtid_b32 v162 offset:512\n"
    "v_mfma_f32_16x16x32_bf16 v[24:27], a[176:179], v[200:203], v[24:27]\n"
    "ds_read_b64_tr_b16 a[168:169], v245 offset:32768\n"
    "buffer_load_dwordx4 a[40:43], v253, s[8:11], s48 offen offset:3072 sc1\n"
    "v_mfma_f32_16x16x32_bf16 v[24:27], a[180:183], v[204:207], v[24:27]\n"
    "s_add_u32 s48, s56, 24576\n"
    "ds_read_b64_tr_b16 a[170:171], v245 offset:34816\n"
    "v_mfma_f32_16x16x32_bf16 v[48:51], a[180:183], v[212:215], v[48:51]\n"
    "ds_read_b64_tr_b16 a[172:173], v245 offset:40960\n"
    "ds_write_addtid_b32 v163 offset:768\n"
    "v_mfma_f32_16x16x32_bf16 v[48:51], a[176:179], v[208:211], v[48:51]\n"
    "ds_read_b64_tr_b16 a[174:175], v245 offset:43008\n"
    "v_lshl_or_b32 v240, v242, 2, v240\n"
    "v_mfma_f32_16x16x32_bf16 v[52:55], a[184:187], v[208:211], v[52:55]\n"
    "ds_read_b64_tr_b16 a[176:177], v246 offset:32768\n"
    "buffer_load_dwordx4 a[12:15], v253, s[8:11], s48 offen offset:0 sc1\n"
    "v_mfma_f32_16x16x32_bf16 v[52:55], a[188:191], v[212:215], v[52:55]\n"
    "s_waitcnt lgkmcnt(2)\n"
    "s_barrier\n"
    "v_mfma_f32_16x16x32_bf16 v[28:31], a[188:191], v[204:207], v[28:31]\n"
    "ds_read_b64_tr_b16 a[178:179], v246 offset:34816\n"
    "v_mfma_f32_16x16x32_bf16 v[28:31], a[184:187], v[200:203], v[28:31]\n"
    "s_waitcnt lgkmcnt(0)\n"
    "ds_read_b64_tr_b16 a[180:181], v246 offset:40960\n"
    "ds_read2st64_b32 v[164:165], v250 offset0:0 offset1:1\n"
    "v_mfma_f32_16x16x32_bf16 v[0:3], a[160:163], v[160:163], v[0:3]\n"
    "ds_read_b64_tr_b16 a[182:183], v246 offset:43008\n"
    "ds_read2st64_b32 v[166:167], v250 offset0:2 offset1:3\n"
    "buffer_load_dwordx4 a[44:47], v253, s[8:11], s48 offen offset:1024 sc1\n"
    "v_mfma_f32_16x16x32_bf16 a[248:251], a[160:163], v[168:171], a[248:251]\n"
    "ds_read_b64_tr_b16 a[184:185], v247 offset:32768\n"
    "ds_read2st64_b32 v[172:173], v250 offset0:16 offset1:17\n"
    "v_mfma_f32_16x16x32_bf16 a[252:255], a[168:171], v[168:171], a[252:255]\n"
    "ds_read_b64_tr_b16 a[186:187], v247 offset:34816\n"
    "ds_read2st64_b32 v[174:175], v250 offset0:18 offset1:19\n"
    "v_mfma_f32_16x16x32_bf16 v[4:7], a[168:171], v[160:163], v[4:7]\n"
    "ds_read_b64_tr_b16 a[188:189], v247 offset:40960\n"
    "v_mfma_f32_16x16x32_bf16 v[8:11], a[176:179], v[160:163], v[8:11]\n"
    "ds_read_b64_tr_b16 a[190:191], v247 offset:43008\n"
    "buffer_load_dwordx4 a[16:19], v253, s[8:11], s48 offen offset:2048 sc1\n"
    "v_mfma_f32_16x16x32_bf16 v[32:35], a[176:179], v[168:171], v[32:35]\n"
    "s_waitcnt lgkmcnt(2)\n"
    "ds_read_b64_tr_b16 a[160:161], v244 offset:36864\n"
    "v_xor_b32_e32 v241, 64, v240\n"
    "v_mfma_f32_16x16x32_bf16 v[0:3], a[164:167], v[164:167], v[0:3]\n"
    "ds_read_b64_tr_b16 a[162:163], v244 offset:38912\n"
    "v_mfma_f32_16x16x32_bf16 a[248:251], a[164:167], v[172:175], a[248:251]\n"
    "s_waitcnt lgkmcnt(2)\n"
    "ds_read_b64_tr_b16 a[164:165], v244 offset:45056\n"
    "v_mfma_f32_16x16x32_bf16 a[252:255], a[172:175], v[172:175], a[252:255]\n"
    "ds_read_b64_tr_b16 a[166:167], v244 offset:47104\n"
    "buffer_load_dwordx4 a[48:51], v253, s[8:11], s48 offen offset:3072 sc1\n"
    "v_mfma_f32_16x16x32_bf16 v[4:7], a[172:175], v[164:167], v[4:7]\n"
    "s_add_u32 s48, s56, 28672\n"
    "ds_read_b64_tr_b16 a[168:169], v245 offset:36864\n"
    "v_mfma_f32_16x16x32_bf16 v[8:11], a[180:183], v[164:167], v[8:11]\n"
    "ds_read_b64_tr_b16 a[170:171], v245 offset:38912\n"
    "v_mfma_f32_16x16x32_bf16 v[32:35], a[180:183], v[172:175], v[32:35]\n"
    "ds_read_b64_tr_b16 a[172:173], v245 offset:45056\n"
    "v_accvgpr_read_b32 v216, a248\n"
    "v_mfma_f32_16x16x32_bf16 v[36:39], a[184:187], v[168:171], v[36:39]\n"
    "ds_read_b64_tr_b16 a[174:175], v245 offset:47104\n"
    "buffer_load_dwordx4 a[20:23], v253, s[8:11], s48 offen offset:0 sc1\n"
    "v_mfma_f32_16x16x32_bf16 v[36:39], a[188:191], v[172:175], v[36:39]\n"
    "s_lshl_b32 s45, s51, 16\n"
    "ds_read_b64_tr_b16 a[176:177], v246 offset:36864\n"
    "v_accvgpr_read_b32 v217, a249\n"
    "v_mfma_f32_16x16x32_bf16 v[12:15], a[188:191], v[164:167], v[12:15]\n"
    "ds_read_b64_tr_b16 a[178:179], v246 offset:38912\n"
    "buffer_load_dword v146, v145, s[8:11], s47 offen offset:128 sc1\n"
    "v_accvgpr_read_b32 v218, a250\n"
    "v_mfma_f32_16x16x32_bf16 v[12:15], a[184:187], v[160:163], v[12:15]\n"
    "s_waitcnt lgkmcnt(2)\n"
    "ds_read_b64_tr_b16 a[180:181], v246 offset:45056\n"
    "v_accvgpr_read_b32 v219, a251\n"
    "v_mfma_f32_16x16x32_bf16 v[16:19], a[160:163], v[160:163], v[16:19]\n"
    "ds_read_b64_tr_b16 a[182:183], v246 offset:47104\n"
    "buffer_load_dwordx4 a[52:55], v253, s[8:11], s48 offen offset:1024 sc1\n"
    "v_mfma_f32_16x16x32_bf16 v[16:19], a[164:167], v[164:167], v[16:19]\n"
    "s_lshl_b32 s46, s50, 11\n"
    "ds_read_b64_tr_b16 a[184:185], v247 offset:36864\n"
    "v_accvgpr_read_b32 v220, a252\n"
    "v_mfma_f32_16x16x32_bf16 v[40:43], a[164:167], v[172:175], v[40:43]\n"
    "s_add_u32 s46, s46, s45\n"
    "ds_read_b64_tr_b16 a[186:187], v247 offset:38912\n"
    "v_accvgpr_read_b32 v221, a253\n"
    "v_mfma_f32_16x16x32_bf16 v[40:43], a[160:163], v[168:171], v[40:43]\n"
    "s_add_u32 s46, s46, %3\n"
    "ds_read_b64_tr_b16 a[188:189], v247 offset:45056\n"
    "v_accvgpr_read_b32 v222, a254\n"
    "v_mfma_f32_16x16x32_bf16 v[44:47], a[168:171], v[168:171], v[44:47]\n"
    "ds_read_b64_tr_b16 a[190:191], v247 offset:47104\n"
    "buffer_load_dwordx4 a[24:27], v253, s[8:11], s48 offen offset:2048 sc1\n"
    "v_mfma_f32_16x16x32_bf16 v[44:47], a[172:175], v[172:175], v[44:47]\n"
    "s_add_u32 s46, s46, 81920\n"
    "ds_read_b128 a[160:163], v241 offset:11264\n"
    "v_accvgpr_read_b32 v223, a255\n"
    "v_mfma_f32_16x16x32_bf16 v[20:23], a[172:175], v[164:167], v[20:23]\n"
    "ds_read_b128 a[164:167], v241 offset:19456\n"
    "v_cvt_pk_bf16_f32 v216, v216, v217\n"
    "v_mfma_f32_16x16x32_bf16 v[20:23], a[168:171], v[160:163], v[20:23]\n"
    "s_waitcnt lgkmcnt(2)\n"
    "ds_read_b128 a[168:171], v241 offset:27648\n"
    "v_cvt_pk_bf16_f32 v217, v218, v219\n"
    "v_mfma_f32_16x16x32_bf16 v[24:27], a[176:179], v[160:163], v[24:27]\n"
    "ds_read_b128 a[172:175], v240 offset:8192\n"
    "buffer_load_dwordx4 a[56:59], v253, s[8:11], s48 offen offset:3072 sc1\n"
    "v_mfma_f32_16x16x32_bf16 v[24:27], a[180:183], v[164:167], v[24:27]\n"
    "v_cvt_pk_bf16_f32 v218, v220, v221\n"
    "v_cvt_pk_bf16_f32 v219, v222, v223\n"
    "v_mfma_f32_16x16x32_bf16 v[48:51], a[180:183], v[172:175], v[48:51]\n"
    "buffer_store_dwordx4 v[216:219], v253, s[8:11], s83 offen offset:0 sc1\n"
    "v_cvt_pk_bf16_f32 v32, v32, v33\n"
    "v_cvt_pk_bf16_f32 v33, v34, v35\n"
    "v_mfma_f32_16x16x32_bf16 v[48:51], a[176:179], v[168:171], v[48:51]\n"
    "ds_read_b128 a[176:179], v240 offset:16384\n"
    "v_cvt_pk_bf16_f32 v34, v36, v37\n"
    "v_mfma_f32_16x16x32_bf16 v[52:55], a[184:187], v[168:171], v[52:55]\n"
    "ds_read_b128 a[180:183], v240 offset:24576\n"
    "v_cvt_pk_bf16_f32 v35, v38, v39\n"
    "v_mfma_f32_16x16x32_bf16 v[52:55], a[188:191], v[172:175], v[52:55]\n"
    "buffer_store_dwordx4 v[32:35], v253, s[8:11], s83 offen offset:1024 sc1\n"
    "v_cvt_pk_bf16_f32 v40, v40, v41\n"
    "v_cvt_pk_bf16_f32 v41, v42, v43\n"
    "v_mfma_f32_16x16x32_bf16 v[28:31], a[188:191], v[164:167], v[28:31]\n"
    "v_cvt_pk_bf16_f32 v42, v44, v45\n"
    "v_cvt_pk_bf16_f32 v43, v46, v47\n"
    "v_mfma_f32_16x16x32_bf16 v[28:31], a[184:187], v[160:163], v[28:31]\n"
    "ds_read_b128 a[184:187], v241 offset:8192\n"
    "buffer_store_dwordx4 v[40:43], v253, s[8:11], s83 offen offset:2048 sc1\n"
    "v_cvt_pk_bf16_f32 v48, v48, v49\n"
    "v_add_u32_e32 v158, s46, v253\n"
    "v_mfma_f32_16x16x32_bf16 v[160:163], v[154:157], a[224:227], 0\n"
    "ds_read_b128 a[188:191], v241 offset:16384\n"
    "v_cvt_pk_bf16_f32 v49, v50, v51\n"
    "v_mfma_f32_16x16x32_bf16 v[164:167], v[152:155], a[224:227], 0\n"
    "v_cvt_pk_bf16_f32 v50, v52, v53\n"
    "v_cvt_pk_bf16_f32 v51, v54, v55\n"
    "v_mfma_f32_16x16x32_bf16 v[168:171], v[154:157], a[228:231], 0\n"
    "s_add_u32 s45, %3, 155840\n"
    "buffer_store_dwordx4 v[48:51], v253, s[8:11], s83 offen offset:3072 sc1\n"
    "v_and_b32_e32 v150, 15, v147\n"
    "v_lshlrev_b32_e32 v151, 9, v150\n"
    "v_mfma_f32_16x16x32_bf16 v[172:175], v[152:155], a[228:231], 0\n"
    "buffer_store_dword v55, v251, s[8:11], s60 offen offset:0 sc1\n"
    "v_lshl_add_u32 v150, v150, 2, s45\n"
    "v_and_or_b32 v151, v147, 48, v151\n"
    "v_mfma_f32_16x16x32_bf16 v[176:179], v[154:157], a[232:235], 0\n"
    "s_lshl_b32 s46, %2, 7\n"
    "s_add_u32 s58, s35, s46\n"
    "s_mul_i32 s45, %1, 74496\n"
    "v_mfma_f32_16x16x32_bf16 v[180:183], v[152:155], a[232:235], 0\n"
    "s_add_u32 s45, s45, 16265216\n"
    "ds_write_b128 v159, v[216:219] offset:0\n"
    "v_mfma_f32_16x16x32_bf16 v[184:187], v[154:157], a[236:239], 0\n"
    "s_lshl_b32 s54, %2, 11\n"
    "v_and_b32_e32 v147, 0xf0, v253\n"
    "v_add_u32_e32 v252, %3, v253\n"
    "v_mfma_f32_16x16x32_bf16 v[188:191], v[152:155], a[236:239], 0\n"
    "s_add_u32 s54, s54, s45\n"
    "ds_write_b128 v159, v[32:35] offset:1024\n"
    "v_mfma_f32_16x16x32_bf16 v[192:195], v[154:157], a[240:243], 0\n"
    "s_add_u32 s54, s54, 16384\n"
    "ds_write_b128 v159, v[40:43] offset:2048\n"
    "v_mfma_f32_16x16x32_bf16 v[196:199], v[152:155], a[240:243], 0\n"
    "s_lshl1_add_u32 s59, s50, s51\n"
    "s_lshl_b32 s59, s59, 12\n"
    "v_mfma_f32_16x16x32_bf16 v[200:203], v[154:157], a[244:247], 0\n"
    "s_add_u32 s46, s59, s45\n"
    "ds_write_b128 v159, v[48:51] offset:3072\n"
    "v_mfma_f32_16x16x32_bf16 v[204:207], v[152:155], a[244:247], 0\n"
    "s_waitcnt lgkmcnt(10)\n"
    "s_bitcmp1_b32 %1, 0\n"
    "s_cselect_b32 s54, s54, 2.0\n"
    "s_cselect_b32 s55, s58, 2.0\n"
    "v_mfma_f32_16x16x32_bf16 v[160:163], a[192:195], a[160:163], v[160:163]\n"
    "ds_write_b32 v254, v55 offset:0\n"
    "v_mfma_f32_16x16x32_bf16 v[164:167], a[196:199], a[160:163], v[164:167]\n"
    "s_add_u32 s61, s54, 0\n"
    "ds_write_b32 v255, v31 offset:0\n"
    "v_mfma_f32_16x16x32_bf16 v[168:171], a[200:203], a[160:163], v[168:171]\n"
    "s_waitcnt vmcnt(8)\n"
    "v_cmp_ne_u32_e32 vcc, s39, v146\n"
    "v_mfma_f32_16x16x32_bf16 v[172:175], a[204:207], a[160:163], v[172:175]\n"
    "ds_read_b128 a[160:163], v241 offset:24576\n"
    "v_mfma_f32_16x16x32_bf16 v[176:179], a[192:195], a[164:167], v[176:179]\n"
    "s_cbranch_vccnz .Lat_xpoll\n"
    ".Lat_xpoll_ret:\n"
    "v_mfma_f32_16x16x32_bf16 v[180:183], a[196:199], a[164:167], v[180:183]\n"
    "v_mfma_f32_16x16x32_bf16 v[184:187], a[200:203], a[164:167], v[184:187]\n"
    "buffer_load_dwordx4 a[224:227], v253, s[8:11], s61 offen offset:0 sc1\n"
    "v_mfma_f32_16x16x32_bf16 v[188:191], a[204:207], a[164:167], v[188:191]\n"
    "s_waitcnt lgkmcnt(11)\n"
    "ds_read_b128 a[164:167], v240 offset:9216\n"
    "v_mfma_f32_16x16x32_bf16 v[192:195], a[192:195], a[168:171], v[192:195]\n"
    "v_mfma_f32_16x16x32_bf16 v[196:199], a[196:199], a[168:171], v[196:199]\n"
    "v_mfma_f32_16x16x32_bf16 v[200:203], a[200:203], a[168:171], v[200:203]\n"
    "v_mfma_f32_16x16x32_bf16 v[204:207], a[204:207], a[168:171], v[204:207]\n"
    "ds_read_b128 a[168:171], v240 offset:17408\n"
    "buffer_load_dwordx4 a[228:231], v253, s[8:11], s61 offen offset:1024 sc1\n"
    "v_mfma_f32_16x16x32_bf16 v[160:163], a[208:211], a[172:175], v[160:163]\n"
    "s_waitcnt lgkmcnt(3)\n"
    "s_barrier\n"
    "v_mfma_f32_16x16x32_bf16 v[164:167], a[212:215], a[172:175], v[164:167]\n"
    "s_add_u32 s61, s54, 24832\n"
    "v_mfma_f32_16x16x32_bf16 v[168:171], a[216:219], a[172:175], v[168:171]\n"
    "v_mfma_f32_16x16x32_bf16 v[172:175], a[220:223], a[172:175], v[172:175]\n"
    "ds_read_b128 a[172:175], v240 offset:25600\n"
    "v_mfma_f32_16x16x32_bf16 v[176:179], a[208:211], a[176:179], v[176:179]\n"
    "buffer_load_dwordx4 a[232:235], v253, s[8:11], s61 offen offset:0 sc1\n"
    "v_mfma_f32_16x16x32_bf16 v[180:183], a[212:215], a[176:179], v[180:183]\n"
    "ds_read_b32 v232, v150 offset:0\n"
    "v_mfma_f32_16x16x32_bf16 v[184:187], a[216:219], a[176:179], v[184:187]\n"
    "v_mfma_f32_16x16x32_bf16 v[188:191], a[220:223], a[176:179], v[188:191]\n"
    "ds_read_b128 a[176:179], v241 offset:9216\n"
    "v_mfma_f32_16x16x32_bf16 v[192:195], a[208:211], a[180:183], v[192:195]\n"
    "v_mfma_f32_16x16x32_bf16 v[196:199], a[212:215], a[180:183], v[196:199]\n"
    "buffer_load_dwordx4 a[236:239], v253, s[8:11], s61 offen offset:1024 sc1\n"
    "v_mfma_f32_16x16x32_bf16 v[200:203], a[216:219], a[180:183], v[200:203]\n"
    "s_add_u32 s61, s54, 49664\n"
    "ds_read_b32 v233, v150 offset:256\n"
    "v_mfma_f32_16x16x32_bf16 v[204:207], a[220:223], a[180:183], v[204:207]\n"
    "ds_read_b128 a[180:183], v241 offset:17408\n"
    "v_mfma_f32_16x16x32_bf16 v[160:163], a[92:95], a[184:187], v[160:163]\n"
    "v_mfma_f32_16x16x32_bf16 v[164:167], a[124:127], a[184:187], v[164:167]\n"
    "v_mfma_f32_16x16x32_bf16 v[168:171], a[156:159], a[184:187], v[168:171]\n"
    "buffer_load_dwordx4 a[240:243], v253, s[8:11], s61 offen offset:0 sc1\n"
    "v_mfma_f32_16x16x32_bf16 v[172:175], a[64:67], a[184:187], v[172:175]\n"
    "ds_read_b128 a[184:187], v241 offset:25600\n"
    "v_mfma_f32_16x16x32_bf16 v[176:179], a[92:95], a[188:191], v[176:179]\n"
    "ds_read_b32 v234, v150 offset:512\n"
    "v_mfma_f32_16x16x32_bf16 v[180:183], a[124:127], a[188:191], v[180:183]\n"
    "v_mfma_f32_16x16x32_bf16 v[184:187], a[156:159], a[188:191], v[184:187]\n"
    "v_mfma_f32_16x16x32_bf16 v[188:191], a[64:67], a[188:191], v[188:191]\n"
    "s_waitcnt lgkmcnt(8)\n"
    "ds_read_b128 a[188:191], v240 offset:10240\n"
    "v_mfma_f32_16x16x32_bf16 v[192:195], a[92:95], a[160:163], v[192:195]\n"
    "buffer_load_dwordx4 a[244:247], v253, s[8:11], s61 offen offset:1024 sc1\n"
    "v_mfma_f32_16x16x32_bf16 v[196:199], a[124:127], a[160:163], v[196:199]\n"
    "s_add_u32 s61, s46, 0\n"
    "ds_read_b128 a[248:251], v158 offset:0\n"
    "v_mfma_f32_16x16x32_bf16 v[200:203], a[156:159], a[160:163], v[200:203]\n"
    "v_mfma_f32_16x16x32_bf16 v[204:207], a[64:67], a[160:163], v[204:207]\n"
    "ds_read_b128 a[160:163], v240 offset:18432\n"
    "v_mfma_f32_16x16x32_bf16 v[160:163], a[96:99], a[164:167], v[160:163]\n"
    "s_waitcnt vmcnt(6)\n"
    "buffer_store_dword v148, v149, s[8:11], s87 offen offset:0 sc1\n"
    "v_mfma_f32_16x16x32_bf16 v[164:167], a[128:131], a[164:167], v[164:167]\n"
    "buffer_load_dwordx4 a[192:195], v253, s[8:11], s61 offen offset:0 sc1\n"
    "v_mfma_f32_16x16x32_bf16 v[168:171], a[68:71], a[164:167], v[168:171]\n"
    "ds_read_b128 a[252:255], v158 offset:1024\n"
    "v_mfma_f32_16x16x32_bf16 v[172:175], a[100:103], a[164:167], v[172:175]\n"
    "s_waitcnt lgkmcnt(10)\n"
    "ds_read_b128 a[164:167], v240 offset:26624\n"
    "v_mfma_f32_16x16x32_bf16 v[176:179], a[96:99], a[168:171], v[176:179]\n"
    "v_mfma_f32_16x16x32_bf16 v[180:183], a[128:131], a[168:171], v[180:183]\n"
    "v_mfma_f32_16x16x32_bf16 v[184:187], a[68:71], a[168:171], v[184:187]\n"
    "buffer_load_dwordx4 a[196:199], v253, s[8:11], s61 offen offset:1024 sc1\n"
    "v_mfma_f32_16x16x32_bf16 v[188:191], a[100:103], a[168:171], v[188:191]\n"
    "s_lshl_b32 s52, %2, 11\n"
    "ds_read_b128 a[168:171], v241 offset:10240\n"
    "v_mfma_f32_16x16x32_bf16 v[192:195], a[96:99], a[172:175], v[192:195]\n"
    "v_mfma_f32_16x16x32_bf16 v[196:199], a[128:131], a[172:175], v[196:199]\n"
    "v_mfma_f32_16x16x32_bf16 v[200:203], a[68:71], a[172:175], v[200:203]\n"
    "v_mfma_f32_16x16x32_bf16 v[204:207], a[100:103], a[172:175], v[204:207]\n"
    "s_waitcnt lgkmcnt(8)\n"
    "ds_read_b128 a[172:175], v241 offset:18432\n"
    "v_mfma_f32_16x16x32_bf16 v[160:163], a[132:135], a[176:179], v[160:163]\n"
    "buffer_load_dwordx4 a[200:203], v253, s[8:11], s61 offen offset:2048 sc1\n"
    "v_mfma_f32_16x16x32_bf16 v[164:167], a[72:75], a[176:179], v[164:167]\n"
    "v_add_u32_e32 v251, s52, v252\n"
    "v_mfma_f32_16x16x32_bf16 v[168:171], a[104:107], a[176:179], v[168:171]\n"
    "v_mfma_f32_16x16x32_bf16 v[172:175], a[136:139], a[176:179], v[172:175]\n"
    "ds_read_b128 a[176:179], v241 offset:26624\n"
    "v_mfma_f32_16x16x32_bf16 v[176:179], a[132:135], a[180:183], v[176:179]\n"
    "v_mfma_f32_16x16x32_bf16 v[180:183], a[72:75], a[180:183], v[180:183]\n"
    "buffer_load_dwordx4 a[204:207], v253, s[8:11], s61 offen offset:3072 sc1\n"
    "v_mfma_f32_16x16x32_bf16 v[184:187], a[104:107], a[180:183], v[184:187]\n"
    "s_add_u32 s61, s46, 24832\n"
    "v_add_u32_e32 v158, s59, v252\n"
    "v_mfma_f32_16x16x32_bf16 v[188:191], a[136:139], a[180:183], v[188:191]\n"
    "s_waitcnt lgkmcnt(7)\n"
    "ds_read_b128 a[180:183], v240 offset:11264\n"
    "v_mfma_f32_16x16x32_bf16 v[192:195], a[132:135], a[184:187], v[192:195]\n"
    "v_mfma_f32_16x16x32_bf16 v[196:199], a[72:75], a[184:187], v[196:199]\n"
    "v_mfma_f32_16x16x32_bf16 v[200:203], a[104:107], a[184:187], v[200:203]\n"
    "buffer_load_dwordx4 a[208:211], v253, s[8:11], s61 offen offset:0 sc1\n"
    "v_mfma_f32_16x16x32_bf16 v[204:207], a[136:139], a[184:187], v[204:207]\n"
    "ds_read_b128 a[184:187], v240 offset:19456\n"
    "v_mfma_f32_16x16x32_bf16 v[160:163], a[76:79], a[188:191], v[160:163]\n"
    "v_and_b32_e32 v145, 12, v145\n"
    "v_mfma_f32_16x16x32_bf16 v[164:167], a[108:111], a[188:191], v[164:167]\n"
    "v_mfma_f32_16x16x32_bf16 v[168:171], a[140:143], a[188:191], v[168:171]\n"
    "v_mfma_f32_16x16x32_bf16 v[172:175], a[80:83], a[188:191], v[172:175]\n"
    "s_waitcnt lgkmcnt(5)\n"
    "ds_read_b128 a[188:191], v240 offset:27648\n"
    "v_mfma_f32_16x16x32_bf16 v[176:179], a[76:79], a[160:163], v[176:179]\n"
    "buffer_load_dwordx4 a[212:215], v253, s[8:11], s61 offen offset:1024 sc1\n"
    "v_mfma_f32_16x16x32_bf16 v[180:183], a[108:111], a[160:163], v[180:183]\n"
    "s_add_u32 s52, s32, 3\n"
    "v_mfma_f32_16x16x32_bf16 v[184:187], a[140:143], a[160:163], v[184:187]\n"
    "v_mfma_f32_16x16x32_bf16 v[188:191], a[80:83], a[160:163], v[188:191]\n"
    "v_mfma_f32_16x16x32_bf16 v[192:195], a[76:79], a[164:167], v[192:195]\n"
    "v_mfma_f32_16x16x32_bf16 v[196:199], a[108:111], a[164:167], v[196:199]\n"
    "buffer_load_dwordx4 a[216:219], v253, s[8:11], s61 offen offset:2048 sc1\n"
    "v_mfma_f32_16x16x32_bf16 v[200:203], a[140:143], a[164:167], v[200:203]\n"
    "s_and_b32 s52, s52, 3\n"
    "v_mfma_f32_16x16x32_bf16 v[204:207], a[80:83], a[164:167], v[204:207]\n"
    "s_waitcnt lgkmcnt(4)\n"
    "v_mfma_f32_16x16x32_bf16 v[160:163], a[112:115], a[168:171], v[160:163]\n"
    "v_mfma_f32_16x16x32_bf16 v[164:167], a[144:147], a[168:171], v[164:167]\n"
    "v_mfma_f32_16x16x32_bf16 v[168:171], a[84:87], a[168:171], v[168:171]\n"
    "buffer_load_dwordx4 a[220:223], v253, s[8:11], s61 offen offset:3072 sc1\n"
    "v_mfma_f32_16x16x32_bf16 v[172:175], a[116:119], a[168:171], v[172:175]\n"
    "s_add_u32 s61, s45, 24576\n"
    "v_mfma_f32_16x16x32_bf16 v[176:179], a[112:115], a[172:175], v[176:179]\n"
    "s_lshl_b32 s52, s52, 8\n"
    "v_mfma_f32_16x16x32_bf16 v[180:183], a[144:147], a[172:175], v[180:183]\n"
    "v_mfma_f32_16x16x32_bf16 v[184:187], a[84:87], a[172:175], v[184:187]\n"
    "v_mfma_f32_16x16x32_bf16 v[188:191], a[116:119], a[172:175], v[188:191]\n"
    "s_waitcnt lgkmcnt(2)\n"
    "buffer_load_dwordx3 v[236:238], v147, s[8:11], s61 offen offset:0 sc1\n"
    "v_mfma_f32_16x16x32_bf16 v[192:195], a[112:115], a[176:179], v[192:195]\n"
    "s_add_u32 s61, s45, 49408\n"
    "v_mfma_f32_16x16x32_bf16 v[196:199], a[144:147], a[176:179], v[196:199]\n"
    "s_add_u32 s52, s52, s47\n"
    "v_mfma_f32_16x16x32_bf16 v[200:203], a[84:87], a[176:179], v[200:203]\n"
    "v_mfma_f32_16x16x32_bf16 v[204:207], a[116:119], a[176:179], v[204:207]\n"
    "v_mfma_f32_16x16x32_bf16 v[160:163], a[148:151], a[180:183], v[160:163]\n"
    "buffer_load_dwordx3 v[248:250], v147, s[8:11], s61 offen offset:0 sc1\n"
    "v_mfma_f32_16x16x32_bf16 v[164:167], a[88:91], a[180:183], v[164:167]\n"
    "v_mfma_f32_16x16x32_bf16 v[168:171], a[120:123], a[180:183], v[168:171]\n"
    "v_mfma_f32_16x16x32_bf16 v[172:175], a[152:155], a[180:183], v[172:175]\n"
    "s_waitcnt lgkmcnt(0)\n"
    "v_mfma_f32_16x16x32_bf16 v[176:179], a[148:151], a[184:187], v[176:179]\n"
    "v_mfma_f32_16x16x32_bf16 v[180:183], a[88:91], a[184:187], v[180:183]\n"
    "v_mfma_f32_16x16x32_bf16 v[184:187], a[120:123], a[184:187], v[184:187]\n"
    "v_mfma_f32_16x16x32_bf16 v[188:191], a[152:155], a[184:187], v[188:191]\n"
    "v_mfma_f32_16x16x32_bf16 v[192:195], a[148:151], a[188:191], v[192:195]\n"
    "v_mfma_f32_16x16x32_bf16 v[196:199], a[88:91], a[188:191], v[196:199]\n"
    "v_mfma_f32_16x16x32_bf16 v[200:203], a[120:123], a[188:191], v[200:203]\n"
    "v_mfma_f32_16x16x32_bf16 v[204:207], a[152:155], a[188:191], v[204:207]\n"
    "v_mfma_f32_16x16x32_bf16 v[128:131], v[154:157], a[248:251], 0\n"
    "v_mfma_f32_16x16x32_bf16 v[132:135], v[152:155], a[248:251], 0\n"
    "v_mfma_f32_16x16x32_bf16 v[136:139], v[154:157], a[252:255], 0\n"
    "v_mfma_f32_16x16x32_bf16 v[140:143], v[152:155], a[252:255], 0\n"
    "s_waitcnt vmcnt(15)\n"
    "v_mfma_f32_16x16x32_bf16 v[128:131], v[154:157], a[224:227], v[128:131]\n"
    "v_mfma_f32_16x16x32_bf16 v[132:135], v[152:155], a[224:227], v[132:135]\n"
    "v_mfma_f32_16x16x32_bf16 v[136:139], v[154:157], a[228:231], v[136:139]\n"
    "buffer_load_dword v144, v145, s[8:11], s52 offen offset:0 sc1\n"
    "v_mfma_f32_16x16x32_bf16 v[140:143], v[152:155], a[228:231], v[140:143]\n"
    "s_waitcnt vmcnt(14)\n"
    "v_mfma_f32_16x16x32_bf16 v[128:131], v[154:157], a[232:235], v[128:131]\n"
    "v_mfma_f32_16x16x32_bf16 v[132:135], v[152:155], a[232:235], v[132:135]\n"
    "v_mfma_f32_16x16x32_bf16 v[136:139], v[154:157], a[236:239], v[136:139]\n"
    "v_mfma_f32_16x16x32_bf16 v[140:143], v[152:155], a[236:239], v[140:143]\n"
    "s_waitcnt vmcnt(12)\n"
    "v_mfma_f32_16x16x32_bf16 v[128:131], v[154:157], a[240:243], v[128:131]\n"
    "v_mfma_f32_16x16x32_bf16 v[132:135], v[152:155], a[240:243], v[132:135]\n"
    "v_mfma_f32_16x16x32_bf16 v[136:139], v[154:157], a[244:247], v[136:139]\n"
    "v_mfma_f32_16x16x32_bf16 v[140:143], v[152:155], a[244:247], v[140:143]\n"
    "s_waitcnt vmcnt(9)\n"
    "v_mfma_f32_16x16x32_bf16 v[0:3], v[154:157], a[192:195], v[0:3]\n"
    "v_cvt_pk_bf16_f32 v128, v128, v129\n"
    "v_mfma_f32_16x16x32_bf16 v[4:7], v[152:155], a[192:195], v[4:7]\n"
    "v_cvt_pk_bf16_f32 v129, v130, v131\n"
    "v_mfma_f32_16x16x32_bf16 v[8:11], v[154:157], a[196:199], v[8:11]\n"
    "v_cvt_pk_bf16_f32 v130, v132, v133\n"
    "v_mfma_f32_16x16x32_bf16 v[12:15], v[152:155], a[196:199], v[12:15]\n"
    "s_waitcnt vmcnt(7)\n"
    "v_cvt_pk_bf16_f32 v131, v134, v135\n"
    "v_mfma_f32_16x16x32_bf16 v[16:19], v[154:157], a[200:203], v[16:19]\n"
    "ds_write_b128 v251, v[128:131] offset:49152\n"
    "v_mfma_f32_16x16x32_bf16 v[20:23], v[152:155], a[200:203], v[20:23]\n"
    "v_cvt_pk_bf16_f32 v136, v136, v137\n"
    "v_mfma_f32_16x16x32_bf16 v[24:27], v[154:157], a[204:207], v[24:27]\n"
    "v_cvt_pk_bf16_f32 v137, v138, v139\n"
    "v_mfma_f32_16x16x32_bf16 v[28:31], v[152:155], a[204:207], v[28:31]\n"
    "s_waitcnt vmcnt(5)\n"
    "v_cvt_pk_bf16_f32 v138, v140, v141\n"
    "v_cvt_pk_bf16_f32 v139, v142, v143\n"
    "v_mfma_f32_16x16x32_bf16 v[0:3], v[154:157], a[208:211], v[0:3]\n"
    "ds_write_b128 v251, v[136:139] offset:50176\n"
    "v_mfma_f32_16x16x32_bf16 v[4:7], v[152:155], a[208:211], v[4:7]\n"
    "v_mfma_f32_16x16x32_bf16 v[8:11], v[154:157], a[212:215], v[8:11]\n"
    "s_waitcnt lgkmcnt(0)\n"
    "s_barrier\n"
    "ds_read_b128 a[128:131], v252 offset:49152\n"
    "ds_read_b128 a[132:135], v252 offset:50176\n"
    "ds_read_b128 a[136:139], v252 offset:51200\n"
    "v_mfma_f32_16x16x32_bf16 v[12:15], v[152:155], a[212:215], v[12:15]\n"
    "s_waitcnt vmcnt(3)\n"
    "v_mfma_f32_16x16x32_bf16 v[16:19], v[154:157], a[216:219], v[16:19]\n"
    "ds_read_b128 a[140:143], v252 offset:52224\n"
    "v_mfma_f32_16x16x32_bf16 v[20:23], v[152:155], a[216:219], v[20:23]\n"
    "v_mfma_f32_16x16x32_bf16 v[24:27], v[154:157], a[220:223], v[24:27]\n"
    "ds_read_b128 a[144:147], v252 offset:53248\n"
    "v_mfma_f32_16x16x32_bf16 v[28:31], v[152:155], a[220:223], v[28:31]\n"
    "s_waitcnt lgkmcnt(3)\n"
    "v_mfma_f32_16x16x32_bf16 v[216:219], v[56:59], a[128:131], 0\n"
    "ds_read_b128 a[148:151], v252 offset:54272\n"
    "v_mfma_f32_16x16x32_bf16 v[220:223], v[60:63], a[128:131], 0\n"
    "ds_read_b128 a[152:155], v252 offset:55296\n"
    "v_mfma_f32_16x16x32_bf16 v[224:227], v[64:67], a[128:131], 0\n"
    "s_waitcnt vmcnt(0)\n"
    "v_cmp_ne_u32_e32 vcc, s39, v144\n"
    "s_nop 3\n"
    "s_cbranch_vccnz .Lat_ypoll\n"
    ".Lat_ypoll_ret:\n"
    "s_add_u32 s61, s46, 49664\n"
    "buffer_load_dwordx4 a[160:163], v253, s[8:11], s61 offen offset:0 sc1\n"
    "buffer_load_dwordx4 a[164:167], v253, s[8:11], s61 offen offset:1024 sc1\n"
    "buffer_load_dwordx4 a[168:171], v253, s[8:11], s61 offen offset:2048 sc1\n"
    "buffer_load_dwordx4 a[172:175], v253, s[8:11], s61 offen offset:3072 sc1\n"
    "s_add_u32 s61, s45, 74240\n"
    "buffer_load_dwordx3 v[244:246], v147, s[8:11], s61 offen offset:0 sc1\n"
    "v_mfma_f32_16x16x32_bf16 v[228:231], v[68:71], a[128:131], 0\n"
    "ds_read_b128 a[156:159], v252 offset:56320\n"
    "v_mfma_f32_16x16x32_bf16 v[216:219], v[72:75], a[132:135], v[216:219]\n"
    "v_mfma_f32_16x16x32_bf16 v[220:223], v[76:79], a[132:135], v[220:223]\n"
    "s_waitcnt vmcnt(6)\n"
    "v_mfma_f32_16x16x32_bf16 v[224:227], v[80:83], a[132:135], v[224:227]\n"
    "v_mfma_f32_16x16x32_bf16 v[228:231], v[84:87], a[132:135], v[228:231]\n"
    "s_waitcnt lgkmcnt(4)\n"
    "v_mfma_f32_16x16x32_bf16 v[216:219], v[88:91], a[136:139], v[216:219]\n"
    "v_mfma_f32_16x16x32_bf16 v[220:223], v[92:95], a[136:139], v[220:223]\n"
    "v_add_f32_e32 v232, v232, v236\n"
    "v_mfma_f32_16x16x32_bf16 v[224:227], v[96:99], a[136:139], v[224:227]\n"
    "v_mfma_f32_16x16x32_bf16 v[228:231], v[100:103], a[136:139], v[228:231]\n"
    "v_mfma_f32_16x16x32_bf16 v[216:219], v[104:107], a[140:143], v[216:219]\n"
    "v_mfma_f32_16x16x32_bf16 v[220:223], v[108:111], a[140:143], v[220:223]\n"
    "v_add_f32_e32 v233, v233, v237\n"
    "v_mfma_f32_16x16x32_bf16 v[224:227], v[112:115], a[140:143], v[224:227]\n"
    "v_mfma_f32_16x16x32_bf16 v[228:231], v[116:119], a[140:143], v[228:231]\n"
    "s_waitcnt lgkmcnt(2)\n"
    "v_mfma_f32_16x16x32_bf16 v[216:219], a[28:31], a[144:147], v[216:219]\n"
    "v_mfma_f32_16x16x32_bf16 v[220:223], a[60:63], a[144:147], v[220:223]\n"
    "v_add_f32_e32 v234, v234, v238\n"
    "v_mfma_f32_16x16x32_bf16 v[224:227], a[0:3], a[144:147], v[224:227]\n"
    "v_mfma_f32_16x16x32_bf16 v[228:231], a[32:35], a[144:147], v[228:231]\n"
    "v_mfma_f32_16x16x32_bf16 v[216:219], a[4:7], a[148:151], v[216:219]\n"
    "v_mfma_f32_16x16x32_bf16 v[220:223], a[36:39], a[148:151], v[220:223]\n"
    "v_add_f32_e32 v232, v232, v248\n"
    "v_mfma_f32_16x16x32_bf16 v[224:227], a[8:11], a[148:151], v[224:227]\n"
    "v_mfma_f32_16x16x32_bf16 v[228:231], a[40:43], a[148:151], v[228:231]\n"
    "s_waitcnt lgkmcnt(0)\n"
    "v_mfma_f32_16x16x32_bf16 v[216:219], a[12:15], a[152:155], v[216:219]\n"
    "v_mfma_f32_16x16x32_bf16 v[220:223], a[44:47], a[152:155], v[220:223]\n"
    "v_add_f32_e32 v233, v233, v249\n"
    "v_mfma_f32_16x16x32_bf16 v[224:227], a[16:19], a[152:155], v[224:227]\n"
    "v_mfma_f32_16x16x32_bf16 v[228:231], a[48:51], a[152:155], v[228:231]\n"
    "v_mfma_f32_16x16x32_bf16 v[216:219], a[20:23], a[156:159], v[216:219]\n"
    "v_mfma_f32_16x16x32_bf16 v[220:223], a[52:55], a[156:159], v[220:223]\n"
    "v_add_f32_e32 v234, v234, v250\n"
    "v_mfma_f32_16x16x32_bf16 v[224:227], a[24:27], a[156:159], v[224:227]\n"
    "v_mfma_f32_16x16x32_bf16 v[228:231], a[56:59], a[156:159], v[228:231]\n"
    "s_waitcnt vmcnt(4)\n"
    "v_mfma_f32_16x16x32_bf16 v[0:3], v[154:157], a[160:163], v[0:3]\n"
    "v_mfma_f32_16x16x32_bf16 v[4:7], v[152:155], a[160:163], v[4:7]\n"
    "s_waitcnt vmcnt(3)\n"
    "v_mfma_f32_16x16x32_bf16 v[8:11], v[154:157], a[164:167], v[8:11]\n"
    "v_mfma_f32_16x16x32_bf16 v[12:15], v[152:155], a[164:167], v[12:15]\n"
    "s_waitcnt vmcnt(2)\n"
    "v_cvt_pk_bf16_f32 v0, v0, v1\n"
    "v_cvt_pk_bf16_f32 v1, v2, v3\n"
    "v_mfma_f32_16x16x32_bf16 v[16:19], v[154:157], a[168:171], v[16:19]\n"
    "v_cvt_pk_bf16_f32 v2, v4, v5\n"
    "v_cvt_pk_bf16_f32 v3, v6, v7\n"
    "v_mfma_f32_16x16x32_bf16 v[20:23], v[152:155], a[168:171], v[20:23]\n"
    "s_waitcnt vmcnt(1)\n"
    "ds_write_b128 v158, v[0:3] offset:32768\n"
    "v_mfma_f32_16x16x32_bf16 v[24:27], v[154:157], a[172:175], v[24:27]\n"
    "v_cvt_pk_bf16_f32 v8, v8, v9\n"
    "v_cvt_pk_bf16_f32 v9, v10, v11\n"
    "v_cvt_pk_bf16_f32 v10, v12, v13\n"
    "v_cvt_pk_bf16_f32 v11, v14, v15\n"
    "v_mfma_f32_16x16x32_bf16 v[28:31], v[152:155], a[172:175], v[28:31]\n"
    "ds_write_b128 v158, v[8:11] offset:33792\n"
    "v_cvt_pk_bf16_f32 v16, v16, v17\n"
    "v_cvt_pk_bf16_f32 v17, v18, v19\n"
    "v_cvt_pk_bf16_f32 v18, v20, v21\n"
    "v_cvt_pk_bf16_f32 v19, v22, v23\n"
    "ds_write_b128 v158, v[16:19] offset:34816\n"
    "v_cvt_pk_bf16_f32 v24, v24, v25\n"
    "v_cvt_pk_bf16_f32 v25, v26, v27\n"
    "v_cvt_pk_bf16_f32 v26, v28, v29\n"
    "v_cvt_pk_bf16_f32 v27, v30, v31\n"
    "ds_write_b128 v158, v[24:27] offset:35840\n"
    "s_waitcnt lgkmcnt(0)\n"
    "s_barrier\n"
    "ds_read_b128 a[96:99], v252 offset:40960\n"
    "ds_read_b128 a[100:103], v252 offset:41984\n"
    "ds_read_b128 a[104:107], v252 offset:43008\n"
    "ds_read_b128 a[108:111], v252 offset:44032\n"
    "ds_read_b128 a[112:115], v252 offset:45056\n"
    "ds_read_b128 a[116:119], v252 offset:46080\n"
    "ds_read_b128 a[120:123], v252 offset:47104\n"
    "ds_read_b128 a[124:127], v252 offset:48128\n"
    "s_waitcnt lgkmcnt(6)\n"
    "v_mfma_f32_16x16x32_bf16 v[120:123], v[56:59], a[96:99], 0\n"
    "s_waitcnt vmcnt(0)\n"
    "v_add_f32_e32 v234, v234, v246\n"
    "v_mfma_f32_16x16x32_bf16 v[124:127], v[60:63], a[96:99], 0\n"
    "ds_read_b128 a[64:67], v252 offset:32768\n"
    "v_mfma_f32_16x16x32_bf16 v[128:131], v[64:67], a[96:99], 0\n"
    "v_add_f32_e32 v233, v233, v245\n"
    "v_add_f32_e32 v232, v232, v244\n"
    "v_mfma_f32_16x16x32_bf16 v[132:135], v[68:71], a[96:99], 0\n"
    "v_rcp_f32_e32 v242, v234\n"
    "v_mfma_f32_16x16x32_bf16 v[120:123], v[72:75], a[100:103], v[120:123]\n"
    "s_add_u32 s48, s55, 16384\n"
    "ds_read_b128 a[68:71], v252 offset:33792\n"
    "v_mfma_f32_16x16x32_bf16 v[124:127], v[76:79], a[100:103], v[124:127]\n"
    "v_fma_f32 v216, v216, v242, v192\n"
    "v_fma_f32 v217, v217, v242, v193\n"
    "v_mfma_f32_16x16x32_bf16 v[128:131], v[80:83], a[100:103], v[128:131]\n"
    "ds_read_b128 a[72:75], v252 offset:34816\n"
    "v_fma_f32 v218, v218, v242, v194\n"
    "v_mfma_f32_16x16x32_bf16 v[132:135], v[84:87], a[100:103], v[132:135]\n"
    "s_waitcnt lgkmcnt(7)\n"
    "v_fma_f32 v219, v219, v242, v195\n"
    "v_fma_f32 v220, v220, v242, v196\n"
    "v_mfma_f32_16x16x32_bf16 v[120:123], v[88:91], a[104:107], v[120:123]\n"
    "v_fma_f32 v221, v221, v242, v197\n"
    "v_mfma_f32_16x16x32_bf16 v[124:127], v[92:95], a[104:107], v[124:127]\n"
    "ds_read_b128 a[76:79], v252 offset:35840\n"
    "v_fma_f32 v222, v222, v242, v198\n"
    "v_mfma_f32_16x16x32_bf16 v[128:131], v[96:99], a[104:107], v[128:131]\n"
    "v_fma_f32 v223, v223, v242, v199\n"
    "v_cvt_pk_bf16_f32 v216, v216, v217\n"
    "v_mfma_f32_16x16x32_bf16 v[132:135], v[100:103], a[104:107], v[132:135]\n"
    "ds_read_b128 a[80:83], v252 offset:36864\n"
    "v_cvt_pk_bf16_f32 v217, v218, v219\n"
    "v_mfma_f32_16x16x32_bf16 v[120:123], v[104:107], a[108:111], v[120:123]\n"
    "v_cvt_pk_bf16_f32 v218, v220, v221\n"
    "v_cvt_pk_bf16_f32 v219, v222, v223\n"
    "v_mfma_f32_16x16x32_bf16 v[124:127], v[108:111], a[108:111], v[124:127]\n"
    "buffer_store_dwordx4 v[216:219], v151, s[16:19], s48 offen offset:0 sc0 sc1\n"
    "v_fma_f32 v224, v224, v242, v200\n"
    "v_mfma_f32_16x16x32_bf16 v[128:131], v[112:115], a[108:111], v[128:131]\n"
    "ds_read_b128 a[84:87], v252 offset:37888\n"
    "v_fma_f32 v225, v225, v242, v201\n"
    "v_mfma_f32_16x16x32_bf16 v[132:135], v[116:119], a[108:111], v[132:135]\n"
    "s_waitcnt lgkmcnt(8)\n"
    "v_fma_f32 v226, v226, v242, v202\n"
    "v_fma_f32 v227, v227, v242, v203\n"
    "v_mfma_f32_16x16x32_bf16 v[120:123], a[28:31], a[112:115], v[120:123]\n"
    "ds_read_b128 a[88:91], v252 offset:38912\n"
    "v_fma_f32 v228, v228, v242, v204\n"
    "v_mfma_f32_16x16x32_bf16 v[124:127], a[60:63], a[112:115], v[124:127]\n"
    "v_fma_f32 v229, v229, v242, v205\n"
    "v_fma_f32 v230, v230, v242, v206\n"
    "v_mfma_f32_16x16x32_bf16 v[128:131], a[0:3], a[112:115], v[128:131]\n"
    "v_fma_f32 v231, v231, v242, v207\n"
    "v_cvt_pk_bf16_f32 v224, v224, v225\n"
    "v_mfma_f32_16x16x32_bf16 v[132:135], a[32:35], a[112:115], v[132:135]\n"
    "ds_read_b128 a[92:95], v252 offset:39936\n"
    "v_cvt_pk_bf16_f32 v225, v226, v227\n"
    "v_mfma_f32_16x16x32_bf16 v[120:123], a[4:7], a[116:119], v[120:123]\n"
    "v_cvt_pk_bf16_f32 v226, v228, v229\n"
    "v_cvt_pk_bf16_f32 v227, v230, v231\n"
    "v_mfma_f32_16x16x32_bf16 v[124:127], a[36:39], a[116:119], v[124:127]\n"
    "buffer_store_dwordx4 v[224:227], v151, s[16:19], s48 offen offset:64 sc0 sc1\n"
    "v_mfma_f32_16x16x32_bf16 v[128:131], a[8:11], a[116:119], v[128:131]\n"
    "s_bitcmp1_b32 %1, 0\n"
    "s_cselect_b64 vcc, -1, 0\n"
    "v_mfma_f32_16x16x32_bf16 v[132:135], a[40:43], a[116:119], v[132:135]\n"
    "s_waitcnt lgkmcnt(8)\n"
    "v_cndmask_b32_e32 v234, v233, v234, vcc\n"
    "v_mfma_f32_16x16x32_bf16 v[120:123], a[12:15], a[120:123], v[120:123]\n"
    "s_mov_b32 s61, 0x12800000\n"
    "v_rcp_f32_e32 v241, v233\n"
    "v_mfma_f32_16x16x32_bf16 v[124:127], a[44:47], a[120:123], v[124:127]\n"
    "v_rcp_f32_e32 v240, v232\n"
    "v_mfma_f32_16x16x32_bf16 v[128:131], a[16:19], a[120:123], v[128:131]\n"
    "v_subrev_u32_e32 v236, s61, v232\n"
    "v_mfma_f32_16x16x32_bf16 v[132:135], a[48:51], a[120:123], v[132:135]\n"
    "v_subrev_u32_e32 v237, s61, v233\n"
    "v_mfma_f32_16x16x32_bf16 v[120:123], a[20:23], a[124:127], v[120:123]\n"
    "v_subrev_u32_e32 v238, s61, v234\n"
    "v_max3_u32 v236, v236, v237, v238\n"
    "v_mfma_f32_16x16x32_bf16 v[124:127], a[52:55], a[124:127], v[124:127]\n"
    "buffer_store_dword v148, v149, s[8:11], s88 offen offset:0 sc1\n"
    "v_cmp_le_u32_e32 vcc, 0x5f000000, v236\n"
    "v_mfma_f32_16x16x32_bf16 v[128:131], a[24:27], a[124:127], v[128:131]\n"
    "v_mfma_f32_16x16x32_bf16 v[132:135], a[56:59], a[124:127], v[132:135]\n"
    "s_waitcnt lgkmcnt(0)\n"
    "v_mfma_f32_16x16x32_bf16 v[32:35], v[56:59], a[64:67], 0\n"
    "s_cbranch_vccnz .Lat_exit\n"
    "v_mfma_f32_16x16x32_bf16 v[36:39], v[60:63], a[64:67], 0\n"
    "s_add_u32 s48, s58, 8192\n"
    "v_fma_f32 v120, v120, v241, v176\n"
    "v_mfma_f32_16x16x32_bf16 v[32:35], v[72:75], a[68:71], v[32:35]\n"
    "v_fma_f32 v121, v121, v241, v177\n"
    "v_mfma_f32_16x16x32_bf16 v[36:39], v[76:79], a[68:71], v[36:39]\n"
    "v_fma_f32 v122, v122, v241, v178\n"
    "v_mfma_f32_16x16x32_bf16 v[32:35], v[88:91], a[72:75], v[32:35]\n"
    "v_fma_f32 v123, v123, v241, v179\n"
    "v_mfma_f32_16x16x32_bf16 v[36:39], v[92:95], a[72:75], v[36:39]\n"
    "v_fma_f32 v124, v124, v241, v180\n"
    "v_mfma_f32_16x16x32_bf16 v[32:35], v[104:107], a[76:79], v[32:35]\n"
    "v_fma_f32 v125, v125, v241, v181\n"
    "v_mfma_f32_16x16x32_bf16 v[36:39], v[108:111], a[76:79], v[36:39]\n"
    "v_fma_f32 v126, v126, v241, v182\n"
    "v_fma_f32 v127, v127, v241, v183\n"
    "v_mfma_f32_16x16x32_bf16 v[32:35], a[28:31], a[80:83], v[32:35]\n"
    "v_cvt_pk_bf16_f32 v120, v120, v121\n"
    "v_mfma_f32_16x16x32_bf16 v[36:39], a[60:63], a[80:83], v[36:39]\n"
    "v_cvt_pk_bf16_f32 v121, v122, v123\n"
    "v_mfma_f32_16x16x32_bf16 v[32:35], a[4:7], a[84:87], v[32:35]\n"
    "v_cvt_pk_bf16_f32 v122, v124, v125\n"
    "v_mfma_f32_16x16x32_bf16 v[36:39], a[36:39], a[84:87], v[36:39]\n"
    "v_cvt_pk_bf16_f32 v123, v126, v127\n"
    "v_mfma_f32_16x16x32_bf16 v[32:35], a[12:15], a[88:91], v[32:35]\n"
    "buffer_store_dwordx4 v[120:123], v151, s[16:19], s48 offen offset:0 sc0 sc1\n"
    "v_fma_f32 v128, v128, v241, v184\n"
    "v_mfma_f32_16x16x32_bf16 v[36:39], a[44:47], a[88:91], v[36:39]\n"
    "v_fma_f32 v129, v129, v241, v185\n"
    "v_fma_f32 v130, v130, v241, v186\n"
    "v_mfma_f32_16x16x32_bf16 v[32:35], a[20:23], a[92:95], v[32:35]\n"
    "v_fma_f32 v131, v131, v241, v187\n"
    "v_mfma_f32_16x16x32_bf16 v[36:39], a[52:55], a[92:95], v[36:39]\n"
    "v_fma_f32 v132, v132, v241, v188\n"
    "v_mfma_f32_16x16x32_bf16 v[40:43], v[64:67], a[64:67], 0\n"
    "v_fma_f32 v133, v133, v241, v189\n"
    "v_mfma_f32_16x16x32_bf16 v[44:47], v[68:71], a[64:67], 0\n"
    "v_fma_f32 v134, v134, v241, v190\n"
    "v_fma_f32 v135, v135, v241, v191\n"
    "v_mfma_f32_16x16x32_bf16 v[40:43], v[80:83], a[68:71], v[40:43]\n"
    "v_cvt_pk_bf16_f32 v128, v128, v129\n"
    "v_mfma_f32_16x16x32_bf16 v[44:47], v[84:87], a[68:71], v[44:47]\n"
    "v_cvt_pk_bf16_f32 v129, v130, v131\n"
    "v_mfma_f32_16x16x32_bf16 v[40:43], v[96:99], a[72:75], v[40:43]\n"
    "v_cvt_pk_bf16_f32 v130, v132, v133\n"
    "v_cvt_pk_bf16_f32 v131, v134, v135\n"
    "v_mfma_f32_16x16x32_bf16 v[44:47], v[100:103], a[72:75], v[44:47]\n"
    "buffer_store_dwordx4 v[128:131], v151, s[16:19], s48 offen offset:64 sc0 sc1\n"
    "v_mfma_f32_16x16x32_bf16 v[40:43], v[112:115], a[76:79], v[40:43]\n"
    "s_add_u32 s48, s58, 0\n"
    "v_fma_f32 v32, v32, v240, v160\n"
    "v_mfma_f32_16x16x32_bf16 v[44:47], v[116:119], a[76:79], v[44:47]\n"
    "v_fma_f32 v33, v33, v240, v161\n"
    "v_mfma_f32_16x16x32_bf16 v[40:43], a[0:3], a[80:83], v[40:43]\n"
    "v_fma_f32 v34, v34, v240, v162\n"
    "v_mfma_f32_16x16x32_bf16 v[44:47], a[32:35], a[80:83], v[44:47]\n"
    "v_fma_f32 v35, v35, v240, v163\n"
    "v_mfma_f32_16x16x32_bf16 v[40:43], a[8:11], a[84:87], v[40:43]\n"
    "v_fma_f32 v36, v36, v240, v164\n"
    "v_fma_f32 v37, v37, v240, v165\n"
    "v_mfma_f32_16x16x32_bf16 v[44:47], a[40:43], a[84:87], v[44:47]\n"
    "v_fma_f32 v38, v38, v240, v166\n"
    "v_mfma_f32_16x16x32_bf16 v[40:43], a[16:19], a[88:91], v[40:43]\n"
    "v_fma_f32 v39, v39, v240, v167\n"
    "v_mfma_f32_16x16x32_bf16 v[44:47], a[48:51], a[88:91], v[44:47]\n"
    "v_cvt_pk_bf16_f32 v32, v32, v33\n"
    "v_cvt_pk_bf16_f32 v33, v34, v35\n"
    "v_mfma_f32_16x16x32_bf16 v[40:43], a[24:27], a[92:95], v[40:43]\n"
    "v_cvt_pk_bf16_f32 v34, v36, v37\n"
    "v_cvt_pk_bf16_f32 v35, v38, v39\n"
    "v_mfma_f32_16x16x32_bf16 v[44:47], a[56:59], a[92:95], v[44:47]\n"
    "buffer_store_dwordx4 v[32:35], v151, s[16:19], s48 offen offset:0 sc0 sc1\n"
    "s_nop 2\n"
    "v_fma_f32 v40, v40, v240, v168\n"
    "v_fma_f32 v41, v41, v240, v169\n"
    "v_fma_f32 v42, v42, v240, v170\n"
    "v_fma_f32 v43, v43, v240, v171\n"
    "v_fma_f32 v44, v44, v240, v172\n"
    "v_fma_f32 v45, v45, v240, v173\n"
    "v_fma_f32 v46, v46, v240, v174\n"
    "v_fma_f32 v47, v47, v240, v175\n"
    "v_cvt_pk_bf16_f32 v40, v40, v41\n"
    "v_cvt_pk_bf16_f32 v41, v42, v43\n"
    "v_cvt_pk_bf16_f32 v42, v44, v45\n"
    "v_cvt_pk_bf16_f32 v43, v46, v47\n"
    "buffer_store_dwordx4 v[40:43], v151, s[16:19], s48 offen offset:64 sc0 sc1\n"
    "s_endpgm\n"
    ".Lat_wpoll:\n"
    "s_mov_b32 s53, 0x400000\n"
    ".Lat_wpoll_l:\n"
    "s_sleep 1\n"
    "buffer_load_dword v255, v254, s[8:11], s45 offen offset:0 sc1\n"
    "s_waitcnt vmcnt(0)\n"
    "v_cmp_ne_u32_e32 vcc, s39, v255\n"
    "s_sub_u32 s53, s53, 1\n"
    "s_cbranch_scc1 .Lat_wpoll_ret\n"
    "s_nop 2\n"
    "s_cbranch_vccnz .Lat_wpoll_l\n"
    "s_branch .Lat_wpoll_ret\n"
    ".Lat_xpoll:\n"
    "s_mov_b32 s53, 0x400000\n"
    ".Lat_xpoll_l:\n"
    "s_sleep 0\n"
    "buffer_load_dword v146, v145, s[8:11], s47 offen offset:128 sc1\n"
    "s_waitcnt vmcnt(0)\n"
    "v_cmp_ne_u32_e32 vcc, s39, v146\n"
    "s_sub_u32 s53, s53, 1\n"
    "s_cbranch_scc1 .Lat_xpoll_ret\n"
    "s_nop 2\n"
    "s_cbranch_vccnz .Lat_xpoll_l\n"
    "s_branch .Lat_xpoll_ret\n"
    ".Lat_ypoll:\n"
    "s_mov_b32 s53, 0x400000\n"
    ".Lat_ypoll_l:\n"
    "s_sleep 0\n"
    "buffer_load_dword v144, v145, s[8:11], s52 offen offset:0 sc1\n"
    "s_waitcnt vmcnt(0)\n"
    "v_cmp_ne_u32_e32 vcc, s39, v144\n"
    "s_sub_u32 s53, s53, 1\n"
    "s_cbranch_scc1 .Lat_ypoll_ret\n"
    "s_nop 2\n"
    "s_cbranch_vccnz .Lat_ypoll_l\n"
    "s_branch .Lat_ypoll_ret\n"
    ".Lat_apoll:\n"
    "s_mov_b32 s53, 0x400000\n"
    ".Lat_apoll_l:\n"
    "s_sleep 1\n"
    "buffer_load_dword v255, v252, s[8:11], 0 offen offset:0 sc1\n"
    "s_waitcnt vmcnt(0)\n"
    "v_cmp_ne_u32_e32 vcc, s39, v255\n"
    "s_sub_u32 s53, s53, 1\n"
    "s_cbranch_scc1 .Lat_apoll_ret\n"
    "s_nop 2\n"
    "s_cbranch_vccnz .Lat_apoll_l\n"
    "s_branch .Lat_apoll_ret\n"
    ".Lat_h1poll:\n"
    "s_mov_b32 s53, 0x400000\n"
    ".Lat_h1poll_l:\n"
    "s_sleep 1\n"
    "buffer_load_dword v0, v3, s[8:11], s59 offen offset:0 sc1\n"
    "s_waitcnt vmcnt(0)\n"
    "v_cmp_ne_u32_e32 vcc, s39, v0\n"
    "s_sub_u32 s53, s53, 1\n"
    "s_cbranch_scc1 .Lat_h1poll_ret\n"
    "s_nop 2\n"
    "s_cbranch_vccnz .Lat_h1poll_l\n"
    "s_branch .Lat_h1poll_ret\n"
    ".Lat_hpoll:\n"
    "s_mov_b32 s53, 0x400000\n"
    ".Lat_hpoll_l:\n"
    "s_sleep 1\n"
    "buffer_load_dword v0, v253, s[8:11], s59 offen offset:0 sc1\n"
    "buffer_load_dword v1, v2, s[8:11], s59 offen offset:0 sc1\n"
    "s_waitcnt vmcnt(0)\n"
    "v_xor_b32_e32 v1, s39, v1\n"
    "v_xor_b32_e32 v0, s39, v0\n"
    "v_or_b32_e32 v0, v0, v1\n"
    "v_cmp_ne_u32_e32 vcc, 0, v0\n"
    "s_sub_u32 s53, s53, 1\n"
    "s_cbranch_scc1 .Lat_hpoll_ret\n"
    "s_nop 2\n"
    "s_cbranch_vccnz .Lat_hpoll_l\n"
    "s_branch .Lat_hpoll_ret\n"
    ".Lat_qpoll:\n"
    "s_mov_b32 s53, 0x400000\n"
    ".Lat_qpoll_l:\n"
    "s_sleep 0\n"
    "buffer_load_dword v11, v5, s[8:11], s47 offen offset:0 sc1\n"
    "s_waitcnt vmcnt(0)\n"
    "v_cmp_ne_u32_e32 vcc, s39, v11\n"
    "s_sub_u32 s53, s53, 1\n"
    "s_cbranch_scc1 .Lat_qpoll_ret\n"
    "s_nop 2\n"
    "s_cbranch_vccnz .Lat_qpoll_l\n"
    "s_branch .Lat_qpoll_ret\n"
    ".Lat_lpoll:\n"
    "s_mov_b32 s53, 0x400000\n"
    ".Lat_lpoll_l:\n"
    "s_sleep 1\n"
    "buffer_load_dword v154, off, s[8:11], s80 sc1\n"
    "s_waitcnt vmcnt(0)\n"
    "v_cmp_ne_u32_e32 vcc, s39, v154\n"
    "s_sub_u32 s53, s53, 1\n"
    "s_cbranch_scc1 .Lat_lpoll_ret\n"
    "s_nop 2\n"
    "s_cbranch_vccnz .Lat_lpoll_l\n"
    ".Lat_lpoll_ret:\n"
    "v_lshlrev_b32_e32 v253, 2, v249\n"
    "s_mov_b32 m0, s82\n"
    "s_nop 0\n"
    "buffer_load_dwordx4 v253, s[8:11], s81 offen offset:0 sc1 lds\n"
    "s_waitcnt vmcnt(0)\n"
    "s_branch .Lat_g\n"
    ".Lat_exit:\n"
    "s_waitcnt vmcnt(0)\n"
    "s_nop 15\n"
    ".Lat_end:\n"
    "s_waitcnt lgkmcnt(0)\n"
    : "+s"(ka), "+s"(b), "+s"(wid) : "s"((unsigned)(size_t)lds)
    : "v0", "v1", "v2", "v3", "v4", "v5", "v6", "v7", "v8", "v9", "v10", "v11", "v12", "v13", "v14", "v15", "v16", "v17", "v18", "v19", "v20", "v21", "v22", "v23", "v24", "v25", "v26", "v27", "v28", "v29", "v30", "v31", "v32", "v33", "v34", "v35", "v36", "v37", "v38", "v39", "v40", "v41", "v42", "v43", "v44", "v45", "v46", "v47", "v48", "v49", "v50", "v51", "v52", "v53", "v54", "v55", "v56", "v57", "v58", "v59", "v60", "v61", "v62", "v63", "v64", "v65", "v66", "v67", "v68", "v69", "v70", "v71", "v72", "v73", "v74", "v75", "v76", "v77", "v78", "v79", "v80", "v81", "v82", "v83", "v84", "v85", "v86", "v87", "v88", "v89", "v90", "v91", "v92", "v93", "v94", "v95", "v96", "v97", "v98", "v99", "v100", "v101", "v102", "v103", "v104", "v105", "v106", "v107", "v108", "v109", "v110", "v111", "v112", "v113", "v114", "v115", "v116", "v117", "v118", "v119", "v120", "v121", "v122", "v123", "v124", "v125", "v126", "v127", "v128", "v129", "v130", "v131", "v132", "v133", "v134", "v135", "v136", "v137", "v138", "v139", "v140", "v141", "v142", "v143", "v144", "v145", "v146", "v147", "v148", "v149", "v150", "v151", "v152", "v153", "v154", "v155", "v156", "v157", "v158", "v159", "v160", "v161", "v162", "v163", "v164", "v165", "v166", "v167", "v168", "v169", "v170", "v171", "v172", "v173", "v174", "v175", "v176", "v177", "v178", "v179", "v180", "v181", "v182", "v183", "v184", "v185", "v186", "v187", "v188", "v189", "v190", "v191", "v192", "v193", "v194", "v195", "v196", "v197", "v198", "v199", "v200", "v201", "v202", "v203", "v204", "v205", "v206", "v207", "v208", "v209", "v210", "v211", "v212", "v213", "v214", "v215", "v216", "v217", "v218", "v219", "v220", "v221", "v222", "v223", "v224", "v225", "v226", "v227", "v228", "v229", "v230", "v231", "v232", "v233", "v234", "v235", "v236", "v237", "v238", "v239", "v240", "v241", "v242", "v243", "v244", "v245", "v246", "v247", "v248", "v249", "v250", "v251", "v252", "v253", "v254", "v255", "a0", "a1", "a2", "a3", "a4", "a5", "a6", "a7", "a8", "a9", "a10", "a11", "a12", "a13", "a14", "a15", "a16", "a17", "a18", "a19", "a20", "a21", "a22", "a23", "a24", "a25", "a26", "a27", "a28", "a29", "a30", "a31", "a32", "a33", "a34", "a35", "a36", "a37", "a38", "a39", "a40", "a41", "a42", "a43", "a44", "a45", "a46", "a47", "a48", "a49", "a50", "a51", "a52", "a53", "a54", "a55", "a56", "a57", "a58", "a59", "a60", "a61", "a62", "a63", "a64", "a65", "a66", "a67", "a68", "a69", "a70", "a71", "a72", "a73", "a74", "a75", "a76", "a77", "a78", "a79", "a80", "a81", "a82", "a83", "a84", "a85", "a86", "a87", "a88", "a89", "a90", "a91", "a92", "a93", "a94", "a95", "a96", "a97", "a98", "a99", "a100", "a101", "a102", "a103", "a104", "a105", "a106", "a107", "a108", "a109", "a110", "a111", "a112", "a113", "a114", "a115", "a116", "a117", "a118", "a119", "a120", "a121", "a122", "a123", "a124", "a125", "a126", "a127", "a128", "a129", "a130", "a131", "a132", "a133", "a134", "a135", "a136", "a137", "a138", "a139", "a140", "a141", "a142", "a143", "a144", "a145", "a146", "a147", "a148", "a149", "a150", "a151", "a152", "a153", "a154", "a155", "a156", "a157", "a158", "a159", "a160", "a161", "a162", "a163", "a164", "a165", "a166", "a167", "a168", "a169", "a170", "a171", "a172", "a173", "a174", "a175", "a176", "a177", "a178", "a179", "a180", "a181", "a182", "a183", "a184", "a185", "a186", "a187", "a188", "a189", "a190", "a191", "a192", "a193", "a194", "a195", "a196", "a197", "a198", "a199", "a200", "a201", "a202", "a203", "a204", "a205", "a206", "a207", "a208", "a209", "a210", "a211", "a212", "a213", "a214", "a215", "a216", "a217", "a218", "a219", "a220", "a221", "a222", "a223", "a224", "a225", "a226", "a227", "a228", "a229", "a230", "a231", "a232", "a233", "a234", "a235", "a236", "a237", "a238", "a239", "a240", "a241", "a242", "a243", "a244", "a245", "a246", "a247", "a248", "a249", "a250", "a251", "a252", "a253", "a254", "a255", "s8", "s9", "s10", "s11", "s12", "s13", "s14", "s15", "s16", "s17", "s18", "s19", "s20", "s21", "s22", "s23", "s24", "s25", "s26", "s27", "s28", "s29", "s30", "s31", "s32", "s33", "s34", "s35", "s36", "s37", "s38", "s39", "s40", "s41", "s42", "s43", "s44", "s45", "s46", "s47", "s48", "s49", "s50", "s51", "s52", "s53", "s54", "s55", "s56", "s57", "s58", "s59", "s60", "s61", "s62", "s63", "s80", "s81", "s82", "s83", "s84", "s85", "s86", "s87", "s88", "s89", "s90", "vcc", "m0", "scc", "memory");
  typedef __attribute__((address_space(1))) char* __attribute__((address_space(4))) const* arg_t;
  const arg_t ap = (arg_t)ka;
  typedef const u16* cu16;
  if (b >= NATT) list_branch(lds, b - NATT, wid, (const int*)ap[1], (const int*)ap[2], (cu16)ap[3], (cu16)ap[8], (cu16)ap[9], (cu16)ap[10], (char*)ap[0]);
  else exact_rows(lds, b, wid, (char*)ap[0], (cu16)ap[4], (u16*)ap[5]);
}

extern "C" void kernel_launch(void* const* d_in, const int* in_sizes, int n_in, void* d_out, int out_size, void* d_ws,
                              size_t ws_size, hipStream_t stream) {
  const u16* x = (const u16*)d_in[0];
  const int* src = (const int*)d_in[1];
  const int* dst = (const int*)d_in[2];
  const u16* deg_inv = (const u16*)d_in[3];
  const u16* wmsg = (const u16*)d_in[4];
  const u16* wq = (const u16*)d_in[5];
  const u16* wk = (const u16*)d_in[6];
  const u16* wv = (const u16*)d_in[7];
  const u16* wout = (const u16*)d_in[8];
  char* ws = (char*)d_ws;
  (void)hipFuncSetAttribute((const void*)attn_kernel, hipFuncAttributeMaxDynamicSharedMemorySize, 163840);
  attn_kernel<<<256, 256, 163840, stream>>>(ws, src, dst, deg_inv, x, (u16*)d_out, wq, wk, wv, wmsg, wout);
}
```

```cpp
#include <hip/hip_runtime.h>
#include <hip/hip_bf16.h>
#include <stdint.h>
#pragma clang diagnostic ignored "-Winline-asm"

typedef unsigned short u16;
typedef __attribute__((ext_vector_type(8))) short bf16x8;
typedef __attribute__((ext_vector_type(4))) short bf16x4;
typedef __attribute__((ext_vector_type(4))) float f32x4;
typedef __attribute__((ext_vector_type(2))) float f32x2;
typedef __attribute__((ext_vector_type(4))) unsigned u32x4;
typedef __attribute__((ext_vector_type(2))) unsigned u32x2;
#define DI __device__ __forceinline__

constexpr int NN = 10000, DD = 256, EE = 160000;
constexpr int NATT = 252, NSP = 4;

constexpr unsigned WS_H = 0;
constexpr unsigned WS_AF = WS_H + 10240 * 512;
constexpr unsigned WS_WV = WS_AF + 131072;
constexpr unsigned WS_WM = WS_WV + 131072;
constexpr unsigned WS_XF = WS_WM + 131072;
constexpr unsigned WS_HF = WS_XF + 256 * 256;
constexpr unsigned WS_BF = WS_HF + 4096;
constexpr unsigned WS_PF = WS_BF + 65536;
constexpr unsigned WS_WF = WS_PF + 4096;
constexpr unsigned WS_LF = WS_WF + 256;
constexpr unsigned WS_EP = WS_LF + 2048;
constexpr unsigned WS_G = WS_EP + 1792;
constexpr unsigned WS_QP = WS_G + 10240 * 512;
constexpr unsigned WS_XB = WS_QP + 10240 * 512;
constexpr unsigned WS_BL = WS_XB + NATT * 3 * 24832;
constexpr unsigned WS_GL = WS_BL + 16 * 5 * 260 * 64;
constexpr unsigned WS_DBG = WS_GL + 256 * 4096;
constexpr unsigned WS_END = WS_DBG + 1024 * 128 + 8192;

DI float bf2f(unsigned x) { return __uint_as_float(x << 16); }
DI float bfhi(unsigned x) { return __uint_as_float(x & 0xffff0000u); }
DI unsigned pk2(float lo, float hi) {
  typedef __attribute__((ext_vector_type(2))) __bf16 bf2_t;
  const bf2_t r = __builtin_convertvector((f32x2){lo, hi}, bf2_t);
  return __builtin_bit_cast(unsigned, r);
}
typedef __attribute__((address_space(3))) bf16x4* lds_bf16x4_p;
DI bf16x4 tr_read(unsigned p) { return __builtin_amdgcn_ds_read_tr16_b64_v4i16((lds_bf16x4_p)p); }
DI bf16x8 as_bf(u32x4 v) { return __builtin_bit_cast(bf16x8, v); }
#define SB() __builtin_amdgcn_sched_barrier(0)
#define MFMA16(a, b, c) __builtin_amdgcn_mfma_f32_16x16x32_bf16(a, b, c, 0, 0, 0)

typedef __amdgpu_buffer_rsrc_t srd_t;
DI void drain() { asm volatile("s_waitcnt vmcnt(0)" ::: "memory"); }
DI void st_flag(srd_t w, unsigned off, unsigned tag) { __builtin_amdgcn_raw_buffer_store_b32(tag, w, off, 0, 16); }
DI void wait_flag(srd_t w, unsigned off, unsigned tag) {
#pragma unroll 1
  for (int it = 0; it < (1 << 22); ++it) {
    unsigned v;
    asm volatile("buffer_load_dword %0, %1, %2, 0 offen sc1\n s_waitcnt vmcnt(0)" : "=v"(v) : "v"(off), "s"(w) : "memory");
    if (__ballot(v != tag) == 0) break;
    __builtin_amdgcn_s_sleep(2);
    asm volatile("" ::: "memory");
  }
  asm volatile("" ::: "memory");
}

DI void prod_store(const f32x4 (&acc)[2][2], int mat, int ks, int J, int lane, srd_t wsrd) {
#pragma unroll
  for (int s = 0; s < 2; ++s) {
    u32x4 o = {pk2(acc[0][s][0], acc[0][s][1]), pk2(acc[0][s][2], acc[0][s][3]), pk2(acc[1][s][0], acc[1][s][1]), pk2(acc[1][s][2], acc[1][s][3])};
    if (ks == 7 && lane >= 48) o[3] &= 0xffffu;
    const int T = 2 * J + s;
    const int at = mat ? 131072 + ((J & 1) ^ 1) * 16384 + ks * 2048 + s * 1024 : ks * 4096 + (T & 3) * 1024;
    __builtin_amdgcn_raw_buffer_store_b128(o, wsrd, WS_WV + (T >> 2) * 32768 + at + lane * 16, 0, 16);
  }
}
DI void corr(unsigned* dl, const unsigned (&lr)[16]) {
  float a0 = 0.f, a1 = 0.f;
#pragma unroll
  for (int r = 0; r < 16; ++r) { a0 += bf2f(lr[r] & 0xffffu); a1 += bfhi(lr[r]); }
  dl[0] = pk2(a0 * -0.0625f, a1 * -0.0625f);
  dl[128] = pk2((bf2f(lr[15] & 0xffffu) - 0.25f * a0) * (1.f / 3.f), (bfhi(lr[15]) - 0.25f * a1) * (1.f / 3.f));
}
DI void product_load(char* lds, int wid, int lane, int job, const u16* __restrict__ wv, const u16* __restrict__ wm, const u16* __restrict__ wo, bool left, bool right) {
  const unsigned ldsb = (unsigned)(size_t)lds;
  const int r = lane >> 3, sw = ((lane & 7) ^ (r & 6)) * 16 + wid * 128;
  const unsigned vp = (8 * (r >> 2) + (r & 3)) * 512 + sw, vn = r * 512 + sw;
  auto image = [&](int n, const u16* w, int row0, bool nat) {
#pragma unroll
    for (int i = 0; i < 8; ++i) {
      const int a = i & 3;
      asm volatile("s_mov_b32 m0, %2\n s_nop 0\n global_load_lds_dwordx4 %0, %1"
                   :: "v"(nat ? vn : vp), "s"(w + (row0 + (i >> 2) * 32 + (nat ? 8 * a : 16 * (a & 1) + 4 * (a >> 1))) * DD),
                      "s"(ldsb + n * 32768 + (i >> 2) * 16384 + wid * 4096 + a * 1024) : "m0", "memory");
    }
  };
  if (left) {
    image(2, wv, (job >> 2) * 64, true);
    image(3, wm, (job >> 2) * 64, false);
  }
  if (right)
#pragma unroll
  for (int k = 0; k < 8; ++k)
    asm volatile("s_mov_b32 m0, %2\n s_nop 0\n global_load_lds_dwordx4 %0, %1"
                 :: "v"((lane >> 3) * 512 + (lane & 7) * 16), "s"(wo + (k * 32 + wid * 8) * DD + (job & 3) * 64), "s"(ldsb + k * 4096 + wid * 1024) : "m0", "memory");
}
DI void product_w(char* lds, int wid, int lane, int job, srd_t wsrd) {
  const int m = lane & 15, g = lane >> 4;
  const int ks = 2 * (job >> 2) + (wid >> 1), J = 2 * (job & 3) + (wid & 1);
  const unsigned ldsb = (unsigned)(size_t)lds;
  unsigned* dl = reinterpret_cast<unsigned*>(lds + 0x26c00);
  const bool hi = (m >> 3) != 0;
  const unsigned ra = (m >> 3) * 1024 + (m & 7) * 128 + (g ^ (m & 6)) * 16;
  auto frag = [&](int n, int tl, int t, int k) {
    return *reinterpret_cast<const bf16x8*>(lds + n * 32768 + tl * 16384 + (k >> 1) * 4096 + t * 2048 + (ra ^ ((k & 1) * 64)));
  };
  f32x4 av[2][2] = {}, am[2][2] = {}, ad[2] = {};
  const unsigned tb = ldsb + (job & 1) * 32768 + (8 * g + ((lane >> 2) & 3)) * 128 + (wid & 1) * 64 + (lane & 3) * 16;
  bf16x8 d0[8], d1[8], d2[8], rr[8][2], vf[8][2], mf[8][2];
#pragma unroll
  for (int kk = 0; kk < 9; ++kk) {
    if (kk < 8) {
      d0[kk] = *reinterpret_cast<const bf16x8*>(dl + 512 + 16 * kk + 4 * g); d1[kk] = *reinterpret_cast<const bf16x8*>(dl + 512 + (ks == 7 ? 128 : 0) + 16 * kk + 4 * g);
      d2[kk] = *reinterpret_cast<const bf16x8*>(dl + 768 + (ks == 7 && hi ? 128 : 0) + 16 * kk + 4 * g);
#pragma unroll
      for (int s = 0; s < 2; ++s) {
        rr[kk][s] = __builtin_shufflevector(tr_read(tb + kk * 4096 + s * 8), tr_read(tb + kk * 4096 + s * 8 + 512), 0, 1, 2, 3, 4, 5, 6, 7);
        vf[kk][s] = frag(2, wid >> 1, s, kk); mf[kk][s] = frag(3, wid >> 1, s, kk);
      }
    }
    SB();
    if (kk) {
      const int k = kk - 1;
#pragma unroll
      for (int s = 0; s < 2; ++s) {
        av[0][s] = MFMA16(d0[k], rr[k][s], av[0][s]); av[1][s] = MFMA16(d1[k], rr[k][s], av[1][s]); ad[s] = MFMA16(d2[k], rr[k][s], ad[s]);
#pragma unroll
        for (int t = 0; t < 2; ++t) { av[t][s] = MFMA16(vf[k][t], rr[k][s], av[t][s]); am[t][s] = MFMA16(mf[k][t], rr[k][s], am[t][s]); }
      }
    }
    SB();
  }
#pragma unroll
  for (int t = 0; t < 2; ++t)
#pragma unroll
    for (int s = 0; s < 2; ++s) am[t][s] += ad[s];
  prod_store(av, 0, ks, J, lane, wsrd);
  prod_store(am, 1, ks, J, lane, wsrd);
}

constexpr unsigned CODE_BYTES = 90368;
DI void code_touch(unsigned i, unsigned ldsdst) {
  unsigned long long a; int o_;
  asm volatile("s_getpc_b64 %0\n s_mov_b32 %1, _Z11attn_kernelPcPKiS1_PKtS3_PtS3_S3_S3_S3_S3_@rel32@lo+4" : "=&s"(a), "=&s"(o_));
  a += (long long)o_;
  asm volatile("s_mov_b32 m0, %2\n s_nop 0\n global_load_lds_dword %0, %1" :: "v"(min(i * 128u, CODE_BYTES - 4u)), "s"(a), "s"(ldsdst) : "m0", "memory");
}

constexpr int LONGS = 1056;
typedef __attribute__((address_space(3))) unsigned* lds_u32_p;
typedef __attribute__((address_space(3))) u16* lds_u16_p;
DI void local_store(srd_t srd, int node, int lane, f32x4 acc, float di) {
  const int n = lane & 15, g = lane >> 4;
  const u32x2 o = {pk2(acc[0] * di, acc[1] * di), pk2(acc[2] * di, acc[3] * di)};
  __builtin_amdgcn_raw_buffer_store_b64(o, srd, WS_G + node * 512 + (g >> 1) * 256 + n * 16 + (g & 1) * 8, 0, 16);
}
DI void list_branch(char* lds, int j, int wid, const int* __restrict__ src, const int* __restrict__ dst, const u16* __restrict__ deg_inv,
                    const u16* wv, const u16* wm, const u16* wo, char* ws) {
  const unsigned rec = (unsigned)(size_t)lds + wid * 33280, ext = (unsigned)(size_t)lds + 0x20800 + wid * 6912;
  int lane; asm volatile("v_mbcnt_lo_u32_b32 %0, -1, 0\n\tv_mbcnt_hi_u32_b32 %0, -1, %0" : "=v"(lane));
  const int tid = wid * 64 + lane, bin = j * 4 + wid, lo = bin * 640;
  code_touch(CODE_BYTES / 128 - tid, (unsigned)(size_t)lds + 0x27f00u);
  const srd_t msrd = __builtin_amdgcn_make_buffer_rsrc(ws + WS_H, 0, 10240 * 512, 0x00020000);
  const srd_t wsrd = __builtin_amdgcn_make_buffer_rsrc(ws, 0, WS_END, 0x00020000);
  const unsigned tag = __builtin_amdgcn_raw_buffer_load_b32(wsrd, WS_EP, 0, 0) + 2u;
  if (lane < 3) st_flag(wsrd, lane == 2 ? WS_WF + 32 : WS_LF + bin * 128 + lane * 64 + 8, 0);
  unsigned lr[16];
#pragma unroll
  for (int r = 0; r < 16; ++r) lr[r] = reinterpret_cast<const unsigned*>((wid >= 2 ? wm : wv) + (240 + r) * DD)[tid & 127];
  unsigned dv[10];
#pragma unroll
  for (int i = 0; i < 10; ++i) dv[i] = (unsigned)deg_inv[min(lo + 64 * i + lane, NN - 1)];
#pragma unroll
  for (int i = 0; i < 10; ++i) ((lds_u32_p)ext)[64 * i + lane] = dv[i] << 16 | (lo + 64 * i + lane < NN ? lo + 64 * i + lane : 0xffff);
  corr(reinterpret_cast<unsigned*>(lds + 0x26c00) + 512 + (wid >> 1) * 256 + (tid & 127), lr);
  const u32x4 none = {~0u, ~0u, ~0u, ~0u};
  auto clear = [&]() {
    unsigned a = rec + lane * 16, r = rec + lane * 52;
    asm volatile("" : "+v"(a), "+v"(r));
#pragma unroll
    for (int i = 0; i < 33; ++i) *(__attribute__((address_space(3))) u32x4*)(a + (i < 32 ? i * 1024 : 32256)) = none;
#pragma unroll
    for (int i = 0; i < 10; ++i) ((lds_u32_p)(r + 64 * 52 * i))[12] = 46;
  };
  clear();
  __builtin_amdgcn_s_sleep(40);
  wait_flag(wsrd, WS_BF + min(tid, NATT - 1) * 128, tag);
  __syncthreads();
  auto take = [&](const u32x4& e0, const u32x4& e1, unsigned (&a)[8], unsigned (&p)[8]) {
#pragma unroll
    for (int r = 0; r < 8; ++r) asm("v_mad_u32_u16 %0, %1, 52, %2 op_sel:[1,0,0,0]" : "=v"(a[r]) : "v"(r < 4 ? e0[r & 3] : e1[r & 3]), "s"(rec));
    SB();
#pragma unroll
    for (int r = 0; r < 8; ++r) p[r] = __atomic_fetch_add((lds_u32_p)a[r] + 12, -2u, __ATOMIC_RELAXED);
    SB();
  };
  auto fill = [&](const u32x4& e0, const u32x4& e1, unsigned (&a)[8], const unsigned (&p)[8]) {
#pragma unroll
    for (int r = 7; r >= 0; --r) a[r] = __builtin_elementwise_add_sat(a[r], p[r]);
    SB();
#pragma unroll
    for (int r = 7; r >= 0; --r) *(lds_u16_p)a[r] = r < 4 ? e0[r & 3] : e1[r & 3];
    SB();
  };
  auto add8 = [&](u32x4 e0, u32x4 e1) { unsigned a[8], p[8]; take(e0, e1, a, p); fill(e0, e1, a, p); };
  const unsigned bo = WS_BL + bin * 5 * 260 * 64 + lane * 16;
  auto ld = [&](int g, int k) { return __builtin_amdgcn_raw_buffer_load_b128(wsrd, bo + (g * 260 + k * 16) * 64, 0, 16); };
  auto fix = [&](u32x4 e, unsigned last) { return e | (unsigned)(__builtin_amdgcn_mov_dpp(last, 0xff, 15, 15, 0) >> 31); };
  bool ovf = false;
  unsigned big = 0; int off[10], tot = 0;
#pragma unroll 1
  for (int pass = 0; pass < 2; ++pass) {
    if (!ovf) {
      u32x4 c[64];
#pragma unroll
      for (int k = 0; k < 16; ++k) c[k] = ld(0, k);
      unsigned m = ~0u, a[2][8], p[2][8];
#pragma unroll
      for (int k = 0; k < 66; k += 2) {
        SB();
        if (k < 64) {
          if (k >= 48) { c[k] = fix(c[k], c[k - 16][3]); c[k + 1] = fix(c[k + 1], c[k - 15][3]); }
          if ((k & 15) == 14 && lane >= 48) c[k + 1] = none;
          if (k >= 48) m &= c[k][3] & c[k + 1][3];
          take(c[k], c[k + 1], a[k >> 1 & 1], p[k >> 1 & 1]);
        }
        if (k < 16) {
#pragma unroll
          for (int i = 16 + 2 * k; i < 20 + 2 * k; ++i) c[i] = ld(i >> 4, i & 15);
        }
        if (k >= 24 && k < 40) {
#pragma unroll
          for (int i = k - 24; i < k - 22; ++i)
            c[48 + i] = __builtin_amdgcn_raw_buffer_load_b128(wsrd, bo + (780 + i * 16) * 64 + (__builtin_amdgcn_mov_dpp(c[32 + i][3], 0xff, 15, 15, 0) & 0x40000000), 0, 16);
        }
        SB();
        if (k) fill(c[k - 2], c[k - 1], a[~k >> 1 & 1], p[~k >> 1 & 1]);
      }
      SB();
      if (__ballot((lane & 3) == 3 && m != ~0u)) {
        unsigned more = 0;
#pragma unroll
        for (int k = 0; k < 16; ++k) more |= __ballot((lane & 3) == 3 && c[48 + k][3] != ~0u) ? 1u << k : 0u;
#pragma unroll 1
        for (; more; more &= more - 1) {
          const int k = __builtin_ctz(more);
          u32x4 e = ld(4, k);
          if (k == 15 && lane >= 48) e = none;
          ovf |= __ballot((lane & 3) == 3 && e[3] != ~0u) != 0;
          add8(e, none);
        }
      }
      if (ovf) clear();
    }
    if (ovf) {
      for (int q = lane; q < EE / 4; q += 64) {
        const u32x4 d = reinterpret_cast<const u32x4*>(dst)[q], s = reinterpret_cast<const u32x4*>(src)[q];
        u32x4 e;
#pragma unroll
        for (int r = 0; r < 4; ++r) e[r] = d[r] - lo < 640u ? (d[r] - lo) << 16 | s[r] : ~0u;
        add8(e, none);
      }
    }
    if (pass) break;
    asm volatile("" : "+v"(lane));
    const int qd = lane & 3, nl = lane >> 2;
    int inc = 0;
#pragma unroll
    for (int i = 0; i < 10; ++i) off[i] = 46 - (int)((lds_u32_p)(rec + (64 * i + lane) * 52))[12];
#pragma unroll
    for (int i = 0; i < 10; ++i) {
      const int cn = off[i];
      off[i] = cn > 48 ? cn : 0; inc += off[i];
      if (cn > 48) { big |= 1u << i; ((lds_u16_p)ext)[2 * (64 * i + lane)] = 0xffff; }
    }
    __builtin_amdgcn_wave_barrier();
    auto put = [&](int odd) {
#pragma unroll 2
      for (int bl = 0; bl < 4; ++bl) {
        u32x4 o[10];
#pragma unroll
        for (int T = 0; T < 10; ++T) if (((2 * T + 1) / 5 & 1) == odd) {
          const lds_u32_p r = (lds_u32_p)(rec + (bl * 160 + T * 16 + nl) * 52);
          o[T] = (u32x4){r[3 * qd], r[1 + 3 * qd], r[2 + 3 * qd], ((lds_u32_p)ext)[bl * 160 + T * 16 + nl]};
        }
#pragma unroll
        for (int T = 0; T < 10; ++T) if (((2 * T + 1) / 5 & 1) == odd) {
          const int c = (2 * T + 1) / 5, ro = 16 * (T - (5 * c >> 1)) + nl, w = c & 1 ? ro * 43 >> 9 : ro >> 3;
          __builtin_amdgcn_raw_buffer_store_b128(o[T], wsrd, WS_GL + c * 4096 + w * (c & 1 ? 256 : 512) + ro * 64 + qd * 16, (bin * 4 + bl) * 16384, 16);
        }
      }
    };
    put(1);
    asm volatile("" ::: "memory");
    put(0);
    asm volatile("s_waitcnt vmcnt(16)" ::: "memory");
    if (lane == 0) st_flag(wsrd, WS_LF + bin * 128 + 64, tag);
#pragma unroll
    for (int o = 1; o < 64; o <<= 1) { const int y = __shfl_up(inc, o); if (lane >= o) inc += y; }
    tot = __builtin_amdgcn_readlane(inc, 63);
    drain();
    if (lane == 0) st_flag(wsrd, WS_LF + bin * 128, tag);
    __syncthreads();
    int j2 = j * 4, w2 = wid, l2 = lane;
    asm volatile("" : "+s"(j2), "+s"(w2), "+v"(l2));
    product_load(lds, w2, l2, j2, wv, wm, wo, true, true);
#pragma unroll 1
    for (int job = 0; job < 4; ++job) {
      if (job) asm volatile("s_waitcnt vmcnt(4)" ::: "memory"); else drain();
      __syncthreads();
      asm volatile("" : "+s"(w2));
      if (job < 3) product_load(lds + (~job & 1) * 32768, w2, l2, j2 + job + 1, wv, wm, wo, false, true);
      product_w(lds, w2, l2, j2 + job, wsrd);
    }
    if (w2 == 0) *(lds_u32_p)(ext - 8) = 0;
    __syncthreads();
    if (tot == 0 || tot > 2 * LONGS) break;
#pragma unroll
    for (int i = 0; i < 10; ++i) {
      const unsigned r = rec + (64 * i + lane) * 52;
      ((lds_u32_p)r)[12] = off[i] ? ext + 2558 + inc - r : -2u;
      inc -= off[i]; off[i] = off[i] << 15 | inc;
    }
  }
  __syncthreads();
  const unsigned ex0 = (unsigned)(size_t)lds + 0x20800;
  const lds_u32_p lng = (lds_u32_p)(size_t)(unsigned)(size_t)lds;
#pragma unroll
  for (int i = 0; i < 10; ++i)
    if (big >> i & 1) {
      const unsigned q = __atomic_fetch_add((lds_u32_p)(ex0 - 8), 1u, __ATOMIC_RELAXED);
      lng[2 * q] = tot > 2 * LONGS ? ~0u : off[i] + wid * 6912 + 2560; lng[2 * q + 1] = (((lds_u32_p)ext)[64 * i + lane] & 0xffff0000u) | (lo + 64 * i + lane);
    }
  __syncthreads();
  const int nl_ = *(lds_u32_p)(ex0 - 8);
  bf16x8 b1;
#pragma unroll
  for (int e = 0; e < 8; ++e) b1[e] = (lane & 15) == e + ((lane >> 1) & 8) ? 0x3f80 : 0;
  const unsigned lo16 = (lane & 31) * 16;
  {
    u32x4 d[3][16]; unsigned nn[3]; int mode[3], q = wid, hf = 0;
    auto issue = [&](u32x4 (&v)[16], unsigned& n, int& md) {
      const unsigned w = __builtin_amdgcn_readfirstlane(lng[2 * q]);
      n = lng[2 * q + 1];
      const int dg = q < nl_ && w >> 16 <= 64 ? w >> 16 : 0, g = dg - hf - (lane >> 5);
      const lds_u16_p f = (lds_u16_p)(ex0 + (w & 0xffffu)) + hf + (lane >> 5);
      md = q >= nl_ ? -1 : dg == 0 ? 0 : dg - hf > 32 ? 1 : 2;
      hf = md == 1 ? 32 : 0; q += md == 1 ? 0 : 4;
      unsigned id[16];
#pragma unroll
      for (int t = 0; t < 16; ++t) id[t] = f[2 * t];
#pragma unroll
      for (int t = 0; t < 16; ++t) { asm volatile("" : "+v"(id[t])); v[t] = __builtin_amdgcn_raw_buffer_load_b128(msrd, (2 * t < g ? id[t] : 0xffffu) * 512u + lo16, 0, 16); }
    };
    issue(d[0], nn[0], mode[0]); issue(d[1], nn[1], mode[1]);
    f32x4 keep = {};
#pragma unroll 1
    for (bool go = true; go;) {
#pragma unroll
      for (int s = 0; s < 3; ++s) {
        if (mode[s] < 0) { go = false; break; }
        issue(d[(s + 2) % 3], nn[(s + 2) % 3], mode[(s + 2) % 3]); SB();
        f32x4 acc = {};
#pragma unroll
        for (int t = 0; t < 16; ++t) acc = MFMA16(b1, as_bf(d[s][t]), acc);
        local_store(wsrd, mode[s] == 2 ? nn[s] & 0xffffu : 0x200000u, lane, acc + keep, bfhi(nn[s]));
        keep = mode[s] == 1 ? acc : (f32x4){0.f, 0.f, 0.f, 0.f}; SB();
      }
    }
  }
#pragma unroll 1
  for (int q0 = 0; q0 < nl_; q0 += 256)
#pragma unroll 1
  for (unsigned long long m = __ballot(q0 + 4 * lane + 3 - wid < nl_ && lng[2 * (q0 + 4 * lane + 3 - wid)] >> 16 > 64); m; m &= m - 1) {
    const int q = q0 + 4 * __builtin_ctzll(m) + 3 - wid;
    const unsigned w = __builtin_amdgcn_readfirstlane(lng[2 * q]), n = __builtin_amdgcn_readfirstlane(lng[2 * q + 1]); const int dg = w >> 16;
    f32x4 acc = {};
    if (w == ~0u) {
      for (int e0 = 0; e0 < EE; e0 += 64) {
        unsigned long long h = __ballot(dst[e0 + lane] == (n & 0xffffu));
        while (h) {
          const int e = e0 + __builtin_ctzll(h); h &= h - 1;
          const unsigned vo = lane < 32 ? (unsigned)src[e] * 512 + lo16 : 0x7fffff00u;
          acc = MFMA16(b1, as_bf(__builtin_amdgcn_raw_buffer_load_b128(msrd, vo, 0, 16)), acc);
        }
      }
    } else {
      const lds_u16_p f = (lds_u16_p)(ex0 + (w & 0xffffu)), u = (lds_u16_p)(ext + 2624 + 2 * LONGS);
      for (int x = lane; x < dg; x += 64) {
        const int key = f[x]; int p = 0;
#pragma unroll 8
        for (int t = 0; t < dg; ++t) { const int v = f[t]; p += v < key || (v == key && t < x); }
        u[p] = key;
      }
      __builtin_amdgcn_wave_barrier();
      for (int x = lane >> 5; x < dg + (lane >> 5); x += 32) {
        u32x4 d[16]; unsigned id[16]; f32x4 a2 = {};
#pragma unroll
        for (int t = 0; t < 16; ++t) id[t] = u[min(x + 2 * t, dg)];
#pragma unroll
        for (int t = 0; t < 16; ++t) { asm volatile("" : "+v"(id[t])); d[t] = __builtin_amdgcn_raw_buffer_load_b128(msrd, (x + 2 * t < dg ? id[t] : 0xffffu) * 512u + lo16, 0, 16); }
#pragma unroll
        for (int t = 0; t < 16; ++t) a2 = MFMA16(b1, as_bf(d[t]), a2);
        acc += a2;
      }
      __builtin_amdgcn_wave_barrier();
    }
    local_store(wsrd, n & 0xffffu, lane, acc, bfhi(n));
  }
  drain();
  __syncthreads();
  if (wid == 0 && lane == 0) st_flag(wsrd, WS_WF + j * 4, tag);
}

DI void exact_rows(char* lds, int b, int wid, char* ws, const u16* x, u16* out) {
  int lane; asm volatile("v_mbcnt_lo_u32_b32 %0, -1, 0\n\tv_mbcnt_hi_u32_b32 %0, -1, %0" : "=v"(lane));
  const srd_t w = __builtin_amdgcn_make_buffer_rsrc(ws, 0, WS_END, 0x00020000);
  const srd_t xs = __builtin_amdgcn_make_buffer_rsrc(const_cast<u16*>(x), 0, NN * 512, 0x00020000);
  const srd_t os = __builtin_amdgcn_make_buffer_rsrc(out, 0, NN * 512, 0x00020000);
  const int n = lane & 15, g = lane >> 4, c = b & 3;
  f32x4* red = reinterpret_cast<f32x4*>(lds);
  float* mx = reinterpret_cast<float*>(lds + 65536);
  auto ld = [&](unsigned off) { return as_bf(__builtin_amdgcn_raw_buffer_load_b128(w, off, 0, 16)); };
#pragma unroll 1
  for (int i = 0; i < 2 + (c & 1); ++i) {
    const int tile = (b >> 2) * 10 + (5 * c >> 1) + i;
    bf16x8 q[8];
#pragma unroll
    for (int ks = 0; ks < 8; ++ks) q[ks] = ld(WS_QP + tile * 8192 + ks * 1024 + lane * 16);
    auto score = [&](int kt) {
      f32x4 s = {};
#pragma unroll
      for (int ks = 0; ks < 8; ++ks) s = MFMA16(ld(WS_H + (kt * 16 + n) * 512 + ks * 64 + g * 16), q[ks], s);
      return s;
    };
    float m = -__builtin_inff();
#pragma unroll 1
    for (int kt = wid; kt < NN / 16; kt += 4) { const f32x4 s = score(kt); m = fmaxf(fmaxf(m, fmaxf(s[0], s[1])), fmaxf(s[2], s[3])); }
    __syncthreads();
    mx[wid * 64 + lane] = m;
    __syncthreads();
#pragma unroll
    for (int r = 0; r < 16; ++r) m = fmaxf(m, mx[r * 16 + n]);
    f32x4 acc[16] = {};
#pragma unroll 1
    for (int kt = wid; kt < NN / 16; kt += 4) {
      const f32x4 s = score(kt);
      const u32x2 p = {pk2(__builtin_amdgcn_exp2f(s[0] - m), __builtin_amdgcn_exp2f(s[1] - m)), pk2(__builtin_amdgcn_exp2f(s[2] - m), __builtin_amdgcn_exp2f(s[3] - m))};
#pragma unroll
      for (int dt = 0; dt < 16; ++dt) {
        bf16x4 v;
#pragma unroll
        for (int e = 0; e < 4; ++e) v[e] = __builtin_amdgcn_raw_buffer_load_b16(w, WS_H + (kt * 16 + 4 * g + e) * 512 + n * 2, dt * 32, 16);
        acc[dt] = __builtin_amdgcn_mfma_f32_16x16x16bf16_1k(v, __builtin_bit_cast(bf16x4, p), acc[dt], 0, 0, 0);
      }
    }
#pragma unroll
    for (int dt = 0; dt < 16; ++dt) red[(wid * 16 + dt) * 64 + lane] = acc[dt];
    __syncthreads();
    float l = 0.f;
#pragma unroll
    for (int r = 0; r < 4; ++r) l += red[(r * 16 + 15) * 64 + 48 + n][3];
    l = 1.f / l;
    f32x4 y[4] = {};
#pragma unroll 1
    for (int ks = 0; ks < 8; ++ks) {
      f32x4 o[2];
#pragma unroll
      for (int h = 0; h < 2; ++h) o[h] = (red[(2 * ks + h) * 64 + lane] + red[(16 + 2 * ks + h) * 64 + lane] + red[(32 + 2 * ks + h) * 64 + lane] + red[(48 + 2 * ks + h) * 64 + lane]) * l;
      const bf16x8 of = as_bf((u32x4){pk2(o[0][0], o[0][1]), pk2(o[0][2], o[0][3]), pk2(o[1][0], o[1][1]), pk2(o[1][2], o[1][3])});
      const bf16x8 gf = ld(WS_G + (tile * 16 + n) * 512 + ks * 64 + g * 16);
#pragma unroll
      for (int et = 0; et < 4; ++et) {
        y[et] = MFMA16(ld(WS_WV + wid * 32768 + ks * 4096 + et * 1024 + lane * 16), of, y[et]);
        y[et] = MFMA16(ld(WS_WM + wid * 32768 + (et < 2 ? 16384 : 0) + ks * 2048 + (et & 1) * 1024 + lane * 16), gf, y[et]);
      }
    }
#pragma unroll
    for (int jj = 0; jj < 2; ++jj) {
      const unsigned at = (tile * 16 + n) * 512 + wid * 128 + jj * 64 + g * 16;
      const u32x4 xv = __builtin_amdgcn_raw_buffer_load_b128(xs, at, 0, 0);
      u32x4 o;
#pragma unroll
      for (int r = 0; r < 4; ++r) o[r] = pk2(y[2 * jj + (r >> 1)][2 * (r & 1)] + bf2f(xv[r] & 0xffffu), y[2 * jj + (r >> 1)][2 * (r & 1) + 1] + bfhi(xv[r]));
      __builtin_amdgcn_raw_buffer_store_b128(o, os, at, 0, 0);
    }
  }
}

__global__ __launch_bounds__(256, 1) void attn_kernel(char* ws, const int* __restrict__ src, const int* __restrict__ dst,
                                                      const u16* __restrict__ deg_inv, const u16* __restrict__ x, u16* out,
                                                      const u16* __restrict__ wq, const u16* __restrict__ wk, const u16* __restrict__ wv,
                                                      const u16* __restrict__ wm, const u16* __restrict__ wo) {
  extern __shared__ __attribute__((aligned(16))) char lds[];
  static_assert(CODE_BYTES > 65536 + 256, "range of the first cache warm-up");
  int b = blockIdx.x;
  int wid = __builtin_amdgcn_readfirstlane(threadIdx.x >> 6);
  static_assert(WS_END == 37557248 && WS_EP == 5777664, "workspace layout of the generated code");
  auto ka = __builtin_amdgcn_kernarg_segment_ptr();
  asm volatile(
    "s_load_dwordx2 s[20:21], %0, 0x30\n"
    "s_load_dwordx2 s[24:25], %0, 0x38\n"
    "s_load_dwordx2 s[12:13], %0, 0x20\n"
    "s_load_dwordx2 s[8:9], %0, 0x0\n"
    "s_load_dwordx2 s[16:17], %0, 0x28\n"
    "s_load_dwordx4 s[28:31], %0, 0x8\n"
    "v_mbcnt_lo_u32_b32 v24, -1, 0\n"
    "v_mbcnt_hi_u32_b32 v24, -1, v24\n"
    "s_and_b32 s45, %1, 6\n"
    "s_lshl_b32 s46, s45, 5\n"
    "s_lshr_b32 s47, %1, 3\n"
    "s_getpc_b64 s[62:63]\n"
    "s_lshl1_add_u32 s46, s47, s46\n"
    "s_and_b32 s47, s47, 1\n"
    "s_lshl2_add_u32 s47, s47, %2\n"
    "s_lshl_b32 s47, s47, 13\n"
    "s_add_u32 m0, %3, 0x27f00\n"
    "v_lshl_add_u32 v29, v24, 7, s47\n"
    "global_load_lds_dword v29, s[62:63]\n"
    "s_add_u32 s48, s45, 2\n"
    "s_lshr_b32 s48, s48, 2\n"
    "s_lshl_b32 s48, s48, 1\n"
    "s_sub_u32 s46, s46, s48\n"
    "s_add_u32 s48, s48, 252\n"
    "s_and_b32 s45, %1, 250\n"
    "s_cmpk_eq_u32 s45, 248\n"
    "s_cselect_b32 s46, s48, s46\n"
    "s_and_b32 %1, %1, 1\n"
    "s_or_b32 %1, %1, s46\n"
    "s_cmpk_ge_u32 %1, 252\n"
    "s_cbranch_scc1 .Lat_end\n"
    "s_and_b32 s50, %2, 1\n"
    "s_lshr_b32 s51, %2, 1\n"
    "v_lshrrev_b32_e32 v25, 3, v24\n"
    "v_and_b32_e32 v26, 6, v25\n"
    "v_and_b32_e32 v27, 7, v24\n"
    "v_xor_b32_e32 v26, v26, v27\n"
    "v_lshlrev_b32_e32 v26, 4, v26\n"
    "v_and_b32_e32 v27, 4, v25\n"
    "v_add_u32_e32 v27, v27, v25\n"
    "v_lshl_add_u32 v254, v27, 9, v26\n"
    "v_add_u32_e32 v255, 0x1c00, v254\n"
    "s_mov_b32 s22, 0x20000\n"
    "s_mov_b32 s23, 0x20000\n"
    "s_mov_b32 s26, 0x20000\n"
    "s_mov_b32 s27, 0x20000\n"
    "s_lshr_b32 s36, %1, 7\n"
    "s_mul_i32 s36, s36, 3\n"
    "s_add_u32 s36, s36, %1\n"
    "s_and_b32 s36, s36, 127\n"
    "s_lshl_b32 s45, %2, 7\n"
    "s_lshl_b32 s46, %2, 12\n"
    "s_add_u32 s46, s46, %3\n"
    "s_and_b32 s47, s36, 7\n"
    "s_lshl_b32 s47, s47, 14\n"
    "s_add_u32 m0, s46, 32768\n"
    "s_add_u32 s47, s47, s45\n"
    "s_and_b32 s32, %1, 3\n"
    "s_lshr_b32 s33, %1, 2\n"
    "s_mul_i32 s38, s32, 5\n"
    "s_lshr_b32 s38, s38, 1\n"
    "s_mul_i32 s35, s33, 10\n"
    "s_add_u32 s35, s35, s38\n"
    "s_lshl_b32 s35, s35, 13\n"
    "s_mov_b32 s14, 5120000\n"
    "s_mov_b32 s15, 0x20000\n"
    "s_waitcnt lgkmcnt(0)\n"
    "buffer_load_dwordx4 v254, s[20:23], s47 offen offset:0 lds\n"
    "s_add_u32 s48, s47, 1024\n"
    "buffer_load_dwordx4 v255, s[20:23], s47 offen offset:3072 lds\n"
    "s_add_u32 s47, s47, 0x1800\n"
    "buffer_load_dwordx4 v254, s[20:23], s48 offen offset:1024 lds\n"
    "buffer_load_dwordx4 v254, s[20:23], s47 offen offset:2048 lds\n"
    "s_lshr_b32 s47, s36, 4\n"
    "s_lshl_b32 s47, s47, 14\n"
    "s_and_b32 s48, s36, 8\n"
    "s_lshl_b32 s48, s48, 8\n"
    "s_add_u32 s47, s47, s48\n"
    "s_add_u32 m0, s46, 49152\n"
    "s_add_u32 s47, s47, s45\n"
    "buffer_load_dwordx4 v254, s[24:27], s47 offen offset:0 lds\n"
    "buffer_load_dwordx4 v255, s[24:27], s47 offen offset:1024 lds\n"
    "s_add_u32 s47, s45, 120832\n"
    "buffer_load_dwordx4 v254, s[24:27], s47 offen offset:2048 lds\n"
    "s_add_u32 s47, s47, 1024\n"
    "buffer_load_dwordx4 v254, s[24:27], s47 offen offset:3072 lds\n"
    "s_mul_i32 s46, %1, 159\n"
    "s_lshl_b32 s45, s50, 6\n"
    "v_add_u32_e32 v111, s45, v24\n"
    "v_add_u32_e32 v108, s46, v111\n"
    "v_min_u32_e32 v108, 39999, v108\n"
    "v_lshlrev_b32_e32 v108, 4, v108\n"
    "s_lshl1_add_u32 s47, s46, 256\n"
    "v_add_u32_e32 v109, s47, v111\n"
    "v_min_u32_e32 v109, 79999, v109\n"
    "v_lshlrev_b32_e32 v109, 3, v109\n"
    "s_cmp_lg_u32 s51, 0\n"
    "s_cselect_b64 exec, -1, 1\n"
    "global_load_dwordx4 v[96:99], v108, s[30:31]\n"
    "global_load_dwordx2 v[92:93], v109, s[30:31]\n"
    "global_load_dwordx4 v[100:103], v108, s[28:29]\n"
    "global_load_dwordx2 v[94:95], v109, s[28:29]\n"
    "s_mov_b64 exec, -1\n"
    "s_lshl_b32 s45, %2, 6\n"
    "v_add_u32_e32 v108, s45, v24\n"
    "s_sub_u32 s59, 40000, s46\n"
    "s_lshl1_add_u32 s61, s59, -256\n"
    "s_min_i32 s61, s61, 62\n"
    "s_lshl_b32 s45, %2, 2\n"
    "s_add_u32 s54, s35, 0x2000\n"
    "s_add_u32 s55, s35, 0x4000\n"
    "v_lshlrev_b32_e32 v253, 4, v24\n"
    "v_lshrrev_b32_e32 v28, 4, v24\n"
    "v_add_u32_e32 v28, s45, v28\n"
    "v_and_b32_e32 v104, 0xf0, v253\n"
    "v_lshl_add_u32 v104, v28, 9, v104\n"
    "buffer_load_dwordx4 v[0:3], v104, s[12:15], s35 offen offset:0\n"
    "buffer_load_dwordx4 v[4:7], v104, s[12:15], s35 offen offset:256\n"
    "s_bitcmp1_b32 %1, 0\n"
    "s_cselect_b32 s60, s55, 2.0\n"
    "buffer_load_dwordx4 v[8:11], v104, s[12:15], s54 offen offset:0\n"
    "buffer_load_dwordx4 v[12:15], v104, s[12:15], s54 offen offset:256\n"
    "buffer_load_dwordx4 v[16:19], v104, s[12:15], s55 offen offset:0\n"
    "buffer_load_dwordx4 v[20:23], v104, s[12:15], s55 offen offset:256\n"
    "s_mov_b32 s10, 37557248\n"
    "s_mov_b32 s11, 0x20000\n"
    "s_mov_b32 s37, 5777664\n"
    "s_buffer_load_dword s39, s[8:11], s37\n"
    "s_bfe_u32 s37, %1, 0x20001\n"
    "v_and_b32_e32 v27, 1, v25\n"
    "v_lshl_add_u32 v248, v27, 9, v26\n"
    "v_lshrrev_b32_e32 v27, 1, v25\n"
    "v_lshl_add_u32 v248, v27, 12, v248\n"
    "s_mul_i32 s40, s32, 39\n"
    "s_and_b32 s34, s32, 1\n"
    "s_add_u32 s40, s40, s34\n"
    "s_lshl1_add_u32 s40, s40, s51\n"
    "s_lshl_b32 s40, s40, 14\n"
    "s_lshl_b32 s47, s50, 8\n"
    "s_add_u32 s40, s40, s47\n"
    "s_add_u32 s41, s40, 128\n"
    "s_xor_b32 s34, s34, 1\n"
    "s_lshl_b32 s34, s34, 15\n"
    "s_xor_b32 s43, s50, s51\n"
    "s_lshl3_add_u32 s43, s50, s43\n"
    "s_lshl_b32 s43, s43, 13\n"
    "s_add_u32 s47, %3, 32768\n"
    "s_add_u32 s43, s43, s47\n"
    "v_bfe_u32 v25, v24, 2, 3\n"
    "v_lshlrev_b32_e32 v244, 7, v25\n"
    "v_lshrrev_b32_e32 v26, 5, v24\n"
    "v_lshl_add_u32 v244, v26, 10, v244\n"
    "v_and_b32_e32 v25, 6, v25\n"
    "v_bfe_u32 v26, v24, 1, 1\n"
    "v_xor_b32_e32 v25, v25, v26\n"
    "v_lshl_add_u32 v244, v25, 4, v244\n"
    "v_and_b32_e32 v26, 1, v24\n"
    "v_lshl_add_u32 v244, v26, 3, v244\n"
    "s_lshl_b32 s48, s51, 16\n"
    "s_add_u32 s48, s48, s47\n"
    "v_add_u32_e32 v244, s48, v244\n"
    "v_xor_b32_e32 v245, 32, v244\n"
    "v_xor_b32_e32 v246, 64, v244\n"
    "v_xor_b32_e32 v247, 0x60, v244\n"
    "v_and_b32_e32 v25, 7, v24\n"
    "v_lshlrev_b32_e32 v27, 7, v25\n"
    "v_bfe_u32 v26, v24, 3, 1\n"
    "v_lshl_add_u32 v27, v26, 10, v27\n"
    "v_and_b32_e32 v25, 6, v24\n"
    "v_lshrrev_b32_e32 v26, 4, v24\n"
    "v_xor_b32_e32 v25, v25, v26\n"
    "v_lshl_add_u32 v27, v25, 4, v27\n"
    "s_lshl_b32 s48, s51, 13\n"
    "s_add_u32 s48, s48, s47\n"
    "v_add_u32_e32 v240, s48, v27\n"
    "v_xor_b32_e32 v241, 64, v240\n"
    "v_and_b32_e32 v29, 15, v24\n"
    "s_cmp_ge_u32 s36, 112\n"
    "s_cselect_b32 s47, 8, 16\n"
    "v_cmp_le_u32_e32 vcc, s47, v29\n"
    "v_mov_b32_e32 v64, 0xbd80bd80\n"
    "v_mov_b32_e32 v65, 0xbdabbdab\n"
    "v_mov_b32_e32 v66, 0x392b392b\n"
    "v_cndmask_b32_e32 v64, v64, v65, vcc\n"
    "v_cndmask_b32_e32 v66, 0, v66, vcc\n"
    "s_cselect_b32 s47, 56, 64\n"
    "v_cmp_le_u32_e32 vcc, s47, v24\n"
    "v_mov_b32_e32 v65, 0x3e80bdab\n"
    "v_mov_b32_e32 v67, 0x392b\n"
    "v_cndmask_b32_e32 v65, v64, v65, vcc\n"
    "v_cndmask_b32_e32 v67, v66, v67, vcc\n"
    "s_cmp_ge_u32 s36, 120\n"
    "s_cselect_b32 s47, 15, 16\n"
    "v_cmp_ne_u32_e32 vcc, s47, v29\n"
    "v_mov_b32_e32 v68, 0x3db8aa3b\n"
    "s_lshr_b32 s47, s36, 5\n"
    "v_cndmask_b32_e32 v68, 0, v68, vcc\n"
    "s_lshl_b32 s47, s47, 15\n"
    "s_and_b32 s48, s36, 7\n"
    "s_lshl_b32 s48, s48, 11\n"
    "s_add_u32 s47, s47, s48\n"
    "s_and_b32 s48, s36, 8\n"
    "s_lshl_b32 s48, s48, 7\n"
    "s_add_u32 s47, s47, s48\n"
    "s_and_b32 s48, s36, 16\n"
    "s_lshl_b32 s48, s48, 10\n"
    "s_add_u32 s47, s47, s48\n"
    "s_add_u32 s52, s47, 5242880\n"
    "s_lshl_b32 s48, s50, 9\n"
    "s_add_u32 s52, s52, s48\n"
    "s_lshl_b32 s48, s50, 11\n"
    "v_add_u32_e32 v72, s48, v240\n"
    "v_add_u32_e32 v73, s48, v241\n"
    "v_and_b32_e32 v74, 0x1f0, v253\n"
    "v_lshrrev_b32_e32 v29, 5, v24\n"
    "v_lshl_add_u32 v74, v29, 3, v74\n"
    "s_lshl_b32 s53, s36, 1\n"
    "s_add_u32 s53, s53, s50\n"
    "s_lshl_b32 s53, s53, 4\n"
    "s_add_u32 s53, s53, 5771264\n"
    "s_cmp_eq_u32 s51, 0\n"
    "s_cselect_b32 s52, s52, 2.0\n"
    "s_cselect_b32 s53, s53, 2.0\n"
    "s_lshl_b32 s47, %1, 4\n"
    "s_add_u32 s47, s47, 5701632\n"
    "s_lshl_b32 s48, %1, 7\n"
    "s_add_u32 s48, s48, 5705728\n"
    "s_cmp_eq_u32 s50, 0\n"
    "s_cselect_b32 s47, s47, s48\n"
    "s_cmp_eq_u32 s51, 0\n"
    "s_cselect_b32 s47, s53, s47\n"
    "buffer_store_dword v24, off, s[8:11], s47 offset:8 sc1\n"
    "v_lshrrev_b32_e32 v109, 2, v108\n"
    "v_mul_u32_u24_e32 v109, 0x4100, v109\n"
    "v_and_b32_e32 v110, 48, v253\n"
    "v_or_b32_e32 v109, v109, v110\n"
    "v_add_u32_e32 v110, 0x104000, v109\n"
    "v_cmp_gt_u32_e32 vcc, 64, v108\n"
    "s_add_u32 s44, %3, 24576\n"
    "v_lshl_add_u32 v108, v108, 4, s44\n"
    "v_cndmask_b32_e32 v110, 2.0, v110, vcc\n"
    "v_mov_b32_e32 v32, -1\n"
    "v_mov_b32_e32 v33, -1\n"
    "v_mov_b32_e32 v34, -1\n"
    "v_mov_b32_e32 v35, -1\n"
    "ds_write_b128 v108, v[32:35] offset:0\n"
    "ds_write_b128 v108, v[32:35] offset:1280\n"
    "v_and_b32_e32 v29, 6, v28\n"
    "v_xor_b32_e32 v29, v29, v24\n"
    "v_and_b32_e32 v29, 7, v29\n"
    "v_lshlrev_b32_e32 v105, 7, v28\n"
    "v_lshl_add_u32 v105, v29, 4, v105\n"
    "v_bfe_u32 v29, v24, 3, 1\n"
    "v_subrev_u32_e32 v29, s37, v29\n"
    "v_and_b32_e32 v29, 3, v29\n"
    "v_lshl_add_u32 v105, v29, 11, v105\n"
    "s_add_u32 s45, %3, 65536\n"
    "v_add_u32_e32 v105, s45, v105\n"
    "v_xor_b32_e32 v106, 0x1000, v105\n"
    "v_and_b32_e32 v29, 15, v24\n"
    "v_cmp_eq_u32_e32 vcc, 15, v29\n"
    "v_mov_b32_e32 v107, 0xffff\n"
    "s_mov_b32 s58, 0x3f803f80\n"
    "s_mov_b32 s57, 0x43c00000\n"
    "v_cndmask_b32_e32 v107, -1, v107, vcc\n"
    "s_mov_b32 s56, 0xc3c00000\n"
    "s_mov_b32 s62, 0xc000c000\n"
    "s_mov_b32 s63, 0xc000c000\n"
    "v_add_u32_e32 v250, s45, v27\n"
    "v_xor_b32_e32 v251, 64, v250\n"
    "s_lshl_b32 s47, %2, 10\n"
    "s_add_u32 s47, s47, 5771264\n"
    "v_lshl_add_u32 v252, v24, 4, s47\n"
    "s_waitcnt vmcnt(11)\n"
    "s_waitcnt lgkmcnt(0)\n"
    "s_barrier\n"
    "s_add_u32 s39, s39, 2\n"
    "s_cmp_lg_u32 s51, 0\n"
    "s_cbranch_scc1 .Lat_bin\n"
    "ds_read_b128 v[112:115], v72 offset:0\n"
    "ds_read_b128 v[208:211], v240 offset:18432\n"
    "ds_read_b128 v[176:179], v240 offset:16384\n"
    "ds_read_b128 v[116:119], v73 offset:0\n"
    "ds_read_b128 v[212:215], v241 offset:18432\n"
    "ds_read_b128 v[180:183], v241 offset:16384\n"
    "ds_read_b128 v[120:123], v72 offset:4096\n"
    "ds_read_b128 v[216:219], v240 offset:22528\n"
    "ds_read_b128 v[184:187], v240 offset:20480\n"
    "ds_read_b128 v[124:127], v73 offset:4096\n"
    "ds_read_b128 v[220:223], v241 offset:22528\n"
    "ds_read_b128 v[188:191], v241 offset:20480\n"
    "ds_read_b128 v[128:131], v72 offset:8192\n"
    "ds_read_b128 v[224:227], v240 offset:26624\n"
    "ds_read_b128 v[192:195], v240 offset:24576\n"
    "s_waitcnt lgkmcnt(12)\n"
    "v_mfma_f32_16x16x32_bf16 v[56:59], v[208:211], v[112:115], 0\n"
    "ds_read_b128 v[132:135], v73 offset:8192\n"
    "ds_read_b128 v[228:231], v241 offset:26624\n"
    "v_mfma_f32_16x16x32_bf16 v[48:51], v[112:115], v[176:179], 0\n"
    "ds_read_b128 v[196:199], v241 offset:24576\n"
    "s_waitcnt lgkmcnt(12)\n"
    "v_mfma_f32_16x16x32_bf16 v[56:59], v[212:215], v[116:119], v[56:59]\n"
    "ds_read_b128 v[136:139], v72 offset:12288\n"
    "ds_read_b128 v[232:235], v240 offset:30720\n"
    "v_mfma_f32_16x16x32_bf16 v[48:51], v[116:119], v[180:183], v[48:51]\n"
    "ds_read_b128 v[200:203], v240 offset:28672\n"
    "s_waitcnt lgkmcnt(12)\n"
    "v_mfma_f32_16x16x32_bf16 v[56:59], v[216:219], v[120:123], v[56:59]\n"
    "ds_read_b128 v[140:143], v73 offset:12288\n"
    "ds_read_b128 v[236:239], v241 offset:30720\n"
    "v_mfma_f32_16x16x32_bf16 v[48:51], v[120:123], v[184:187], v[48:51]\n"
    "ds_read_b128 v[204:207], v241 offset:28672\n"
    "s_waitcnt lgkmcnt(12)\n"
    "v_mfma_f32_16x16x32_bf16 v[56:59], v[220:223], v[124:127], v[56:59]\n"
    "v_mfma_f32_16x16x32_bf16 v[48:51], v[124:127], v[188:191], v[48:51]\n"
    "s_waitcnt lgkmcnt(9)\n"
    "v_mfma_f32_16x16x32_bf16 v[56:59], v[224:227], v[128:131], v[56:59]\n"
    "v_mfma_f32_16x16x32_bf16 v[48:51], v[128:131], v[192:195], v[48:51]\n"
    "s_waitcnt lgkmcnt(6)\n"
    "v_mfma_f32_16x16x32_bf16 v[56:59], v[228:231], v[132:135], v[56:59]\n"
    "v_mfma_f32_16x16x32_bf16 v[48:51], v[132:135], v[196:199], v[48:51]\n"
    "s_waitcnt lgkmcnt(3)\n"
    "v_mfma_f32_16x16x32_bf16 v[56:59], v[232:235], v[136:139], v[56:59]\n"
    "v_mfma_f32_16x16x32_bf16 v[48:51], v[136:139], v[200:203], v[48:51]\n"
    "s_waitcnt lgkmcnt(0)\n"
    "v_mfma_f32_16x16x32_bf16 v[56:59], v[236:239], v[140:143], v[56:59]\n"
    "v_mfma_f32_16x16x32_bf16 v[48:51], v[140:143], v[204:207], v[48:51]\n"
    "s_nop 3\n"
    "v_cvt_pk_bf16_f32 v56, v56, v57\n"
    "v_cvt_pk_bf16_f32 v57, v58, v59\n"
    "s_nop 1\n"
    "v_mfma_f32_16x16x16_bf16 v[48:51], v[56:57], v[64:65], v[48:51]\n"
    "v_mfma_f32_16x16x16_bf16 v[48:51], v[56:57], v[66:67], v[48:51]\n"
    "s_nop 7\n"
    "v_mul_f32_e32 v48, v48, v68\n"
    "v_mul_f32_e32 v49, v49, v68\n"
    "v_mul_f32_e32 v50, v50, v68\n"
    "v_mul_f32_e32 v51, v51, v68\n"
    "v_cvt_pk_bf16_f32 v48, v48, v49\n"
    "v_cvt_pk_bf16_f32 v49, v50, v51\n"
    "s_branch .Lat_noj2\n"
    ".Lat_bin:\n"
    "s_movk_i32 s45, -640\n"
    "s_movk_i32 s48, 80\n"
    "v_mov_b32_e32 v60, 1\n"
    "v_cmp_gt_u32_e32 vcc, s59, v111\n"
    "v_cmp_gt_i32_e64 s[46:47], s61, v111\n"
    "s_waitcnt vmcnt(9)\n"
    "v_mul_u32_u24_e32 v48, 0x6667, v96\n"
    "v_mul_u32_u24_e32 v49, 0x6667, v97\n"
    "v_mul_u32_u24_e32 v50, 0x6667, v98\n"
    "v_mul_u32_u24_e32 v51, 0x6667, v99\n"
    "v_mul_u32_u24_e32 v52, 0x6667, v92\n"
    "v_mul_u32_u24_e32 v53, 0x6667, v93\n"
    "v_lshrrev_b32_e32 v48, 24, v48\n"
    "v_lshrrev_b32_e32 v49, 24, v49\n"
    "v_lshrrev_b32_e32 v50, 24, v50\n"
    "v_lshrrev_b32_e32 v51, 24, v51\n"
    "v_lshrrev_b32_e32 v52, 24, v52\n"
    "v_lshrrev_b32_e32 v53, 24, v53\n"
    "v_min_u32_e32 v48, 15, v48\n"
    "v_min_u32_e32 v49, 15, v49\n"
    "v_min_u32_e32 v50, 15, v50\n"
    "v_min_u32_e32 v51, 15, v51\n"
    "v_min_u32_e32 v52, 15, v52\n"
    "v_min_u32_e32 v53, 15, v53\n"
    "v_lshl_add_u32 v54, v48, 2, s44\n"
    "v_lshl_add_u32 v55, v49, 2, s44\n"
    "v_lshl_add_u32 v56, v50, 2, s44\n"
    "v_lshl_add_u32 v57, v51, 2, s44\n"
    "v_lshl_add_u32 v58, v52, 2, s44\n"
    "v_lshl_add_u32 v59, v53, 2, s44\n"
    "s_mov_b64 exec, vcc\n"
    "ds_add_rtn_u32 v54, v54, v60 offset:5248\n"
    "ds_add_rtn_u32 v55, v55, v60 offset:5248\n"
    "ds_add_rtn_u32 v56, v56, v60 offset:5248\n"
    "ds_add_rtn_u32 v57, v57, v60 offset:5248\n"
    "s_mov_b64 exec, s[46:47]\n"
    "ds_add_rtn_u32 v58, v58, v60 offset:5248\n"
    "ds_add_rtn_u32 v59, v59, v60 offset:5248\n"
    "s_mov_b64 exec, -1\n"
    "s_waitcnt vmcnt(7)\n"
    "v_mad_i32_i24 v96, v48, s45, v96\n"
    "v_mad_i32_i24 v97, v49, s45, v97\n"
    "v_mad_i32_i24 v98, v50, s45, v98\n"
    "v_mad_i32_i24 v99, v51, s45, v99\n"
    "v_mad_i32_i24 v92, v52, s45, v92\n"
    "v_mad_i32_i24 v93, v53, s45, v93\n"
    "v_lshl_or_b32 v96, v96, 16, v100\n"
    "v_lshl_or_b32 v97, v97, 16, v101\n"
    "v_lshl_or_b32 v98, v98, 16, v102\n"
    "v_lshl_or_b32 v99, v99, 16, v103\n"
    "v_lshl_or_b32 v92, v92, 16, v94\n"
    "v_lshl_or_b32 v93, v93, 16, v95\n"
    "s_mov_b64 exec, vcc\n"
    "s_waitcnt lgkmcnt(0)\n"
    "v_min_i32_e32 v54, 78, v54\n"
    "v_min_i32_e32 v55, 78, v55\n"
    "v_min_i32_e32 v56, 78, v56\n"
    "v_min_i32_e32 v57, 78, v57\n"
    "v_mad_u32_u24 v48, v48, s48, v54\n"
    "v_mad_u32_u24 v49, v49, s48, v55\n"
    "v_mad_u32_u24 v50, v50, s48, v56\n"
    "v_mad_u32_u24 v51, v51, s48, v57\n"
    "v_lshl_add_u32 v48, v48, 2, s44\n"
    "v_lshl_add_u32 v49, v49, 2, s44\n"
    "v_lshl_add_u32 v50, v50, 2, s44\n"
    "v_lshl_add_u32 v51, v51, 2, s44\n"
    "ds_write_b32 v48, v96 offset:4\n"
    "ds_write_b32 v49, v97 offset:4\n"
    "ds_write_b32 v50, v98 offset:4\n"
    "ds_write_b32 v51, v99 offset:4\n"
    "s_mov_b64 exec, s[46:47]\n"
    "v_min_i32_e32 v58, 78, v58\n"
    "v_min_i32_e32 v59, 78, v59\n"
    "v_mad_u32_u24 v52, v52, s48, v58\n"
    "v_mad_u32_u24 v53, v53, s48, v59\n"
    "v_lshl_add_u32 v52, v52, 2, s44\n"
    "v_lshl_add_u32 v53, v53, 2, s44\n"
    "ds_write_b32 v52, v92 offset:4\n"
    "ds_write_b32 v53, v93 offset:4\n"
    "s_mov_b64 exec, -1\n"
    ".Lat_noj2:\n"
    "buffer_store_dwordx2 v[48:49], v74, s[8:11], s52 offen offset:0 sc1\n"
    "v_mov_b32_e32 v112, s39\n"
    "v_mov_b32_e32 v113, 0\n"
    "s_waitcnt vmcnt(2)\n"
    "v_dot2_f32_bf16 v24, v0, s58, 0\n"
    "v_dot2_f32_bf16 v32, v8, s58, 0\n"
    "v_dot2_f32_bf16 v40, v16, s58, 0\n"
    "v_dot2_f32_bf16 v25, v4, s58, 0\n"
    "v_dot2_f32_bf16 v33, v12, s58, 0\n"
    "v_dot2_f32_bf16 v41, v20, s58, 0\n"
    "v_dot2_f32_bf16 v26, v0, v0, 0\n"
    "v_dot2_f32_bf16 v34, v8, v8, 0\n"
    "v_dot2_f32_bf16 v42, v16, v16, 0\n"
    "v_dot2_f32_bf16 v27, v4, v4, 0\n"
    "v_dot2_f32_bf16 v35, v12, v12, 0\n"
    "v_dot2_f32_bf16 v43, v20, v20, 0\n"
    "v_dot2_f32_bf16 v24, v1, s58, v24\n"
    "v_dot2_f32_bf16 v32, v9, s58, v32\n"
    "v_dot2_f32_bf16 v40, v17, s58, v40\n"
    "v_dot2_f32_bf16 v25, v5, s58, v25\n"
    "v_dot2_f32_bf16 v33, v13, s58, v33\n"
    "v_dot2_f32_bf16 v41, v21, s58, v41\n"
    "v_dot2_f32_bf16 v26, v1, v1, v26\n"
    "v_dot2_f32_bf16 v34, v9, v9, v34\n"
    "v_dot2_f32_bf16 v42, v17, v17, v42\n"
    "v_dot2_f32_bf16 v27, v5, v5, v27\n"
    "v_dot2_f32_bf16 v35, v13, v13, v35\n"
    "v_dot2_f32_bf16 v43, v21, v21, v43\n"
    "v_dot2_f32_bf16 v24, v2, s58, v24\n"
    "v_dot2_f32_bf16 v32, v10, s58, v32\n"
    "v_dot2_f32_bf16 v40, v18, s58, v40\n"
    "v_dot2_f32_bf16 v25, v6, s58, v25\n"
    "v_dot2_f32_bf16 v33, v14, s58, v33\n"
    "v_dot2_f32_bf16 v41, v22, s58, v41\n"
    "v_dot2_f32_bf16 v26, v2, v2, v26\n"
    "v_dot2_f32_bf16 v34, v10, v10, v34\n"
    "v_dot2_f32_bf16 v42, v18, v18, v42\n"
    "v_dot2_f32_bf16 v27, v6, v6, v27\n"
    "v_dot2_f32_bf16 v35, v14, v14, v35\n"
    "v_dot2_f32_bf16 v43, v22, v22, v43\n"
    "v_dot2_f32_bf16 v24, v3, s58, v24\n"
    "v_dot2_f32_bf16 v32, v11, s58, v32\n"
    "v_dot2_f32_bf16 v40, v19, s58, v40\n"
    "v_dot2_f32_bf16 v25, v7, s58, v25\n"
    "v_dot2_f32_bf16 v33, v15, s58, v33\n"
    "v_dot2_f32_bf16 v41, v23, s58, v41\n"
    "v_dot2_f32_bf16 v26, v3, v3, v26\n"
    "v_dot2_f32_bf16 v34, v11, v11, v34\n"
    "v_dot2_f32_bf16 v42, v19, v19, v42\n"
    "v_dot2_f32_bf16 v27, v7, v7, v27\n"
    "v_dot2_f32_bf16 v35, v15, v15, v35\n"
    "v_dot2_f32_bf16 v43, v23, v23, v43\n"
    "v_and_b32_e32 v30, 0xffff0000, v7\n"
    "v_and_b32_e32 v38, 0xffff0000, v15\n"
    "v_and_b32_e32 v46, 0xffff0000, v23\n"
    "v_add_f32_e32 v24, v24, v25\n"
    "v_add_f32_e32 v32, v32, v33\n"
    "v_add_f32_e32 v40, v40, v41\n"
    "v_add_f32_e32 v26, v26, v27\n"
    "v_add_f32_e32 v34, v34, v35\n"
    "v_add_f32_e32 v42, v42, v43\n"
    "v_add_f32_dpp v25, v25, v25 quad_perm:[1,0,3,2] row_mask:0xf bank_mask:0xf bound_ctrl:1\n"
    "v_lshlrev_b32_e32 v48, 16, v0\n"
    "v_add_f32_dpp v33, v33, v33 quad_perm:[1,0,3,2] row_mask:0xf bank_mask:0xf bound_ctrl:1\n"
    "v_and_b32_e32 v49, 0xffff0000, v0\n"
    "v_add_f32_dpp v41, v41, v41 quad_perm:[1,0,3,2] row_mask:0xf bank_mask:0xf bound_ctrl:1\n"
    "v_lshlrev_b32_e32 v50, 16, v1\n"
    "v_mov_b32_dpp v30, v30 quad_perm:[3,3,3,3] row_mask:0xf bank_mask:0xf bound_ctrl:1\n"
    "v_and_b32_e32 v51, 0xffff0000, v1\n"
    "v_mov_b32_dpp v38, v38 quad_perm:[3,3,3,3] row_mask:0xf bank_mask:0xf bound_ctrl:1\n"
    "v_lshlrev_b32_e32 v52, 16, v2\n"
    "v_mov_b32_dpp v46, v46 quad_perm:[3,3,3,3] row_mask:0xf bank_mask:0xf bound_ctrl:1\n"
    "v_and_b32_e32 v53, 0xffff0000, v2\n"
    "v_add_f32_dpp v24, v24, v24 quad_perm:[1,0,3,2] row_mask:0xf bank_mask:0xf bound_ctrl:1\n"
    "v_lshlrev_b32_e32 v54, 16, v3\n"
    "v_add_f32_dpp v32, v32, v32 quad_perm:[1,0,3,2] row_mask:0xf bank_mask:0xf bound_ctrl:1\n"
    "v_and_b32_e32 v55, 0xffff0000, v3\n"
    "v_add_f32_dpp v40, v40, v40 quad_perm:[1,0,3,2] row_mask:0xf bank_mask:0xf bound_ctrl:1\n"
    "v_lshlrev_b32_e32 v56, 16, v4\n"
    "v_add_f32_dpp v26, v26, v26 quad_perm:[1,0,3,2] row_mask:0xf bank_mask:0xf bound_ctrl:1\n"
    "v_and_b32_e32 v57, 0xffff0000, v4\n"
    "v_add_f32_dpp v34, v34, v34 quad_perm:[1,0,3,2] row_mask:0xf bank_mask:0xf bound_ctrl:1\n"
    "v_lshlrev_b32_e32 v58, 16, v5\n"
    "s_waitcnt vmcnt(0)\n"
    "buffer_store_dword v112, v113, s[8:11], s53 offen offset:0 sc1\n"
    "v_add_f32_dpp v42, v42, v42 quad_perm:[1,0,3,2] row_mask:0xf bank_mask:0xf bound_ctrl:1\n"
    "v_and_b32_e32 v59, 0xffff0000, v5\n"
    "v_add_f32_dpp v24, v24, v24 quad_perm:[2,3,0,1] row_mask:0xf bank_mask:0xf bound_ctrl:1\n"
    "v_lshlrev_b32_e32 v60, 16, v6\n"
    "v_add_f32_dpp v32, v32, v32 quad_perm:[2,3,0,1] row_mask:0xf bank_mask:0xf bound_ctrl:1\n"
    "v_and_b32_e32 v61, 0xffff0000, v6\n"
    "s_waitcnt lgkmcnt(0)\n"
    "s_barrier\n"
    "ds_read_b128 v[96:99], v108 offset:0\n"
    "ds_read_b128 v[100:103], v108 offset:4096\n"
    "s_lshl_b32 s45, %1, 6\n"
    "s_add_u32 s45, s45, 35038208\n"
    "v_add_f32_dpp v40, v40, v40 quad_perm:[2,3,0,1] row_mask:0xf bank_mask:0xf bound_ctrl:1\n"
    "v_lshlrev_b32_e32 v62, 16, v7\n"
    "v_add_f32_dpp v26, v26, v26 quad_perm:[2,3,0,1] row_mask:0xf bank_mask:0xf bound_ctrl:1\n"
    "v_and_b32_e32 v63, 0xffff0000, v7\n"
    "v_add_f32_dpp v34, v34, v34 quad_perm:[2,3,0,1] row_mask:0xf bank_mask:0xf bound_ctrl:1\n"
    "v_lshlrev_b32_e32 v64, 16, v8\n"
    "v_add_f32_dpp v42, v42, v42 quad_perm:[2,3,0,1] row_mask:0xf bank_mask:0xf bound_ctrl:1\n"
    "v_and_b32_e32 v65, 0xffff0000, v8\n"
    "v_add_f32_dpp v24, v24, v24 row_half_mirror row_mask:0xf bank_mask:0xf bound_ctrl:1\n"
    "v_lshlrev_b32_e32 v66, 16, v9\n"
    "v_add_f32_dpp v32, v32, v32 row_half_mirror row_mask:0xf bank_mask:0xf bound_ctrl:1\n"
    "v_and_b32_e32 v67, 0xffff0000, v9\n"
    "v_add_f32_dpp v40, v40, v40 row_half_mirror row_mask:0xf bank_mask:0xf bound_ctrl:1\n"
    "v_lshlrev_b32_e32 v68, 16, v10\n"
    "v_add_f32_dpp v26, v26, v26 row_half_mirror row_mask:0xf bank_mask:0xf bound_ctrl:1\n"
    "v_and_b32_e32 v69, 0xffff0000, v10\n"
    "v_add_f32_dpp v34, v34, v34 row_half_mirror row_mask:0xf bank_mask:0xf bound_ctrl:1\n"
    "v_lshlrev_b32_e32 v70, 16, v11\n"
    "v_add_f32_dpp v42, v42, v42 row_half_mirror row_mask:0xf bank_mask:0xf bound_ctrl:1\n"
    "v_and_b32_e32 v71, 0xffff0000, v11\n"
    "v_add_f32_dpp v24, v24, v24 row_mirror row_mask:0xf bank_mask:0xf bound_ctrl:1\n"
    "v_lshlrev_b32_e32 v72, 16, v12\n"
    "v_add_f32_dpp v32, v32, v32 row_mirror row_mask:0xf bank_mask:0xf bound_ctrl:1\n"
    "v_and_b32_e32 v73, 0xffff0000, v12\n"
    "v_add_f32_dpp v40, v40, v40 row_mirror row_mask:0xf bank_mask:0xf bound_ctrl:1\n"
    "v_lshlrev_b32_e32 v74, 16, v13\n"
    "v_add_f32_dpp v26, v26, v26 row_mirror row_mask:0xf bank_mask:0xf bound_ctrl:1\n"
    "v_and_b32_e32 v75, 0xffff0000, v13\n"
    "v_add_f32_dpp v34, v34, v34 row_mirror row_mask:0xf bank_mask:0xf bound_ctrl:1\n"
    "v_lshlrev_b32_e32 v76, 16, v14\n"
    "s_waitcnt lgkmcnt(0)\n"
    "buffer_store_dwordx4 v[96:99], v109, s[8:11], s45 offen offset:0 sc1\n"
    "buffer_store_dwordx4 v[100:103], v110, s[8:11], s45 offen offset:0 sc1\n"
    "v_add_f32_dpp v42, v42, v42 row_mirror row_mask:0xf bank_mask:0xf bound_ctrl:1\n"
    "v_and_b32_e32 v77, 0xffff0000, v14\n"
    "v_mul_f32_e32 v24, 0x3b800000, v24\n"
    "v_lshlrev_b32_e32 v78, 16, v15\n"
    "v_mul_f32_e32 v32, 0x3b800000, v32\n"
    "v_and_b32_e32 v79, 0xffff0000, v15\n"
    "v_mul_f32_e32 v40, 0x3b800000, v40\n"
    "v_lshlrev_b32_e32 v80, 16, v16\n"
    "v_mul_f32_e32 v25, 0.25, v25\n"
    "v_and_b32_e32 v81, 0xffff0000, v16\n"
    "v_mul_f32_e32 v33, 0.25, v33\n"
    "v_lshlrev_b32_e32 v82, 16, v17\n"
    "v_mul_f32_e32 v41, 0.25, v41\n"
    "v_and_b32_e32 v83, 0xffff0000, v17\n"
    "v_mul_f32_e64 v27, v24, -v24\n"
    "v_lshlrev_b32_e32 v84, 16, v18\n"
    "v_mul_f32_e64 v35, v32, -v32\n"
    "v_and_b32_e32 v85, 0xffff0000, v18\n"
    "v_mul_f32_e64 v43, v40, -v40\n"
    "v_lshlrev_b32_e32 v86, 16, v19\n"
    "v_sub_f32_e32 v25, v25, v30\n"
    "v_and_b32_e32 v87, 0xffff0000, v19\n"
    "v_sub_f32_e32 v33, v33, v38\n"
    "v_lshlrev_b32_e32 v88, 16, v20\n"
    "v_sub_f32_e32 v41, v41, v46\n"
    "v_and_b32_e32 v89, 0xffff0000, v20\n"
    "v_fmac_f32_e32 v27, 0x3b800000, v26\n"
    "v_lshlrev_b32_e32 v90, 16, v21\n"
    "v_fmac_f32_e32 v35, 0x3b800000, v34\n"
    "v_and_b32_e32 v91, 0xffff0000, v21\n"
    "v_fmac_f32_e32 v43, 0x3b800000, v42\n"
    "v_lshlrev_b32_e32 v92, 16, v22\n"
    "v_mul_f32_e32 v25, 0x3eaaaaab, v25\n"
    "v_and_b32_e32 v93, 0xffff0000, v22\n"
    "v_mul_f32_e32 v33, 0x3eaaaaab, v33\n"
    "v_lshlrev_b32_e32 v94, 16, v23\n"
    "v_mul_f32_e32 v41, 0x3eaaaaab, v41\n"
    "v_and_b32_e32 v95, 0xffff0000, v23\n"
    "v_max_f32_e32 v27, 0, v27\n"
    "v_max_f32_e32 v35, 0, v35\n"
    "v_max_f32_e32 v43, 0, v43\n"
    "v_cndmask_b32_e64 v25, v24, v25, s[62:63]\n"
    "v_cndmask_b32_e64 v33, v32, v33, s[62:63]\n"
    "v_cndmask_b32_e64 v41, v40, v41, s[62:63]\n"
    "v_add_f32_e32 v27, 0x3727c5ac, v27\n"
    "v_add_f32_e32 v35, 0x3727c5ac, v35\n"
    "v_add_f32_e32 v43, 0x3727c5ac, v43\n"
    "v_rsq_f32_e32 v28, v27\n"
    "v_rsq_f32_e32 v36, v35\n"
    "v_rsq_f32_e32 v44, v43\n"
    "v_fma_f32 v29, -v24, v28, s57\n"
    "v_fma_f32 v37, -v32, v36, s57\n"
    "v_fma_f32 v45, -v40, v44, s57\n"
    "v_fma_f32 v31, -v25, v28, s57\n"
    "v_fma_f32 v39, -v33, v36, s57\n"
    "v_fma_f32 v47, -v41, v44, s57\n"
    "v_sub_f32_e32 v24, v29, v31\n"
    "v_sub_f32_e32 v32, v37, v39\n"
    "v_sub_f32_e32 v40, v45, v47\n"
    "v_pk_fma_f32 v[48:49], v[48:49], v[28:29], v[28:29] op_sel:[0,0,1] op_sel_hi:[1,0,1]\n"
    "v_pk_fma_f32 v[50:51], v[50:51], v[28:29], v[28:29] op_sel:[0,0,1] op_sel_hi:[1,0,1]\n"
    "v_pk_fma_f32 v[52:53], v[52:53], v[28:29], v[28:29] op_sel:[0,0,1] op_sel_hi:[1,0,1]\n"
    "v_pk_fma_f32 v[54:55], v[54:55], v[28:29], v[28:29] op_sel:[0,0,1] op_sel_hi:[1,0,1]\n"
    "v_pk_fma_f32 v[56:57], v[56:57], v[28:29], v[30:31] op_sel:[0,0,1] op_sel_hi:[1,0,1]\n"
    "v_pk_fma_f32 v[58:59], v[58:59], v[28:29], v[30:31] op_sel:[0,0,1] op_sel_hi:[1,0,1]\n"
    "v_pk_fma_f32 v[60:61], v[60:61], v[28:29], v[30:31] op_sel:[0,0,1] op_sel_hi:[1,0,1]\n"
    "v_pk_fma_f32 v[62:63], v[62:63], v[28:29], v[30:31] op_sel:[0,0,1] op_sel_hi:[1,0,1]\n"
    "v_pk_add_f32 v[48:49], v[48:49], s[56:57] op_sel_hi:[1,0]\n"
    "v_pk_add_f32 v[50:51], v[50:51], s[56:57] op_sel_hi:[1,0]\n"
    "v_pk_add_f32 v[52:53], v[52:53], s[56:57] op_sel_hi:[1,0]\n"
    "v_pk_add_f32 v[54:55], v[54:55], s[56:57] op_sel_hi:[1,0]\n"
    "v_pk_add_f32 v[56:57], v[56:57], s[56:57] op_sel_hi:[1,0]\n"
    "v_pk_add_f32 v[58:59], v[58:59], s[56:57] op_sel_hi:[1,0]\n"
    "v_pk_add_f32 v[60:61], v[60:61], s[56:57] op_sel_hi:[1,0]\n"
    "v_pk_add_f32 v[62:63], v[62:63], s[56:57] op_sel_hi:[1,0]\n"
    "v_cvt_pk_bf16_f32 v0, v48, v49\n"
    "v_cvt_pk_bf16_f32 v1, v50, v51\n"
    "v_cvt_pk_bf16_f32 v2, v52, v53\n"
    "v_cvt_pk_bf16_f32 v3, v54, v55\n"
    "v_cvt_pk_bf16_f32 v4, v56, v57\n"
    "v_cvt_pk_bf16_f32 v5, v58, v59\n"
    "v_cvt_pk_bf16_f32 v6, v60, v61\n"
    "v_cvt_pk_bf16_f32 v7, v62, v63\n"
    "buffer_store_dwordx4 v[0:3], v104, s[8:11], s35 offen offset:0 sc1\n"
    "v_bfi_b32 v7, v107, v7, 1.0\n"
    "ds_write_b128 v105, v[0:3] offset:0\n"
    "buffer_store_dwordx4 v[4:7], v104, s[8:11], s35 offen offset:256 sc1\n"
    "v_pk_fma_f32 v[64:65], v[64:65], v[36:37], v[36:37] op_sel:[0,0,1] op_sel_hi:[1,0,1]\n"
    "v_pk_fma_f32 v[66:67], v[66:67], v[36:37], v[36:37] op_sel:[0,0,1] op_sel_hi:[1,0,1]\n"
    "v_pk_fma_f32 v[68:69], v[68:69], v[36:37], v[36:37] op_sel:[0,0,1] op_sel_hi:[1,0,1]\n"
    "v_pk_fma_f32 v[70:71], v[70:71], v[36:37], v[36:37] op_sel:[0,0,1] op_sel_hi:[1,0,1]\n"
    "v_pk_fma_f32 v[72:73], v[72:73], v[36:37], v[38:39] op_sel:[0,0,1] op_sel_hi:[1,0,1]\n"
    "v_pk_fma_f32 v[74:75], v[74:75], v[36:37], v[38:39] op_sel:[0,0,1] op_sel_hi:[1,0,1]\n"
    "v_pk_fma_f32 v[76:77], v[76:77], v[36:37], v[38:39] op_sel:[0,0,1] op_sel_hi:[1,0,1]\n"
    "v_pk_fma_f32 v[78:79], v[78:79], v[36:37], v[38:39] op_sel:[0,0,1] op_sel_hi:[1,0,1]\n"
    "v_pk_add_f32 v[64:65], v[64:65], s[56:57] op_sel_hi:[1,0]\n"
    "v_pk_add_f32 v[66:67], v[66:67], s[56:57] op_sel_hi:[1,0]\n"
    "v_pk_add_f32 v[68:69], v[68:69], s[56:57] op_sel_hi:[1,0]\n"
    "v_pk_add_f32 v[70:71], v[70:71], s[56:57] op_sel_hi:[1,0]\n"
    "v_pk_add_f32 v[72:73], v[72:73], s[56:57] op_sel_hi:[1,0]\n"
    "v_pk_add_f32 v[74:75], v[74:75], s[56:57] op_sel_hi:[1,0]\n"
    "v_pk_add_f32 v[76:77], v[76:77], s[56:57] op_sel_hi:[1,0]\n"
    "v_pk_add_f32 v[78:79], v[78:79], s[56:57] op_sel_hi:[1,0]\n"
    "v_cvt_pk_bf16_f32 v8, v64, v65\n"
    "v_cvt_pk_bf16_f32 v9, v66, v67\n"
    "v_cvt_pk_bf16_f32 v10, v68, v69\n"
    "v_cvt_pk_bf16_f32 v11, v70, v71\n"
    "v_cvt_pk_bf16_f32 v12, v72, v73\n"
    "v_cvt_pk_bf16_f32 v13, v74, v75\n"
    "v_cvt_pk_bf16_f32 v14, v76, v77\n"
    "v_cvt_pk_bf16_f32 v15, v78, v79\n"
    "buffer_store_dwordx4 v[8:11], v104, s[8:11], s54 offen offset:0 sc1\n"
    "v_bfi_b32 v15, v107, v15, 1.0\n"
    "ds_write_b128 v105, v[8:11] offset:8192\n"
    "buffer_store_dwordx4 v[12:15], v104, s[8:11], s54 offen offset:256 sc1\n"
    "v_pk_fma_f32 v[80:81], v[80:81], v[44:45], v[44:45] op_sel:[0,0,1] op_sel_hi:[1,0,1]\n"
    "v_pk_fma_f32 v[82:83], v[82:83], v[44:45], v[44:45] op_sel:[0,0,1] op_sel_hi:[1,0,1]\n"
    "v_pk_fma_f32 v[84:85], v[84:85], v[44:45], v[44:45] op_sel:[0,0,1] op_sel_hi:[1,0,1]\n"
    "v_pk_fma_f32 v[86:87], v[86:87], v[44:45], v[44:45] op_sel:[0,0,1] op_sel_hi:[1,0,1]\n"
    "v_pk_fma_f32 v[88:89], v[88:89], v[44:45], v[46:47] op_sel:[0,0,1] op_sel_hi:[1,0,1]\n"
    "v_pk_fma_f32 v[90:91], v[90:91], v[44:45], v[46:47] op_sel:[0,0,1] op_sel_hi:[1,0,1]\n"
    "v_pk_fma_f32 v[92:93], v[92:93], v[44:45], v[46:47] op_sel:[0,0,1] op_sel_hi:[1,0,1]\n"
    "v_pk_fma_f32 v[94:95], v[94:95], v[44:45], v[46:47] op_sel:[0,0,1] op_sel_hi:[1,0,1]\n"
    "v_pk_add_f32 v[80:81], v[80:81], s[56:57] op_sel_hi:[1,0]\n"
    "v_pk_add_f32 v[82:83], v[82:83], s[56:57] op_sel_hi:[1,0]\n"
    "v_pk_add_f32 v[84:85], v[84:85], s[56:57] op_sel_hi:[1,0]\n"
    "v_pk_add_f32 v[86:87], v[86:87], s[56:57] op_sel_hi:[1,0]\n"
    "v_pk_add_f32 v[88:89], v[88:89], s[56:57] op_sel_hi:[1,0]\n"
    "v_pk_add_f32 v[90:91], v[90:91], s[56:57] op_sel_hi:[1,0]\n"
    "v_pk_add_f32 v[92:93], v[92:93], s[56:57] op_sel_hi:[1,0]\n"
    "v_pk_add_f32 v[94:95], v[94:95], s[56:57] op_sel_hi:[1,0]\n"
    "v_cvt_pk_bf16_f32 v16, v80, v81\n"
    "v_cvt_pk_bf16_f32 v17, v82, v83\n"
    "v_cvt_pk_bf16_f32 v18, v84, v85\n"
    "v_cvt_pk_bf16_f32 v19, v86, v87\n"
    "v_cvt_pk_bf16_f32 v20, v88, v89\n"
    "v_cvt_pk_bf16_f32 v21, v90, v91\n"
    "v_cvt_pk_bf16_f32 v22, v92, v93\n"
    "v_cvt_pk_bf16_f32 v23, v94, v95\n"
    "buffer_store_dwordx4 v[16:19], v104, s[8:11], s60 offen offset:0 sc1\n"
    "v_bfi_b32 v23, v107, v23, 1.0\n"
    "ds_write_b128 v105, v[16:19] offset:16384\n"
    "buffer_store_dwordx4 v[20:23], v104, s[8:11], s60 offen offset:256 sc1\n"
    "v_pk_add_f32 v[56:57], v[56:57], v[24:25] op_sel_hi:[1,0]\n"
    "v_pk_add_f32 v[58:59], v[58:59], v[24:25] op_sel_hi:[1,0]\n"
    "v_pk_add_f32 v[60:61], v[60:61], v[24:25] op_sel_hi:[1,0]\n"
    "v_pk_add_f32 v[62:63], v[62:63], v[24:25] op_sel_hi:[1,0]\n"
    "v_cvt_pk_bf16_f32 v56, v56, v57\n"
    "v_cvt_pk_bf16_f32 v57, v58, v59\n"
    "v_cvt_pk_bf16_f32 v58, v60, v61\n"
    "v_cvt_pk_bf16_f32 v59, v62, v63\n"
    "ds_write_b128 v106, v[56:59] offset:0\n"
    "v_pk_add_f32 v[72:73], v[72:73], v[32:33] op_sel_hi:[1,0]\n"
    "v_pk_add_f32 v[74:75], v[74:75], v[32:33] op_sel_hi:[1,0]\n"
    "v_pk_add_f32 v[76:77], v[76:77], v[32:33] op_sel_hi:[1,0]\n"
    "v_pk_add_f32 v[78:79], v[78:79], v[32:33] op_sel_hi:[1,0]\n"
    "v_cvt_pk_bf16_f32 v72, v72, v73\n"
    "v_cvt_pk_bf16_f32 v73, v74, v75\n"
    "v_cvt_pk_bf16_f32 v74, v76, v77\n"
    "v_cvt_pk_bf16_f32 v75, v78, v79\n"
    "ds_write_b128 v106, v[72:75] offset:8192\n"
    "v_pk_add_f32 v[88:89], v[88:89], v[40:41] op_sel_hi:[1,0]\n"
    "v_pk_add_f32 v[90:91], v[90:91], v[40:41] op_sel_hi:[1,0]\n"
    "v_pk_add_f32 v[92:93], v[92:93], v[40:41] op_sel_hi:[1,0]\n"
    "v_pk_add_f32 v[94:95], v[94:95], v[40:41] op_sel_hi:[1,0]\n"
    "v_cvt_pk_bf16_f32 v88, v88, v89\n"
    "v_cvt_pk_bf16_f32 v89, v90, v91\n"
    "v_cvt_pk_bf16_f32 v90, v92, v93\n"
    "v_cvt_pk_bf16_f32 v91, v94, v95\n"
    "ds_write_b128 v106, v[88:91] offset:16384\n"
    "s_waitcnt lgkmcnt(0)\n"
    "s_barrier\n"
    "ds_read_b128 a[0:3], v250 offset:0\n"
    "ds_read_b128 a[32:35], v250 offset:8192\n"
    "ds_read_b128 a[64:67], v250 offset:16384\n"
    "ds_read_b128 a[4:7], v251 offset:0\n"
    "ds_read_b128 a[36:39], v251 offset:8192\n"
    "ds_read_b128 a[68:71], v251 offset:16384\n"
    "ds_read_b128 a[8:11], v250 offset:2048\n"
    "ds_read_b128 a[40:43], v250 offset:10240\n"
    "ds_read_b128 a[72:75], v250 offset:18432\n"
    "ds_read_b128 a[12:15], v251 offset:2048\n"
    "ds_read_b128 a[44:47], v251 offset:10240\n"
    "ds_read_b128 a[76:79], v251 offset:18432\n"
    "buffer_load_dword v255, v252, s[8:11], 0 offen offset:0 sc1\n"
    "ds_read_b128 a[16:19], v250 offset:4096\n"
    "ds_read_b128 a[48:51], v250 offset:12288\n"
    "ds_read_b128 a[80:83], v250 offset:20480\n"
    "ds_read_b128 a[20:23], v251 offset:4096\n"
    "ds_read_b128 a[52:55], v251 offset:12288\n"
    "ds_read_b128 a[84:87], v251 offset:20480\n"
    "ds_read_b128 a[24:27], v250 offset:6144\n"
    "ds_read_b128 a[56:59], v250 offset:14336\n"
    "ds_read_b128 a[88:91], v250 offset:22528\n"
    "ds_read_b128 a[28:31], v251 offset:6144\n"
    "ds_read_b128 a[60:63], v251 offset:14336\n"
    "ds_read_b128 a[92:95], v251 offset:22528\n"
    "s_lshl_b32 s52, %2, 15\n"
    "s_add_u32 s52, s52, 5242880\n"
    "s_lshl_b32 s61, s37, 12\n"
    "s_lshl_b32 s46, %1, 4\n"
    "s_lshl_b32 s47, %1, 7\n"
    "s_add_u32 s47, s47, 4096\n"
    "s_cmp_eq_u32 s50, 0\n"
    "s_cselect_b32 s46, s46, s47\n"
    "s_add_u32 s46, s46, 5701632\n"
    "s_cmp_eq_u32 s51, 0\n"
    "s_cselect_b32 s46, s46, 2.0\n"
    "v_mov_b32_e32 v40, s39\n"
    "v_mov_b32_e32 v41, 0\n"
    "s_waitcnt vmcnt(1)\n"
    "s_waitcnt lgkmcnt(0)\n"
    "s_barrier\n"
    "buffer_store_dword v40, v41, s[8:11], s46 offen offset:0 sc1\n"
    "s_waitcnt vmcnt(1)\n"
    "v_cmp_ne_u32_e32 vcc, s39, v255\n"
    "s_nop 4\n"
    "s_cbranch_vccnz .Lat_apoll\n"
    ".Lat_apoll_ret:\n"
    "s_add_u32 s47, s52, s61\n"
    "buffer_load_dwordx4 v[112:115], v253, s[8:11], s47 offen offset:0 sc1\n"
    "buffer_load_dwordx4 v[116:119], v253, s[8:11], s47 offen offset:1024 sc1\n"
    "buffer_load_dwordx4 v[128:131], v253, s[8:11], s47 offen offset:2048 sc1\n"
    "s_add_u32 s61, s61, 0x1000\n"
    "buffer_load_dwordx4 v[132:135], v253, s[8:11], s47 offen offset:3072 sc1\n"
    "s_bitset0_b32 s61, 14\n"
    "s_add_u32 s47, s52, s61\n"
    "buffer_load_dwordx4 v[144:147], v253, s[8:11], s47 offen offset:0 sc1\n"
    "buffer_load_dwordx4 v[148:151], v253, s[8:11], s47 offen offset:1024 sc1\n"
    "buffer_load_dwordx4 v[160:163], v253, s[8:11], s47 offen offset:2048 sc1\n"
    "s_add_u32 s61, s61, 0x1000\n"
    "buffer_load_dwordx4 v[164:167], v253, s[8:11], s47 offen offset:3072 sc1\n"
    "s_bitset0_b32 s61, 14\n"
    "s_add_u32 s47, s52, s61\n"
    "buffer_load_dwordx4 v[176:179], v253, s[8:11], s47 offen offset:0 sc1\n"
    "buffer_load_dwordx4 v[180:183], v253, s[8:11], s47 offen offset:1024 sc1\n"
    "buffer_load_dwordx4 v[192:195], v253, s[8:11], s47 offen offset:2048 sc1\n"
    "s_add_u32 s61, s61, 0x1000\n"
    "s_lshl_b32 s59, s32, 4\n"
    "s_lshr_b32 s48, s32, 1\n"
    "s_sub_u32 s59, s59, s48\n"
    "s_lshl_b32 s59, s59, 6\n"
    "s_sub_u32 s48, 3984, s59\n"
    "v_cmp_le_u32_e32 vcc, 0x40, v253\n"
    "v_mov_b32_e32 v0, s48\n"
    "s_cmp_eq_u32 s32, 2\n"
    "s_cselect_b32 s48, 1024, 0\n"
    "v_mov_b32_e32 v2, s48\n"
    "s_add_u32 s59, s59, 5701632\n"
    "v_cndmask_b32_e32 v3, v253, v0, vcc\n"
    "s_lshl_b32 s45, %2, 11\n"
    "s_add_u32 s48, s45, %3\n"
    "v_add_u32_e32 v9, s48, v253\n"
    "s_add_u32 s45, s45, 11022336\n"
    "v_add_u32_e32 v8, s45, v253\n"
    "v_lshrrev_b32_e32 v13, 7, v253\n"
    "v_and_b32_e32 v12, 0x70, v254\n"
    "v_lshl_add_u32 v12, v13, 9, v12\n"
    "v_add_u32_e32 v13, 0xc00, v12\n"
    "v_add_u32_e32 v14, 0x1800, v12\n"
    "v_add_u32_e32 v15, 0x2400, v12\n"
    "s_lshl_b32 s57, s50, 8\n"
    "s_add_u32 s57, s57, 5111808\n"
    "s_add_u32 s62, s43, s34\n"
    "s_xor_b32 s63, s34, 0xc000\n"
    "s_add_u32 s63, s63, s43\n"
    "s_waitcnt vmcnt(9)\n"
    "v_mfma_f32_16x16x32_bf16 v[24:27], v[112:115], a[0:3], 0\n"
    "buffer_load_dwordx4 v[196:199], v253, s[8:11], s47 offen offset:3072 sc1\n"
    "v_mfma_f32_16x16x32_bf16 v[28:31], v[116:119], a[0:3], 0\n"
    "s_bitset0_b32 s61, 14\n"
    "s_mov_b32 s18, 5120000\n"
    "s_mov_b32 s19, 0x20000\n"
    "v_mfma_f32_16x16x32_bf16 v[48:51], v[112:115], a[32:35], 0\n"
    "s_add_u32 s47, s52, s61\n"
    "v_xor_b32_e32 v242, 0x12000, v240\n"
    "v_xor_b32_e32 v243, 0x12000, v241\n"
    "v_mfma_f32_16x16x32_bf16 v[52:55], v[116:119], a[32:35], 0\n"
    "buffer_load_dwordx4 v[208:211], v253, s[8:11], s47 offen offset:0 sc1\n"
    "v_mfma_f32_16x16x32_bf16 v[64:67], v[112:115], a[64:67], 0\n"
    "s_cmp_eq_u32 %2, 3\n"
    "s_cselect_b32 s88, 0x2ff, -1\n"
    "v_mov_b32_e32 v10, 0xc2980000\n"
    "v_mfma_f32_16x16x32_bf16 v[68:71], v[116:119], a[64:67], 0\n"
    "s_waitcnt vmcnt(9)\n"
    "v_cmp_lt_u32_e32 vcc, s88, v253\n"
    "s_nop 1\n"
    "v_cndmask_b32_e32 v10, 0, v10, vcc\n"
    "v_mfma_f32_16x16x32_bf16 v[24:27], v[128:131], a[4:7], v[24:27]\n"
    "buffer_load_dwordx4 v[212:215], v253, s[8:11], s47 offen offset:1024 sc1\n"
    "v_mfma_f32_16x16x32_bf16 v[28:31], v[132:135], a[4:7], v[28:31]\n"
    "s_add_u32 s52, s52, 0x4000\n"
    "v_lshrrev_b32_e32 v5, 4, v253\n"
    "v_min_u32_e32 v5, 2, v5\n"
    "v_mfma_f32_16x16x32_bf16 v[48:51], v[128:131], a[36:39], v[48:51]\n"
    "s_add_u32 s88, s32, 1\n"
    "v_add_u32_e32 v5, s88, v5\n"
    "v_mov_b32_e32 v80, 0\n"
    "v_mfma_f32_16x16x32_bf16 v[52:55], v[132:135], a[36:39], v[52:55]\n"
    "buffer_load_dwordx4 v[224:227], v253, s[8:11], s47 offen offset:2048 sc1\n"
    "v_mfma_f32_16x16x32_bf16 v[64:67], v[128:131], a[68:71], v[64:67]\n"
    "s_add_u32 s61, s61, 0x1000\n"
    "v_and_b32_e32 v5, 3, v5\n"
    "v_lshlrev_b32_e32 v5, 7, v5\n"
    "v_mfma_f32_16x16x32_bf16 v[68:71], v[132:135], a[68:71], v[68:71]\n"
    "s_waitcnt vmcnt(9)\n"
    "s_lshl_b32 s58, %1, 7\n"
    "s_add_u32 s58, s58, 5738432\n"
    "s_cmp_eq_u32 %2, 2\n"
    "s_cselect_b32 s58, s58, 2.0\n"
    "v_mfma_f32_16x16x32_bf16 v[24:27], v[144:147], a[8:11], v[24:27]\n"
    "buffer_load_dwordx4 v[228:231], v253, s[8:11], s47 offen offset:3072 sc1\n"
    "v_mfma_f32_16x16x32_bf16 v[28:31], v[148:151], a[8:11], v[28:31]\n"
    "s_bitset0_b32 s61, 14\n"
    "s_lshl_b32 s88, s50, 13\n"
    "s_add_u32 s88, s88, %3\n"
    "v_mfma_f32_16x16x32_bf16 v[48:51], v[144:147], a[40:43], v[48:51]\n"
    "s_add_u32 s47, s52, s61\n"
    "v_add_u32_e32 v6, s88, v253\n"
    "v_add_u32_e32 v7, %3, v253\n"
    "v_mfma_f32_16x16x32_bf16 v[52:55], v[148:151], a[40:43], v[52:55]\n"
    "buffer_load_dwordx4 v[120:123], v253, s[8:11], s47 offen offset:0 sc1\n"
    "v_mfma_f32_16x16x32_bf16 v[64:67], v[144:147], a[72:75], v[64:67]\n"
    "s_mul_i32 s88, %2, 1024\n"
    "s_add_u32 s88, s88, %3\n"
    "v_lshrrev_b32_e32 v250, 2, v253\n"
    "v_mfma_f32_16x16x32_bf16 v[68:71], v[148:151], a[72:75], v[68:71]\n"
    "s_waitcnt vmcnt(9)\n"
    "v_add_u32_e32 v249, s88, v250\n"
    "s_mov_b32 s49, s88\n"
    "v_mfma_f32_16x16x32_bf16 v[24:27], v[160:163], a[12:15], v[24:27]\n"
    "buffer_load_dwordx4 v[124:127], v253, s[8:11], s47 offen offset:1024 sc1\n"
    "v_mfma_f32_16x16x32_bf16 v[28:31], v[164:167], a[12:15], v[28:31]\n"
    "s_lshl_b32 s81, s88, 2\n"
    "s_lshl2_add_u32 s90, %1, %2\n"
    "s_lshl_b32 s90, s90, 10\n"
    "v_mfma_f32_16x16x32_bf16 v[48:51], v[160:163], a[44:47], v[48:51]\n"
    "s_sub_u32 s81, s90, s81\n"
    "s_add_u32 s81, s81, 36369408\n"
    "s_xor_b32 s88, %2, 2\n"
    "v_mfma_f32_16x16x32_bf16 v[52:55], v[164:167], a[44:47], v[52:55]\n"
    "buffer_load_dwordx4 v[136:139], v253, s[8:11], s47 offen offset:2048 sc1\n"
    "v_mfma_f32_16x16x32_bf16 v[64:67], v[160:163], a[76:79], v[64:67]\n"
    "s_add_u32 s61, s61, 0x1000\n"
    "s_mul_i32 s88, s88, 1024\n"
    "s_add_u32 s88, s88, %3\n"
    "v_mfma_f32_16x16x32_bf16 v[68:71], v[164:167], a[76:79], v[68:71]\n"
    "s_waitcnt vmcnt(9)\n"
    "v_add_u32_e32 v250, s88, v250\n"
    "v_and_b32_e32 v251, 0x1f0, v253\n"
    "v_mfma_f32_16x16x32_bf16 v[24:27], v[176:179], a[16:19], v[24:27]\n"
    "buffer_load_dwordx4 v[140:143], v253, s[8:11], s47 offen offset:3072 sc1\n"
    "v_mfma_f32_16x16x32_bf16 v[28:31], v[180:183], a[16:19], v[28:31]\n"
    "s_bitset0_b32 s61, 14\n"
    "s_lshl_b32 s82, %2, 10\n"
    "s_add_u32 s82, s82, %3\n"
    "v_mfma_f32_16x16x32_bf16 v[48:51], v[176:179], a[48:51], v[48:51]\n"
    "s_add_u32 s47, s52, s61\n"
    "s_add_u32 s82, s82, 20480\n"
    "v_lshrrev_b32_e32 v252, 9, v253\n"
    "v_mfma_f32_16x16x32_bf16 v[52:55], v[180:183], a[48:51], v[52:55]\n"
    "buffer_load_dwordx4 v[152:155], v253, s[8:11], s47 offen offset:0 sc1\n"
    "v_mfma_f32_16x16x32_bf16 v[64:67], v[176:179], a[80:83], v[64:67]\n"
    "s_lshr_b32 s80, %1, 4\n"
    "s_lshl_b32 s80, s80, 7\n"
    "v_lshl_add_u32 v252, v252, 4, s82\n"
    "v_mfma_f32_16x16x32_bf16 v[68:71], v[180:183], a[80:83], v[68:71]\n"
    "s_waitcnt vmcnt(9)\n"
    "s_add_u32 s80, s80, 5775616\n"
    "s_and_b32 s88, s32, 1\n"
    "v_mfma_f32_16x16x32_bf16 v[24:27], v[192:195], a[20:23], v[24:27]\n"
    "buffer_load_dwordx4 v[156:159], v253, s[8:11], s47 offen offset:1024 sc1\n"
    "v_mfma_f32_16x16x32_bf16 v[28:31], v[196:199], a[20:23], v[28:31]\n"
    "s_lshl_b32 s88, s88, 6\n"
    "s_add_u32 s80, s80, s88\n"
    "s_mov_b64 s[20:21], s[8:9]\n"
    "v_mfma_f32_16x16x32_bf16 v[48:51], v[192:195], a[52:55], v[48:51]\n"
    "s_mov_b32 s22, 5242880\n"
    "s_add_u32 s24, s8, 5779456\n"
    "s_addc_u32 s25, s9, 0\n"
    "v_mfma_f32_16x16x32_bf16 v[52:55], v[196:199], a[52:55], v[52:55]\n"
    "buffer_load_dwordx4 v[168:171], v253, s[8:11], s47 offen offset:2048 sc1\n"
    "v_mfma_f32_16x16x32_bf16 v[64:67], v[192:195], a[84:87], v[64:67]\n"
    "s_add_u32 s61, s61, 0x1000\n"
    "s_mov_b32 s26, 5242880\n"
    "s_movk_i32 s42, 512\n"
    "v_mfma_f32_16x16x32_bf16 v[68:71], v[196:199], a[84:87], v[68:71]\n"
    "s_waitcnt vmcnt(9)\n"
    "s_mov_b32 s37, 0\n"
    "s_add_u32 s38, s38, s50\n"
    "v_mfma_f32_16x16x32_bf16 v[24:27], v[208:211], a[24:27], v[24:27]\n"
    "buffer_load_dwordx4 v[172:175], v253, s[8:11], s47 offen offset:3072 sc1\n"
    "v_mfma_f32_16x16x32_bf16 v[28:31], v[212:215], a[24:27], v[28:31]\n"
    "s_bitset0_b32 s61, 14\n"
    "v_bfe_u32 v16, v253, 4, 2\n"
    "v_bfe_u32 v17, v253, 6, 2\n"
    "v_mov_b32_e32 v18, 0x3f80\n"
    "v_mfma_f32_16x16x32_bf16 v[48:51], v[208:211], a[56:59], v[48:51]\n"
    "s_add_u32 s47, s52, s61\n"
    "v_cmp_eq_u32_e32 vcc, 3, v16\n"
    "s_nop 1\n"
    "v_cndmask_b32_e64 v18, v18, 1.0, vcc\n"
    "v_cndmask_b32_e64 v16, v16, 0, vcc\n"
    "v_mfma_f32_16x16x32_bf16 v[52:55], v[212:215], a[56:59], v[52:55]\n"
    "buffer_load_dwordx4 v[184:187], v253, s[8:11], s47 offen offset:0 sc1\n"
    "v_mfma_f32_16x16x32_bf16 v[64:67], v[208:211], a[88:91], v[64:67]\n"
    "v_or_b32_e32 v16, 12, v16\n"
    "v_mul_u32_u24_e32 v16, 0x1111, v16\n"
    "v_mfma_f32_16x16x32_bf16 v[68:71], v[212:215], a[88:91], v[68:71]\n"
    "s_waitcnt vmcnt(9)\n"
    "v_cmp_eq_u32_e32 vcc, 0, v17\n"
    "s_nop 1\n"
    "v_cndmask_b32_e32 v19, 0, v18, vcc\n"
    "v_mfma_f32_16x16x32_bf16 v[24:27], v[224:227], a[28:31], v[24:27]\n"
    "buffer_load_dwordx4 v[188:191], v253, s[8:11], s47 offen offset:1024 sc1\n"
    "v_mfma_f32_16x16x32_bf16 v[28:31], v[228:231], a[28:31], v[28:31]\n"
    "v_accvgpr_write_b32 a192, v19\n"
    "v_mov_b32_e32 v81, 0\n"
    "v_mfma_f32_16x16x32_bf16 v[48:51], v[224:227], a[60:63], v[48:51]\n"
    "v_cmp_eq_u32_e32 vcc, 1, v17\n"
    "s_nop 1\n"
    "v_cndmask_b32_e32 v19, 0, v18, vcc\n"
    "v_mfma_f32_16x16x32_bf16 v[52:55], v[228:231], a[60:63], v[52:55]\n"
    "buffer_load_dwordx4 v[200:203], v253, s[8:11], s47 offen offset:2048 sc1\n"
    "v_mfma_f32_16x16x32_bf16 v[64:67], v[224:227], a[92:95], v[64:67]\n"
    "s_add_u32 s61, s61, 0x1000\n"
    "v_cvt_pk_bf16_f32 v24, v24, v25\n"
    "v_mov_b32_e32 v82, 0\n"
    "v_mfma_f32_16x16x32_bf16 v[68:71], v[228:231], a[92:95], v[68:71]\n"
    "s_waitcnt vmcnt(9)\n"
    "v_cvt_pk_bf16_f32 v25, v26, v27\n"
    "v_accvgpr_write_b32 a193, v19\n"
    "v_mfma_f32_16x16x32_bf16 v[32:35], v[120:123], a[0:3], 0\n"
    "buffer_load_dwordx4 v[204:207], v253, s[8:11], s47 offen offset:3072 sc1\n"
    "v_mfma_f32_16x16x32_bf16 v[36:39], v[124:127], a[0:3], 0\n"
    "s_bitset0_b32 s61, 14\n"
    "v_cvt_pk_bf16_f32 v26, v28, v29\n"
    "v_cmp_eq_u32_e32 vcc, 2, v17\n"
    "s_nop 1\n"
    "v_cndmask_b32_e32 v19, 0, v18, vcc\n"
    "v_mfma_f32_16x16x32_bf16 v[56:59], v[120:123], a[32:35], 0\n"
    "s_add_u32 s47, s52, s61\n"
    "v_cvt_pk_bf16_f32 v27, v30, v31\n"
    "v_accvgpr_write_b32 a194, v19\n"
    "v_mfma_f32_16x16x32_bf16 v[60:63], v[124:127], a[32:35], 0\n"
    "buffer_load_dwordx4 v[216:219], v253, s[8:11], s47 offen offset:0 sc1\n"
    "v_mfma_f32_16x16x32_bf16 v[72:75], v[120:123], a[64:67], 0\n"
    "buffer_store_dwordx4 v[24:27], v8, s[8:11], s35 offen offset:0 sc1\n"
    "v_cvt_pk_bf16_f32 v48, v48, v49\n"
    "v_cmp_eq_u32_e32 vcc, 3, v17\n"
    "s_nop 1\n"
    "v_cndmask_b32_e32 v19, 0, v18, vcc\n"
    "v_mfma_f32_16x16x32_bf16 v[76:79], v[124:127], a[64:67], 0\n"
    "s_waitcnt vmcnt(10)\n"
    "v_cvt_pk_bf16_f32 v49, v50, v51\n"
    "v_accvgpr_write_b32 a195, v19\n"
    "v_mfma_f32_16x16x32_bf16 v[32:35], v[136:139], a[4:7], v[32:35]\n"
    "buffer_load_dwordx4 v[220:223], v253, s[8:11], s47 offen offset:1024 sc1\n"
    "v_mfma_f32_16x16x32_bf16 v[36:39], v[140:143], a[4:7], v[36:39]\n"
    "s_add_u32 s52, s52, 0x4000\n"
    "v_cvt_pk_bf16_f32 v50, v52, v53\n"
    "v_cvt_pk_bf16_f32 v51, v54, v55\n"
    "v_mfma_f32_16x16x32_bf16 v[56:59], v[136:139], a[36:39], v[56:59]\n"
    "buffer_load_dword v0, v3, s[8:11], s59 offen offset:0 sc1\n"
    "v_mfma_f32_16x16x32_bf16 v[60:63], v[140:143], a[36:39], v[60:63]\n"
    "buffer_load_dwordx4 v[232:235], v253, s[8:11], s47 offen offset:2048 sc1\n"
    "buffer_store_dwordx4 v[48:51], v8, s[8:11], s54 offen offset:0 sc1\n"
    "v_mov_b32_e32 v83, 0\n"
    "v_mfma_f32_16x16x32_bf16 v[72:75], v[136:139], a[68:71], v[72:75]\n"
    "s_add_u32 s61, s61, 0x1000\n"
    "v_cvt_pk_bf16_f32 v64, v64, v65\n"
    "v_mov_b32_e32 v84, 0\n"
    "v_mfma_f32_16x16x32_bf16 v[76:79], v[140:143], a[68:71], v[76:79]\n"
    "s_waitcnt vmcnt(12)\n"
    "v_cvt_pk_bf16_f32 v65, v66, v67\n"
    "v_mov_b32_e32 v85, 0\n"
    "v_mfma_f32_16x16x32_bf16 v[32:35], v[152:155], a[8:11], v[32:35]\n"
    "buffer_load_dwordx4 v[236:239], v253, s[8:11], s47 offen offset:3072 sc1\n"
    "v_mfma_f32_16x16x32_bf16 v[36:39], v[156:159], a[8:11], v[36:39]\n"
    "s_bitset0_b32 s61, 14\n"
    "v_cvt_pk_bf16_f32 v66, v68, v69\n"
    "v_mov_b32_e32 v86, 0\n"
    "v_mfma_f32_16x16x32_bf16 v[56:59], v[152:155], a[40:43], v[56:59]\n"
    "s_add_u32 s28, s38, 2\n"
    "v_cvt_pk_bf16_f32 v67, v70, v71\n"
    "v_mov_b32_e32 v87, 0\n"
    "v_mfma_f32_16x16x32_bf16 v[60:63], v[156:159], a[40:43], v[60:63]\n"
    "buffer_store_dwordx4 v[64:67], v8, s[8:11], s60 offen offset:0 sc1\n"
    "v_mov_b32_e32 v88, 0\n"
    "v_mov_b32_e32 v89, 0\n"
    "v_mfma_f32_16x16x32_bf16 v[72:75], v[152:155], a[72:75], v[72:75]\n"
    "s_sub_u32 s88, s28, 10\n"
    "s_min_u32 s28, s28, s88\n"
    "v_mov_b32_e32 v90, 0\n"
    "v_mfma_f32_16x16x32_bf16 v[76:79], v[156:159], a[72:75], v[76:79]\n"
    "s_waitcnt vmcnt(12)\n"
    "s_lshl_b32 s88, s28, 0\n"
    "v_mov_b32_e32 v91, 0\n"
    "v_mfma_f32_16x16x32_bf16 v[32:35], v[168:171], a[12:15], v[32:35]\n"
    "s_or_b32 s36, s88, s88\n"
    "s_lshl1_add_u32 s88, s28, 1\n"
    "v_mov_b32_e32 v92, 0\n"
    "v_mfma_f32_16x16x32_bf16 v[36:39], v[172:175], a[12:15], v[36:39]\n"
    "s_mul_i32 s88, s88, 52\n"
    "s_lshr_b32 s88, s88, 8\n"
    "v_mov_b32_e32 v93, 0\n"
    "v_mfma_f32_16x16x32_bf16 v[56:59], v[168:171], a[44:47], v[56:59]\n"
    "s_mul_i32 s90, s88, 5\n"
    "ds_write_b128 v9, v[24:27] offset:0\n"
    "v_mfma_f32_16x16x32_bf16 v[60:63], v[172:175], a[44:47], v[60:63]\n"
    "s_lshr_b32 s90, s90, 1\n"
    "s_sub_u32 s28, s28, s90\n"
    "v_mov_b32_e32 v94, 0\n"
    "v_mfma_f32_16x16x32_bf16 v[72:75], v[168:171], a[76:79], v[72:75]\n"
    "s_sub_u32 s90, s32, s88\n"
    "s_sub_u32 s90, s90, 1\n"
    "v_mov_b32_e32 v95, 0\n"
    "v_mfma_f32_16x16x32_bf16 v[76:79], v[172:175], a[76:79], v[76:79]\n"
    "s_waitcnt vmcnt(10)\n"
    "s_and_b32 s90, s90, 3\n"
    "v_mov_b32_e32 v96, 0\n"
    "v_mfma_f32_16x16x32_bf16 v[32:35], v[184:187], a[16:19], v[32:35]\n"
    "s_lshl2_add_u32 s88, s33, s88\n"
    "s_mul_i32 s88, s88, 3\n"
    "v_mov_b32_e32 v97, 0\n"
    "v_mfma_f32_16x16x32_bf16 v[36:39], v[188:191], a[16:19], v[36:39]\n"
    "s_add_u32 s88, s88, s90\n"
    "v_mov_b32_e32 v98, 0\n"
    "v_mov_b32_e32 v99, 0\n"
    "v_mfma_f32_16x16x32_bf16 v[56:59], v[184:187], a[48:51], v[56:59]\n"
    "s_mul_i32 s88, s88, 24832\n"
    "s_add_u32 s88, s88, 16265216\n"
    "v_mov_b32_e32 v100, 0\n"
    "v_mfma_f32_16x16x32_bf16 v[60:63], v[188:191], a[48:51], v[60:63]\n"
    "s_cmp_eq_u32 s90, 3\n"
    "s_cselect_b32 s88, 2.0, s88\n"
    "s_cselect_b32 s89, 2, 3\n"
    "v_mfma_f32_16x16x32_bf16 v[72:75], v[184:187], a[80:83], v[72:75]\n"
    "s_lshl1_add_u32 s83, s28, s51\n"
    "s_lshl_b32 s83, s83, 12\n"
    "v_mov_b32_e32 v101, 0\n"
    "v_mfma_f32_16x16x32_bf16 v[76:79], v[188:191], a[80:83], v[76:79]\n"
    "s_waitcnt vmcnt(8)\n"
    "ds_write_b128 v9, v[48:51] offset:8192\n"
    "v_mfma_f32_16x16x32_bf16 v[32:35], v[200:203], a[20:23], v[32:35]\n"
    "s_add_u32 s83, s83, s88\n"
    "s_lshl2_add_u32 s28, s28, s88\n"
    "v_mov_b32_e32 v102, 0\n"
    "v_mfma_f32_16x16x32_bf16 v[36:39], v[204:207], a[20:23], v[36:39]\n"
    "s_add_u32 s29, s38, 4\n"
    "s_sub_u32 s88, s29, 10\n"
    "v_mov_b32_e32 v103, 0\n"
    "v_mfma_f32_16x16x32_bf16 v[56:59], v[200:203], a[52:55], v[56:59]\n"
    "s_min_u32 s29, s29, s88\n"
    "s_lshl_b32 s88, s29, 4\n"
    "v_mov_b32_e32 v104, 0\n"
    "v_mfma_f32_16x16x32_bf16 v[60:63], v[204:207], a[52:55], v[60:63]\n"
    "s_or_b32 s36, s88, s36\n"
    "s_lshl1_add_u32 s88, s29, 1\n"
    "v_mov_b32_e32 v105, 0\n"
    "v_mfma_f32_16x16x32_bf16 v[72:75], v[200:203], a[84:87], v[72:75]\n"
    "s_waitcnt vmcnt(4)\n"
    "v_cmp_ne_u32_e32 vcc, s39, v0\n"
    "v_mov_b32_e32 v106, 0\n"
    "v_mfma_f32_16x16x32_bf16 v[76:79], v[204:207], a[84:87], v[76:79]\n"
    "s_waitcnt vmcnt(5)\n"
    "s_mul_i32 s88, s88, 52\n"
    "v_mov_b32_e32 v107, 0\n"
    "v_mfma_f32_16x16x32_bf16 v[32:35], v[216:219], a[24:27], v[32:35]\n"
    "s_lshr_b32 s88, s88, 8\n"
    "s_mul_i32 s90, s88, 5\n"
    "v_mfma_f32_16x16x32_bf16 v[36:39], v[220:223], a[24:27], v[36:39]\n"
    "s_lshr_b32 s90, s90, 1\n"
    "ds_write_b128 v9, v[64:67] offset:16384\n"
    "v_mfma_f32_16x16x32_bf16 v[56:59], v[216:219], a[56:59], v[56:59]\n"
    "s_cbranch_vccnz .Lat_h1poll\n"
    ".Lat_h1poll_ret:\n"
    "v_mfma_f32_16x16x32_bf16 v[60:63], v[220:223], a[56:59], v[60:63]\n"
    "s_mov_b32 m0, s63\n"
    "s_sub_u32 s29, s29, s90\n"
    "v_mov_b32_e32 v108, 0\n"
    "v_mfma_f32_16x16x32_bf16 v[72:75], v[216:219], a[88:91], v[72:75]\n"
    "buffer_load_dwordx4 v254, s[8:11], s57 offen offset:0 sc1 lds\n"
    "v_mfma_f32_16x16x32_bf16 v[76:79], v[220:223], a[88:91], v[76:79]\n"
    "s_waitcnt vmcnt(2)\n"
    "s_sub_u32 m0, s63, 1024\n"
    "v_mov_b32_e32 v109, 0\n"
    "v_mfma_f32_16x16x32_bf16 v[32:35], v[232:235], a[28:31], v[32:35]\n"
    "buffer_load_dwordx4 v254, s[8:11], s57 offen offset:2048 sc1 lds\n"
    "v_mfma_f32_16x16x32_bf16 v[36:39], v[236:239], a[28:31], v[36:39]\n"
    "s_add_u32 m0, s63, 3968\n"
    "s_sub_u32 s90, s32, s88\n"
    "v_mov_b32_e32 v110, 0\n"
    "v_mfma_f32_16x16x32_bf16 v[56:59], v[232:235], a[60:63], v[56:59]\n"
    "buffer_load_dwordx4 v254, s[8:11], s57 offen offset:128 sc1 lds\n"
    "v_mfma_f32_16x16x32_bf16 v[60:63], v[236:239], a[60:63], v[60:63]\n"
    "s_add_u32 m0, s63, 2944\n"
    "s_sub_u32 s90, s90, 1\n"
    "s_and_b32 s90, s90, 3\n"
    "v_mfma_f32_16x16x32_bf16 v[72:75], v[232:235], a[92:95], v[72:75]\n"
    "buffer_load_dwordx4 v254, s[8:11], s57 offen offset:2176 sc1 lds\n"
    "v_mfma_f32_16x16x32_bf16 v[76:79], v[236:239], a[92:95], v[76:79]\n"
    "s_lshl2_add_u32 s88, s33, s88\n"
    "s_mul_i32 s88, s88, 3\n"
    "v_mov_b32_e32 v111, 0\n"
    "v_cvt_pk_bf16_f32 v32, v32, v33\n"
    "v_cvt_pk_bf16_f32 v33, v34, v35\n"
    "v_cvt_pk_bf16_f32 v34, v36, v37\n"
    "v_cvt_pk_bf16_f32 v35, v38, v39\n"
    "v_or_b32_e32 v35, v10, v35\n"
    "ds_write_b128 v9, v[32:35] offset:1024\n"
    "buffer_store_dwordx4 v[32:35], v8, s[8:11], s35 offen offset:1024 sc1\n"
    "v_cvt_pk_bf16_f32 v56, v56, v57\n"
    "v_cvt_pk_bf16_f32 v57, v58, v59\n"
    "v_cvt_pk_bf16_f32 v58, v60, v61\n"
    "v_cvt_pk_bf16_f32 v59, v62, v63\n"
    "v_or_b32_e32 v59, v10, v59\n"
    "ds_write_b128 v9, v[56:59] offset:9216\n"
    "buffer_store_dwordx4 v[56:59], v8, s[8:11], s54 offen offset:1024 sc1\n"
    "v_cvt_pk_bf16_f32 v72, v72, v73\n"
    "v_cvt_pk_bf16_f32 v73, v74, v75\n"
    "v_cvt_pk_bf16_f32 v74, v76, v77\n"
    "v_cvt_pk_bf16_f32 v75, v78, v79\n"
    "v_or_b32_e32 v75, v10, v75\n"
    "ds_write_b128 v9, v[72:75] offset:17408\n"
    "buffer_store_dwordx4 v[72:75], v8, s[8:11], s60 offen offset:1024 sc1\n"
    "v_mov_b32_e32 v112, 0\n"
    "v_mov_b32_e32 v113, 0\n"
    "v_mov_b32_e32 v114, 0\n"
    "s_add_u32 m0, s62, 0\n"
    "v_mov_b32_e32 v115, 0\n"
    "v_mov_b32_e32 v116, 0\n"
    "v_mov_b32_e32 v117, 0\n"
    "buffer_load_dwordx4 v12, s[8:11], s40 offen offset:0 sc1 lds\n"
    "v_mov_b32_e32 v118, 0\n"
    "v_mov_b32_e32 v119, 0\n"
    "v_mov_b32_e32 v120, 0\n"
    "buffer_load_dwordx4 v13, s[8:11], s40 offen offset:1024 sc1 lds\n"
    "v_mov_b32_e32 v121, 0\n"
    "v_mov_b32_e32 v122, 0\n"
    "v_mov_b32_e32 v123, 0\n"
    "buffer_load_dwordx4 v14, s[8:11], s40 offen offset:2048 sc1 lds\n"
    "v_mov_b32_e32 v124, 0\n"
    "v_mov_b32_e32 v125, 0\n"
    "v_mov_b32_e32 v126, 0\n"
    "buffer_load_dwordx4 v15, s[8:11], s40 offen offset:3072 sc1 lds\n"
    "v_mov_b32_e32 v127, 0\n"
    "v_mov_b32_e32 v128, 0\n"
    "v_mov_b32_e32 v129, 0\n"
    "s_add_u32 s40, s40, 0x8000\n"
    "v_mov_b32_e32 v130, 0\n"
    "v_mov_b32_e32 v131, 0\n"
    "v_mov_b32_e32 v132, 0\n"
    "s_add_u32 m0, s62, 4096\n"
    "v_mov_b32_e32 v133, 0\n"
    "v_mov_b32_e32 v134, 0\n"
    "v_mov_b32_e32 v135, 0\n"
    "buffer_load_dwordx4 v12, s[8:11], s41 offen offset:0 sc1 lds\n"
    "v_mov_b32_e32 v136, 0\n"
    "v_mov_b32_e32 v137, 0\n"
    "v_mov_b32_e32 v138, 0\n"
    "buffer_load_dwordx4 v13, s[8:11], s41 offen offset:1024 sc1 lds\n"
    "v_mov_b32_e32 v139, 0\n"
    "v_mov_b32_e32 v140, 0\n"
    "v_mov_b32_e32 v141, 0\n"
    "buffer_load_dwordx4 v14, s[8:11], s41 offen offset:2048 sc1 lds\n"
    "v_mov_b32_e32 v142, 0\n"
    "v_mov_b32_e32 v143, 0\n"
    "v_mov_b32_e32 v144, 0\n"
    "buffer_load_dwordx4 v15, s[8:11], s41 offen offset:3072 sc1 lds\n"
    "v_mov_b32_e32 v145, 0\n"
    "v_mov_b32_e32 v146, 0\n"
    "v_mov_b32_e32 v147, 0\n"
    "s_add_u32 s41, s41, 0x8000\n"
    "v_mov_b32_e32 v148, 0\n"
    "v_mov_b32_e32 v149, 0\n"
    "v_mov_b32_e32 v150, 0\n"
    "v_mov_b32_e32 v151, 0\n"
    "s_xor_b32 s45, s34, 0xc000\n"
    "v_add_u32_e32 v204, s45, v240\n"
    "v_add_u32_e32 v205, s45, v241\n"
    "v_add_u32_e32 v206, s45, v242\n"
    "v_add_u32_e32 v207, s45, v243\n"
    "v_add_u32_e32 v208, s45, v244\n"
    "v_add_u32_e32 v209, s45, v245\n"
    "v_add_u32_e32 v210, s45, v246\n"
    "buffer_load_dword v0, v253, s[8:11], s59 offen offset:0 sc1\n"
    "buffer_load_dword v1, v2, s[8:11], s59 offen offset:0 sc1\n"
    "v_add_u32_e32 v211, s45, v247\n"
    "v_add_u32_e32 v200, s34, v240\n"
    "v_add_u32_e32 v201, s34, v241\n"
    "v_add_u32_e32 v202, s34, v242\n"
    "v_add_u32_e32 v203, s34, v243\n"
    "v_mov_b32_e32 v152, v16\n"
    "v_and_b32_e32 v153, 0xf0, v253\n"
    "v_lshrrev_b32_e32 v154, 9, v253\n"
    "v_lshl_add_u32 v153, v154, 8, v153\n"
    "v_bfe_u32 v154, v253, 8, 1\n"
    "v_lshl_add_u32 v153, v154, 3, v153\n"
    "v_mov_b32_e32 v156, 0\n"
    "v_mov_b32_e32 v157, 0\n"
    "v_mov_b32_e32 v158, 0\n"
    "v_mov_b32_e32 v159, 0\n"
    "v_mov_b32_e32 v56, 0\n"
    "v_mov_b32_e32 v57, 0\n"
    "v_mov_b32_e32 v58, 0\n"
    "v_mov_b32_e32 v59, 0\n"
    "v_mov_b32_e32 v60, 0\n"
    "v_mov_b32_e32 v61, 0\n"
    "v_mov_b32_e32 v62, 0\n"
    "v_mov_b32_e32 v63, 0\n"
    "v_mov_b32_e32 v64, 0\n"
    "v_mov_b32_e32 v65, 0\n"
    "v_mov_b32_e32 v66, 0\n"
    "v_mov_b32_e32 v67, 0\n"
    "v_mov_b32_e32 v68, 0\n"
    "v_mov_b32_e32 v69, 0\n"
    "v_mov_b32_e32 v70, 0\n"
    "v_mov_b32_e32 v71, 0\n"
    "v_mov_b32_e32 v72, 0\n"
    "v_mov_b32_e32 v73, 0\n"
    "v_mov_b32_e32 v74, 0\n"
    "v_mov_b32_e32 v75, 0\n"
    "v_mov_b32_e32 v76, 0\n"
    "v_mov_b32_e32 v77, 0\n"
    "v_mov_b32_e32 v78, 0\n"
    "v_mov_b32_e32 v79, 0\n"
    "s_add_u32 s88, s88, s90\n"
    "s_mul_i32 s88, s88, 24832\n"
    "s_add_u32 s88, s88, 16265216\n"
    "s_lshl1_add_u32 s84, s29, s51\n"
    "s_lshl_b32 s84, s84, 12\n"
    "s_add_u32 s84, s84, s88\n"
    "s_lshl2_add_u32 s29, s29, s88\n"
    "s_add_u32 s30, s38, 6\n"
    "s_sub_u32 s88, s30, 10\n"
    "s_min_u32 s30, s30, s88\n"
    "s_lshl_b32 s88, s30, 8\n"
    "s_or_b32 s36, s88, s36\n"
    "s_lshl1_add_u32 s88, s30, 1\n"
    "s_mul_i32 s88, s88, 52\n"
    "s_lshr_b32 s88, s88, 8\n"
    "s_mul_i32 s90, s88, 5\n"
    "s_lshr_b32 s90, s90, 1\n"
    "s_sub_u32 s30, s30, s90\n"
    "s_sub_u32 s90, s32, s88\n"
    "s_sub_u32 s90, s90, 1\n"
    "s_and_b32 s90, s90, 3\n"
    "s_lshl2_add_u32 s88, s33, s88\n"
    "s_mul_i32 s88, s88, 3\n"
    "s_add_u32 s88, s88, s90\n"
    "s_mul_i32 s88, s88, 24832\n"
    "s_add_u32 s88, s88, 16265216\n"
    "s_lshl1_add_u32 s85, s30, s51\n"
    "s_lshl_b32 s85, s85, 12\n"
    "s_add_u32 s85, s85, s88\n"
    "s_lshl2_add_u32 s30, s30, s88\n"
    "s_add_u32 s31, s38, 8\n"
    "s_sub_u32 s88, s31, 10\n"
    "s_min_u32 s31, s31, s88\n"
    "s_lshl_b32 s88, s31, 12\n"
    "s_or_b32 s36, s88, s36\n"
    "s_lshl1_add_u32 s88, s31, 1\n"
    "s_mul_i32 s88, s88, 52\n"
    "s_lshr_b32 s88, s88, 8\n"
    "s_mul_i32 s90, s88, 5\n"
    "s_lshr_b32 s90, s90, 1\n"
    "s_sub_u32 s31, s31, s90\n"
    "s_sub_u32 s90, s32, s88\n"
    "s_sub_u32 s90, s90, 1\n"
    "s_and_b32 s90, s90, 3\n"
    "s_lshl2_add_u32 s88, s33, s88\n"
    "s_mul_i32 s88, s88, 3\n"
    "s_add_u32 s88, s88, s90\n"
    "s_mul_i32 s88, s88, 24832\n"
    "s_add_u32 s88, s88, 16265216\n"
    "s_lshl1_add_u32 s86, s31, s51\n"
    "s_lshl_b32 s86, s86, 12\n"
    "s_add_u32 s86, s86, s88\n"
    "s_lshl2_add_u32 s31, s31, s88\n"
    "s_lshl_b32 s87, %1, 6\n"
    "s_add_u32 s87, s87, %2\n"
    "s_lshl2_add_u32 s87, s87, 5636096\n"
    "s_or_b32 s88, %1, %2\n"
    "s_cselect_b32 s88, 2.0, 5777664\n"
    "s_lshl_b32 s47, s33, 9\n"
    "s_add_u32 s47, s47, 5738496\n"
    "v_lshlrev_b32_e32 v9, 3, v253\n"
    "s_cmp_eq_u32 s50, 0\n"
    "s_cselect_b32 s46, s35, s54\n"
    "s_cselect_b32 s45, s60, 2.0\n"
    "s_cmp_eq_u32 s51, 0\n"
    "s_cselect_b32 s46, s46, s45\n"
    "s_add_u32 s46, s46, 11022336\n"
    "s_waitcnt vmcnt(10)\n"
    "s_waitcnt lgkmcnt(0)\n"
    "s_barrier\n"
    "buffer_store_dword v40, v41, s[8:11], s58 offen offset:64 sc1\n"
    "buffer_load_dword v228, v9, s[8:11], s46 offen offset:0 sc1\n"
    "ds_read_b128 a[0:3], v6 offset:0\n"
    "ds_read_b128 a[4:7], v6 offset:1024\n"
    "ds_read_b128 a[8:11], v6 offset:2048\n"
    "ds_read_b128 a[12:15], v6 offset:3072\n"
    "ds_read_b128 a[16:19], v6 offset:4096\n"
    "ds_read_b128 a[20:23], v6 offset:5120\n"
    "ds_read_b128 a[24:27], v6 offset:6144\n"
    "ds_read_b128 a[28:31], v6 offset:7168\n"
    "s_waitcnt vmcnt(4)\n"
    "s_barrier\n"
    "ds_read_b128 a[160:163], v204 offset:0\n"
    "ds_read_b128 a[164:167], v205 offset:0\n"
    "ds_read_b128 a[168:171], v204 offset:4096\n"
    "ds_read_b128 a[172:175], v205 offset:4096\n"
    "ds_read_b128 a[176:179], v206 offset:0\n"
    "ds_read_b128 a[180:183], v207 offset:0\n"
    "ds_read_b128 a[184:187], v206 offset:4096\n"
    "ds_read_b128 a[188:191], v207 offset:4096\n"
    "s_add_u32 m0, s62, 16384\n"
    "s_nop 0\n"
    "buffer_load_dwordx4 v12, s[8:11], s40 offen offset:0 sc1 lds\n"
    "buffer_load_dwordx4 v13, s[8:11], s40 offen offset:1024 sc1 lds\n"
    "buffer_load_dwordx4 v14, s[8:11], s40 offen offset:2048 sc1 lds\n"
    "buffer_load_dwordx4 v15, s[8:11], s40 offen offset:3072 sc1 lds\n"
    "s_add_u32 s40, s40, 0x8000\n"
    "s_add_u32 m0, s62, 20480\n"
    "s_nop 0\n"
    "buffer_load_dwordx4 v12, s[8:11], s41 offen offset:0 sc1 lds\n"
    "buffer_load_dwordx4 v13, s[8:11], s41 offen offset:1024 sc1 lds\n"
    "buffer_load_dwordx4 v14, s[8:11], s41 offen offset:2048 sc1 lds\n"
    "buffer_load_dwordx4 v15, s[8:11], s41 offen offset:3072 sc1 lds\n"
    "s_add_u32 s41, s41, 0x8000\n"
    "s_waitcnt lgkmcnt(4)\n"
    "v_mfma_f32_16x16x32_bf16 v[220:223], a[160:163], a[0:3], 0\n"
    "ds_read_b128 a[32:35], v7 offset:16384\n"
    "ds_read_b128 a[36:39], v7 offset:17408\n"
    "v_mfma_f32_16x16x32_bf16 v[220:223], a[164:167], a[4:7], v[220:223]\n"
    "ds_read_b128 a[40:43], v7 offset:18432\n"
    "ds_read_b128 a[44:47], v7 offset:19456\n"
    "v_mfma_f32_16x16x32_bf16 v[220:223], a[168:171], a[8:11], v[220:223]\n"
    "ds_read_b128 a[48:51], v7 offset:20480\n"
    "ds_read_b128 a[52:55], v7 offset:21504\n"
    "v_mfma_f32_16x16x32_bf16 v[220:223], a[172:175], a[12:15], v[220:223]\n"
    "ds_read_b128 a[56:59], v7 offset:22528\n"
    "ds_read_b128 a[60:63], v7 offset:23552\n"
    "s_waitcnt lgkmcnt(8)\n"
    "v_mfma_f32_16x16x32_bf16 v[220:223], a[176:179], a[16:19], v[220:223]\n"
    "ds_read_b64_tr_b16 a[232:233], v208 offset:0\n"
    "ds_read_b64_tr_b16 a[234:235], v209 offset:0\n"
    "v_mfma_f32_16x16x32_bf16 v[220:223], a[180:183], a[20:23], v[220:223]\n"
    "ds_read_b64_tr_b16 a[236:237], v210 offset:0\n"
    "ds_read_b64_tr_b16 a[238:239], v211 offset:0\n"
    "v_mfma_f32_16x16x32_bf16 v[220:223], a[184:187], a[24:27], v[220:223]\n"
    "ds_read_b64_tr_b16 a[240:241], v208 offset:4096\n"
    "ds_read_b64_tr_b16 a[242:243], v209 offset:4096\n"
    "v_mfma_f32_16x16x32_bf16 v[220:223], a[188:191], a[28:31], v[220:223]\n"
    "buffer_load_dword v11, v5, s[8:11], s47 offen offset:0 sc1\n"
    "ds_read_b64_tr_b16 a[244:245], v210 offset:4096\n"
    "s_waitcnt lgkmcnt(11)\n"
    "v_mfma_f32_16x16x32_bf16 v[224:227], a[160:163], a[32:35], 0\n"
    "ds_read_b64_tr_b16 a[246:247], v211 offset:4096\n"
    "ds_read_b128 a[200:203], v203 offset:6144\n"
    "v_mfma_f32_16x16x32_bf16 v[224:227], a[164:167], a[36:39], v[224:227]\n"
    "ds_read_b128 a[204:207], v200 offset:2048\n"
    "ds_read_b128 a[208:211], v201 offset:2048\n"
    "v_mfma_f32_16x16x32_bf16 v[224:227], a[168:171], a[40:43], v[224:227]\n"
    "ds_read_b128 a[212:215], v200 offset:6144\n"
    "ds_read_b128 a[216:219], v201 offset:6144\n"
    "v_mfma_f32_16x16x32_bf16 v[224:227], a[172:175], a[44:47], v[224:227]\n"
    "ds_read_b128 a[220:223], v202 offset:2048\n"
    "ds_read_b128 a[224:227], v203 offset:2048\n"
    "s_waitcnt lgkmcnt(15)\n"
    "v_mfma_f32_16x16x32_bf16 v[224:227], a[176:179], a[48:51], v[224:227]\n"
    "ds_read_b128 a[228:231], v202 offset:6144\n"
    "v_mfma_f32_16x16x32_bf16 v[224:227], a[180:183], a[52:55], v[224:227]\n"
    "v_mfma_f32_16x16x32_bf16 v[224:227], a[184:187], a[56:59], v[224:227]\n"
    "v_mfma_f32_16x16x32_bf16 v[224:227], a[188:191], a[60:63], v[224:227]\n"
    "s_andn2_b32 s61, s32, s50\n"
    "s_bfe_i32 s61, s61, 0x10000\n"
    "ds_read_b128 a[160:163], v203 offset:4096\n"
    "ds_read_b128 a[164:167], v200 offset:0\n"
    "ds_read_b128 a[168:171], v201 offset:0\n"
    "ds_read_b128 a[172:175], v200 offset:4096\n"
    "ds_read_b128 a[176:179], v201 offset:4096\n"
    "ds_read_b128 a[180:183], v202 offset:0\n"
    "ds_read_b128 a[184:187], v203 offset:0\n"
    "ds_read_b128 a[188:191], v202 offset:4096\n"
    "s_and_b32 s57, s63, 0xffffdfff\n"
    "s_sub_u32 s57, s57, 16384\n"
    "v_add_u32_e32 v216, s57, v253\n"
    "s_lshl_b32 s45, s51, 12\n"
    "s_add_u32 s57, s57, s45\n"
    "s_mul_i32 s48, s33, 81920\n"
    "s_add_u32 s48, s48, s45\n"
    "s_add_u32 s48, s48, 11022336\n"
    "v_exp_f32_e32 v220, v220\n"
    "v_exp_f32_e32 v221, v221\n"
    "v_exp_f32_e32 v222, v222\n"
    "v_exp_f32_e32 v223, v223\n"
    "v_exp_f32_e32 v224, v224\n"
    "v_exp_f32_e32 v225, v225\n"
    "v_exp_f32_e32 v226, v226\n"
    "v_exp_f32_e32 v227, v227\n"
    "v_cvt_pk_bf16_f32 v220, v220, v221\n"
    "v_cvt_pk_bf16_f32 v221, v222, v223\n"
    "v_cvt_pk_bf16_f32 v222, v224, v225\n"
    "v_cvt_pk_bf16_f32 v223, v226, v227\n"
    "v_and_b32_e32 v222, s61, v222\n"
    "v_and_b32_e32 v223, s61, v223\n"
    "s_waitcnt vmcnt(11)\n"
    "s_waitcnt lgkmcnt(0)\n"
    "s_barrier\n"
    "v_xor_b32_e32 v1, s39, v1\n"
    "v_xor_b32_e32 v0, s39, v0\n"
    "v_or_b32_e32 v0, v0, v1\n"
    "v_cmp_ne_u32_e32 vcc, 0, v0\n"
    "s_nop 3\n"
    "s_cbranch_vccnz .Lat_hpoll\n"
    ".Lat_hpoll_ret:\n"
    "s_waitcnt vmcnt(0)\n"
    "v_cmp_ne_u32_e32 vcc, s39, v11\n"
    "s_nop 3\n"
    "s_cbranch_vccnz .Lat_qpoll\n"
    ".Lat_qpoll_ret:\n"
    "s_cmp_eq_u32 s50, 0\n"
    "s_cbranch_scc1 .Lat_qx\n"
    "s_bfe_u32 s45, s36, 0x40000\n"
    "s_add_u32 m0, s57, 0\n"
    "s_lshl_b32 s45, s45, 13\n"
    "s_add_u32 s45, s45, s48\n"
    "buffer_load_dwordx4 v253, s[8:11], s45 offen offset:0 sc1 lds\n"
    "buffer_load_dwordx4 v253, s[8:11], s45 offen offset:1024 sc1 lds\n"
    "buffer_load_dwordx4 v253, s[8:11], s45 offen offset:2048 sc1 lds\n"
    "buffer_load_dwordx4 v253, s[8:11], s45 offen offset:3072 sc1 lds\n"
    ".Lat_qx:\n"
    "s_bfe_u32 s45, s36, 0x40004\n"
    "s_add_u32 m0, s57, 8192\n"
    "s_lshl_b32 s45, s45, 13\n"
    "s_add_u32 s45, s45, s48\n"
    "buffer_load_dwordx4 v253, s[8:11], s45 offen offset:0 sc1 lds\n"
    "buffer_load_dwordx4 v253, s[8:11], s45 offen offset:1024 sc1 lds\n"
    "buffer_load_dwordx4 v253, s[8:11], s45 offen offset:2048 sc1 lds\n"
    "buffer_load_dwordx4 v253, s[8:11], s45 offen offset:3072 sc1 lds\n"
    "v_mfma_f32_16x16x16_bf16 v[52:55], a[246:247], v[222:223], 0\n"
    "v_mfma_f32_16x16x16_bf16 v[48:51], a[244:245], v[222:223], 0\n"
    "s_bfe_u32 s45, s36, 0x40008\n"
    "s_add_u32 m0, s57, 16384\n"
    "s_lshl_b32 s45, s45, 13\n"
    "s_add_u32 s45, s45, s48\n"
    "v_mfma_f32_16x16x16_bf16 v[44:47], a[242:243], v[222:223], 0\n"
    "buffer_load_dwordx4 v253, s[8:11], s45 offen offset:0 sc1 lds\n"
    "v_mfma_f32_16x16x16_bf16 v[40:43], a[240:241], v[222:223], 0\n"
    "v_mfma_f32_16x16x16_bf16 v[36:39], a[238:239], v[222:223], 0\n"
    "buffer_load_dwordx4 v253, s[8:11], s45 offen offset:1024 sc1 lds\n"
    "v_mfma_f32_16x16x16_bf16 v[32:35], a[236:237], v[222:223], 0\n"
    "v_mfma_f32_16x16x16_bf16 a[252:255], a[234:235], v[222:223], 0\n"
    "buffer_load_dwordx4 v253, s[8:11], s45 offen offset:2048 sc1 lds\n"
    "v_mfma_f32_16x16x16_bf16 a[248:251], a[232:233], v[222:223], 0\n"
    "v_mfma_f32_16x16x16_bf16 v[28:31], a[246:247], v[220:221], 0\n"
    "buffer_load_dwordx4 v253, s[8:11], s45 offen offset:3072 sc1 lds\n"
    "v_mfma_f32_16x16x16_bf16 v[24:27], a[244:245], v[220:221], 0\n"
    "s_bfe_u32 s45, s36, 0x4000c\n"
    "s_add_u32 m0, s57, 24576\n"
    "s_lshl_b32 s45, s45, 13\n"
    "s_add_u32 s45, s45, s48\n"
    "v_mfma_f32_16x16x16_bf16 v[20:23], a[242:243], v[220:221], 0\n"
    "buffer_load_dwordx4 v253, s[8:11], s45 offen offset:0 sc1 lds\n"
    "v_mfma_f32_16x16x16_bf16 v[16:19], a[240:241], v[220:221], 0\n"
    "v_mfma_f32_16x16x16_bf16 v[12:15], a[238:239], v[220:221], 0\n"
    "buffer_load_dwordx4 v253, s[8:11], s45 offen offset:1024 sc1 lds\n"
    "v_mfma_f32_16x16x16_bf16 v[8:11], a[236:237], v[220:221], 0\n"
    "v_mfma_f32_16x16x16_bf16 v[4:7], a[234:235], v[220:221], 0\n"
    "buffer_load_dwordx4 v253, s[8:11], s45 offen offset:2048 sc1 lds\n"
    "v_mfma_f32_16x16x16_bf16 v[0:3], a[232:233], v[220:221], 0\n"
    "v_mfma_f32_16x16x32_bf16 v[160:163], a[160:163], a[28:31], 0\n"
    "buffer_load_dwordx4 v253, s[8:11], s45 offen offset:3072 sc1 lds\n"
    "v_mfma_f32_16x16x32_bf16 v[160:163], a[164:167], a[0:3], v[160:163]\n"
    "s_mov_b32 m0, s49\n"
    "v_mfma_f32_16x16x32_bf16 v[160:163], a[168:171], a[4:7], v[160:163]\n"
    "v_mfma_f32_16x16x32_bf16 v[160:163], a[172:175], a[8:11], v[160:163]\n"
    "v_mfma_f32_16x16x32_bf16 v[160:163], a[176:179], a[12:15], v[160:163]\n"
    "v_mfma_f32_16x16x32_bf16 v[160:163], a[180:183], a[16:19], v[160:163]\n"
    "v_mfma_f32_16x16x32_bf16 v[160:163], a[184:187], a[20:23], v[160:163]\n"
    "v_mfma_f32_16x16x32_bf16 v[160:163], a[188:191], a[24:27], v[160:163]\n"
    "v_mfma_f32_16x16x32_bf16 v[164:167], a[200:203], a[28:31], 0\n"
    "v_mfma_f32_16x16x32_bf16 v[164:167], a[204:207], a[0:3], v[164:167]\n"
    "v_mfma_f32_16x16x32_bf16 v[164:167], a[208:211], a[4:7], v[164:167]\n"
    "v_exp_f32_e32 v160, v160\n"
    "v_mfma_f32_16x16x32_bf16 v[164:167], a[212:215], a[8:11], v[164:167]\n"
    "v_exp_f32_e32 v161, v161\n"
    "v_mfma_f32_16x16x32_bf16 v[164:167], a[216:219], a[12:15], v[164:167]\n"
    "v_exp_f32_e32 v162, v162\n"
    "v_mfma_f32_16x16x32_bf16 v[164:167], a[220:223], a[16:19], v[164:167]\n"
    "v_exp_f32_e32 v163, v163\n"
    "v_mfma_f32_16x16x32_bf16 v[164:167], a[224:227], a[20:23], v[164:167]\n"
    "v_cvt_pk_bf16_f32 v160, v160, v161\n"
    "v_mfma_f32_16x16x32_bf16 v[164:167], a[228:231], a[24:27], v[164:167]\n"
    "s_waitcnt vmcnt(8)\n"
    "s_barrier\n"
    "s_cmp_eq_u32 s50, 0\n"
    "s_cbranch_scc1 .Lat_qy\n"
    "ds_read_b128 a[60:63], v216 offset:7168\n"
    "ds_read_b128 a[32:35], v216 offset:0\n"
    "ds_read_b128 a[36:39], v216 offset:1024\n"
    "ds_read_b128 a[40:43], v216 offset:2048\n"
    "ds_read_b128 a[44:47], v216 offset:3072\n"
    "ds_read_b128 a[48:51], v216 offset:4096\n"
    "ds_read_b128 a[52:55], v216 offset:5120\n"
    "ds_read_b128 a[56:59], v216 offset:6144\n"
    ".Lat_qy:\n"
    "ds_read_b128 a[92:95], v216 offset:15360\n"
    "ds_read_b128 a[64:67], v216 offset:8192\n"
    "s_waitcnt lgkmcnt(8)\n"
    "v_mfma_f32_16x16x32_bf16 v[168:171], a[160:163], a[60:63], 0\n"
    "v_cvt_pk_bf16_f32 v161, v162, v163\n"
    "v_mfma_f32_16x16x32_bf16 v[168:171], a[164:167], a[32:35], v[168:171]\n"
    "s_waitcnt lgkmcnt(6)\n"
    "ds_read_b128 a[68:71], v216 offset:9216\n"
    "v_mfma_f32_16x16x32_bf16 v[168:171], a[168:171], a[36:39], v[168:171]\n"
    "ds_read_b128 a[72:75], v216 offset:10240\n"
    "v_mfma_f32_16x16x32_bf16 v[168:171], a[172:175], a[40:43], v[168:171]\n"
    "s_waitcnt lgkmcnt(6)\n"
    "ds_read_b128 a[76:79], v216 offset:11264\n"
    "ds_write_addtid_b32 v160 offset:0\n"
    "v_mfma_f32_16x16x32_bf16 v[168:171], a[176:179], a[44:47], v[168:171]\n"
    "v_exp_f32_e32 v164, v164\n"
    "v_mfma_f32_16x16x32_bf16 v[168:171], a[180:183], a[48:51], v[168:171]\n"
    "s_waitcnt lgkmcnt(6)\n"
    "ds_read_b128 a[80:83], v216 offset:12288\n"
    "v_mfma_f32_16x16x32_bf16 v[168:171], a[184:187], a[52:55], v[168:171]\n"
    "ds_read_b128 a[84:87], v216 offset:13312\n"
    "v_mfma_f32_16x16x32_bf16 v[168:171], a[188:191], a[56:59], v[168:171]\n"
    "ds_read_b128 a[88:91], v216 offset:14336\n"
    "ds_write_addtid_b32 v161 offset:256\n"
    "v_mfma_f32_16x16x32_bf16 v[172:175], a[200:203], a[60:63], 0\n"
    "v_exp_f32_e32 v165, v165\n"
    "v_mfma_f32_16x16x32_bf16 v[172:175], a[204:207], a[32:35], v[172:175]\n"
    "v_exp_f32_e32 v166, v166\n"
    "v_mfma_f32_16x16x32_bf16 v[172:175], a[208:211], a[36:39], v[172:175]\n"
    "v_exp_f32_e32 v167, v167\n"
    "v_mfma_f32_16x16x32_bf16 v[172:175], a[212:215], a[40:43], v[172:175]\n"
    "v_cvt_pk_bf16_f32 v162, v164, v165\n"
    "v_cvt_pk_bf16_f32 v163, v166, v167\n"
    "v_mfma_f32_16x16x32_bf16 v[172:175], a[216:219], a[44:47], v[172:175]\n"
    "ds_write_addtid_b32 v162 offset:512\n"
    "v_exp_f32_e32 v168, v168\n"
    "v_mfma_f32_16x16x32_bf16 v[172:175], a[220:223], a[48:51], v[172:175]\n"
    "v_exp_f32_e32 v169, v169\n"
    "v_mfma_f32_16x16x32_bf16 v[172:175], a[224:227], a[52:55], v[172:175]\n"
    "v_exp_f32_e32 v170, v170\n"
    "v_mfma_f32_16x16x32_bf16 v[172:175], a[228:231], a[56:59], v[172:175]\n"
    "s_waitcnt lgkmcnt(9)\n"
    "v_exp_f32_e32 v171, v171\n"
    "v_mfma_f32_16x16x32_bf16 v[176:179], a[160:163], a[92:95], 0\n"
    "ds_write_addtid_b32 v163 offset:768\n"
    "v_cvt_pk_bf16_f32 v168, v168, v169\n"
    "v_mfma_f32_16x16x32_bf16 v[176:179], a[164:167], a[64:67], v[176:179]\n"
    "s_waitcnt lgkmcnt(8)\n"
    "ds_write_addtid_b32 v168 offset:4096\n"
    "v_cvt_pk_bf16_f32 v169, v170, v171\n"
    "v_mfma_f32_16x16x32_bf16 v[176:179], a[168:171], a[68:71], v[176:179]\n"
    "v_exp_f32_e32 v172, v172\n"
    "v_mfma_f32_16x16x32_bf16 v[176:179], a[172:175], a[72:75], v[176:179]\n"
    "s_waitcnt lgkmcnt(6)\n"
    "v_exp_f32_e32 v173, v173\n"
    "v_mfma_f32_16x16x32_bf16 v[176:179], a[176:179], a[76:79], v[176:179]\n"
    "v_exp_f32_e32 v174, v174\n"
    "v_mfma_f32_16x16x32_bf16 v[176:179], a[180:183], a[80:83], v[176:179]\n"
    "s_waitcnt vmcnt(0)\n"
    "s_barrier\n"
    "s_waitcnt lgkmcnt(4)\n"
    "v_mfma_f32_16x16x32_bf16 v[176:179], a[184:187], a[84:87], v[176:179]\n"
    "ds_write_addtid_b32 v169 offset:4352\n"
    "v_exp_f32_e32 v175, v175\n"
    "v_mfma_f32_16x16x32_bf16 v[176:179], a[188:191], a[88:91], v[176:179]\n"
    "ds_read_b128 a[124:127], v216 offset:23552\n"
    "v_cvt_pk_bf16_f32 v170, v172, v173\n"
    "v_mfma_f32_16x16x32_bf16 v[180:183], a[200:203], a[92:95], 0\n"
    "ds_read_b128 a[96:99], v216 offset:16384\n"
    "v_cvt_pk_bf16_f32 v171, v174, v175\n"
    "v_mfma_f32_16x16x32_bf16 v[180:183], a[204:207], a[64:67], v[180:183]\n"
    "ds_read_b128 a[100:103], v216 offset:17408\n"
    "v_exp_f32_e32 v176, v176\n"
    "v_mfma_f32_16x16x32_bf16 v[180:183], a[208:211], a[68:71], v[180:183]\n"
    "ds_read_b128 a[104:107], v216 offset:18432\n"
    "ds_write_addtid_b32 v170 offset:4608\n"
    "v_mfma_f32_16x16x32_bf16 v[180:183], a[212:215], a[72:75], v[180:183]\n"
    "ds_read_b128 a[108:111], v216 offset:19456\n"
    "v_exp_f32_e32 v177, v177\n"
    "v_mfma_f32_16x16x32_bf16 v[180:183], a[216:219], a[76:79], v[180:183]\n"
    "ds_read_b128 a[112:115], v216 offset:20480\n"
    "v_exp_f32_e32 v178, v178\n"
    "v_mfma_f32_16x16x32_bf16 v[180:183], a[220:223], a[80:83], v[180:183]\n"
    "ds_read_b128 a[116:119], v216 offset:21504\n"
    "ds_write_addtid_b32 v171 offset:4864\n"
    "v_mfma_f32_16x16x32_bf16 v[180:183], a[224:227], a[84:87], v[180:183]\n"
    "ds_read_b128 a[120:123], v216 offset:22528\n"
    "v_mfma_f32_16x16x32_bf16 v[180:183], a[228:231], a[88:91], v[180:183]\n"
    "s_waitcnt lgkmcnt(8)\n"
    "s_sub_u32 m0, s63, 16384\n"
    "v_exp_f32_e32 v179, v179\n"
    "v_mfma_f32_16x16x32_bf16 v[184:187], a[160:163], a[124:127], 0\n"
    "ds_read_b128 a[156:159], v216 offset:31744\n"
    "buffer_load_dwordx4 v248, s[8:11], s40 offen offset:0 sc1 lds\n"
    "v_cvt_pk_bf16_f32 v176, v176, v177\n"
    "v_mfma_f32_16x16x32_bf16 v[184:187], a[164:167], a[96:99], v[184:187]\n"
    "s_waitcnt lgkmcnt(7)\n"
    "ds_read_b128 a[128:131], v216 offset:24576\n"
    "v_cvt_pk_bf16_f32 v177, v178, v179\n"
    "v_mfma_f32_16x16x32_bf16 v[184:187], a[168:171], a[100:103], v[184:187]\n"
    "ds_read_b128 a[132:135], v216 offset:25600\n"
    "v_mfma_f32_16x16x32_bf16 v[184:187], a[172:175], a[104:107], v[184:187]\n"
    "s_waitcnt lgkmcnt(6)\n"
    "ds_read_b128 a[136:139], v216 offset:26624\n"
    "v_mfma_f32_16x16x32_bf16 v[184:187], a[176:179], a[108:111], v[184:187]\n"
    "ds_read_b128 a[140:143], v216 offset:27648\n"
    "buffer_load_dwordx4 v248, s[8:11], s40 offen offset:1024 sc1 lds\n"
    "v_mfma_f32_16x16x32_bf16 v[184:187], a[180:183], a[112:115], v[184:187]\n"
    "s_waitcnt lgkmcnt(5)\n"
    "ds_read_b128 a[144:147], v216 offset:28672\n"
    "v_mfma_f32_16x16x32_bf16 v[184:187], a[184:187], a[116:119], v[184:187]\n"
    "ds_read_b128 a[148:151], v216 offset:29696\n"
    "v_mfma_f32_16x16x32_bf16 v[184:187], a[188:191], a[120:123], v[184:187]\n"
    "ds_read_b128 a[152:155], v216 offset:30720\n"
    "v_mfma_f32_16x16x32_bf16 v[188:191], a[200:203], a[124:127], 0\n"
    "buffer_load_dwordx4 v248, s[8:11], s40 offen offset:2048 sc1 lds\n"
    "v_exp_f32_e32 v180, v180\n"
    "v_mfma_f32_16x16x32_bf16 v[188:191], a[204:207], a[96:99], v[188:191]\n"
    "ds_write_b32 v249, v176 offset:8192\n"
    "v_exp_f32_e32 v181, v181\n"
    "v_mfma_f32_16x16x32_bf16 v[188:191], a[208:211], a[100:103], v[188:191]\n"
    "ds_write_b32 v249, v177 offset:8448\n"
    "v_exp_f32_e32 v182, v182\n"
    "v_mfma_f32_16x16x32_bf16 v[188:191], a[212:215], a[104:107], v[188:191]\n"
    "v_exp_f32_e32 v183, v183\n"
    "v_mfma_f32_16x16x32_bf16 v[188:191], a[216:219], a[108:111], v[188:191]\n"
    "buffer_load_dwordx4 v248, s[8:11], s40 offen offset:3072 sc1 lds\n"
    "v_cvt_pk_bf16_f32 v178, v180, v181\n"
    "v_cvt_pk_bf16_f32 v179, v182, v183\n"
    "v_mfma_f32_16x16x32_bf16 v[188:191], a[220:223], a[112:115], v[188:191]\n"
    "s_add_u32 s40, s40, 0x8000\n"
    "ds_write_b32 v249, v178 offset:8704\n"
    "v_exp_f32_e32 v184, v184\n"
    "v_mfma_f32_16x16x32_bf16 v[188:191], a[224:227], a[116:119], v[188:191]\n"
    "s_sub_u32 m0, s63, 12288\n"
    "ds_write_b32 v249, v179 offset:8960\n"
    "v_exp_f32_e32 v185, v185\n"
    "v_mfma_f32_16x16x32_bf16 v[188:191], a[228:231], a[120:123], v[188:191]\n"
    "s_waitcnt lgkmcnt(10)\n"
    "v_exp_f32_e32 v186, v186\n"
    "v_mfma_f32_16x16x32_bf16 v[192:195], a[160:163], a[156:159], 0\n"
    "buffer_load_dwordx4 v248, s[8:11], s41 offen offset:0 sc1 lds\n"
    "v_exp_f32_e32 v187, v187\n"
    "v_mfma_f32_16x16x32_bf16 v[192:195], a[164:167], a[128:131], v[192:195]\n"
    "s_waitcnt lgkmcnt(8)\n"
    "ds_read_b128 a[160:163], v203 offset:20480\n"
    "v_cvt_pk_bf16_f32 v184, v184, v185\n"
    "v_mfma_f32_16x16x32_bf16 v[192:195], a[168:171], a[132:135], v[192:195]\n"
    "ds_read_b128 a[164:167], v200 offset:16384\n"
    "v_cvt_pk_bf16_f32 v185, v186, v187\n"
    "v_mfma_f32_16x16x32_bf16 v[192:195], a[172:175], a[136:139], v[192:195]\n"
    "s_waitcnt lgkmcnt(8)\n"
    "ds_read_b128 a[168:171], v201 offset:16384\n"
    "v_exp_f32_e32 v188, v188\n"
    "v_mfma_f32_16x16x32_bf16 v[192:195], a[176:179], a[140:143], v[192:195]\n"
    "ds_read_b128 a[172:175], v200 offset:20480\n"
    "buffer_load_dwordx4 v248, s[8:11], s41 offen offset:1024 sc1 lds\n"
    "v_exp_f32_e32 v189, v189\n"
    "v_mfma_f32_16x16x32_bf16 v[192:195], a[180:183], a[144:147], v[192:195]\n"
    "s_waitcnt lgkmcnt(8)\n"
    "ds_read_b128 a[176:179], v201 offset:20480\n"
    "v_exp_f32_e32 v190, v190\n"
    "v_mfma_f32_16x16x32_bf16 v[192:195], a[184:187], a[148:151], v[192:195]\n"
    "ds_read_b128 a[180:183], v202 offset:16384\n"
    "v_mfma_f32_16x16x32_bf16 v[192:195], a[188:191], a[152:155], v[192:195]\n"
    "ds_read_b128 a[184:187], v203 offset:16384\n"
    "v_mfma_f32_16x16x32_bf16 v[196:199], a[200:203], a[156:159], 0\n"
    "ds_read_b128 a[188:191], v202 offset:20480\n"
    "buffer_load_dwordx4 v248, s[8:11], s41 offen offset:2048 sc1 lds\n"
    "v_mfma_f32_16x16x32_bf16 v[196:199], a[204:207], a[128:131], v[196:199]\n"
    "ds_write_b32 v249, v184 offset:12288\n"
    "v_exp_f32_e32 v191, v191\n"
    "v_mfma_f32_16x16x32_bf16 v[196:199], a[208:211], a[132:135], v[196:199]\n"
    "ds_write_b32 v249, v185 offset:12544\n"
    "v_cvt_pk_bf16_f32 v186, v188, v189\n"
    "v_cvt_pk_bf16_f32 v187, v190, v191\n"
    "v_mfma_f32_16x16x32_bf16 v[196:199], a[212:215], a[136:139], v[196:199]\n"
    "ds_write_b32 v249, v186 offset:12800\n"
    "v_exp_f32_e32 v192, v192\n"
    "v_mfma_f32_16x16x32_bf16 v[196:199], a[216:219], a[140:143], v[196:199]\n"
    "buffer_load_dwordx4 v248, s[8:11], s41 offen offset:3072 sc1 lds\n"
    "v_exp_f32_e32 v193, v193\n"
    "v_mfma_f32_16x16x32_bf16 v[196:199], a[220:223], a[144:147], v[196:199]\n"
    "s_add_u32 s41, s41, 0x8000\n"
    "ds_write_b32 v249, v187 offset:13056\n"
    "v_exp_f32_e32 v194, v194\n"
    "v_mfma_f32_16x16x32_bf16 v[196:199], a[224:227], a[148:151], v[196:199]\n"
    "v_exp_f32_e32 v195, v195\n"
    "v_mfma_f32_16x16x32_bf16 v[196:199], a[228:231], a[152:155], v[196:199]\n"
    "v_cvt_pk_bf16_f32 v192, v192, v193\n"
    "ds_write_b32 v249, v192 offset:16384\n"
    "v_cvt_pk_bf16_f32 v193, v194, v195\n"
    "ds_write_b32 v249, v193 offset:16640\n"
    "s_cmp_lg_u32 s34, 0\n"
    "s_cselect_b32 s44, 2, 1\n"
    "s_waitcnt lgkmcnt(4)\n"
    "s_nop 0\n"
    "v_exp_f32_e32 v196, v196\n"
    "v_exp_f32_e32 v197, v197\n"
    "v_exp_f32_e32 v198, v198\n"
    "v_exp_f32_e32 v199, v199\n"
    "v_cvt_pk_bf16_f32 v194, v196, v197\n"
    "v_cvt_pk_bf16_f32 v195, v198, v199\n"
    "ds_write_b32 v249, v194 offset:16896\n"
    "ds_write_b32 v249, v195 offset:17152\n"
    "s_cbranch_scc1 .Lat_p2e\n"
    ".p2align 6\n"
    ".Lat_p:\n"
    "v_mfma_f32_16x16x32_bf16 v[200:203], a[160:163], a[28:31], 0\n"
    "v_mfma_f32_16x16x32_bf16 v[200:203], a[164:167], a[0:3], v[200:203]\n"
    "v_mfma_f32_16x16x32_bf16 v[208:211], a[164:167], a[32:35], 0\n"
    "v_mfma_f32_16x16x32_bf16 v[208:211], a[160:163], a[60:63], v[208:211]\n"
    "v_mfma_f32_16x16x32_bf16 v[216:219], a[160:163], a[92:95], 0\n"
    "v_mfma_f32_16x16x32_bf16 v[216:219], a[164:167], a[64:67], v[216:219]\n"
    "s_waitcnt lgkmcnt(0)\n"
    "v_mfma_f32_16x16x32_bf16 v[224:227], a[164:167], a[96:99], 0\n"
    "s_barrier\n"
    "v_mfma_f32_16x16x32_bf16 v[224:227], a[160:163], a[124:127], v[224:227]\n"
    "s_add_u32 m0, s43, 49152\n"
    "ds_read2st64_b32 v[164:165], v250 offset0:0 offset1:1\n"
    "v_mfma_f32_16x16x32_bf16 v[232:235], a[160:163], a[156:159], 0\n"
    "ds_read_b128 a[160:163], v243 offset:22528\n"
    "v_mfma_f32_16x16x32_bf16 v[232:235], a[164:167], a[128:131], v[232:235]\n"
    "ds_read_b128 a[164:167], v240 offset:18432\n"
    "v_mfma_f32_16x16x32_bf16 v[232:235], a[168:171], a[132:135], v[232:235]\n"
    "buffer_load_dwordx4 v248, s[8:11], s40 offen offset:0 sc1 lds\n"
    "v_mfma_f32_16x16x32_bf16 v[232:235], a[172:175], a[136:139], v[232:235]\n"
    "v_mfma_f32_16x16x32_bf16 v[224:227], a[172:175], a[104:107], v[224:227]\n"
    "v_mfma_f32_16x16x32_bf16 v[224:227], a[168:171], a[100:103], v[224:227]\n"
    "ds_read2st64_b32 v[166:167], v250 offset0:2 offset1:3\n"
    "v_mfma_f32_16x16x32_bf16 v[216:219], a[168:171], a[68:71], v[216:219]\n"
    "v_mfma_f32_16x16x32_bf16 v[216:219], a[172:175], a[72:75], v[216:219]\n"
    "buffer_load_dwordx4 v248, s[8:11], s40 offen offset:1024 sc1 lds\n"
    "v_mfma_f32_16x16x32_bf16 v[208:211], a[172:175], a[40:43], v[208:211]\n"
    "v_mfma_f32_16x16x32_bf16 v[208:211], a[168:171], a[36:39], v[208:211]\n"
    "ds_read2st64_b32 v[172:173], v250 offset0:16 offset1:17\n"
    "v_mfma_f32_16x16x32_bf16 v[200:203], a[168:171], a[4:7], v[200:203]\n"
    "ds_read_b128 a[168:171], v241 offset:18432\n"
    "v_mfma_f32_16x16x32_bf16 v[200:203], a[172:175], a[8:11], v[200:203]\n"
    "ds_read_b128 a[172:175], v240 offset:22528\n"
    "v_mfma_f32_16x16x32_bf16 v[200:203], a[176:179], a[12:15], v[200:203]\n"
    "buffer_load_dwordx4 v248, s[8:11], s40 offen offset:2048 sc1 lds\n"
    "v_mfma_f32_16x16x32_bf16 v[200:203], a[180:183], a[16:19], v[200:203]\n"
    "ds_read2st64_b32 v[174:175], v250 offset0:18 offset1:19\n"
    "v_mfma_f32_16x16x32_bf16 v[208:211], a[180:183], a[48:51], v[208:211]\n"
    "v_mfma_f32_16x16x32_bf16 v[208:211], a[176:179], a[44:47], v[208:211]\n"
    "v_mfma_f32_16x16x32_bf16 v[216:219], a[176:179], a[76:79], v[216:219]\n"
    "ds_read2st64_b32 v[180:181], v250 offset0:32 offset1:33\n"
    "v_mfma_f32_16x16x32_bf16 v[216:219], a[180:183], a[80:83], v[216:219]\n"
    "buffer_load_dwordx4 v248, s[8:11], s40 offen offset:3072 sc1 lds\n"
    "v_mfma_f32_16x16x32_bf16 v[224:227], a[180:183], a[112:115], v[224:227]\n"
    "s_add_u32 m0, s43, 53248\n"
    "v_mfma_f32_16x16x32_bf16 v[224:227], a[176:179], a[108:111], v[224:227]\n"
    "s_add_u32 s40, s40, 0x8000\n"
    "ds_read2st64_b32 v[182:183], v250 offset0:34 offset1:35\n"
    "v_mfma_f32_16x16x32_bf16 v[232:235], a[176:179], a[140:143], v[232:235]\n"
    "ds_read_b128 a[176:179], v241 offset:22528\n"
    "v_mfma_f32_16x16x32_bf16 v[232:235], a[180:183], a[144:147], v[232:235]\n"
    "ds_read_b128 a[180:183], v242 offset:18432\n"
    "v_mfma_f32_16x16x32_bf16 v[232:235], a[184:187], a[148:151], v[232:235]\n"
    "buffer_load_dwordx4 v248, s[8:11], s41 offen offset:0 sc1 lds\n"
    "v_mfma_f32_16x16x32_bf16 v[232:235], a[188:191], a[152:155], v[232:235]\n"
    "v_mfma_f32_16x16x32_bf16 v[224:227], a[188:191], a[120:123], v[224:227]\n"
    "ds_read2st64_b32 v[188:189], v250 offset0:48 offset1:49\n"
    "v_mfma_f32_16x16x32_bf16 v[224:227], a[184:187], a[116:119], v[224:227]\n"
    "v_mfma_f32_16x16x32_bf16 v[216:219], a[184:187], a[84:87], v[216:219]\n"
    "v_exp_f32_e32 v232, v232\n"
    "v_mfma_f32_16x16x32_bf16 v[216:219], a[188:191], a[88:91], v[216:219]\n"
    "buffer_load_dwordx4 v248, s[8:11], s41 offen offset:1024 sc1 lds\n"
    "v_mfma_f32_16x16x32_bf16 v[208:211], a[188:191], a[56:59], v[208:211]\n"
    "ds_read2st64_b32 v[190:191], v250 offset0:50 offset1:51\n"
    "v_mfma_f32_16x16x32_bf16 v[208:211], a[184:187], a[52:55], v[208:211]\n"
    "v_exp_f32_e32 v233, v233\n"
    "v_mfma_f32_16x16x32_bf16 v[200:203], a[184:187], a[20:23], v[200:203]\n"
    "ds_read_b128 a[184:187], v243 offset:18432\n"
    "v_mfma_f32_16x16x32_bf16 v[200:203], a[188:191], a[24:27], v[200:203]\n"
    "s_waitcnt lgkmcnt(8)\n"
    "ds_read_b128 a[188:191], v242 offset:22528\n"
    "v_mfma_f32_16x16x32_bf16 v[204:207], a[160:163], a[28:31], 0\n"
    "buffer_load_dwordx4 v248, s[8:11], s41 offen offset:2048 sc1 lds\n"
    "v_mfma_f32_16x16x32_bf16 v[204:207], a[164:167], a[0:3], v[204:207]\n"
    "ds_read2st64_b32 v[196:197], v250 offset0:64 offset1:65\n"
    "v_mfma_f32_16x16x32_bf16 v[212:215], a[164:167], a[32:35], 0\n"
    "v_exp_f32_e32 v234, v234\n"
    "v_mfma_f32_16x16x32_bf16 v[212:215], a[160:163], a[60:63], v[212:215]\n"
    "ds_read2st64_b32 v[198:199], v250 offset0:66 offset1:67\n"
    "v_mfma_f32_16x16x32_bf16 v[220:223], a[160:163], a[92:95], 0\n"
    "v_exp_f32_e32 v235, v235\n"
    "v_mfma_f32_16x16x32_bf16 v[220:223], a[164:167], a[64:67], v[220:223]\n"
    "buffer_load_dwordx4 v248, s[8:11], s41 offen offset:3072 sc1 lds\n"
    "v_mfma_f32_16x16x32_bf16 v[228:231], a[164:167], a[96:99], 0\n"
    "s_add_u32 s41, s41, 0x8000\n"
    "v_cvt_pk_bf16_f32 v232, v232, v233\n"
    "v_mfma_f32_16x16x32_bf16 v[228:231], a[160:163], a[124:127], v[228:231]\n"
    "v_exp_f32_e32 v224, v224\n"
    "v_mfma_f32_16x16x32_bf16 v[236:239], a[160:163], a[156:159], 0\n"
    "ds_read_b64_tr_b16 a[160:161], v244 offset:0\n"
    "v_mfma_f32_16x16x32_bf16 v[236:239], a[164:167], a[128:131], v[236:239]\n"
    "ds_read_b64_tr_b16 a[162:163], v244 offset:2048\n"
    "v_mfma_f32_16x16x32_bf16 v[236:239], a[168:171], a[132:135], v[236:239]\n"
    "ds_read_b64_tr_b16 a[164:165], v244 offset:8192\n"
    "v_mfma_f32_16x16x32_bf16 v[236:239], a[172:175], a[136:139], v[236:239]\n"
    "ds_read_b64_tr_b16 a[166:167], v244 offset:10240\n"
    "v_mfma_f32_16x16x32_bf16 v[228:231], a[172:175], a[104:107], v[228:231]\n"
    "v_exp_f32_e32 v225, v225\n"
    "v_mfma_f32_16x16x32_bf16 v[228:231], a[168:171], a[100:103], v[228:231]\n"
    "v_cvt_pk_bf16_f32 v233, v234, v235\n"
    "v_mfma_f32_16x16x32_bf16 v[220:223], a[168:171], a[68:71], v[220:223]\n"
    "v_exp_f32_e32 v226, v226\n"
    "v_mfma_f32_16x16x32_bf16 v[220:223], a[172:175], a[72:75], v[220:223]\n"
    "v_exp_f32_e32 v227, v227\n"
    "v_mfma_f32_16x16x32_bf16 v[212:215], a[172:175], a[40:43], v[212:215]\n"
    "v_cvt_pk_bf16_f32 v224, v224, v225\n"
    "v_mfma_f32_16x16x32_bf16 v[212:215], a[168:171], a[36:39], v[212:215]\n"
    "v_exp_f32_e32 v216, v216\n"
    "v_mfma_f32_16x16x32_bf16 v[204:207], a[168:171], a[4:7], v[204:207]\n"
    "ds_read_b64_tr_b16 a[168:169], v245 offset:0\n"
    "v_mfma_f32_16x16x32_bf16 v[204:207], a[172:175], a[8:11], v[204:207]\n"
    "s_waitcnt lgkmcnt(7)\n"
    "ds_read_b64_tr_b16 a[170:171], v245 offset:2048\n"
    "v_mfma_f32_16x16x32_bf16 v[204:207], a[176:179], a[12:15], v[204:207]\n"
    "ds_read_b64_tr_b16 a[172:173], v245 offset:8192\n"
    "v_mfma_f32_16x16x32_bf16 v[204:207], a[180:183], a[16:19], v[204:207]\n"
    "ds_read_b64_tr_b16 a[174:175], v245 offset:10240\n"
    "v_mfma_f32_16x16x32_bf16 v[212:215], a[180:183], a[48:51], v[212:215]\n"
    "v_exp_f32_e32 v217, v217\n"
    "v_mfma_f32_16x16x32_bf16 v[212:215], a[176:179], a[44:47], v[212:215]\n"
    "v_cvt_pk_bf16_f32 v225, v226, v227\n"
    "v_mfma_f32_16x16x32_bf16 v[220:223], a[176:179], a[76:79], v[220:223]\n"
    "v_exp_f32_e32 v218, v218\n"
    "v_mfma_f32_16x16x32_bf16 v[220:223], a[180:183], a[80:83], v[220:223]\n"
    "v_exp_f32_e32 v219, v219\n"
    "v_mfma_f32_16x16x32_bf16 v[228:231], a[180:183], a[112:115], v[228:231]\n"
    "v_cvt_pk_bf16_f32 v216, v216, v217\n"
    "v_mfma_f32_16x16x32_bf16 v[228:231], a[176:179], a[108:111], v[228:231]\n"
    "v_exp_f32_e32 v208, v208\n"
    "v_mfma_f32_16x16x32_bf16 v[236:239], a[176:179], a[140:143], v[236:239]\n"
    "ds_read_b64_tr_b16 a[176:177], v246 offset:0\n"
    "v_mfma_f32_16x16x32_bf16 v[236:239], a[180:183], a[144:147], v[236:239]\n"
    "ds_read_b64_tr_b16 a[178:179], v246 offset:2048\n"
    "v_mfma_f32_16x16x32_bf16 v[236:239], a[184:187], a[148:151], v[236:239]\n"
    "ds_read_b64_tr_b16 a[180:181], v246 offset:8192\n"
    "v_mfma_f32_16x16x32_bf16 v[236:239], a[188:191], a[152:155], v[236:239]\n"
    "ds_read_b64_tr_b16 a[182:183], v246 offset:10240\n"
    "v_mfma_f32_16x16x32_bf16 v[228:231], a[188:191], a[120:123], v[228:231]\n"
    "v_exp_f32_e32 v209, v209\n"
    "v_mfma_f32_16x16x32_bf16 v[228:231], a[184:187], a[116:119], v[228:231]\n"
    "v_cvt_pk_bf16_f32 v217, v218, v219\n"
    "v_mfma_f32_16x16x32_bf16 v[220:223], a[184:187], a[84:87], v[220:223]\n"
    "v_exp_f32_e32 v210, v210\n"
    "v_mfma_f32_16x16x32_bf16 v[220:223], a[188:191], a[88:91], v[220:223]\n"
    "v_exp_f32_e32 v211, v211\n"
    "v_mfma_f32_16x16x32_bf16 v[212:215], a[188:191], a[56:59], v[212:215]\n"
    "v_cvt_pk_bf16_f32 v208, v208, v209\n"
    "v_mfma_f32_16x16x32_bf16 v[212:215], a[184:187], a[52:55], v[212:215]\n"
    "v_exp_f32_e32 v200, v200\n"
    "v_mfma_f32_16x16x32_bf16 v[204:207], a[184:187], a[20:23], v[204:207]\n"
    "ds_read_b64_tr_b16 a[184:185], v247 offset:0\n"
    "v_mfma_f32_16x16x32_bf16 v[204:207], a[188:191], a[24:27], v[204:207]\n"
    "s_waitcnt lgkmcnt(5)\n"
    "ds_read_b64_tr_b16 a[186:187], v247 offset:2048\n"
    "v_mfma_f32_16x16x32_bf16 v[0:3], a[160:163], v[160:163], v[0:3]\n"
    "ds_read_b64_tr_b16 a[188:189], v247 offset:8192\n"
    "v_mfma_f32_16x16x32_bf16 v[0:3], a[164:167], v[164:167], v[0:3]\n"
    "ds_read_b64_tr_b16 a[190:191], v247 offset:10240\n"
    "v_mfma_f32_16x16x32_bf16 a[248:251], a[164:167], v[172:175], a[248:251]\n"
    "v_exp_f32_e32 v201, v201\n"
    "v_mfma_f32_16x16x32_bf16 a[248:251], a[160:163], v[168:171], a[248:251]\n"
    "v_cvt_pk_bf16_f32 v209, v210, v211\n"
    "v_mfma_f32_16x16x32_bf16 v[56:59], a[160:163], v[176:179], v[56:59]\n"
    "v_exp_f32_e32 v202, v202\n"
    "v_mfma_f32_16x16x32_bf16 v[56:59], a[164:167], v[180:183], v[56:59]\n"
    "v_exp_f32_e32 v203, v203\n"
    "v_mfma_f32_16x16x32_bf16 v[60:63], a[164:167], v[188:191], v[60:63]\n"
    "v_cvt_pk_bf16_f32 v200, v200, v201\n"
    "v_mfma_f32_16x16x32_bf16 v[60:63], a[160:163], v[184:187], v[60:63]\n"
    "v_cvt_pk_bf16_f32 v201, v202, v203\n"
    "v_mfma_f32_16x16x32_bf16 v[64:67], a[160:163], v[192:195], v[64:67]\n"
    "ds_read_b64_tr_b16 a[160:161], v244 offset:4096\n"
    "v_mfma_f32_16x16x32_bf16 v[64:67], a[164:167], v[196:199], v[64:67]\n"
    "ds_read_b64_tr_b16 a[162:163], v244 offset:6144\n"
    "v_mfma_f32_16x16x32_bf16 v[76:79], a[168:171], v[192:195], v[76:79]\n"
    "ds_read_b64_tr_b16 a[164:165], v244 offset:12288\n"
    "v_mfma_f32_16x16x32_bf16 v[76:79], a[172:175], v[196:199], v[76:79]\n"
    "ds_read_b64_tr_b16 a[166:167], v244 offset:14336\n"
    "v_mfma_f32_16x16x32_bf16 v[72:75], a[172:175], v[188:191], v[72:75]\n"
    "v_exp_f32_e32 v236, v236\n"
    "v_mfma_f32_16x16x32_bf16 v[72:75], a[168:171], v[184:187], v[72:75]\n"
    "v_exp_f32_e32 v237, v237\n"
    "v_mfma_f32_16x16x32_bf16 v[68:71], a[168:171], v[176:179], v[68:71]\n"
    "v_exp_f32_e32 v238, v238\n"
    "v_mfma_f32_16x16x32_bf16 v[68:71], a[172:175], v[180:183], v[68:71]\n"
    "v_exp_f32_e32 v239, v239\n"
    "v_mfma_f32_16x16x32_bf16 a[252:255], a[172:175], v[172:175], a[252:255]\n"
    "v_cvt_pk_bf16_f32 v234, v236, v237\n"
    "v_mfma_f32_16x16x32_bf16 a[252:255], a[168:171], v[168:171], a[252:255]\n"
    "v_exp_f32_e32 v228, v228\n"
    "v_mfma_f32_16x16x32_bf16 v[4:7], a[168:171], v[160:163], v[4:7]\n"
    "ds_read_b64_tr_b16 a[168:169], v245 offset:4096\n"
    "v_mfma_f32_16x16x32_bf16 v[4:7], a[172:175], v[164:167], v[4:7]\n"
    "s_waitcnt lgkmcnt(5)\n"
    "ds_read_b64_tr_b16 a[170:171], v245 offset:6144\n"
    "v_mfma_f32_16x16x32_bf16 v[8:11], a[176:179], v[160:163], v[8:11]\n"
    "ds_read_b64_tr_b16 a[172:173], v245 offset:12288\n"
    "v_mfma_f32_16x16x32_bf16 v[8:11], a[180:183], v[164:167], v[8:11]\n"
    "ds_read_b64_tr_b16 a[174:175], v245 offset:14336\n"
    "v_mfma_f32_16x16x32_bf16 v[32:35], a[180:183], v[172:175], v[32:35]\n"
    "v_exp_f32_e32 v229, v229\n"
    "v_mfma_f32_16x16x32_bf16 v[32:35], a[176:179], v[168:171], v[32:35]\n"
    "v_cvt_pk_bf16_f32 v235, v238, v239\n"
    "v_mfma_f32_16x16x32_bf16 v[80:83], a[176:179], v[176:179], v[80:83]\n"
    "v_exp_f32_e32 v230, v230\n"
    "v_mfma_f32_16x16x32_bf16 v[80:83], a[180:183], v[180:183], v[80:83]\n"
    "v_exp_f32_e32 v231, v231\n"
    "v_mfma_f32_16x16x32_bf16 v[84:87], a[180:183], v[188:191], v[84:87]\n"
    "v_cvt_pk_bf16_f32 v226, v228, v229\n"
    "v_mfma_f32_16x16x32_bf16 v[84:87], a[176:179], v[184:187], v[84:87]\n"
    "v_exp_f32_e32 v220, v220\n"
    "v_mfma_f32_16x16x32_bf16 v[88:91], a[176:179], v[192:195], v[88:91]\n"
    "ds_read_b64_tr_b16 a[176:177], v246 offset:4096\n"
    "v_mfma_f32_16x16x32_bf16 v[88:91], a[180:183], v[196:199], v[88:91]\n"
    "ds_read_b64_tr_b16 a[178:179], v246 offset:6144\n"
    "v_mfma_f32_16x16x32_bf16 v[100:103], a[184:187], v[192:195], v[100:103]\n"
    "ds_read_b64_tr_b16 a[180:181], v246 offset:12288\n"
    "v_mfma_f32_16x16x32_bf16 v[100:103], a[188:191], v[196:199], v[100:103]\n"
    "ds_read_b64_tr_b16 a[182:183], v246 offset:14336\n"
    "v_mfma_f32_16x16x32_bf16 v[96:99], a[188:191], v[188:191], v[96:99]\n"
    "v_exp_f32_e32 v221, v221\n"
    "v_mfma_f32_16x16x32_bf16 v[96:99], a[184:187], v[184:187], v[96:99]\n"
    "v_cvt_pk_bf16_f32 v227, v230, v231\n"
    "v_mfma_f32_16x16x32_bf16 v[92:95], a[184:187], v[176:179], v[92:95]\n"
    "v_exp_f32_e32 v222, v222\n"
    "v_mfma_f32_16x16x32_bf16 v[92:95], a[188:191], v[180:183], v[92:95]\n"
    "v_exp_f32_e32 v223, v223\n"
    "v_mfma_f32_16x16x32_bf16 v[36:39], a[188:191], v[172:175], v[36:39]\n"
    "v_cvt_pk_bf16_f32 v218, v220, v221\n"
    "v_mfma_f32_16x16x32_bf16 v[36:39], a[184:187], v[168:171], v[36:39]\n"
    "v_exp_f32_e32 v212, v212\n"
    "v_mfma_f32_16x16x32_bf16 v[12:15], a[184:187], v[160:163], v[12:15]\n"
    "s_mov_b32 m0, s49\n"
    "ds_read_b64_tr_b16 a[184:185], v247 offset:4096\n"
    "v_mfma_f32_16x16x32_bf16 v[12:15], a[188:191], v[164:167], v[12:15]\n"
    "s_waitcnt lgkmcnt(5)\n"
    "ds_read_b64_tr_b16 a[186:187], v247 offset:6144\n"
    "v_mfma_f32_16x16x32_bf16 v[16:19], a[160:163], v[160:163], v[16:19]\n"
    "ds_read_b64_tr_b16 a[188:189], v247 offset:12288\n"
    "v_mfma_f32_16x16x32_bf16 v[16:19], a[164:167], v[164:167], v[16:19]\n"
    "ds_read_b64_tr_b16 a[190:191], v247 offset:14336\n"
    "v_mfma_f32_16x16x32_bf16 v[40:43], a[164:167], v[172:175], v[40:43]\n"
    "v_exp_f32_e32 v213, v213\n"
    "v_mfma_f32_16x16x32_bf16 v[40:43], a[160:163], v[168:171], v[40:43]\n"
    "v_cvt_pk_bf16_f32 v219, v222, v223\n"
    "v_mfma_f32_16x16x32_bf16 v[104:107], a[160:163], v[176:179], v[104:107]\n"
    "v_exp_f32_e32 v214, v214\n"
    "v_mfma_f32_16x16x32_bf16 v[104:107], a[164:167], v[180:183], v[104:107]\n"
    "s_waitcnt vmcnt(8)\n"
    "v_mfma_f32_16x16x32_bf16 v[108:111], a[164:167], v[188:191], v[108:111]\n"
    "s_barrier\n"
    "v_mfma_f32_16x16x32_bf16 v[108:111], a[160:163], v[184:187], v[108:111]\n"
    "v_exp_f32_e32 v215, v215\n"
    "v_mfma_f32_16x16x32_bf16 v[112:115], a[160:163], v[192:195], v[112:115]\n"
    "ds_read_b128 a[160:163], v243 offset:36864\n"
    "v_mfma_f32_16x16x32_bf16 v[112:115], a[164:167], v[196:199], v[112:115]\n"
    "ds_read_b128 a[164:167], v240 offset:32768\n"
    "v_mfma_f32_16x16x32_bf16 v[124:127], a[168:171], v[192:195], v[124:127]\n"
    "ds_write_addtid_b32 v232 offset:16384\n"
    "v_cvt_pk_bf16_f32 v210, v212, v213\n"
    "v_mfma_f32_16x16x32_bf16 v[124:127], a[172:175], v[196:199], v[124:127]\n"
    "v_exp_f32_e32 v204, v204\n"
    "v_mfma_f32_16x16x32_bf16 v[120:123], a[172:175], v[188:191], v[120:123]\n"
    "v_exp_f32_e32 v205, v205\n"
    "v_mfma_f32_16x16x32_bf16 v[120:123], a[168:171], v[184:187], v[120:123]\n"
    "ds_write_addtid_b32 v233 offset:16640\n"
    "v_cvt_pk_bf16_f32 v211, v214, v215\n"
    "v_mfma_f32_16x16x32_bf16 v[116:119], a[168:171], v[176:179], v[116:119]\n"
    "v_exp_f32_e32 v206, v206\n"
    "v_mfma_f32_16x16x32_bf16 v[116:119], a[172:175], v[180:183], v[116:119]\n"
    "v_exp_f32_e32 v207, v207\n"
    "v_mfma_f32_16x16x32_bf16 v[44:47], a[172:175], v[172:175], v[44:47]\n"
    "ds_write_addtid_b32 v224 offset:12288\n"
    "v_cvt_pk_bf16_f32 v202, v204, v205\n"
    "v_mfma_f32_16x16x32_bf16 v[44:47], a[168:171], v[168:171], v[44:47]\n"
    "ds_write_addtid_b32 v225 offset:12544\n"
    "v_cvt_pk_bf16_f32 v203, v206, v207\n"
    "v_mfma_f32_16x16x32_bf16 v[20:23], a[168:171], v[160:163], v[20:23]\n"
    "ds_read_b128 a[168:171], v241 offset:32768\n"
    "v_mfma_f32_16x16x32_bf16 v[20:23], a[172:175], v[164:167], v[20:23]\n"
    "s_waitcnt lgkmcnt(7)\n"
    "ds_read_b128 a[172:175], v240 offset:36864\n"
    "v_mfma_f32_16x16x32_bf16 v[24:27], a[176:179], v[160:163], v[24:27]\n"
    "ds_write_addtid_b32 v216 offset:8192\n"
    "v_mfma_f32_16x16x32_bf16 v[24:27], a[180:183], v[164:167], v[24:27]\n"
    "ds_write_addtid_b32 v217 offset:8448\n"
    "v_mfma_f32_16x16x32_bf16 v[48:51], a[180:183], v[172:175], v[48:51]\n"
    "ds_write_addtid_b32 v208 offset:4096\n"
    "v_mfma_f32_16x16x32_bf16 v[48:51], a[176:179], v[168:171], v[48:51]\n"
    "ds_write_addtid_b32 v209 offset:4352\n"
    "v_mfma_f32_16x16x32_bf16 v[128:131], a[176:179], v[176:179], v[128:131]\n"
    "ds_write_addtid_b32 v200 offset:0\n"
    "v_mfma_f32_16x16x32_bf16 v[128:131], a[180:183], v[180:183], v[128:131]\n"
    "ds_write_addtid_b32 v201 offset:256\n"
    "v_mfma_f32_16x16x32_bf16 v[132:135], a[180:183], v[188:191], v[132:135]\n"
    "ds_write_addtid_b32 v234 offset:16896\n"
    "v_mfma_f32_16x16x32_bf16 v[132:135], a[176:179], v[184:187], v[132:135]\n"
    "ds_write_addtid_b32 v235 offset:17152\n"
    "v_mfma_f32_16x16x32_bf16 v[136:139], a[176:179], v[192:195], v[136:139]\n"
    "ds_read_b128 a[176:179], v241 offset:36864\n"
    "v_mfma_f32_16x16x32_bf16 v[136:139], a[180:183], v[196:199], v[136:139]\n"
    "ds_read_b128 a[180:183], v242 offset:32768\n"
    "v_mfma_f32_16x16x32_bf16 v[148:151], a[184:187], v[192:195], v[148:151]\n"
    "ds_write_addtid_b32 v226 offset:12800\n"
    "v_mfma_f32_16x16x32_bf16 v[148:151], a[188:191], v[196:199], v[148:151]\n"
    "ds_write_addtid_b32 v227 offset:13056\n"
    "v_mfma_f32_16x16x32_bf16 v[144:147], a[188:191], v[188:191], v[144:147]\n"
    "ds_write_addtid_b32 v218 offset:8704\n"
    "v_mfma_f32_16x16x32_bf16 v[144:147], a[184:187], v[184:187], v[144:147]\n"
    "ds_write_addtid_b32 v219 offset:8960\n"
    "v_mfma_f32_16x16x32_bf16 v[140:143], a[184:187], v[176:179], v[140:143]\n"
    "ds_write_addtid_b32 v210 offset:4608\n"
    "v_mfma_f32_16x16x32_bf16 v[140:143], a[188:191], v[180:183], v[140:143]\n"
    "ds_write_addtid_b32 v211 offset:4864\n"
    "v_mfma_f32_16x16x32_bf16 v[52:55], a[188:191], v[172:175], v[52:55]\n"
    "ds_write_addtid_b32 v202 offset:512\n"
    "v_mfma_f32_16x16x32_bf16 v[52:55], a[184:187], v[168:171], v[52:55]\n"
    "ds_write_addtid_b32 v203 offset:768\n"
    "v_mfma_f32_16x16x32_bf16 v[28:31], a[184:187], v[160:163], v[28:31]\n"
    "ds_read_b128 a[184:187], v243 offset:32768\n"
    "v_mfma_f32_16x16x32_bf16 v[28:31], a[188:191], v[164:167], v[28:31]\n"
    "s_waitcnt lgkmcnt(15)\n"
    "ds_read_b128 a[188:191], v242 offset:36864\n"
    "v_mfma_f32_16x16x32_bf16 v[160:163], a[160:163], a[28:31], 0\n"
    "v_mfma_f32_16x16x32_bf16 v[160:163], a[164:167], a[0:3], v[160:163]\n"
    "v_mfma_f32_16x16x32_bf16 v[168:171], a[164:167], a[32:35], 0\n"
    "v_mfma_f32_16x16x32_bf16 v[168:171], a[160:163], a[60:63], v[168:171]\n"
    "v_mfma_f32_16x16x32_bf16 v[176:179], a[160:163], a[92:95], 0\n"
    "v_mfma_f32_16x16x32_bf16 v[176:179], a[164:167], a[64:67], v[176:179]\n"
    "s_waitcnt lgkmcnt(0)\n"
    "v_mfma_f32_16x16x32_bf16 v[184:187], a[164:167], a[96:99], 0\n"
    "s_barrier\n"
    "v_mfma_f32_16x16x32_bf16 v[184:187], a[160:163], a[124:127], v[184:187]\n"
    "s_add_u32 m0, s43, 0\n"
    "ds_read2st64_b32 v[204:205], v250 offset0:0 offset1:1\n"
    "v_mfma_f32_16x16x32_bf16 v[192:195], a[160:163], a[156:159], 0\n"
    "ds_read_b128 a[160:163], v243 offset:38912\n"
    "v_mfma_f32_16x16x32_bf16 v[192:195], a[164:167], a[128:131], v[192:195]\n"
    "ds_read_b128 a[164:167], v240 offset:34816\n"
    "v_mfma_f32_16x16x32_bf16 v[192:195], a[168:171], a[132:135], v[192:195]\n"
    "buffer_load_dwordx4 v248, s[8:11], s40 offen offset:0 sc1 lds\n"
    "v_mfma_f32_16x16x32_bf16 v[192:195], a[172:175], a[136:139], v[192:195]\n"
    "v_mfma_f32_16x16x32_bf16 v[184:187], a[172:175], a[104:107], v[184:187]\n"
    "v_mfma_f32_16x16x32_bf16 v[184:187], a[168:171], a[100:103], v[184:187]\n"
    "ds_read2st64_b32 v[206:207], v250 offset0:2 offset1:3\n"
    "v_mfma_f32_16x16x32_bf16 v[176:179], a[168:171], a[68:71], v[176:179]\n"
    "v_mfma_f32_16x16x32_bf16 v[176:179], a[172:175], a[72:75], v[176:179]\n"
    "buffer_load_dwordx4 v248, s[8:11], s40 offen offset:1024 sc1 lds\n"
    "v_mfma_f32_16x16x32_bf16 v[168:171], a[172:175], a[40:43], v[168:171]\n"
    "v_mfma_f32_16x16x32_bf16 v[168:171], a[168:171], a[36:39], v[168:171]\n"
    "ds_read2st64_b32 v[212:213], v250 offset0:16 offset1:17\n"
    "v_mfma_f32_16x16x32_bf16 v[160:163], a[168:171], a[4:7], v[160:163]\n"
    "ds_read_b128 a[168:171], v241 offset:34816\n"
    "v_mfma_f32_16x16x32_bf16 v[160:163], a[172:175], a[8:11], v[160:163]\n"
    "ds_read_b128 a[172:175], v240 offset:38912\n"
    "v_mfma_f32_16x16x32_bf16 v[160:163], a[176:179], a[12:15], v[160:163]\n"
    "buffer_load_dwordx4 v248, s[8:11], s40 offen offset:2048 sc1 lds\n"
    "v_mfma_f32_16x16x32_bf16 v[160:163], a[180:183], a[16:19], v[160:163]\n"
    "ds_read2st64_b32 v[214:215], v250 offset0:18 offset1:19\n"
    "v_mfma_f32_16x16x32_bf16 v[168:171], a[180:183], a[48:51], v[168:171]\n"
    "v_mfma_f32_16x16x32_bf16 v[168:171], a[176:179], a[44:47], v[168:171]\n"
    "v_mfma_f32_16x16x32_bf16 v[176:179], a[176:179], a[76:79], v[176:179]\n"
    "ds_read2st64_b32 v[220:221], v250 offset0:32 offset1:33\n"
    "v_mfma_f32_16x16x32_bf16 v[176:179], a[180:183], a[80:83], v[176:179]\n"
    "buffer_load_dwordx4 v248, s[8:11], s40 offen offset:3072 sc1 lds\n"
    "v_mfma_f32_16x16x32_bf16 v[184:187], a[180:183], a[112:115], v[184:187]\n"
    "s_add_u32 m0, s43, 4096\n"
    "v_mfma_f32_16x16x32_bf16 v[184:187], a[176:179], a[108:111], v[184:187]\n"
    "s_add_u32 s40, s40, 0x8000\n"
    "ds_read2st64_b32 v[222:223], v250 offset0:34 offset1:35\n"
    "v_mfma_f32_16x16x32_bf16 v[192:195], a[176:179], a[140:143], v[192:195]\n"
    "ds_read_b128 a[176:179], v241 offset:38912\n"
    "v_mfma_f32_16x16x32_bf16 v[192:195], a[180:183], a[144:147], v[192:195]\n"
    "ds_read_b128 a[180:183], v242 offset:34816\n"
    "v_mfma_f32_16x16x32_bf16 v[192:195], a[184:187], a[148:151], v[192:195]\n"
    "buffer_load_dwordx4 v248, s[8:11], s41 offen offset:0 sc1 lds\n"
    "v_mfma_f32_16x16x32_bf16 v[192:195], a[188:191], a[152:155], v[192:195]\n"
    "v_mfma_f32_16x16x32_bf16 v[184:187], a[188:191], a[120:123], v[184:187]\n"
    "ds_read2st64_b32 v[228:229], v250 offset0:48 offset1:49\n"
    "v_mfma_f32_16x16x32_bf16 v[184:187], a[184:187], a[116:119], v[184:187]\n"
    "v_mfma_f32_16x16x32_bf16 v[176:179], a[184:187], a[84:87], v[176:179]\n"
    "v_exp_f32_e32 v192, v192\n"
    "v_mfma_f32_16x16x32_bf16 v[176:179], a[188:191], a[88:91], v[176:179]\n"
    "buffer_load_dwordx4 v248, s[8:11], s41 offen offset:1024 sc1 lds\n"
    "v_mfma_f32_16x16x32_bf16 v[168:171], a[188:191], a[56:59], v[168:171]\n"
    "ds_read2st64_b32 v[230:231], v250 offset0:50 offset1:51\n"
    "v_mfma_f32_16x16x32_bf16 v[168:171], a[184:187], a[52:55], v[168:171]\n"
    "v_exp_f32_e32 v193, v193\n"
    "v_mfma_f32_16x16x32_bf16 v[160:163], a[184:187], a[20:23], v[160:163]\n"
    "ds_read_b128 a[184:187], v243 offset:34816\n"
    "v_mfma_f32_16x16x32_bf16 v[160:163], a[188:191], a[24:27], v[160:163]\n"
    "s_waitcnt lgkmcnt(8)\n"
    "ds_read_b128 a[188:191], v242 offset:38912\n"
    "v_mfma_f32_16x16x32_bf16 v[164:167], a[160:163], a[28:31], 0\n"
    "buffer_load_dwordx4 v248, s[8:11], s41 offen offset:2048 sc1 lds\n"
    "v_mfma_f32_16x16x32_bf16 v[164:167], a[164:167], a[0:3], v[164:167]\n"
    "ds_read2st64_b32 v[236:237], v250 offset0:64 offset1:65\n"
    "v_mfma_f32_16x16x32_bf16 v[172:175], a[164:167], a[32:35], 0\n"
    "v_exp_f32_e32 v194, v194\n"
    "v_mfma_f32_16x16x32_bf16 v[172:175], a[160:163], a[60:63], v[172:175]\n"
    "ds_read2st64_b32 v[238:239], v250 offset0:66 offset1:67\n"
    "v_mfma_f32_16x16x32_bf16 v[180:183], a[160:163], a[92:95], 0\n"
    "v_exp_f32_e32 v195, v195\n"
    "v_mfma_f32_16x16x32_bf16 v[180:183], a[164:167], a[64:67], v[180:183]\n"
    "buffer_load_dwordx4 v248, s[8:11], s41 offen offset:3072 sc1 lds\n"
    "v_mfma_f32_16x16x32_bf16 v[188:191], a[164:167], a[96:99], 0\n"
    "s_add_u32 s41, s41, 0x8000\n"
    "v_cvt_pk_bf16_f32 v192, v192, v193\n"
    "v_mfma_f32_16x16x32_bf16 v[188:191], a[160:163], a[124:127], v[188:191]\n"
    "v_exp_f32_e32 v184, v184\n"
    "v_mfma_f32_16x16x32_bf16 v[196:199], a[160:163], a[156:159], 0\n"
    "ds_read_b64_tr_b16 a[160:161], v244 offset:16384\n"
    "v_mfma_f32_16x16x32_bf16 v[196:199], a[164:167], a[128:131], v[196:199]\n"
    "ds_read_b64_tr_b16 a[162:163], v244 offset:18432\n"
    "v_mfma_f32_16x16x32_bf16 v[196:199], a[168:171], a[132:135], v[196:199]\n"
    "ds_read_b64_tr_b16 a[164:165], v244 offset:24576\n"
    "v_mfma_f32_16x16x32_bf16 v[196:199], a[172:175], a[136:139], v[196:199]\n"
    "ds_read_b64_tr_b16 a[166:167], v244 offset:26624\n"
    "v_mfma_f32_16x16x32_bf16 v[188:191], a[172:175], a[104:107], v[188:191]\n"
    "v_exp_f32_e32 v185, v185\n"
    "v_mfma_f32_16x16x32_bf16 v[188:191], a[168:171], a[100:103], v[188:191]\n"
    "v_cvt_pk_bf16_f32 v193, v194, v195\n"
    "v_mfma_f32_16x16x32_bf16 v[180:183], a[168:171], a[68:71], v[180:183]\n"
    "v_exp_f32_e32 v186, v186\n"
    "v_mfma_f32_16x16x32_bf16 v[180:183], a[172:175], a[72:75], v[180:183]\n"
    "v_exp_f32_e32 v187, v187\n"
    "v_mfma_f32_16x16x32_bf16 v[172:175], a[172:175], a[40:43], v[172:175]\n"
    "v_cvt_pk_bf16_f32 v184, v184, v185\n"
    "v_mfma_f32_16x16x32_bf16 v[172:175], a[168:171], a[36:39], v[172:175]\n"
    "v_exp_f32_e32 v176, v176\n"
    "v_mfma_f32_16x16x32_bf16 v[164:167], a[168:171], a[4:7], v[164:167]\n"
    "ds_read_b64_tr_b16 a[168:169], v245 offset:16384\n"
    "v_mfma_f32_16x16x32_bf16 v[164:167], a[172:175], a[8:11], v[164:167]\n"
    "s_waitcnt lgkmcnt(7)\n"
    "ds_read_b64_tr_b16 a[170:171], v245 offset:18432\n"
    "v_mfma_f32_16x16x32_bf16 v[164:167], a[176:179], a[12:15], v[164:167]\n"
    "ds_read_b64_tr_b16 a[172:173], v245 offset:24576\n"
    "v_mfma_f32_16x16x32_bf16 v[164:167], a[180:183], a[16:19], v[164:167]\n"
    "ds_read_b64_tr_b16 a[174:175], v245 offset:26624\n"
    "v_mfma_f32_16x16x32_bf16 v[172:175], a[180:183], a[48:51], v[172:175]\n"
    "v_exp_f32_e32 v177, v177\n"
    "v_mfma_f32_16x16x32_bf16 v[172:175], a[176:179], a[44:47], v[172:175]\n"
    "v_cvt_pk_bf16_f32 v185, v186, v187\n"
    "v_mfma_f32_16x16x32_bf16 v[180:183], a[176:179], a[76:79], v[180:183]\n"
    "v_exp_f32_e32 v178, v178\n"
    "v_mfma_f32_16x16x32_bf16 v[180:183], a[180:183], a[80:83], v[180:183]\n"
    "v_exp_f32_e32 v179, v179\n"
    "v_mfma_f32_16x16x32_bf16 v[188:191], a[180:183], a[112:115], v[188:191]\n"
    "v_cvt_pk_bf16_f32 v176, v176, v177\n"
    "v_mfma_f32_16x16x32_bf16 v[188:191], a[176:179], a[108:111], v[188:191]\n"
    "v_exp_f32_e32 v168, v168\n"
    "v_mfma_f32_16x16x32_bf16 v[196:199], a[176:179], a[140:143], v[196:199]\n"
    "ds_read_b64_tr_b16 a[176:177], v246 offset:16384\n"
    "v_mfma_f32_16x16x32_bf16 v[196:199], a[180:183], a[144:147], v[196:199]\n"
    "ds_read_b64_tr_b16 a[178:179], v246 offset:18432\n"
    "v_mfma_f32_16x16x32_bf16 v[196:199], a[184:187], a[148:151], v[196:199]\n"
    "ds_read_b64_tr_b16 a[180:181], v246 offset:24576\n"
    "v_mfma_f32_16x16x32_bf16 v[196:199], a[188:191], a[152:155], v[196:199]\n"
    "ds_read_b64_tr_b16 a[182:183], v246 offset:26624\n"
    "v_mfma_f32_16x16x32_bf16 v[188:191], a[188:191], a[120:123], v[188:191]\n"
    "v_exp_f32_e32 v169, v169\n"
    "v_mfma_f32_16x16x32_bf16 v[188:191], a[184:187], a[116:119], v[188:191]\n"
    "v_cvt_pk_bf16_f32 v177, v178, v179\n"
    "v_mfma_f32_16x16x32_bf16 v[180:183], a[184:187], a[84:87], v[180:183]\n"
    "v_exp_f32_e32 v170, v170\n"
    "v_mfma_f32_16x16x32_bf16 v[180:183], a[188:191], a[88:91], v[180:183]\n"
    "v_exp_f32_e32 v171, v171\n"
    "v_mfma_f32_16x16x32_bf16 v[172:175], a[188:191], a[56:59], v[172:175]\n"
    "v_cvt_pk_bf16_f32 v168, v168, v169\n"
    "v_mfma_f32_16x16x32_bf16 v[172:175], a[184:187], a[52:55], v[172:175]\n"
    "v_exp_f32_e32 v160, v160\n"
    "v_mfma_f32_16x16x32_bf16 v[164:167], a[184:187], a[20:23], v[164:167]\n"
    "ds_read_b64_tr_b16 a[184:185], v247 offset:16384\n"
    "v_mfma_f32_16x16x32_bf16 v[164:167], a[188:191], a[24:27], v[164:167]\n"
    "s_waitcnt lgkmcnt(5)\n"
    "ds_read_b64_tr_b16 a[186:187], v247 offset:18432\n"
    "v_mfma_f32_16x16x32_bf16 v[0:3], a[160:163], v[200:203], v[0:3]\n"
    "ds_read_b64_tr_b16 a[188:189], v247 offset:24576\n"
    "v_mfma_f32_16x16x32_bf16 v[0:3], a[164:167], v[204:207], v[0:3]\n"
    "ds_read_b64_tr_b16 a[190:191], v247 offset:26624\n"
    "v_mfma_f32_16x16x32_bf16 a[248:251], a[164:167], v[212:215], a[248:251]\n"
    "v_exp_f32_e32 v161, v161\n"
    "v_mfma_f32_16x16x32_bf16 a[248:251], a[160:163], v[208:211], a[248:251]\n"
    "v_cvt_pk_bf16_f32 v169, v170, v171\n"
    "v_mfma_f32_16x16x32_bf16 v[56:59], a[160:163], v[216:219], v[56:59]\n"
    "v_exp_f32_e32 v162, v162\n"
    "v_mfma_f32_16x16x32_bf16 v[56:59], a[164:167], v[220:223], v[56:59]\n"
    "v_exp_f32_e32 v163, v163\n"
    "v_mfma_f32_16x16x32_bf16 v[60:63], a[164:167], v[228:231], v[60:63]\n"
    "v_cvt_pk_bf16_f32 v160, v160, v161\n"
    "v_mfma_f32_16x16x32_bf16 v[60:63], a[160:163], v[224:227], v[60:63]\n"
    "v_cvt_pk_bf16_f32 v161, v162, v163\n"
    "v_mfma_f32_16x16x32_bf16 v[64:67], a[160:163], v[232:235], v[64:67]\n"
    "ds_read_b64_tr_b16 a[160:161], v244 offset:20480\n"
    "v_mfma_f32_16x16x32_bf16 v[64:67], a[164:167], v[236:239], v[64:67]\n"
    "ds_read_b64_tr_b16 a[162:163], v244 offset:22528\n"
    "v_mfma_f32_16x16x32_bf16 v[76:79], a[168:171], v[232:235], v[76:79]\n"
    "ds_read_b64_tr_b16 a[164:165], v244 offset:28672\n"
    "v_mfma_f32_16x16x32_bf16 v[76:79], a[172:175], v[236:239], v[76:79]\n"
    "ds_read_b64_tr_b16 a[166:167], v244 offset:30720\n"
    "v_mfma_f32_16x16x32_bf16 v[72:75], a[172:175], v[228:231], v[72:75]\n"
    "v_exp_f32_e32 v196, v196\n"
    "v_mfma_f32_16x16x32_bf16 v[72:75], a[168:171], v[224:227], v[72:75]\n"
    "v_exp_f32_e32 v197, v197\n"
    "v_mfma_f32_16x16x32_bf16 v[68:71], a[168:171], v[216:219], v[68:71]\n"
    "v_exp_f32_e32 v198, v198\n"
    "v_mfma_f32_16x16x32_bf16 v[68:71], a[172:175], v[220:223], v[68:71]\n"
    "v_exp_f32_e32 v199, v199\n"
    "v_mfma_f32_16x16x32_bf16 a[252:255], a[172:175], v[212:215], a[252:255]\n"
    "v_cvt_pk_bf16_f32 v194, v196, v197\n"
    "v_mfma_f32_16x16x32_bf16 a[252:255], a[168:171], v[208:211], a[252:255]\n"
    "v_exp_f32_e32 v188, v188\n"
    "v_mfma_f32_16x16x32_bf16 v[4:7], a[168:171], v[200:203], v[4:7]\n"
    "ds_read_b64_tr_b16 a[168:169], v245 offset:20480\n"
    "v_mfma_f32_16x16x32_bf16 v[4:7], a[172:175], v[204:207], v[4:7]\n"
    "s_waitcnt lgkmcnt(5)\n"
    "ds_read_b64_tr_b16 a[170:171], v245 offset:22528\n"
    "v_mfma_f32_16x16x32_bf16 v[8:11], a[176:179], v[200:203], v[8:11]\n"
    "ds_read_b64_tr_b16 a[172:173], v245 offset:28672\n"
    "v_mfma_f32_16x16x32_bf16 v[8:11], a[180:183], v[204:207], v[8:11]\n"
    "ds_read_b64_tr_b16 a[174:175], v245 offset:30720\n"
    "v_mfma_f32_16x16x32_bf16 v[32:35], a[180:183], v[212:215], v[32:35]\n"
    "v_exp_f32_e32 v189, v189\n"
    "v_mfma_f32_16x16x32_bf16 v[32:35], a[176:179], v[208:211], v[32:35]\n"
    "v_cvt_pk_bf16_f32 v195, v198, v199\n"
    "v_mfma_f32_16x16x32_bf16 v[80:83], a[176:179], v[216:219], v[80:83]\n"
    "v_exp_f32_e32 v190, v190\n"
    "v_mfma_f32_16x16x32_bf16 v[80:83], a[180:183], v[220:223], v[80:83]\n"
    "v_exp_f32_e32 v191, v191\n"
    "v_mfma_f32_16x16x32_bf16 v[84:87], a[180:183], v[228:231], v[84:87]\n"
    "v_cvt_pk_bf16_f32 v186, v188, v189\n"
    "v_mfma_f32_16x16x32_bf16 v[84:87], a[176:179], v[224:227], v[84:87]\n"
    "v_exp_f32_e32 v180, v180\n"
    "v_mfma_f32_16x16x32_bf16 v[88:91], a[176:179], v[232:235], v[88:91]\n"
    "ds_read_b64_tr_b16 a[176:177], v246 offset:20480\n"
    "v_mfma_f32_16x16x32_bf16 v[88:91], a[180:183], v[236:239], v[88:91]\n"
    "ds_read_b64_tr_b16 a[178:179], v246 offset:22528\n"
    "v_mfma_f32_16x16x32_bf16 v[100:103], a[184:187], v[232:235], v[100:103]\n"
    "ds_read_b64_tr_b16 a[180:181], v246 offset:28672\n"
    "v_mfma_f32_16x16x32_bf16 v[100:103], a[188:191], v[236:239], v[100:103]\n"
    "ds_read_b64_tr_b16 a[182:183], v246 offset:30720\n"
    "v_mfma_f32_16x16x32_bf16 v[96:99], a[188:191], v[228:231], v[96:99]\n"
    "v_exp_f32_e32 v181, v181\n"
    "v_mfma_f32_16x16x32_bf16 v[96:99], a[184:187], v[224:227], v[96:99]\n"
    "v_cvt_pk_bf16_f32 v187, v190, v191\n"
    "v_mfma_f32_16x16x32_bf16 v[92:95], a[184:187], v[216:219], v[92:95]\n"
    "v_exp_f32_e32 v182, v182\n"
    "v_mfma_f32_16x16x32_bf16 v[92:95], a[188:191], v[220:223], v[92:95]\n"
    "v_exp_f32_e32 v183, v183\n"
    "v_mfma_f32_16x16x32_bf16 v[36:39], a[188:191], v[212:215], v[36:39]\n"
    "v_cvt_pk_bf16_f32 v178, v180, v181\n"
    "v_mfma_f32_16x16x32_bf16 v[36:39], a[184:187], v[208:211], v[36:39]\n"
    "v_exp_f32_e32 v172, v172\n"
    "v_mfma_f32_16x16x32_bf16 v[12:15], a[184:187], v[200:203], v[12:15]\n"
    "s_mov_b32 m0, s49\n"
    "ds_read_b64_tr_b16 a[184:185], v247 offset:20480\n"
    "v_mfma_f32_16x16x32_bf16 v[12:15], a[188:191], v[204:207], v[12:15]\n"
    "s_waitcnt lgkmcnt(5)\n"
    "ds_read_b64_tr_b16 a[186:187], v247 offset:22528\n"
    "v_mfma_f32_16x16x32_bf16 v[16:19], a[160:163], v[200:203], v[16:19]\n"
    "ds_read_b64_tr_b16 a[188:189], v247 offset:28672\n"
    "v_mfma_f32_16x16x32_bf16 v[16:19], a[164:167], v[204:207], v[16:19]\n"
    "ds_read_b64_tr_b16 a[190:191], v247 offset:30720\n"
    "v_mfma_f32_16x16x32_bf16 v[40:43], a[164:167], v[212:215], v[40:43]\n"
    "v_exp_f32_e32 v173, v173\n"
    "v_mfma_f32_16x16x32_bf16 v[40:43], a[160:163], v[208:211], v[40:43]\n"
    "v_cvt_pk_bf16_f32 v179, v182, v183\n"
    "v_mfma_f32_16x16x32_bf16 v[104:107], a[160:163], v[216:219], v[104:107]\n"
    "v_exp_f32_e32 v174, v174\n"
    "v_mfma_f32_16x16x32_bf16 v[104:107], a[164:167], v[220:223], v[104:107]\n"
    "s_waitcnt vmcnt(8)\n"
    "v_mfma_f32_16x16x32_bf16 v[108:111], a[164:167], v[228:231], v[108:111]\n"
    "s_barrier\n"
    "v_mfma_f32_16x16x32_bf16 v[108:111], a[160:163], v[224:227], v[108:111]\n"
    "v_exp_f32_e32 v175, v175\n"
    "v_mfma_f32_16x16x32_bf16 v[112:115], a[160:163], v[232:235], v[112:115]\n"
    "ds_read_b128 a[160:163], v243 offset:53248\n"
    "v_mfma_f32_16x16x32_bf16 v[112:115], a[164:167], v[236:239], v[112:115]\n"
    "ds_read_b128 a[164:167], v240 offset:49152\n"
    "v_mfma_f32_16x16x32_bf16 v[124:127], a[168:171], v[232:235], v[124:127]\n"
    "ds_write_addtid_b32 v192 offset:16384\n"
    "v_cvt_pk_bf16_f32 v170, v172, v173\n"
    "v_mfma_f32_16x16x32_bf16 v[124:127], a[172:175], v[236:239], v[124:127]\n"
    "v_exp_f32_e32 v164, v164\n"
    "v_mfma_f32_16x16x32_bf16 v[120:123], a[172:175], v[228:231], v[120:123]\n"
    "v_exp_f32_e32 v165, v165\n"
    "v_mfma_f32_16x16x32_bf16 v[120:123], a[168:171], v[224:227], v[120:123]\n"
    "ds_write_addtid_b32 v193 offset:16640\n"
    "v_cvt_pk_bf16_f32 v171, v174, v175\n"
    "v_mfma_f32_16x16x32_bf16 v[116:119], a[168:171], v[216:219], v[116:119]\n"
    "v_exp_f32_e32 v166, v166\n"
    "v_mfma_f32_16x16x32_bf16 v[116:119], a[172:175], v[220:223], v[116:119]\n"
    "v_exp_f32_e32 v167, v167\n"
    "v_mfma_f32_16x16x32_bf16 v[44:47], a[172:175], v[212:215], v[44:47]\n"
    "ds_write_addtid_b32 v184 offset:12288\n"
    "v_cvt_pk_bf16_f32 v162, v164, v165\n"
    "v_mfma_f32_16x16x32_bf16 v[44:47], a[168:171], v[208:211], v[44:47]\n"
    "ds_write_addtid_b32 v185 offset:12544\n"
    "v_cvt_pk_bf16_f32 v163, v166, v167\n"
    "v_mfma_f32_16x16x32_bf16 v[20:23], a[168:171], v[200:203], v[20:23]\n"
    "ds_read_b128 a[168:171], v241 offset:49152\n"
    "v_mfma_f32_16x16x32_bf16 v[20:23], a[172:175], v[204:207], v[20:23]\n"
    "s_waitcnt lgkmcnt(7)\n"
    "ds_read_b128 a[172:175], v240 offset:53248\n"
    "v_mfma_f32_16x16x32_bf16 v[24:27], a[176:179], v[200:203], v[24:27]\n"
    "ds_write_addtid_b32 v176 offset:8192\n"
    "v_mfma_f32_16x16x32_bf16 v[24:27], a[180:183], v[204:207], v[24:27]\n"
    "ds_write_addtid_b32 v177 offset:8448\n"
    "v_mfma_f32_16x16x32_bf16 v[48:51], a[180:183], v[212:215], v[48:51]\n"
    "ds_write_addtid_b32 v168 offset:4096\n"
    "v_mfma_f32_16x16x32_bf16 v[48:51], a[176:179], v[208:211], v[48:51]\n"
    "ds_write_addtid_b32 v169 offset:4352\n"
    "v_mfma_f32_16x16x32_bf16 v[128:131], a[176:179], v[216:219], v[128:131]\n"
    "ds_write_addtid_b32 v160 offset:0\n"
    "v_mfma_f32_16x16x32_bf16 v[128:131], a[180:183], v[220:223], v[128:131]\n"
    "ds_write_addtid_b32 v161 offset:256\n"
    "v_mfma_f32_16x16x32_bf16 v[132:135], a[180:183], v[228:231], v[132:135]\n"
    "ds_write_addtid_b32 v194 offset:16896\n"
    "v_mfma_f32_16x16x32_bf16 v[132:135], a[176:179], v[224:227], v[132:135]\n"
    "ds_write_addtid_b32 v195 offset:17152\n"
    "v_mfma_f32_16x16x32_bf16 v[136:139], a[176:179], v[232:235], v[136:139]\n"
    "ds_read_b128 a[176:179], v241 offset:53248\n"
    "v_mfma_f32_16x16x32_bf16 v[136:139], a[180:183], v[236:239], v[136:139]\n"
    "ds_read_b128 a[180:183], v242 offset:49152\n"
    "v_mfma_f32_16x16x32_bf16 v[148:151], a[184:187], v[232:235], v[148:151]\n"
    "ds_write_addtid_b32 v186 offset:12800\n"
    "v_mfma_f32_16x16x32_bf16 v[148:151], a[188:191], v[236:239], v[148:151]\n"
    "ds_write_addtid_b32 v187 offset:13056\n"
    "v_mfma_f32_16x16x32_bf16 v[144:147], a[188:191], v[228:231], v[144:147]\n"
    "ds_write_addtid_b32 v178 offset:8704\n"
    "v_mfma_f32_16x16x32_bf16 v[144:147], a[184:187], v[224:227], v[144:147]\n"
    "ds_write_addtid_b32 v179 offset:8960\n"
    "v_mfma_f32_16x16x32_bf16 v[140:143], a[184:187], v[216:219], v[140:143]\n"
    "ds_write_addtid_b32 v170 offset:4608\n"
    "v_mfma_f32_16x16x32_bf16 v[140:143], a[188:191], v[220:223], v[140:143]\n"
    "ds_write_addtid_b32 v171 offset:4864\n"
    "v_mfma_f32_16x16x32_bf16 v[52:55], a[188:191], v[212:215], v[52:55]\n"
    "ds_write_addtid_b32 v162 offset:512\n"
    "v_mfma_f32_16x16x32_bf16 v[52:55], a[184:187], v[208:211], v[52:55]\n"
    "ds_write_addtid_b32 v163 offset:768\n"
    "v_mfma_f32_16x16x32_bf16 v[28:31], a[184:187], v[200:203], v[28:31]\n"
    "ds_read_b128 a[184:187], v243 offset:49152\n"
    "v_mfma_f32_16x16x32_bf16 v[28:31], a[188:191], v[204:207], v[28:31]\n"
    "s_waitcnt lgkmcnt(15)\n"
    "ds_read_b128 a[188:191], v242 offset:53248\n"
    ".Lat_p2e:\n"
    "v_mfma_f32_16x16x32_bf16 v[200:203], a[160:163], a[28:31], 0\n"
    "v_mfma_f32_16x16x32_bf16 v[200:203], a[164:167], a[0:3], v[200:203]\n"
    "v_mfma_f32_16x16x32_bf16 v[208:211], a[164:167], a[32:35], 0\n"
    "v_mfma_f32_16x16x32_bf16 v[208:211], a[160:163], a[60:63], v[208:211]\n"
    "v_mfma_f32_16x16x32_bf16 v[216:219], a[160:163], a[92:95], 0\n"
    "v_mfma_f32_16x16x32_bf16 v[216:219], a[164:167], a[64:67], v[216:219]\n"
    "s_waitcnt lgkmcnt(0)\n"
    "v_mfma_f32_16x16x32_bf16 v[224:227], a[164:167], a[96:99], 0\n"
    "s_barrier\n"
    "v_mfma_f32_16x16x32_bf16 v[224:227], a[160:163], a[124:127], v[224:227]\n"
    "s_add_u32 m0, s43, 16384\n"
    "ds_read2st64_b32 v[164:165], v250 offset0:0 offset1:1\n"
    "v_mfma_f32_16x16x32_bf16 v[232:235], a[160:163], a[156:159], 0\n"
    "ds_read_b128 a[160:163], v243 offset:55296\n"
    "v_mfma_f32_16x16x32_bf16 v[232:235], a[164:167], a[128:131], v[232:235]\n"
    "ds_read_b128 a[164:167], v240 offset:51200\n"
    "v_mfma_f32_16x16x32_bf16 v[232:235], a[168:171], a[132:135], v[232:235]\n"
    "buffer_load_dwordx4 v248, s[8:11], s40 offen offset:0 sc1 lds\n"
    "v_mfma_f32_16x16x32_bf16 v[232:235], a[172:175], a[136:139], v[232:235]\n"
    "v_mfma_f32_16x16x32_bf16 v[224:227], a[172:175], a[104:107], v[224:227]\n"
    "v_mfma_f32_16x16x32_bf16 v[224:227], a[168:171], a[100:103], v[224:227]\n"
    "ds_read2st64_b32 v[166:167], v250 offset0:2 offset1:3\n"
    "v_mfma_f32_16x16x32_bf16 v[216:219], a[168:171], a[68:71], v[216:219]\n"
    "v_mfma_f32_16x16x32_bf16 v[216:219], a[172:175], a[72:75], v[216:219]\n"
    "buffer_load_dwordx4 v248, s[8:11], s40 offen offset:1024 sc1 lds\n"
    "v_mfma_f32_16x16x32_bf16 v[208:211], a[172:175], a[40:43], v[208:211]\n"
    "v_mfma_f32_16x16x32_bf16 v[208:211], a[168:171], a[36:39], v[208:211]\n"
    "ds_read2st64_b32 v[172:173], v250 offset0:16 offset1:17\n"
    "v_mfma_f32_16x16x32_bf16 v[200:203], a[168:171], a[4:7], v[200:203]\n"
    "ds_read_b128 a[168:171], v241 offset:51200\n"
    "v_mfma_f32_16x16x32_bf16 v[200:203], a[172:175], a[8:11], v[200:203]\n"
    "ds_read_b128 a[172:175], v240 offset:55296\n"
    "v_mfma_f32_16x16x32_bf16 v[200:203], a[176:179], a[12:15], v[200:203]\n"
    "buffer_load_dwordx4 v248, s[8:11], s40 offen offset:2048 sc1 lds\n"
    "v_mfma_f32_16x16x32_bf16 v[200:203], a[180:183], a[16:19], v[200:203]\n"
    "ds_read2st64_b32 v[174:175], v250 offset0:18 offset1:19\n"
    "v_mfma_f32_16x16x32_bf16 v[208:211], a[180:183], a[48:51], v[208:211]\n"
    "v_mfma_f32_16x16x32_bf16 v[208:211], a[176:179], a[44:47], v[208:211]\n"
    "v_mfma_f32_16x16x32_bf16 v[216:219], a[176:179], a[76:79], v[216:219]\n"
    "ds_read2st64_b32 v[180:181], v250 offset0:32 offset1:33\n"
    "v_mfma_f32_16x16x32_bf16 v[216:219], a[180:183], a[80:83], v[216:219]\n"
    "buffer_load_dwordx4 v248, s[8:11], s40 offen offset:3072 sc1 lds\n"
    "v_mfma_f32_16x16x32_bf16 v[224:227], a[180:183], a[112:115], v[224:227]\n"
    "s_add_u32 m0, s43, 20480\n"
    "v_mfma_f32_16x16x32_bf16 v[224:227], a[176:179], a[108:111], v[224:227]\n"
    "s_add_u32 s40, s40, 0x8000\n"
    "ds_read2st64_b32 v[182:183], v250 offset0:34 offset1:35\n"
    "v_mfma_f32_16x16x32_bf16 v[232:235], a[176:179], a[140:143], v[232:235]\n"
    "ds_read_b128 a[176:179], v241 offset:55296\n"
    "v_mfma_f32_16x16x32_bf16 v[232:235], a[180:183], a[144:147], v[232:235]\n"
    "ds_read_b128 a[180:183], v242 offset:51200\n"
    "v_mfma_f32_16x16x32_bf16 v[232:235], a[184:187], a[148:151], v[232:235]\n"
    "buffer_load_dwordx4 v248, s[8:11], s41 offen offset:0 sc1 lds\n"
    "v_mfma_f32_16x16x32_bf16 v[232:235], a[188:191], a[152:155], v[232:235]\n"
    "v_mfma_f32_16x16x32_bf16 v[224:227], a[188:191], a[120:123], v[224:227]\n"
    "ds_read2st64_b32 v[188:189], v250 offset0:48 offset1:49\n"
    "v_mfma_f32_16x16x32_bf16 v[224:227], a[184:187], a[116:119], v[224:227]\n"
    "v_mfma_f32_16x16x32_bf16 v[216:219], a[184:187], a[84:87], v[216:219]\n"
    "v_exp_f32_e32 v232, v232\n"
    "v_mfma_f32_16x16x32_bf16 v[216:219], a[188:191], a[88:91], v[216:219]\n"
    "buffer_load_dwordx4 v248, s[8:11], s41 offen offset:1024 sc1 lds\n"
    "v_mfma_f32_16x16x32_bf16 v[208:211], a[188:191], a[56:59], v[208:211]\n"
    "ds_read2st64_b32 v[190:191], v250 offset0:50 offset1:51\n"
    "v_mfma_f32_16x16x32_bf16 v[208:211], a[184:187], a[52:55], v[208:211]\n"
    "v_exp_f32_e32 v233, v233\n"
    "v_mfma_f32_16x16x32_bf16 v[200:203], a[184:187], a[20:23], v[200:203]\n"
    "ds_read_b128 a[184:187], v243 offset:51200\n"
    "v_mfma_f32_16x16x32_bf16 v[200:203], a[188:191], a[24:27], v[200:203]\n"
    "s_waitcnt lgkmcnt(8)\n"
    "ds_read_b128 a[188:191], v242 offset:55296\n"
    "v_mfma_f32_16x16x32_bf16 v[204:207], a[160:163], a[28:31], 0\n"
    "buffer_load_dwordx4 v248, s[8:11], s41 offen offset:2048 sc1 lds\n"
    "v_mfma_f32_16x16x32_bf16 v[204:207], a[164:167], a[0:3], v[204:207]\n"
    "ds_read2st64_b32 v[196:197], v250 offset0:64 offset1:65\n"
    "v_mfma_f32_16x16x32_bf16 v[212:215], a[164:167], a[32:35], 0\n"
    "v_exp_f32_e32 v234, v234\n"
    "v_mfma_f32_16x16x32_bf16 v[212:215], a[160:163], a[60:63], v[212:215]\n"
    "ds_read2st64_b32 v[198:199], v250 offset0:66 offset1:67\n"
    "v_mfma_f32_16x16x32_bf16 v[220:223], a[160:163], a[92:95], 0\n"
    "v_exp_f32_e32 v235, v235\n"
    "v_mfma_f32_16x16x32_bf16 v[220:223], a[164:167], a[64:67], v[220:223]\n"
    "buffer_load_dwordx4 v248, s[8:11], s41 offen offset:3072 sc1 lds\n"
    "v_cvt_pk_bf16_f32 v232, v232, v233\n"
    "v_mfma_f32_16x16x32_bf16 v[228:231], a[164:167], a[96:99], 0\n"
    "s_add_u32 s41, s41, 0x8000\n"
    "v_exp_f32_e32 v224, v224\n"
    "v_mfma_f32_16x16x32_bf16 v[228:231], a[160:163], a[124:127], v[228:231]\n"
    "v_exp_f32_e32 v225, v225\n"
    "v_mfma_f32_16x16x32_bf16 v[236:239], a[160:163], a[156:159], 0\n"
    "ds_read_b64_tr_b16 a[160:161], v244 offset:32768\n"
    "v_cvt_pk_bf16_f32 v233, v234, v235\n"
    "v_mfma_f32_16x16x32_bf16 v[236:239], a[164:167], a[128:131], v[236:239]\n"
    "ds_read_b64_tr_b16 a[162:163], v244 offset:34816\n"
    "v_mfma_f32_16x16x32_bf16 v[236:239], a[168:171], a[132:135], v[236:239]\n"
    "ds_read_b64_tr_b16 a[164:165], v244 offset:40960\n"
    "v_mfma_f32_16x16x32_bf16 v[236:239], a[172:175], a[136:139], v[236:239]\n"
    "ds_read_b64_tr_b16 a[166:167], v244 offset:43008\n"
    "v_mfma_f32_16x16x32_bf16 v[228:231], a[172:175], a[104:107], v[228:231]\n"
    "v_exp_f32_e32 v226, v226\n"
    "v_mfma_f32_16x16x32_bf16 v[228:231], a[168:171], a[100:103], v[228:231]\n"
    "v_exp_f32_e32 v227, v227\n"
    "v_mfma_f32_16x16x32_bf16 v[220:223], a[168:171], a[68:71], v[220:223]\n"
    "v_cvt_pk_bf16_f32 v224, v224, v225\n"
    "v_mfma_f32_16x16x32_bf16 v[220:223], a[172:175], a[72:75], v[220:223]\n"
    "v_exp_f32_e32 v216, v216\n"
    "v_mfma_f32_16x16x32_bf16 v[212:215], a[172:175], a[40:43], v[212:215]\n"
    "v_exp_f32_e32 v217, v217\n"
    "v_mfma_f32_16x16x32_bf16 v[212:215], a[168:171], a[36:39], v[212:215]\n"
    "v_cvt_pk_bf16_f32 v225, v226, v227\n"
    "v_mfma_f32_16x16x32_bf16 v[204:207], a[168:171], a[4:7], v[204:207]\n"
    "ds_read_b64_tr_b16 a[168:169], v245 offset:32768\n"
    "v_mfma_f32_16x16x32_bf16 v[204:207], a[172:175], a[8:11], v[204:207]\n"
    "s_waitcnt lgkmcnt(7)\n"
    "ds_read_b64_tr_b16 a[170:171], v245 offset:34816\n"
    "v_mfma_f32_16x16x32_bf16 v[204:207], a[176:179], a[12:15], v[204:207]\n"
    "ds_read_b64_tr_b16 a[172:173], v245 offset:40960\n"
    "v_mfma_f32_16x16x32_bf16 v[204:207], a[180:183], a[16:19], v[204:207]\n"
    "ds_read_b64_tr_b16 a[174:175], v245 offset:43008\n"
    "v_mfma_f32_16x16x32_bf16 v[212:215], a[180:183], a[48:51], v[212:215]\n"
    "v_exp_f32_e32 v218, v218\n"
    "v_mfma_f32_16x16x32_bf16 v[212:215], a[176:179], a[44:47], v[212:215]\n"
    "v_exp_f32_e32 v219, v219\n"
    "v_mfma_f32_16x16x32_bf16 v[220:223], a[176:179], a[76:79], v[220:223]\n"
    "v_cvt_pk_bf16_f32 v216, v216, v217\n"
    "v_mfma_f32_16x16x32_bf16 v[220:223], a[180:183], a[80:83], v[220:223]\n"
    "v_exp_f32_e32 v208, v208\n"
    "v_mfma_f32_16x16x32_bf16 v[228:231], a[180:183], a[112:115], v[228:231]\n"
    "v_exp_f32_e32 v209, v209\n"
    "v_mfma_f32_16x16x32_bf16 v[228:231], a[176:179], a[108:111], v[228:231]\n"
    "v_cvt_pk_bf16_f32 v217, v218, v219\n"
    "v_mfma_f32_16x16x32_bf16 v[236:239], a[176:179], a[140:143], v[236:239]\n"
    "ds_read_b64_tr_b16 a[176:177], v246 offset:32768\n"
    "v_mfma_f32_16x16x32_bf16 v[236:239], a[180:183], a[144:147], v[236:239]\n"
    "ds_read_b64_tr_b16 a[178:179], v246 offset:34816\n"
    "v_mfma_f32_16x16x32_bf16 v[236:239], a[184:187], a[148:151], v[236:239]\n"
    "ds_read_b64_tr_b16 a[180:181], v246 offset:40960\n"
    "v_mfma_f32_16x16x32_bf16 v[236:239], a[188:191], a[152:155], v[236:239]\n"
    "ds_read_b64_tr_b16 a[182:183], v246 offset:43008\n"
    "v_mfma_f32_16x16x32_bf16 v[228:231], a[188:191], a[120:123], v[228:231]\n"
    "v_exp_f32_e32 v210, v210\n"
    "v_mfma_f32_16x16x32_bf16 v[228:231], a[184:187], a[116:119], v[228:231]\n"
    "v_exp_f32_e32 v211, v211\n"
    "v_mfma_f32_16x16x32_bf16 v[220:223], a[184:187], a[84:87], v[220:223]\n"
    "v_cvt_pk_bf16_f32 v208, v208, v209\n"
    "v_mfma_f32_16x16x32_bf16 v[220:223], a[188:191], a[88:91], v[220:223]\n"
    "v_exp_f32_e32 v200, v200\n"
    "v_mfma_f32_16x16x32_bf16 v[212:215], a[188:191], a[56:59], v[212:215]\n"
    "v_exp_f32_e32 v201, v201\n"
    "v_mfma_f32_16x16x32_bf16 v[212:215], a[184:187], a[52:55], v[212:215]\n"
    "v_cvt_pk_bf16_f32 v209, v210, v211\n"
    "v_mfma_f32_16x16x32_bf16 v[204:207], a[184:187], a[20:23], v[204:207]\n"
    "ds_read_b64_tr_b16 a[184:185], v247 offset:32768\n"
    "v_mfma_f32_16x16x32_bf16 v[204:207], a[188:191], a[24:27], v[204:207]\n"
    "s_waitcnt lgkmcnt(5)\n"
    "ds_read_b64_tr_b16 a[186:187], v247 offset:34816\n"
    "v_mfma_f32_16x16x32_bf16 v[0:3], a[160:163], v[160:163], v[0:3]\n"
    "ds_read_b64_tr_b16 a[188:189], v247 offset:40960\n"
    "v_mfma_f32_16x16x32_bf16 v[0:3], a[164:167], v[164:167], v[0:3]\n"
    "ds_read_b64_tr_b16 a[190:191], v247 offset:43008\n"
    "v_mfma_f32_16x16x32_bf16 a[248:251], a[164:167], v[172:175], a[248:251]\n"
    "v_exp_f32_e32 v202, v202\n"
    "v_mfma_f32_16x16x32_bf16 a[248:251], a[160:163], v[168:171], a[248:251]\n"
    "v_exp_f32_e32 v203, v203\n"
    "v_mfma_f32_16x16x32_bf16 v[56:59], a[160:163], v[176:179], v[56:59]\n"
    "v_cvt_pk_bf16_f32 v200, v200, v201\n"
    "v_mfma_f32_16x16x32_bf16 v[56:59], a[164:167], v[180:183], v[56:59]\n"
    "v_cvt_pk_bf16_f32 v201, v202, v203\n"
    "v_mfma_f32_16x16x32_bf16 v[60:63], a[164:167], v[188:191], v[60:63]\n"
    "v_exp_f32_e32 v236, v236\n"
    "v_mfma_f32_16x16x32_bf16 v[60:63], a[160:163], v[184:187], v[60:63]\n"
    "v_exp_f32_e32 v237, v237\n"
    "v_mfma_f32_16x16x32_bf16 v[64:67], a[160:163], v[192:195], v[64:67]\n"
    "ds_read_b64_tr_b16 a[160:161], v244 offset:36864\n"
    "v_mfma_f32_16x16x32_bf16 v[64:67], a[164:167], v[196:199], v[64:67]\n"
    "ds_read_b64_tr_b16 a[162:163], v244 offset:38912\n"
    "v_mfma_f32_16x16x32_bf16 v[76:79], a[168:171], v[192:195], v[76:79]\n"
    "ds_read_b64_tr_b16 a[164:165], v244 offset:45056\n"
    "v_mfma_f32_16x16x32_bf16 v[76:79], a[172:175], v[196:199], v[76:79]\n"
    "ds_read_b64_tr_b16 a[166:167], v244 offset:47104\n"
    "v_mfma_f32_16x16x32_bf16 v[72:75], a[172:175], v[188:191], v[72:75]\n"
    "v_exp_f32_e32 v238, v238\n"
    "v_mfma_f32_16x16x32_bf16 v[72:75], a[168:171], v[184:187], v[72:75]\n"
    "v_exp_f32_e32 v239, v239\n"
    "v_mfma_f32_16x16x32_bf16 v[68:71], a[168:171], v[176:179], v[68:71]\n"
    "v_cvt_pk_bf16_f32 v234, v236, v237\n"
    "v_mfma_f32_16x16x32_bf16 v[68:71], a[172:175], v[180:183], v[68:71]\n"
    "v_exp_f32_e32 v228, v228\n"
    "v_mfma_f32_16x16x32_bf16 a[252:255], a[172:175], v[172:175], a[252:255]\n"
    "v_exp_f32_e32 v229, v229\n"
    "v_mfma_f32_16x16x32_bf16 a[252:255], a[168:171], v[168:171], a[252:255]\n"
    "v_cvt_pk_bf16_f32 v235, v238, v239\n"
    "v_mfma_f32_16x16x32_bf16 v[4:7], a[168:171], v[160:163], v[4:7]\n"
    "ds_read_b64_tr_b16 a[168:169], v245 offset:36864\n"
    "v_mfma_f32_16x16x32_bf16 v[4:7], a[172:175], v[164:167], v[4:7]\n"
    "s_waitcnt lgkmcnt(5)\n"
    "ds_read_b64_tr_b16 a[170:171], v245 offset:38912\n"
    "v_mfma_f32_16x16x32_bf16 v[8:11], a[176:179], v[160:163], v[8:11]\n"
    "ds_read_b64_tr_b16 a[172:173], v245 offset:45056\n"
    "v_mfma_f32_16x16x32_bf16 v[8:11], a[180:183], v[164:167], v[8:11]\n"
    "ds_read_b64_tr_b16 a[174:175], v245 offset:47104\n"
    "v_mfma_f32_16x16x32_bf16 v[32:35], a[180:183], v[172:175], v[32:35]\n"
    "s_or_b32 s45, s44, s37\n"
    "s_cselect_b32 s46, 2.0, s81\n"
    "s_cselect_b32 s45, 2.0, s80\n"
    "v_mfma_f32_16x16x32_bf16 v[32:35], a[176:179], v[168:171], v[32:35]\n"
    "v_exp_f32_e32 v230, v230\n"
    "v_mfma_f32_16x16x32_bf16 v[80:83], a[176:179], v[176:179], v[80:83]\n"
    "v_exp_f32_e32 v231, v231\n"
    "v_mfma_f32_16x16x32_bf16 v[80:83], a[180:183], v[180:183], v[80:83]\n"
    "v_cvt_pk_bf16_f32 v226, v228, v229\n"
    "v_mfma_f32_16x16x32_bf16 v[84:87], a[180:183], v[188:191], v[84:87]\n"
    "v_exp_f32_e32 v220, v220\n"
    "v_mfma_f32_16x16x32_bf16 v[84:87], a[176:179], v[184:187], v[84:87]\n"
    "v_exp_f32_e32 v221, v221\n"
    "v_mfma_f32_16x16x32_bf16 v[88:91], a[176:179], v[192:195], v[88:91]\n"
    "ds_read_b64_tr_b16 a[176:177], v246 offset:36864\n"
    "v_mfma_f32_16x16x32_bf16 v[88:91], a[180:183], v[196:199], v[88:91]\n"
    "ds_read_b64_tr_b16 a[178:179], v246 offset:38912\n"
    "v_mfma_f32_16x16x32_bf16 v[100:103], a[184:187], v[192:195], v[100:103]\n"
    "ds_read_b64_tr_b16 a[180:181], v246 offset:45056\n"
    "v_mfma_f32_16x16x32_bf16 v[100:103], a[188:191], v[196:199], v[100:103]\n"
    "ds_read_b64_tr_b16 a[182:183], v246 offset:47104\n"
    "v_mfma_f32_16x16x32_bf16 v[96:99], a[188:191], v[188:191], v[96:99]\n"
    "v_cvt_pk_bf16_f32 v227, v230, v231\n"
    "v_mfma_f32_16x16x32_bf16 v[96:99], a[184:187], v[184:187], v[96:99]\n"
    "v_exp_f32_e32 v222, v222\n"
    "v_mfma_f32_16x16x32_bf16 v[92:95], a[184:187], v[176:179], v[92:95]\n"
    "v_exp_f32_e32 v223, v223\n"
    "v_mfma_f32_16x16x32_bf16 v[92:95], a[188:191], v[180:183], v[92:95]\n"
    "v_cvt_pk_bf16_f32 v218, v220, v221\n"
    "v_mfma_f32_16x16x32_bf16 v[36:39], a[188:191], v[172:175], v[36:39]\n"
    "v_exp_f32_e32 v212, v212\n"
    "v_mfma_f32_16x16x32_bf16 v[36:39], a[184:187], v[168:171], v[36:39]\n"
    "v_exp_f32_e32 v213, v213\n"
    "v_mfma_f32_16x16x32_bf16 v[12:15], a[184:187], v[160:163], v[12:15]\n"
    "s_mov_b32 m0, s49\n"
    "ds_read_b64_tr_b16 a[184:185], v247 offset:36864\n"
    "v_mfma_f32_16x16x32_bf16 v[12:15], a[188:191], v[164:167], v[12:15]\n"
    "s_waitcnt lgkmcnt(5)\n"
    "ds_read_b64_tr_b16 a[186:187], v247 offset:38912\n"
    "v_mfma_f32_16x16x32_bf16 v[16:19], a[160:163], v[160:163], v[16:19]\n"
    "ds_read_b64_tr_b16 a[188:189], v247 offset:45056\n"
    "v_mfma_f32_16x16x32_bf16 v[16:19], a[164:167], v[164:167], v[16:19]\n"
    "ds_read_b64_tr_b16 a[190:191], v247 offset:47104\n"
    "v_mfma_f32_16x16x32_bf16 v[40:43], a[164:167], v[172:175], v[40:43]\n"
    "buffer_load_dword v154, off, s[8:11], s45 sc1\n"
    "v_mfma_f32_16x16x32_bf16 v[40:43], a[160:163], v[168:171], v[40:43]\n"
    "v_cvt_pk_bf16_f32 v219, v222, v223\n"
    "v_mfma_f32_16x16x32_bf16 v[104:107], a[160:163], v[176:179], v[104:107]\n"
    "v_exp_f32_e32 v214, v214\n"
    "v_mfma_f32_16x16x32_bf16 v[104:107], a[164:167], v[180:183], v[104:107]\n"
    "s_waitcnt vmcnt(9)\n"
    "v_mfma_f32_16x16x32_bf16 v[108:111], a[164:167], v[188:191], v[108:111]\n"
    "s_barrier\n"
    "v_mfma_f32_16x16x32_bf16 v[108:111], a[160:163], v[184:187], v[108:111]\n"
    "v_exp_f32_e32 v215, v215\n"
    "v_mfma_f32_16x16x32_bf16 v[112:115], a[160:163], v[192:195], v[112:115]\n"
    "ds_read_b128 a[160:163], v243 offset:4096\n"
    "v_mfma_f32_16x16x32_bf16 v[112:115], a[164:167], v[196:199], v[112:115]\n"
    "ds_read_b128 a[164:167], v240 offset:0\n"
    "v_mfma_f32_16x16x32_bf16 v[124:127], a[168:171], v[192:195], v[124:127]\n"
    "ds_write_addtid_b32 v232 offset:16384\n"
    "v_cvt_pk_bf16_f32 v210, v212, v213\n"
    "v_mfma_f32_16x16x32_bf16 v[124:127], a[172:175], v[196:199], v[124:127]\n"
    "v_exp_f32_e32 v204, v204\n"
    "v_mfma_f32_16x16x32_bf16 v[120:123], a[172:175], v[188:191], v[120:123]\n"
    "v_exp_f32_e32 v205, v205\n"
    "v_mfma_f32_16x16x32_bf16 v[120:123], a[168:171], v[184:187], v[120:123]\n"
    "ds_write_addtid_b32 v233 offset:16640\n"
    "v_cvt_pk_bf16_f32 v211, v214, v215\n"
    "v_mfma_f32_16x16x32_bf16 v[116:119], a[168:171], v[176:179], v[116:119]\n"
    "v_exp_f32_e32 v206, v206\n"
    "v_mfma_f32_16x16x32_bf16 v[116:119], a[172:175], v[180:183], v[116:119]\n"
    "v_exp_f32_e32 v207, v207\n"
    "v_mfma_f32_16x16x32_bf16 v[44:47], a[172:175], v[172:175], v[44:47]\n"
    "ds_write_addtid_b32 v224 offset:12288\n"
    "v_cvt_pk_bf16_f32 v202, v204, v205\n"
    "v_mfma_f32_16x16x32_bf16 v[44:47], a[168:171], v[168:171], v[44:47]\n"
    "ds_write_addtid_b32 v225 offset:12544\n"
    "v_cvt_pk_bf16_f32 v203, v206, v207\n"
    "v_mfma_f32_16x16x32_bf16 v[20:23], a[168:171], v[160:163], v[20:23]\n"
    "ds_read_b128 a[168:171], v241 offset:0\n"
    "v_mfma_f32_16x16x32_bf16 v[20:23], a[172:175], v[164:167], v[20:23]\n"
    "s_waitcnt lgkmcnt(7)\n"
    "ds_read_b128 a[172:175], v240 offset:4096\n"
    "v_mfma_f32_16x16x32_bf16 v[24:27], a[176:179], v[160:163], v[24:27]\n"
    "ds_write_addtid_b32 v216 offset:8192\n"
    "v_mfma_f32_16x16x32_bf16 v[24:27], a[180:183], v[164:167], v[24:27]\n"
    "ds_write_addtid_b32 v217 offset:8448\n"
    "v_mfma_f32_16x16x32_bf16 v[48:51], a[180:183], v[172:175], v[48:51]\n"
    "ds_write_addtid_b32 v208 offset:4096\n"
    "v_mfma_f32_16x16x32_bf16 v[48:51], a[176:179], v[168:171], v[48:51]\n"
    "ds_write_addtid_b32 v209 offset:4352\n"
    "v_mfma_f32_16x16x32_bf16 v[128:131], a[176:179], v[176:179], v[128:131]\n"
    "ds_write_addtid_b32 v200 offset:0\n"
    "v_mfma_f32_16x16x32_bf16 v[128:131], a[180:183], v[180:183], v[128:131]\n"
    "ds_write_addtid_b32 v201 offset:256\n"
    "v_mfma_f32_16x16x32_bf16 v[132:135], a[180:183], v[188:191], v[132:135]\n"
    "ds_write_addtid_b32 v234 offset:16896\n"
    "v_mfma_f32_16x16x32_bf16 v[132:135], a[176:179], v[184:187], v[132:135]\n"
    "ds_write_addtid_b32 v235 offset:17152\n"
    "v_mfma_f32_16x16x32_bf16 v[136:139], a[176:179], v[192:195], v[136:139]\n"
    "ds_read_b128 a[176:179], v241 offset:4096\n"
    "v_mfma_f32_16x16x32_bf16 v[136:139], a[180:183], v[196:199], v[136:139]\n"
    "ds_read_b128 a[180:183], v242 offset:0\n"
    "v_mfma_f32_16x16x32_bf16 v[148:151], a[184:187], v[192:195], v[148:151]\n"
    "ds_write_addtid_b32 v226 offset:12800\n"
    "v_mfma_f32_16x16x32_bf16 v[148:151], a[188:191], v[196:199], v[148:151]\n"
    "ds_write_addtid_b32 v227 offset:13056\n"
    "v_mfma_f32_16x16x32_bf16 v[144:147], a[188:191], v[188:191], v[144:147]\n"
    "ds_write_addtid_b32 v218 offset:8704\n"
    "v_mfma_f32_16x16x32_bf16 v[144:147], a[184:187], v[184:187], v[144:147]\n"
    "ds_write_addtid_b32 v219 offset:8960\n"
    "v_mfma_f32_16x16x32_bf16 v[140:143], a[184:187], v[176:179], v[140:143]\n"
    "ds_write_addtid_b32 v210 offset:4608\n"
    "v_mfma_f32_16x16x32_bf16 v[140:143], a[188:191], v[180:183], v[140:143]\n"
    "ds_write_addtid_b32 v211 offset:4864\n"
    "v_mfma_f32_16x16x32_bf16 v[52:55], a[188:191], v[172:175], v[52:55]\n"
    "ds_write_addtid_b32 v202 offset:512\n"
    "v_mfma_f32_16x16x32_bf16 v[52:55], a[184:187], v[168:171], v[52:55]\n"
    "ds_write_addtid_b32 v203 offset:768\n"
    "v_mfma_f32_16x16x32_bf16 v[28:31], a[184:187], v[160:163], v[28:31]\n"
    "ds_read_b128 a[184:187], v243 offset:0\n"
    "v_mfma_f32_16x16x32_bf16 v[28:31], a[188:191], v[164:167], v[28:31]\n"
    "s_waitcnt lgkmcnt(15)\n"
    "ds_read_b128 a[188:191], v242 offset:4096\n"
    "v_mfma_f32_16x16x32_bf16 v[160:163], a[160:163], a[28:31], 0\n"
    "v_lshlrev_b32_e32 v253, 2, v249\n"
    "v_mfma_f32_16x16x32_bf16 v[160:163], a[164:167], a[0:3], v[160:163]\n"
    "v_mfma_f32_16x16x32_bf16 v[168:171], a[164:167], a[32:35], 0\n"
    "v_mfma_f32_16x16x32_bf16 v[168:171], a[160:163], a[60:63], v[168:171]\n"
    "v_mfma_f32_16x16x32_bf16 v[176:179], a[160:163], a[92:95], 0\n"
    "v_mfma_f32_16x16x32_bf16 v[176:179], a[164:167], a[64:67], v[176:179]\n"
    "s_waitcnt lgkmcnt(0)\n"
    "v_mfma_f32_16x16x32_bf16 v[184:187], a[164:167], a[96:99], 0\n"
    "s_barrier\n"
    "v_mfma_f32_16x16x32_bf16 v[184:187], a[160:163], a[124:127], v[184:187]\n"
    "s_add_u32 m0, s43, 32768\n"
    "ds_read2st64_b32 v[204:205], v250 offset0:0 offset1:1\n"
    "v_mfma_f32_16x16x32_bf16 v[192:195], a[160:163], a[156:159], 0\n"
    "ds_read_b128 a[160:163], v243 offset:6144\n"
    "v_mfma_f32_16x16x32_bf16 v[192:195], a[164:167], a[128:131], v[192:195]\n"
    "ds_read_b128 a[164:167], v240 offset:2048\n"
    "v_mfma_f32_16x16x32_bf16 v[192:195], a[168:171], a[132:135], v[192:195]\n"
    "buffer_load_dwordx4 v248, s[8:11], s40 offen offset:0 sc1 lds\n"
    "v_mfma_f32_16x16x32_bf16 v[192:195], a[172:175], a[136:139], v[192:195]\n"
    "v_mfma_f32_16x16x32_bf16 v[184:187], a[172:175], a[104:107], v[184:187]\n"
    "v_mfma_f32_16x16x32_bf16 v[184:187], a[168:171], a[100:103], v[184:187]\n"
    "ds_read2st64_b32 v[206:207], v250 offset0:2 offset1:3\n"
    "v_mfma_f32_16x16x32_bf16 v[176:179], a[168:171], a[68:71], v[176:179]\n"
    "v_mfma_f32_16x16x32_bf16 v[176:179], a[172:175], a[72:75], v[176:179]\n"
    "buffer_load_dwordx4 v248, s[8:11], s40 offen offset:1024 sc1 lds\n"
    "v_mfma_f32_16x16x32_bf16 v[168:171], a[172:175], a[40:43], v[168:171]\n"
    "v_mfma_f32_16x16x32_bf16 v[168:171], a[168:171], a[36:39], v[168:171]\n"
    "ds_read2st64_b32 v[212:213], v250 offset0:16 offset1:17\n"
    "v_mfma_f32_16x16x32_bf16 v[160:163], a[168:171], a[4:7], v[160:163]\n"
    "ds_read_b128 a[168:171], v241 offset:2048\n"
    "v_mfma_f32_16x16x32_bf16 v[160:163], a[172:175], a[8:11], v[160:163]\n"
    "ds_read_b128 a[172:175], v240 offset:6144\n"
    "v_mfma_f32_16x16x32_bf16 v[160:163], a[176:179], a[12:15], v[160:163]\n"
    "buffer_load_dwordx4 v248, s[8:11], s40 offen offset:2048 sc1 lds\n"
    "v_mfma_f32_16x16x32_bf16 v[160:163], a[180:183], a[16:19], v[160:163]\n"
    "ds_read2st64_b32 v[214:215], v250 offset0:18 offset1:19\n"
    "v_mfma_f32_16x16x32_bf16 v[168:171], a[180:183], a[48:51], v[168:171]\n"
    "v_mfma_f32_16x16x32_bf16 v[168:171], a[176:179], a[44:47], v[168:171]\n"
    "v_mfma_f32_16x16x32_bf16 v[176:179], a[176:179], a[76:79], v[176:179]\n"
    "ds_read2st64_b32 v[220:221], v250 offset0:32 offset1:33\n"
    "v_mfma_f32_16x16x32_bf16 v[176:179], a[180:183], a[80:83], v[176:179]\n"
    "buffer_load_dwordx4 v248, s[8:11], s40 offen offset:3072 sc1 lds\n"
    "v_mfma_f32_16x16x32_bf16 v[184:187], a[180:183], a[112:115], v[184:187]\n"
    "s_add_u32 m0, s43, 36864\n"
    "v_mfma_f32_16x16x32_bf16 v[184:187], a[176:179], a[108:111], v[184:187]\n"
    "s_add_u32 s40, s40, 0x8000\n"
    "ds_read2st64_b32 v[222:223], v250 offset0:34 offset1:35\n"
    "v_mfma_f32_16x16x32_bf16 v[192:195], a[176:179], a[140:143], v[192:195]\n"
    "ds_read_b128 a[176:179], v241 offset:6144\n"
    "v_mfma_f32_16x16x32_bf16 v[192:195], a[180:183], a[144:147], v[192:195]\n"
    "ds_read_b128 a[180:183], v242 offset:2048\n"
    "v_mfma_f32_16x16x32_bf16 v[192:195], a[184:187], a[148:151], v[192:195]\n"
    "buffer_load_dwordx4 v248, s[8:11], s41 offen offset:0 sc1 lds\n"
    "v_mfma_f32_16x16x32_bf16 v[192:195], a[188:191], a[152:155], v[192:195]\n"
    "v_mfma_f32_16x16x32_bf16 v[184:187], a[188:191], a[120:123], v[184:187]\n"
    "ds_read2st64_b32 v[228:229], v250 offset0:48 offset1:49\n"
    "v_mfma_f32_16x16x32_bf16 v[184:187], a[184:187], a[116:119], v[184:187]\n"
    "v_mfma_f32_16x16x32_bf16 v[176:179], a[184:187], a[84:87], v[176:179]\n"
    "v_exp_f32_e32 v192, v192\n"
    "v_mfma_f32_16x16x32_bf16 v[176:179], a[188:191], a[88:91], v[176:179]\n"
    "buffer_load_dwordx4 v248, s[8:11], s41 offen offset:1024 sc1 lds\n"
    "v_mfma_f32_16x16x32_bf16 v[168:171], a[188:191], a[56:59], v[168:171]\n"
    "ds_read2st64_b32 v[230:231], v250 offset0:50 offset1:51\n"
    "v_mfma_f32_16x16x32_bf16 v[168:171], a[184:187], a[52:55], v[168:171]\n"
    "v_exp_f32_e32 v193, v193\n"
    "v_mfma_f32_16x16x32_bf16 v[160:163], a[184:187], a[20:23], v[160:163]\n"
    "ds_read_b128 a[184:187], v243 offset:2048\n"
    "v_mfma_f32_16x16x32_bf16 v[160:163], a[188:191], a[24:27], v[160:163]\n"
    "s_waitcnt lgkmcnt(8)\n"
    "ds_read_b128 a[188:191], v242 offset:6144\n"
    "v_mfma_f32_16x16x32_bf16 v[164:167], a[160:163], a[28:31], 0\n"
    "buffer_load_dwordx4 v248, s[8:11], s41 offen offset:2048 sc1 lds\n"
    "v_mfma_f32_16x16x32_bf16 v[164:167], a[164:167], a[0:3], v[164:167]\n"
    "ds_read2st64_b32 v[236:237], v250 offset0:64 offset1:65\n"
    "v_mfma_f32_16x16x32_bf16 v[172:175], a[164:167], a[32:35], 0\n"
    "v_exp_f32_e32 v194, v194\n"
    "v_mfma_f32_16x16x32_bf16 v[172:175], a[160:163], a[60:63], v[172:175]\n"
    "ds_read2st64_b32 v[238:239], v250 offset0:66 offset1:67\n"
    "v_mfma_f32_16x16x32_bf16 v[180:183], a[160:163], a[92:95], 0\n"
    "v_exp_f32_e32 v195, v195\n"
    "v_mfma_f32_16x16x32_bf16 v[180:183], a[164:167], a[64:67], v[180:183]\n"
    "buffer_load_dwordx4 v248, s[8:11], s41 offen offset:3072 sc1 lds\n"
    "v_cvt_pk_bf16_f32 v192, v192, v193\n"
    "v_mfma_f32_16x16x32_bf16 v[188:191], a[164:167], a[96:99], 0\n"
    "s_add_u32 s41, s41, 0x8000\n"
    "v_exp_f32_e32 v184, v184\n"
    "v_mfma_f32_16x16x32_bf16 v[188:191], a[160:163], a[124:127], v[188:191]\n"
    "v_exp_f32_e32 v185, v185\n"
    "v_mfma_f32_16x16x32_bf16 v[196:199], a[160:163], a[156:159], 0\n"
    "ds_read_b64_tr_b16 a[160:161], v244 offset:49152\n"
    "v_mfma_f32_16x16x32_bf16 v[196:199], a[164:167], a[128:131], v[196:199]\n"
    "ds_read_b64_tr_b16 a[162:163], v244 offset:51200\n"
    "v_mfma_f32_16x16x32_bf16 v[196:199], a[168:171], a[132:135], v[196:199]\n"
    "ds_read_b64_tr_b16 a[164:165], v244 offset:57344\n"
    "v_mfma_f32_16x16x32_bf16 v[196:199], a[172:175], a[136:139], v[196:199]\n"
    "ds_read_b64_tr_b16 a[166:167], v244 offset:59392\n"
    "v_mfma_f32_16x16x32_bf16 v[188:191], a[172:175], a[104:107], v[188:191]\n"
    "v_cvt_pk_bf16_f32 v193, v194, v195\n"
    "v_mfma_f32_16x16x32_bf16 v[188:191], a[168:171], a[100:103], v[188:191]\n"
    "v_exp_f32_e32 v186, v186\n"
    "v_mfma_f32_16x16x32_bf16 v[180:183], a[168:171], a[68:71], v[180:183]\n"
    "v_exp_f32_e32 v187, v187\n"
    "v_mfma_f32_16x16x32_bf16 v[180:183], a[172:175], a[72:75], v[180:183]\n"
    "v_cvt_pk_bf16_f32 v184, v184, v185\n"
    "v_mfma_f32_16x16x32_bf16 v[172:175], a[172:175], a[40:43], v[172:175]\n"
    "v_exp_f32_e32 v176, v176\n"
    "v_mfma_f32_16x16x32_bf16 v[172:175], a[168:171], a[36:39], v[172:175]\n"
    "v_exp_f32_e32 v177, v177\n"
    "v_mfma_f32_16x16x32_bf16 v[164:167], a[168:171], a[4:7], v[164:167]\n"
    "ds_read_b64_tr_b16 a[168:169], v245 offset:49152\n"
    "v_mfma_f32_16x16x32_bf16 v[164:167], a[172:175], a[8:11], v[164:167]\n"
    "s_waitcnt lgkmcnt(7)\n"
    "ds_read_b64_tr_b16 a[170:171], v245 offset:51200\n"
    "v_mfma_f32_16x16x32_bf16 v[164:167], a[176:179], a[12:15], v[164:167]\n"
    "s_mov_b32 m0, s82\n"
    "ds_read_b64_tr_b16 a[172:173], v245 offset:57344\n"
    "v_mfma_f32_16x16x32_bf16 v[164:167], a[180:183], a[16:19], v[164:167]\n"
    "s_waitcnt vmcnt(8)\n"
    "ds_read_b64_tr_b16 a[174:175], v245 offset:59392\n"
    "v_mfma_f32_16x16x32_bf16 v[172:175], a[180:183], a[48:51], v[172:175]\n"
    "buffer_load_dwordx4 v253, s[8:11], s46 offen offset:0 sc1 lds\n"
    "v_mfma_f32_16x16x32_bf16 v[172:175], a[176:179], a[44:47], v[172:175]\n"
    "v_cvt_pk_bf16_f32 v185, v186, v187\n"
    "v_mfma_f32_16x16x32_bf16 v[180:183], a[176:179], a[76:79], v[180:183]\n"
    "v_exp_f32_e32 v178, v178\n"
    "v_mfma_f32_16x16x32_bf16 v[180:183], a[180:183], a[80:83], v[180:183]\n"
    "v_exp_f32_e32 v179, v179\n"
    "v_mfma_f32_16x16x32_bf16 v[188:191], a[180:183], a[112:115], v[188:191]\n"
    "v_cvt_pk_bf16_f32 v176, v176, v177\n"
    "v_mfma_f32_16x16x32_bf16 v[188:191], a[176:179], a[108:111], v[188:191]\n"
    "v_exp_f32_e32 v168, v168\n"
    "v_mfma_f32_16x16x32_bf16 v[196:199], a[176:179], a[140:143], v[196:199]\n"
    "ds_read_b64_tr_b16 a[176:177], v246 offset:49152\n"
    "v_mfma_f32_16x16x32_bf16 v[196:199], a[180:183], a[144:147], v[196:199]\n"
    "ds_read_b64_tr_b16 a[178:179], v246 offset:51200\n"
    "v_mfma_f32_16x16x32_bf16 v[196:199], a[184:187], a[148:151], v[196:199]\n"
    "ds_read_b64_tr_b16 a[180:181], v246 offset:57344\n"
    "v_mfma_f32_16x16x32_bf16 v[196:199], a[188:191], a[152:155], v[196:199]\n"
    "ds_read_b64_tr_b16 a[182:183], v246 offset:59392\n"
    "v_mfma_f32_16x16x32_bf16 v[188:191], a[188:191], a[120:123], v[188:191]\n"
    "v_exp_f32_e32 v169, v169\n"
    "v_mfma_f32_16x16x32_bf16 v[188:191], a[184:187], a[116:119], v[188:191]\n"
    "v_cvt_pk_bf16_f32 v177, v178, v179\n"
    "v_mfma_f32_16x16x32_bf16 v[180:183], a[184:187], a[84:87], v[180:183]\n"
    "v_exp_f32_e32 v170, v170\n"
    "v_mfma_f32_16x16x32_bf16 v[180:183], a[188:191], a[88:91], v[180:183]\n"
    "v_exp_f32_e32 v171, v171\n"
    "v_mfma_f32_16x16x32_bf16 v[172:175], a[188:191], a[56:59], v[172:175]\n"
    "v_cvt_pk_bf16_f32 v168, v168, v169\n"
    "v_mfma_f32_16x16x32_bf16 v[172:175], a[184:187], a[52:55], v[172:175]\n"
    "v_exp_f32_e32 v160, v160\n"
    "v_mfma_f32_16x16x32_bf16 v[164:167], a[184:187], a[20:23], v[164:167]\n"
    "ds_read_b64_tr_b16 a[184:185], v247 offset:49152\n"
    "v_mfma_f32_16x16x32_bf16 v[164:167], a[188:191], a[24:27], v[164:167]\n"
    "s_waitcnt lgkmcnt(5)\n"
    "ds_read_b64_tr_b16 a[186:187], v247 offset:51200\n"
    "v_mfma_f32_16x16x32_bf16 v[0:3], a[160:163], v[200:203], v[0:3]\n"
    "ds_read_b64_tr_b16 a[188:189], v247 offset:57344\n"
    "v_mfma_f32_16x16x32_bf16 v[0:3], a[164:167], v[204:207], v[0:3]\n"
    "ds_read_b64_tr_b16 a[190:191], v247 offset:59392\n"
    "v_mfma_f32_16x16x32_bf16 a[248:251], a[164:167], v[212:215], a[248:251]\n"
    "v_exp_f32_e32 v161, v161\n"
    "v_mfma_f32_16x16x32_bf16 a[248:251], a[160:163], v[208:211], a[248:251]\n"
    "v_cvt_pk_bf16_f32 v169, v170, v171\n"
    "v_mfma_f32_16x16x32_bf16 v[56:59], a[160:163], v[216:219], v[56:59]\n"
    "v_exp_f32_e32 v162, v162\n"
    "v_mfma_f32_16x16x32_bf16 v[56:59], a[164:167], v[220:223], v[56:59]\n"
    "v_exp_f32_e32 v163, v163\n"
    "v_mfma_f32_16x16x32_bf16 v[60:63], a[164:167], v[228:231], v[60:63]\n"
    "v_cvt_pk_bf16_f32 v160, v160, v161\n"
    "v_mfma_f32_16x16x32_bf16 v[60:63], a[160:163], v[224:227], v[60:63]\n"
    "v_cvt_pk_bf16_f32 v161, v162, v163\n"
    "v_mfma_f32_16x16x32_bf16 v[64:67], a[160:163], v[232:235], v[64:67]\n"
    "ds_read_b64_tr_b16 a[160:161], v244 offset:53248\n"
    "v_mfma_f32_16x16x32_bf16 v[64:67], a[164:167], v[236:239], v[64:67]\n"
    "ds_read_b64_tr_b16 a[162:163], v244 offset:55296\n"
    "v_mfma_f32_16x16x32_bf16 v[76:79], a[168:171], v[232:235], v[76:79]\n"
    "ds_read_b64_tr_b16 a[164:165], v244 offset:61440\n"
    "v_mfma_f32_16x16x32_bf16 v[76:79], a[172:175], v[236:239], v[76:79]\n"
    "ds_read_b64_tr_b16 a[166:167], v244 offset:63488\n"
    "v_mfma_f32_16x16x32_bf16 v[72:75], a[172:175], v[228:231], v[72:75]\n"
    "v_exp_f32_e32 v196, v196\n"
    "v_mfma_f32_16x16x32_bf16 v[72:75], a[168:171], v[224:227], v[72:75]\n"
    "v_exp_f32_e32 v197, v197\n"
    "v_mfma_f32_16x16x32_bf16 v[68:71], a[168:171], v[216:219], v[68:71]\n"
    "v_exp_f32_e32 v198, v198\n"
    "v_mfma_f32_16x16x32_bf16 v[68:71], a[172:175], v[220:223], v[68:71]\n"
    "v_exp_f32_e32 v199, v199\n"
    "v_mfma_f32_16x16x32_bf16 a[252:255], a[172:175], v[212:215], a[252:255]\n"
    "v_cvt_pk_bf16_f32 v194, v196, v197\n"
    "v_mfma_f32_16x16x32_bf16 a[252:255], a[168:171], v[208:211], a[252:255]\n"
    "v_exp_f32_e32 v188, v188\n"
    "v_mfma_f32_16x16x32_bf16 v[4:7], a[168:171], v[200:203], v[4:7]\n"
    "ds_read_b64_tr_b16 a[168:169], v245 offset:53248\n"
    "v_mfma_f32_16x16x32_bf16 v[4:7], a[172:175], v[204:207], v[4:7]\n"
    "s_waitcnt lgkmcnt(5)\n"
    "ds_read_b64_tr_b16 a[170:171], v245 offset:55296\n"
    "v_mfma_f32_16x16x32_bf16 v[8:11], a[176:179], v[200:203], v[8:11]\n"
    "ds_read_b64_tr_b16 a[172:173], v245 offset:61440\n"
    "v_mfma_f32_16x16x32_bf16 v[8:11], a[180:183], v[204:207], v[8:11]\n"
    "ds_read_b64_tr_b16 a[174:175], v245 offset:63488\n"
    "v_mfma_f32_16x16x32_bf16 v[32:35], a[180:183], v[212:215], v[32:35]\n"
    "v_exp_f32_e32 v189, v189\n"
    "v_mfma_f32_16x16x32_bf16 v[32:35], a[176:179], v[208:211], v[32:35]\n"
    "v_cvt_pk_bf16_f32 v195, v198, v199\n"
    "v_mfma_f32_16x16x32_bf16 v[80:83], a[176:179], v[216:219], v[80:83]\n"
    "v_exp_f32_e32 v190, v190\n"
    "v_mfma_f32_16x16x32_bf16 v[80:83], a[180:183], v[220:223], v[80:83]\n"
    "v_exp_f32_e32 v191, v191\n"
    "v_mfma_f32_16x16x32_bf16 v[84:87], a[180:183], v[228:231], v[84:87]\n"
    "v_cvt_pk_bf16_f32 v186, v188, v189\n"
    "v_mfma_f32_16x16x32_bf16 v[84:87], a[176:179], v[224:227], v[84:87]\n"
    "v_exp_f32_e32 v180, v180\n"
    "v_mfma_f32_16x16x32_bf16 v[88:91], a[176:179], v[232:235], v[88:91]\n"
    "ds_read_b64_tr_b16 a[176:177], v246 offset:53248\n"
    "v_mfma_f32_16x16x32_bf16 v[88:91], a[180:183], v[236:239], v[88:91]\n"
    "ds_read_b64_tr_b16 a[178:179], v246 offset:55296\n"
    "v_mfma_f32_16x16x32_bf16 v[100:103], a[184:187], v[232:235], v[100:103]\n"
    "ds_read_b64_tr_b16 a[180:181], v246 offset:61440\n"
    "v_mfma_f32_16x16x32_bf16 v[100:103], a[188:191], v[236:239], v[100:103]\n"
    "ds_read_b64_tr_b16 a[182:183], v246 offset:63488\n"
    "v_mfma_f32_16x16x32_bf16 v[96:99], a[188:191], v[228:231], v[96:99]\n"
    "v_exp_f32_e32 v181, v181\n"
    "v_mfma_f32_16x16x32_bf16 v[96:99], a[184:187], v[224:227], v[96:99]\n"
    "v_cvt_pk_bf16_f32 v187, v190, v191\n"
    "v_mfma_f32_16x16x32_bf16 v[92:95], a[184:187], v[216:219], v[92:95]\n"
    "v_exp_f32_e32 v182, v182\n"
    "v_mfma_f32_16x16x32_bf16 v[92:95], a[188:191], v[220:223], v[92:95]\n"
    "v_exp_f32_e32 v183, v183\n"
    "v_mfma_f32_16x16x32_bf16 v[36:39], a[188:191], v[212:215], v[36:39]\n"
    "v_cvt_pk_bf16_f32 v178, v180, v181\n"
    "v_mfma_f32_16x16x32_bf16 v[36:39], a[184:187], v[208:211], v[36:39]\n"
    "v_exp_f32_e32 v172, v172\n"
    "v_mfma_f32_16x16x32_bf16 v[12:15], a[184:187], v[200:203], v[12:15]\n"
    "s_mov_b32 m0, s49\n"
    "ds_read_b64_tr_b16 a[184:185], v247 offset:53248\n"
    "v_mfma_f32_16x16x32_bf16 v[12:15], a[188:191], v[204:207], v[12:15]\n"
    "s_waitcnt lgkmcnt(5)\n"
    "ds_read_b64_tr_b16 a[186:187], v247 offset:55296\n"
    "v_mfma_f32_16x16x32_bf16 v[16:19], a[160:163], v[200:203], v[16:19]\n"
    "ds_read_b64_tr_b16 a[188:189], v247 offset:61440\n"
    "v_mfma_f32_16x16x32_bf16 v[16:19], a[164:167], v[204:207], v[16:19]\n"
    "ds_read_b64_tr_b16 a[190:191], v247 offset:63488\n"
    "v_mfma_f32_16x16x32_bf16 v[40:43], a[164:167], v[212:215], v[40:43]\n"
    "v_exp_f32_e32 v173, v173\n"
    "v_mfma_f32_16x16x32_bf16 v[40:43], a[160:163], v[208:211], v[40:43]\n"
    "v_cvt_pk_bf16_f32 v179, v182, v183\n"
    "v_mfma_f32_16x16x32_bf16 v[104:107], a[160:163], v[216:219], v[104:107]\n"
    "v_exp_f32_e32 v174, v174\n"
    "v_mfma_f32_16x16x32_bf16 v[104:107], a[164:167], v[220:223], v[104:107]\n"
    "s_waitcnt vmcnt(9)\n"
    "v_mfma_f32_16x16x32_bf16 v[108:111], a[164:167], v[228:231], v[108:111]\n"
    "s_barrier\n"
    "v_mfma_f32_16x16x32_bf16 v[108:111], a[160:163], v[224:227], v[108:111]\n"
    "v_exp_f32_e32 v175, v175\n"
    "v_mfma_f32_16x16x32_bf16 v[112:115], a[160:163], v[232:235], v[112:115]\n"
    "ds_read_b128 a[160:163], v243 offset:20480\n"
    "v_mfma_f32_16x16x32_bf16 v[112:115], a[164:167], v[236:239], v[112:115]\n"
    "ds_read_b128 a[164:167], v240 offset:16384\n"
    "v_mfma_f32_16x16x32_bf16 v[124:127], a[168:171], v[232:235], v[124:127]\n"
    "ds_write_addtid_b32 v192 offset:16384\n"
    "v_cvt_pk_bf16_f32 v170, v172, v173\n"
    "v_mfma_f32_16x16x32_bf16 v[124:127], a[172:175], v[236:239], v[124:127]\n"
    "v_exp_f32_e32 v164, v164\n"
    "v_mfma_f32_16x16x32_bf16 v[120:123], a[172:175], v[228:231], v[120:123]\n"
    "v_exp_f32_e32 v165, v165\n"
    "v_mfma_f32_16x16x32_bf16 v[120:123], a[168:171], v[224:227], v[120:123]\n"
    "ds_write_addtid_b32 v193 offset:16640\n"
    "v_cvt_pk_bf16_f32 v171, v174, v175\n"
    "v_mfma_f32_16x16x32_bf16 v[116:119], a[168:171], v[216:219], v[116:119]\n"
    "v_exp_f32_e32 v166, v166\n"
    "v_mfma_f32_16x16x32_bf16 v[116:119], a[172:175], v[220:223], v[116:119]\n"
    "v_exp_f32_e32 v167, v167\n"
    "v_mfma_f32_16x16x32_bf16 v[44:47], a[172:175], v[212:215], v[44:47]\n"
    "ds_write_addtid_b32 v184 offset:12288\n"
    "v_cvt_pk_bf16_f32 v162, v164, v165\n"
    "v_mfma_f32_16x16x32_bf16 v[44:47], a[168:171], v[208:211], v[44:47]\n"
    "ds_write_addtid_b32 v185 offset:12544\n"
    "v_cvt_pk_bf16_f32 v163, v166, v167\n"
    "v_mfma_f32_16x16x32_bf16 v[20:23], a[168:171], v[200:203], v[20:23]\n"
    "ds_read_b128 a[168:171], v241 offset:16384\n"
    "v_mfma_f32_16x16x32_bf16 v[20:23], a[172:175], v[204:207], v[20:23]\n"
    "s_waitcnt lgkmcnt(7)\n"
    "ds_read_b128 a[172:175], v240 offset:20480\n"
    "v_mfma_f32_16x16x32_bf16 v[24:27], a[176:179], v[200:203], v[24:27]\n"
    "ds_write_addtid_b32 v176 offset:8192\n"
    "v_mfma_f32_16x16x32_bf16 v[24:27], a[180:183], v[204:207], v[24:27]\n"
    "ds_write_addtid_b32 v177 offset:8448\n"
    "v_mfma_f32_16x16x32_bf16 v[48:51], a[180:183], v[212:215], v[48:51]\n"
    "ds_write_addtid_b32 v168 offset:4096\n"
    "v_mfma_f32_16x16x32_bf16 v[48:51], a[176:179], v[208:211], v[48:51]\n"
    "ds_write_addtid_b32 v169 offset:4352\n"
    "v_mfma_f32_16x16x32_bf16 v[128:131], a[176:179], v[216:219], v[128:131]\n"
    "ds_write_addtid_b32 v160 offset:0\n"
    "v_mfma_f32_16x16x32_bf16 v[128:131], a[180:183], v[220:223], v[128:131]\n"
    "ds_write_addtid_b32 v161 offset:256\n"
    "v_mfma_f32_16x16x32_bf16 v[132:135], a[180:183], v[228:231], v[132:135]\n"
    "ds_write_addtid_b32 v194 offset:16896\n"
    "v_mfma_f32_16x16x32_bf16 v[132:135], a[176:179], v[224:227], v[132:135]\n"
    "ds_write_addtid_b32 v195 offset:17152\n"
    "v_mfma_f32_16x16x32_bf16 v[136:139], a[176:179], v[232:235], v[136:139]\n"
    "ds_read_b128 a[176:179], v241 offset:20480\n"
    "v_mfma_f32_16x16x32_bf16 v[136:139], a[180:183], v[236:239], v[136:139]\n"
    "ds_read_b128 a[180:183], v242 offset:16384\n"
    "v_mfma_f32_16x16x32_bf16 v[148:151], a[184:187], v[232:235], v[148:151]\n"
    "ds_write_addtid_b32 v186 offset:12800\n"
    "v_mfma_f32_16x16x32_bf16 v[148:151], a[188:191], v[236:239], v[148:151]\n"
    "ds_write_addtid_b32 v187 offset:13056\n"
    "v_mfma_f32_16x16x32_bf16 v[144:147], a[188:191], v[228:231], v[144:147]\n"
    "ds_write_addtid_b32 v178 offset:8704\n"
    "v_mfma_f32_16x16x32_bf16 v[144:147], a[184:187], v[224:227], v[144:147]\n"
    "ds_write_addtid_b32 v179 offset:8960\n"
    "v_mfma_f32_16x16x32_bf16 v[140:143], a[184:187], v[216:219], v[140:143]\n"
    "s_sub_u32 s44, s44, 1\n"
    "ds_write_addtid_b32 v170 offset:4608\n"
    "v_mfma_f32_16x16x32_bf16 v[140:143], a[188:191], v[220:223], v[140:143]\n"
    "ds_write_addtid_b32 v171 offset:4864\n"
    "v_mfma_f32_16x16x32_bf16 v[52:55], a[188:191], v[212:215], v[52:55]\n"
    "ds_write_addtid_b32 v162 offset:512\n"
    "v_mfma_f32_16x16x32_bf16 v[52:55], a[184:187], v[208:211], v[52:55]\n"
    "ds_write_addtid_b32 v163 offset:768\n"
    "v_mfma_f32_16x16x32_bf16 v[28:31], a[184:187], v[200:203], v[28:31]\n"
    "ds_read_b128 a[184:187], v243 offset:16384\n"
    "v_mfma_f32_16x16x32_bf16 v[28:31], a[188:191], v[204:207], v[28:31]\n"
    "s_waitcnt lgkmcnt(15)\n"
    "ds_read_b128 a[188:191], v242 offset:20480\n"
    "s_cbranch_scc0 .Lat_p\n"
    "s_cmp_lg_u32 s37, 0\n"
    "s_cbranch_scc1 .Lat_tail\n"
    "s_waitcnt vmcnt(0)\n"
    "v_cmp_ne_u32_e32 vcc, s39, v154\n"
    "s_mov_b32 s37, 1\n"
    "s_bitcmp1_b32 %1, 0\n"
    "s_cselect_b32 s44, 5, 3\n"
    "s_nop 1\n"
    "s_cbranch_vccnz .Lat_lpoll\n"
    ".p2align 4\n"
    ".Lat_g:\n"
    "v_mfma_f32_16x16x32_bf16 v[200:203], a[160:163], a[28:31], 0\n"
    "v_mfma_f32_16x16x32_bf16 v[200:203], a[164:167], a[0:3], v[200:203]\n"
    "v_mfma_f32_16x16x32_bf16 v[208:211], a[164:167], a[32:35], 0\n"
    "v_mfma_f32_16x16x32_bf16 v[208:211], a[160:163], a[60:63], v[208:211]\n"
    "v_mfma_f32_16x16x32_bf16 v[216:219], a[160:163], a[92:95], 0\n"
    "v_mfma_f32_16x16x32_bf16 v[216:219], a[164:167], a[64:67], v[216:219]\n"
    "s_waitcnt lgkmcnt(0)\n"
    "v_mfma_f32_16x16x32_bf16 v[224:227], a[164:167], a[96:99], 0\n"
    "s_barrier\n"
    "v_mfma_f32_16x16x32_bf16 v[224:227], a[160:163], a[124:127], v[224:227]\n"
    "s_add_u32 m0, s43, 49152\n"
    "ds_read2st64_b32 v[164:165], v250 offset0:0 offset1:1\n"
    "v_mfma_f32_16x16x32_bf16 v[232:235], a[160:163], a[156:159], 0\n"
    "ds_read_b128 a[160:163], v243 offset:22528\n"
    "v_mfma_f32_16x16x32_bf16 v[232:235], a[164:167], a[128:131], v[232:235]\n"
    "ds_read_b128 a[164:167], v240 offset:18432\n"
    "v_mfma_f32_16x16x32_bf16 v[232:235], a[168:171], a[132:135], v[232:235]\n"
    "buffer_load_dwordx4 v248, s[8:11], s40 offen offset:0 sc1 lds\n"
    "v_mfma_f32_16x16x32_bf16 v[232:235], a[172:175], a[136:139], v[232:235]\n"
    "v_mfma_f32_16x16x32_bf16 v[224:227], a[172:175], a[104:107], v[224:227]\n"
    "v_mfma_f32_16x16x32_bf16 v[224:227], a[168:171], a[100:103], v[224:227]\n"
    "ds_read2st64_b32 v[166:167], v250 offset0:2 offset1:3\n"
    "v_mfma_f32_16x16x32_bf16 v[216:219], a[168:171], a[68:71], v[216:219]\n"
    "buffer_load_dwordx4 v248, s[8:11], s40 offen offset:1024 sc1 lds\n"
    "v_mfma_f32_16x16x32_bf16 v[216:219], a[172:175], a[72:75], v[216:219]\n"
    "v_mfma_f32_16x16x32_bf16 v[208:211], a[172:175], a[40:43], v[208:211]\n"
    "v_mfma_f32_16x16x32_bf16 v[208:211], a[168:171], a[36:39], v[208:211]\n"
    "ds_read2st64_b32 v[172:173], v250 offset0:16 offset1:17\n"
    "v_mfma_f32_16x16x32_bf16 v[200:203], a[168:171], a[4:7], v[200:203]\n"
    "ds_read_b128 a[168:171], v241 offset:18432\n"
    "buffer_load_dwordx4 v248, s[8:11], s40 offen offset:2048 sc1 lds\n"
    "v_mfma_f32_16x16x32_bf16 v[200:203], a[172:175], a[8:11], v[200:203]\n"
    "ds_read_b128 a[172:175], v240 offset:22528\n"
    "v_mfma_f32_16x16x32_bf16 v[200:203], a[176:179], a[12:15], v[200:203]\n"
    "ds_read2st64_b32 v[174:175], v250 offset0:18 offset1:19\n"
    "v_mfma_f32_16x16x32_bf16 v[200:203], a[180:183], a[16:19], v[200:203]\n"
    "v_mfma_f32_16x16x32_bf16 v[208:211], a[180:183], a[48:51], v[208:211]\n"
    "buffer_load_dwordx4 v248, s[8:11], s40 offen offset:3072 sc1 lds\n"
    "v_mfma_f32_16x16x32_bf16 v[208:211], a[176:179], a[44:47], v[208:211]\n"
    "s_add_u32 m0, s43, 53248\n"
    "v_mfma_f32_16x16x32_bf16 v[216:219], a[176:179], a[76:79], v[216:219]\n"
    "s_add_u32 s40, s40, 0x8000\n"
    "ds_read2st64_b32 v[180:181], v250 offset0:32 offset1:33\n"
    "v_mfma_f32_16x16x32_bf16 v[216:219], a[180:183], a[80:83], v[216:219]\n"
    "v_mfma_f32_16x16x32_bf16 v[224:227], a[180:183], a[112:115], v[224:227]\n"
    "buffer_load_dwordx4 v248, s[8:11], s41 offen offset:0 sc1 lds\n"
    "v_mfma_f32_16x16x32_bf16 v[224:227], a[176:179], a[108:111], v[224:227]\n"
    "ds_read2st64_b32 v[182:183], v250 offset0:34 offset1:35\n"
    "v_mfma_f32_16x16x32_bf16 v[232:235], a[176:179], a[140:143], v[232:235]\n"
    "ds_read_b128 a[176:179], v241 offset:22528\n"
    "v_mfma_f32_16x16x32_bf16 v[232:235], a[180:183], a[144:147], v[232:235]\n"
    "ds_read_b128 a[180:183], v242 offset:18432\n"
    "v_mfma_f32_16x16x32_bf16 v[232:235], a[184:187], a[148:151], v[232:235]\n"
    "buffer_load_dwordx4 v248, s[8:11], s41 offen offset:1024 sc1 lds\n"
    "v_mfma_f32_16x16x32_bf16 v[232:235], a[188:191], a[152:155], v[232:235]\n"
    "v_mfma_f32_16x16x32_bf16 v[224:227], a[188:191], a[120:123], v[224:227]\n"
    "ds_read2st64_b32 v[188:189], v250 offset0:48 offset1:49\n"
    "v_mfma_f32_16x16x32_bf16 v[224:227], a[184:187], a[116:119], v[224:227]\n"
    "v_mfma_f32_16x16x32_bf16 v[216:219], a[184:187], a[84:87], v[216:219]\n"
    "buffer_load_dwordx4 v248, s[8:11], s41 offen offset:2048 sc1 lds\n"
    "v_mfma_f32_16x16x32_bf16 v[216:219], a[188:191], a[88:91], v[216:219]\n"
    "ds_read2st64_b32 v[190:191], v250 offset0:50 offset1:51\n"
    "v_mfma_f32_16x16x32_bf16 v[208:211], a[188:191], a[56:59], v[208:211]\n"
    "ds_read_b64 v[254:255], v252 offset:0\n"
    "v_mfma_f32_16x16x32_bf16 v[208:211], a[184:187], a[52:55], v[208:211]\n"
    "ds_read2st64_b32 v[196:197], v250 offset0:64 offset1:65\n"
    "v_mfma_f32_16x16x32_bf16 v[200:203], a[184:187], a[20:23], v[200:203]\n"
    "ds_read_b128 a[184:187], v243 offset:18432\n"
    "buffer_load_dwordx4 v248, s[8:11], s41 offen offset:3072 sc1 lds\n"
    "v_mfma_f32_16x16x32_bf16 v[200:203], a[188:191], a[24:27], v[200:203]\n"
    "s_waitcnt lgkmcnt(15)\n"
    "s_add_u32 s41, s41, 0x8000\n"
    "ds_read_b128 a[188:191], v242 offset:22528\n"
    "v_mfma_f32_16x16x32_bf16 v[204:207], a[160:163], a[28:31], 0\n"
    "s_waitcnt lgkmcnt(3)\n"
    "v_mad_u32_u16 v253, v254, s42, v251 op_sel:[0,0,0,0]\n"
    "v_mfma_f32_16x16x32_bf16 v[204:207], a[164:167], a[0:3], v[204:207]\n"
    "buffer_load_dwordx4 a[200:203], v253, s[20:23], 0 offen offset:0 sc1\n"
    "v_mfma_f32_16x16x32_bf16 v[212:215], a[164:167], a[32:35], 0\n"
    "v_exp_f32_e32 v232, v232\n"
    "v_mfma_f32_16x16x32_bf16 v[212:215], a[160:163], a[60:63], v[212:215]\n"
    "ds_read2st64_b32 v[198:199], v250 offset0:66 offset1:67\n"
    "v_mfma_f32_16x16x32_bf16 v[220:223], a[160:163], a[92:95], 0\n"
    "v_mad_u32_u16 v253, v254, s42, v251 op_sel:[1,0,0,0]\n"
    "v_mfma_f32_16x16x32_bf16 v[220:223], a[164:167], a[64:67], v[220:223]\n"
    "buffer_load_dwordx4 a[204:207], v253, s[20:23], 0 offen offset:0 sc1\n"
    "v_mfma_f32_16x16x32_bf16 v[228:231], a[164:167], a[96:99], 0\n"
    "v_exp_f32_e32 v233, v233\n"
    "v_mfma_f32_16x16x32_bf16 v[228:231], a[160:163], a[124:127], v[228:231]\n"
    "v_exp_f32_e32 v234, v234\n"
    "v_mfma_f32_16x16x32_bf16 v[236:239], a[160:163], a[156:159], 0\n"
    "ds_read_b64_tr_b16 a[160:161], v244 offset:0\n"
    "v_mad_u32_u16 v253, v255, s42, v251 op_sel:[0,0,0,0]\n"
    "v_mfma_f32_16x16x32_bf16 v[236:239], a[164:167], a[128:131], v[236:239]\n"
    "ds_read_b64_tr_b16 a[162:163], v244 offset:2048\n"
    "buffer_load_dwordx4 a[208:211], v253, s[20:23], 0 offen offset:0 sc1\n"
    "v_mfma_f32_16x16x32_bf16 v[236:239], a[168:171], a[132:135], v[236:239]\n"
    "ds_read_b64_tr_b16 a[164:165], v244 offset:8192\n"
    "v_mfma_f32_16x16x32_bf16 v[236:239], a[172:175], a[136:139], v[236:239]\n"
    "ds_read_b64_tr_b16 a[166:167], v244 offset:10240\n"
    "v_mfma_f32_16x16x32_bf16 v[228:231], a[172:175], a[104:107], v[228:231]\n"
    "v_mad_u32_u16 v253, v255, s42, v251 op_sel:[1,0,0,0]\n"
    "v_mfma_f32_16x16x32_bf16 v[228:231], a[168:171], a[100:103], v[228:231]\n"
    "buffer_load_dwordx4 a[212:215], v253, s[20:23], 0 offen offset:0 sc1\n"
    "ds_read_b64 v[254:255], v252 offset:8\n"
    "v_mfma_f32_16x16x32_bf16 v[220:223], a[168:171], a[68:71], v[220:223]\n"
    "v_exp_f32_e32 v235, v235\n"
    "v_mfma_f32_16x16x32_bf16 v[220:223], a[172:175], a[72:75], v[220:223]\n"
    "v_cvt_pk_bf16_f32 v232, v232, v233\n"
    "v_mfma_f32_16x16x32_bf16 v[212:215], a[172:175], a[40:43], v[212:215]\n"
    "v_exp_f32_e32 v224, v224\n"
    "v_mfma_f32_16x16x32_bf16 v[212:215], a[168:171], a[36:39], v[212:215]\n"
    "v_exp_f32_e32 v225, v225\n"
    "v_mfma_f32_16x16x32_bf16 v[204:207], a[168:171], a[4:7], v[204:207]\n"
    "ds_read_b64_tr_b16 a[168:169], v245 offset:0\n"
    "v_cvt_pk_bf16_f32 v233, v234, v235\n"
    "v_mfma_f32_16x16x32_bf16 v[204:207], a[172:175], a[8:11], v[204:207]\n"
    "ds_read_b64_tr_b16 a[170:171], v245 offset:2048\n"
    "v_mfma_f32_16x16x32_bf16 v[204:207], a[176:179], a[12:15], v[204:207]\n"
    "s_waitcnt lgkmcnt(2)\n"
    "ds_read_b64_tr_b16 a[172:173], v245 offset:8192\n"
    "v_mad_u32_u16 v253, v254, s42, v251 op_sel:[0,0,0,0]\n"
    "v_mfma_f32_16x16x32_bf16 v[204:207], a[180:183], a[16:19], v[204:207]\n"
    "ds_read_b64_tr_b16 a[174:175], v245 offset:10240\n"
    "buffer_load_dwordx4 a[216:219], v253, s[20:23], 0 offen offset:0 sc1\n"
    "v_mfma_f32_16x16x32_bf16 v[212:215], a[180:183], a[48:51], v[212:215]\n"
    "v_exp_f32_e32 v226, v226\n"
    "v_mfma_f32_16x16x32_bf16 v[212:215], a[176:179], a[44:47], v[212:215]\n"
    "v_exp_f32_e32 v227, v227\n"
    "v_mfma_f32_16x16x32_bf16 v[220:223], a[176:179], a[76:79], v[220:223]\n"
    "v_mad_u32_u16 v253, v254, s42, v251 op_sel:[1,0,0,0]\n"
    "v_cvt_pk_bf16_f32 v224, v224, v225\n"
    "v_mfma_f32_16x16x32_bf16 v[220:223], a[180:183], a[80:83], v[220:223]\n"
    "buffer_load_dwordx4 a[220:223], v253, s[20:23], 0 offen offset:0 sc1\n"
    "v_mfma_f32_16x16x32_bf16 v[228:231], a[180:183], a[112:115], v[228:231]\n"
    "v_exp_f32_e32 v216, v216\n"
    "v_mfma_f32_16x16x32_bf16 v[228:231], a[176:179], a[108:111], v[228:231]\n"
    "v_exp_f32_e32 v217, v217\n"
    "v_mfma_f32_16x16x32_bf16 v[236:239], a[176:179], a[140:143], v[236:239]\n"
    "ds_read_b64_tr_b16 a[176:177], v246 offset:0\n"
    "v_cvt_pk_bf16_f32 v225, v226, v227\n"
    "v_mfma_f32_16x16x32_bf16 v[236:239], a[180:183], a[144:147], v[236:239]\n"
    "ds_read_b64_tr_b16 a[178:179], v246 offset:2048\n"
    "v_mfma_f32_16x16x32_bf16 v[236:239], a[184:187], a[148:151], v[236:239]\n"
    "ds_read_b64_tr_b16 a[180:181], v246 offset:8192\n"
    "v_mfma_f32_16x16x32_bf16 v[236:239], a[188:191], a[152:155], v[236:239]\n"
    "ds_read_b64_tr_b16 a[182:183], v246 offset:10240\n"
    "v_mfma_f32_16x16x32_bf16 v[228:231], a[188:191], a[120:123], v[228:231]\n"
    "ds_read_b64 v[254:255], v252 offset:32\n"
    "v_mfma_f32_16x16x32_bf16 v[228:231], a[184:187], a[116:119], v[228:231]\n"
    "v_exp_f32_e32 v218, v218\n"
    "v_mfma_f32_16x16x32_bf16 v[220:223], a[184:187], a[84:87], v[220:223]\n"
    "v_exp_f32_e32 v219, v219\n"
    "v_mfma_f32_16x16x32_bf16 v[220:223], a[188:191], a[88:91], v[220:223]\n"
    "v_cvt_pk_bf16_f32 v216, v216, v217\n"
    "v_mfma_f32_16x16x32_bf16 v[212:215], a[188:191], a[56:59], v[212:215]\n"
    "s_waitcnt lgkmcnt(0)\n"
    "v_mad_u32_u16 v253, v254, s42, v251 op_sel:[0,0,0,0]\n"
    "v_mfma_f32_16x16x32_bf16 v[212:215], a[184:187], a[52:55], v[212:215]\n"
    "buffer_load_dwordx4 a[224:227], v253, s[20:23], 0 offen offset:0 sc1\n"
    "v_mfma_f32_16x16x32_bf16 v[204:207], a[184:187], a[20:23], v[204:207]\n"
    "ds_read_b64_tr_b16 a[184:185], v247 offset:0\n"
    "v_mfma_f32_16x16x32_bf16 v[204:207], a[188:191], a[24:27], v[204:207]\n"
    "ds_read_b64_tr_b16 a[186:187], v247 offset:2048\n"
    "v_mfma_f32_16x16x32_bf16 v[0:3], a[160:163], v[160:163], v[0:3]\n"
    "ds_read_b64_tr_b16 a[188:189], v247 offset:8192\n"
    "v_mad_u32_u16 v253, v254, s42, v251 op_sel:[1,0,0,0]\n"
    "v_mfma_f32_16x16x32_bf16 v[0:3], a[164:167], v[164:167], v[0:3]\n"
    "ds_read_b64_tr_b16 a[190:191], v247 offset:10240\n"
    "buffer_load_dwordx4 a[228:231], v253, s[20:23], 0 offen offset:0 sc1\n"
    "v_mfma_f32_16x16x32_bf16 a[248:251], a[164:167], v[172:175], a[248:251]\n"
    "v_exp_f32_e32 v208, v208\n"
    "v_mfma_f32_16x16x32_bf16 a[248:251], a[160:163], v[168:171], a[248:251]\n"
    "v_exp_f32_e32 v209, v209\n"
    "v_mfma_f32_16x16x32_bf16 v[56:59], a[160:163], v[176:179], v[56:59]\n"
    "v_mad_u32_u16 v253, v255, s42, v251 op_sel:[0,0,0,0]\n"
    "v_cvt_pk_bf16_f32 v217, v218, v219\n"
    "v_mfma_f32_16x16x32_bf16 v[56:59], a[164:167], v[180:183], v[56:59]\n"
    "buffer_load_dwordx4 a[232:235], v253, s[20:23], 0 offen offset:0 sc1\n"
    "v_mfma_f32_16x16x32_bf16 v[60:63], a[164:167], v[188:191], v[60:63]\n"
    "v_exp_f32_e32 v210, v210\n"
    "v_mfma_f32_16x16x32_bf16 v[60:63], a[160:163], v[184:187], v[60:63]\n"
    "v_exp_f32_e32 v211, v211\n"
    "v_mfma_f32_16x16x32_bf16 v[64:67], a[160:163], v[192:195], v[64:67]\n"
    "ds_read_b64_tr_b16 a[160:161], v244 offset:4096\n"
    "v_mad_u32_u16 v253, v255, s42, v251 op_sel:[1,0,0,0]\n"
    "v_mfma_f32_16x16x32_bf16 v[64:67], a[164:167], v[196:199], v[64:67]\n"
    "ds_read_b64_tr_b16 a[162:163], v244 offset:6144\n"
    "buffer_load_dwordx4 a[236:239], v253, s[20:23], 0 offen offset:0 sc1\n"
    "v_mfma_f32_16x16x32_bf16 v[76:79], a[168:171], v[192:195], v[76:79]\n"
    "ds_read_b64_tr_b16 a[164:165], v244 offset:12288\n"
    "v_cvt_pk_bf16_f32 v208, v208, v209\n"
    "v_mfma_f32_16x16x32_bf16 v[76:79], a[172:175], v[196:199], v[76:79]\n"
    "ds_read_b64_tr_b16 a[166:167], v244 offset:14336\n"
    "v_mfma_f32_16x16x32_bf16 v[72:75], a[172:175], v[188:191], v[72:75]\n"
    "ds_read_b64 v[254:255], v252 offset:40\n"
    "v_mfma_f32_16x16x32_bf16 v[72:75], a[168:171], v[184:187], v[72:75]\n"
    "v_exp_f32_e32 v200, v200\n"
    "v_mfma_f32_16x16x32_bf16 v[68:71], a[168:171], v[176:179], v[68:71]\n"
    "v_exp_f32_e32 v201, v201\n"
    "v_mfma_f32_16x16x32_bf16 v[68:71], a[172:175], v[180:183], v[68:71]\n"
    "v_cvt_pk_bf16_f32 v209, v210, v211\n"
    "v_mfma_f32_16x16x32_bf16 a[252:255], a[172:175], v[172:175], a[252:255]\n"
    "v_exp_f32_e32 v202, v202\n"
    "v_mfma_f32_16x16x32_bf16 a[252:255], a[168:171], v[168:171], a[252:255]\n"
    "v_exp_f32_e32 v203, v203\n"
    "v_mfma_f32_16x16x32_bf16 v[4:7], a[168:171], v[160:163], v[4:7]\n"
    "s_waitcnt lgkmcnt(0)\n"
    "ds_read_b64_tr_b16 a[168:169], v245 offset:4096\n"
    "v_mad_u32_u16 v253, v254, s42, v251 op_sel:[0,0,0,0]\n"
    "v_mfma_f32_16x16x32_bf16 v[4:7], a[172:175], v[164:167], v[4:7]\n"
    "ds_read_b64_tr_b16 a[170:171], v245 offset:6144\n"
    "v_cvt_pk_bf16_f32 v200, v200, v201\n"
    "v_mfma_f32_16x16x32_bf16 v[8:11], a[176:179], v[160:163], v[8:11]\n"
    "ds_read_b64_tr_b16 a[172:173], v245 offset:12288\n"
    "buffer_load_dwordx4 a[240:243], v253, s[20:23], 0 offen offset:0 sc1\n"
    "v_mfma_f32_16x16x32_bf16 v[8:11], a[180:183], v[164:167], v[8:11]\n"
    "ds_read_b64_tr_b16 a[174:175], v245 offset:14336\n"
    "v_cvt_pk_bf16_f32 v201, v202, v203\n"
    "v_mfma_f32_16x16x32_bf16 v[32:35], a[180:183], v[172:175], v[32:35]\n"
    "v_exp_f32_e32 v236, v236\n"
    "v_mfma_f32_16x16x32_bf16 v[32:35], a[176:179], v[168:171], v[32:35]\n"
    "v_mad_u32_u16 v253, v254, s42, v251 op_sel:[1,0,0,0]\n"
    "v_mfma_f32_16x16x32_bf16 v[80:83], a[176:179], v[176:179], v[80:83]\n"
    "buffer_load_dwordx4 a[244:247], v253, s[20:23], 0 offen offset:0 sc1\n"
    "v_mfma_f32_16x16x32_bf16 v[80:83], a[180:183], v[180:183], v[80:83]\n"
    "v_exp_f32_e32 v237, v237\n"
    "v_mfma_f32_16x16x32_bf16 v[84:87], a[180:183], v[188:191], v[84:87]\n"
    "v_exp_f32_e32 v238, v238\n"
    "v_mfma_f32_16x16x32_bf16 v[84:87], a[176:179], v[184:187], v[84:87]\n"
    "v_exp_f32_e32 v239, v239\n"
    "v_mfma_f32_16x16x32_bf16 v[88:91], a[176:179], v[192:195], v[88:91]\n"
    "ds_read_b64_tr_b16 a[176:177], v246 offset:4096\n"
    "v_cvt_pk_bf16_f32 v234, v236, v237\n"
    "v_mfma_f32_16x16x32_bf16 v[88:91], a[180:183], v[196:199], v[88:91]\n"
    "ds_read_b64_tr_b16 a[178:179], v246 offset:6144\n"
    "v_exp_f32_e32 v228, v228\n"
    "v_mfma_f32_16x16x32_bf16 v[100:103], a[184:187], v[192:195], v[100:103]\n"
    "ds_read_b64_tr_b16 a[180:181], v246 offset:12288\n"
    "v_exp_f32_e32 v229, v229\n"
    "v_mfma_f32_16x16x32_bf16 v[100:103], a[188:191], v[196:199], v[100:103]\n"
    "ds_read_b64_tr_b16 a[182:183], v246 offset:14336\n"
    "v_cvt_pk_bf16_f32 v235, v238, v239\n"
    "v_mfma_f32_16x16x32_bf16 v[96:99], a[188:191], v[188:191], v[96:99]\n"
    "v_exp_f32_e32 v230, v230\n"
    "v_mfma_f32_16x16x32_bf16 v[96:99], a[184:187], v[184:187], v[96:99]\n"
    "v_exp_f32_e32 v231, v231\n"
    "v_cvt_pk_bf16_f32 v226, v228, v229\n"
    "v_mfma_f32_16x16x32_bf16 v[92:95], a[184:187], v[176:179], v[92:95]\n"
    "v_exp_f32_e32 v220, v220\n"
    "v_mfma_f32_16x16x32_bf16 v[92:95], a[188:191], v[180:183], v[92:95]\n"
    "v_exp_f32_e32 v221, v221\n"
    "v_mfma_f32_16x16x32_bf16 v[36:39], a[188:191], v[172:175], v[36:39]\n"
    "v_cvt_pk_bf16_f32 v227, v230, v231\n"
    "v_mfma_f32_16x16x32_bf16 v[36:39], a[184:187], v[168:171], v[36:39]\n"
    "v_exp_f32_e32 v222, v222\n"
    "v_mfma_f32_16x16x32_bf16 v[12:15], a[184:187], v[160:163], v[12:15]\n"
    "s_mov_b32 m0, s49\n"
    "ds_read_b64_tr_b16 a[184:185], v247 offset:4096\n"
    "v_mfma_f32_16x16x32_bf16 v[12:15], a[188:191], v[164:167], v[12:15]\n"
    "s_waitcnt lgkmcnt(5)\n"
    "ds_read_b64_tr_b16 a[186:187], v247 offset:6144\n"
    "v_mfma_f32_16x16x32_bf16 v[16:19], a[160:163], v[160:163], v[16:19]\n"
    "ds_read_b64_tr_b16 a[188:189], v247 offset:12288\n"
    "v_mfma_f32_16x16x32_bf16 v[16:19], a[164:167], v[164:167], v[16:19]\n"
    "ds_read_b64_tr_b16 a[190:191], v247 offset:14336\n"
    "v_mfma_f32_16x16x32_bf16 v[40:43], a[164:167], v[172:175], v[40:43]\n"
    "v_exp_f32_e32 v223, v223\n"
    "v_mfma_f32_16x16x32_bf16 v[40:43], a[160:163], v[168:171], v[40:43]\n"
    "v_cvt_pk_bf16_f32 v218, v220, v221\n"
    "v_mfma_f32_16x16x32_bf16 v[104:107], a[160:163], v[176:179], v[104:107]\n"
    "v_exp_f32_e32 v212, v212\n"
    "v_mfma_f32_16x16x32_bf16 v[104:107], a[164:167], v[180:183], v[104:107]\n"
    "s_waitcnt vmcnt(20)\n"
    "v_mfma_f32_16x16x32_bf16 v[108:111], a[164:167], v[188:191], v[108:111]\n"
    "s_barrier\n"
    "v_mfma_f32_16x16x32_bf16 v[108:111], a[160:163], v[184:187], v[108:111]\n"
    "v_exp_f32_e32 v213, v213\n"
    "v_mfma_f32_16x16x32_bf16 v[112:115], a[160:163], v[192:195], v[112:115]\n"
    "ds_read_b128 a[160:163], v243 offset:36864\n"
    "v_cvt_pk_bf16_f32 v219, v222, v223\n"
    "v_mfma_f32_16x16x32_bf16 v[112:115], a[164:167], v[196:199], v[112:115]\n"
    "ds_read_b128 a[164:167], v240 offset:32768\n"
    "ds_write_addtid_b32 v232 offset:16384\n"
    "v_mfma_f32_16x16x32_bf16 v[124:127], a[168:171], v[192:195], v[124:127]\n"
    "v_exp_f32_e32 v214, v214\n"
    "v_mfma_f32_16x16x32_bf16 v[124:127], a[172:175], v[196:199], v[124:127]\n"
    "v_exp_f32_e32 v215, v215\n"
    "v_mfma_f32_16x16x32_bf16 v[120:123], a[172:175], v[188:191], v[120:123]\n"
    "ds_write_addtid_b32 v233 offset:16640\n"
    "v_cvt_pk_bf16_f32 v210, v212, v213\n"
    "v_mfma_f32_16x16x32_bf16 v[120:123], a[168:171], v[184:187], v[120:123]\n"
    "v_exp_f32_e32 v204, v204\n"
    "v_mfma_f32_16x16x32_bf16 v[116:119], a[168:171], v[176:179], v[116:119]\n"
    "v_exp_f32_e32 v205, v205\n"
    "v_mfma_f32_16x16x32_bf16 v[116:119], a[172:175], v[180:183], v[116:119]\n"
    "ds_write_addtid_b32 v224 offset:12288\n"
    "v_cvt_pk_bf16_f32 v211, v214, v215\n"
    "v_mfma_f32_16x16x32_bf16 v[44:47], a[172:175], v[172:175], v[44:47]\n"
    "v_exp_f32_e32 v206, v206\n"
    "v_mfma_f32_16x16x32_bf16 v[44:47], a[168:171], v[168:171], v[44:47]\n"
    "v_exp_f32_e32 v207, v207\n"
    "v_mfma_f32_16x16x32_bf16 v[20:23], a[168:171], v[160:163], v[20:23]\n"
    "ds_read_b128 a[168:171], v241 offset:32768\n"
    "v_cvt_pk_bf16_f32 v202, v204, v205\n"
    "v_mfma_f32_16x16x32_bf16 v[20:23], a[172:175], v[164:167], v[20:23]\n"
    "s_waitcnt lgkmcnt(6)\n"
    "ds_read_b128 a[172:175], v240 offset:36864\n"
    "v_cvt_pk_bf16_f32 v203, v206, v207\n"
    "v_mfma_f32_16x16x32_bf16 v[24:27], a[176:179], v[160:163], v[24:27]\n"
    "ds_write_addtid_b32 v225 offset:12544\n"
    "ds_write_addtid_b32 v216 offset:8192\n"
    "v_mfma_f32_16x16x32_bf16 v[24:27], a[180:183], v[164:167], v[24:27]\n"
    "ds_write_addtid_b32 v217 offset:8448\n"
    "v_mfma_f32_16x16x32_bf16 v[48:51], a[180:183], v[172:175], v[48:51]\n"
    "ds_write_addtid_b32 v208 offset:4096\n"
    "v_mfma_f32_16x16x32_bf16 v[48:51], a[176:179], v[168:171], v[48:51]\n"
    "ds_write_addtid_b32 v209 offset:4352\n"
    "v_mfma_f32_16x16x32_bf16 v[128:131], a[176:179], v[176:179], v[128:131]\n"
    "ds_write_addtid_b32 v200 offset:0\n"
    "v_mfma_f32_16x16x32_bf16 v[128:131], a[180:183], v[180:183], v[128:131]\n"
    "ds_write_addtid_b32 v201 offset:256\n"
    "v_mfma_f32_16x16x32_bf16 v[132:135], a[180:183], v[188:191], v[132:135]\n"
    "ds_write_addtid_b32 v234 offset:16896\n"
    "v_mfma_f32_16x16x32_bf16 v[132:135], a[176:179], v[184:187], v[132:135]\n"
    "ds_write_addtid_b32 v235 offset:17152\n"
    "v_mfma_f32_16x16x32_bf16 v[136:139], a[176:179], v[192:195], v[136:139]\n"
    "ds_read_b128 a[176:179], v241 offset:36864\n"
    "v_mfma_f32_16x16x32_bf16 v[136:139], a[180:183], v[196:199], v[136:139]\n"
    "ds_read_b128 a[180:183], v242 offset:32768\n"
    "v_mfma_f32_16x16x32_bf16 v[148:151], a[184:187], v[192:195], v[148:151]\n"
    "ds_write_addtid_b32 v226 offset:12800\n"
    "v_mfma_f32_16x16x32_bf16 v[148:151], a[188:191], v[196:199], v[148:151]\n"
    "ds_write_addtid_b32 v227 offset:13056\n"
    "v_mfma_f32_16x16x32_bf16 v[144:147], a[188:191], v[188:191], v[144:147]\n"
    "ds_write_addtid_b32 v218 offset:8704\n"
    "v_mfma_f32_16x16x32_bf16 v[144:147], a[184:187], v[184:187], v[144:147]\n"
    "ds_write_addtid_b32 v219 offset:8960\n"
    "v_mfma_f32_16x16x32_bf16 v[140:143], a[184:187], v[176:179], v[140:143]\n"
    "ds_write_addtid_b32 v210 offset:4608\n"
    "v_mfma_f32_16x16x32_bf16 v[140:143], a[188:191], v[180:183], v[140:143]\n"
    "ds_write_addtid_b32 v211 offset:4864\n"
    "v_mfma_f32_16x16x32_bf16 v[52:55], a[188:191], v[172:175], v[52:55]\n"
    "ds_write_addtid_b32 v202 offset:512\n"
    "v_mfma_f32_16x16x32_bf16 v[52:55], a[184:187], v[168:171], v[52:55]\n"
    "ds_write_addtid_b32 v203 offset:768\n"
    "v_mfma_f32_16x16x32_bf16 v[28:31], a[184:187], v[160:163], v[28:31]\n"
    "ds_read_b128 a[184:187], v243 offset:32768\n"
    "v_mfma_f32_16x16x32_bf16 v[28:31], a[188:191], v[164:167], v[28:31]\n"
    "s_waitcnt lgkmcnt(15)\n"
    "ds_read_b128 a[188:191], v242 offset:36864\n"
    "v_mfma_f32_16x16x32_bf16 v[160:163], a[160:163], a[28:31], 0\n"
    "v_mfma_f32_16x16x32_bf16 v[160:163], a[164:167], a[0:3], v[160:163]\n"
    "v_mfma_f32_16x16x32_bf16 v[168:171], a[164:167], a[32:35], 0\n"
    "v_mfma_f32_16x16x32_bf16 v[168:171], a[160:163], a[60:63], v[168:171]\n"
    "v_mfma_f32_16x16x32_bf16 v[176:179], a[160:163], a[92:95], 0\n"
    "v_mfma_f32_16x16x32_bf16 v[176:179], a[164:167], a[64:67], v[176:179]\n"
    "s_waitcnt lgkmcnt(0)\n"
    "v_mfma_f32_16x16x32_bf16 v[184:187], a[164:167], a[96:99], 0\n"
    "s_barrier\n"
    "v_mfma_f32_16x16x32_bf16 v[184:187], a[160:163], a[124:127], v[184:187]\n"
    "s_add_u32 m0, s43, 0\n"
    "v_mfma_f32_16x16x32_bf16 v[192:195], a[160:163], a[156:159], 0\n"
    "ds_read_b128 a[160:163], v243 offset:38912\n"
    "v_mfma_f32_16x16x32_bf16 v[192:195], a[164:167], a[128:131], v[192:195]\n"
    "ds_read_b128 a[164:167], v240 offset:34816\n"
    "v_mfma_f32_16x16x32_bf16 v[192:195], a[168:171], a[132:135], v[192:195]\n"
    "buffer_load_dwordx4 v248, s[8:11], s40 offen offset:0 sc1 lds\n"
    "v_mfma_f32_16x16x32_bf16 v[192:195], a[172:175], a[136:139], v[192:195]\n"
    "ds_read2st64_b32 v[204:205], v250 offset0:0 offset1:1\n"
    "v_mfma_f32_16x16x32_bf16 v[184:187], a[172:175], a[104:107], v[184:187]\n"
    "s_waitcnt vmcnt(11)\n"
    "v_smfmac_f32_16x16x64_bf16 v[156:159], a[192:195], a[200:207], v152\n"
    "v_mfma_f32_16x16x32_bf16 v[184:187], a[168:171], a[100:103], v[184:187]\n"
    "ds_read2st64_b32 v[206:207], v250 offset0:2 offset1:3\n"
    "v_mfma_f32_16x16x32_bf16 v[176:179], a[168:171], a[68:71], v[176:179]\n"
    "buffer_load_dwordx4 v248, s[8:11], s40 offen offset:1024 sc1 lds\n"
    "v_mfma_f32_16x16x32_bf16 v[176:179], a[172:175], a[72:75], v[176:179]\n"
    "v_mfma_f32_16x16x32_bf16 v[168:171], a[172:175], a[40:43], v[168:171]\n"
    "s_waitcnt vmcnt(10)\n"
    "v_smfmac_f32_16x16x64_bf16 v[156:159], a[192:195], a[208:215], v152\n"
    "ds_read2st64_b32 v[212:213], v250 offset0:16 offset1:17\n"
    "v_mfma_f32_16x16x32_bf16 v[168:171], a[168:171], a[36:39], v[168:171]\n"
    "v_mfma_f32_16x16x32_bf16 v[160:163], a[168:171], a[4:7], v[160:163]\n"
    "ds_read_b128 a[168:171], v241 offset:34816\n"
    "buffer_load_dwordx4 v248, s[8:11], s40 offen offset:2048 sc1 lds\n"
    "v_mfma_f32_16x16x32_bf16 v[160:163], a[172:175], a[8:11], v[160:163]\n"
    "ds_read_b128 a[172:175], v240 offset:38912\n"
    "v_mfma_f32_16x16x32_bf16 v[160:163], a[176:179], a[12:15], v[160:163]\n"
    "s_waitcnt vmcnt(9)\n"
    "ds_read2st64_b32 v[214:215], v250 offset0:18 offset1:19\n"
    "v_smfmac_f32_16x16x64_bf16 v[156:159], a[192:195], a[216:223], v152\n"
    "v_mfma_f32_16x16x32_bf16 v[160:163], a[180:183], a[16:19], v[160:163]\n"
    "v_mfma_f32_16x16x32_bf16 v[168:171], a[180:183], a[48:51], v[168:171]\n"
    "buffer_load_dwordx4 v248, s[8:11], s40 offen offset:3072 sc1 lds\n"
    "v_mfma_f32_16x16x32_bf16 v[168:171], a[176:179], a[44:47], v[168:171]\n"
    "s_add_u32 m0, s43, 4096\n"
    "v_mfma_f32_16x16x32_bf16 v[176:179], a[176:179], a[76:79], v[176:179]\n"
    "s_add_u32 s40, s40, 0x8000\n"
    "ds_read2st64_b32 v[220:221], v250 offset0:32 offset1:33\n"
    "v_mfma_f32_16x16x32_bf16 v[176:179], a[180:183], a[80:83], v[176:179]\n"
    "v_mfma_f32_16x16x32_bf16 v[184:187], a[180:183], a[112:115], v[184:187]\n"
    "v_mfma_f32_16x16x32_bf16 v[184:187], a[176:179], a[108:111], v[184:187]\n"
    "buffer_load_dwordx4 v248, s[8:11], s41 offen offset:0 sc1 lds\n"
    "v_mfma_f32_16x16x32_bf16 v[192:195], a[176:179], a[140:143], v[192:195]\n"
    "ds_read_b128 a[176:179], v241 offset:38912\n"
    "v_mfma_f32_16x16x32_bf16 v[192:195], a[180:183], a[144:147], v[192:195]\n"
    "ds_read_b128 a[180:183], v242 offset:34816\n"
    "v_mfma_f32_16x16x32_bf16 v[192:195], a[184:187], a[148:151], v[192:195]\n"
    "ds_read2st64_b32 v[222:223], v250 offset0:34 offset1:35\n"
    "v_mfma_f32_16x16x32_bf16 v[192:195], a[188:191], a[152:155], v[192:195]\n"
    "v_mfma_f32_16x16x32_bf16 v[184:187], a[188:191], a[120:123], v[184:187]\n"
    "buffer_load_dwordx4 v248, s[8:11], s41 offen offset:1024 sc1 lds\n"
    "v_mfma_f32_16x16x32_bf16 v[184:187], a[184:187], a[116:119], v[184:187]\n"
    "ds_read2st64_b32 v[228:229], v250 offset0:48 offset1:49\n"
    "v_mfma_f32_16x16x32_bf16 v[176:179], a[184:187], a[84:87], v[176:179]\n"
    "s_waitcnt vmcnt(10)\n"
    "v_exp_f32_e32 v192, v192\n"
    "v_smfmac_f32_16x16x64_bf16 v[156:159], a[192:195], a[224:231], v152\n"
    "v_exp_f32_e32 v193, v193\n"
    "v_mfma_f32_16x16x32_bf16 v[176:179], a[188:191], a[88:91], v[176:179]\n"
    "v_exp_f32_e32 v194, v194\n"
    "v_mfma_f32_16x16x32_bf16 v[168:171], a[188:191], a[56:59], v[168:171]\n"
    "buffer_load_dwordx4 v248, s[8:11], s41 offen offset:2048 sc1 lds\n"
    "v_mfma_f32_16x16x32_bf16 v[168:171], a[184:187], a[52:55], v[168:171]\n"
    "ds_read2st64_b32 v[230:231], v250 offset0:50 offset1:51\n"
    "v_mfma_f32_16x16x32_bf16 v[160:163], a[184:187], a[20:23], v[160:163]\n"
    "s_waitcnt vmcnt(9)\n"
    "ds_read_b128 a[184:187], v243 offset:34816\n"
    "v_smfmac_f32_16x16x64_bf16 v[156:159], a[192:195], a[232:239], v152\n"
    "ds_read2st64_b32 v[236:237], v250 offset0:64 offset1:65\n"
    "v_mfma_f32_16x16x32_bf16 v[160:163], a[188:191], a[24:27], v[160:163]\n"
    "s_waitcnt lgkmcnt(9)\n"
    "ds_read_b128 a[188:191], v242 offset:38912\n"
    "v_mfma_f32_16x16x32_bf16 v[164:167], a[160:163], a[28:31], 0\n"
    "buffer_load_dwordx4 v248, s[8:11], s41 offen offset:3072 sc1 lds\n"
    "v_mfma_f32_16x16x32_bf16 v[164:167], a[164:167], a[0:3], v[164:167]\n"
    "s_add_u32 s41, s41, 0x8000\n"
    "v_exp_f32_e32 v195, v195\n"
    "v_mfma_f32_16x16x32_bf16 v[172:175], a[164:167], a[32:35], 0\n"
    "s_waitcnt vmcnt(8)\n"
    "v_cvt_pk_bf16_f32 v192, v192, v193\n"
    "v_smfmac_f32_16x16x64_bf16 v[156:159], a[192:195], a[240:247], v152\n"
    "v_exp_f32_e32 v184, v184\n"
    "v_mfma_f32_16x16x32_bf16 v[172:175], a[160:163], a[60:63], v[172:175]\n"
    "ds_read2st64_b32 v[238:239], v250 offset0:66 offset1:67\n"
    "v_mfma_f32_16x16x32_bf16 v[180:183], a[160:163], a[92:95], 0\n"
    "v_exp_f32_e32 v185, v185\n"
    "v_mfma_f32_16x16x32_bf16 v[180:183], a[164:167], a[64:67], v[180:183]\n"
    "v_cvt_pk_bf16_f32 v193, v194, v195\n"
    "v_mfma_f32_16x16x32_bf16 v[188:191], a[164:167], a[96:99], 0\n"
    "v_exp_f32_e32 v186, v186\n"
    "v_mfma_f32_16x16x32_bf16 v[188:191], a[160:163], a[124:127], v[188:191]\n"
    "v_exp_f32_e32 v187, v187\n"
    "v_mfma_f32_16x16x32_bf16 v[196:199], a[160:163], a[156:159], 0\n"
    "ds_read_b64_tr_b16 a[160:161], v244 offset:16384\n"
    "v_and_b32_e32 v253, 0xffff0000, v255\n"
    "v_mfma_f32_16x16x32_bf16 v[196:199], a[164:167], a[128:131], v[196:199]\n"
    "ds_read_b64_tr_b16 a[162:163], v244 offset:18432\n"
    "v_mfma_f32_16x16x32_bf16 v[196:199], a[168:171], a[132:135], v[196:199]\n"
    "ds_read_b64_tr_b16 a[164:165], v244 offset:24576\n"
    "v_mfma_f32_16x16x32_bf16 v[196:199], a[172:175], a[136:139], v[196:199]\n"
    "ds_read_b64_tr_b16 a[166:167], v244 offset:26624\n"
    "v_mfma_f32_16x16x32_bf16 v[188:191], a[172:175], a[104:107], v[188:191]\n"
    "v_cvt_pk_bf16_f32 v184, v184, v185\n"
    "v_mfma_f32_16x16x32_bf16 v[188:191], a[168:171], a[100:103], v[188:191]\n"
    "v_exp_f32_e32 v176, v176\n"
    "v_mfma_f32_16x16x32_bf16 v[180:183], a[168:171], a[68:71], v[180:183]\n"
    "v_exp_f32_e32 v177, v177\n"
    "v_mfma_f32_16x16x32_bf16 v[180:183], a[172:175], a[72:75], v[180:183]\n"
    "v_cvt_pk_bf16_f32 v185, v186, v187\n"
    "v_mul_f32_e32 v156, v253, v156\n"
    "v_mfma_f32_16x16x32_bf16 v[172:175], a[172:175], a[40:43], v[172:175]\n"
    "v_exp_f32_e32 v178, v178\n"
    "v_mfma_f32_16x16x32_bf16 v[172:175], a[168:171], a[36:39], v[172:175]\n"
    "v_exp_f32_e32 v179, v179\n"
    "v_mfma_f32_16x16x32_bf16 v[164:167], a[168:171], a[4:7], v[164:167]\n"
    "ds_read_b64_tr_b16 a[168:169], v245 offset:16384\n"
    "v_mfma_f32_16x16x32_bf16 v[164:167], a[172:175], a[8:11], v[164:167]\n"
    "s_waitcnt lgkmcnt(6)\n"
    "ds_read_b64_tr_b16 a[170:171], v245 offset:18432\n"
    "v_mfma_f32_16x16x32_bf16 v[164:167], a[176:179], a[12:15], v[164:167]\n"
    "ds_read_b64_tr_b16 a[172:173], v245 offset:24576\n"
    "v_mfma_f32_16x16x32_bf16 v[164:167], a[180:183], a[16:19], v[164:167]\n"
    "ds_read_b64_tr_b16 a[174:175], v245 offset:26624\n"
    "v_mfma_f32_16x16x32_bf16 v[172:175], a[180:183], a[48:51], v[172:175]\n"
    "v_cvt_pk_bf16_f32 v176, v176, v177\n"
    "v_mfma_f32_16x16x32_bf16 v[172:175], a[176:179], a[44:47], v[172:175]\n"
    "v_exp_f32_e32 v168, v168\n"
    "v_mfma_f32_16x16x32_bf16 v[180:183], a[176:179], a[76:79], v[180:183]\n"
    "v_mul_f32_e32 v157, v253, v157\n"
    "v_mfma_f32_16x16x32_bf16 v[180:183], a[180:183], a[80:83], v[180:183]\n"
    "v_exp_f32_e32 v169, v169\n"
    "v_mfma_f32_16x16x32_bf16 v[188:191], a[180:183], a[112:115], v[188:191]\n"
    "v_cvt_pk_bf16_f32 v177, v178, v179\n"
    "v_mfma_f32_16x16x32_bf16 v[188:191], a[176:179], a[108:111], v[188:191]\n"
    "v_exp_f32_e32 v170, v170\n"
    "v_mfma_f32_16x16x32_bf16 v[196:199], a[176:179], a[140:143], v[196:199]\n"
    "ds_read_b64_tr_b16 a[176:177], v246 offset:16384\n"
    "v_mul_f32_e32 v158, v253, v158\n"
    "v_mfma_f32_16x16x32_bf16 v[196:199], a[180:183], a[144:147], v[196:199]\n"
    "ds_read_b64_tr_b16 a[178:179], v246 offset:18432\n"
    "v_mfma_f32_16x16x32_bf16 v[196:199], a[184:187], a[148:151], v[196:199]\n"
    "ds_read_b64_tr_b16 a[180:181], v246 offset:24576\n"
    "v_mfma_f32_16x16x32_bf16 v[196:199], a[188:191], a[152:155], v[196:199]\n"
    "ds_read_b64_tr_b16 a[182:183], v246 offset:26624\n"
    "v_mfma_f32_16x16x32_bf16 v[188:191], a[188:191], a[120:123], v[188:191]\n"
    "v_exp_f32_e32 v171, v171\n"
    "v_mfma_f32_16x16x32_bf16 v[188:191], a[184:187], a[116:119], v[188:191]\n"
    "v_cvt_pk_bf16_f32 v168, v168, v169\n"
    "v_mul_f32_e32 v159, v253, v159\n"
    "v_mfma_f32_16x16x32_bf16 v[180:183], a[184:187], a[84:87], v[180:183]\n"
    "v_exp_f32_e32 v160, v160\n"
    "v_mfma_f32_16x16x32_bf16 v[180:183], a[188:191], a[88:91], v[180:183]\n"
    "v_exp_f32_e32 v161, v161\n"
    "v_mfma_f32_16x16x32_bf16 v[172:175], a[188:191], a[56:59], v[172:175]\n"
    "v_cvt_pk_bf16_f32 v169, v170, v171\n"
    "v_mfma_f32_16x16x32_bf16 v[172:175], a[184:187], a[52:55], v[172:175]\n"
    "v_exp_f32_e32 v162, v162\n"
    "v_mfma_f32_16x16x32_bf16 v[164:167], a[184:187], a[20:23], v[164:167]\n"
    "ds_read_b64_tr_b16 a[184:185], v247 offset:16384\n"
    "v_mfma_f32_16x16x32_bf16 v[164:167], a[188:191], a[24:27], v[164:167]\n"
    "s_waitcnt lgkmcnt(5)\n"
    "ds_read_b64_tr_b16 a[186:187], v247 offset:18432\n"
    "v_mfma_f32_16x16x32_bf16 v[0:3], a[160:163], v[200:203], v[0:3]\n"
    "ds_read_b64_tr_b16 a[188:189], v247 offset:24576\n"
    "v_mad_u32_u16 v253, v255, s42, v153 op_sel:[0,0,0,0]\n"
    "v_mfma_f32_16x16x32_bf16 v[0:3], a[164:167], v[204:207], v[0:3]\n"
    "ds_read_b64_tr_b16 a[190:191], v247 offset:26624\n"
    "v_mfma_f32_16x16x32_bf16 a[248:251], a[164:167], v[212:215], a[248:251]\n"
    "v_exp_f32_e32 v163, v163\n"
    "v_mfma_f32_16x16x32_bf16 a[248:251], a[160:163], v[208:211], a[248:251]\n"
    "v_cvt_pk_bf16_f32 v160, v160, v161\n"
    "v_mfma_f32_16x16x32_bf16 v[56:59], a[160:163], v[216:219], v[56:59]\n"
    "v_cvt_pk_bf16_f32 v154, v156, v157\n"
    "v_mfma_f32_16x16x32_bf16 v[56:59], a[164:167], v[220:223], v[56:59]\n"
    "v_cvt_pk_bf16_f32 v161, v162, v163\n"
    "v_mfma_f32_16x16x32_bf16 v[60:63], a[164:167], v[228:231], v[60:63]\n"
    "v_exp_f32_e32 v196, v196\n"
    "v_mfma_f32_16x16x32_bf16 v[60:63], a[160:163], v[224:227], v[60:63]\n"
    "v_exp_f32_e32 v197, v197\n"
    "v_mfma_f32_16x16x32_bf16 v[64:67], a[160:163], v[232:235], v[64:67]\n"
    "ds_read_b64_tr_b16 a[160:161], v244 offset:20480\n"
    "v_mfma_f32_16x16x32_bf16 v[64:67], a[164:167], v[236:239], v[64:67]\n"
    "ds_read_b64_tr_b16 a[162:163], v244 offset:22528\n"
    "v_mfma_f32_16x16x32_bf16 v[76:79], a[168:171], v[232:235], v[76:79]\n"
    "ds_read_b64_tr_b16 a[164:165], v244 offset:28672\n"
    "v_mfma_f32_16x16x32_bf16 v[76:79], a[172:175], v[236:239], v[76:79]\n"
    "ds_read_b64_tr_b16 a[166:167], v244 offset:30720\n"
    "v_mfma_f32_16x16x32_bf16 v[72:75], a[172:175], v[228:231], v[72:75]\n"
    "v_exp_f32_e32 v198, v198\n"
    "v_mfma_f32_16x16x32_bf16 v[72:75], a[168:171], v[224:227], v[72:75]\n"
    "v_exp_f32_e32 v199, v199\n"
    "v_mfma_f32_16x16x32_bf16 v[68:71], a[168:171], v[216:219], v[68:71]\n"
    "v_cvt_pk_bf16_f32 v194, v196, v197\n"
    "v_cvt_pk_bf16_f32 v155, v158, v159\n"
    "v_mfma_f32_16x16x32_bf16 v[68:71], a[172:175], v[220:223], v[68:71]\n"
    "v_exp_f32_e32 v188, v188\n"
    "v_mfma_f32_16x16x32_bf16 a[252:255], a[172:175], v[212:215], a[252:255]\n"
    "v_exp_f32_e32 v189, v189\n"
    "v_mfma_f32_16x16x32_bf16 a[252:255], a[168:171], v[208:211], a[252:255]\n"
    "v_cvt_pk_bf16_f32 v195, v198, v199\n"
    "v_mfma_f32_16x16x32_bf16 v[4:7], a[168:171], v[200:203], v[4:7]\n"
    "ds_read_b64_tr_b16 a[168:169], v245 offset:20480\n"
    "v_mfma_f32_16x16x32_bf16 v[4:7], a[172:175], v[204:207], v[4:7]\n"
    "s_waitcnt lgkmcnt(5)\n"
    "ds_read_b64_tr_b16 a[170:171], v245 offset:22528\n"
    "v_mfma_f32_16x16x32_bf16 v[8:11], a[176:179], v[200:203], v[8:11]\n"
    "ds_read_b64_tr_b16 a[172:173], v245 offset:28672\n"
    "v_mov_b32_e32 v156, 0\n"
    "v_mfma_f32_16x16x32_bf16 v[8:11], a[180:183], v[204:207], v[8:11]\n"
    "ds_read_b64_tr_b16 a[174:175], v245 offset:30720\n"
    "v_mfma_f32_16x16x32_bf16 v[32:35], a[180:183], v[212:215], v[32:35]\n"
    "v_exp_f32_e32 v190, v190\n"
    "v_mfma_f32_16x16x32_bf16 v[32:35], a[176:179], v[208:211], v[32:35]\n"
    "v_exp_f32_e32 v191, v191\n"
    "v_mfma_f32_16x16x32_bf16 v[80:83], a[176:179], v[216:219], v[80:83]\n"
    "v_mov_b32_e32 v157, 0\n"
    "v_mfma_f32_16x16x32_bf16 v[80:83], a[180:183], v[220:223], v[80:83]\n"
    "v_cvt_pk_bf16_f32 v186, v188, v189\n"
    "v_mfma_f32_16x16x32_bf16 v[84:87], a[180:183], v[228:231], v[84:87]\n"
    "v_exp_f32_e32 v180, v180\n"
    "v_mfma_f32_16x16x32_bf16 v[84:87], a[176:179], v[224:227], v[84:87]\n"
    "v_exp_f32_e32 v181, v181\n"
    "v_mfma_f32_16x16x32_bf16 v[88:91], a[176:179], v[232:235], v[88:91]\n"
    "ds_read_b64_tr_b16 a[176:177], v246 offset:20480\n"
    "v_mfma_f32_16x16x32_bf16 v[88:91], a[180:183], v[236:239], v[88:91]\n"
    "ds_read_b64_tr_b16 a[178:179], v246 offset:22528\n"
    "v_mfma_f32_16x16x32_bf16 v[100:103], a[184:187], v[232:235], v[100:103]\n"
    "ds_read_b64_tr_b16 a[180:181], v246 offset:28672\n"
    "v_mfma_f32_16x16x32_bf16 v[100:103], a[188:191], v[236:239], v[100:103]\n"
    "ds_read_b64_tr_b16 a[182:183], v246 offset:30720\n"
    "v_mfma_f32_16x16x32_bf16 v[96:99], a[188:191], v[228:231], v[96:99]\n"
    "v_cvt_pk_bf16_f32 v187, v190, v191\n"
    "v_mov_b32_e32 v158, 0\n"
    "v_mfma_f32_16x16x32_bf16 v[96:99], a[184:187], v[224:227], v[96:99]\n"
    "v_exp_f32_e32 v182, v182\n"
    "v_mfma_f32_16x16x32_bf16 v[92:95], a[184:187], v[216:219], v[92:95]\n"
    "v_exp_f32_e32 v183, v183\n"
    "v_mfma_f32_16x16x32_bf16 v[92:95], a[188:191], v[220:223], v[92:95]\n"
    "v_cvt_pk_bf16_f32 v178, v180, v181\n"
    "v_mfma_f32_16x16x32_bf16 v[36:39], a[188:191], v[212:215], v[36:39]\n"
    "v_exp_f32_e32 v172, v172\n"
    "v_mfma_f32_16x16x32_bf16 v[36:39], a[184:187], v[208:211], v[36:39]\n"
    "v_exp_f32_e32 v173, v173\n"
    "v_mfma_f32_16x16x32_bf16 v[12:15], a[184:187], v[200:203], v[12:15]\n"
    "s_mov_b32 m0, s49\n"
    "ds_read_b64_tr_b16 a[184:185], v247 offset:20480\n"
    "v_mfma_f32_16x16x32_bf16 v[12:15], a[188:191], v[204:207], v[12:15]\n"
    "s_waitcnt lgkmcnt(5)\n"
    "ds_read_b64_tr_b16 a[186:187], v247 offset:22528\n"
    "v_mov_b32_e32 v159, 0\n"
    "v_mfma_f32_16x16x32_bf16 v[16:19], a[160:163], v[200:203], v[16:19]\n"
    "ds_read_b64_tr_b16 a[188:189], v247 offset:28672\n"
    "v_mfma_f32_16x16x32_bf16 v[16:19], a[164:167], v[204:207], v[16:19]\n"
    "ds_read_b64_tr_b16 a[190:191], v247 offset:30720\n"
    "v_mfma_f32_16x16x32_bf16 v[40:43], a[164:167], v[212:215], v[40:43]\n"
    "buffer_store_dwordx2 v[154:155], v253, s[24:27], 0 offen offset:0 sc1\n"
    "v_cvt_pk_bf16_f32 v179, v182, v183\n"
    "v_mfma_f32_16x16x32_bf16 v[40:43], a[160:163], v[208:211], v[40:43]\n"
    "v_exp_f32_e32 v174, v174\n"
    "v_mfma_f32_16x16x32_bf16 v[104:107], a[160:163], v[216:219], v[104:107]\n"
    "v_exp_f32_e32 v175, v175\n"
    "v_mfma_f32_16x16x32_bf16 v[104:107], a[164:167], v[220:223], v[104:107]\n"
    "s_waitcnt vmcnt(9)\n"
    "v_mfma_f32_16x16x32_bf16 v[108:111], a[164:167], v[228:231], v[108:111]\n"
    "s_barrier\n"
    "v_mfma_f32_16x16x32_bf16 v[108:111], a[160:163], v[224:227], v[108:111]\n"
    "ds_write_addtid_b32 v192 offset:16384\n"
    "v_cvt_pk_bf16_f32 v170, v172, v173\n"
    "v_mfma_f32_16x16x32_bf16 v[112:115], a[160:163], v[232:235], v[112:115]\n"
    "ds_read_b128 a[160:163], v243 offset:53248\n"
    "v_mfma_f32_16x16x32_bf16 v[112:115], a[164:167], v[236:239], v[112:115]\n"
    "ds_read_b128 a[164:167], v240 offset:49152\n"
    "v_mfma_f32_16x16x32_bf16 v[124:127], a[168:171], v[232:235], v[124:127]\n"
    "v_exp_f32_e32 v164, v164\n"
    "v_mfma_f32_16x16x32_bf16 v[124:127], a[172:175], v[236:239], v[124:127]\n"
    "v_exp_f32_e32 v165, v165\n"
    "v_mfma_f32_16x16x32_bf16 v[120:123], a[172:175], v[228:231], v[120:123]\n"
    "ds_write_addtid_b32 v193 offset:16640\n"
    "v_cvt_pk_bf16_f32 v171, v174, v175\n"
    "v_mfma_f32_16x16x32_bf16 v[120:123], a[168:171], v[224:227], v[120:123]\n"
    "ds_write_addtid_b32 v184 offset:12288\n"
    "v_mfma_f32_16x16x32_bf16 v[116:119], a[168:171], v[216:219], v[116:119]\n"
    "v_exp_f32_e32 v166, v166\n"
    "v_mfma_f32_16x16x32_bf16 v[116:119], a[172:175], v[220:223], v[116:119]\n"
    "v_exp_f32_e32 v167, v167\n"
    "v_mfma_f32_16x16x32_bf16 v[44:47], a[172:175], v[212:215], v[44:47]\n"
    "ds_write_addtid_b32 v185 offset:12544\n"
    "v_cvt_pk_bf16_f32 v162, v164, v165\n"
    "v_mfma_f32_16x16x32_bf16 v[44:47], a[168:171], v[208:211], v[44:47]\n"
    "v_cvt_pk_bf16_f32 v163, v166, v167\n"
    "v_mfma_f32_16x16x32_bf16 v[20:23], a[168:171], v[200:203], v[20:23]\n"
    "ds_read_b128 a[168:171], v241 offset:49152\n"
    "v_mfma_f32_16x16x32_bf16 v[20:23], a[172:175], v[204:207], v[20:23]\n"
    "s_waitcnt lgkmcnt(7)\n"
    "ds_read_b128 a[172:175], v240 offset:53248\n"
    "v_mfma_f32_16x16x32_bf16 v[24:27], a[176:179], v[200:203], v[24:27]\n"
    "ds_write_addtid_b32 v176 offset:8192\n"
    "v_mfma_f32_16x16x32_bf16 v[24:27], a[180:183], v[204:207], v[24:27]\n"
    "ds_write_addtid_b32 v177 offset:8448\n"
    "v_mfma_f32_16x16x32_bf16 v[48:51], a[180:183], v[212:215], v[48:51]\n"
    "ds_write_addtid_b32 v168 offset:4096\n"
    "v_mfma_f32_16x16x32_bf16 v[48:51], a[176:179], v[208:211], v[48:51]\n"
    "ds_write_addtid_b32 v169 offset:4352\n"
    "v_mfma_f32_16x16x32_bf16 v[128:131], a[176:179], v[216:219], v[128:131]\n"
    "ds_write_addtid_b32 v160 offset:0\n"
    "v_mfma_f32_16x16x32_bf16 v[128:131], a[180:183], v[220:223], v[128:131]\n"
    "ds_write_addtid_b32 v161 offset:256\n"
    "v_mfma_f32_16x16x32_bf16 v[132:135], a[180:183], v[228:231], v[132:135]\n"
    "ds_write_addtid_b32 v194 offset:16896\n"
    "v_mfma_f32_16x16x32_bf16 v[132:135], a[176:179], v[224:227], v[132:135]\n"
    "ds_write_addtid_b32 v195 offset:17152\n"
    "v_mfma_f32_16x16x32_bf16 v[136:139], a[176:179], v[232:235], v[136:139]\n"
    "ds_read_b128 a[176:179], v241 offset:53248\n"
    "v_mfma_f32_16x16x32_bf16 v[136:139], a[180:183], v[236:239], v[136:139]\n"
    "ds_read_b128 a[180:183], v242 offset:49152\n"
    "v_mfma_f32_16x16x32_bf16 v[148:151], a[184:187], v[232:235], v[148:151]\n"
    "ds_write_addtid_b32 v186 offset:12800\n"
    "v_mfma_f32_16x16x32_bf16 v[148:151], a[188:191], v[236:239], v[148:151]\n"
    "ds_write_addtid_b32 v187 offset:13056\n"
    "v_mfma_f32_16x16x32_bf16 v[144:147], a[188:191], v[228:231], v[144:147]\n"
    "ds_write_addtid_b32 v178 offset:8704\n"
    "v_mfma_f32_16x16x32_bf16 v[144:147], a[184:187], v[224:227], v[144:147]\n"
    "ds_write_addtid_b32 v179 offset:8960\n"
    "v_mfma_f32_16x16x32_bf16 v[140:143], a[184:187], v[216:219], v[140:143]\n"
    "ds_write_addtid_b32 v170 offset:4608\n"
    "v_mfma_f32_16x16x32_bf16 v[140:143], a[188:191], v[220:223], v[140:143]\n"
    "ds_write_addtid_b32 v171 offset:4864\n"
    "v_mfma_f32_16x16x32_bf16 v[52:55], a[188:191], v[212:215], v[52:55]\n"
    "ds_write_addtid_b32 v162 offset:512\n"
    "v_mfma_f32_16x16x32_bf16 v[52:55], a[184:187], v[208:211], v[52:55]\n"
    "ds_write_addtid_b32 v163 offset:768\n"
    "v_mfma_f32_16x16x32_bf16 v[28:31], a[184:187], v[200:203], v[28:31]\n"
    "ds_read_b128 a[184:187], v243 offset:49152\n"
    "v_mfma_f32_16x16x32_bf16 v[28:31], a[188:191], v[204:207], v[28:31]\n"
    "s_waitcnt lgkmcnt(15)\n"
    "ds_read_b128 a[188:191], v242 offset:53248\n"
    "v_mfma_f32_16x16x32_bf16 v[200:203], a[160:163], a[28:31], 0\n"
    "v_mfma_f32_16x16x32_bf16 v[200:203], a[164:167], a[0:3], v[200:203]\n"
    "v_mfma_f32_16x16x32_bf16 v[208:211], a[164:167], a[32:35], 0\n"
    "v_mfma_f32_16x16x32_bf16 v[208:211], a[160:163], a[60:63], v[208:211]\n"
    "v_mfma_f32_16x16x32_bf16 v[216:219], a[160:163], a[92:95], 0\n"
    "v_mfma_f32_16x16x32_bf16 v[216:219], a[164:167], a[64:67], v[216:219]\n"
    "s_waitcnt lgkmcnt(0)\n"
    "v_mfma_f32_16x16x32_bf16 v[224:227], a[164:167], a[96:99], 0\n"
    "s_barrier\n"
    "v_mfma_f32_16x16x32_bf16 v[224:227], a[160:163], a[124:127], v[224:227]\n"
    "s_add_u32 m0, s43, 16384\n"
    "ds_read2st64_b32 v[164:165], v250 offset0:0 offset1:1\n"
    "v_mfma_f32_16x16x32_bf16 v[232:235], a[160:163], a[156:159], 0\n"
    "ds_read_b128 a[160:163], v243 offset:55296\n"
    "v_mfma_f32_16x16x32_bf16 v[232:235], a[164:167], a[128:131], v[232:235]\n"
    "ds_read_b128 a[164:167], v240 offset:51200\n"
    "v_mfma_f32_16x16x32_bf16 v[232:235], a[168:171], a[132:135], v[232:235]\n"
    "buffer_load_dwordx4 v248, s[8:11], s40 offen offset:0 sc1 lds\n"
    "v_mfma_f32_16x16x32_bf16 v[232:235], a[172:175], a[136:139], v[232:235]\n"
    "v_mfma_f32_16x16x32_bf16 v[224:227], a[172:175], a[104:107], v[224:227]\n"
    "v_mfma_f32_16x16x32_bf16 v[224:227], a[168:171], a[100:103], v[224:227]\n"
    "ds_read2st64_b32 v[166:167], v250 offset0:2 offset1:3\n"
    "v_mfma_f32_16x16x32_bf16 v[216:219], a[168:171], a[68:71], v[216:219]\n"
    "buffer_load_dwordx4 v248, s[8:11], s40 offen offset:1024 sc1 lds\n"
    "v_mfma_f32_16x16x32_bf16 v[216:219], a[172:175], a[72:75], v[216:219]\n"
    "v_mfma_f32_16x16x32_bf16 v[208:211], a[172:175], a[40:43], v[208:211]\n"
    "v_mfma_f32_16x16x32_bf16 v[208:211], a[168:171], a[36:39], v[208:211]\n"
    "ds_read2st64_b32 v[172:173], v250 offset0:16 offset1:17\n"
    "v_mfma_f32_16x16x32_bf16 v[200:203], a[168:171], a[4:7], v[200:203]\n"
    "ds_read_b128 a[168:171], v241 offset:51200\n"
    "buffer_load_dwordx4 v248, s[8:11], s40 offen offset:2048 sc1 lds\n"
    "v_mfma_f32_16x16x32_bf16 v[200:203], a[172:175], a[8:11], v[200:203]\n"
    "ds_read_b128 a[172:175], v240 offset:55296\n"
    "v_mfma_f32_16x16x32_bf16 v[200:203], a[176:179], a[12:15], v[200:203]\n"
    "ds_read2st64_b32 v[174:175], v250 offset0:18 offset1:19\n"
    "v_mfma_f32_16x16x32_bf16 v[200:203], a[180:183], a[16:19], v[200:203]\n"
    "v_mfma_f32_16x16x32_bf16 v[208:211], a[180:183], a[48:51], v[208:211]\n"
    "buffer_load_dwordx4 v248, s[8:11], s40 offen offset:3072 sc1 lds\n"
    "v_mfma_f32_16x16x32_bf16 v[208:211], a[176:179], a[44:47], v[208:211]\n"
    "s_add_u32 m0, s43, 20480\n"
    "v_mfma_f32_16x16x32_bf16 v[216:219], a[176:179], a[76:79], v[216:219]\n"
    "s_add_u32 s40, s40, 0x8000\n"
    "ds_read2st64_b32 v[180:181], v250 offset0:32 offset1:33\n"
    "v_mfma_f32_16x16x32_bf16 v[216:219], a[180:183], a[80:83], v[216:219]\n"
    "v_mfma_f32_16x16x32_bf16 v[224:227], a[180:183], a[112:115], v[224:227]\n"
    "buffer_load_dwordx4 v248, s[8:11], s41 offen offset:0 sc1 lds\n"
    "v_mfma_f32_16x16x32_bf16 v[224:227], a[176:179], a[108:111], v[224:227]\n"
    "ds_read2st64_b32 v[182:183], v250 offset0:34 offset1:35\n"
    "v_mfma_f32_16x16x32_bf16 v[232:235], a[176:179], a[140:143], v[232:235]\n"
    "ds_read_b128 a[176:179], v241 offset:55296\n"
    "v_mfma_f32_16x16x32_bf16 v[232:235], a[180:183], a[144:147], v[232:235]\n"
    "ds_read_b128 a[180:183], v242 offset:51200\n"
    "v_mfma_f32_16x16x32_bf16 v[232:235], a[184:187], a[148:151], v[232:235]\n"
    "buffer_load_dwordx4 v248, s[8:11], s41 offen offset:1024 sc1 lds\n"
    "v_mfma_f32_16x16x32_bf16 v[232:235], a[188:191], a[152:155], v[232:235]\n"
    "v_mfma_f32_16x16x32_bf16 v[224:227], a[188:191], a[120:123], v[224:227]\n"
    "ds_read2st64_b32 v[188:189], v250 offset0:48 offset1:49\n"
    "v_mfma_f32_16x16x32_bf16 v[224:227], a[184:187], a[116:119], v[224:227]\n"
    "v_mfma_f32_16x16x32_bf16 v[216:219], a[184:187], a[84:87], v[216:219]\n"
    "buffer_load_dwordx4 v248, s[8:11], s41 offen offset:2048 sc1 lds\n"
    "v_mfma_f32_16x16x32_bf16 v[216:219], a[188:191], a[88:91], v[216:219]\n"
    "ds_read2st64_b32 v[190:191], v250 offset0:50 offset1:51\n"
    "v_mfma_f32_16x16x32_bf16 v[208:211], a[188:191], a[56:59], v[208:211]\n"
    "ds_read_b64 v[254:255], v252 offset:64\n"
    "v_mfma_f32_16x16x32_bf16 v[208:211], a[184:187], a[52:55], v[208:211]\n"
    "ds_read2st64_b32 v[196:197], v250 offset0:64 offset1:65\n"
    "v_mfma_f32_16x16x32_bf16 v[200:203], a[184:187], a[20:23], v[200:203]\n"
    "ds_read_b128 a[184:187], v243 offset:51200\n"
    "buffer_load_dwordx4 v248, s[8:11], s41 offen offset:3072 sc1 lds\n"
    "v_mfma_f32_16x16x32_bf16 v[200:203], a[188:191], a[24:27], v[200:203]\n"
    "s_waitcnt lgkmcnt(15)\n"
    "s_add_u32 s41, s41, 0x8000\n"
    "ds_read_b128 a[188:191], v242 offset:55296\n"
    "v_mfma_f32_16x16x32_bf16 v[204:207], a[160:163], a[28:31], 0\n"
    "s_waitcnt lgkmcnt(3)\n"
    "v_mad_u32_u16 v253, v254, s42, v251 op_sel:[0,0,0,0]\n"
    "v_mfma_f32_16x16x32_bf16 v[204:207], a[164:167], a[0:3], v[204:207]\n"
    "buffer_load_dwordx4 a[200:203], v253, s[20:23], 0 offen offset:0 sc1\n"
    "v_mfma_f32_16x16x32_bf16 v[212:215], a[164:167], a[32:35], 0\n"
    "v_exp_f32_e32 v232, v232\n"
    "v_mfma_f32_16x16x32_bf16 v[212:215], a[160:163], a[60:63], v[212:215]\n"
    "ds_read2st64_b32 v[198:199], v250 offset0:66 offset1:67\n"
    "v_mfma_f32_16x16x32_bf16 v[220:223], a[160:163], a[92:95], 0\n"
    "v_mad_u32_u16 v253, v254, s42, v251 op_sel:[1,0,0,0]\n"
    "v_mfma_f32_16x16x32_bf16 v[220:223], a[164:167], a[64:67], v[220:223]\n"
    "buffer_load_dwordx4 a[204:207], v253, s[20:23], 0 offen offset:0 sc1\n"
    "v_mfma_f32_16x16x32_bf16 v[228:231], a[164:167], a[96:99], 0\n"
    "v_exp_f32_e32 v233, v233\n"
    "v_mfma_f32_16x16x32_bf16 v[228:231], a[160:163], a[124:127], v[228:231]\n"
    "v_exp_f32_e32 v234, v234\n"
    "v_mfma_f32_16x16x32_bf16 v[236:239], a[160:163], a[156:159], 0\n"
    "ds_read_b64_tr_b16 a[160:161], v244 offset:32768\n"
    "v_mad_u32_u16 v253, v255, s42, v251 op_sel:[0,0,0,0]\n"
    "v_mfma_f32_16x16x32_bf16 v[236:239], a[164:167], a[128:131], v[236:239]\n"
    "ds_read_b64_tr_b16 a[162:163], v244 offset:34816\n"
    "buffer_load_dwordx4 a[208:211], v253, s[20:23], 0 offen offset:0 sc1\n"
    "v_mfma_f32_16x16x32_bf16 v[236:239], a[168:171], a[132:135], v[236:239]\n"
    "ds_read_b64_tr_b16 a[164:165], v244 offset:40960\n"
    "v_mfma_f32_16x16x32_bf16 v[236:239], a[172:175], a[136:139], v[236:239]\n"
    "ds_read_b64_tr_b16 a[166:167], v244 offset:43008\n"
    "v_mfma_f32_16x16x32_bf16 v[228:231], a[172:175], a[104:107], v[228:231]\n"
    "v_mad_u32_u16 v253, v255, s42, v251 op_sel:[1,0,0,0]\n"
    "v_mfma_f32_16x16x32_bf16 v[228:231], a[168:171], a[100:103], v[228:231]\n"
    "buffer_load_dwordx4 a[212:215], v253, s[20:23], 0 offen offset:0 sc1\n"
    "ds_read_b64 v[254:255], v252 offset:72\n"
    "v_mfma_f32_16x16x32_bf16 v[220:223], a[168:171], a[68:71], v[220:223]\n"
    "v_exp_f32_e32 v235, v235\n"
    "v_mfma_f32_16x16x32_bf16 v[220:223], a[172:175], a[72:75], v[220:223]\n"
    "v_cvt_pk_bf16_f32 v232, v232, v233\n"
    "v_mfma_f32_16x16x32_bf16 v[212:215], a[172:175], a[40:43], v[212:215]\n"
    "v_exp_f32_e32 v224, v224\n"
    "v_mfma_f32_16x16x32_bf16 v[212:215], a[168:171], a[36:39], v[212:215]\n"
    "v_exp_f32_e32 v225, v225\n"
    "v_mfma_f32_16x16x32_bf16 v[204:207], a[168:171], a[4:7], v[204:207]\n"
    "ds_read_b64_tr_b16 a[168:169], v245 offset:32768\n"
    "v_cvt_pk_bf16_f32 v233, v234, v235\n"
    "v_mfma_f32_16x16x32_bf16 v[204:207], a[172:175], a[8:11], v[204:207]\n"
    "ds_read_b64_tr_b16 a[170:171], v245 offset:34816\n"
    "v_mfma_f32_16x16x32_bf16 v[204:207], a[176:179], a[12:15], v[204:207]\n"
    "s_waitcnt lgkmcnt(2)\n"
    "ds_read_b64_tr_b16 a[172:173], v245 offset:40960\n"
    "v_mad_u32_u16 v253, v254, s42, v251 op_sel:[0,0,0,0]\n"
    "v_mfma_f32_16x16x32_bf16 v[204:207], a[180:183], a[16:19], v[204:207]\n"
    "ds_read_b64_tr_b16 a[174:175], v245 offset:43008\n"
    "buffer_load_dwordx4 a[216:219], v253, s[20:23], 0 offen offset:0 sc1\n"
    "v_mfma_f32_16x16x32_bf16 v[212:215], a[180:183], a[48:51], v[212:215]\n"
    "v_exp_f32_e32 v226, v226\n"
    "v_mfma_f32_16x16x32_bf16 v[212:215], a[176:179], a[44:47], v[212:215]\n"
    "v_exp_f32_e32 v227, v227\n"
    "v_mfma_f32_16x16x32_bf16 v[220:223], a[176:179], a[76:79], v[220:223]\n"
    "v_mad_u32_u16 v253, v254, s42, v251 op_sel:[1,0,0,0]\n"
    "v_cvt_pk_bf16_f32 v224, v224, v225\n"
    "v_mfma_f32_16x16x32_bf16 v[220:223], a[180:183], a[80:83], v[220:223]\n"
    "buffer_load_dwordx4 a[220:223], v253, s[20:23], 0 offen offset:0 sc1\n"
    "v_mfma_f32_16x16x32_bf16 v[228:231], a[180:183], a[112:115], v[228:231]\n"
    "v_exp_f32_e32 v216, v216\n"
    "v_mfma_f32_16x16x32_bf16 v[228:231], a[176:179], a[108:111], v[228:231]\n"
    "v_exp_f32_e32 v217, v217\n"
    "v_mfma_f32_16x16x32_bf16 v[236:239], a[176:179], a[140:143], v[236:239]\n"
    "ds_read_b64_tr_b16 a[176:177], v246 offset:32768\n"
    "v_cvt_pk_bf16_f32 v225, v226, v227\n"
    "v_mfma_f32_16x16x32_bf16 v[236:239], a[180:183], a[144:147], v[236:239]\n"
    "ds_read_b64_tr_b16 a[178:179], v246 offset:34816\n"
    "v_mfma_f32_16x16x32_bf16 v[236:239], a[184:187], a[148:151], v[236:239]\n"
    "ds_read_b64_tr_b16 a[180:181], v246 offset:40960\n"
    "v_mfma_f32_16x16x32_bf16 v[236:239], a[188:191], a[152:155], v[236:239]\n"
    "ds_read_b64_tr_b16 a[182:183], v246 offset:43008\n"
    "v_mfma_f32_16x16x32_bf16 v[228:231], a[188:191], a[120:123], v[228:231]\n"
    "ds_read_b64 v[254:255], v252 offset:96\n"
    "v_mfma_f32_16x16x32_bf16 v[228:231], a[184:187], a[116:119], v[228:231]\n"
    "v_exp_f32_e32 v218, v218\n"
    "v_mfma_f32_16x16x32_bf16 v[220:223], a[184:187], a[84:87], v[220:223]\n"
    "v_exp_f32_e32 v219, v219\n"
    "v_mfma_f32_16x16x32_bf16 v[220:223], a[188:191], a[88:91], v[220:223]\n"
    "v_cvt_pk_bf16_f32 v216, v216, v217\n"
    "v_mfma_f32_16x16x32_bf16 v[212:215], a[188:191], a[56:59], v[212:215]\n"
    "s_waitcnt lgkmcnt(0)\n"
    "v_mad_u32_u16 v253, v254, s42, v251 op_sel:[0,0,0,0]\n"
    "v_mfma_f32_16x16x32_bf16 v[212:215], a[184:187], a[52:55], v[212:215]\n"
    "buffer_load_dwordx4 a[224:227], v253, s[20:23], 0 offen offset:0 sc1\n"
    "v_mfma_f32_16x16x32_bf16 v[204:207], a[184:187], a[20:23], v[204:207]\n"
    "ds_read_b64_tr_b16 a[184:185], v247 offset:32768\n"
    "v_mfma_f32_16x16x32_bf16 v[204:207], a[188:191], a[24:27], v[204:207]\n"
    "ds_read_b64_tr_b16 a[186:187], v247 offset:34816\n"
    "v_mfma_f32_16x16x32_bf16 v[0:3], a[160:163], v[160:163], v[0:3]\n"
    "ds_read_b64_tr_b16 a[188:189], v247 offset:40960\n"
    "v_mad_u32_u16 v253, v254, s42, v251 op_sel:[1,0,0,0]\n"
    "v_mfma_f32_16x16x32_bf16 v[0:3], a[164:167], v[164:167], v[0:3]\n"
    "ds_read_b64_tr_b16 a[190:191], v247 offset:43008\n"
    "buffer_load_dwordx4 a[228:231], v253, s[20:23], 0 offen offset:0 sc1\n"
    "v_mfma_f32_16x16x32_bf16 a[248:251], a[164:167], v[172:175], a[248:251]\n"
    "v_exp_f32_e32 v208, v208\n"
    "v_mfma_f32_16x16x32_bf16 a[248:251], a[160:163], v[168:171], a[248:251]\n"
    "v_exp_f32_e32 v209, v209\n"
    "v_mfma_f32_16x16x32_bf16 v[56:59], a[160:163], v[176:179], v[56:59]\n"
    "v_mad_u32_u16 v253, v255, s42, v251 op_sel:[0,0,0,0]\n"
    "v_cvt_pk_bf16_f32 v217, v218, v219\n"
    "v_mfma_f32_16x16x32_bf16 v[56:59], a[164:167], v[180:183], v[56:59]\n"
    "buffer_load_dwordx4 a[232:235], v253, s[20:23], 0 offen offset:0 sc1\n"
    "v_mfma_f32_16x16x32_bf16 v[60:63], a[164:167], v[188:191], v[60:63]\n"
    "v_exp_f32_e32 v210, v210\n"
    "v_mfma_f32_16x16x32_bf16 v[60:63], a[160:163], v[184:187], v[60:63]\n"
    "v_exp_f32_e32 v211, v211\n"
    "v_mfma_f32_16x16x32_bf16 v[64:67], a[160:163], v[192:195], v[64:67]\n"
    "ds_read_b64_tr_b16 a[160:161], v244 offset:36864\n"
    "v_mad_u32_u16 v253, v255, s42, v251 op_sel:[1,0,0,0]\n"
    "v_mfma_f32_16x16x32_bf16 v[64:67], a[164:167], v[196:199], v[64:67]\n"
    "ds_read_b64_tr_b16 a[162:163], v244 offset:38912\n"
    "buffer_load_dwordx4 a[236:239], v253, s[20:23], 0 offen offset:0 sc1\n"
    "v_mfma_f32_16x16x32_bf16 v[76:79], a[168:171], v[192:195], v[76:79]\n"
    "ds_read_b64_tr_b16 a[164:165], v244 offset:45056\n"
    "v_cvt_pk_bf16_f32 v208, v208, v209\n"
    "v_mfma_f32_16x16x32_bf16 v[76:79], a[172:175], v[196:199], v[76:79]\n"
    "ds_read_b64_tr_b16 a[166:167], v244 offset:47104\n"
    "v_mfma_f32_16x16x32_bf16 v[72:75], a[172:175], v[188:191], v[72:75]\n"
    "ds_read_b64 v[254:255], v252 offset:104\n"
    "v_mfma_f32_16x16x32_bf16 v[72:75], a[168:171], v[184:187], v[72:75]\n"
    "v_exp_f32_e32 v200, v200\n"
    "v_mfma_f32_16x16x32_bf16 v[68:71], a[168:171], v[176:179], v[68:71]\n"
    "v_exp_f32_e32 v201, v201\n"
    "v_mfma_f32_16x16x32_bf16 v[68:71], a[172:175], v[180:183], v[68:71]\n"
    "v_cvt_pk_bf16_f32 v209, v210, v211\n"
    "v_mfma_f32_16x16x32_bf16 a[252:255], a[172:175], v[172:175], a[252:255]\n"
    "v_exp_f32_e32 v202, v202\n"
    "v_mfma_f32_16x16x32_bf16 a[252:255], a[168:171], v[168:171], a[252:255]\n"
    "v_exp_f32_e32 v203, v203\n"
    "v_mfma_f32_16x16x32_bf16 v[4:7], a[168:171], v[160:163], v[4:7]\n"
    "s_waitcnt lgkmcnt(0)\n"
    "ds_read_b64_tr_b16 a[168:169], v245 offset:36864\n"
    "v_mad_u32_u16 v253, v254, s42, v251 op_sel:[0,0,0,0]\n"
    "v_mfma_f32_16x16x32_bf16 v[4:7], a[172:175], v[164:167], v[4:7]\n"
    "ds_read_b64_tr_b16 a[170:171], v245 offset:38912\n"
    "v_cvt_pk_bf16_f32 v200, v200, v201\n"
    "v_mfma_f32_16x16x32_bf16 v[8:11], a[176:179], v[160:163], v[8:11]\n"
    "ds_read_b64_tr_b16 a[172:173], v245 offset:45056\n"
    "buffer_load_dwordx4 a[240:243], v253, s[20:23], 0 offen offset:0 sc1\n"
    "v_mfma_f32_16x16x32_bf16 v[8:11], a[180:183], v[164:167], v[8:11]\n"
    "ds_read_b64_tr_b16 a[174:175], v245 offset:47104\n"
    "v_cvt_pk_bf16_f32 v201, v202, v203\n"
    "v_mfma_f32_16x16x32_bf16 v[32:35], a[180:183], v[172:175], v[32:35]\n"
    "v_exp_f32_e32 v236, v236\n"
    "v_mfma_f32_16x16x32_bf16 v[32:35], a[176:179], v[168:171], v[32:35]\n"
    "v_mad_u32_u16 v253, v254, s42, v251 op_sel:[1,0,0,0]\n"
    "v_mfma_f32_16x16x32_bf16 v[80:83], a[176:179], v[176:179], v[80:83]\n"
    "buffer_load_dwordx4 a[244:247], v253, s[20:23], 0 offen offset:0 sc1\n"
    "v_mfma_f32_16x16x32_bf16 v[80:83], a[180:183], v[180:183], v[80:83]\n"
    "v_exp_f32_e32 v237, v237\n"
    "v_mfma_f32_16x16x32_bf16 v[84:87], a[180:183], v[188:191], v[84:87]\n"
    "v_exp_f32_e32 v238, v238\n"
    "v_mfma_f32_16x16x32_bf16 v[84:87], a[176:179], v[184:187], v[84:87]\n"
    "v_exp_f32_e32 v239, v239\n"
    "v_mfma_f32_16x16x32_bf16 v[88:91], a[176:179], v[192:195], v[88:91]\n"
    "ds_read_b64_tr_b16 a[176:177], v246 offset:36864\n"
    "v_cvt_pk_bf16_f32 v234, v236, v237\n"
    "v_mfma_f32_16x16x32_bf16 v[88:91], a[180:183], v[196:199], v[88:91]\n"
    "ds_read_b64_tr_b16 a[178:179], v246 offset:38912\n"
    "v_exp_f32_e32 v228, v228\n"
    "v_mfma_f32_16x16x32_bf16 v[100:103], a[184:187], v[192:195], v[100:103]\n"
    "ds_read_b64_tr_b16 a[180:181], v246 offset:45056\n"
    "v_exp_f32_e32 v229, v229\n"
    "v_mfma_f32_16x16x32_bf16 v[100:103], a[188:191], v[196:199], v[100:103]\n"
    "ds_read_b64_tr_b16 a[182:183], v246 offset:47104\n"
    "v_cvt_pk_bf16_f32 v235, v238, v239\n"
    "v_mfma_f32_16x16x32_bf16 v[96:99], a[188:191], v[188:191], v[96:99]\n"
    "v_exp_f32_e32 v230, v230\n"
    "v_mfma_f32_16x16x32_bf16 v[96:99], a[184:187], v[184:187], v[96:99]\n"
    "v_exp_f32_e32 v231, v231\n"
    "v_cvt_pk_bf16_f32 v226, v228, v229\n"
    "v_mfma_f32_16x16x32_bf16 v[92:95], a[184:187], v[176:179], v[92:95]\n"
    "v_exp_f32_e32 v220, v220\n"
    "v_mfma_f32_16x16x32_bf16 v[92:95], a[188:191], v[180:183], v[92:95]\n"
    "v_exp_f32_e32 v221, v221\n"
    "v_mfma_f32_16x16x32_bf16 v[36:39], a[188:191], v[172:175], v[36:39]\n"
    "v_cvt_pk_bf16_f32 v227, v230, v231\n"
    "v_mfma_f32_16x16x32_bf16 v[36:39], a[184:187], v[168:171], v[36:39]\n"
    "v_exp_f32_e32 v222, v222\n"
    "v_mfma_f32_16x16x32_bf16 v[12:15], a[184:187], v[160:163], v[12:15]\n"
    "s_mov_b32 m0, s49\n"
    "ds_read_b64_tr_b16 a[184:185], v247 offset:36864\n"
    "v_mfma_f32_16x16x32_bf16 v[12:15], a[188:191], v[164:167], v[12:15]\n"
    "s_waitcnt lgkmcnt(5)\n"
    "ds_read_b64_tr_b16 a[186:187], v247 offset:38912\n"
    "v_mfma_f32_16x16x32_bf16 v[16:19], a[160:163], v[160:163], v[16:19]\n"
    "ds_read_b64_tr_b16 a[188:189], v247 offset:45056\n"
    "v_mfma_f32_16x16x32_bf16 v[16:19], a[164:167], v[164:167], v[16:19]\n"
    "ds_read_b64_tr_b16 a[190:191], v247 offset:47104\n"
    "v_mfma_f32_16x16x32_bf16 v[40:43], a[164:167], v[172:175], v[40:43]\n"
    "v_exp_f32_e32 v223, v223\n"
    "v_mfma_f32_16x16x32_bf16 v[40:43], a[160:163], v[168:171], v[40:43]\n"
    "v_cvt_pk_bf16_f32 v218, v220, v221\n"
    "v_mfma_f32_16x16x32_bf16 v[104:107], a[160:163], v[176:179], v[104:107]\n"
    "v_exp_f32_e32 v212, v212\n"
    "v_mfma_f32_16x16x32_bf16 v[104:107], a[164:167], v[180:183], v[104:107]\n"
    "s_waitcnt vmcnt(20)\n"
    "v_mfma_f32_16x16x32_bf16 v[108:111], a[164:167], v[188:191], v[108:111]\n"
    "s_barrier\n"
    "v_mfma_f32_16x16x32_bf16 v[108:111], a[160:163], v[184:187], v[108:111]\n"
    "v_exp_f32_e32 v213, v213\n"
    "v_mfma_f32_16x16x32_bf16 v[112:115], a[160:163], v[192:195], v[112:115]\n"
    "ds_read_b128 a[160:163], v243 offset:4096\n"
    "v_cvt_pk_bf16_f32 v219, v222, v223\n"
    "v_mfma_f32_16x16x32_bf16 v[112:115], a[164:167], v[196:199], v[112:115]\n"
    "ds_read_b128 a[164:167], v240 offset:0\n"
    "ds_write_addtid_b32 v232 offset:16384\n"
    "v_mfma_f32_16x16x32_bf16 v[124:127], a[168:171], v[192:195], v[124:127]\n"
    "v_exp_f32_e32 v214, v214\n"
    "v_mfma_f32_16x16x32_bf16 v[124:127], a[172:175], v[196:199], v[124:127]\n"
    "v_exp_f32_e32 v215, v215\n"
    "v_mfma_f32_16x16x32_bf16 v[120:123], a[172:175], v[188:191], v[120:123]\n"
    "ds_write_addtid_b32 v233 offset:16640\n"
    "v_cvt_pk_bf16_f32 v210, v212, v213\n"
    "v_mfma_f32_16x16x32_bf16 v[120:123], a[168:171], v[184:187], v[120:123]\n"
    "v_exp_f32_e32 v204, v204\n"
    "v_mfma_f32_16x16x32_bf16 v[116:119], a[168:171], v[176:179], v[116:119]\n"
    "v_exp_f32_e32 v205, v205\n"
    "v_mfma_f32_16x16x32_bf16 v[116:119], a[172:175], v[180:183], v[116:119]\n"
    "ds_write_addtid_b32 v224 offset:12288\n"
    "v_cvt_pk_bf16_f32 v211, v214, v215\n"
    "v_mfma_f32_16x16x32_bf16 v[44:47], a[172:175], v[172:175], v[44:47]\n"
    "v_exp_f32_e32 v206, v206\n"
    "v_mfma_f32_16x16x32_bf16 v[44:47], a[168:171], v[168:171], v[44:47]\n"
    "v_exp_f32_e32 v207, v207\n"
    "v_mfma_f32_16x16x32_bf16 v[20:23], a[168:171], v[160:163], v[20:23]\n"
    "ds_read_b128 a[168:171], v241 offset:0\n"
    "v_cvt_pk_bf16_f32 v202, v204, v205\n"
    "v_mfma_f32_16x16x32_bf16 v[20:23], a[172:175], v[164:167], v[20:23]\n"
    "s_waitcnt lgkmcnt(6)\n"
    "ds_read_b128 a[172:175], v240 offset:4096\n"
    "v_cvt_pk_bf16_f32 v203, v206, v207\n"
    "v_mfma_f32_16x16x32_bf16 v[24:27], a[176:179], v[160:163], v[24:27]\n"
    "ds_write_addtid_b32 v225 offset:12544\n"
    "ds_write_addtid_b32 v216 offset:8192\n"
    "v_mfma_f32_16x16x32_bf16 v[24:27], a[180:183], v[164:167], v[24:27]\n"
    "ds_write_addtid_b32 v217 offset:8448\n"
    "v_mfma_f32_16x16x32_bf16 v[48:51], a[180:183], v[172:175], v[48:51]\n"
    "ds_write_addtid_b32 v208 offset:4096\n"
    "v_mfma_f32_16x16x32_bf16 v[48:51], a[176:179], v[168:171], v[48:51]\n"
    "ds_write_addtid_b32 v209 offset:4352\n"
    "v_mfma_f32_16x16x32_bf16 v[128:131], a[176:179], v[176:179], v[128:131]\n"
    "ds_write_addtid_b32 v200 offset:0\n"
    "v_mfma_f32_16x16x32_bf16 v[128:131], a[180:183], v[180:183], v[128:131]\n"
    "ds_write_addtid_b32 v201 offset:256\n"
    "v_mfma_f32_16x16x32_bf16 v[132:135], a[180:183], v[188:191], v[132:135]\n"
    "ds_write_addtid_b32 v234 offset:16896\n"
    "v_mfma_f32_16x16x32_bf16 v[132:135], a[176:179], v[184:187], v[132:135]\n"
    "ds_write_addtid_b32 v235 offset:17152\n"
    "v_mfma_f32_16x16x32_bf16 v[136:139], a[176:179], v[192:195], v[136:139]\n"
    "ds_read_b128 a[176:179], v241 offset:4096\n"
    "v_mfma_f32_16x16x32_bf16 v[136:139], a[180:183], v[196:199], v[136:139]\n"
    "ds_read_b128 a[180:183], v242 offset:0\n"
    "v_mfma_f32_16x16x32_bf16 v[148:151], a[184:187], v[192:195], v[148:151]\n"
    "ds_write_addtid_b32 v226 offset:12800\n"
    "v_mfma_f32_16x16x32_bf16 v[148:151], a[188:191], v[196:199], v[148:151]\n"
    "ds_write_addtid_b32 v227 offset:13056\n"
    "v_mfma_f32_16x16x32_bf16 v[144:147], a[188:191], v[188:191], v[144:147]\n"
    "ds_write_addtid_b32 v218 offset:8704\n"
    "v_mfma_f32_16x16x32_bf16 v[144:147], a[184:187], v[184:187], v[144:147]\n"
    "ds_write_addtid_b32 v219 offset:8960\n"
    "v_mfma_f32_16x16x32_bf16 v[140:143], a[184:187], v[176:179], v[140:143]\n"
    "ds_write_addtid_b32 v210 offset:4608\n"
    "v_mfma_f32_16x16x32_bf16 v[140:143], a[188:191], v[180:183], v[140:143]\n"
    "ds_write_addtid_b32 v211 offset:4864\n"
    "v_mfma_f32_16x16x32_bf16 v[52:55], a[188:191], v[172:175], v[52:55]\n"
    "ds_write_addtid_b32 v202 offset:512\n"
    "v_mfma_f32_16x16x32_bf16 v[52:55], a[184:187], v[168:171], v[52:55]\n"
    "ds_write_addtid_b32 v203 offset:768\n"
    "v_mfma_f32_16x16x32_bf16 v[28:31], a[184:187], v[160:163], v[28:31]\n"
    "ds_read_b128 a[184:187], v243 offset:0\n"
    "v_mfma_f32_16x16x32_bf16 v[28:31], a[188:191], v[164:167], v[28:31]\n"
    "s_waitcnt lgkmcnt(15)\n"
    "ds_read_b128 a[188:191], v242 offset:4096\n"
    "v_mfma_f32_16x16x32_bf16 v[160:163], a[160:163], a[28:31], 0\n"
    "v_mfma_f32_16x16x32_bf16 v[160:163], a[164:167], a[0:3], v[160:163]\n"
    "v_mfma_f32_16x16x32_bf16 v[168:171], a[164:167], a[32:35], 0\n"
    "v_mfma_f32_16x16x32_bf16 v[168:171], a[160:163], a[60:63], v[168:171]\n"
    "v_mfma_f32_16x16x32_bf16 v[176:179], a[160:163], a[92:95], 0\n"
    "v_mfma_f32_16x16x32_bf16 v[176:179], a[164:167], a[64:67], v[176:179]\n"
    "s_waitcnt lgkmcnt(0)\n"
    "v_mfma_f32_16x16x32_bf16 v[184:187], a[164:167], a[96:99], 0\n"
    "s_barrier\n"
    "v_mfma_f32_16x16x32_bf16 v[184:187], a[160:163], a[124:127], v[184:187]\n"
    "s_add_u32 m0, s43, 32768\n"
    "v_mfma_f32_16x16x32_bf16 v[192:195], a[160:163], a[156:159], 0\n"
    "ds_read_b128 a[160:163], v243 offset:6144\n"
    "v_mfma_f32_16x16x32_bf16 v[192:195], a[164:167], a[128:131], v[192:195]\n"
    "ds_read_b128 a[164:167], v240 offset:2048\n"
    "v_mfma_f32_16x16x32_bf16 v[192:195], a[168:171], a[132:135], v[192:195]\n"
    "buffer_load_dwordx4 v248, s[8:11], s40 offen offset:0 sc1 lds\n"
    "v_mfma_f32_16x16x32_bf16 v[192:195], a[172:175], a[136:139], v[192:195]\n"
    "ds_read2st64_b32 v[204:205], v250 offset0:0 offset1:1\n"
    "v_mfma_f32_16x16x32_bf16 v[184:187], a[172:175], a[104:107], v[184:187]\n"
    "s_waitcnt vmcnt(11)\n"
    "v_smfmac_f32_16x16x64_bf16 v[156:159], a[192:195], a[200:207], v152\n"
    "v_mfma_f32_16x16x32_bf16 v[184:187], a[168:171], a[100:103], v[184:187]\n"
    "ds_read2st64_b32 v[206:207], v250 offset0:2 offset1:3\n"
    "v_mfma_f32_16x16x32_bf16 v[176:179], a[168:171], a[68:71], v[176:179]\n"
    "buffer_load_dwordx4 v248, s[8:11], s40 offen offset:1024 sc1 lds\n"
    "v_mfma_f32_16x16x32_bf16 v[176:179], a[172:175], a[72:75], v[176:179]\n"
    "v_mfma_f32_16x16x32_bf16 v[168:171], a[172:175], a[40:43], v[168:171]\n"
    "s_waitcnt vmcnt(10)\n"
    "v_smfmac_f32_16x16x64_bf16 v[156:159], a[192:195], a[208:215], v152\n"
    "ds_read2st64_b32 v[212:213], v250 offset0:16 offset1:17\n"
    "v_mfma_f32_16x16x32_bf16 v[168:171], a[168:171], a[36:39], v[168:171]\n"
    "v_mfma_f32_16x16x32_bf16 v[160:163], a[168:171], a[4:7], v[160:163]\n"
    "ds_read_b128 a[168:171], v241 offset:2048\n"
    "buffer_load_dwordx4 v248, s[8:11], s40 offen offset:2048 sc1 lds\n"
    "v_mfma_f32_16x16x32_bf16 v[160:163], a[172:175], a[8:11], v[160:163]\n"
    "ds_read_b128 a[172:175], v240 offset:6144\n"
    "v_mfma_f32_16x16x32_bf16 v[160:163], a[176:179], a[12:15], v[160:163]\n"
    "s_waitcnt vmcnt(9)\n"
    "ds_read2st64_b32 v[214:215], v250 offset0:18 offset1:19\n"
    "v_smfmac_f32_16x16x64_bf16 v[156:159], a[192:195], a[216:223], v152\n"
    "v_mfma_f32_16x16x32_bf16 v[160:163], a[180:183], a[16:19], v[160:163]\n"
    "v_mfma_f32_16x16x32_bf16 v[168:171], a[180:183], a[48:51], v[168:171]\n"
    "buffer_load_dwordx4 v248, s[8:11], s40 offen offset:3072 sc1 lds\n"
    "v_mfma_f32_16x16x32_bf16 v[168:171], a[176:179], a[44:47], v[168:171]\n"
    "s_add_u32 m0, s43, 36864\n"
    "v_mfma_f32_16x16x32_bf16 v[176:179], a[176:179], a[76:79], v[176:179]\n"
    "s_add_u32 s40, s40, 0x8000\n"
    "ds_read2st64_b32 v[220:221], v250 offset0:32 offset1:33\n"
    "v_mfma_f32_16x16x32_bf16 v[176:179], a[180:183], a[80:83], v[176:179]\n"
    "v_mfma_f32_16x16x32_bf16 v[184:187], a[180:183], a[112:115], v[184:187]\n"
    "v_mfma_f32_16x16x32_bf16 v[184:187], a[176:179], a[108:111], v[184:187]\n"
    "buffer_load_dwordx4 v248, s[8:11], s41 offen offset:0 sc1 lds\n"
    "v_mfma_f32_16x16x32_bf16 v[192:195], a[176:179], a[140:143], v[192:195]\n"
    "ds_read_b128 a[176:179], v241 offset:6144\n"
    "v_mfma_f32_16x16x32_bf16 v[192:195], a[180:183], a[144:147], v[192:195]\n"
    "ds_read_b128 a[180:183], v242 offset:2048\n"
    "v_mfma_f32_16x16x32_bf16 v[192:195], a[184:187], a[148:151], v[192:195]\n"
    "ds_read2st64_b32 v[222:223], v250 offset0:34 offset1:35\n"
    "v_mfma_f32_16x16x32_bf16 v[192:195], a[188:191], a[152:155], v[192:195]\n"
    "v_mfma_f32_16x16x32_bf16 v[184:187], a[188:191], a[120:123], v[184:187]\n"
    "buffer_load_dwordx4 v248, s[8:11], s41 offen offset:1024 sc1 lds\n"
    "v_mfma_f32_16x16x32_bf16 v[184:187], a[184:187], a[116:119], v[184:187]\n"
    "ds_read2st64_b32 v[228:229], v250 offset0:48 offset1:49\n"
    "v_mfma_f32_16x16x32_bf16 v[176:179], a[184:187], a[84:87], v[176:179]\n"
    "s_waitcnt vmcnt(10)\n"
    "v_exp_f32_e32 v192, v192\n"
    "v_smfmac_f32_16x16x64_bf16 v[156:159], a[192:195], a[224:231], v152\n"
    "v_exp_f32_e32 v193, v193\n"
    "v_mfma_f32_16x16x32_bf16 v[176:179], a[188:191], a[88:91], v[176:179]\n"
    "v_exp_f32_e32 v194, v194\n"
    "v_mfma_f32_16x16x32_bf16 v[168:171], a[188:191], a[56:59], v[168:171]\n"
    "buffer_load_dwordx4 v248, s[8:11], s41 offen offset:2048 sc1 lds\n"
    "v_mfma_f32_16x16x32_bf16 v[168:171], a[184:187], a[52:55], v[168:171]\n"
    "ds_read2st64_b32 v[230:231], v250 offset0:50 offset1:51\n"
    "v_mfma_f32_16x16x32_bf16 v[160:163], a[184:187], a[20:23], v[160:163]\n"
    "s_waitcnt vmcnt(9)\n"
    "ds_read_b128 a[184:187], v243 offset:2048\n"
    "v_smfmac_f32_16x16x64_bf16 v[156:159], a[192:195], a[232:239], v152\n"
    "ds_read2st64_b32 v[236:237], v250 offset0:64 offset1:65\n"
    "v_mfma_f32_16x16x32_bf16 v[160:163], a[188:191], a[24:27], v[160:163]\n"
    "s_waitcnt lgkmcnt(9)\n"
    "ds_read_b128 a[188:191], v242 offset:6144\n"
    "v_mfma_f32_16x16x32_bf16 v[164:167], a[160:163], a[28:31], 0\n"
    "buffer_load_dwordx4 v248, s[8:11], s41 offen offset:3072 sc1 lds\n"
    "v_mfma_f32_16x16x32_bf16 v[164:167], a[164:167], a[0:3], v[164:167]\n"
    "s_add_u32 s41, s41, 0x8000\n"
    "v_exp_f32_e32 v195, v195\n"
    "v_mfma_f32_16x16x32_bf16 v[172:175], a[164:167], a[32:35], 0\n"
    "s_waitcnt vmcnt(8)\n"
    "v_cvt_pk_bf16_f32 v192, v192, v193\n"
    "v_smfmac_f32_16x16x64_bf16 v[156:159], a[192:195], a[240:247], v152\n"
    "v_exp_f32_e32 v184, v184\n"
    "v_mfma_f32_16x16x32_bf16 v[172:175], a[160:163], a[60:63], v[172:175]\n"
    "ds_read2st64_b32 v[238:239], v250 offset0:66 offset1:67\n"
    "v_mfma_f32_16x16x32_bf16 v[180:183], a[160:163], a[92:95], 0\n"
    "v_exp_f32_e32 v185, v185\n"
    "v_mfma_f32_16x16x32_bf16 v[180:183], a[164:167], a[64:67], v[180:183]\n"
    "v_cvt_pk_bf16_f32 v193, v194, v195\n"
    "v_mfma_f32_16x16x32_bf16 v[188:191], a[164:167], a[96:99], 0\n"
    "v_exp_f32_e32 v186, v186\n"
    "v_mfma_f32_16x16x32_bf16 v[188:191], a[160:163], a[124:127], v[188:191]\n"
    "v_exp_f32_e32 v187, v187\n"
    "v_mfma_f32_16x16x32_bf16 v[196:199], a[160:163], a[156:159], 0\n"
    "ds_read_b64_tr_b16 a[160:161], v244 offset:49152\n"
    "v_and_b32_e32 v253, 0xffff0000, v255\n"
    "v_mfma_f32_16x16x32_bf16 v[196:199], a[164:167], a[128:131], v[196:199]\n"
    "ds_read_b64_tr_b16 a[162:163], v244 offset:51200\n"
    "v_mfma_f32_16x16x32_bf16 v[196:199], a[168:171], a[132:135], v[196:199]\n"
    "ds_read_b64_tr_b16 a[164:165], v244 offset:57344\n"
    "v_mfma_f32_16x16x32_bf16 v[196:199], a[172:175], a[136:139], v[196:199]\n"
    "ds_read_b64_tr_b16 a[166:167], v244 offset:59392\n"
    "v_mfma_f32_16x16x32_bf16 v[188:191], a[172:175], a[104:107], v[188:191]\n"
    "v_cvt_pk_bf16_f32 v184, v184, v185\n"
    "v_mfma_f32_16x16x32_bf16 v[188:191], a[168:171], a[100:103], v[188:191]\n"
    "v_exp_f32_e32 v176, v176\n"
    "v_mfma_f32_16x16x32_bf16 v[180:183], a[168:171], a[68:71], v[180:183]\n"
    "v_exp_f32_e32 v177, v177\n"
    "v_mfma_f32_16x16x32_bf16 v[180:183], a[172:175], a[72:75], v[180:183]\n"
    "v_cvt_pk_bf16_f32 v185, v186, v187\n"
    "v_mul_f32_e32 v156, v253, v156\n"
    "v_mfma_f32_16x16x32_bf16 v[172:175], a[172:175], a[40:43], v[172:175]\n"
    "v_exp_f32_e32 v178, v178\n"
    "v_mfma_f32_16x16x32_bf16 v[172:175], a[168:171], a[36:39], v[172:175]\n"
    "v_exp_f32_e32 v179, v179\n"
    "v_mfma_f32_16x16x32_bf16 v[164:167], a[168:171], a[4:7], v[164:167]\n"
    "ds_read_b64_tr_b16 a[168:169], v245 offset:49152\n"
    "v_mfma_f32_16x16x32_bf16 v[164:167], a[172:175], a[8:11], v[164:167]\n"
    "s_waitcnt lgkmcnt(6)\n"
    "ds_read_b64_tr_b16 a[170:171], v245 offset:51200\n"
    "v_mfma_f32_16x16x32_bf16 v[164:167], a[176:179], a[12:15], v[164:167]\n"
    "ds_read_b64_tr_b16 a[172:173], v245 offset:57344\n"
    "v_mul_f32_e32 v157, v253, v157\n"
    "v_mfma_f32_16x16x32_bf16 v[164:167], a[180:183], a[16:19], v[164:167]\n"
    "ds_read_b64_tr_b16 a[174:175], v245 offset:59392\n"
    "v_mfma_f32_16x16x32_bf16 v[172:175], a[180:183], a[48:51], v[172:175]\n"
    "v_cvt_pk_bf16_f32 v176, v176, v177\n"
    "v_mfma_f32_16x16x32_bf16 v[172:175], a[176:179], a[44:47], v[172:175]\n"
    "v_exp_f32_e32 v168, v168\n"
    "v_mfma_f32_16x16x32_bf16 v[180:183], a[176:179], a[76:79], v[180:183]\n"
    "v_mul_f32_e32 v158, v253, v158\n"
    "v_mfma_f32_16x16x32_bf16 v[180:183], a[180:183], a[80:83], v[180:183]\n"
    "v_exp_f32_e32 v169, v169\n"
    "v_mfma_f32_16x16x32_bf16 v[188:191], a[180:183], a[112:115], v[188:191]\n"
    "v_cvt_pk_bf16_f32 v177, v178, v179\n"
    "v_mfma_f32_16x16x32_bf16 v[188:191], a[176:179], a[108:111], v[188:191]\n"
    "v_exp_f32_e32 v170, v170\n"
    "v_mfma_f32_16x16x32_bf16 v[196:199], a[176:179], a[140:143], v[196:199]\n"
    "ds_read_b64_tr_b16 a[176:177], v246 offset:49152\n"
    "v_mfma_f32_16x16x32_bf16 v[196:199], a[180:183], a[144:147], v[196:199]\n"
    "ds_read_b64_tr_b16 a[178:179], v246 offset:51200\n"
    "v_mfma_f32_16x16x32_bf16 v[196:199], a[184:187], a[148:151], v[196:199]\n"
    "ds_read_b64_tr_b16 a[180:181], v246 offset:57344\n"
    "v_mfma_f32_16x16x32_bf16 v[196:199], a[188:191], a[152:155], v[196:199]\n"
    "ds_read_b64_tr_b16 a[182:183], v246 offset:59392\n"
    "v_mul_f32_e32 v159, v253, v159\n"
    "v_mfma_f32_16x16x32_bf16 v[188:191], a[188:191], a[120:123], v[188:191]\n"
    "v_exp_f32_e32 v171, v171\n"
    "v_mfma_f32_16x16x32_bf16 v[188:191], a[184:187], a[116:119], v[188:191]\n"
    "v_cvt_pk_bf16_f32 v168, v168, v169\n"
    "v_mfma_f32_16x16x32_bf16 v[180:183], a[184:187], a[84:87], v[180:183]\n"
    "v_exp_f32_e32 v160, v160\n"
    "v_mfma_f32_16x16x32_bf16 v[180:183], a[188:191], a[88:91], v[180:183]\n"
    "v_exp_f32_e32 v161, v161\n"
    "v_mfma_f32_16x16x32_bf16 v[172:175], a[188:191], a[56:59], v[172:175]\n"
    "v_cvt_pk_bf16_f32 v169, v170, v171\n"
    "v_mfma_f32_16x16x32_bf16 v[172:175], a[184:187], a[52:55], v[172:175]\n"
    "v_exp_f32_e32 v162, v162\n"
    "v_mfma_f32_16x16x32_bf16 v[164:167], a[184:187], a[20:23], v[164:167]\n"
    "ds_read_b64_tr_b16 a[184:185], v247 offset:49152\n"
    "v_mad_u32_u16 v253, v255, s42, v153 op_sel:[0,0,0,0]\n"
    "v_mfma_f32_16x16x32_bf16 v[164:167], a[188:191], a[24:27], v[164:167]\n"
    "s_waitcnt lgkmcnt(5)\n"
    "ds_read_b64_tr_b16 a[186:187], v247 offset:51200\n"
    "v_mfma_f32_16x16x32_bf16 v[0:3], a[160:163], v[200:203], v[0:3]\n"
    "ds_read_b64_tr_b16 a[188:189], v247 offset:57344\n"
    "v_mfma_f32_16x16x32_bf16 v[0:3], a[164:167], v[204:207], v[0:3]\n"
    "ds_read_b64_tr_b16 a[190:191], v247 offset:59392\n"
    "v_mfma_f32_16x16x32_bf16 a[248:251], a[164:167], v[212:215], a[248:251]\n"
    "v_exp_f32_e32 v163, v163\n"
    "v_mfma_f32_16x16x32_bf16 a[248:251], a[160:163], v[208:211], a[248:251]\n"
    "v_cvt_pk_bf16_f32 v160, v160, v161\n"
    "v_mfma_f32_16x16x32_bf16 v[56:59], a[160:163], v[216:219], v[56:59]\n"
    "v_cvt_pk_bf16_f32 v154, v156, v157\n"
    "v_mfma_f32_16x16x32_bf16 v[56:59], a[164:167], v[220:223], v[56:59]\n"
    "v_cvt_pk_bf16_f32 v161, v162, v163\n"
    "v_mfma_f32_16x16x32_bf16 v[60:63], a[164:167], v[228:231], v[60:63]\n"
    "v_exp_f32_e32 v196, v196\n"
    "v_mfma_f32_16x16x32_bf16 v[60:63], a[160:163], v[224:227], v[60:63]\n"
    "v_exp_f32_e32 v197, v197\n"
    "v_mfma_f32_16x16x32_bf16 v[64:67], a[160:163], v[232:235], v[64:67]\n"
    "ds_read_b64_tr_b16 a[160:161], v244 offset:53248\n"
    "v_mfma_f32_16x16x32_bf16 v[64:67], a[164:167], v[236:239], v[64:67]\n"
    "ds_read_b64_tr_b16 a[162:163], v244 offset:55296\n"
    "v_cvt_pk_bf16_f32 v155, v158, v159\n"
    "v_mfma_f32_16x16x32_bf16 v[76:79], a[168:171], v[232:235], v[76:79]\n"
    "ds_read_b64_tr_b16 a[164:165], v244 offset:61440\n"
    "v_mfma_f32_16x16x32_bf16 v[76:79], a[172:175], v[236:239], v[76:79]\n"
    "ds_read_b64_tr_b16 a[166:167], v244 offset:63488\n"
    "v_mfma_f32_16x16x32_bf16 v[72:75], a[172:175], v[228:231], v[72:75]\n"
    "v_exp_f32_e32 v198, v198\n"
    "v_mfma_f32_16x16x32_bf16 v[72:75], a[168:171], v[224:227], v[72:75]\n"
    "v_exp_f32_e32 v199, v199\n"
    "v_mfma_f32_16x16x32_bf16 v[68:71], a[168:171], v[216:219], v[68:71]\n"
    "v_cvt_pk_bf16_f32 v194, v196, v197\n"
    "v_mov_b32_e32 v156, 0\n"
    "v_mfma_f32_16x16x32_bf16 v[68:71], a[172:175], v[220:223], v[68:71]\n"
    "v_exp_f32_e32 v188, v188\n"
    "v_mfma_f32_16x16x32_bf16 a[252:255], a[172:175], v[212:215], a[252:255]\n"
    "v_exp_f32_e32 v189, v189\n"
    "v_mfma_f32_16x16x32_bf16 a[252:255], a[168:171], v[208:211], a[252:255]\n"
    "v_cvt_pk_bf16_f32 v195, v198, v199\n"
    "v_mfma_f32_16x16x32_bf16 v[4:7], a[168:171], v[200:203], v[4:7]\n"
    "ds_read_b64_tr_b16 a[168:169], v245 offset:53248\n"
    "v_mfma_f32_16x16x32_bf16 v[4:7], a[172:175], v[204:207], v[4:7]\n"
    "s_waitcnt lgkmcnt(5)\n"
    "ds_read_b64_tr_b16 a[170:171], v245 offset:55296\n"
    "v_mfma_f32_16x16x32_bf16 v[8:11], a[176:179], v[200:203], v[8:11]\n"
    "ds_read_b64_tr_b16 a[172:173], v245 offset:61440\n"
    "v_mfma_f32_16x16x32_bf16 v[8:11], a[180:183], v[204:207], v[8:11]\n"
    "ds_read_b64_tr_b16 a[174:175], v245 offset:63488\n"
    "v_mov_b32_e32 v157, 0\n"
    "v_mfma_f32_16x16x32_bf16 v[32:35], a[180:183], v[212:215], v[32:35]\n"
    "v_exp_f32_e32 v190, v190\n"
    "v_mfma_f32_16x16x32_bf16 v[32:35], a[176:179], v[208:211], v[32:35]\n"
    "v_exp_f32_e32 v191, v191\n"
    "v_mfma_f32_16x16x32_bf16 v[80:83], a[176:179], v[216:219], v[80:83]\n"
    "v_mov_b32_e32 v158, 0\n"
    "v_mfma_f32_16x16x32_bf16 v[80:83], a[180:183], v[220:223], v[80:83]\n"
    "v_cvt_pk_bf16_f32 v186, v188, v189\n"
    "v_mfma_f32_16x16x32_bf16 v[84:87], a[180:183], v[228:231], v[84:87]\n"
    "v_exp_f32_e32 v180, v180\n"
    "v_mfma_f32_16x16x32_bf16 v[84:87], a[176:179], v[224:227], v[84:87]\n"
    "v_exp_f32_e32 v181, v181\n"
    "v_mfma_f32_16x16x32_bf16 v[88:91], a[176:179], v[232:235], v[88:91]\n"
    "ds_read_b64_tr_b16 a[176:177], v246 offset:53248\n"
    "v_mfma_f32_16x16x32_bf16 v[88:91], a[180:183], v[236:239], v[88:91]\n"
    "ds_read_b64_tr_b16 a[178:179], v246 offset:55296\n"
    "v_mfma_f32_16x16x32_bf16 v[100:103], a[184:187], v[232:235], v[100:103]\n"
    "ds_read_b64_tr_b16 a[180:181], v246 offset:61440\n"
    "v_mfma_f32_16x16x32_bf16 v[100:103], a[188:191], v[236:239], v[100:103]\n"
    "ds_read_b64_tr_b16 a[182:183], v246 offset:63488\n"
    "v_mfma_f32_16x16x32_bf16 v[96:99], a[188:191], v[228:231], v[96:99]\n"
    "v_cvt_pk_bf16_f32 v187, v190, v191\n"
    "v_mov_b32_e32 v159, 0\n"
    "v_mfma_f32_16x16x32_bf16 v[96:99], a[184:187], v[224:227], v[96:99]\n"
    "v_exp_f32_e32 v182, v182\n"
    "v_mfma_f32_16x16x32_bf16 v[92:95], a[184:187], v[216:219], v[92:95]\n"
    "v_exp_f32_e32 v183, v183\n"
    "v_mfma_f32_16x16x32_bf16 v[92:95], a[188:191], v[220:223], v[92:95]\n"
    "v_cvt_pk_bf16_f32 v178, v180, v181\n"
    "v_mfma_f32_16x16x32_bf16 v[36:39], a[188:191], v[212:215], v[36:39]\n"
    "v_exp_f32_e32 v172, v172\n"
    "v_mfma_f32_16x16x32_bf16 v[36:39], a[184:187], v[208:211], v[36:39]\n"
    "v_exp_f32_e32 v173, v173\n"
    "v_mfma_f32_16x16x32_bf16 v[12:15], a[184:187], v[200:203], v[12:15]\n"
    "s_mov_b32 m0, s49\n"
    "ds_read_b64_tr_b16 a[184:185], v247 offset:53248\n"
    "v_mfma_f32_16x16x32_bf16 v[12:15], a[188:191], v[204:207], v[12:15]\n"
    "s_waitcnt lgkmcnt(5)\n"
    "ds_read_b64_tr_b16 a[186:187], v247 offset:55296\n"
    "v_add_u32_e32 v252, 0x80, v252\n"
    "v_mfma_f32_16x16x32_bf16 v[16:19], a[160:163], v[200:203], v[16:19]\n"
    "ds_read_b64_tr_b16 a[188:189], v247 offset:61440\n"
    "v_mfma_f32_16x16x32_bf16 v[16:19], a[164:167], v[204:207], v[16:19]\n"
    "ds_read_b64_tr_b16 a[190:191], v247 offset:63488\n"
    "v_mfma_f32_16x16x32_bf16 v[40:43], a[164:167], v[212:215], v[40:43]\n"
    "buffer_store_dwordx2 v[154:155], v253, s[24:27], 0 offen offset:0 sc1\n"
    "v_cvt_pk_bf16_f32 v179, v182, v183\n"
    "v_mfma_f32_16x16x32_bf16 v[40:43], a[160:163], v[208:211], v[40:43]\n"
    "v_exp_f32_e32 v174, v174\n"
    "v_mfma_f32_16x16x32_bf16 v[104:107], a[160:163], v[216:219], v[104:107]\n"
    "v_exp_f32_e32 v175, v175\n"
    "v_mfma_f32_16x16x32_bf16 v[104:107], a[164:167], v[220:223], v[104:107]\n"
    "s_waitcnt vmcnt(9)\n"
    "v_mfma_f32_16x16x32_bf16 v[108:111], a[164:167], v[228:231], v[108:111]\n"
    "s_barrier\n"
    "v_mfma_f32_16x16x32_bf16 v[108:111], a[160:163], v[224:227], v[108:111]\n"
    "ds_write_addtid_b32 v192 offset:16384\n"
    "v_cvt_pk_bf16_f32 v170, v172, v173\n"
    "v_mfma_f32_16x16x32_bf16 v[112:115], a[160:163], v[232:235], v[112:115]\n"
    "ds_read_b128 a[160:163], v243 offset:20480\n"
    "v_mfma_f32_16x16x32_bf16 v[112:115], a[164:167], v[236:239], v[112:115]\n"
    "ds_read_b128 a[164:167], v240 offset:16384\n"
    "v_mfma_f32_16x16x32_bf16 v[124:127], a[168:171], v[232:235], v[124:127]\n"
    "v_exp_f32_e32 v164, v164\n"
    "v_mfma_f32_16x16x32_bf16 v[124:127], a[172:175], v[236:239], v[124:127]\n"
    "v_exp_f32_e32 v165, v165\n"
    "v_mfma_f32_16x16x32_bf16 v[120:123], a[172:175], v[228:231], v[120:123]\n"
    "ds_write_addtid_b32 v193 offset:16640\n"
    "v_cvt_pk_bf16_f32 v171, v174, v175\n"
    "v_mfma_f32_16x16x32_bf16 v[120:123], a[168:171], v[224:227], v[120:123]\n"
    "ds_write_addtid_b32 v184 offset:12288\n"
    "v_mfma_f32_16x16x32_bf16 v[116:119], a[168:171], v[216:219], v[116:119]\n"
    "v_exp_f32_e32 v166, v166\n"
    "v_mfma_f32_16x16x32_bf16 v[116:119], a[172:175], v[220:223], v[116:119]\n"
    "v_exp_f32_e32 v167, v167\n"
    "v_mfma_f32_16x16x32_bf16 v[44:47], a[172:175], v[212:215], v[44:47]\n"
    "ds_write_addtid_b32 v185 offset:12544\n"
    "v_cvt_pk_bf16_f32 v162, v164, v165\n"
    "v_mfma_f32_16x16x32_bf16 v[44:47], a[168:171], v[208:211], v[44:47]\n"
    "v_cvt_pk_bf16_f32 v163, v166, v167\n"
    "v_mfma_f32_16x16x32_bf16 v[20:23], a[168:171], v[200:203], v[20:23]\n"
    "ds_read_b128 a[168:171], v241 offset:16384\n"
    "v_mfma_f32_16x16x32_bf16 v[20:23], a[172:175], v[204:207], v[20:23]\n"
    "s_waitcnt lgkmcnt(7)\n"
    "ds_read_b128 a[172:175], v240 offset:20480\n"
    "v_mfma_f32_16x16x32_bf16 v[24:27], a[176:179], v[200:203], v[24:27]\n"
    "ds_write_addtid_b32 v176 offset:8192\n"
    "v_mfma_f32_16x16x32_bf16 v[24:27], a[180:183], v[204:207], v[24:27]\n"
    "ds_write_addtid_b32 v177 offset:8448\n"
    "v_mfma_f32_16x16x32_bf16 v[48:51], a[180:183], v[212:215], v[48:51]\n"
    "ds_write_addtid_b32 v168 offset:4096\n"
    "v_mfma_f32_16x16x32_bf16 v[48:51], a[176:179], v[208:211], v[48:51]\n"
    "ds_write_addtid_b32 v169 offset:4352\n"
    "v_mfma_f32_16x16x32_bf16 v[128:131], a[176:179], v[216:219], v[128:131]\n"
    "ds_write_addtid_b32 v160 offset:0\n"
    "v_mfma_f32_16x16x32_bf16 v[128:131], a[180:183], v[220:223], v[128:131]\n"
    "ds_write_addtid_b32 v161 offset:256\n"
    "v_mfma_f32_16x16x32_bf16 v[132:135], a[180:183], v[228:231], v[132:135]\n"
    "ds_write_addtid_b32 v194 offset:16896\n"
    "v_mfma_f32_16x16x32_bf16 v[132:135], a[176:179], v[224:227], v[132:135]\n"
    "ds_write_addtid_b32 v195 offset:17152\n"
    "v_mfma_f32_16x16x32_bf16 v[136:139], a[176:179], v[232:235], v[136:139]\n"
    "ds_read_b128 a[176:179], v241 offset:20480\n"
    "v_mfma_f32_16x16x32_bf16 v[136:139], a[180:183], v[236:239], v[136:139]\n"
    "ds_read_b128 a[180:183], v242 offset:16384\n"
    "v_mfma_f32_16x16x32_bf16 v[148:151], a[184:187], v[232:235], v[148:151]\n"
    "ds_write_addtid_b32 v186 offset:12800\n"
    "v_mfma_f32_16x16x32_bf16 v[148:151], a[188:191], v[236:239], v[148:151]\n"
    "ds_write_addtid_b32 v187 offset:13056\n"
    "v_mfma_f32_16x16x32_bf16 v[144:147], a[188:191], v[228:231], v[144:147]\n"
    "ds_write_addtid_b32 v178 offset:8704\n"
    "v_mfma_f32_16x16x32_bf16 v[144:147], a[184:187], v[224:227], v[144:147]\n"
    "ds_write_addtid_b32 v179 offset:8960\n"
    "v_mfma_f32_16x16x32_bf16 v[140:143], a[184:187], v[216:219], v[140:143]\n"
    "s_sub_u32 s44, s44, 1\n"
    "ds_write_addtid_b32 v170 offset:4608\n"
    "v_mfma_f32_16x16x32_bf16 v[140:143], a[188:191], v[220:223], v[140:143]\n"
    "ds_write_addtid_b32 v171 offset:4864\n"
    "v_mfma_f32_16x16x32_bf16 v[52:55], a[188:191], v[212:215], v[52:55]\n"
    "ds_write_addtid_b32 v162 offset:512\n"
    "v_mfma_f32_16x16x32_bf16 v[52:55], a[184:187], v[208:211], v[52:55]\n"
    "ds_write_addtid_b32 v163 offset:768\n"
    "v_mfma_f32_16x16x32_bf16 v[28:31], a[184:187], v[200:203], v[28:31]\n"
    "ds_read_b128 a[184:187], v243 offset:16384\n"
    "v_mfma_f32_16x16x32_bf16 v[28:31], a[188:191], v[204:207], v[28:31]\n"
    "s_waitcnt lgkmcnt(15)\n"
    "ds_read_b128 a[188:191], v242 offset:20480\n"
    "s_cbranch_scc0 .Lat_g\n"
    "s_mov_b32 s44, 1\n"
    "s_bitcmp0_b32 %1, 0\n"
    "s_cbranch_scc1 .Lat_p\n"
    ".Lat_tail:\n"
    "v_mfma_f32_16x16x32_bf16 v[216:219], a[160:163], a[92:95], 0\n"
    "s_mov_b32 s45, 5775360\n"
    "v_mbcnt_lo_u32_b32 v253, -1, 0\n"
    "v_mfma_f32_16x16x32_bf16 v[216:219], a[164:167], a[64:67], v[216:219]\n"
    "s_add_u32 m0, s43, 49152\n"
    "v_mbcnt_hi_u32_b32 v253, -1, v253\n"
    "v_mfma_f32_16x16x32_bf16 v[224:227], a[164:167], a[96:99], 0\n"
    "buffer_load_dwordx4 v248, s[8:11], s40 offen offset:0 sc1 lds\n"
    "v_mfma_f32_16x16x32_bf16 v[224:227], a[160:163], a[124:127], v[224:227]\n"
    "s_lshl_b32 s56, %2, 15\n"
    "v_and_b32_e32 v254, 3, v253\n"
    "v_mfma_f32_16x16x32_bf16 v[232:235], a[160:163], a[156:159], 0\n"
    "ds_read_b128 a[160:163], v243 offset:22528\n"
    "v_mfma_f32_16x16x32_bf16 v[232:235], a[164:167], a[128:131], v[232:235]\n"
    "ds_read_b128 a[164:167], v240 offset:18432\n"
    "v_mfma_f32_16x16x32_bf16 v[232:235], a[168:171], a[132:135], v[232:235]\n"
    "s_waitcnt lgkmcnt(0)\n"
    "s_barrier\n"
    "v_mfma_f32_16x16x32_bf16 v[232:235], a[172:175], a[136:139], v[232:235]\n"
    "buffer_load_dwordx4 v248, s[8:11], s40 offen offset:1024 sc1 lds\n"
    "v_mfma_f32_16x16x32_bf16 v[224:227], a[172:175], a[104:107], v[224:227]\n"
    "s_add_u32 s57, s56, 5505024\n"
    "v_lshlrev_b32_e32 v254, 2, v254\n"
    "v_mfma_f32_16x16x32_bf16 v[224:227], a[168:171], a[100:103], v[224:227]\n"
    "s_add_u32 s56, s56, 5373952\n"
    "ds_read2st64_b32 v[164:165], v250 offset0:0 offset1:1\n"
    "v_and_b32_e32 v158, 15, v253\n"
    "v_mfma_f32_16x16x32_bf16 v[216:219], a[168:171], a[68:71], v[216:219]\n"
    "ds_read_b128 a[168:171], v241 offset:18432\n"
    "v_mfma_f32_16x16x32_bf16 v[216:219], a[172:175], a[72:75], v[216:219]\n"
    "ds_read_b128 a[172:175], v240 offset:22528\n"
    "v_mfma_f32_16x16x32_bf16 v[216:219], a[176:179], a[76:79], v[216:219]\n"
    "buffer_load_dwordx4 v248, s[8:11], s40 offen offset:2048 sc1 lds\n"
    "ds_read2st64_b32 v[166:167], v250 offset0:2 offset1:3\n"
    "v_mfma_f32_16x16x32_bf16 v[216:219], a[180:183], a[80:83], v[216:219]\n"
    "s_lshl_b32 s47, %2, 7\n"
    "v_lshlrev_b32_e32 v158, 9, v158\n"
    "v_mfma_f32_16x16x32_bf16 v[224:227], a[180:183], a[112:115], v[224:227]\n"
    "s_add_u32 s47, s47, s35\n"
    "v_and_or_b32 v158, v253, 48, v158\n"
    "v_mfma_f32_16x16x32_bf16 v[224:227], a[176:179], a[108:111], v[224:227]\n"
    "ds_read2st64_b32 v[172:173], v250 offset0:16 offset1:17\n"
    "v_bfe_u32 v252, v253, 4, 2\n"
    "v_mfma_f32_16x16x32_bf16 v[232:235], a[176:179], a[140:143], v[232:235]\n"
    "ds_read_b128 a[176:179], v241 offset:22528\n"
    "buffer_load_dwordx4 v248, s[8:11], s40 offen offset:3072 sc1 lds\n"
    "v_mfma_f32_16x16x32_bf16 v[232:235], a[180:183], a[144:147], v[232:235]\n"
    "s_add_u32 s40, s40, 32768\n"
    "ds_read_b128 a[180:183], v242 offset:18432\n"
    "v_mfma_f32_16x16x32_bf16 v[232:235], a[184:187], a[148:151], v[232:235]\n"
    "s_add_u32 m0, s43, 53248\n"
    "v_lshlrev_b32_e32 v252, 1, v252\n"
    "v_mfma_f32_16x16x32_bf16 v[232:235], a[188:191], a[152:155], v[232:235]\n"
    "ds_read2st64_b32 v[174:175], v250 offset0:18 offset1:19\n"
    "v_xor_b32_e32 v252, v252, v253\n"
    "v_mfma_f32_16x16x32_bf16 v[224:227], a[188:191], a[120:123], v[224:227]\n"
    "buffer_load_dwordx4 v248, s[8:11], s41 offen offset:0 sc1 lds\n"
    "v_mfma_f32_16x16x32_bf16 v[224:227], a[184:187], a[116:119], v[224:227]\n"
    "v_and_b32_e32 v252, 7, v252\n"
    "v_mfma_f32_16x16x32_bf16 v[216:219], a[184:187], a[84:87], v[216:219]\n"
    "ds_read_b128 a[184:187], v243 offset:18432\n"
    "v_mfma_f32_16x16x32_bf16 v[216:219], a[188:191], a[88:91], v[216:219]\n"
    "ds_read_b128 a[188:191], v242 offset:22528\n"
    "v_mfma_f32_16x16x32_bf16 v[220:223], a[160:163], a[92:95], 0\n"
    "buffer_load_dwordx4 v248, s[8:11], s41 offen offset:1024 sc1 lds\n"
    "ds_read2st64_b32 v[180:181], v250 offset0:32 offset1:33\n"
    "v_mfma_f32_16x16x32_bf16 v[220:223], a[164:167], a[64:67], v[220:223]\n"
    "v_lshlrev_b32_e32 v252, 4, v252\n"
    "v_mfma_f32_16x16x32_bf16 v[228:231], a[164:167], a[96:99], 0\n"
    "ds_read2st64_b32 v[182:183], v250 offset0:34 offset1:35\n"
    "v_lshrrev_b32_e32 v255, 3, v253\n"
    "v_mfma_f32_16x16x32_bf16 v[228:231], a[160:163], a[124:127], v[228:231]\n"
    "ds_read2st64_b32 v[188:189], v250 offset0:48 offset1:49\n"
    "v_lshl_add_u32 v252, v255, 9, v252\n"
    "v_mfma_f32_16x16x32_bf16 v[236:239], a[160:163], a[156:159], 0\n"
    "ds_read_b64_tr_b16 a[160:161], v244 offset:0\n"
    "buffer_load_dwordx4 v248, s[8:11], s41 offen offset:2048 sc1 lds\n"
    "v_mfma_f32_16x16x32_bf16 v[236:239], a[164:167], a[128:131], v[236:239]\n"
    "s_waitcnt lgkmcnt(11)\n"
    "ds_read_b64_tr_b16 a[162:163], v244 offset:2048\n"
    "v_mfma_f32_16x16x32_bf16 v[236:239], a[168:171], a[132:135], v[236:239]\n"
    "ds_read_b64_tr_b16 a[164:165], v244 offset:8192\n"
    "v_mfma_f32_16x16x32_bf16 v[236:239], a[172:175], a[136:139], v[236:239]\n"
    "ds_read_b64_tr_b16 a[166:167], v244 offset:10240\n"
    "v_mfma_f32_16x16x32_bf16 v[228:231], a[172:175], a[104:107], v[228:231]\n"
    "buffer_load_dwordx4 v248, s[8:11], s41 offen offset:3072 sc1 lds\n"
    "ds_read2st64_b32 v[190:191], v250 offset0:50 offset1:51\n"
    "v_mfma_f32_16x16x32_bf16 v[228:231], a[168:171], a[100:103], v[228:231]\n"
    "s_add_u32 s41, s41, 32768\n"
    "s_add_u32 s61, s47, 0\n"
    "v_lshlrev_b32_e32 v253, 4, v253\n"
    "v_mfma_f32_16x16x32_bf16 v[220:223], a[168:171], a[68:71], v[220:223]\n"
    "s_mov_b32 m0, s49\n"
    "ds_read_b64_tr_b16 a[168:169], v245 offset:0\n"
    "v_mfma_f32_16x16x32_bf16 v[220:223], a[172:175], a[72:75], v[220:223]\n"
    "s_waitcnt lgkmcnt(12)\n"
    "ds_read_b64_tr_b16 a[170:171], v245 offset:2048\n"
    "v_mfma_f32_16x16x32_bf16 v[220:223], a[176:179], a[76:79], v[220:223]\n"
    "ds_read_b64_tr_b16 a[172:173], v245 offset:8192\n"
    "buffer_load_dwordx4 a[224:227], v158, s[12:15], s61 offen offset:0\n"
    "v_mfma_f32_16x16x32_bf16 v[220:223], a[180:183], a[80:83], v[220:223]\n"
    "ds_read_b64_tr_b16 a[174:175], v245 offset:10240\n"
    "v_mfma_f32_16x16x32_bf16 v[228:231], a[180:183], a[112:115], v[228:231]\n"
    "ds_read2st64_b32 v[196:197], v250 offset0:64 offset1:65\n"
    "v_mfma_f32_16x16x32_bf16 v[228:231], a[176:179], a[108:111], v[228:231]\n"
    "ds_read2st64_b32 v[198:199], v250 offset0:66 offset1:67\n"
    "v_mfma_f32_16x16x32_bf16 v[236:239], a[176:179], a[140:143], v[236:239]\n"
    "ds_read_b64_tr_b16 a[176:177], v246 offset:0\n"
    "v_mfma_f32_16x16x32_bf16 v[236:239], a[180:183], a[144:147], v[236:239]\n"
    "s_waitcnt lgkmcnt(15)\n"
    "ds_read_b64_tr_b16 a[178:179], v246 offset:2048\n"
    "v_mfma_f32_16x16x32_bf16 v[236:239], a[184:187], a[148:151], v[236:239]\n"
    "ds_read_b64_tr_b16 a[180:181], v246 offset:8192\n"
    "v_mfma_f32_16x16x32_bf16 v[236:239], a[188:191], a[152:155], v[236:239]\n"
    "ds_read_b64_tr_b16 a[182:183], v246 offset:10240\n"
    "v_mfma_f32_16x16x32_bf16 v[228:231], a[188:191], a[120:123], v[228:231]\n"
    "v_exp_f32_e32 v232, v232\n"
    "v_mfma_f32_16x16x32_bf16 v[228:231], a[184:187], a[116:119], v[228:231]\n"
    "v_exp_f32_e32 v233, v233\n"
    "v_mfma_f32_16x16x32_bf16 v[220:223], a[184:187], a[84:87], v[220:223]\n"
    "ds_read_b64_tr_b16 a[184:185], v247 offset:0\n"
    "v_mfma_f32_16x16x32_bf16 v[220:223], a[188:191], a[88:91], v[220:223]\n"
    "s_waitcnt lgkmcnt(5)\n"
    "ds_read_b64_tr_b16 a[186:187], v247 offset:2048\n"
    "v_mfma_f32_16x16x32_bf16 v[0:3], a[160:163], v[160:163], v[0:3]\n"
    "ds_read_b64_tr_b16 a[188:189], v247 offset:8192\n"
    "buffer_load_dwordx4 a[228:231], v158, s[12:15], s61 offen offset:64\n"
    "v_mfma_f32_16x16x32_bf16 v[0:3], a[164:167], v[164:167], v[0:3]\n"
    "s_add_u32 s61, s47, 8192\n"
    "ds_read_b64_tr_b16 a[190:191], v247 offset:10240\n"
    "v_mfma_f32_16x16x32_bf16 a[248:251], a[164:167], v[172:175], a[248:251]\n"
    "v_exp_f32_e32 v234, v234\n"
    "v_mfma_f32_16x16x32_bf16 a[248:251], a[160:163], v[168:171], a[248:251]\n"
    "v_exp_f32_e32 v235, v235\n"
    "v_mfma_f32_16x16x32_bf16 v[56:59], a[160:163], v[176:179], v[56:59]\n"
    "v_cvt_pk_bf16_f32 v232, v232, v233\n"
    "v_mfma_f32_16x16x32_bf16 v[56:59], a[164:167], v[180:183], v[56:59]\n"
    "v_exp_f32_e32 v224, v224\n"
    "v_mfma_f32_16x16x32_bf16 v[60:63], a[164:167], v[188:191], v[60:63]\n"
    "v_exp_f32_e32 v225, v225\n"
    "v_mfma_f32_16x16x32_bf16 v[60:63], a[160:163], v[184:187], v[60:63]\n"
    "v_cvt_pk_bf16_f32 v233, v234, v235\n"
    "v_mfma_f32_16x16x32_bf16 v[64:67], a[160:163], v[192:195], v[64:67]\n"
    "ds_read_b64_tr_b16 a[160:161], v244 offset:4096\n"
    "v_mfma_f32_16x16x32_bf16 v[64:67], a[164:167], v[196:199], v[64:67]\n"
    "ds_read_b64_tr_b16 a[162:163], v244 offset:6144\n"
    "v_mfma_f32_16x16x32_bf16 v[76:79], a[168:171], v[192:195], v[76:79]\n"
    "ds_read_b64_tr_b16 a[164:165], v244 offset:12288\n"
    "v_mfma_f32_16x16x32_bf16 v[76:79], a[172:175], v[196:199], v[76:79]\n"
    "ds_read_b64_tr_b16 a[166:167], v244 offset:14336\n"
    "v_mfma_f32_16x16x32_bf16 v[72:75], a[172:175], v[188:191], v[72:75]\n"
    "buffer_load_dwordx4 a[232:235], v158, s[12:15], s61 offen offset:0\n"
    "v_mfma_f32_16x16x32_bf16 v[72:75], a[168:171], v[184:187], v[72:75]\n"
    "v_exp_f32_e32 v226, v226\n"
    "v_mfma_f32_16x16x32_bf16 v[68:71], a[168:171], v[176:179], v[68:71]\n"
    "s_waitcnt vmcnt(11)\n"
    "s_barrier\n"
    "v_mfma_f32_16x16x32_bf16 v[68:71], a[172:175], v[180:183], v[68:71]\n"
    "v_exp_f32_e32 v227, v227\n"
    "v_mfma_f32_16x16x32_bf16 a[252:255], a[172:175], v[172:175], a[252:255]\n"
    "v_cvt_pk_bf16_f32 v224, v224, v225\n"
    "v_mfma_f32_16x16x32_bf16 a[252:255], a[168:171], v[168:171], a[252:255]\n"
    "v_exp_f32_e32 v216, v216\n"
    "v_mfma_f32_16x16x32_bf16 v[4:7], a[168:171], v[160:163], v[4:7]\n"
    "ds_read_b64_tr_b16 a[168:169], v245 offset:4096\n"
    "v_mfma_f32_16x16x32_bf16 v[4:7], a[172:175], v[164:167], v[4:7]\n"
    "s_waitcnt lgkmcnt(9)\n"
    "ds_read_b64_tr_b16 a[170:171], v245 offset:6144\n"
    "v_mfma_f32_16x16x32_bf16 v[8:11], a[176:179], v[160:163], v[8:11]\n"
    "ds_read_b64_tr_b16 a[172:173], v245 offset:12288\n"
    "v_mfma_f32_16x16x32_bf16 v[8:11], a[180:183], v[164:167], v[8:11]\n"
    "ds_read_b64_tr_b16 a[174:175], v245 offset:14336\n"
    "v_mfma_f32_16x16x32_bf16 v[32:35], a[180:183], v[172:175], v[32:35]\n"
    "v_exp_f32_e32 v217, v217\n"
    "v_mfma_f32_16x16x32_bf16 v[32:35], a[176:179], v[168:171], v[32:35]\n"
    "v_cvt_pk_bf16_f32 v225, v226, v227\n"
    "v_mfma_f32_16x16x32_bf16 v[80:83], a[176:179], v[176:179], v[80:83]\n"
    "buffer_load_dwordx4 a[236:239], v158, s[12:15], s61 offen offset:64\n"
    "v_mfma_f32_16x16x32_bf16 v[80:83], a[180:183], v[180:183], v[80:83]\n"
    "s_add_u32 s61, s47, 16384\n"
    "v_exp_f32_e32 v218, v218\n"
    "v_mfma_f32_16x16x32_bf16 v[84:87], a[180:183], v[188:191], v[84:87]\n"
    "v_exp_f32_e32 v219, v219\n"
    "v_mfma_f32_16x16x32_bf16 v[84:87], a[176:179], v[184:187], v[84:87]\n"
    "v_cvt_pk_bf16_f32 v216, v216, v217\n"
    "v_mfma_f32_16x16x32_bf16 v[88:91], a[176:179], v[192:195], v[88:91]\n"
    "ds_read_b64_tr_b16 a[176:177], v246 offset:4096\n"
    "v_mfma_f32_16x16x32_bf16 v[88:91], a[180:183], v[196:199], v[88:91]\n"
    "s_waitcnt lgkmcnt(9)\n"
    "ds_read_b64_tr_b16 a[178:179], v246 offset:6144\n"
    "v_mfma_f32_16x16x32_bf16 v[100:103], a[184:187], v[192:195], v[100:103]\n"
    "ds_read_b64_tr_b16 a[180:181], v246 offset:12288\n"
    "v_mfma_f32_16x16x32_bf16 v[100:103], a[188:191], v[196:199], v[100:103]\n"
    "ds_read_b64_tr_b16 a[182:183], v246 offset:14336\n"
    "v_mfma_f32_16x16x32_bf16 v[96:99], a[188:191], v[188:191], v[96:99]\n"
    "v_cvt_pk_bf16_f32 v217, v218, v219\n"
    "v_mfma_f32_16x16x32_bf16 v[96:99], a[184:187], v[184:187], v[96:99]\n"
    "v_exp_f32_e32 v236, v236\n"
    "v_mfma_f32_16x16x32_bf16 v[92:95], a[184:187], v[176:179], v[92:95]\n"
    "v_exp_f32_e32 v237, v237\n"
    "v_mfma_f32_16x16x32_bf16 v[92:95], a[188:191], v[180:183], v[92:95]\n"
    "v_exp_f32_e32 v238, v238\n"
    "v_mfma_f32_16x16x32_bf16 v[36:39], a[188:191], v[172:175], v[36:39]\n"
    "buffer_load_dwordx4 a[240:243], v158, s[12:15], s61 offen offset:0\n"
    "v_mfma_f32_16x16x32_bf16 v[36:39], a[184:187], v[168:171], v[36:39]\n"
    "v_exp_f32_e32 v239, v239\n"
    "v_mfma_f32_16x16x32_bf16 v[12:15], a[184:187], v[160:163], v[12:15]\n"
    "ds_read_b64_tr_b16 a[184:185], v247 offset:4096\n"
    "v_mfma_f32_16x16x32_bf16 v[12:15], a[188:191], v[164:167], v[12:15]\n"
    "s_waitcnt lgkmcnt(9)\n"
    "ds_read_b64_tr_b16 a[186:187], v247 offset:6144\n"
    "v_mfma_f32_16x16x32_bf16 v[16:19], a[160:163], v[160:163], v[16:19]\n"
    "ds_read_b64_tr_b16 a[188:189], v247 offset:12288\n"
    "v_mfma_f32_16x16x32_bf16 v[16:19], a[164:167], v[164:167], v[16:19]\n"
    "ds_read_b64_tr_b16 a[190:191], v247 offset:14336\n"
    "v_mfma_f32_16x16x32_bf16 v[40:43], a[164:167], v[172:175], v[40:43]\n"
    "v_cvt_pk_bf16_f32 v234, v236, v237\n"
    "v_mfma_f32_16x16x32_bf16 v[40:43], a[160:163], v[168:171], v[40:43]\n"
    "v_exp_f32_e32 v228, v228\n"
    "v_mfma_f32_16x16x32_bf16 v[104:107], a[160:163], v[176:179], v[104:107]\n"
    "ds_write_addtid_b32 v232 offset:16384\n"
    "v_mfma_f32_16x16x32_bf16 v[104:107], a[164:167], v[180:183], v[104:107]\n"
    "ds_write_addtid_b32 v233 offset:16640\n"
    "v_mfma_f32_16x16x32_bf16 v[108:111], a[164:167], v[188:191], v[108:111]\n"
    "v_exp_f32_e32 v229, v229\n"
    "v_mfma_f32_16x16x32_bf16 v[108:111], a[160:163], v[184:187], v[108:111]\n"
    "ds_write_addtid_b32 v224 offset:12288\n"
    "v_mfma_f32_16x16x32_bf16 v[112:115], a[160:163], v[192:195], v[112:115]\n"
    "ds_read_b128 a[160:163], v243 offset:36864\n"
    "buffer_load_dwordx4 a[244:247], v158, s[12:15], s61 offen offset:64\n"
    "v_mfma_f32_16x16x32_bf16 v[112:115], a[164:167], v[196:199], v[112:115]\n"
    "s_waitcnt lgkmcnt(12)\n"
    "ds_read_b128 a[164:167], v240 offset:32768\n"
    "v_mfma_f32_16x16x32_bf16 v[124:127], a[168:171], v[192:195], v[124:127]\n"
    "v_cvt_pk_bf16_f32 v235, v238, v239\n"
    "v_mfma_f32_16x16x32_bf16 v[124:127], a[172:175], v[196:199], v[124:127]\n"
    "ds_write_addtid_b32 v225 offset:12544\n"
    "v_mfma_f32_16x16x32_bf16 v[120:123], a[172:175], v[188:191], v[120:123]\n"
    "v_exp_f32_e32 v230, v230\n"
    "v_mfma_f32_16x16x32_bf16 v[120:123], a[168:171], v[184:187], v[120:123]\n"
    "ds_write_addtid_b32 v216 offset:8192\n"
    "v_mfma_f32_16x16x32_bf16 v[116:119], a[168:171], v[176:179], v[116:119]\n"
    "ds_write_addtid_b32 v217 offset:8448\n"
    "v_mfma_f32_16x16x32_bf16 v[116:119], a[172:175], v[180:183], v[116:119]\n"
    "v_exp_f32_e32 v231, v231\n"
    "v_mfma_f32_16x16x32_bf16 v[44:47], a[172:175], v[172:175], v[44:47]\n"
    "ds_write_addtid_b32 v234 offset:16896\n"
    "v_mfma_f32_16x16x32_bf16 v[44:47], a[168:171], v[168:171], v[44:47]\n"
    "v_cvt_pk_bf16_f32 v226, v228, v229\n"
    "v_mfma_f32_16x16x32_bf16 v[20:23], a[168:171], v[160:163], v[20:23]\n"
    "ds_read_b128 a[168:171], v241 offset:32768\n"
    "v_mfma_f32_16x16x32_bf16 v[20:23], a[172:175], v[164:167], v[20:23]\n"
    "s_waitcnt lgkmcnt(14)\n"
    "ds_read_b128 a[172:175], v240 offset:36864\n"
    "v_mfma_f32_16x16x32_bf16 v[24:27], a[176:179], v[160:163], v[24:27]\n"
    "v_mfma_f32_16x16x32_bf16 v[24:27], a[180:183], v[164:167], v[24:27]\n"
    "v_exp_f32_e32 v220, v220\n"
    "v_mfma_f32_16x16x32_bf16 v[48:51], a[180:183], v[172:175], v[48:51]\n"
    "v_mfma_f32_16x16x32_bf16 v[48:51], a[176:179], v[168:171], v[48:51]\n"
    "ds_write_addtid_b32 v235 offset:17152\n"
    "v_mfma_f32_16x16x32_bf16 v[128:131], a[176:179], v[176:179], v[128:131]\n"
    "v_exp_f32_e32 v221, v221\n"
    "v_mfma_f32_16x16x32_bf16 v[128:131], a[180:183], v[180:183], v[128:131]\n"
    "v_mfma_f32_16x16x32_bf16 v[132:135], a[180:183], v[188:191], v[132:135]\n"
    "ds_write_addtid_b32 v226 offset:12800\n"
    "v_mfma_f32_16x16x32_bf16 v[132:135], a[176:179], v[184:187], v[132:135]\n"
    "v_cvt_pk_bf16_f32 v227, v230, v231\n"
    "v_mfma_f32_16x16x32_bf16 v[136:139], a[176:179], v[192:195], v[136:139]\n"
    "ds_read_b128 a[176:179], v241 offset:36864\n"
    "v_mfma_f32_16x16x32_bf16 v[136:139], a[180:183], v[196:199], v[136:139]\n"
    "s_waitcnt lgkmcnt(14)\n"
    "ds_read_b128 a[180:183], v242 offset:32768\n"
    "v_mfma_f32_16x16x32_bf16 v[148:151], a[184:187], v[192:195], v[148:151]\n"
    "v_exp_f32_e32 v222, v222\n"
    "v_mfma_f32_16x16x32_bf16 v[148:151], a[188:191], v[196:199], v[148:151]\n"
    "v_mfma_f32_16x16x32_bf16 v[144:147], a[188:191], v[188:191], v[144:147]\n"
    "ds_write_addtid_b32 v227 offset:13056\n"
    "v_mfma_f32_16x16x32_bf16 v[144:147], a[184:187], v[184:187], v[144:147]\n"
    "v_exp_f32_e32 v223, v223\n"
    "v_mfma_f32_16x16x32_bf16 v[140:143], a[184:187], v[176:179], v[140:143]\n"
    "v_mfma_f32_16x16x32_bf16 v[140:143], a[188:191], v[180:183], v[140:143]\n"
    "v_mfma_f32_16x16x32_bf16 v[52:55], a[188:191], v[172:175], v[52:55]\n"
    "v_cvt_pk_bf16_f32 v218, v220, v221\n"
    "v_mfma_f32_16x16x32_bf16 v[52:55], a[184:187], v[168:171], v[52:55]\n"
    "v_cvt_pk_bf16_f32 v219, v222, v223\n"
    "v_mfma_f32_16x16x32_bf16 v[28:31], a[184:187], v[160:163], v[28:31]\n"
    "ds_read_b128 a[184:187], v243 offset:32768\n"
    "v_mfma_f32_16x16x32_bf16 v[28:31], a[188:191], v[164:167], v[28:31]\n"
    "s_waitcnt lgkmcnt(12)\n"
    "ds_read_b128 a[188:191], v242 offset:36864\n"
    "v_mfma_f32_16x16x32_bf16 v[176:179], a[160:163], a[92:95], 0\n"
    "ds_write_addtid_b32 v218 offset:8704\n"
    "v_mfma_f32_16x16x32_bf16 v[176:179], a[164:167], a[64:67], v[176:179]\n"
    "v_mfma_f32_16x16x32_bf16 v[184:187], a[164:167], a[96:99], 0\n"
    "v_mfma_f32_16x16x32_bf16 v[184:187], a[160:163], a[124:127], v[184:187]\n"
    "ds_write_addtid_b32 v219 offset:8960\n"
    "v_mfma_f32_16x16x32_bf16 v[192:195], a[160:163], a[156:159], 0\n"
    "ds_read_b128 a[160:163], v243 offset:38912\n"
    "v_mfma_f32_16x16x32_bf16 v[192:195], a[164:167], a[128:131], v[192:195]\n"
    "s_waitcnt lgkmcnt(10)\n"
    "ds_read_b128 a[164:167], v240 offset:34816\n"
    "v_mfma_f32_16x16x32_bf16 v[192:195], a[168:171], a[132:135], v[192:195]\n"
    "v_mfma_f32_16x16x32_bf16 v[192:195], a[172:175], a[136:139], v[192:195]\n"
    "v_mfma_f32_16x16x32_bf16 v[184:187], a[172:175], a[104:107], v[184:187]\n"
    "s_waitcnt lgkmcnt(2)\n"
    "s_barrier\n"
    "v_mfma_f32_16x16x32_bf16 v[184:187], a[168:171], a[100:103], v[184:187]\n"
    "s_add_u32 m0, s43, 0\n"
    "v_mfma_f32_16x16x32_bf16 v[176:179], a[168:171], a[68:71], v[176:179]\n"
    "ds_read_b128 a[168:171], v241 offset:34816\n"
    "buffer_load_dwordx4 v248, s[8:11], s40 offen offset:0 sc1 lds\n"
    "v_mfma_f32_16x16x32_bf16 v[176:179], a[172:175], a[72:75], v[176:179]\n"
    "ds_read_b128 a[172:175], v240 offset:38912\n"
    "v_mfma_f32_16x16x32_bf16 v[176:179], a[176:179], a[76:79], v[176:179]\n"
    "ds_read2st64_b32 v[220:221], v250 offset0:32 offset1:33\n"
    "v_mfma_f32_16x16x32_bf16 v[176:179], a[180:183], a[80:83], v[176:179]\n"
    "v_mfma_f32_16x16x32_bf16 v[184:187], a[180:183], a[112:115], v[184:187]\n"
    "buffer_load_dwordx4 v248, s[8:11], s40 offen offset:1024 sc1 lds\n"
    "v_mfma_f32_16x16x32_bf16 v[184:187], a[176:179], a[108:111], v[184:187]\n"
    "v_mfma_f32_16x16x32_bf16 v[192:195], a[176:179], a[140:143], v[192:195]\n"
    "ds_read_b128 a[176:179], v241 offset:38912\n"
    "v_mfma_f32_16x16x32_bf16 v[192:195], a[180:183], a[144:147], v[192:195]\n"
    "ds_read_b128 a[180:183], v242 offset:34816\n"
    "v_mfma_f32_16x16x32_bf16 v[192:195], a[184:187], a[148:151], v[192:195]\n"
    "buffer_load_dwordx4 v248, s[8:11], s40 offen offset:2048 sc1 lds\n"
    "v_mfma_f32_16x16x32_bf16 v[192:195], a[188:191], a[152:155], v[192:195]\n"
    "ds_read2st64_b32 v[222:223], v250 offset0:34 offset1:35\n"
    "v_mfma_f32_16x16x32_bf16 v[184:187], a[188:191], a[120:123], v[184:187]\n"
    "v_mfma_f32_16x16x32_bf16 v[184:187], a[184:187], a[116:119], v[184:187]\n"
    "v_exp_f32_e32 v192, v192\n"
    "v_mfma_f32_16x16x32_bf16 v[176:179], a[184:187], a[84:87], v[176:179]\n"
    "ds_read_b128 a[184:187], v243 offset:34816\n"
    "buffer_load_dwordx4 v248, s[8:11], s40 offen offset:3072 sc1 lds\n"
    "v_mfma_f32_16x16x32_bf16 v[176:179], a[188:191], a[88:91], v[176:179]\n"
    "s_waitcnt lgkmcnt(7)\n"
    "s_add_u32 s40, s40, 32768\n"
    "ds_read_b128 a[188:191], v242 offset:38912\n"
    "v_mfma_f32_16x16x32_bf16 v[180:183], a[160:163], a[92:95], 0\n"
    "s_add_u32 m0, s43, 4096\n"
    "ds_read2st64_b32 v[228:229], v250 offset0:48 offset1:49\n"
    "v_mfma_f32_16x16x32_bf16 v[180:183], a[164:167], a[64:67], v[180:183]\n"
    "v_exp_f32_e32 v193, v193\n"
    "v_mfma_f32_16x16x32_bf16 v[188:191], a[164:167], a[96:99], 0\n"
    "buffer_load_dwordx4 v248, s[8:11], s41 offen offset:0 sc1 lds\n"
    "v_exp_f32_e32 v194, v194\n"
    "v_mfma_f32_16x16x32_bf16 v[188:191], a[160:163], a[124:127], v[188:191]\n"
    "ds_read2st64_b32 v[230:231], v250 offset0:50 offset1:51\n"
    "v_mfma_f32_16x16x32_bf16 v[196:199], a[160:163], a[156:159], 0\n"
    "ds_read_b64_tr_b16 a[160:161], v244 offset:16384\n"
    "v_mfma_f32_16x16x32_bf16 v[196:199], a[164:167], a[128:131], v[196:199]\n"
    "s_waitcnt lgkmcnt(9)\n"
    "ds_read_b64_tr_b16 a[162:163], v244 offset:18432\n"
    "v_mfma_f32_16x16x32_bf16 v[196:199], a[168:171], a[132:135], v[196:199]\n"
    "ds_read_b64_tr_b16 a[164:165], v244 offset:24576\n"
    "buffer_load_dwordx4 v248, s[8:11], s41 offen offset:1024 sc1 lds\n"
    "v_mfma_f32_16x16x32_bf16 v[196:199], a[172:175], a[136:139], v[196:199]\n"
    "ds_read_b64_tr_b16 a[166:167], v244 offset:26624\n"
    "v_mfma_f32_16x16x32_bf16 v[188:191], a[172:175], a[104:107], v[188:191]\n"
    "v_exp_f32_e32 v195, v195\n"
    "v_mfma_f32_16x16x32_bf16 v[188:191], a[168:171], a[100:103], v[188:191]\n"
    "ds_read2st64_b32 v[236:237], v250 offset0:64 offset1:65\n"
    "v_cvt_pk_bf16_f32 v192, v192, v193\n"
    "v_mfma_f32_16x16x32_bf16 v[180:183], a[168:171], a[68:71], v[180:183]\n"
    "ds_read_b64_tr_b16 a[168:169], v245 offset:16384\n"
    "buffer_load_dwordx4 v248, s[8:11], s41 offen offset:2048 sc1 lds\n"
    "v_mfma_f32_16x16x32_bf16 v[180:183], a[172:175], a[72:75], v[180:183]\n"
    "s_waitcnt lgkmcnt(11)\n"
    "ds_read_b64_tr_b16 a[170:171], v245 offset:18432\n"
    "v_mfma_f32_16x16x32_bf16 v[180:183], a[176:179], a[76:79], v[180:183]\n"
    "ds_read_b64_tr_b16 a[172:173], v245 offset:24576\n"
    "v_mfma_f32_16x16x32_bf16 v[180:183], a[180:183], a[80:83], v[180:183]\n"
    "ds_read_b64_tr_b16 a[174:175], v245 offset:26624\n"
    "v_mfma_f32_16x16x32_bf16 v[188:191], a[180:183], a[112:115], v[188:191]\n"
    "buffer_load_dwordx4 v248, s[8:11], s41 offen offset:3072 sc1 lds\n"
    "v_mfma_f32_16x16x32_bf16 v[188:191], a[176:179], a[108:111], v[188:191]\n"
    "s_add_u32 s41, s41, 32768\n"
    "ds_read2st64_b32 v[238:239], v250 offset0:66 offset1:67\n"
    "v_mfma_f32_16x16x32_bf16 v[196:199], a[176:179], a[140:143], v[196:199]\n"
    "s_mov_b32 m0, s49\n"
    "ds_read_b64_tr_b16 a[176:177], v246 offset:16384\n"
    "v_mfma_f32_16x16x32_bf16 v[196:199], a[180:183], a[144:147], v[196:199]\n"
    "s_waitcnt lgkmcnt(13)\n"
    "ds_read_b64_tr_b16 a[178:179], v246 offset:18432\n"
    "v_mfma_f32_16x16x32_bf16 v[196:199], a[184:187], a[148:151], v[196:199]\n"
    "ds_read_b64_tr_b16 a[180:181], v246 offset:24576\n"
    "v_mfma_f32_16x16x32_bf16 v[196:199], a[188:191], a[152:155], v[196:199]\n"
    "ds_read_b64_tr_b16 a[182:183], v246 offset:26624\n"
    "v_mfma_f32_16x16x32_bf16 v[188:191], a[188:191], a[120:123], v[188:191]\n"
    "v_exp_f32_e32 v184, v184\n"
    "v_mfma_f32_16x16x32_bf16 v[188:191], a[184:187], a[116:119], v[188:191]\n"
    "v_exp_f32_e32 v185, v185\n"
    "v_mfma_f32_16x16x32_bf16 v[180:183], a[184:187], a[84:87], v[180:183]\n"
    "ds_read_b64_tr_b16 a[184:185], v247 offset:16384\n"
    "v_cvt_pk_bf16_f32 v193, v194, v195\n"
    "v_mfma_f32_16x16x32_bf16 v[180:183], a[188:191], a[88:91], v[180:183]\n"
    "s_waitcnt lgkmcnt(5)\n"
    "ds_read_b64_tr_b16 a[186:187], v247 offset:18432\n"
    "v_mfma_f32_16x16x32_bf16 v[56:59], a[160:163], v[216:219], v[56:59]\n"
    "ds_read_b64_tr_b16 a[188:189], v247 offset:24576\n"
    "v_mfma_f32_16x16x32_bf16 v[56:59], a[164:167], v[220:223], v[56:59]\n"
    "ds_read_b64_tr_b16 a[190:191], v247 offset:26624\n"
    "v_mfma_f32_16x16x32_bf16 v[60:63], a[164:167], v[228:231], v[60:63]\n"
    "v_exp_f32_e32 v186, v186\n"
    "v_mfma_f32_16x16x32_bf16 v[60:63], a[160:163], v[224:227], v[60:63]\n"
    "v_exp_f32_e32 v187, v187\n"
    "v_mfma_f32_16x16x32_bf16 v[64:67], a[160:163], v[232:235], v[64:67]\n"
    "ds_read_b64_tr_b16 a[160:161], v244 offset:20480\n"
    "v_cvt_pk_bf16_f32 v184, v184, v185\n"
    "v_mfma_f32_16x16x32_bf16 v[64:67], a[164:167], v[236:239], v[64:67]\n"
    "ds_read_b64_tr_b16 a[162:163], v244 offset:22528\n"
    "v_mfma_f32_16x16x32_bf16 v[76:79], a[168:171], v[232:235], v[76:79]\n"
    "ds_read_b64_tr_b16 a[164:165], v244 offset:28672\n"
    "v_mfma_f32_16x16x32_bf16 v[76:79], a[172:175], v[236:239], v[76:79]\n"
    "ds_read_b64_tr_b16 a[166:167], v244 offset:30720\n"
    "v_mfma_f32_16x16x32_bf16 v[72:75], a[172:175], v[228:231], v[72:75]\n"
    "v_exp_f32_e32 v176, v176\n"
    "v_mfma_f32_16x16x32_bf16 v[72:75], a[168:171], v[224:227], v[72:75]\n"
    "v_exp_f32_e32 v177, v177\n"
    "v_mfma_f32_16x16x32_bf16 v[68:71], a[168:171], v[216:219], v[68:71]\n"
    "ds_read_b64_tr_b16 a[168:169], v245 offset:20480\n"
    "v_cvt_pk_bf16_f32 v185, v186, v187\n"
    "v_mfma_f32_16x16x32_bf16 v[68:71], a[172:175], v[220:223], v[68:71]\n"
    "s_waitcnt lgkmcnt(9)\n"
    "ds_read_b64_tr_b16 a[170:171], v245 offset:22528\n"
    "v_mfma_f32_16x16x32_bf16 v[80:83], a[176:179], v[216:219], v[80:83]\n"
    "ds_read_b64_tr_b16 a[172:173], v245 offset:28672\n"
    "v_mfma_f32_16x16x32_bf16 v[80:83], a[180:183], v[220:223], v[80:83]\n"
    "ds_read_b64_tr_b16 a[174:175], v245 offset:30720\n"
    "v_mfma_f32_16x16x32_bf16 v[84:87], a[180:183], v[228:231], v[84:87]\n"
    "v_exp_f32_e32 v178, v178\n"
    "v_mfma_f32_16x16x32_bf16 v[84:87], a[176:179], v[224:227], v[84:87]\n"
    "v_exp_f32_e32 v179, v179\n"
    "v_mfma_f32_16x16x32_bf16 v[88:91], a[176:179], v[232:235], v[88:91]\n"
    "s_waitcnt vmcnt(14)\n"
    "s_barrier\n"
    "v_mfma_f32_16x16x32_bf16 v[88:91], a[180:183], v[236:239], v[88:91]\n"
    "s_waitcnt lgkmcnt(8)\n"
    "ds_read_b64_tr_b16 a[176:177], v246 offset:20480\n"
    "v_cvt_pk_bf16_f32 v176, v176, v177\n"
    "v_mfma_f32_16x16x32_bf16 v[100:103], a[184:187], v[232:235], v[100:103]\n"
    "ds_read_b64_tr_b16 a[178:179], v246 offset:22528\n"
    "v_cvt_pk_bf16_f32 v177, v178, v179\n"
    "v_mfma_f32_16x16x32_bf16 v[100:103], a[188:191], v[236:239], v[100:103]\n"
    "ds_read_b64_tr_b16 a[180:181], v246 offset:28672\n"
    "v_mfma_f32_16x16x32_bf16 v[96:99], a[188:191], v[228:231], v[96:99]\n"
    "ds_read_b64_tr_b16 a[182:183], v246 offset:30720\n"
    "v_mfma_f32_16x16x32_bf16 v[96:99], a[184:187], v[224:227], v[96:99]\n"
    "v_exp_f32_e32 v196, v196\n"
    "v_mfma_f32_16x16x32_bf16 v[92:95], a[184:187], v[216:219], v[92:95]\n"
    "ds_read_b64_tr_b16 a[184:185], v247 offset:20480\n"
    "ds_write_addtid_b32 v192 offset:16384\n"
    "v_mfma_f32_16x16x32_bf16 v[92:95], a[188:191], v[220:223], v[92:95]\n"
    "s_waitcnt lgkmcnt(10)\n"
    "ds_read_b64_tr_b16 a[186:187], v247 offset:22528\n"
    "v_mfma_f32_16x16x32_bf16 v[104:107], a[160:163], v[216:219], v[104:107]\n"
    "ds_read_b64_tr_b16 a[188:189], v247 offset:28672\n"
    "ds_write_addtid_b32 v193 offset:16640\n"
    "v_mfma_f32_16x16x32_bf16 v[104:107], a[164:167], v[220:223], v[104:107]\n"
    "ds_read_b64_tr_b16 a[190:191], v247 offset:30720\n"
    "v_mfma_f32_16x16x32_bf16 v[108:111], a[164:167], v[228:231], v[108:111]\n"
    "v_exp_f32_e32 v197, v197\n"
    "v_mfma_f32_16x16x32_bf16 v[108:111], a[160:163], v[224:227], v[108:111]\n"
    "v_exp_f32_e32 v198, v198\n"
    "v_mfma_f32_16x16x32_bf16 v[112:115], a[160:163], v[232:235], v[112:115]\n"
    "ds_read_b128 a[160:163], v243 offset:53248\n"
    "ds_write_addtid_b32 v184 offset:12288\n"
    "v_mfma_f32_16x16x32_bf16 v[112:115], a[164:167], v[236:239], v[112:115]\n"
    "s_waitcnt lgkmcnt(12)\n"
    "ds_read_b128 a[164:167], v240 offset:49152\n"
    "ds_write_addtid_b32 v185 offset:12544\n"
    "v_mfma_f32_16x16x32_bf16 v[124:127], a[168:171], v[232:235], v[124:127]\n"
    "v_exp_f32_e32 v199, v199\n"
    "v_mfma_f32_16x16x32_bf16 v[124:127], a[172:175], v[236:239], v[124:127]\n"
    "ds_write_addtid_b32 v176 offset:8192\n"
    "v_cvt_pk_bf16_f32 v194, v196, v197\n"
    "v_mfma_f32_16x16x32_bf16 v[120:123], a[172:175], v[228:231], v[120:123]\n"
    "v_exp_f32_e32 v188, v188\n"
    "v_mfma_f32_16x16x32_bf16 v[120:123], a[168:171], v[224:227], v[120:123]\n"
    "v_exp_f32_e32 v189, v189\n"
    "v_mfma_f32_16x16x32_bf16 v[116:119], a[168:171], v[216:219], v[116:119]\n"
    "ds_read_b128 a[168:171], v241 offset:49152\n"
    "v_cvt_pk_bf16_f32 v195, v198, v199\n"
    "v_mfma_f32_16x16x32_bf16 v[116:119], a[172:175], v[220:223], v[116:119]\n"
    "s_waitcnt lgkmcnt(12)\n"
    "ds_read_b128 a[172:175], v240 offset:53248\n"
    "ds_write_addtid_b32 v177 offset:8448\n"
    "v_mfma_f32_16x16x32_bf16 v[128:131], a[176:179], v[216:219], v[128:131]\n"
    "v_exp_f32_e32 v190, v190\n"
    "v_mfma_f32_16x16x32_bf16 v[128:131], a[180:183], v[220:223], v[128:131]\n"
    "v_exp_f32_e32 v191, v191\n"
    "v_mfma_f32_16x16x32_bf16 v[132:135], a[180:183], v[228:231], v[132:135]\n"
    "ds_write_addtid_b32 v194 offset:16896\n"
    "v_cvt_pk_bf16_f32 v186, v188, v189\n"
    "v_mfma_f32_16x16x32_bf16 v[132:135], a[176:179], v[224:227], v[132:135]\n"
    "v_exp_f32_e32 v180, v180\n"
    "v_mfma_f32_16x16x32_bf16 v[136:139], a[176:179], v[232:235], v[136:139]\n"
    "ds_read_b128 a[176:179], v241 offset:53248\n"
    "v_mfma_f32_16x16x32_bf16 v[136:139], a[180:183], v[236:239], v[136:139]\n"
    "s_waitcnt lgkmcnt(10)\n"
    "ds_read_b128 a[180:183], v242 offset:49152\n"
    "v_mfma_f32_16x16x32_bf16 v[148:151], a[184:187], v[232:235], v[148:151]\n"
    "v_exp_f32_e32 v181, v181\n"
    "v_mfma_f32_16x16x32_bf16 v[148:151], a[188:191], v[236:239], v[148:151]\n"
    "ds_write_addtid_b32 v195 offset:17152\n"
    "v_cvt_pk_bf16_f32 v187, v190, v191\n"
    "v_mfma_f32_16x16x32_bf16 v[144:147], a[188:191], v[228:231], v[144:147]\n"
    "v_exp_f32_e32 v182, v182\n"
    "v_mfma_f32_16x16x32_bf16 v[144:147], a[184:187], v[224:227], v[144:147]\n"
    "v_exp_f32_e32 v183, v183\n"
    "v_mfma_f32_16x16x32_bf16 v[140:143], a[184:187], v[216:219], v[140:143]\n"
    "ds_read_b128 a[184:187], v243 offset:49152\n"
    "v_cvt_pk_bf16_f32 v178, v180, v181\n"
    "v_mfma_f32_16x16x32_bf16 v[140:143], a[188:191], v[220:223], v[140:143]\n"
    "s_waitcnt lgkmcnt(10)\n"
    "ds_read_b128 a[188:191], v242 offset:53248\n"
    "v_cvt_pk_bf16_f32 v179, v182, v183\n"
    "v_mfma_f32_16x16x32_bf16 v[216:219], a[160:163], a[92:95], 0\n"
    "ds_write_addtid_b32 v186 offset:12800\n"
    "v_mfma_f32_16x16x32_bf16 v[216:219], a[164:167], a[64:67], v[216:219]\n"
    "ds_write_addtid_b32 v187 offset:13056\n"
    "v_mfma_f32_16x16x32_bf16 v[224:227], a[164:167], a[96:99], 0\n"
    "ds_write_addtid_b32 v178 offset:8704\n"
    "v_mfma_f32_16x16x32_bf16 v[224:227], a[160:163], a[124:127], v[224:227]\n"
    "ds_write_addtid_b32 v179 offset:8960\n"
    "v_mfma_f32_16x16x32_bf16 v[232:235], a[160:163], a[156:159], 0\n"
    "ds_read_b128 a[160:163], v243 offset:55296\n"
    "v_mfma_f32_16x16x32_bf16 v[232:235], a[164:167], a[128:131], v[232:235]\n"
    "s_waitcnt lgkmcnt(12)\n"
    "ds_read_b128 a[164:167], v240 offset:51200\n"
    "v_mfma_f32_16x16x32_bf16 v[232:235], a[168:171], a[132:135], v[232:235]\n"
    "v_mfma_f32_16x16x32_bf16 v[232:235], a[172:175], a[136:139], v[232:235]\n"
    "v_mfma_f32_16x16x32_bf16 v[224:227], a[172:175], a[104:107], v[224:227]\n"
    "s_waitcnt lgkmcnt(2)\n"
    "s_barrier\n"
    "v_mfma_f32_16x16x32_bf16 v[224:227], a[168:171], a[100:103], v[224:227]\n"
    "s_add_u32 m0, s43, 16384\n"
    "v_mfma_f32_16x16x32_bf16 v[216:219], a[168:171], a[68:71], v[216:219]\n"
    "ds_read_b128 a[168:171], v241 offset:51200\n"
    "buffer_load_dwordx4 v248, s[8:11], s40 offen offset:0 sc1 lds\n"
    "v_mfma_f32_16x16x32_bf16 v[216:219], a[172:175], a[72:75], v[216:219]\n"
    "ds_read_b128 a[172:175], v240 offset:55296\n"
    "v_mfma_f32_16x16x32_bf16 v[216:219], a[176:179], a[76:79], v[216:219]\n"
    "ds_read2st64_b32 v[180:181], v250 offset0:32 offset1:33\n"
    "v_mfma_f32_16x16x32_bf16 v[216:219], a[180:183], a[80:83], v[216:219]\n"
    "v_mfma_f32_16x16x32_bf16 v[224:227], a[180:183], a[112:115], v[224:227]\n"
    "buffer_load_dwordx4 v248, s[8:11], s40 offen offset:1024 sc1 lds\n"
    "v_mfma_f32_16x16x32_bf16 v[224:227], a[176:179], a[108:111], v[224:227]\n"
    "v_mfma_f32_16x16x32_bf16 v[232:235], a[176:179], a[140:143], v[232:235]\n"
    "ds_read_b128 a[176:179], v241 offset:55296\n"
    "v_mfma_f32_16x16x32_bf16 v[232:235], a[180:183], a[144:147], v[232:235]\n"
    "ds_read_b128 a[180:183], v242 offset:51200\n"
    "v_mfma_f32_16x16x32_bf16 v[232:235], a[184:187], a[148:151], v[232:235]\n"
    "buffer_load_dwordx4 v248, s[8:11], s40 offen offset:2048 sc1 lds\n"
    "v_mfma_f32_16x16x32_bf16 v[232:235], a[188:191], a[152:155], v[232:235]\n"
    "ds_read2st64_b32 v[182:183], v250 offset0:34 offset1:35\n"
    "v_mfma_f32_16x16x32_bf16 v[224:227], a[188:191], a[120:123], v[224:227]\n"
    "v_mfma_f32_16x16x32_bf16 v[224:227], a[184:187], a[116:119], v[224:227]\n"
    "v_exp_f32_e32 v232, v232\n"
    "v_mfma_f32_16x16x32_bf16 v[216:219], a[184:187], a[84:87], v[216:219]\n"
    "ds_read_b128 a[184:187], v243 offset:51200\n"
    "buffer_load_dwordx4 v248, s[8:11], s40 offen offset:3072 sc1 lds\n"
    "v_mfma_f32_16x16x32_bf16 v[216:219], a[188:191], a[88:91], v[216:219]\n"
    "s_waitcnt lgkmcnt(7)\n"
    "s_add_u32 s40, s40, -131072\n"
    "ds_read_b128 a[188:191], v242 offset:55296\n"
    "v_mfma_f32_16x16x32_bf16 v[220:223], a[160:163], a[92:95], 0\n"
    "s_add_u32 m0, s43, 20480\n"
    "ds_read2st64_b32 v[188:189], v250 offset0:48 offset1:49\n"
    "v_mfma_f32_16x16x32_bf16 v[220:223], a[164:167], a[64:67], v[220:223]\n"
    "v_exp_f32_e32 v233, v233\n"
    "v_mfma_f32_16x16x32_bf16 v[228:231], a[164:167], a[96:99], 0\n"
    "buffer_load_dwordx4 v248, s[8:11], s41 offen offset:0 sc1 lds\n"
    "v_exp_f32_e32 v234, v234\n"
    "v_mfma_f32_16x16x32_bf16 v[228:231], a[160:163], a[124:127], v[228:231]\n"
    "ds_read2st64_b32 v[190:191], v250 offset0:50 offset1:51\n"
    "v_mfma_f32_16x16x32_bf16 v[236:239], a[160:163], a[156:159], 0\n"
    "ds_read_b64_tr_b16 a[160:161], v244 offset:32768\n"
    "v_mfma_f32_16x16x32_bf16 v[236:239], a[164:167], a[128:131], v[236:239]\n"
    "s_waitcnt lgkmcnt(9)\n"
    "ds_read_b64_tr_b16 a[162:163], v244 offset:34816\n"
    "v_mfma_f32_16x16x32_bf16 v[236:239], a[168:171], a[132:135], v[236:239]\n"
    "ds_read_b64_tr_b16 a[164:165], v244 offset:40960\n"
    "buffer_load_dwordx4 v248, s[8:11], s41 offen offset:1024 sc1 lds\n"
    "v_mfma_f32_16x16x32_bf16 v[236:239], a[172:175], a[136:139], v[236:239]\n"
    "ds_read_b64_tr_b16 a[166:167], v244 offset:43008\n"
    "v_mfma_f32_16x16x32_bf16 v[228:231], a[172:175], a[104:107], v[228:231]\n"
    "v_exp_f32_e32 v235, v235\n"
    "v_mfma_f32_16x16x32_bf16 v[228:231], a[168:171], a[100:103], v[228:231]\n"
    "ds_read2st64_b32 v[196:197], v250 offset0:64 offset1:65\n"
    "v_cvt_pk_bf16_f32 v232, v232, v233\n"
    "v_mfma_f32_16x16x32_bf16 v[220:223], a[168:171], a[68:71], v[220:223]\n"
    "ds_read_b64_tr_b16 a[168:169], v245 offset:32768\n"
    "buffer_load_dwordx4 v248, s[8:11], s41 offen offset:2048 sc1 lds\n"
    "v_mfma_f32_16x16x32_bf16 v[220:223], a[172:175], a[72:75], v[220:223]\n"
    "s_waitcnt lgkmcnt(11)\n"
    "ds_read_b64_tr_b16 a[170:171], v245 offset:34816\n"
    "v_mfma_f32_16x16x32_bf16 v[220:223], a[176:179], a[76:79], v[220:223]\n"
    "ds_read_b64_tr_b16 a[172:173], v245 offset:40960\n"
    "v_mfma_f32_16x16x32_bf16 v[220:223], a[180:183], a[80:83], v[220:223]\n"
    "ds_read_b64_tr_b16 a[174:175], v245 offset:43008\n"
    "v_mfma_f32_16x16x32_bf16 v[228:231], a[180:183], a[112:115], v[228:231]\n"
    "buffer_load_dwordx4 v248, s[8:11], s41 offen offset:3072 sc1 lds\n"
    "v_mfma_f32_16x16x32_bf16 v[228:231], a[176:179], a[108:111], v[228:231]\n"
    "s_add_u32 s41, s41, -131072\n"
    "ds_read2st64_b32 v[198:199], v250 offset0:66 offset1:67\n"
    "v_mfma_f32_16x16x32_bf16 v[236:239], a[176:179], a[140:143], v[236:239]\n"
    "s_mov_b32 m0, s49\n"
    "ds_read_b64_tr_b16 a[176:177], v246 offset:32768\n"
    "v_mfma_f32_16x16x32_bf16 v[236:239], a[180:183], a[144:147], v[236:239]\n"
    "s_waitcnt lgkmcnt(13)\n"
    "ds_read_b64_tr_b16 a[178:179], v246 offset:34816\n"
    "v_mfma_f32_16x16x32_bf16 v[236:239], a[184:187], a[148:151], v[236:239]\n"
    "ds_read_b64_tr_b16 a[180:181], v246 offset:40960\n"
    "buffer_load_dword v255, v254, s[8:11], s45 offen offset:0 sc1\n"
    "v_mfma_f32_16x16x32_bf16 v[236:239], a[188:191], a[152:155], v[236:239]\n"
    "ds_read_b64_tr_b16 a[182:183], v246 offset:43008\n"
    "v_mfma_f32_16x16x32_bf16 v[228:231], a[188:191], a[120:123], v[228:231]\n"
    "v_exp_f32_e32 v224, v224\n"
    "v_mfma_f32_16x16x32_bf16 v[228:231], a[184:187], a[116:119], v[228:231]\n"
    "v_exp_f32_e32 v225, v225\n"
    "v_mfma_f32_16x16x32_bf16 v[220:223], a[184:187], a[84:87], v[220:223]\n"
    "ds_read_b64_tr_b16 a[184:185], v247 offset:32768\n"
    "v_cvt_pk_bf16_f32 v233, v234, v235\n"
    "v_mfma_f32_16x16x32_bf16 v[220:223], a[188:191], a[88:91], v[220:223]\n"
    "s_waitcnt lgkmcnt(5)\n"
    "ds_read_b64_tr_b16 a[186:187], v247 offset:34816\n"
    "v_mfma_f32_16x16x32_bf16 v[56:59], a[160:163], v[176:179], v[56:59]\n"
    "ds_read_b64_tr_b16 a[188:189], v247 offset:40960\n"
    "v_mfma_f32_16x16x32_bf16 v[56:59], a[164:167], v[180:183], v[56:59]\n"
    "ds_read_b64_tr_b16 a[190:191], v247 offset:43008\n"
    "v_mfma_f32_16x16x32_bf16 v[60:63], a[164:167], v[188:191], v[60:63]\n"
    "v_exp_f32_e32 v226, v226\n"
    "v_mfma_f32_16x16x32_bf16 v[60:63], a[160:163], v[184:187], v[60:63]\n"
    "v_exp_f32_e32 v227, v227\n"
    "v_mfma_f32_16x16x32_bf16 v[64:67], a[160:163], v[192:195], v[64:67]\n"
    "ds_read_b64_tr_b16 a[160:161], v244 offset:36864\n"
    "v_cvt_pk_bf16_f32 v224, v224, v225\n"
    "v_mfma_f32_16x16x32_bf16 v[64:67], a[164:167], v[196:199], v[64:67]\n"
    "ds_read_b64_tr_b16 a[162:163], v244 offset:38912\n"
    "v_mfma_f32_16x16x32_bf16 v[76:79], a[168:171], v[192:195], v[76:79]\n"
    "ds_read_b64_tr_b16 a[164:165], v244 offset:45056\n"
    "v_mfma_f32_16x16x32_bf16 v[76:79], a[172:175], v[196:199], v[76:79]\n"
    "ds_read_b64_tr_b16 a[166:167], v244 offset:47104\n"
    "v_mfma_f32_16x16x32_bf16 v[72:75], a[172:175], v[188:191], v[72:75]\n"
    "v_exp_f32_e32 v216, v216\n"
    "v_mfma_f32_16x16x32_bf16 v[72:75], a[168:171], v[184:187], v[72:75]\n"
    "v_exp_f32_e32 v217, v217\n"
    "v_mfma_f32_16x16x32_bf16 v[68:71], a[168:171], v[176:179], v[68:71]\n"
    "ds_read_b64_tr_b16 a[168:169], v245 offset:36864\n"
    "v_cvt_pk_bf16_f32 v225, v226, v227\n"
    "v_mfma_f32_16x16x32_bf16 v[68:71], a[172:175], v[180:183], v[68:71]\n"
    "s_waitcnt lgkmcnt(9)\n"
    "ds_read_b64_tr_b16 a[170:171], v245 offset:38912\n"
    "v_mfma_f32_16x16x32_bf16 v[80:83], a[176:179], v[176:179], v[80:83]\n"
    "ds_read_b64_tr_b16 a[172:173], v245 offset:45056\n"
    "v_mfma_f32_16x16x32_bf16 v[80:83], a[180:183], v[180:183], v[80:83]\n"
    "ds_read_b64_tr_b16 a[174:175], v245 offset:47104\n"
    "v_mfma_f32_16x16x32_bf16 v[84:87], a[180:183], v[188:191], v[84:87]\n"
    "v_exp_f32_e32 v218, v218\n"
    "v_mfma_f32_16x16x32_bf16 v[84:87], a[176:179], v[184:187], v[84:87]\n"
    "v_exp_f32_e32 v219, v219\n"
    "v_mfma_f32_16x16x32_bf16 v[88:91], a[176:179], v[192:195], v[88:91]\n"
    "s_waitcnt vmcnt(9)\n"
    "s_barrier\n"
    "v_mfma_f32_16x16x32_bf16 v[88:91], a[180:183], v[196:199], v[88:91]\n"
    "s_waitcnt lgkmcnt(8)\n"
    "ds_read_b64_tr_b16 a[176:177], v246 offset:36864\n"
    "v_cvt_pk_bf16_f32 v216, v216, v217\n"
    "v_mfma_f32_16x16x32_bf16 v[100:103], a[184:187], v[192:195], v[100:103]\n"
    "ds_read_b64_tr_b16 a[178:179], v246 offset:38912\n"
    "v_cvt_pk_bf16_f32 v217, v218, v219\n"
    "v_mfma_f32_16x16x32_bf16 v[100:103], a[188:191], v[196:199], v[100:103]\n"
    "ds_read_b64_tr_b16 a[180:181], v246 offset:45056\n"
    "v_mfma_f32_16x16x32_bf16 v[96:99], a[188:191], v[188:191], v[96:99]\n"
    "ds_read_b64_tr_b16 a[182:183], v246 offset:47104\n"
    "v_mfma_f32_16x16x32_bf16 v[96:99], a[184:187], v[184:187], v[96:99]\n"
    "v_exp_f32_e32 v236, v236\n"
    "v_mfma_f32_16x16x32_bf16 v[92:95], a[184:187], v[176:179], v[92:95]\n"
    "ds_read_b64_tr_b16 a[184:185], v247 offset:36864\n"
    "ds_write_addtid_b32 v232 offset:16384\n"
    "v_mfma_f32_16x16x32_bf16 v[92:95], a[188:191], v[180:183], v[92:95]\n"
    "s_waitcnt lgkmcnt(10)\n"
    "ds_read_b64_tr_b16 a[186:187], v247 offset:38912\n"
    "v_mfma_f32_16x16x32_bf16 v[104:107], a[160:163], v[176:179], v[104:107]\n"
    "ds_read_b64_tr_b16 a[188:189], v247 offset:45056\n"
    "ds_write_addtid_b32 v233 offset:16640\n"
    "v_mfma_f32_16x16x32_bf16 v[104:107], a[164:167], v[180:183], v[104:107]\n"
    "ds_read_b64_tr_b16 a[190:191], v247 offset:47104\n"
    "v_mfma_f32_16x16x32_bf16 v[108:111], a[164:167], v[188:191], v[108:111]\n"
    "v_exp_f32_e32 v237, v237\n"
    "v_mfma_f32_16x16x32_bf16 v[108:111], a[160:163], v[184:187], v[108:111]\n"
    "v_exp_f32_e32 v238, v238\n"
    "v_mfma_f32_16x16x32_bf16 v[112:115], a[160:163], v[192:195], v[112:115]\n"
    "ds_read_b128 a[160:163], v243 offset:4096\n"
    "ds_write_addtid_b32 v224 offset:12288\n"
    "v_mfma_f32_16x16x32_bf16 v[112:115], a[164:167], v[196:199], v[112:115]\n"
    "s_waitcnt lgkmcnt(12)\n"
    "ds_read_b128 a[164:167], v240 offset:0\n"
    "ds_write_addtid_b32 v225 offset:12544\n"
    "v_mfma_f32_16x16x32_bf16 v[124:127], a[168:171], v[192:195], v[124:127]\n"
    "v_exp_f32_e32 v239, v239\n"
    "v_mfma_f32_16x16x32_bf16 v[124:127], a[172:175], v[196:199], v[124:127]\n"
    "ds_write_addtid_b32 v216 offset:8192\n"
    "v_cvt_pk_bf16_f32 v234, v236, v237\n"
    "v_mfma_f32_16x16x32_bf16 v[120:123], a[172:175], v[188:191], v[120:123]\n"
    "v_exp_f32_e32 v228, v228\n"
    "v_mfma_f32_16x16x32_bf16 v[120:123], a[168:171], v[184:187], v[120:123]\n"
    "v_exp_f32_e32 v229, v229\n"
    "v_mfma_f32_16x16x32_bf16 v[116:119], a[168:171], v[176:179], v[116:119]\n"
    "ds_read_b128 a[168:171], v241 offset:0\n"
    "v_cvt_pk_bf16_f32 v235, v238, v239\n"
    "v_mfma_f32_16x16x32_bf16 v[116:119], a[172:175], v[180:183], v[116:119]\n"
    "s_waitcnt lgkmcnt(12)\n"
    "ds_read_b128 a[172:175], v240 offset:4096\n"
    "ds_write_addtid_b32 v217 offset:8448\n"
    "v_mfma_f32_16x16x32_bf16 v[128:131], a[176:179], v[176:179], v[128:131]\n"
    "v_exp_f32_e32 v230, v230\n"
    "v_mfma_f32_16x16x32_bf16 v[128:131], a[180:183], v[180:183], v[128:131]\n"
    "v_exp_f32_e32 v231, v231\n"
    "v_mfma_f32_16x16x32_bf16 v[132:135], a[180:183], v[188:191], v[132:135]\n"
    "ds_write_addtid_b32 v234 offset:16896\n"
    "v_cvt_pk_bf16_f32 v226, v228, v229\n"
    "v_mfma_f32_16x16x32_bf16 v[132:135], a[176:179], v[184:187], v[132:135]\n"
    "v_exp_f32_e32 v220, v220\n"
    "v_mfma_f32_16x16x32_bf16 v[136:139], a[176:179], v[192:195], v[136:139]\n"
    "ds_read_b128 a[176:179], v241 offset:4096\n"
    "v_mfma_f32_16x16x32_bf16 v[136:139], a[180:183], v[196:199], v[136:139]\n"
    "s_waitcnt lgkmcnt(10)\n"
    "ds_read_b128 a[180:183], v242 offset:0\n"
    "ds_write_addtid_b32 v235 offset:17152\n"
    "v_mfma_f32_16x16x32_bf16 v[148:151], a[184:187], v[192:195], v[148:151]\n"
    "v_exp_f32_e32 v221, v221\n"
    "v_mfma_f32_16x16x32_bf16 v[148:151], a[188:191], v[196:199], v[148:151]\n"
    "ds_write_addtid_b32 v226 offset:12800\n"
    "v_cvt_pk_bf16_f32 v227, v230, v231\n"
    "v_mfma_f32_16x16x32_bf16 v[144:147], a[188:191], v[188:191], v[144:147]\n"
    "v_exp_f32_e32 v222, v222\n"
    "v_mfma_f32_16x16x32_bf16 v[144:147], a[184:187], v[184:187], v[144:147]\n"
    "v_exp_f32_e32 v223, v223\n"
    "v_mfma_f32_16x16x32_bf16 v[140:143], a[184:187], v[176:179], v[140:143]\n"
    "ds_read_b128 a[184:187], v243 offset:0\n"
    "v_cvt_pk_bf16_f32 v218, v220, v221\n"
    "v_mfma_f32_16x16x32_bf16 v[140:143], a[188:191], v[180:183], v[140:143]\n"
    "s_waitcnt lgkmcnt(11)\n"
    "ds_read_b128 a[188:191], v242 offset:4096\n"
    "v_cvt_pk_bf16_f32 v219, v222, v223\n"
    "v_mfma_f32_16x16x32_bf16 v[176:179], a[160:163], a[92:95], 0\n"
    "s_waitcnt vmcnt(0)\n"
    "ds_write_addtid_b32 v227 offset:13056\n"
    "v_cmp_ne_u32_e32 vcc, s39, v255\n"
    "v_mfma_f32_16x16x32_bf16 v[176:179], a[164:167], a[64:67], v[176:179]\n"
    "ds_write_addtid_b32 v218 offset:8704\n"
    "v_mfma_f32_16x16x32_bf16 v[184:187], a[164:167], a[96:99], 0\n"
    "s_cbranch_vccnz .Lat_wpoll\n"
    ".Lat_wpoll_ret:\n"
    "v_mfma_f32_16x16x32_bf16 v[184:187], a[160:163], a[124:127], v[184:187]\n"
    "ds_write_addtid_b32 v219 offset:8960\n"
    "v_mfma_f32_16x16x32_bf16 v[192:195], a[160:163], a[156:159], 0\n"
    "ds_read_b128 a[160:163], v243 offset:6144\n"
    "v_mfma_f32_16x16x32_bf16 v[192:195], a[164:167], a[128:131], v[192:195]\n"
    "s_waitcnt lgkmcnt(12)\n"
    "ds_read_b128 a[164:167], v240 offset:2048\n"
    "v_mfma_f32_16x16x32_bf16 v[192:195], a[168:171], a[132:135], v[192:195]\n"
    "v_mfma_f32_16x16x32_bf16 v[192:195], a[172:175], a[136:139], v[192:195]\n"
    "v_mfma_f32_16x16x32_bf16 v[184:187], a[172:175], a[104:107], v[184:187]\n"
    "s_waitcnt lgkmcnt(2)\n"
    "s_barrier\n"
    "v_mfma_f32_16x16x32_bf16 v[184:187], a[168:171], a[100:103], v[184:187]\n"
    "s_add_u32 s61, s47, 5791744\n"
    "v_mfma_f32_16x16x32_bf16 v[176:179], a[168:171], a[68:71], v[176:179]\n"
    "s_add_u32 m0, s49, 20480\n"
    "ds_read_b128 a[168:171], v241 offset:2048\n"
    "v_mfma_f32_16x16x32_bf16 v[176:179], a[172:175], a[72:75], v[176:179]\n"
    "ds_read_b128 a[172:175], v240 offset:6144\n"
    "v_mfma_f32_16x16x32_bf16 v[176:179], a[176:179], a[76:79], v[176:179]\n"
    "buffer_load_dwordx4 v252, s[8:11], s61 offen offset:0 sc1 lds\n"
    "v_mfma_f32_16x16x32_bf16 v[176:179], a[180:183], a[80:83], v[176:179]\n"
    "s_add_u32 s61, s47, 5795840\n"
    "ds_read2st64_b32 v[220:221], v250 offset0:32 offset1:33\n"
    "v_mfma_f32_16x16x32_bf16 v[184:187], a[180:183], a[112:115], v[184:187]\n"
    "s_add_u32 m0, s49, 24576\n"
    "v_mfma_f32_16x16x32_bf16 v[184:187], a[176:179], a[108:111], v[184:187]\n"
    "v_mfma_f32_16x16x32_bf16 v[192:195], a[176:179], a[140:143], v[192:195]\n"
    "ds_read_b128 a[176:179], v241 offset:6144\n"
    "buffer_load_dwordx4 v252, s[8:11], s61 offen offset:0 sc1 lds\n"
    "v_mfma_f32_16x16x32_bf16 v[192:195], a[180:183], a[144:147], v[192:195]\n"
    "s_add_u32 s61, s47, 5799936\n"
    "ds_read_b128 a[180:183], v242 offset:2048\n"
    "v_mfma_f32_16x16x32_bf16 v[192:195], a[184:187], a[148:151], v[192:195]\n"
    "s_add_u32 m0, s49, 28672\n"
    "ds_read2st64_b32 v[222:223], v250 offset0:34 offset1:35\n"
    "v_mfma_f32_16x16x32_bf16 v[192:195], a[188:191], a[152:155], v[192:195]\n"
    "v_mfma_f32_16x16x32_bf16 v[184:187], a[188:191], a[120:123], v[184:187]\n"
    "buffer_load_dwordx4 v252, s[8:11], s61 offen offset:0 sc1 lds\n"
    "v_mfma_f32_16x16x32_bf16 v[184:187], a[184:187], a[116:119], v[184:187]\n"
    "s_mov_b32 m0, s49\n"
    "ds_read2st64_b32 v[228:229], v250 offset0:48 offset1:49\n"
    "v_mfma_f32_16x16x32_bf16 v[176:179], a[184:187], a[84:87], v[176:179]\n"
    "s_add_u32 s48, s57, 30720\n"
    "ds_read_b128 a[184:187], v243 offset:2048\n"
    "v_exp_f32_e32 v192, v192\n"
    "v_mfma_f32_16x16x32_bf16 v[176:179], a[188:191], a[88:91], v[176:179]\n"
    "s_waitcnt lgkmcnt(8)\n"
    "ds_read_b128 a[188:191], v242 offset:6144\n"
    "v_mfma_f32_16x16x32_bf16 v[180:183], a[160:163], a[92:95], 0\n"
    "buffer_load_dwordx4 a[192:195], v253, s[8:11], s48 offen offset:0 sc1\n"
    "v_mfma_f32_16x16x32_bf16 v[180:183], a[164:167], a[64:67], v[180:183]\n"
    "v_exp_f32_e32 v193, v193\n"
    "v_mfma_f32_16x16x32_bf16 v[188:191], a[164:167], a[96:99], 0\n"
    "v_exp_f32_e32 v194, v194\n"
    "v_mfma_f32_16x16x32_bf16 v[188:191], a[160:163], a[124:127], v[188:191]\n"
    "ds_read2st64_b32 v[230:231], v250 offset0:50 offset1:51\n"
    "v_exp_f32_e32 v195, v195\n"
    "v_mfma_f32_16x16x32_bf16 v[196:199], a[160:163], a[156:159], 0\n"
    "ds_read_b64_tr_b16 a[160:161], v244 offset:49152\n"
    "buffer_load_dwordx4 a[196:199], v253, s[8:11], s48 offen offset:1024 sc1\n"
    "v_mfma_f32_16x16x32_bf16 v[196:199], a[164:167], a[128:131], v[196:199]\n"
    "s_waitcnt lgkmcnt(9)\n"
    "s_add_u32 s48, s57, 14336\n"
    "ds_read_b64_tr_b16 a[162:163], v244 offset:51200\n"
    "v_mfma_f32_16x16x32_bf16 v[196:199], a[168:171], a[132:135], v[196:199]\n"
    "ds_read_b64_tr_b16 a[164:165], v244 offset:57344\n"
    "v_cvt_pk_bf16_f32 v192, v192, v193\n"
    "v_mfma_f32_16x16x32_bf16 v[196:199], a[172:175], a[136:139], v[196:199]\n"
    "ds_read_b64_tr_b16 a[166:167], v244 offset:59392\n"
    "v_mfma_f32_16x16x32_bf16 v[188:191], a[172:175], a[104:107], v[188:191]\n"
    "buffer_load_dwordx4 a[200:203], v253, s[8:11], s48 offen offset:0 sc1\n"
    "v_mfma_f32_16x16x32_bf16 v[188:191], a[168:171], a[100:103], v[188:191]\n"
    "ds_read2st64_b32 v[236:237], v250 offset0:64 offset1:65\n"
    "v_mfma_f32_16x16x32_bf16 v[180:183], a[168:171], a[68:71], v[180:183]\n"
    "ds_read_b64_tr_b16 a[168:169], v245 offset:49152\n"
    "v_mfma_f32_16x16x32_bf16 v[180:183], a[172:175], a[72:75], v[180:183]\n"
    "s_waitcnt lgkmcnt(11)\n"
    "ds_read_b64_tr_b16 a[170:171], v245 offset:51200\n"
    "v_mfma_f32_16x16x32_bf16 v[180:183], a[176:179], a[76:79], v[180:183]\n"
    "ds_read_b64_tr_b16 a[172:173], v245 offset:57344\n"
    "buffer_load_dwordx4 a[204:207], v253, s[8:11], s48 offen offset:1024 sc1\n"
    "v_mfma_f32_16x16x32_bf16 v[180:183], a[180:183], a[80:83], v[180:183]\n"
    "s_add_u32 s48, s57, 16384\n"
    "ds_read_b64_tr_b16 a[174:175], v245 offset:59392\n"
    "v_mfma_f32_16x16x32_bf16 v[188:191], a[180:183], a[112:115], v[188:191]\n"
    "ds_read2st64_b32 v[238:239], v250 offset0:66 offset1:67\n"
    "v_mfma_f32_16x16x32_bf16 v[188:191], a[176:179], a[108:111], v[188:191]\n"
    "v_exp_f32_e32 v184, v184\n"
    "v_mfma_f32_16x16x32_bf16 v[196:199], a[176:179], a[140:143], v[196:199]\n"
    "ds_read_b64_tr_b16 a[176:177], v246 offset:49152\n"
    "buffer_load_dwordx4 a[208:211], v253, s[8:11], s48 offen offset:0 sc1\n"
    "v_mfma_f32_16x16x32_bf16 v[196:199], a[180:183], a[144:147], v[196:199]\n"
    "s_waitcnt lgkmcnt(12)\n"
    "ds_read_b64_tr_b16 a[178:179], v246 offset:51200\n"
    "v_mfma_f32_16x16x32_bf16 v[196:199], a[184:187], a[148:151], v[196:199]\n"
    "ds_read_b64_tr_b16 a[180:181], v246 offset:57344\n"
    "v_mfma_f32_16x16x32_bf16 v[196:199], a[188:191], a[152:155], v[196:199]\n"
    "ds_read_b64_tr_b16 a[182:183], v246 offset:59392\n"
    "v_mfma_f32_16x16x32_bf16 v[188:191], a[188:191], a[120:123], v[188:191]\n"
    "buffer_load_dwordx4 a[212:215], v253, s[8:11], s48 offen offset:1024 sc1\n"
    "v_mfma_f32_16x16x32_bf16 v[188:191], a[184:187], a[116:119], v[188:191]\n"
    "s_add_u32 s48, s57, 0\n"
    "v_exp_f32_e32 v185, v185\n"
    "v_mfma_f32_16x16x32_bf16 v[180:183], a[184:187], a[84:87], v[180:183]\n"
    "ds_read_b64_tr_b16 a[184:185], v247 offset:49152\n"
    "v_cvt_pk_bf16_f32 v193, v194, v195\n"
    "v_mfma_f32_16x16x32_bf16 v[180:183], a[188:191], a[88:91], v[180:183]\n"
    "s_waitcnt lgkmcnt(5)\n"
    "ds_read_b64_tr_b16 a[186:187], v247 offset:51200\n"
    "v_mfma_f32_16x16x32_bf16 v[56:59], a[160:163], v[216:219], v[56:59]\n"
    "ds_read_b64_tr_b16 a[188:189], v247 offset:57344\n"
    "buffer_load_dwordx4 a[216:219], v253, s[8:11], s48 offen offset:0 sc1\n"
    "v_mfma_f32_16x16x32_bf16 v[56:59], a[164:167], v[220:223], v[56:59]\n"
    "ds_read_b64_tr_b16 a[190:191], v247 offset:59392\n"
    "v_mfma_f32_16x16x32_bf16 v[60:63], a[164:167], v[228:231], v[60:63]\n"
    "v_exp_f32_e32 v186, v186\n"
    "v_mfma_f32_16x16x32_bf16 v[60:63], a[160:163], v[224:227], v[60:63]\n"
    "v_exp_f32_e32 v187, v187\n"
    "v_mfma_f32_16x16x32_bf16 v[64:67], a[160:163], v[232:235], v[64:67]\n"
    "ds_read_b64_tr_b16 a[160:161], v244 offset:53248\n"
    "buffer_load_dwordx4 a[220:223], v253, s[8:11], s48 offen offset:1024 sc1\n"
    "v_mfma_f32_16x16x32_bf16 v[64:67], a[164:167], v[236:239], v[64:67]\n"
    "ds_read_b64_tr_b16 a[162:163], v244 offset:55296\n"
    "v_cvt_pk_bf16_f32 v184, v184, v185\n"
    "v_mfma_f32_16x16x32_bf16 v[76:79], a[168:171], v[232:235], v[76:79]\n"
    "ds_read_b64_tr_b16 a[164:165], v244 offset:61440\n"
    "v_exp_f32_e32 v176, v176\n"
    "v_mfma_f32_16x16x32_bf16 v[76:79], a[172:175], v[236:239], v[76:79]\n"
    "s_add_u32 s61, s28, 24576\n"
    "ds_read_b64_tr_b16 a[166:167], v244 offset:63488\n"
    "v_mfma_f32_16x16x32_bf16 v[72:75], a[172:175], v[228:231], v[72:75]\n"
    "v_and_b32_e32 v254, 0x80, v253\n"
    "v_mfma_f32_16x16x32_bf16 v[72:75], a[168:171], v[224:227], v[72:75]\n"
    "v_exp_f32_e32 v177, v177\n"
    "v_mfma_f32_16x16x32_bf16 v[68:71], a[168:171], v[216:219], v[68:71]\n"
    "ds_read_b64_tr_b16 a[168:169], v245 offset:53248\n"
    "buffer_load_dword v249, v254, s[8:11], s61 offen offset:0 sc1\n"
    "v_mfma_f32_16x16x32_bf16 v[68:71], a[172:175], v[220:223], v[68:71]\n"
    "s_waitcnt lgkmcnt(9)\n"
    "s_add_u32 s61, s29, 24576\n"
    "ds_read_b64_tr_b16 a[170:171], v245 offset:55296\n"
    "v_mfma_f32_16x16x32_bf16 v[80:83], a[176:179], v[216:219], v[80:83]\n"
    "ds_read_b64_tr_b16 a[172:173], v245 offset:61440\n"
    "v_cvt_pk_bf16_f32 v185, v186, v187\n"
    "v_mfma_f32_16x16x32_bf16 v[80:83], a[180:183], v[220:223], v[80:83]\n"
    "ds_read_b64_tr_b16 a[174:175], v245 offset:63488\n"
    "buffer_load_dword v249, v254, s[8:11], s61 offen offset:0 sc1\n"
    "v_mfma_f32_16x16x32_bf16 v[84:87], a[180:183], v[228:231], v[84:87]\n"
    "s_add_u32 s61, s30, 24576\n"
    "v_exp_f32_e32 v178, v178\n"
    "v_mfma_f32_16x16x32_bf16 v[84:87], a[176:179], v[224:227], v[84:87]\n"
    "v_exp_f32_e32 v179, v179\n"
    "v_mfma_f32_16x16x32_bf16 v[88:91], a[176:179], v[232:235], v[88:91]\n"
    "s_waitcnt vmcnt(14)\n"
    "s_barrier\n"
    "v_mfma_f32_16x16x32_bf16 v[88:91], a[180:183], v[236:239], v[88:91]\n"
    "s_waitcnt lgkmcnt(8)\n"
    "ds_read_b64_tr_b16 a[176:177], v246 offset:53248\n"
    "buffer_load_dword v249, v254, s[8:11], s61 offen offset:0 sc1\n"
    "v_mfma_f32_16x16x32_bf16 v[100:103], a[184:187], v[232:235], v[100:103]\n"
    "s_add_u32 s61, s31, 24576\n"
    "ds_read_b64_tr_b16 a[178:179], v246 offset:55296\n"
    "v_cvt_pk_bf16_f32 v176, v176, v177\n"
    "v_mfma_f32_16x16x32_bf16 v[100:103], a[188:191], v[236:239], v[100:103]\n"
    "ds_read_b64_tr_b16 a[180:181], v246 offset:61440\n"
    "v_cvt_pk_bf16_f32 v177, v178, v179\n"
    "v_mfma_f32_16x16x32_bf16 v[96:99], a[188:191], v[228:231], v[96:99]\n"
    "ds_read_b64_tr_b16 a[182:183], v246 offset:63488\n"
    "buffer_load_dword v249, v254, s[8:11], s61 offen offset:0 sc1\n"
    "v_mfma_f32_16x16x32_bf16 v[96:99], a[184:187], v[224:227], v[96:99]\n"
    "v_exp_f32_e32 v196, v196\n"
    "v_mfma_f32_16x16x32_bf16 v[92:95], a[184:187], v[216:219], v[92:95]\n"
    "ds_read_b64_tr_b16 a[184:185], v247 offset:53248\n"
    "ds_write_addtid_b32 v192 offset:16384\n"
    "v_mfma_f32_16x16x32_bf16 v[92:95], a[188:191], v[220:223], v[92:95]\n"
    "s_waitcnt lgkmcnt(10)\n"
    "ds_read_b64_tr_b16 a[186:187], v247 offset:55296\n"
    "ds_write_addtid_b32 v193 offset:16640\n"
    "v_mfma_f32_16x16x32_bf16 v[104:107], a[160:163], v[216:219], v[104:107]\n"
    "ds_read_b64_tr_b16 a[188:189], v247 offset:61440\n"
    "buffer_load_dword v249, v254, s[8:11], s87 offen offset:0 sc1\n"
    "v_mfma_f32_16x16x32_bf16 v[104:107], a[164:167], v[220:223], v[104:107]\n"
    "ds_read_b64_tr_b16 a[190:191], v247 offset:63488\n"
    "ds_write_addtid_b32 v184 offset:12288\n"
    "v_mfma_f32_16x16x32_bf16 v[108:111], a[164:167], v[228:231], v[108:111]\n"
    "v_exp_f32_e32 v197, v197\n"
    "v_mfma_f32_16x16x32_bf16 v[108:111], a[160:163], v[224:227], v[108:111]\n"
    "v_exp_f32_e32 v198, v198\n"
    "v_mfma_f32_16x16x32_bf16 v[112:115], a[160:163], v[232:235], v[112:115]\n"
    "ds_read_b128 a[160:163], v243 offset:20480\n"
    "ds_write_addtid_b32 v185 offset:12544\n"
    "v_mfma_f32_16x16x32_bf16 v[112:115], a[164:167], v[236:239], v[112:115]\n"
    "s_waitcnt lgkmcnt(13)\n"
    "ds_read_b128 a[164:167], v240 offset:16384\n"
    "v_mfma_f32_16x16x32_bf16 v[124:127], a[168:171], v[232:235], v[124:127]\n"
    "v_exp_f32_e32 v199, v199\n"
    "v_mfma_f32_16x16x32_bf16 v[124:127], a[172:175], v[236:239], v[124:127]\n"
    "ds_write_addtid_b32 v176 offset:8192\n"
    "v_cvt_pk_bf16_f32 v194, v196, v197\n"
    "v_mfma_f32_16x16x32_bf16 v[120:123], a[172:175], v[228:231], v[120:123]\n"
    "v_exp_f32_e32 v188, v188\n"
    "v_mfma_f32_16x16x32_bf16 v[120:123], a[168:171], v[224:227], v[120:123]\n"
    "v_exp_f32_e32 v189, v189\n"
    "v_mfma_f32_16x16x32_bf16 v[116:119], a[168:171], v[216:219], v[116:119]\n"
    "ds_read_b128 a[168:171], v241 offset:16384\n"
    "v_cvt_pk_bf16_f32 v195, v198, v199\n"
    "v_mfma_f32_16x16x32_bf16 v[116:119], a[172:175], v[220:223], v[116:119]\n"
    "s_waitcnt lgkmcnt(12)\n"
    "ds_read_b128 a[172:175], v240 offset:20480\n"
    "ds_write_addtid_b32 v177 offset:8448\n"
    "v_mfma_f32_16x16x32_bf16 v[128:131], a[176:179], v[216:219], v[128:131]\n"
    "v_exp_f32_e32 v190, v190\n"
    "v_mfma_f32_16x16x32_bf16 v[128:131], a[180:183], v[220:223], v[128:131]\n"
    "v_exp_f32_e32 v191, v191\n"
    "v_mfma_f32_16x16x32_bf16 v[132:135], a[180:183], v[228:231], v[132:135]\n"
    "ds_write_addtid_b32 v194 offset:16896\n"
    "v_cvt_pk_bf16_f32 v186, v188, v189\n"
    "v_mfma_f32_16x16x32_bf16 v[132:135], a[176:179], v[224:227], v[132:135]\n"
    "v_exp_f32_e32 v180, v180\n"
    "v_mfma_f32_16x16x32_bf16 v[136:139], a[176:179], v[232:235], v[136:139]\n"
    "ds_read_b128 a[176:179], v241 offset:20480\n"
    "v_mfma_f32_16x16x32_bf16 v[136:139], a[180:183], v[236:239], v[136:139]\n"
    "s_waitcnt lgkmcnt(10)\n"
    "ds_read_b128 a[180:183], v242 offset:16384\n"
    "v_mfma_f32_16x16x32_bf16 v[148:151], a[184:187], v[232:235], v[148:151]\n"
    "v_exp_f32_e32 v181, v181\n"
    "v_mfma_f32_16x16x32_bf16 v[148:151], a[188:191], v[236:239], v[148:151]\n"
    "ds_write_addtid_b32 v195 offset:17152\n"
    "v_cvt_pk_bf16_f32 v187, v190, v191\n"
    "v_mfma_f32_16x16x32_bf16 v[144:147], a[188:191], v[228:231], v[144:147]\n"
    "v_exp_f32_e32 v182, v182\n"
    "v_mfma_f32_16x16x32_bf16 v[144:147], a[184:187], v[224:227], v[144:147]\n"
    "v_exp_f32_e32 v183, v183\n"
    "v_mfma_f32_16x16x32_bf16 v[140:143], a[184:187], v[216:219], v[140:143]\n"
    "ds_read_b128 a[184:187], v243 offset:16384\n"
    "v_cvt_pk_bf16_f32 v178, v180, v181\n"
    "v_mfma_f32_16x16x32_bf16 v[140:143], a[188:191], v[220:223], v[140:143]\n"
    "s_waitcnt lgkmcnt(9)\n"
    "ds_read_b128 a[188:191], v242 offset:20480\n"
    "v_cvt_pk_bf16_f32 v179, v182, v183\n"
    "v_mfma_f32_16x16x32_bf16 v[216:219], a[160:163], a[92:95], 0\n"
    "ds_write_addtid_b32 v186 offset:12800\n"
    "v_mfma_f32_16x16x32_bf16 v[216:219], a[164:167], a[64:67], v[216:219]\n"
    "ds_write_addtid_b32 v187 offset:13056\n"
    "v_mfma_f32_16x16x32_bf16 v[224:227], a[164:167], a[96:99], 0\n"
    "ds_write_addtid_b32 v178 offset:8704\n"
    "v_mfma_f32_16x16x32_bf16 v[224:227], a[160:163], a[124:127], v[224:227]\n"
    "ds_write_addtid_b32 v179 offset:8960\n"
    "v_mfma_f32_16x16x32_bf16 v[232:235], a[160:163], a[156:159], 0\n"
    "ds_read_b128 a[160:163], v243 offset:22528\n"
    "v_mfma_f32_16x16x32_bf16 v[232:235], a[164:167], a[128:131], v[232:235]\n"
    "s_waitcnt lgkmcnt(12)\n"
    "ds_read_b128 a[164:167], v240 offset:18432\n"
    "v_mfma_f32_16x16x32_bf16 v[232:235], a[168:171], a[132:135], v[232:235]\n"
    "v_mfma_f32_16x16x32_bf16 v[232:235], a[172:175], a[136:139], v[232:235]\n"
    "v_mfma_f32_16x16x32_bf16 v[224:227], a[172:175], a[104:107], v[224:227]\n"
    "s_waitcnt lgkmcnt(2)\n"
    "s_barrier\n"
    "v_mfma_f32_16x16x32_bf16 v[224:227], a[168:171], a[100:103], v[224:227]\n"
    "v_mfma_f32_16x16x32_bf16 v[216:219], a[168:171], a[68:71], v[216:219]\n"
    "ds_read_b128 a[168:171], v241 offset:18432\n"
    "v_mfma_f32_16x16x32_bf16 v[216:219], a[172:175], a[72:75], v[216:219]\n"
    "ds_read_b128 a[172:175], v240 offset:22528\n"
    "v_mfma_f32_16x16x32_bf16 v[216:219], a[176:179], a[76:79], v[216:219]\n"
    "ds_read2st64_b32 v[180:181], v250 offset0:32 offset1:33\n"
    "v_mfma_f32_16x16x32_bf16 v[216:219], a[180:183], a[80:83], v[216:219]\n"
    "v_mfma_f32_16x16x32_bf16 v[224:227], a[180:183], a[112:115], v[224:227]\n"
    "v_mfma_f32_16x16x32_bf16 v[224:227], a[176:179], a[108:111], v[224:227]\n"
    "v_mfma_f32_16x16x32_bf16 v[232:235], a[176:179], a[140:143], v[232:235]\n"
    "ds_read_b128 a[176:179], v241 offset:22528\n"
    "v_mfma_f32_16x16x32_bf16 v[232:235], a[180:183], a[144:147], v[232:235]\n"
    "ds_read_b128 a[180:183], v242 offset:18432\n"
    "v_mfma_f32_16x16x32_bf16 v[232:235], a[184:187], a[148:151], v[232:235]\n"
    "ds_read2st64_b32 v[182:183], v250 offset0:34 offset1:35\n"
    "v_mfma_f32_16x16x32_bf16 v[232:235], a[188:191], a[152:155], v[232:235]\n"
    "v_mfma_f32_16x16x32_bf16 v[224:227], a[188:191], a[120:123], v[224:227]\n"
    "v_mfma_f32_16x16x32_bf16 v[224:227], a[184:187], a[116:119], v[224:227]\n"
    "ds_read2st64_b32 v[188:189], v250 offset0:48 offset1:49\n"
    "v_mfma_f32_16x16x32_bf16 v[216:219], a[184:187], a[84:87], v[216:219]\n"
    "ds_read_b128 a[184:187], v243 offset:18432\n"
    "v_mfma_f32_16x16x32_bf16 v[216:219], a[188:191], a[88:91], v[216:219]\n"
    "s_waitcnt lgkmcnt(8)\n"
    "ds_read_b128 a[188:191], v242 offset:22528\n"
    "v_mfma_f32_16x16x32_bf16 v[220:223], a[160:163], a[92:95], 0\n"
    "v_exp_f32_e32 v232, v232\n"
    "v_mfma_f32_16x16x32_bf16 v[220:223], a[164:167], a[64:67], v[220:223]\n"
    "v_exp_f32_e32 v233, v233\n"
    "v_mfma_f32_16x16x32_bf16 v[228:231], a[164:167], a[96:99], 0\n"
    "s_add_u32 s48, s57, 18432\n"
    "v_exp_f32_e32 v234, v234\n"
    "v_mfma_f32_16x16x32_bf16 v[228:231], a[160:163], a[124:127], v[228:231]\n"
    "ds_read2st64_b32 v[190:191], v250 offset0:50 offset1:51\n"
    "v_mfma_f32_16x16x32_bf16 v[236:239], a[160:163], a[156:159], 0\n"
    "ds_read_b64_tr_b16 a[160:161], v244 offset:0\n"
    "v_exp_f32_e32 v235, v235\n"
    "v_mfma_f32_16x16x32_bf16 v[236:239], a[164:167], a[128:131], v[236:239]\n"
    "s_waitcnt lgkmcnt(9)\n"
    "ds_read_b64_tr_b16 a[162:163], v244 offset:2048\n"
    "v_mfma_f32_16x16x32_bf16 v[236:239], a[168:171], a[132:135], v[236:239]\n"
    "ds_read_b64_tr_b16 a[164:165], v244 offset:8192\n"
    "buffer_load_dwordx4 a[92:95], v253, s[8:11], s48 offen offset:0 sc1\n"
    "v_mfma_f32_16x16x32_bf16 v[236:239], a[172:175], a[136:139], v[236:239]\n"
    "ds_read_b64_tr_b16 a[166:167], v244 offset:10240\n"
    "v_cvt_pk_bf16_f32 v232, v232, v233\n"
    "v_mfma_f32_16x16x32_bf16 v[228:231], a[172:175], a[104:107], v[228:231]\n"
    "ds_read2st64_b32 v[196:197], v250 offset0:64 offset1:65\n"
    "v_mfma_f32_16x16x32_bf16 v[228:231], a[168:171], a[100:103], v[228:231]\n"
    "v_exp_f32_e32 v224, v224\n"
    "v_mfma_f32_16x16x32_bf16 v[220:223], a[168:171], a[68:71], v[220:223]\n"
    "ds_read_b64_tr_b16 a[168:169], v245 offset:0\n"
    "buffer_load_dwordx4 a[124:127], v253, s[8:11], s48 offen offset:1024 sc1\n"
    "v_mfma_f32_16x16x32_bf16 v[220:223], a[172:175], a[72:75], v[220:223]\n"
    "s_waitcnt lgkmcnt(11)\n"
    "s_add_u32 s48, s57, 2048\n"
    "ds_read_b64_tr_b16 a[170:171], v245 offset:2048\n"
    "v_mfma_f32_16x16x32_bf16 v[220:223], a[176:179], a[76:79], v[220:223]\n"
    "ds_read_b64_tr_b16 a[172:173], v245 offset:8192\n"
    "v_exp_f32_e32 v225, v225\n"
    "v_mfma_f32_16x16x32_bf16 v[220:223], a[180:183], a[80:83], v[220:223]\n"
    "ds_read_b64_tr_b16 a[174:175], v245 offset:10240\n"
    "v_cvt_pk_bf16_f32 v233, v234, v235\n"
    "v_mfma_f32_16x16x32_bf16 v[228:231], a[180:183], a[112:115], v[228:231]\n"
    "buffer_load_dwordx4 a[156:159], v253, s[8:11], s48 offen offset:0 sc1\n"
    "v_mfma_f32_16x16x32_bf16 v[228:231], a[176:179], a[108:111], v[228:231]\n"
    "ds_read2st64_b32 v[198:199], v250 offset0:66 offset1:67\n"
    "v_exp_f32_e32 v226, v226\n"
    "v_mfma_f32_16x16x32_bf16 v[236:239], a[176:179], a[140:143], v[236:239]\n"
    "ds_read_b64_tr_b16 a[176:177], v246 offset:0\n"
    "v_mfma_f32_16x16x32_bf16 v[236:239], a[180:183], a[144:147], v[236:239]\n"
    "s_waitcnt lgkmcnt(12)\n"
    "ds_read_b64_tr_b16 a[178:179], v246 offset:2048\n"
    "v_exp_f32_e32 v227, v227\n"
    "v_mfma_f32_16x16x32_bf16 v[236:239], a[184:187], a[148:151], v[236:239]\n"
    "ds_read_b64_tr_b16 a[180:181], v246 offset:8192\n"
    "buffer_load_dwordx4 a[64:67], v253, s[8:11], s48 offen offset:1024 sc1\n"
    "v_mfma_f32_16x16x32_bf16 v[236:239], a[188:191], a[152:155], v[236:239]\n"
    "s_add_u32 s48, s57, 20480\n"
    "ds_read_b64_tr_b16 a[182:183], v246 offset:10240\n"
    "v_cvt_pk_bf16_f32 v224, v224, v225\n"
    "v_mfma_f32_16x16x32_bf16 v[228:231], a[188:191], a[120:123], v[228:231]\n"
    "v_exp_f32_e32 v216, v216\n"
    "v_mfma_f32_16x16x32_bf16 v[228:231], a[184:187], a[116:119], v[228:231]\n"
    "v_exp_f32_e32 v217, v217\n"
    "v_mfma_f32_16x16x32_bf16 v[220:223], a[184:187], a[84:87], v[220:223]\n"
    "ds_read_b64_tr_b16 a[184:185], v247 offset:0\n"
    "buffer_load_dwordx4 a[96:99], v253, s[8:11], s48 offen offset:0 sc1\n"
    "v_mfma_f32_16x16x32_bf16 v[220:223], a[188:191], a[88:91], v[220:223]\n"
    "s_waitcnt lgkmcnt(5)\n"
    "ds_read_b64_tr_b16 a[186:187], v247 offset:2048\n"
    "v_mfma_f32_16x16x32_bf16 v[56:59], a[160:163], v[176:179], v[56:59]\n"
    "ds_read_b64_tr_b16 a[188:189], v247 offset:8192\n"
    "v_mfma_f32_16x16x32_bf16 v[56:59], a[164:167], v[180:183], v[56:59]\n"
    "ds_read_b64_tr_b16 a[190:191], v247 offset:10240\n"
    "v_mfma_f32_16x16x32_bf16 v[60:63], a[164:167], v[188:191], v[60:63]\n"
    "buffer_load_dwordx4 a[128:131], v253, s[8:11], s48 offen offset:1024 sc1\n"
    "v_cvt_pk_bf16_f32 v225, v226, v227\n"
    "v_mfma_f32_16x16x32_bf16 v[60:63], a[160:163], v[184:187], v[60:63]\n"
    "s_add_u32 s48, s57, 4096\n"
    "v_exp_f32_e32 v218, v218\n"
    "v_mfma_f32_16x16x32_bf16 v[64:67], a[160:163], v[192:195], v[64:67]\n"
    "ds_read_b64_tr_b16 a[160:161], v244 offset:4096\n"
    "v_mfma_f32_16x16x32_bf16 v[64:67], a[164:167], v[196:199], v[64:67]\n"
    "ds_read_b64_tr_b16 a[162:163], v244 offset:6144\n"
    "v_mfma_f32_16x16x32_bf16 v[76:79], a[168:171], v[192:195], v[76:79]\n"
    "ds_read_b64_tr_b16 a[164:165], v244 offset:12288\n"
    "buffer_load_dwordx4 a[68:71], v253, s[8:11], s48 offen offset:0 sc1\n"
    "v_mfma_f32_16x16x32_bf16 v[76:79], a[172:175], v[196:199], v[76:79]\n"
    "ds_read_b64_tr_b16 a[166:167], v244 offset:14336\n"
    "v_mfma_f32_16x16x32_bf16 v[72:75], a[172:175], v[188:191], v[72:75]\n"
    "s_barrier\n"
    "v_mfma_f32_16x16x32_bf16 v[72:75], a[168:171], v[184:187], v[72:75]\n"
    "v_exp_f32_e32 v219, v219\n"
    "v_mfma_f32_16x16x32_bf16 v[68:71], a[168:171], v[176:179], v[68:71]\n"
    "ds_read_b64_tr_b16 a[168:169], v245 offset:4096\n"
    "buffer_load_dwordx4 a[100:103], v253, s[8:11], s48 offen offset:1024 sc1\n"
    "v_mfma_f32_16x16x32_bf16 v[68:71], a[172:175], v[180:183], v[68:71]\n"
    "s_waitcnt lgkmcnt(9)\n"
    "s_add_u32 s48, s57, 22528\n"
    "ds_read_b64_tr_b16 a[170:171], v245 offset:6144\n"
    "v_mfma_f32_16x16x32_bf16 v[80:83], a[176:179], v[176:179], v[80:83]\n"
    "ds_read_b64_tr_b16 a[172:173], v245 offset:12288\n"
    "v_cvt_pk_bf16_f32 v216, v216, v217\n"
    "v_mfma_f32_16x16x32_bf16 v[80:83], a[180:183], v[180:183], v[80:83]\n"
    "ds_read_b64_tr_b16 a[174:175], v245 offset:14336\n"
    "v_cvt_pk_bf16_f32 v217, v218, v219\n"
    "v_mfma_f32_16x16x32_bf16 v[84:87], a[180:183], v[188:191], v[84:87]\n"
    "buffer_load_dwordx4 a[132:135], v253, s[8:11], s48 offen offset:0 sc1\n"
    "ds_write_addtid_b32 v232 offset:16384\n"
    "v_mfma_f32_16x16x32_bf16 v[84:87], a[176:179], v[184:187], v[84:87]\n"
    "v_exp_f32_e32 v236, v236\n"
    "v_mfma_f32_16x16x32_bf16 v[88:91], a[176:179], v[192:195], v[88:91]\n"
    "ds_read_b64_tr_b16 a[176:177], v246 offset:4096\n"
    "ds_write_addtid_b32 v233 offset:16640\n"
    "v_mfma_f32_16x16x32_bf16 v[88:91], a[180:183], v[196:199], v[88:91]\n"
    "s_waitcnt lgkmcnt(11)\n"
    "ds_read_b64_tr_b16 a[178:179], v246 offset:6144\n"
    "v_mfma_f32_16x16x32_bf16 v[100:103], a[184:187], v[192:195], v[100:103]\n"
    "ds_read_b64_tr_b16 a[180:181], v246 offset:12288\n"
    "buffer_load_dwordx4 a[72:75], v253, s[8:11], s48 offen offset:1024 sc1\n"
    "v_mfma_f32_16x16x32_bf16 v[100:103], a[188:191], v[196:199], v[100:103]\n"
    "s_add_u32 s48, s57, 6144\n"
    "ds_read_b64_tr_b16 a[182:183], v246 offset:14336\n"
    "ds_write_addtid_b32 v224 offset:12288\n"
    "v_mfma_f32_16x16x32_bf16 v[96:99], a[188:191], v[188:191], v[96:99]\n"
    "v_exp_f32_e32 v237, v237\n"
    "v_mfma_f32_16x16x32_bf16 v[96:99], a[184:187], v[184:187], v[96:99]\n"
    "v_exp_f32_e32 v238, v238\n"
    "v_mfma_f32_16x16x32_bf16 v[92:95], a[184:187], v[176:179], v[92:95]\n"
    "ds_read_b64_tr_b16 a[184:185], v247 offset:4096\n"
    "buffer_load_dwordx4 a[104:107], v253, s[8:11], s48 offen offset:0 sc1\n"
    "v_mfma_f32_16x16x32_bf16 v[92:95], a[188:191], v[180:183], v[92:95]\n"
    "s_waitcnt lgkmcnt(12)\n"
    "ds_read_b64_tr_b16 a[186:187], v247 offset:6144\n"
    "ds_write_addtid_b32 v225 offset:12544\n"
    "v_mfma_f32_16x16x32_bf16 v[104:107], a[160:163], v[176:179], v[104:107]\n"
    "ds_read_b64_tr_b16 a[188:189], v247 offset:12288\n"
    "v_mfma_f32_16x16x32_bf16 v[104:107], a[164:167], v[180:183], v[104:107]\n"
    "ds_read_b64_tr_b16 a[190:191], v247 offset:14336\n"
    "v_mfma_f32_16x16x32_bf16 v[108:111], a[164:167], v[188:191], v[108:111]\n"
    "buffer_load_dwordx4 a[136:139], v253, s[8:11], s48 offen offset:1024 sc1\n"
    "ds_write_addtid_b32 v216 offset:8192\n"
    "v_mfma_f32_16x16x32_bf16 v[108:111], a[160:163], v[184:187], v[108:111]\n"
    "s_add_u32 s48, s57, 24576\n"
    "v_exp_f32_e32 v239, v239\n"
    "v_mfma_f32_16x16x32_bf16 v[112:115], a[160:163], v[192:195], v[112:115]\n"
    "ds_read_b128 a[160:163], v243 offset:36864\n"
    "v_cvt_pk_bf16_f32 v234, v236, v237\n"
    "v_mfma_f32_16x16x32_bf16 v[112:115], a[164:167], v[196:199], v[112:115]\n"
    "s_waitcnt lgkmcnt(14)\n"
    "ds_read_b128 a[164:167], v240 offset:32768\n"
    "v_mfma_f32_16x16x32_bf16 v[124:127], a[168:171], v[192:195], v[124:127]\n"
    "buffer_load_dwordx4 a[76:79], v253, s[8:11], s48 offen offset:0 sc1\n"
    "ds_write_addtid_b32 v217 offset:8448\n"
    "v_mfma_f32_16x16x32_bf16 v[124:127], a[172:175], v[196:199], v[124:127]\n"
    "v_exp_f32_e32 v228, v228\n"
    "v_mfma_f32_16x16x32_bf16 v[120:123], a[172:175], v[188:191], v[120:123]\n"
    "v_exp_f32_e32 v229, v229\n"
    "v_mfma_f32_16x16x32_bf16 v[120:123], a[168:171], v[184:187], v[120:123]\n"
    "ds_write_addtid_b32 v234 offset:16896\n"
    "v_cvt_pk_bf16_f32 v235, v238, v239\n"
    "v_mfma_f32_16x16x32_bf16 v[116:119], a[168:171], v[176:179], v[116:119]\n"
    "ds_read_b128 a[168:171], v241 offset:32768\n"
    "buffer_load_dwordx4 a[108:111], v253, s[8:11], s48 offen offset:1024 sc1\n"
    "v_mfma_f32_16x16x32_bf16 v[116:119], a[172:175], v[180:183], v[116:119]\n"
    "s_waitcnt lgkmcnt(12)\n"
    "s_add_u32 s48, s57, 8192\n"
    "ds_read_b128 a[172:175], v240 offset:36864\n"
    "v_mfma_f32_16x16x32_bf16 v[128:131], a[176:179], v[176:179], v[128:131]\n"
    "v_exp_f32_e32 v230, v230\n"
    "v_mfma_f32_16x16x32_bf16 v[128:131], a[180:183], v[180:183], v[128:131]\n"
    "v_exp_f32_e32 v231, v231\n"
    "v_mfma_f32_16x16x32_bf16 v[132:135], a[180:183], v[188:191], v[132:135]\n"
    "buffer_load_dwordx4 a[140:143], v253, s[8:11], s48 offen offset:0 sc1\n"
    "v_cvt_pk_bf16_f32 v226, v228, v229\n"
    "v_mfma_f32_16x16x32_bf16 v[132:135], a[176:179], v[184:187], v[132:135]\n"
    "v_exp_f32_e32 v220, v220\n"
    "v_mfma_f32_16x16x32_bf16 v[136:139], a[176:179], v[192:195], v[136:139]\n"
    "ds_read_b128 a[176:179], v241 offset:36864\n"
    "v_mfma_f32_16x16x32_bf16 v[136:139], a[180:183], v[196:199], v[136:139]\n"
    "s_waitcnt lgkmcnt(8)\n"
    "ds_read_b128 a[180:183], v242 offset:32768\n"
    "v_mfma_f32_16x16x32_bf16 v[148:151], a[184:187], v[192:195], v[148:151]\n"
    "buffer_load_dwordx4 a[80:83], v253, s[8:11], s48 offen offset:1024 sc1\n"
    "ds_write_addtid_b32 v235 offset:17152\n"
    "v_mfma_f32_16x16x32_bf16 v[148:151], a[188:191], v[196:199], v[148:151]\n"
    "s_add_u32 s48, s57, 26624\n"
    "v_exp_f32_e32 v221, v221\n"
    "v_mfma_f32_16x16x32_bf16 v[144:147], a[188:191], v[188:191], v[144:147]\n"
    "ds_write_addtid_b32 v226 offset:12800\n"
    "v_cvt_pk_bf16_f32 v227, v230, v231\n"
    "v_mfma_f32_16x16x32_bf16 v[144:147], a[184:187], v[184:187], v[144:147]\n"
    "v_exp_f32_e32 v222, v222\n"
    "v_mfma_f32_16x16x32_bf16 v[140:143], a[184:187], v[176:179], v[140:143]\n"
    "ds_read_b128 a[184:187], v243 offset:32768\n"
    "buffer_load_dwordx4 a[112:115], v253, s[8:11], s48 offen offset:0 sc1\n"
    "v_mfma_f32_16x16x32_bf16 v[140:143], a[188:191], v[180:183], v[140:143]\n"
    "s_waitcnt lgkmcnt(5)\n"
    "ds_read_b128 a[188:191], v242 offset:36864\n"
    "v_mfma_f32_16x16x32_bf16 v[160:163], a[160:163], a[28:31], 0\n"
    "v_exp_f32_e32 v223, v223\n"
    "v_mfma_f32_16x16x32_bf16 v[160:163], a[164:167], a[0:3], v[160:163]\n"
    "v_cvt_pk_bf16_f32 v218, v220, v221\n"
    "v_cvt_pk_bf16_f32 v219, v222, v223\n"
    "v_mfma_f32_16x16x32_bf16 v[168:171], a[164:167], a[32:35], 0\n"
    "buffer_load_dwordx4 a[144:147], v253, s[8:11], s48 offen offset:1024 sc1\n"
    "ds_write_addtid_b32 v227 offset:13056\n"
    "v_mfma_f32_16x16x32_bf16 v[168:171], a[160:163], a[60:63], v[168:171]\n"
    "s_add_u32 s48, s57, 10240\n"
    "ds_read_b128 a[160:163], v243 offset:38912\n"
    "v_mfma_f32_16x16x32_bf16 v[168:171], a[168:171], a[36:39], v[168:171]\n"
    "ds_read_b128 a[164:167], v240 offset:34816\n"
    "v_mfma_f32_16x16x32_bf16 v[168:171], a[172:175], a[40:43], v[168:171]\n"
    "ds_write_addtid_b32 v218 offset:8704\n"
    "v_mfma_f32_16x16x32_bf16 v[160:163], a[172:175], a[8:11], v[160:163]\n"
    "buffer_load_dwordx4 a[84:87], v253, s[8:11], s48 offen offset:0 sc1\n"
    "ds_write_addtid_b32 v219 offset:8960\n"
    "v_mfma_f32_16x16x32_bf16 v[160:163], a[168:171], a[4:7], v[160:163]\n"
    "s_waitcnt lgkmcnt(5)\n"
    "ds_read_b128 a[168:171], v241 offset:34816\n"
    "v_mfma_f32_16x16x32_bf16 v[160:163], a[176:179], a[12:15], v[160:163]\n"
    "ds_read_b128 a[172:175], v240 offset:38912\n"
    "v_mfma_f32_16x16x32_bf16 v[160:163], a[180:183], a[16:19], v[160:163]\n"
    "v_mfma_f32_16x16x32_bf16 v[168:171], a[180:183], a[48:51], v[168:171]\n"
    "buffer_load_dwordx4 a[116:119], v253, s[8:11], s48 offen offset:1024 sc1\n"
    "v_mfma_f32_16x16x32_bf16 v[168:171], a[176:179], a[44:47], v[168:171]\n"
    "s_add_u32 s48, s57, 28672\n"
    "ds_read_b128 a[176:179], v241 offset:38912\n"
    "v_mfma_f32_16x16x32_bf16 v[168:171], a[184:187], a[52:55], v[168:171]\n"
    "s_waitcnt lgkmcnt(3)\n"
    "s_barrier\n"
    "v_mfma_f32_16x16x32_bf16 v[168:171], a[188:191], a[56:59], v[168:171]\n"
    "ds_read_b128 a[180:183], v242 offset:34816\n"
    "v_mfma_f32_16x16x32_bf16 v[160:163], a[188:191], a[24:27], v[160:163]\n"
    "buffer_load_dwordx4 a[148:151], v253, s[8:11], s48 offen offset:0 sc1\n"
    "ds_read2st64_b32 v[220:221], v250 offset0:32 offset1:33\n"
    "v_mfma_f32_16x16x32_bf16 v[160:163], a[184:187], a[20:23], v[160:163]\n"
    "s_waitcnt lgkmcnt(3)\n"
    "ds_read_b128 a[184:187], v243 offset:34816\n"
    "v_mfma_f32_16x16x32_bf16 v[164:167], a[160:163], a[28:31], 0\n"
    "ds_read_b128 a[188:191], v242 offset:38912\n"
    "v_mfma_f32_16x16x32_bf16 v[164:167], a[164:167], a[0:3], v[164:167]\n"
    "ds_read2st64_b32 v[222:223], v250 offset0:34 offset1:35\n"
    "v_mfma_f32_16x16x32_bf16 v[172:175], a[164:167], a[32:35], 0\n"
    "buffer_load_dwordx4 a[88:91], v253, s[8:11], s48 offen offset:1024 sc1\n"
    "ds_read2st64_b32 v[228:229], v250 offset0:48 offset1:49\n"
    "v_mfma_f32_16x16x32_bf16 v[172:175], a[160:163], a[60:63], v[172:175]\n"
    "s_add_u32 s48, s57, 12288\n"
    "ds_read_b64_tr_b16 a[160:161], v244 offset:16384\n"
    "ds_read2st64_b32 v[230:231], v250 offset0:50 offset1:51\n"
    "v_mfma_f32_16x16x32_bf16 v[172:175], a[168:171], a[36:39], v[172:175]\n"
    "ds_read_b64_tr_b16 a[162:163], v244 offset:18432\n"
    "v_mfma_f32_16x16x32_bf16 v[172:175], a[172:175], a[40:43], v[172:175]\n"
    "ds_read_b64_tr_b16 a[164:165], v244 offset:24576\n"
    "ds_read2st64_b32 v[236:237], v250 offset0:64 offset1:65\n"
    "v_mfma_f32_16x16x32_bf16 v[164:167], a[172:175], a[8:11], v[164:167]\n"
    "ds_read_b64_tr_b16 a[166:167], v244 offset:26624\n"
    "buffer_load_dwordx4 a[120:123], v253, s[8:11], s48 offen offset:0 sc1\n"
    "v_mfma_f32_16x16x32_bf16 v[164:167], a[168:171], a[4:7], v[164:167]\n"
    "s_waitcnt lgkmcnt(8)\n"
    "ds_read_b64_tr_b16 a[168:169], v245 offset:16384\n"
    "ds_read2st64_b32 v[238:239], v250 offset0:66 offset1:67\n"
    "v_mfma_f32_16x16x32_bf16 v[164:167], a[176:179], a[12:15], v[164:167]\n"
    "ds_read_b64_tr_b16 a[170:171], v245 offset:18432\n"
    "v_mfma_f32_16x16x32_bf16 v[164:167], a[180:183], a[16:19], v[164:167]\n"
    "ds_read_b64_tr_b16 a[172:173], v245 offset:24576\n"
    "v_mfma_f32_16x16x32_bf16 v[172:175], a[180:183], a[48:51], v[172:175]\n"
    "ds_read_b64_tr_b16 a[174:175], v245 offset:26624\n"
    "buffer_load_dwordx4 a[152:155], v253, s[8:11], s48 offen offset:1024 sc1\n"
    "v_mfma_f32_16x16x32_bf16 v[172:175], a[176:179], a[44:47], v[172:175]\n"
    "ds_read_b64_tr_b16 a[176:177], v246 offset:16384\n"
    "v_mfma_f32_16x16x32_bf16 v[172:175], a[184:187], a[52:55], v[172:175]\n"
    "s_waitcnt lgkmcnt(4)\n"
    "s_barrier\n"
    "v_mfma_f32_16x16x32_bf16 v[172:175], a[188:191], a[56:59], v[172:175]\n"
    "s_add_u32 s61, s47, 5779456\n"
    "ds_read_b64_tr_b16 a[178:179], v246 offset:18432\n"
    "v_mfma_f32_16x16x32_bf16 v[164:167], a[188:191], a[24:27], v[164:167]\n"
    "s_add_u32 m0, s49, 8192\n"
    "ds_read_b64_tr_b16 a[180:181], v246 offset:24576\n"
    "v_mfma_f32_16x16x32_bf16 v[164:167], a[184:187], a[20:23], v[164:167]\n"
    "ds_read_b64_tr_b16 a[182:183], v246 offset:26624\n"
    "buffer_load_dwordx4 v252, s[8:11], s61 offen offset:0 sc1 lds\n"
    "v_mfma_f32_16x16x32_bf16 v[56:59], a[160:163], v[216:219], v[56:59]\n"
    "s_add_u32 s61, s47, 5783552\n"
    "ds_read_b64_tr_b16 a[184:185], v247 offset:16384\n"
    "v_mfma_f32_16x16x32_bf16 v[56:59], a[164:167], v[220:223], v[56:59]\n"
    "s_add_u32 m0, s49, 12288\n"
    "ds_read_b64_tr_b16 a[186:187], v247 offset:18432\n"
    "v_mfma_f32_16x16x32_bf16 v[60:63], a[164:167], v[228:231], v[60:63]\n"
    "ds_read_b64_tr_b16 a[188:189], v247 offset:24576\n"
    "buffer_load_dwordx4 v252, s[8:11], s61 offen offset:0 sc1 lds\n"
    "v_mfma_f32_16x16x32_bf16 v[60:63], a[160:163], v[224:227], v[60:63]\n"
    "s_add_u32 s61, s47, 5787648\n"
    "ds_read_b64_tr_b16 a[190:191], v247 offset:26624\n"
    "v_mfma_f32_16x16x32_bf16 v[64:67], a[160:163], v[232:235], v[64:67]\n"
    "s_add_u32 m0, s49, 16384\n"
    "ds_read_b64_tr_b16 a[160:161], v244 offset:20480\n"
    "v_lshrrev_b32_e32 v158, 4, v253\n"
    "v_mfma_f32_16x16x32_bf16 v[64:67], a[164:167], v[236:239], v[64:67]\n"
    "s_waitcnt lgkmcnt(9)\n"
    "ds_read_b64_tr_b16 a[162:163], v244 offset:22528\n"
    "buffer_load_dwordx4 v252, s[8:11], s61 offen offset:0 sc1 lds\n"
    "v_mfma_f32_16x16x32_bf16 v[76:79], a[168:171], v[232:235], v[76:79]\n"
    "s_mov_b32 m0, s49\n"
    "ds_read_b64_tr_b16 a[164:165], v244 offset:28672\n"
    "v_lshrrev_b32_e32 v159, 4, v158\n"
    "v_mfma_f32_16x16x32_bf16 v[76:79], a[172:175], v[236:239], v[76:79]\n"
    "ds_read_b64_tr_b16 a[166:167], v244 offset:30720\n"
    "v_cvt_pk_bf16_f32 v64, v64, v65\n"
    "v_mfma_f32_16x16x32_bf16 v[72:75], a[172:175], v[228:231], v[72:75]\n"
    "v_exp_f32_e32 v168, v168\n"
    "v_mfma_f32_16x16x32_bf16 v[72:75], a[168:171], v[224:227], v[72:75]\n"
    "v_exp_f32_e32 v169, v169\n"
    "v_mfma_f32_16x16x32_bf16 v[68:71], a[168:171], v[216:219], v[68:71]\n"
    "ds_read_b64_tr_b16 a[168:169], v245 offset:20480\n"
    "v_cvt_pk_bf16_f32 v65, v66, v67\n"
    "v_mfma_f32_16x16x32_bf16 v[68:71], a[172:175], v[220:223], v[68:71]\n"
    "s_waitcnt lgkmcnt(9)\n"
    "ds_read_b64_tr_b16 a[170:171], v245 offset:22528\n"
    "v_cvt_pk_bf16_f32 v66, v76, v77\n"
    "v_mfma_f32_16x16x32_bf16 v[80:83], a[176:179], v[216:219], v[80:83]\n"
    "ds_read_b64_tr_b16 a[172:173], v245 offset:28672\n"
    "v_cvt_pk_bf16_f32 v67, v78, v79\n"
    "v_mfma_f32_16x16x32_bf16 v[80:83], a[180:183], v[220:223], v[80:83]\n"
    "ds_read_b64_tr_b16 a[174:175], v245 offset:30720\n"
    "v_cvt_pk_bf16_f32 v60, v60, v61\n"
    "v_mfma_f32_16x16x32_bf16 v[84:87], a[180:183], v[228:231], v[84:87]\n"
    "v_exp_f32_e32 v170, v170\n"
    "v_mfma_f32_16x16x32_bf16 v[84:87], a[176:179], v[224:227], v[84:87]\n"
    "v_exp_f32_e32 v171, v171\n"
    "v_mfma_f32_16x16x32_bf16 v[88:91], a[176:179], v[232:235], v[88:91]\n"
    "ds_read_b64_tr_b16 a[176:177], v246 offset:20480\n"
    "buffer_store_dwordx4 v[64:67], v253, s[8:11], s86 offen offset:0 sc1\n"
    "v_cvt_pk_bf16_f32 v168, v168, v169\n"
    "v_cvt_pk_bf16_f32 v61, v62, v63\n"
    "v_bfe_u32 v255, v158, 2, 2\n"
    "v_mfma_f32_16x16x32_bf16 v[88:91], a[180:183], v[236:239], v[88:91]\n"
    "s_waitcnt lgkmcnt(9)\n"
    "ds_read_b64_tr_b16 a[178:179], v246 offset:22528\n"
    "v_cvt_pk_bf16_f32 v62, v72, v73\n"
    "v_mfma_f32_16x16x32_bf16 v[100:103], a[184:187], v[232:235], v[100:103]\n"
    "ds_read_b64_tr_b16 a[180:181], v246 offset:28672\n"
    "v_cvt_pk_bf16_f32 v63, v74, v75\n"
    "v_mfma_f32_16x16x32_bf16 v[100:103], a[188:191], v[236:239], v[100:103]\n"
    "ds_read_b64_tr_b16 a[182:183], v246 offset:30720\n"
    "v_cvt_pk_bf16_f32 v56, v56, v57\n"
    "v_mfma_f32_16x16x32_bf16 v[96:99], a[188:191], v[228:231], v[96:99]\n"
    "v_exp_f32_e32 v160, v160\n"
    "v_mfma_f32_16x16x32_bf16 v[96:99], a[184:187], v[224:227], v[96:99]\n"
    "v_exp_f32_e32 v161, v161\n"
    "v_and_b32_e32 v158, 3, v158\n"
    "v_mfma_f32_16x16x32_bf16 v[92:95], a[184:187], v[216:219], v[92:95]\n"
    "ds_read_b64_tr_b16 a[184:185], v247 offset:20480\n"
    "v_cvt_pk_bf16_f32 v169, v170, v171\n"
    "v_mfma_f32_16x16x32_bf16 v[92:95], a[188:191], v[220:223], v[92:95]\n"
    "s_waitcnt lgkmcnt(9)\n"
    "ds_read_b64_tr_b16 a[186:187], v247 offset:22528\n"
    "v_cvt_pk_bf16_f32 v57, v58, v59\n"
    "v_mfma_f32_16x16x32_bf16 v[104:107], a[160:163], v[216:219], v[104:107]\n"
    "ds_read_b64_tr_b16 a[188:189], v247 offset:28672\n"
    "v_cvt_pk_bf16_f32 v58, v68, v69\n"
    "v_mfma_f32_16x16x32_bf16 v[104:107], a[164:167], v[220:223], v[104:107]\n"
    "ds_read_b64_tr_b16 a[190:191], v247 offset:30720\n"
    "v_cvt_pk_bf16_f32 v59, v70, v71\n"
    "v_xor_b32_e32 v159, v159, v255\n"
    "v_mfma_f32_16x16x32_bf16 v[108:111], a[164:167], v[228:231], v[108:111]\n"
    "buffer_store_dwordx4 v[60:63], v253, s[8:11], s85 offen offset:0 sc1\n"
    "v_exp_f32_e32 v162, v162\n"
    "v_mfma_f32_16x16x32_bf16 v[108:111], a[160:163], v[224:227], v[108:111]\n"
    "v_exp_f32_e32 v163, v163\n"
    "v_mfma_f32_16x16x32_bf16 v[112:115], a[160:163], v[232:235], v[112:115]\n"
    "ds_read_b128 a[160:163], v243 offset:53248\n"
    "v_cvt_pk_bf16_f32 v160, v160, v161\n"
    "v_mfma_f32_16x16x32_bf16 v[112:115], a[164:167], v[236:239], v[112:115]\n"
    "s_waitcnt lgkmcnt(9)\n"
    "ds_read_b128 a[164:167], v240 offset:49152\n"
    "v_cvt_pk_bf16_f32 v161, v162, v163\n"
    "v_mfma_f32_16x16x32_bf16 v[124:127], a[168:171], v[232:235], v[124:127]\n"
    "v_exp_f32_e32 v172, v172\n"
    "v_lshl_or_b32 v158, v159, 2, v158\n"
    "v_mfma_f32_16x16x32_bf16 v[124:127], a[172:175], v[236:239], v[124:127]\n"
    "v_exp_f32_e32 v173, v173\n"
    "v_mfma_f32_16x16x32_bf16 v[120:123], a[172:175], v[228:231], v[120:123]\n"
    "v_exp_f32_e32 v174, v174\n"
    "v_mfma_f32_16x16x32_bf16 v[120:123], a[168:171], v[224:227], v[120:123]\n"
    "v_exp_f32_e32 v175, v175\n"
    "v_mfma_f32_16x16x32_bf16 v[116:119], a[168:171], v[216:219], v[116:119]\n"
    "ds_read_b128 a[168:171], v241 offset:49152\n"
    "ds_write_addtid_b32 v168 offset:4096\n"
    "v_mfma_f32_16x16x32_bf16 v[116:119], a[172:175], v[220:223], v[116:119]\n"
    "s_waitcnt lgkmcnt(8)\n"
    "ds_read_b128 a[172:175], v240 offset:53248\n"
    "v_cvt_pk_bf16_f32 v88, v88, v89\n"
    "v_mov_b32_e32 v159, 0x3f80\n"
    "v_mfma_f32_16x16x32_bf16 v[128:131], a[176:179], v[216:219], v[128:131]\n"
    "ds_write_addtid_b32 v169 offset:4352\n"
    "v_cvt_pk_bf16_f32 v170, v172, v173\n"
    "v_mfma_f32_16x16x32_bf16 v[128:131], a[180:183], v[220:223], v[128:131]\n"
    "buffer_store_dwordx4 v[56:59], v253, s[8:11], s84 offen offset:0 sc1\n"
    "v_exp_f32_e32 v164, v164\n"
    "v_mfma_f32_16x16x32_bf16 v[132:135], a[180:183], v[228:231], v[132:135]\n"
    "v_exp_f32_e32 v165, v165\n"
    "v_mfma_f32_16x16x32_bf16 v[132:135], a[176:179], v[224:227], v[132:135]\n"
    "ds_write_addtid_b32 v160 offset:0\n"
    "v_cvt_pk_bf16_f32 v171, v174, v175\n"
    "v_mfma_f32_16x16x32_bf16 v[136:139], a[176:179], v[232:235], v[136:139]\n"
    "ds_read_b128 a[176:179], v241 offset:53248\n"
    "v_cvt_pk_bf16_f32 v89, v90, v91\n"
    "v_mfma_f32_16x16x32_bf16 v[136:139], a[180:183], v[236:239], v[136:139]\n"
    "s_waitcnt lgkmcnt(8)\n"
    "ds_read_b128 a[180:183], v242 offset:49152\n"
    "v_cvt_pk_bf16_f32 v90, v100, v101\n"
    "v_mfma_f32_16x16x32_bf16 v[148:151], a[184:187], v[232:235], v[148:151]\n"
    "v_exp_f32_e32 v166, v166\n"
    "v_mfma_f32_16x16x32_bf16 v[148:151], a[188:191], v[236:239], v[148:151]\n"
    "v_exp_f32_e32 v167, v167\n"
    "v_mfma_f32_16x16x32_bf16 v[144:147], a[188:191], v[228:231], v[144:147]\n"
    "ds_write_addtid_b32 v161 offset:256\n"
    "v_cvt_pk_bf16_f32 v162, v164, v165\n"
    "v_mfma_f32_16x16x32_bf16 v[144:147], a[184:187], v[224:227], v[144:147]\n"
    "ds_write_addtid_b32 v170 offset:4608\n"
    "v_cvt_pk_bf16_f32 v163, v166, v167\n"
    "v_cvt_pk_bf16_f32 v91, v102, v103\n"
    "v_mfma_f32_16x16x32_bf16 v[140:143], a[184:187], v[216:219], v[140:143]\n"
    "ds_read_b128 a[184:187], v243 offset:49152\n"
    "buffer_store_dwordx4 v[88:91], v253, s[8:11], s86 offen offset:1024 sc1\n"
    "v_cvt_pk_bf16_f32 v84, v84, v85\n"
    "v_mov_b32_e32 v152, 0\n"
    "v_mfma_f32_16x16x32_bf16 v[140:143], a[188:191], v[220:223], v[140:143]\n"
    "s_waitcnt lgkmcnt(7)\n"
    "ds_read_b128 a[188:191], v242 offset:53248\n"
    "v_mov_b32_e32 v153, 0\n"
    "v_mfma_f32_16x16x32_bf16 v[200:203], a[160:163], a[28:31], 0\n"
    "ds_write_addtid_b32 v171 offset:4864\n"
    "v_mov_b32_e32 v156, 0\n"
    "v_mfma_f32_16x16x32_bf16 v[200:203], a[164:167], a[0:3], v[200:203]\n"
    "ds_write_addtid_b32 v162 offset:512\n"
    "v_mov_b32_e32 v157, 0\n"
    "v_mfma_f32_16x16x32_bf16 v[208:211], a[164:167], a[32:35], 0\n"
    "ds_write_addtid_b32 v163 offset:768\n"
    "v_cmp_eq_u32_e32 vcc, 0, v158\n"
    "v_mfma_f32_16x16x32_bf16 v[208:211], a[160:163], a[60:63], v[208:211]\n"
    "ds_read_b128 a[160:163], v243 offset:55296\n"
    "v_cndmask_b32_e32 v154, 0, v159, vcc\n"
    "v_mfma_f32_16x16x32_bf16 v[208:211], a[168:171], a[36:39], v[208:211]\n"
    "ds_read_b128 a[164:167], v240 offset:51200\n"
    "v_cmp_eq_u32_e32 vcc, 1, v158\n"
    "v_mfma_f32_16x16x32_bf16 v[208:211], a[172:175], a[40:43], v[208:211]\n"
    "v_cvt_pk_bf16_f32 v85, v86, v87\n"
    "v_cndmask_b32_e64 v154, v154, 1.0, vcc\n"
    "v_mfma_f32_16x16x32_bf16 v[200:203], a[172:175], a[8:11], v[200:203]\n"
    "s_waitcnt lgkmcnt(2)\n"
    "s_barrier\n"
    "v_mfma_f32_16x16x32_bf16 v[200:203], a[168:171], a[4:7], v[200:203]\n"
    "ds_read_b128 a[168:171], v241 offset:51200\n"
    "v_cmp_eq_u32_e32 vcc, 2, v158\n"
    "v_mfma_f32_16x16x32_bf16 v[200:203], a[176:179], a[12:15], v[200:203]\n"
    "ds_read_b128 a[172:175], v240 offset:55296\n"
    "v_cndmask_b32_e32 v155, 0, v159, vcc\n"
    "v_mfma_f32_16x16x32_bf16 v[200:203], a[180:183], a[16:19], v[200:203]\n"
    "ds_read2st64_b32 v[164:165], v250 offset0:0 offset1:1\n"
    "v_cmp_eq_u32_e32 vcc, 3, v158\n"
    "s_nop 1\n"
    "v_cndmask_b32_e64 v155, v155, 1.0, vcc\n"
    "v_mfma_f32_16x16x32_bf16 v[208:211], a[180:183], a[48:51], v[208:211]\n"
    "v_cvt_pk_bf16_f32 v86, v96, v97\n"
    "v_and_b32_e32 v251, 0xf0, v253\n"
    "v_mov_b32_e32 v254, 2.0\n"
    "v_mfma_f32_16x16x32_bf16 v[208:211], a[176:179], a[44:47], v[208:211]\n"
    "ds_read_b128 a[176:179], v241 offset:55296\n"
    "v_cmp_le_u32_e32 vcc, 0x100, v253\n"
    "v_mov_b32_e32 v255, s29\n"
    "v_mfma_f32_16x16x32_bf16 v[208:211], a[184:187], a[52:55], v[208:211]\n"
    "ds_read_b128 a[180:183], v242 offset:51200\n"
    "v_cvt_pk_bf16_f32 v87, v98, v99\n"
    "v_mfma_f32_16x16x32_bf16 v[208:211], a[188:191], a[56:59], v[208:211]\n"
    "v_cvt_pk_bf16_f32 v80, v80, v81\n"
    "v_cvt_pk_bf16_f32 v81, v82, v83\n"
    "v_mfma_f32_16x16x32_bf16 v[200:203], a[188:191], a[24:27], v[200:203]\n"
    "ds_read2st64_b32 v[166:167], v250 offset0:2 offset1:3\n"
    "v_cvt_pk_bf16_f32 v82, v92, v93\n"
    "v_mfma_f32_16x16x32_bf16 v[200:203], a[184:187], a[20:23], v[200:203]\n"
    "s_waitcnt lgkmcnt(4)\n"
    "ds_read_b128 a[184:187], v243 offset:51200\n"
    "v_cvt_pk_bf16_f32 v83, v94, v95\n"
    "v_mfma_f32_16x16x32_bf16 v[204:207], a[160:163], a[28:31], 0\n"
    "ds_read_b128 a[188:191], v242 offset:55296\n"
    "v_cvt_pk_bf16_f32 v112, v112, v113\n"
    "v_mfma_f32_16x16x32_bf16 v[204:207], a[164:167], a[0:3], v[204:207]\n"
    "v_exp_f32_e32 v208, v208\n"
    "v_mfma_f32_16x16x32_bf16 v[212:215], a[164:167], a[32:35], 0\n"
    "ds_read2st64_b32 v[172:173], v250 offset0:16 offset1:17\n"
    "v_cvt_pk_bf16_f32 v113, v114, v115\n"
    "v_mfma_f32_16x16x32_bf16 v[212:215], a[160:163], a[60:63], v[212:215]\n"
    "ds_read_b64_tr_b16 a[160:161], v244 offset:32768\n"
    "ds_read_b64_tr_b16 a[162:163], v244 offset:34816\n"
    "v_mfma_f32_16x16x32_bf16 v[212:215], a[168:171], a[36:39], v[212:215]\n"
    "v_exp_f32_e32 v209, v209\n"
    "v_mfma_f32_16x16x32_bf16 v[212:215], a[172:175], a[40:43], v[212:215]\n"
    "ds_read_b64_tr_b16 a[164:165], v244 offset:40960\n"
    "ds_read_b64_tr_b16 a[166:167], v244 offset:43008\n"
    "v_mfma_f32_16x16x32_bf16 v[204:207], a[172:175], a[8:11], v[204:207]\n"
    "v_exp_f32_e32 v210, v210\n"
    "v_mfma_f32_16x16x32_bf16 v[204:207], a[168:171], a[4:7], v[204:207]\n"
    "s_waitcnt lgkmcnt(5)\n"
    "ds_read_b64_tr_b16 a[168:169], v245 offset:32768\n"
    "ds_read_b64_tr_b16 a[170:171], v245 offset:34816\n"
    "v_mfma_f32_16x16x32_bf16 v[204:207], a[176:179], a[12:15], v[204:207]\n"
    "ds_read2st64_b32 v[174:175], v250 offset0:18 offset1:19\n"
    "v_cvt_pk_bf16_f32 v114, v124, v125\n"
    "v_mfma_f32_16x16x32_bf16 v[204:207], a[180:183], a[16:19], v[204:207]\n"
    "ds_read_b64_tr_b16 a[172:173], v245 offset:40960\n"
    "ds_read_b64_tr_b16 a[174:175], v245 offset:43008\n"
    "v_mfma_f32_16x16x32_bf16 v[212:215], a[180:183], a[48:51], v[212:215]\n"
    "v_exp_f32_e32 v211, v211\n"
    "v_mfma_f32_16x16x32_bf16 v[212:215], a[176:179], a[44:47], v[212:215]\n"
    "ds_read_b64_tr_b16 a[176:177], v246 offset:32768\n"
    "ds_read_b64_tr_b16 a[178:179], v246 offset:34816\n"
    "v_mfma_f32_16x16x32_bf16 v[212:215], a[184:187], a[52:55], v[212:215]\n"
    "v_cvt_pk_bf16_f32 v208, v208, v209\n"
    "v_cvt_pk_bf16_f32 v115, v126, v127\n"
    "v_mfma_f32_16x16x32_bf16 v[212:215], a[188:191], a[56:59], v[212:215]\n"
    "ds_read_b64_tr_b16 a[180:181], v246 offset:40960\n"
    "ds_read_b64_tr_b16 a[182:183], v246 offset:43008\n"
    "v_mfma_f32_16x16x32_bf16 v[204:207], a[188:191], a[24:27], v[204:207]\n"
    "v_exp_f32_e32 v200, v200\n"
    "v_mfma_f32_16x16x32_bf16 v[204:207], a[184:187], a[20:23], v[204:207]\n"
    "s_waitcnt lgkmcnt(4)\n"
    "ds_read_b64_tr_b16 a[184:185], v247 offset:32768\n"
    "ds_read_b64_tr_b16 a[186:187], v247 offset:34816\n"
    "v_mfma_f32_16x16x32_bf16 v[0:3], a[160:163], v[160:163], v[0:3]\n"
    "v_exp_f32_e32 v201, v201\n"
    "v_mfma_f32_16x16x32_bf16 v[0:3], a[164:167], v[164:167], v[0:3]\n"
    "ds_read_b64_tr_b16 a[188:189], v247 offset:40960\n"
    "ds_read_b64_tr_b16 a[190:191], v247 offset:43008\n"
    "v_mfma_f32_16x16x32_bf16 a[248:251], a[164:167], v[172:175], a[248:251]\n"
    "v_cvt_pk_bf16_f32 v209, v210, v211\n"
    "v_cvt_pk_bf16_f32 v108, v108, v109\n"
    "v_mfma_f32_16x16x32_bf16 a[248:251], a[160:163], v[168:171], a[248:251]\n"
    "ds_read_b64_tr_b16 a[160:161], v244 offset:36864\n"
    "ds_read_b64_tr_b16 a[162:163], v244 offset:38912\n"
    "v_mfma_f32_16x16x32_bf16 a[252:255], a[168:171], v[168:171], a[252:255]\n"
    "v_exp_f32_e32 v202, v202\n"
    "v_mfma_f32_16x16x32_bf16 a[252:255], a[172:175], v[172:175], a[252:255]\n"
    "s_barrier\n"
    "v_mfma_f32_16x16x32_bf16 v[4:7], a[172:175], v[164:167], v[4:7]\n"
    "ds_read_b64_tr_b16 a[164:165], v244 offset:45056\n"
    "ds_read_b64_tr_b16 a[166:167], v244 offset:47104\n"
    "v_mfma_f32_16x16x32_bf16 v[4:7], a[168:171], v[160:163], v[4:7]\n"
    "s_waitcnt lgkmcnt(4)\n"
    "v_exp_f32_e32 v203, v203\n"
    "v_mfma_f32_16x16x32_bf16 v[8:11], a[176:179], v[160:163], v[8:11]\n"
    "ds_read_b64_tr_b16 a[168:169], v245 offset:36864\n"
    "ds_read_b64_tr_b16 a[170:171], v245 offset:38912\n"
    "v_mfma_f32_16x16x32_bf16 v[8:11], a[180:183], v[164:167], v[8:11]\n"
    "buffer_store_dwordx4 v[84:87], v253, s[8:11], s85 offen offset:1024 sc1\n"
    "v_cvt_pk_bf16_f32 v200, v200, v201\n"
    "v_cvt_pk_bf16_f32 v201, v202, v203\n"
    "v_mfma_f32_16x16x32_bf16 v[32:35], a[180:183], v[172:175], v[32:35]\n"
    "ds_read_b64_tr_b16 a[172:173], v245 offset:45056\n"
    "ds_read_b64_tr_b16 a[174:175], v245 offset:47104\n"
    "v_mfma_f32_16x16x32_bf16 v[32:35], a[176:179], v[168:171], v[32:35]\n"
    "v_exp_f32_e32 v212, v212\n"
    "v_mfma_f32_16x16x32_bf16 v[36:39], a[184:187], v[168:171], v[36:39]\n"
    "ds_read_b64_tr_b16 a[176:177], v246 offset:36864\n"
    "ds_read_b64_tr_b16 a[178:179], v246 offset:38912\n"
    "v_mfma_f32_16x16x32_bf16 v[36:39], a[188:191], v[172:175], v[36:39]\n"
    "buffer_store_dwordx4 v[80:83], v253, s[8:11], s84 offen offset:1024 sc1\n"
    "v_exp_f32_e32 v213, v213\n"
    "v_mfma_f32_16x16x32_bf16 v[12:15], a[188:191], v[164:167], v[12:15]\n"
    "ds_read_b64_tr_b16 a[180:181], v246 offset:45056\n"
    "ds_read_b64_tr_b16 a[182:183], v246 offset:47104\n"
    "v_mfma_f32_16x16x32_bf16 v[12:15], a[184:187], v[160:163], v[12:15]\n"
    "s_waitcnt lgkmcnt(4)\n"
    "v_exp_f32_e32 v214, v214\n"
    "v_mfma_f32_16x16x32_bf16 v[16:19], a[160:163], v[160:163], v[16:19]\n"
    "ds_read_b64_tr_b16 a[184:185], v247 offset:36864\n"
    "ds_read_b64_tr_b16 a[186:187], v247 offset:38912\n"
    "v_mfma_f32_16x16x32_bf16 v[16:19], a[164:167], v[164:167], v[16:19]\n"
    "v_exp_f32_e32 v215, v215\n"
    "v_mfma_f32_16x16x32_bf16 v[40:43], a[164:167], v[172:175], v[40:43]\n"
    "ds_read_b64_tr_b16 a[188:189], v247 offset:45056\n"
    "ds_read_b64_tr_b16 a[190:191], v247 offset:47104\n"
    "v_mfma_f32_16x16x32_bf16 v[40:43], a[160:163], v[168:171], v[40:43]\n"
    "ds_write_addtid_b32 v208 offset:4096\n"
    "buffer_store_dwordx4 v[112:115], v253, s[8:11], s86 offen offset:2048 sc1\n"
    "v_cvt_pk_bf16_f32 v210, v212, v213\n"
    "v_cvt_pk_bf16_f32 v109, v110, v111\n"
    "v_mfma_f32_16x16x32_bf16 v[44:47], a[168:171], v[168:171], v[44:47]\n"
    "ds_read_b128 a[160:163], v243 offset:4096\n"
    "ds_write_addtid_b32 v209 offset:4352\n"
    "v_mfma_f32_16x16x32_bf16 v[44:47], a[172:175], v[172:175], v[44:47]\n"
    "ds_read_b128 a[164:167], v240 offset:0\n"
    "ds_write_addtid_b32 v200 offset:0\n"
    "v_mfma_f32_16x16x32_bf16 v[20:23], a[172:175], v[164:167], v[20:23]\n"
    "v_exp_f32_e32 v204, v204\n"
    "v_mfma_f32_16x16x32_bf16 v[20:23], a[168:171], v[160:163], v[20:23]\n"
    "s_waitcnt lgkmcnt(5)\n"
    "ds_read_b128 a[168:171], v241 offset:0\n"
    "v_cvt_pk_bf16_f32 v110, v120, v121\n"
    "v_mfma_f32_16x16x32_bf16 v[24:27], a[176:179], v[160:163], v[24:27]\n"
    "ds_read_b128 a[172:175], v240 offset:4096\n"
    "v_cvt_pk_bf16_f32 v111, v122, v123\n"
    "v_mfma_f32_16x16x32_bf16 v[24:27], a[180:183], v[164:167], v[24:27]\n"
    "buffer_store_dwordx4 v[108:111], v253, s[8:11], s85 offen offset:2048 sc1\n"
    "v_exp_f32_e32 v205, v205\n"
    "v_mfma_f32_16x16x32_bf16 v[48:51], a[180:183], v[172:175], v[48:51]\n"
    "ds_write_addtid_b32 v201 offset:256\n"
    "v_cvt_pk_bf16_f32 v211, v214, v215\n"
    "v_mfma_f32_16x16x32_bf16 v[48:51], a[176:179], v[168:171], v[48:51]\n"
    "ds_read_b128 a[176:179], v241 offset:4096\n"
    "v_cndmask_b32_e32 v254, v254, v255, vcc\n"
    "v_mfma_f32_16x16x32_bf16 v[52:55], a[184:187], v[168:171], v[52:55]\n"
    "ds_read_b128 a[180:183], v242 offset:0\n"
    "v_cmp_le_u32_e32 vcc, 0x200, v253\n"
    "v_mov_b32_e32 v255, s30\n"
    "v_mfma_f32_16x16x32_bf16 v[52:55], a[188:191], v[172:175], v[52:55]\n"
    "v_exp_f32_e32 v206, v206\n"
    "v_mfma_f32_16x16x32_bf16 v[28:31], a[188:191], v[164:167], v[28:31]\n"
    "v_exp_f32_e32 v207, v207\n"
    "v_mfma_f32_16x16x32_bf16 v[28:31], a[184:187], v[160:163], v[28:31]\n"
    "s_waitcnt lgkmcnt(3)\n"
    "ds_read_b128 a[184:187], v243 offset:0\n"
    "v_cvt_pk_bf16_f32 v202, v204, v205\n"
    "v_cndmask_b32_e32 v254, v254, v255, vcc\n"
    "v_mfma_f32_16x16x32_bf16 v[160:163], a[160:163], a[28:31], 0\n"
    "ds_read_b128 a[188:191], v242 offset:4096\n"
    "v_cmp_le_u32_e32 vcc, 0x300, v253\n"
    "v_mov_b32_e32 v255, s31\n"
    "v_mfma_f32_16x16x32_bf16 v[160:163], a[164:167], a[0:3], v[160:163]\n"
    "ds_write_addtid_b32 v210 offset:4608\n"
    "v_cvt_pk_bf16_f32 v203, v206, v207\n"
    "v_cndmask_b32_e32 v254, v254, v255, vcc\n"
    "v_mfma_f32_16x16x32_bf16 v[168:171], a[164:167], a[32:35], 0\n"
    "s_cmp_eq_u32 s51, 0\n"
    "s_cselect_b32 s60, 2.0, 24576\n"
    "ds_write_addtid_b32 v211 offset:4864\n"
    "v_add_u32_e32 v251, v251, v254\n"
    "v_mfma_f32_16x16x32_bf16 v[168:171], a[160:163], a[60:63], v[168:171]\n"
    "s_lshl_b32 s45, s51, 16\n"
    "s_add_u32 s45, s45, %3\n"
    "s_add_u32 s45, s45, 73728\n"
    "ds_read_b128 a[160:163], v243 offset:6144\n"
    "v_mfma_f32_16x16x32_bf16 v[168:171], a[168:171], a[36:39], v[168:171]\n"
    "s_lshl_b32 s46, s89, 12\n"
    "s_add_u32 s46, s46, s45\n"
    "ds_read_b128 a[164:167], v240 offset:2048\n"
    "v_add_u32_e32 v159, s46, v253\n"
    "v_mfma_f32_16x16x32_bf16 v[168:171], a[172:175], a[40:43], v[168:171]\n"
    "ds_write_addtid_b32 v202 offset:512\n"
    "v_cvt_pk_bf16_f32 v104, v104, v105\n"
    "v_mfma_f32_16x16x32_bf16 v[160:163], a[172:175], a[8:11], v[160:163]\n"
    "ds_write_addtid_b32 v203 offset:768\n"
    "v_cvt_pk_bf16_f32 v105, v106, v107\n"
    "v_mfma_f32_16x16x32_bf16 v[160:163], a[168:171], a[4:7], v[160:163]\n"
    "s_waitcnt lgkmcnt(6)\n"
    "ds_read_b128 a[168:171], v241 offset:2048\n"
    "v_cvt_pk_bf16_f32 v106, v116, v117\n"
    "v_mfma_f32_16x16x32_bf16 v[160:163], a[176:179], a[12:15], v[160:163]\n"
    "ds_read_b128 a[172:175], v240 offset:6144\n"
    "v_cvt_pk_bf16_f32 v107, v118, v119\n"
    "v_mfma_f32_16x16x32_bf16 v[160:163], a[180:183], a[16:19], v[160:163]\n"
    "v_cvt_pk_bf16_f32 v136, v136, v137\n"
    "v_cvt_pk_bf16_f32 v137, v138, v139\n"
    "v_mfma_f32_16x16x32_bf16 v[168:171], a[180:183], a[48:51], v[168:171]\n"
    "s_waitcnt lgkmcnt(2)\n"
    "s_barrier\n"
    "v_mfma_f32_16x16x32_bf16 v[168:171], a[176:179], a[44:47], v[168:171]\n"
    "s_add_u32 m0, s43, 32768\n"
    "ds_read_b128 a[176:179], v241 offset:6144\n"
    "v_cvt_pk_bf16_f32 v138, v148, v149\n"
    "v_mfma_f32_16x16x32_bf16 v[168:171], a[184:187], a[52:55], v[168:171]\n"
    "ds_read_b128 a[180:183], v242 offset:2048\n"
    "buffer_load_dwordx4 v248, s[8:11], s40 offen offset:0 sc1 lds\n"
    "v_mfma_f32_16x16x32_bf16 v[168:171], a[188:191], a[56:59], v[168:171]\n"
    "ds_read2st64_b32 v[204:205], v250 offset0:0 offset1:1\n"
    "v_cvt_pk_bf16_f32 v139, v150, v151\n"
    "v_mfma_f32_16x16x32_bf16 v[160:163], a[188:191], a[24:27], v[160:163]\n"
    "v_cvt_pk_bf16_f32 v132, v132, v133\n"
    "v_cvt_pk_bf16_f32 v133, v134, v135\n"
    "v_mfma_f32_16x16x32_bf16 v[160:163], a[184:187], a[20:23], v[160:163]\n"
    "s_waitcnt lgkmcnt(3)\n"
    "ds_read_b128 a[184:187], v243 offset:2048\n"
    "buffer_load_dwordx4 v248, s[8:11], s40 offen offset:1024 sc1 lds\n"
    "v_mfma_f32_16x16x32_bf16 v[164:167], a[160:163], a[28:31], 0\n"
    "ds_read_b128 a[188:191], v242 offset:6144\n"
    "v_exp_f32_e32 v168, v168\n"
    "v_mfma_f32_16x16x32_bf16 v[164:167], a[164:167], a[0:3], v[164:167]\n"
    "ds_read2st64_b32 v[206:207], v250 offset0:2 offset1:3\n"
    "v_cvt_pk_bf16_f32 v134, v144, v145\n"
    "v_mfma_f32_16x16x32_bf16 v[172:175], a[164:167], a[32:35], 0\n"
    "buffer_load_dwordx4 v248, s[8:11], s40 offen offset:2048 sc1 lds\n"
    "v_exp_f32_e32 v169, v169\n"
    "v_mfma_f32_16x16x32_bf16 v[172:175], a[160:163], a[60:63], v[172:175]\n"
    "ds_read_b64_tr_b16 a[160:161], v244 offset:49152\n"
    "ds_read_b64_tr_b16 a[162:163], v244 offset:51200\n"
    "v_mfma_f32_16x16x32_bf16 v[172:175], a[168:171], a[36:39], v[172:175]\n"
    "ds_read2st64_b32 v[212:213], v250 offset0:16 offset1:17\n"
    "v_cvt_pk_bf16_f32 v135, v146, v147\n"
    "v_mfma_f32_16x16x32_bf16 v[172:175], a[172:175], a[40:43], v[172:175]\n"
    "ds_read_b64_tr_b16 a[164:165], v244 offset:57344\n"
    "ds_read_b64_tr_b16 a[166:167], v244 offset:59392\n"
    "buffer_load_dwordx4 v248, s[8:11], s40 offen offset:3072 sc1 lds\n"
    "v_mfma_f32_16x16x32_bf16 v[164:167], a[172:175], a[8:11], v[164:167]\n"
    "s_add_u32 s40, s40, 32768\n"
    "ds_read2st64_b32 v[214:215], v250 offset0:18 offset1:19\n"
    "v_cvt_pk_bf16_f32 v128, v128, v129\n"
    "v_mfma_f32_16x16x32_bf16 v[164:167], a[168:171], a[4:7], v[164:167]\n"
    "s_waitcnt lgkmcnt(7)\n"
    "s_add_u32 m0, s43, 36864\n"
    "ds_read_b64_tr_b16 a[168:169], v245 offset:49152\n"
    "ds_read_b64_tr_b16 a[170:171], v245 offset:51200\n"
    "v_mfma_f32_16x16x32_bf16 v[164:167], a[176:179], a[12:15], v[164:167]\n"
    "buffer_load_dwordx4 v248, s[8:11], s41 offen offset:0 sc1 lds\n"
    "v_cvt_pk_bf16_f32 v129, v130, v131\n"
    "v_mfma_f32_16x16x32_bf16 v[164:167], a[180:183], a[16:19], v[164:167]\n"
    "ds_read_b64_tr_b16 a[172:173], v245 offset:57344\n"
    "ds_read_b64_tr_b16 a[174:175], v245 offset:59392\n"
    "v_mfma_f32_16x16x32_bf16 v[172:175], a[180:183], a[48:51], v[172:175]\n"
    "v_exp_f32_e32 v170, v170\n"
    "v_mfma_f32_16x16x32_bf16 v[172:175], a[176:179], a[44:47], v[172:175]\n"
    "ds_read_b64_tr_b16 a[176:177], v246 offset:49152\n"
    "ds_read_b64_tr_b16 a[178:179], v246 offset:51200\n"
    "buffer_load_dwordx4 v248, s[8:11], s41 offen offset:1024 sc1 lds\n"
    "v_mfma_f32_16x16x32_bf16 v[172:175], a[184:187], a[52:55], v[172:175]\n"
    "v_exp_f32_e32 v171, v171\n"
    "v_mfma_f32_16x16x32_bf16 v[172:175], a[188:191], a[56:59], v[172:175]\n"
    "ds_read_b64_tr_b16 a[180:181], v246 offset:57344\n"
    "ds_read_b64_tr_b16 a[182:183], v246 offset:59392\n"
    "v_mfma_f32_16x16x32_bf16 v[164:167], a[188:191], a[24:27], v[164:167]\n"
    "buffer_load_dwordx4 v248, s[8:11], s41 offen offset:2048 sc1 lds\n"
    "v_cvt_pk_bf16_f32 v168, v168, v169\n"
    "v_mfma_f32_16x16x32_bf16 v[164:167], a[184:187], a[20:23], v[164:167]\n"
    "s_waitcnt lgkmcnt(4)\n"
    "ds_read_b64_tr_b16 a[184:185], v247 offset:49152\n"
    "ds_read_b64_tr_b16 a[186:187], v247 offset:51200\n"
    "v_mfma_f32_16x16x32_bf16 v[0:3], a[160:163], v[200:203], v[0:3]\n"
    "v_exp_f32_e32 v160, v160\n"
    "v_mfma_f32_16x16x32_bf16 v[0:3], a[164:167], v[204:207], v[0:3]\n"
    "ds_read_b64_tr_b16 a[188:189], v247 offset:57344\n"
    "ds_read_b64_tr_b16 a[190:191], v247 offset:59392\n"
    "buffer_load_dwordx4 v248, s[8:11], s41 offen offset:3072 sc1 lds\n"
    "v_mfma_f32_16x16x32_bf16 a[248:251], a[164:167], v[212:215], a[248:251]\n"
    "s_add_u32 s41, s41, 32768\n"
    "v_exp_f32_e32 v161, v161\n"
    "v_mfma_f32_16x16x32_bf16 a[248:251], a[160:163], v[208:211], a[248:251]\n"
    "s_mov_b32 m0, s49\n"
    "ds_read_b64_tr_b16 a[160:161], v244 offset:53248\n"
    "ds_read_b64_tr_b16 a[162:163], v244 offset:55296\n"
    "v_mfma_f32_16x16x32_bf16 a[252:255], a[168:171], v[208:211], a[252:255]\n"
    "v_cvt_pk_bf16_f32 v169, v170, v171\n"
    "v_cvt_pk_bf16_f32 v130, v140, v141\n"
    "v_mfma_f32_16x16x32_bf16 a[252:255], a[172:175], v[212:215], a[252:255]\n"
    "s_barrier\n"
    "v_mfma_f32_16x16x32_bf16 v[4:7], a[172:175], v[204:207], v[4:7]\n"
    "ds_read_b64_tr_b16 a[164:165], v244 offset:61440\n"
    "ds_read_b64_tr_b16 a[166:167], v244 offset:63488\n"
    "v_mfma_f32_16x16x32_bf16 v[4:7], a[168:171], v[200:203], v[4:7]\n"
    "s_waitcnt lgkmcnt(4)\n"
    "v_exp_f32_e32 v162, v162\n"
    "v_mfma_f32_16x16x32_bf16 v[8:11], a[176:179], v[200:203], v[8:11]\n"
    "s_add_u32 s45, s45, 16384\n"
    "ds_read_b64_tr_b16 a[168:169], v245 offset:53248\n"
    "ds_read_b64_tr_b16 a[170:171], v245 offset:55296\n"
    "v_lshrrev_b32_e32 v255, 2, v253\n"
    "v_mfma_f32_16x16x32_bf16 v[8:11], a[180:183], v[204:207], v[8:11]\n"
    "buffer_store_dwordx4 v[104:107], v253, s[8:11], s84 offen offset:2048 sc1\n"
    "v_exp_f32_e32 v163, v163\n"
    "v_mfma_f32_16x16x32_bf16 v[32:35], a[180:183], v[212:215], v[32:35]\n"
    "ds_read_b64_tr_b16 a[172:173], v245 offset:61440\n"
    "ds_read_b64_tr_b16 a[174:175], v245 offset:63488\n"
    "v_mfma_f32_16x16x32_bf16 v[32:35], a[176:179], v[208:211], v[32:35]\n"
    "v_cvt_pk_bf16_f32 v160, v160, v161\n"
    "v_cvt_pk_bf16_f32 v161, v162, v163\n"
    "v_mfma_f32_16x16x32_bf16 v[36:39], a[184:187], v[208:211], v[36:39]\n"
    "ds_read_b64_tr_b16 a[176:177], v246 offset:53248\n"
    "ds_read_b64_tr_b16 a[178:179], v246 offset:55296\n"
    "v_mfma_f32_16x16x32_bf16 v[36:39], a[188:191], v[212:215], v[36:39]\n"
    "buffer_store_dwordx4 v[136:139], v253, s[8:11], s86 offen offset:3072 sc1\n"
    "v_exp_f32_e32 v172, v172\n"
    "v_add_u32_e32 v255, s45, v255\n"
    "v_mfma_f32_16x16x32_bf16 v[12:15], a[188:191], v[204:207], v[12:15]\n"
    "ds_read_b64_tr_b16 a[180:181], v246 offset:61440\n"
    "ds_read_b64_tr_b16 a[182:183], v246 offset:63488\n"
    "v_mfma_f32_16x16x32_bf16 v[12:15], a[184:187], v[200:203], v[12:15]\n"
    "s_waitcnt lgkmcnt(4)\n"
    "v_exp_f32_e32 v173, v173\n"
    "v_mfma_f32_16x16x32_bf16 v[16:19], a[160:163], v[200:203], v[16:19]\n"
    "ds_read_b64_tr_b16 a[184:185], v247 offset:53248\n"
    "ds_read_b64_tr_b16 a[186:187], v247 offset:55296\n"
    "v_mfma_f32_16x16x32_bf16 v[16:19], a[164:167], v[204:207], v[16:19]\n"
    "v_exp_f32_e32 v174, v174\n"
    "v_mfma_f32_16x16x32_bf16 v[40:43], a[164:167], v[212:215], v[40:43]\n"
    "s_lshl_b32 s45, s89, 8\n"
    "ds_read_b64_tr_b16 a[188:189], v247 offset:61440\n"
    "ds_read_b64_tr_b16 a[190:191], v247 offset:63488\n"
    "v_add_u32_e32 v254, s45, v255\n"
    "v_mfma_f32_16x16x32_bf16 v[40:43], a[160:163], v[208:211], v[40:43]\n"
    "buffer_store_dwordx4 v[132:135], v253, s[8:11], s85 offen offset:3072 sc1\n"
    "v_exp_f32_e32 v175, v175\n"
    "v_mfma_f32_16x16x32_bf16 v[44:47], a[168:171], v[208:211], v[44:47]\n"
    "ds_read_b128 a[160:163], v243 offset:20480\n"
    "v_cvt_pk_bf16_f32 v170, v172, v173\n"
    "v_mfma_f32_16x16x32_bf16 v[44:47], a[172:175], v[212:215], v[44:47]\n"
    "ds_read_b128 a[164:167], v240 offset:16384\n"
    "ds_write_addtid_b32 v168 offset:4096\n"
    "v_mfma_f32_16x16x32_bf16 v[20:23], a[172:175], v[204:207], v[20:23]\n"
    "s_lshl_b32 s45, s50, 8\n"
    "v_exp_f32_e32 v164, v164\n"
    "v_add_u32_e32 v255, s45, v255\n"
    "v_mfma_f32_16x16x32_bf16 v[20:23], a[168:171], v[200:203], v[20:23]\n"
    "s_waitcnt lgkmcnt(3)\n"
    "ds_read_b128 a[168:171], v241 offset:16384\n"
    "ds_write_addtid_b32 v169 offset:4352\n"
    "v_mfma_f32_16x16x32_bf16 v[24:27], a[176:179], v[200:203], v[24:27]\n"
    "ds_read_b128 a[172:175], v240 offset:20480\n"
    "v_cvt_pk_bf16_f32 v131, v142, v143\n"
    "v_mfma_f32_16x16x32_bf16 v[24:27], a[180:183], v[204:207], v[24:27]\n"
    "buffer_store_dwordx4 v[128:131], v253, s[8:11], s84 offen offset:3072 sc1\n"
    "v_exp_f32_e32 v165, v165\n"
    "v_mfma_f32_16x16x32_bf16 v[48:51], a[180:183], v[212:215], v[48:51]\n"
    "ds_write_addtid_b32 v160 offset:0\n"
    "v_cvt_pk_bf16_f32 v171, v174, v175\n"
    "v_permlane16_swap_b32_e32 v147, v151\n"
    "v_mfma_f32_16x16x32_bf16 v[48:51], a[176:179], v[208:211], v[48:51]\n"
    "ds_read_b128 a[176:179], v241 offset:20480\n"
    "v_mov_b32_e32 v148, s39\n"
    "v_mfma_f32_16x16x32_bf16 v[52:55], a[184:187], v[208:211], v[52:55]\n"
    "ds_read_b128 a[180:183], v242 offset:16384\n"
    "ds_write_addtid_b32 v161 offset:256\n"
    "v_mfma_f32_16x16x32_bf16 v[52:55], a[188:191], v[212:215], v[52:55]\n"
    "v_exp_f32_e32 v166, v166\n"
    "v_mov_b32_e32 v149, 0\n"
    "v_mfma_f32_16x16x32_bf16 v[28:31], a[188:191], v[204:207], v[28:31]\n"
    "v_exp_f32_e32 v167, v167\n"
    "v_permlane32_swap_b32_e32 v143, v151\n"
    "v_mfma_f32_16x16x32_bf16 v[28:31], a[184:187], v[200:203], v[28:31]\n"
    "s_waitcnt lgkmcnt(4)\n"
    "ds_read_b128 a[184:187], v243 offset:16384\n"
    "v_cvt_pk_bf16_f32 v162, v164, v165\n"
    "v_mfma_f32_16x16x32_bf16 v[200:203], a[160:163], a[28:31], 0\n"
    "ds_read_b128 a[188:191], v242 offset:20480\n"
    "v_mfma_f32_16x16x32_bf16 v[200:203], a[164:167], a[0:3], v[200:203]\n"
    "ds_write_addtid_b32 v170 offset:4608\n"
    "buffer_store_dword v151, v251, s[8:11], s60 offen offset:0 sc1\n"
    "v_cvt_pk_bf16_f32 v163, v166, v167\n"
    "v_mfma_f32_16x16x32_bf16 v[208:211], a[164:167], a[32:35], 0\n"
    "ds_write_addtid_b32 v171 offset:4864\n"
    "v_mfma_f32_16x16x32_bf16 v[208:211], a[160:163], a[60:63], v[208:211]\n"
    "ds_read_b128 a[160:163], v243 offset:22528\n"
    "v_lshrrev_b32_e32 v147, 4, v253\n"
    "v_mfma_f32_16x16x32_bf16 v[208:211], a[168:171], a[36:39], v[208:211]\n"
    "ds_read_b128 a[164:167], v240 offset:18432\n"
    "v_mfma_f32_16x16x32_bf16 v[208:211], a[172:175], a[40:43], v[208:211]\n"
    "ds_write_addtid_b32 v162 offset:512\n"
    "v_mfma_f32_16x16x32_bf16 v[200:203], a[172:175], a[8:11], v[200:203]\n"
    "ds_write_addtid_b32 v163 offset:768\n"
    "v_bfe_u32 v145, v147, 2, 2\n"
    "v_mfma_f32_16x16x32_bf16 v[200:203], a[168:171], a[4:7], v[200:203]\n"
    "s_waitcnt lgkmcnt(6)\n"
    "ds_read_b128 a[168:171], v241 offset:18432\n"
    "v_mfma_f32_16x16x32_bf16 v[200:203], a[176:179], a[12:15], v[200:203]\n"
    "ds_read_b128 a[172:175], v240 offset:22528\n"
    "v_mfma_f32_16x16x32_bf16 v[200:203], a[180:183], a[16:19], v[200:203]\n"
    "v_min_u32_e32 v145, 2, v145\n"
    "v_mfma_f32_16x16x32_bf16 v[208:211], a[180:183], a[48:51], v[208:211]\n"
    "s_waitcnt lgkmcnt(2)\n"
    "s_barrier\n"
    "v_mfma_f32_16x16x32_bf16 v[208:211], a[176:179], a[44:47], v[208:211]\n"
    "s_add_u32 s46, s32, 1\n"
    "ds_read_b128 a[176:179], v241 offset:22528\n"
    "v_mfma_f32_16x16x32_bf16 v[208:211], a[184:187], a[52:55], v[208:211]\n"
    "ds_read_b128 a[180:183], v242 offset:18432\n"
    "v_mfma_f32_16x16x32_bf16 v[208:211], a[188:191], a[56:59], v[208:211]\n"
    "ds_read2st64_b32 v[164:165], v250 offset0:0 offset1:1\n"
    "v_mfma_f32_16x16x32_bf16 v[200:203], a[188:191], a[24:27], v[200:203]\n"
    "v_add_u32_e32 v145, s46, v145\n"
    "v_mfma_f32_16x16x32_bf16 v[200:203], a[184:187], a[20:23], v[200:203]\n"
    "s_waitcnt lgkmcnt(3)\n"
    "ds_read_b128 a[184:187], v243 offset:18432\n"
    "v_and_b32_e32 v145, 3, v145\n"
    "v_mfma_f32_16x16x32_bf16 v[204:207], a[160:163], a[28:31], 0\n"
    "ds_read_b128 a[188:191], v242 offset:22528\n"
    "v_lshlrev_b32_e32 v145, 6, v145\n"
    "v_mfma_f32_16x16x32_bf16 v[204:207], a[164:167], a[0:3], v[204:207]\n"
    "ds_read2st64_b32 v[166:167], v250 offset0:2 offset1:3\n"
    "v_and_or_b32 v145, v147, 3, v145\n"
    "v_mfma_f32_16x16x32_bf16 v[212:215], a[164:167], a[32:35], 0\n"
    "v_exp_f32_e32 v208, v208\n"
    "v_mfma_f32_16x16x32_bf16 v[212:215], a[160:163], a[60:63], v[212:215]\n"
    "ds_read_b64_tr_b16 a[160:161], v244 offset:0\n"
    "ds_read_b64_tr_b16 a[162:163], v244 offset:2048\n"
    "v_mfma_f32_16x16x32_bf16 v[212:215], a[168:171], a[36:39], v[212:215]\n"
    "ds_read2st64_b32 v[172:173], v250 offset0:16 offset1:17\n"
    "v_lshlrev_b32_e32 v145, 2, v145\n"
    "v_mfma_f32_16x16x32_bf16 v[212:215], a[172:175], a[40:43], v[212:215]\n"
    "ds_read_b64_tr_b16 a[164:165], v244 offset:8192\n"
    "ds_read_b64_tr_b16 a[166:167], v244 offset:10240\n"
    "v_mfma_f32_16x16x32_bf16 v[204:207], a[172:175], a[8:11], v[204:207]\n"
    "s_lshl_b32 s47, s33, 10\n"
    "v_exp_f32_e32 v209, v209\n"
    "v_mfma_f32_16x16x32_bf16 v[204:207], a[168:171], a[4:7], v[204:207]\n"
    "s_waitcnt lgkmcnt(6)\n"
    "ds_read_b64_tr_b16 a[168:169], v245 offset:0\n"
    "ds_read_b64_tr_b16 a[170:171], v245 offset:2048\n"
    "v_mfma_f32_16x16x32_bf16 v[204:207], a[176:179], a[12:15], v[204:207]\n"
    "s_add_u32 s47, s47, 5636096\n"
    "ds_read2st64_b32 v[174:175], v250 offset0:18 offset1:19\n"
    "v_cmp_gt_u32_e32 vcc, 0x300, v253\n"
    "v_mfma_f32_16x16x32_bf16 v[204:207], a[180:183], a[16:19], v[204:207]\n"
    "s_add_u32 s48, s56, 0\n"
    "ds_read_b64_tr_b16 a[172:173], v245 offset:8192\n"
    "ds_read_b64_tr_b16 a[174:175], v245 offset:10240\n"
    "v_mfma_f32_16x16x32_bf16 v[212:215], a[180:183], a[48:51], v[212:215]\n"
    "v_exp_f32_e32 v210, v210\n"
    "v_mfma_f32_16x16x32_bf16 v[212:215], a[176:179], a[44:47], v[212:215]\n"
    "ds_read_b64_tr_b16 a[176:177], v246 offset:0\n"
    "ds_read_b64_tr_b16 a[178:179], v246 offset:2048\n"
    "v_mfma_f32_16x16x32_bf16 v[212:215], a[184:187], a[52:55], v[212:215]\n"
    "buffer_load_dwordx4 v[56:59], v253, s[8:11], s48 offen offset:0 sc1\n"
    "v_exp_f32_e32 v211, v211\n"
    "v_mfma_f32_16x16x32_bf16 v[212:215], a[188:191], a[56:59], v[212:215]\n"
    "ds_read_b64_tr_b16 a[180:181], v246 offset:8192\n"
    "ds_read_b64_tr_b16 a[182:183], v246 offset:10240\n"
    "v_mfma_f32_16x16x32_bf16 v[204:207], a[188:191], a[24:27], v[204:207]\n"
    "s_add_u32 s60, s60, s28\n"
    "v_cvt_pk_bf16_f32 v208, v208, v209\n"
    "v_and_b32_e32 v251, 0xf0, v253\n"
    "v_mfma_f32_16x16x32_bf16 v[204:207], a[184:187], a[20:23], v[204:207]\n"
    "s_waitcnt lgkmcnt(4)\n"
    "ds_read_b64_tr_b16 a[184:185], v247 offset:0\n"
    "ds_read_b64_tr_b16 a[186:187], v247 offset:2048\n"
    "v_mfma_f32_16x16x32_bf16 v[0:3], a[160:163], v[160:163], v[0:3]\n"
    "buffer_load_dwordx4 v[60:63], v253, s[8:11], s48 offen offset:1024 sc1\n"
    "v_exp_f32_e32 v200, v200\n"
    "v_mfma_f32_16x16x32_bf16 v[0:3], a[164:167], v[164:167], v[0:3]\n"
    "ds_read_b64_tr_b16 a[188:189], v247 offset:8192\n"
    "ds_read_b64_tr_b16 a[190:191], v247 offset:10240\n"
    "v_mfma_f32_16x16x32_bf16 a[248:251], a[164:167], v[172:175], a[248:251]\n"
    "v_exp_f32_e32 v201, v201\n"
    "v_mfma_f32_16x16x32_bf16 a[248:251], a[160:163], v[168:171], a[248:251]\n"
    "ds_read_b64_tr_b16 a[160:161], v244 offset:4096\n"
    "ds_read_b64_tr_b16 a[162:163], v244 offset:6144\n"
    "v_mfma_f32_16x16x32_bf16 a[252:255], a[168:171], v[168:171], a[252:255]\n"
    "buffer_load_dwordx4 v[64:67], v253, s[8:11], s48 offen offset:2048 sc1\n"
    "v_cvt_pk_bf16_f32 v209, v210, v211\n"
    "v_cndmask_b32_e64 v251, v251, 2.0, vcc\n"
    "v_mfma_f32_16x16x32_bf16 a[252:255], a[172:175], v[172:175], a[252:255]\n"
    "s_waitcnt vmcnt(8)\n"
    "s_barrier\n"
    "v_mfma_f32_16x16x32_bf16 v[4:7], a[172:175], v[164:167], v[4:7]\n"
    "ds_read_b64_tr_b16 a[164:165], v244 offset:12288\n"
    "ds_read_b64_tr_b16 a[166:167], v244 offset:14336\n"
    "v_mfma_f32_16x16x32_bf16 v[4:7], a[168:171], v[160:163], v[4:7]\n"
    "s_waitcnt lgkmcnt(4)\n"
    "v_exp_f32_e32 v202, v202\n"
    "v_mfma_f32_16x16x32_bf16 v[8:11], a[176:179], v[160:163], v[8:11]\n"
    "ds_read_b64_tr_b16 a[168:169], v245 offset:4096\n"
    "ds_read_b64_tr_b16 a[170:171], v245 offset:6144\n"
    "buffer_load_dwordx4 v[68:71], v253, s[8:11], s48 offen offset:3072 sc1\n"
    "v_mfma_f32_16x16x32_bf16 v[8:11], a[180:183], v[164:167], v[8:11]\n"
    "s_add_u32 s48, s56, 4096\n"
    "v_exp_f32_e32 v203, v203\n"
    "v_mfma_f32_16x16x32_bf16 v[32:35], a[180:183], v[172:175], v[32:35]\n"
    "ds_read_b64_tr_b16 a[172:173], v245 offset:12288\n"
    "ds_read_b64_tr_b16 a[174:175], v245 offset:14336\n"
    "v_mfma_f32_16x16x32_bf16 v[32:35], a[176:179], v[168:171], v[32:35]\n"
    "v_cvt_pk_bf16_f32 v200, v200, v201\n"
    "v_cvt_pk_bf16_f32 v201, v202, v203\n"
    "v_mfma_f32_16x16x32_bf16 v[36:39], a[184:187], v[168:171], v[36:39]\n"
    "ds_read_b64_tr_b16 a[176:177], v246 offset:4096\n"
    "ds_read_b64_tr_b16 a[178:179], v246 offset:6144\n"
    "buffer_load_dwordx4 v[72:75], v253, s[8:11], s48 offen offset:0 sc1\n"
    "v_mfma_f32_16x16x32_bf16 v[36:39], a[188:191], v[172:175], v[36:39]\n"
    "v_exp_f32_e32 v212, v212\n"
    "v_mfma_f32_16x16x32_bf16 v[12:15], a[188:191], v[164:167], v[12:15]\n"
    "ds_read_b64_tr_b16 a[180:181], v246 offset:12288\n"
    "ds_read_b64_tr_b16 a[182:183], v246 offset:14336\n"
    "v_mfma_f32_16x16x32_bf16 v[12:15], a[184:187], v[160:163], v[12:15]\n"
    "s_waitcnt lgkmcnt(4)\n"
    "v_exp_f32_e32 v213, v213\n"
    "v_mfma_f32_16x16x32_bf16 v[16:19], a[160:163], v[160:163], v[16:19]\n"
    "ds_read_b64_tr_b16 a[184:185], v247 offset:4096\n"
    "ds_read_b64_tr_b16 a[186:187], v247 offset:6144\n"
    "buffer_load_dwordx4 v[76:79], v253, s[8:11], s48 offen offset:1024 sc1\n"
    "v_mfma_f32_16x16x32_bf16 v[16:19], a[164:167], v[164:167], v[16:19]\n"
    "v_exp_f32_e32 v214, v214\n"
    "v_mfma_f32_16x16x32_bf16 v[40:43], a[164:167], v[172:175], v[40:43]\n"
    "ds_read_b64_tr_b16 a[188:189], v247 offset:12288\n"
    "ds_read_b64_tr_b16 a[190:191], v247 offset:14336\n"
    "v_mfma_f32_16x16x32_bf16 v[40:43], a[160:163], v[168:171], v[40:43]\n"
    "v_exp_f32_e32 v215, v215\n"
    "v_mfma_f32_16x16x32_bf16 v[44:47], a[168:171], v[168:171], v[44:47]\n"
    "ds_read_b128 a[160:163], v243 offset:36864\n"
    "buffer_load_dwordx4 v[80:83], v253, s[8:11], s48 offen offset:2048 sc1\n"
    "v_cvt_pk_bf16_f32 v210, v212, v213\n"
    "v_mfma_f32_16x16x32_bf16 v[44:47], a[172:175], v[172:175], v[44:47]\n"
    "ds_read_b128 a[164:167], v240 offset:32768\n"
    "ds_write_addtid_b32 v208 offset:4096\n"
    "v_mfma_f32_16x16x32_bf16 v[20:23], a[172:175], v[164:167], v[20:23]\n"
    "v_exp_f32_e32 v204, v204\n"
    "v_mfma_f32_16x16x32_bf16 v[20:23], a[168:171], v[160:163], v[20:23]\n"
    "s_waitcnt lgkmcnt(3)\n"
    "ds_read_b128 a[168:171], v241 offset:32768\n"
    "ds_write_addtid_b32 v209 offset:4352\n"
    "v_mfma_f32_16x16x32_bf16 v[24:27], a[176:179], v[160:163], v[24:27]\n"
    "ds_read_b128 a[172:175], v240 offset:36864\n"
    "ds_write_addtid_b32 v200 offset:0\n"
    "buffer_load_dwordx4 v[84:87], v253, s[8:11], s48 offen offset:3072 sc1\n"
    "v_mfma_f32_16x16x32_bf16 v[24:27], a[180:183], v[164:167], v[24:27]\n"
    "s_add_u32 s48, s56, 8192\n"
    "v_exp_f32_e32 v205, v205\n"
    "v_mfma_f32_16x16x32_bf16 v[48:51], a[180:183], v[172:175], v[48:51]\n"
    "s_waitcnt vmcnt(8)\n"
    "ds_write_addtid_b32 v201 offset:256\n"
    "buffer_store_dword v148, v149, s[8:11], s87 offen offset:128 sc1\n"
    "v_cvt_pk_bf16_f32 v211, v214, v215\n"
    "v_mfma_f32_16x16x32_bf16 v[48:51], a[176:179], v[168:171], v[48:51]\n"
    "ds_read_b128 a[176:179], v241 offset:36864\n"
    "v_mfma_f32_16x16x32_bf16 v[52:55], a[184:187], v[168:171], v[52:55]\n"
    "ds_read_b128 a[180:183], v242 offset:32768\n"
    "buffer_load_dwordx4 v[88:91], v253, s[8:11], s48 offen offset:0 sc1\n"
    "v_mfma_f32_16x16x32_bf16 v[52:55], a[188:191], v[172:175], v[52:55]\n"
    "v_exp_f32_e32 v206, v206\n"
    "v_mfma_f32_16x16x32_bf16 v[28:31], a[188:191], v[164:167], v[28:31]\n"
    "v_exp_f32_e32 v207, v207\n"
    "v_mfma_f32_16x16x32_bf16 v[28:31], a[184:187], v[160:163], v[28:31]\n"
    "s_waitcnt lgkmcnt(4)\n"
    "ds_read_b128 a[184:187], v243 offset:32768\n"
    "v_cvt_pk_bf16_f32 v202, v204, v205\n"
    "v_mfma_f32_16x16x32_bf16 v[160:163], a[160:163], a[28:31], 0\n"
    "ds_read_b128 a[188:191], v242 offset:36864\n"
    "buffer_load_dwordx4 v[92:95], v253, s[8:11], s48 offen offset:1024 sc1\n"
    "v_mfma_f32_16x16x32_bf16 v[160:163], a[164:167], a[0:3], v[160:163]\n"
    "ds_write_addtid_b32 v210 offset:4608\n"
    "v_cvt_pk_bf16_f32 v203, v206, v207\n"
    "v_mfma_f32_16x16x32_bf16 v[168:171], a[164:167], a[32:35], 0\n"
    "ds_write_addtid_b32 v211 offset:4864\n"
    "v_mfma_f32_16x16x32_bf16 v[168:171], a[160:163], a[60:63], v[168:171]\n"
    "ds_read_b128 a[160:163], v243 offset:38912\n"
    "v_mfma_f32_16x16x32_bf16 v[168:171], a[168:171], a[36:39], v[168:171]\n"
    "ds_read_b128 a[164:167], v240 offset:34816\n"
    "buffer_load_dwordx4 v[96:99], v253, s[8:11], s48 offen offset:2048 sc1\n"
    "v_mfma_f32_16x16x32_bf16 v[168:171], a[172:175], a[40:43], v[168:171]\n"
    "ds_write_addtid_b32 v202 offset:512\n"
    "v_mfma_f32_16x16x32_bf16 v[160:163], a[172:175], a[8:11], v[160:163]\n"
    "ds_write_addtid_b32 v203 offset:768\n"
    "v_mfma_f32_16x16x32_bf16 v[160:163], a[168:171], a[4:7], v[160:163]\n"
    "s_waitcnt lgkmcnt(6)\n"
    "ds_read_b128 a[168:171], v241 offset:34816\n"
    "v_mfma_f32_16x16x32_bf16 v[160:163], a[176:179], a[12:15], v[160:163]\n"
    "ds_read_b128 a[172:175], v240 offset:38912\n"
    "buffer_load_dwordx4 v[100:103], v253, s[8:11], s48 offen offset:3072 sc1\n"
    "v_mfma_f32_16x16x32_bf16 v[160:163], a[180:183], a[16:19], v[160:163]\n"
    "s_add_u32 s48, s56, 12288\n"
    "v_mfma_f32_16x16x32_bf16 v[168:171], a[180:183], a[48:51], v[168:171]\n"
    "s_waitcnt lgkmcnt(2)\n"
    "s_barrier\n"
    "v_mfma_f32_16x16x32_bf16 v[168:171], a[176:179], a[44:47], v[168:171]\n"
    "ds_read_b128 a[176:179], v241 offset:38912\n"
    "v_mfma_f32_16x16x32_bf16 v[168:171], a[184:187], a[52:55], v[168:171]\n"
    "ds_read_b128 a[180:183], v242 offset:34816\n"
    "buffer_load_dwordx4 v[104:107], v253, s[8:11], s48 offen offset:0 sc1\n"
    "v_mfma_f32_16x16x32_bf16 v[168:171], a[188:191], a[56:59], v[168:171]\n"
    "ds_read2st64_b32 v[204:205], v250 offset0:0 offset1:1\n"
    "v_mfma_f32_16x16x32_bf16 v[160:163], a[188:191], a[24:27], v[160:163]\n"
    "v_mfma_f32_16x16x32_bf16 v[160:163], a[184:187], a[20:23], v[160:163]\n"
    "s_waitcnt lgkmcnt(3)\n"
    "ds_read_b128 a[184:187], v243 offset:34816\n"
    "v_exp_f32_e32 v168, v168\n"
    "v_mfma_f32_16x16x32_bf16 v[164:167], a[160:163], a[28:31], 0\n"
    "ds_read_b128 a[188:191], v242 offset:38912\n"
    "buffer_load_dwordx4 v[108:111], v253, s[8:11], s48 offen offset:1024 sc1\n"
    "v_exp_f32_e32 v169, v169\n"
    "v_mfma_f32_16x16x32_bf16 v[164:167], a[164:167], a[0:3], v[164:167]\n"
    "ds_read2st64_b32 v[206:207], v250 offset0:2 offset1:3\n"
    "v_exp_f32_e32 v170, v170\n"
    "v_mfma_f32_16x16x32_bf16 v[172:175], a[164:167], a[32:35], 0\n"
    "v_exp_f32_e32 v171, v171\n"
    "v_mfma_f32_16x16x32_bf16 v[172:175], a[160:163], a[60:63], v[172:175]\n"
    "ds_read_b64_tr_b16 a[160:161], v244 offset:16384\n"
    "ds_read_b64_tr_b16 a[162:163], v244 offset:18432\n"
    "v_mfma_f32_16x16x32_bf16 v[172:175], a[168:171], a[36:39], v[172:175]\n"
    "ds_read2st64_b32 v[212:213], v250 offset0:16 offset1:17\n"
    "buffer_load_dwordx4 v[112:115], v253, s[8:11], s48 offen offset:2048 sc1\n"
    "v_cvt_pk_bf16_f32 v168, v168, v169\n"
    "v_mfma_f32_16x16x32_bf16 v[172:175], a[172:175], a[40:43], v[172:175]\n"
    "ds_read_b64_tr_b16 a[164:165], v244 offset:24576\n"
    "ds_read_b64_tr_b16 a[166:167], v244 offset:26624\n"
    "v_mfma_f32_16x16x32_bf16 v[164:167], a[172:175], a[8:11], v[164:167]\n"
    "v_exp_f32_e32 v160, v160\n"
    "v_mfma_f32_16x16x32_bf16 v[164:167], a[168:171], a[4:7], v[164:167]\n"
    "s_waitcnt lgkmcnt(6)\n"
    "ds_read_b64_tr_b16 a[168:169], v245 offset:16384\n"
    "ds_read_b64_tr_b16 a[170:171], v245 offset:18432\n"
    "v_mfma_f32_16x16x32_bf16 v[164:167], a[176:179], a[12:15], v[164:167]\n"
    "ds_read2st64_b32 v[214:215], v250 offset0:18 offset1:19\n"
    "buffer_load_dwordx4 v[116:119], v253, s[8:11], s48 offen offset:3072 sc1\n"
    "v_mfma_f32_16x16x32_bf16 v[164:167], a[180:183], a[16:19], v[164:167]\n"
    "s_add_u32 s48, s56, 16384\n"
    "ds_read_b64_tr_b16 a[172:173], v245 offset:24576\n"
    "ds_read_b64_tr_b16 a[174:175], v245 offset:26624\n"
    "v_mfma_f32_16x16x32_bf16 v[172:175], a[180:183], a[48:51], v[172:175]\n"
    "v_exp_f32_e32 v161, v161\n"
    "v_mfma_f32_16x16x32_bf16 v[172:175], a[176:179], a[44:47], v[172:175]\n"
    "ds_read_b64_tr_b16 a[176:177], v246 offset:16384\n"
    "ds_read_b64_tr_b16 a[178:179], v246 offset:18432\n"
    "v_cvt_pk_bf16_f32 v169, v170, v171\n"
    "v_mfma_f32_16x16x32_bf16 v[172:175], a[184:187], a[52:55], v[172:175]\n"
    "buffer_load_dwordx4 a[28:31], v253, s[8:11], s48 offen offset:0 sc1\n"
    "v_exp_f32_e32 v162, v162\n"
    "v_mfma_f32_16x16x32_bf16 v[172:175], a[188:191], a[56:59], v[172:175]\n"
    "ds_read_b64_tr_b16 a[180:181], v246 offset:24576\n"
    "ds_read_b64_tr_b16 a[182:183], v246 offset:26624\n"
    "v_mfma_f32_16x16x32_bf16 v[164:167], a[188:191], a[24:27], v[164:167]\n"
    "v_exp_f32_e32 v163, v163\n"
    "v_mfma_f32_16x16x32_bf16 v[164:167], a[184:187], a[20:23], v[164:167]\n"
    "s_waitcnt lgkmcnt(4)\n"
    "ds_read_b64_tr_b16 a[184:185], v247 offset:16384\n"
    "ds_read_b64_tr_b16 a[186:187], v247 offset:18432\n"
    "v_mfma_f32_16x16x32_bf16 v[0:3], a[160:163], v[200:203], v[0:3]\n"
    "buffer_load_dwordx4 a[60:63], v253, s[8:11], s48 offen offset:1024 sc1\n"
    "v_cvt_pk_bf16_f32 v160, v160, v161\n"
    "v_cvt_pk_bf16_f32 v161, v162, v163\n"
    "v_mfma_f32_16x16x32_bf16 v[0:3], a[164:167], v[204:207], v[0:3]\n"
    "ds_read_b64_tr_b16 a[188:189], v247 offset:24576\n"
    "ds_read_b64_tr_b16 a[190:191], v247 offset:26624\n"
    "v_mfma_f32_16x16x32_bf16 a[248:251], a[164:167], v[212:215], a[248:251]\n"
    "v_exp_f32_e32 v172, v172\n"
    "v_mfma_f32_16x16x32_bf16 a[248:251], a[160:163], v[208:211], a[248:251]\n"
    "ds_read_b64_tr_b16 a[160:161], v244 offset:20480\n"
    "ds_read_b64_tr_b16 a[162:163], v244 offset:22528\n"
    "v_mfma_f32_16x16x32_bf16 a[252:255], a[168:171], v[208:211], a[252:255]\n"
    "buffer_load_dwordx4 a[0:3], v253, s[8:11], s48 offen offset:2048 sc1\n"
    "v_exp_f32_e32 v173, v173\n"
    "v_mfma_f32_16x16x32_bf16 a[252:255], a[172:175], v[212:215], a[252:255]\n"
    "s_waitcnt vmcnt(41)\n"
    "s_barrier\n"
    "v_mfma_f32_16x16x32_bf16 v[4:7], a[172:175], v[204:207], v[4:7]\n"
    "ds_read_b64_tr_b16 a[164:165], v244 offset:28672\n"
    "ds_read_b64_tr_b16 a[166:167], v244 offset:30720\n"
    "v_mfma_f32_16x16x32_bf16 v[4:7], a[168:171], v[200:203], v[4:7]\n"
    "s_waitcnt lgkmcnt(4)\n"
    "ds_write_addtid_b32 v168 offset:4096\n"
    "v_exp_f32_e32 v174, v174\n"
    "v_mfma_f32_16x16x32_bf16 v[8:11], a[176:179], v[200:203], v[8:11]\n"
    "ds_read_b64_tr_b16 a[168:169], v245 offset:20480\n"
    "ds_read_b64_tr_b16 a[170:171], v245 offset:22528\n"
    "buffer_load_dwordx4 a[32:35], v253, s[8:11], s48 offen offset:3072 sc1\n"
    "v_mfma_f32_16x16x32_bf16 v[8:11], a[180:183], v[204:207], v[8:11]\n"
    "s_add_u32 s48, s56, 20480\n"
    "ds_write_addtid_b32 v169 offset:4352\n"
    "v_exp_f32_e32 v175, v175\n"
    "v_mfma_f32_16x16x32_bf16 v[32:35], a[180:183], v[212:215], v[32:35]\n"
    "ds_read_b64_tr_b16 a[172:173], v245 offset:28672\n"
    "ds_read_b64_tr_b16 a[174:175], v245 offset:30720\n"
    "v_cvt_pk_bf16_f32 v170, v172, v173\n"
    "v_mfma_f32_16x16x32_bf16 v[32:35], a[176:179], v[208:211], v[32:35]\n"
    "v_exp_f32_e32 v164, v164\n"
    "v_mfma_f32_16x16x32_bf16 v[36:39], a[184:187], v[208:211], v[36:39]\n"
    "ds_read_b64_tr_b16 a[176:177], v246 offset:20480\n"
    "ds_read_b64_tr_b16 a[178:179], v246 offset:22528\n"
    "buffer_load_dwordx4 a[4:7], v253, s[8:11], s48 offen offset:0 sc1\n"
    "v_mfma_f32_16x16x32_bf16 v[36:39], a[188:191], v[212:215], v[36:39]\n"
    "v_exp_f32_e32 v165, v165\n"
    "v_mfma_f32_16x16x32_bf16 v[12:15], a[188:191], v[204:207], v[12:15]\n"
    "ds_read_b64_tr_b16 a[180:181], v246 offset:28672\n"
    "ds_read_b64_tr_b16 a[182:183], v246 offset:30720\n"
    "v_mfma_f32_16x16x32_bf16 v[12:15], a[184:187], v[200:203], v[12:15]\n"
    "s_waitcnt lgkmcnt(4)\n"
    "ds_write_addtid_b32 v160 offset:0\n"
    "v_cvt_pk_bf16_f32 v171, v174, v175\n"
    "v_mfma_f32_16x16x32_bf16 v[16:19], a[160:163], v[200:203], v[16:19]\n"
    "ds_read_b64_tr_b16 a[184:185], v247 offset:20480\n"
    "ds_read_b64_tr_b16 a[186:187], v247 offset:22528\n"
    "buffer_load_dwordx4 a[36:39], v253, s[8:11], s48 offen offset:1024 sc1\n"
    "v_mfma_f32_16x16x32_bf16 v[16:19], a[164:167], v[204:207], v[16:19]\n"
    "v_exp_f32_e32 v166, v166\n"
    "v_mfma_f32_16x16x32_bf16 v[40:43], a[164:167], v[212:215], v[40:43]\n"
    "ds_read_b64_tr_b16 a[188:189], v247 offset:28672\n"
    "ds_read_b64_tr_b16 a[190:191], v247 offset:30720\n"
    "v_mfma_f32_16x16x32_bf16 v[40:43], a[160:163], v[208:211], v[40:43]\n"
    "v_exp_f32_e32 v167, v167\n"
    "v_mfma_f32_16x16x32_bf16 v[44:47], a[168:171], v[208:211], v[44:47]\n"
    "ds_read_b64_tr_b16 a[160:161], v244 offset:32768\n"
    "buffer_load_dwordx4 a[8:11], v253, s[8:11], s48 offen offset:2048 sc1\n"
    "v_cvt_pk_bf16_f32 v162, v164, v165\n"
    "v_mfma_f32_16x16x32_bf16 v[44:47], a[172:175], v[212:215], v[44:47]\n"
    "ds_read_b64_tr_b16 a[162:163], v244 offset:34816\n"
    "v_cvt_pk_bf16_f32 v163, v166, v167\n"
    "v_mfma_f32_16x16x32_bf16 v[20:23], a[172:175], v[204:207], v[20:23]\n"
    "ds_read_b64_tr_b16 a[164:165], v244 offset:40960\n"
    "ds_write_addtid_b32 v161 offset:256\n"
    "v_mfma_f32_16x16x32_bf16 v[20:23], a[168:171], v[200:203], v[20:23]\n"
    "s_waitcnt lgkmcnt(4)\n"
    "ds_read_b64_tr_b16 a[166:167], v244 offset:43008\n"
    "ds_write_addtid_b32 v170 offset:4608\n"
    "v_mfma_f32_16x16x32_bf16 v[24:27], a[176:179], v[200:203], v[24:27]\n"
    "ds_read_b64_tr_b16 a[168:169], v245 offset:32768\n"
    "ds_write_addtid_b32 v171 offset:4864\n"
    "buffer_load_dwordx4 a[40:43], v253, s[8:11], s48 offen offset:3072 sc1\n"
    "v_mfma_f32_16x16x32_bf16 v[24:27], a[180:183], v[204:207], v[24:27]\n"
    "s_add_u32 s48, s56, 24576\n"
    "ds_read_b64_tr_b16 a[170:171], v245 offset:34816\n"
    "ds_write_addtid_b32 v162 offset:512\n"
    "v_mfma_f32_16x16x32_bf16 v[48:51], a[180:183], v[212:215], v[48:51]\n"
    "ds_read_b64_tr_b16 a[172:173], v245 offset:40960\n"
    "ds_write_addtid_b32 v163 offset:768\n"
    "v_mfma_f32_16x16x32_bf16 v[48:51], a[176:179], v[208:211], v[48:51]\n"
    "ds_read_b64_tr_b16 a[174:175], v245 offset:43008\n"
    "v_and_b32_e32 v242, 0x400, v240\n"
    "v_mfma_f32_16x16x32_bf16 v[52:55], a[184:187], v[208:211], v[52:55]\n"
    "ds_read_b64_tr_b16 a[176:177], v246 offset:32768\n"
    "buffer_load_dwordx4 a[12:15], v253, s[8:11], s48 offen offset:0 sc1\n"
    "v_and_b32_e32 v240, 0x3ff, v240\n"
    "v_mfma_f32_16x16x32_bf16 v[52:55], a[188:191], v[212:215], v[52:55]\n"
    "s_waitcnt lgkmcnt(2)\n"
    "s_barrier\n"
    "v_mfma_f32_16x16x32_bf16 v[28:31], a[188:191], v[204:207], v[28:31]\n"
    "ds_read_b64_tr_b16 a[178:179], v246 offset:34816\n"
    "v_lshl_or_b32 v240, v242, 2, v240\n"
    "v_mfma_f32_16x16x32_bf16 v[28:31], a[184:187], v[200:203], v[28:31]\n"
    "s_waitcnt lgkmcnt(0)\n"
    "ds_read_b64_tr_b16 a[180:181], v246 offset:40960\n"
    "ds_read2st64_b32 v[164:165], v250 offset0:0 offset1:1\n"
    "v_mfma_f32_16x16x32_bf16 v[0:3], a[160:163], v[160:163], v[0:3]\n"
    "ds_read_b64_tr_b16 a[182:183], v246 offset:43008\n"
    "ds_read2st64_b32 v[166:167], v250 offset0:2 offset1:3\n"
    "buffer_load_dwordx4 a[44:47], v253, s[8:11], s48 offen offset:1024 sc1\n"
    "v_mfma_f32_16x16x32_bf16 a[248:251], a[160:163], v[168:171], a[248:251]\n"
    "ds_read_b64_tr_b16 a[184:185], v247 offset:32768\n"
    "ds_read2st64_b32 v[172:173], v250 offset0:16 offset1:17\n"
    "v_mfma_f32_16x16x32_bf16 a[252:255], a[168:171], v[168:171], a[252:255]\n"
    "ds_read_b64_tr_b16 a[186:187], v247 offset:34816\n"
    "ds_read2st64_b32 v[174:175], v250 offset0:18 offset1:19\n"
    "v_mfma_f32_16x16x32_bf16 v[4:7], a[168:171], v[160:163], v[4:7]\n"
    "ds_read_b64_tr_b16 a[188:189], v247 offset:40960\n"
    "v_mfma_f32_16x16x32_bf16 v[8:11], a[176:179], v[160:163], v[8:11]\n"
    "ds_read_b64_tr_b16 a[190:191], v247 offset:43008\n"
    "buffer_load_dwordx4 a[16:19], v253, s[8:11], s48 offen offset:2048 sc1\n"
    "v_mfma_f32_16x16x32_bf16 v[32:35], a[176:179], v[168:171], v[32:35]\n"
    "s_waitcnt lgkmcnt(2)\n"
    "ds_read_b64_tr_b16 a[160:161], v244 offset:36864\n"
    "v_xor_b32_e32 v241, 64, v240\n"
    "v_mfma_f32_16x16x32_bf16 v[0:3], a[164:167], v[164:167], v[0:3]\n"
    "ds_read_b64_tr_b16 a[162:163], v244 offset:38912\n"
    "v_mfma_f32_16x16x32_bf16 a[248:251], a[164:167], v[172:175], a[248:251]\n"
    "s_waitcnt lgkmcnt(2)\n"
    "ds_read_b64_tr_b16 a[164:165], v244 offset:45056\n"
    "v_mfma_f32_16x16x32_bf16 a[252:255], a[172:175], v[172:175], a[252:255]\n"
    "ds_read_b64_tr_b16 a[166:167], v244 offset:47104\n"
    "buffer_load_dwordx4 a[48:51], v253, s[8:11], s48 offen offset:3072 sc1\n"
    "v_mfma_f32_16x16x32_bf16 v[4:7], a[172:175], v[164:167], v[4:7]\n"
    "s_add_u32 s48, s56, 28672\n"
    "ds_read_b64_tr_b16 a[168:169], v245 offset:36864\n"
    "v_mfma_f32_16x16x32_bf16 v[8:11], a[180:183], v[164:167], v[8:11]\n"
    "ds_read_b64_tr_b16 a[170:171], v245 offset:38912\n"
    "v_mfma_f32_16x16x32_bf16 v[32:35], a[180:183], v[172:175], v[32:35]\n"
    "ds_read_b64_tr_b16 a[172:173], v245 offset:45056\n"
    "v_accvgpr_read_b32 v216, a248\n"
    "v_mfma_f32_16x16x32_bf16 v[36:39], a[184:187], v[168:171], v[36:39]\n"
    "ds_read_b64_tr_b16 a[174:175], v245 offset:47104\n"
    "buffer_load_dwordx4 a[20:23], v253, s[8:11], s48 offen offset:0 sc1\n"
    "v_mfma_f32_16x16x32_bf16 v[36:39], a[188:191], v[172:175], v[36:39]\n"
    "s_lshl_b32 s45, s51, 16\n"
    "ds_read_b64_tr_b16 a[176:177], v246 offset:36864\n"
    "v_accvgpr_read_b32 v217, a249\n"
    "v_mfma_f32_16x16x32_bf16 v[12:15], a[188:191], v[164:167], v[12:15]\n"
    "ds_read_b64_tr_b16 a[178:179], v246 offset:38912\n"
    "buffer_load_dword v146, v145, s[8:11], s47 offen offset:128 sc1\n"
    "v_accvgpr_read_b32 v218, a250\n"
    "v_mfma_f32_16x16x32_bf16 v[12:15], a[184:187], v[160:163], v[12:15]\n"
    "s_waitcnt lgkmcnt(2)\n"
    "ds_read_b64_tr_b16 a[180:181], v246 offset:45056\n"
    "v_accvgpr_read_b32 v219, a251\n"
    "v_mfma_f32_16x16x32_bf16 v[16:19], a[160:163], v[160:163], v[16:19]\n"
    "ds_read_b64_tr_b16 a[182:183], v246 offset:47104\n"
    "buffer_load_dwordx4 a[52:55], v253, s[8:11], s48 offen offset:1024 sc1\n"
    "v_mfma_f32_16x16x32_bf16 v[16:19], a[164:167], v[164:167], v[16:19]\n"
    "s_lshl_b32 s46, s50, 11\n"
    "ds_read_b64_tr_b16 a[184:185], v247 offset:36864\n"
    "v_accvgpr_read_b32 v220, a252\n"
    "v_mfma_f32_16x16x32_bf16 v[40:43], a[164:167], v[172:175], v[40:43]\n"
    "s_add_u32 s46, s46, s45\n"
    "ds_read_b64_tr_b16 a[186:187], v247 offset:38912\n"
    "v_accvgpr_read_b32 v221, a253\n"
    "v_mfma_f32_16x16x32_bf16 v[40:43], a[160:163], v[168:171], v[40:43]\n"
    "s_add_u32 s46, s46, %3\n"
    "ds_read_b64_tr_b16 a[188:189], v247 offset:45056\n"
    "v_accvgpr_read_b32 v222, a254\n"
    "v_mfma_f32_16x16x32_bf16 v[44:47], a[168:171], v[168:171], v[44:47]\n"
    "ds_read_b64_tr_b16 a[190:191], v247 offset:47104\n"
    "buffer_load_dwordx4 a[24:27], v253, s[8:11], s48 offen offset:2048 sc1\n"
    "v_mfma_f32_16x16x32_bf16 v[44:47], a[172:175], v[172:175], v[44:47]\n"
    "s_add_u32 s46, s46, 81920\n"
    "ds_read_b128 a[160:163], v241 offset:11264\n"
    "v_accvgpr_read_b32 v223, a255\n"
    "v_mfma_f32_16x16x32_bf16 v[20:23], a[172:175], v[164:167], v[20:23]\n"
    "ds_read_b128 a[164:167], v241 offset:19456\n"
    "v_cvt_pk_bf16_f32 v216, v216, v217\n"
    "v_mfma_f32_16x16x32_bf16 v[20:23], a[168:171], v[160:163], v[20:23]\n"
    "s_waitcnt lgkmcnt(2)\n"
    "ds_read_b128 a[168:171], v241 offset:27648\n"
    "v_cvt_pk_bf16_f32 v217, v218, v219\n"
    "v_mfma_f32_16x16x32_bf16 v[24:27], a[176:179], v[160:163], v[24:27]\n"
    "ds_read_b128 a[172:175], v240 offset:8192\n"
    "buffer_load_dwordx4 a[56:59], v253, s[8:11], s48 offen offset:3072 sc1\n"
    "v_mfma_f32_16x16x32_bf16 v[24:27], a[180:183], v[164:167], v[24:27]\n"
    "v_cvt_pk_bf16_f32 v218, v220, v221\n"
    "v_cvt_pk_bf16_f32 v219, v222, v223\n"
    "v_mfma_f32_16x16x32_bf16 v[48:51], a[180:183], v[172:175], v[48:51]\n"
    "buffer_store_dwordx4 v[216:219], v253, s[8:11], s83 offen offset:0 sc1\n"
    "v_cvt_pk_bf16_f32 v32, v32, v33\n"
    "v_cvt_pk_bf16_f32 v33, v34, v35\n"
    "v_mfma_f32_16x16x32_bf16 v[48:51], a[176:179], v[168:171], v[48:51]\n"
    "ds_read_b128 a[176:179], v240 offset:16384\n"
    "v_cvt_pk_bf16_f32 v34, v36, v37\n"
    "v_mfma_f32_16x16x32_bf16 v[52:55], a[184:187], v[168:171], v[52:55]\n"
    "ds_read_b128 a[180:183], v240 offset:24576\n"
    "v_cvt_pk_bf16_f32 v35, v38, v39\n"
    "v_mfma_f32_16x16x32_bf16 v[52:55], a[188:191], v[172:175], v[52:55]\n"
    "buffer_store_dwordx4 v[32:35], v253, s[8:11], s83 offen offset:1024 sc1\n"
    "v_cvt_pk_bf16_f32 v40, v40, v41\n"
    "v_cvt_pk_bf16_f32 v41, v42, v43\n"
    "v_mfma_f32_16x16x32_bf16 v[28:31], a[188:191], v[164:167], v[28:31]\n"
    "v_cvt_pk_bf16_f32 v42, v44, v45\n"
    "v_cvt_pk_bf16_f32 v43, v46, v47\n"
    "v_mfma_f32_16x16x32_bf16 v[28:31], a[184:187], v[160:163], v[28:31]\n"
    "ds_read_b128 a[184:187], v241 offset:8192\n"
    "buffer_store_dwordx4 v[40:43], v253, s[8:11], s83 offen offset:2048 sc1\n"
    "v_cvt_pk_bf16_f32 v48, v48, v49\n"
    "v_add_u32_e32 v158, s46, v253\n"
    "v_mfma_f32_16x16x32_bf16 v[160:163], v[154:157], a[224:227], 0\n"
    "ds_read_b128 a[188:191], v241 offset:16384\n"
    "v_cvt_pk_bf16_f32 v49, v50, v51\n"
    "v_mfma_f32_16x16x32_bf16 v[164:167], v[152:155], a[224:227], 0\n"
    "v_cvt_pk_bf16_f32 v50, v52, v53\n"
    "v_cvt_pk_bf16_f32 v51, v54, v55\n"
    "v_mfma_f32_16x16x32_bf16 v[168:171], v[154:157], a[228:231], 0\n"
    "s_add_u32 s45, %3, 155840\n"
    "buffer_store_dwordx4 v[48:51], v253, s[8:11], s83 offen offset:3072 sc1\n"
    "v_and_b32_e32 v150, 15, v147\n"
    "v_lshlrev_b32_e32 v151, 9, v150\n"
    "v_mfma_f32_16x16x32_bf16 v[172:175], v[152:155], a[228:231], 0\n"
    "buffer_store_dword v55, v251, s[8:11], s60 offen offset:0 sc1\n"
    "v_lshl_add_u32 v150, v150, 2, s45\n"
    "v_and_or_b32 v151, v147, 48, v151\n"
    "v_mfma_f32_16x16x32_bf16 v[176:179], v[154:157], a[232:235], 0\n"
    "s_lshl_b32 s46, %2, 7\n"
    "s_add_u32 s58, s35, s46\n"
    "s_mul_i32 s45, %1, 74496\n"
    "v_mfma_f32_16x16x32_bf16 v[180:183], v[152:155], a[232:235], 0\n"
    "s_add_u32 s45, s45, 16265216\n"
    "ds_write_b128 v159, v[216:219] offset:0\n"
    "v_mfma_f32_16x16x32_bf16 v[184:187], v[154:157], a[236:239], 0\n"
    "s_lshl_b32 s54, %2, 11\n"
    "v_and_b32_e32 v147, 0xf0, v253\n"
    "v_add_u32_e32 v252, %3, v253\n"
    "v_mfma_f32_16x16x32_bf16 v[188:191], v[152:155], a[236:239], 0\n"
    "s_add_u32 s54, s54, s45\n"
    "ds_write_b128 v159, v[32:35] offset:1024\n"
    "v_mfma_f32_16x16x32_bf16 v[192:195], v[154:157], a[240:243], 0\n"
    "s_add_u32 s54, s54, 16384\n"
    "ds_write_b128 v159, v[40:43] offset:2048\n"
    "v_mfma_f32_16x16x32_bf16 v[196:199], v[152:155], a[240:243], 0\n"
    "s_lshl1_add_u32 s59, s50, s51\n"
    "s_lshl_b32 s59, s59, 12\n"
    "v_mfma_f32_16x16x32_bf16 v[200:203], v[154:157], a[244:247], 0\n"
    "s_add_u32 s46, s59, s45\n"
    "ds_write_b128 v159, v[48:51] offset:3072\n"
    "v_mfma_f32_16x16x32_bf16 v[204:207], v[152:155], a[244:247], 0\n"
    "s_waitcnt lgkmcnt(10)\n"
    "s_bitcmp1_b32 %1, 0\n"
    "s_cselect_b32 s54, s54, 2.0\n"
    "s_cselect_b32 s55, s58, 2.0\n"
    "v_mfma_f32_16x16x32_bf16 v[160:163], a[192:195], a[160:163], v[160:163]\n"
    "ds_write_b32 v254, v55 offset:0\n"
    "v_mfma_f32_16x16x32_bf16 v[164:167], a[196:199], a[160:163], v[164:167]\n"
    "s_add_u32 s61, s54, 0\n"
    "ds_write_b32 v255, v31 offset:0\n"
    "v_mfma_f32_16x16x32_bf16 v[168:171], a[200:203], a[160:163], v[168:171]\n"
    "s_waitcnt vmcnt(8)\n"
    "v_cmp_ne_u32_e32 vcc, s39, v146\n"
    "v_mfma_f32_16x16x32_bf16 v[172:175], a[204:207], a[160:163], v[172:175]\n"
    "ds_read_b128 a[160:163], v241 offset:24576\n"
    "v_mfma_f32_16x16x32_bf16 v[176:179], a[192:195], a[164:167], v[176:179]\n"
    "s_cbranch_vccnz .Lat_xpoll\n"
    ".Lat_xpoll_ret:\n"
    "v_mfma_f32_16x16x32_bf16 v[180:183], a[196:199], a[164:167], v[180:183]\n"
    "v_mfma_f32_16x16x32_bf16 v[184:187], a[200:203], a[164:167], v[184:187]\n"
    "buffer_load_dwordx4 a[224:227], v253, s[8:11], s61 offen offset:0 sc1\n"
    "v_mfma_f32_16x16x32_bf16 v[188:191], a[204:207], a[164:167], v[188:191]\n"
    "s_waitcnt lgkmcnt(11)\n"
    "ds_read_b128 a[164:167], v240 offset:9216\n"
    "v_mfma_f32_16x16x32_bf16 v[192:195], a[192:195], a[168:171], v[192:195]\n"
    "v_mfma_f32_16x16x32_bf16 v[196:199], a[196:199], a[168:171], v[196:199]\n"
    "v_mfma_f32_16x16x32_bf16 v[200:203], a[200:203], a[168:171], v[200:203]\n"
    "v_mfma_f32_16x16x32_bf16 v[204:207], a[204:207], a[168:171], v[204:207]\n"
    "ds_read_b128 a[168:171], v240 offset:17408\n"
    "buffer_load_dwordx4 a[228:231], v253, s[8:11], s61 offen offset:1024 sc1\n"
    "v_mfma_f32_16x16x32_bf16 v[160:163], a[208:211], a[172:175], v[160:163]\n"
    "s_waitcnt lgkmcnt(3)\n"
    "s_barrier\n"
    "v_mfma_f32_16x16x32_bf16 v[164:167], a[212:215], a[172:175], v[164:167]\n"
    "s_add_u32 s61, s54, 24832\n"
    "v_mfma_f32_16x16x32_bf16 v[168:171], a[216:219], a[172:175], v[168:171]\n"
    "v_mfma_f32_16x16x32_bf16 v[172:175], a[220:223], a[172:175], v[172:175]\n"
    "ds_read_b128 a[172:175], v240 offset:25600\n"
    "v_mfma_f32_16x16x32_bf16 v[176:179], a[208:211], a[176:179], v[176:179]\n"
    "buffer_load_dwordx4 a[232:235], v253, s[8:11], s61 offen offset:0 sc1\n"
    "v_mfma_f32_16x16x32_bf16 v[180:183], a[212:215], a[176:179], v[180:183]\n"
    "ds_read_b32 v232, v150 offset:0\n"
    "v_mfma_f32_16x16x32_bf16 v[184:187], a[216:219], a[176:179], v[184:187]\n"
    "v_mfma_f32_16x16x32_bf16 v[188:191], a[220:223], a[176:179], v[188:191]\n"
    "ds_read_b128 a[176:179], v241 offset:9216\n"
    "v_mfma_f32_16x16x32_bf16 v[192:195], a[208:211], a[180:183], v[192:195]\n"
    "v_mfma_f32_16x16x32_bf16 v[196:199], a[212:215], a[180:183], v[196:199]\n"
    "buffer_load_dwordx4 a[236:239], v253, s[8:11], s61 offen offset:1024 sc1\n"
    "v_mfma_f32_16x16x32_bf16 v[200:203], a[216:219], a[180:183], v[200:203]\n"
    "s_add_u32 s61, s54, 49664\n"
    "ds_read_b32 v233, v150 offset:256\n"
    "v_mfma_f32_16x16x32_bf16 v[204:207], a[220:223], a[180:183], v[204:207]\n"
    "ds_read_b128 a[180:183], v241 offset:17408\n"
    "v_mfma_f32_16x16x32_bf16 v[160:163], a[92:95], a[184:187], v[160:163]\n"
    "v_mfma_f32_16x16x32_bf16 v[164:167], a[124:127], a[184:187], v[164:167]\n"
    "v_mfma_f32_16x16x32_bf16 v[168:171], a[156:159], a[184:187], v[168:171]\n"
    "buffer_load_dwordx4 a[240:243], v253, s[8:11], s61 offen offset:0 sc1\n"
    "v_mfma_f32_16x16x32_bf16 v[172:175], a[64:67], a[184:187], v[172:175]\n"
    "ds_read_b128 a[184:187], v241 offset:25600\n"
    "v_mfma_f32_16x16x32_bf16 v[176:179], a[92:95], a[188:191], v[176:179]\n"
    "ds_read_b32 v234, v150 offset:512\n"
    "v_mfma_f32_16x16x32_bf16 v[180:183], a[124:127], a[188:191], v[180:183]\n"
    "v_mfma_f32_16x16x32_bf16 v[184:187], a[156:159], a[188:191], v[184:187]\n"
    "v_mfma_f32_16x16x32_bf16 v[188:191], a[64:67], a[188:191], v[188:191]\n"
    "s_waitcnt lgkmcnt(8)\n"
    "ds_read_b128 a[188:191], v240 offset:10240\n"
    "v_mfma_f32_16x16x32_bf16 v[192:195], a[92:95], a[160:163], v[192:195]\n"
    "buffer_load_dwordx4 a[244:247], v253, s[8:11], s61 offen offset:1024 sc1\n"
    "v_mfma_f32_16x16x32_bf16 v[196:199], a[124:127], a[160:163], v[196:199]\n"
    "s_add_u32 s61, s46, 0\n"
    "ds_read_b128 a[248:251], v158 offset:0\n"
    "v_mfma_f32_16x16x32_bf16 v[200:203], a[156:159], a[160:163], v[200:203]\n"
    "v_mfma_f32_16x16x32_bf16 v[204:207], a[64:67], a[160:163], v[204:207]\n"
    "ds_read_b128 a[160:163], v240 offset:18432\n"
    "v_mfma_f32_16x16x32_bf16 v[160:163], a[96:99], a[164:167], v[160:163]\n"
    "s_waitcnt vmcnt(6)\n"
    "buffer_store_dword v148, v149, s[8:11], s87 offen offset:0 sc1\n"
    "v_mfma_f32_16x16x32_bf16 v[164:167], a[128:131], a[164:167], v[164:167]\n"
    "buffer_load_dwordx4 a[192:195], v253, s[8:11], s61 offen offset:0 sc1\n"
    "v_mfma_f32_16x16x32_bf16 v[168:171], a[68:71], a[164:167], v[168:171]\n"
    "ds_read_b128 a[252:255], v158 offset:1024\n"
    "v_mfma_f32_16x16x32_bf16 v[172:175], a[100:103], a[164:167], v[172:175]\n"
    "s_waitcnt lgkmcnt(10)\n"
    "ds_read_b128 a[164:167], v240 offset:26624\n"
    "v_mfma_f32_16x16x32_bf16 v[176:179], a[96:99], a[168:171], v[176:179]\n"
    "v_mfma_f32_16x16x32_bf16 v[180:183], a[128:131], a[168:171], v[180:183]\n"
    "v_mfma_f32_16x16x32_bf16 v[184:187], a[68:71], a[168:171], v[184:187]\n"
    "buffer_load_dwordx4 a[196:199], v253, s[8:11], s61 offen offset:1024 sc1\n"
    "v_mfma_f32_16x16x32_bf16 v[188:191], a[100:103], a[168:171], v[188:191]\n"
    "s_lshl_b32 s52, %2, 11\n"
    "ds_read_b128 a[168:171], v241 offset:10240\n"
    "v_mfma_f32_16x16x32_bf16 v[192:195], a[96:99], a[172:175], v[192:195]\n"
    "v_mfma_f32_16x16x32_bf16 v[196:199], a[128:131], a[172:175], v[196:199]\n"
    "v_mfma_f32_16x16x32_bf16 v[200:203], a[68:71], a[172:175], v[200:203]\n"
    "v_mfma_f32_16x16x32_bf16 v[204:207], a[100:103], a[172:175], v[204:207]\n"
    "s_waitcnt lgkmcnt(8)\n"
    "ds_read_b128 a[172:175], v241 offset:18432\n"
    "v_mfma_f32_16x16x32_bf16 v[160:163], a[132:135], a[176:179], v[160:163]\n"
    "buffer_load_dwordx4 a[200:203], v253, s[8:11], s61 offen offset:2048 sc1\n"
    "v_mfma_f32_16x16x32_bf16 v[164:167], a[72:75], a[176:179], v[164:167]\n"
    "v_add_u32_e32 v251, s52, v252\n"
    "v_mfma_f32_16x16x32_bf16 v[168:171], a[104:107], a[176:179], v[168:171]\n"
    "v_mfma_f32_16x16x32_bf16 v[172:175], a[136:139], a[176:179], v[172:175]\n"
    "ds_read_b128 a[176:179], v241 offset:26624\n"
    "v_mfma_f32_16x16x32_bf16 v[176:179], a[132:135], a[180:183], v[176:179]\n"
    "v_mfma_f32_16x16x32_bf16 v[180:183], a[72:75], a[180:183], v[180:183]\n"
    "buffer_load_dwordx4 a[204:207], v253, s[8:11], s61 offen offset:3072 sc1\n"
    "v_mfma_f32_16x16x32_bf16 v[184:187], a[104:107], a[180:183], v[184:187]\n"
    "s_add_u32 s61, s46, 24832\n"
    "v_add_u32_e32 v158, s59, v252\n"
    "v_mfma_f32_16x16x32_bf16 v[188:191], a[136:139], a[180:183], v[188:191]\n"
    "s_waitcnt lgkmcnt(7)\n"
    "ds_read_b128 a[180:183], v240 offset:11264\n"
    "v_mfma_f32_16x16x32_bf16 v[192:195], a[132:135], a[184:187], v[192:195]\n"
    "v_mfma_f32_16x16x32_bf16 v[196:199], a[72:75], a[184:187], v[196:199]\n"
    "v_mfma_f32_16x16x32_bf16 v[200:203], a[104:107], a[184:187], v[200:203]\n"
    "buffer_load_dwordx4 a[208:211], v253, s[8:11], s61 offen offset:0 sc1\n"
    "v_mfma_f32_16x16x32_bf16 v[204:207], a[136:139], a[184:187], v[204:207]\n"
    "ds_read_b128 a[184:187], v240 offset:19456\n"
    "v_mfma_f32_16x16x32_bf16 v[160:163], a[76:79], a[188:191], v[160:163]\n"
    "v_and_b32_e32 v145, 12, v145\n"
    "v_mfma_f32_16x16x32_bf16 v[164:167], a[108:111], a[188:191], v[164:167]\n"
    "v_mfma_f32_16x16x32_bf16 v[168:171], a[140:143], a[188:191], v[168:171]\n"
    "v_mfma_f32_16x16x32_bf16 v[172:175], a[80:83], a[188:191], v[172:175]\n"
    "s_waitcnt lgkmcnt(5)\n"
    "ds_read_b128 a[188:191], v240 offset:27648\n"
    "v_mfma_f32_16x16x32_bf16 v[176:179], a[76:79], a[160:163], v[176:179]\n"
    "buffer_load_dwordx4 a[212:215], v253, s[8:11], s61 offen offset:1024 sc1\n"
    "v_mfma_f32_16x16x32_bf16 v[180:183], a[108:111], a[160:163], v[180:183]\n"
    "s_add_u32 s52, s32, 3\n"
    "v_mfma_f32_16x16x32_bf16 v[184:187], a[140:143], a[160:163], v[184:187]\n"
    "v_mfma_f32_16x16x32_bf16 v[188:191], a[80:83], a[160:163], v[188:191]\n"
    "v_mfma_f32_16x16x32_bf16 v[192:195], a[76:79], a[164:167], v[192:195]\n"
    "v_mfma_f32_16x16x32_bf16 v[196:199], a[108:111], a[164:167], v[196:199]\n"
    "buffer_load_dwordx4 a[216:219], v253, s[8:11], s61 offen offset:2048 sc1\n"
    "v_mfma_f32_16x16x32_bf16 v[200:203], a[140:143], a[164:167], v[200:203]\n"
    "s_and_b32 s52, s52, 3\n"
    "v_mfma_f32_16x16x32_bf16 v[204:207], a[80:83], a[164:167], v[204:207]\n"
    "s_waitcnt lgkmcnt(4)\n"
    "v_mfma_f32_16x16x32_bf16 v[160:163], a[112:115], a[168:171], v[160:163]\n"
    "v_mfma_f32_16x16x32_bf16 v[164:167], a[144:147], a[168:171], v[164:167]\n"
    "v_mfma_f32_16x16x32_bf16 v[168:171], a[84:87], a[168:171], v[168:171]\n"
    "buffer_load_dwordx4 a[220:223], v253, s[8:11], s61 offen offset:3072 sc1\n"
    "v_mfma_f32_16x16x32_bf16 v[172:175], a[116:119], a[168:171], v[172:175]\n"
    "s_add_u32 s61, s45, 24576\n"
    "v_mfma_f32_16x16x32_bf16 v[176:179], a[112:115], a[172:175], v[176:179]\n"
    "s_lshl_b32 s52, s52, 8\n"
    "v_mfma_f32_16x16x32_bf16 v[180:183], a[144:147], a[172:175], v[180:183]\n"
    "v_mfma_f32_16x16x32_bf16 v[184:187], a[84:87], a[172:175], v[184:187]\n"
    "v_mfma_f32_16x16x32_bf16 v[188:191], a[116:119], a[172:175], v[188:191]\n"
    "s_waitcnt lgkmcnt(2)\n"
    "buffer_load_dwordx3 v[236:238], v147, s[8:11], s61 offen offset:0 sc1\n"
    "v_mfma_f32_16x16x32_bf16 v[192:195], a[112:115], a[176:179], v[192:195]\n"
    "s_add_u32 s61, s45, 49408\n"
    "v_mfma_f32_16x16x32_bf16 v[196:199], a[144:147], a[176:179], v[196:199]\n"
    "s_add_u32 s52, s52, s47\n"
    "v_mfma_f32_16x16x32_bf16 v[200:203], a[84:87], a[176:179], v[200:203]\n"
    "v_mfma_f32_16x16x32_bf16 v[204:207], a[116:119], a[176:179], v[204:207]\n"
    "v_mfma_f32_16x16x32_bf16 v[160:163], a[148:151], a[180:183], v[160:163]\n"
    "buffer_load_dwordx3 v[248:250], v147, s[8:11], s61 offen offset:0 sc1\n"
    "v_mfma_f32_16x16x32_bf16 v[164:167], a[88:91], a[180:183], v[164:167]\n"
    "v_mfma_f32_16x16x32_bf16 v[168:171], a[120:123], a[180:183], v[168:171]\n"
    "v_mfma_f32_16x16x32_bf16 v[172:175], a[152:155], a[180:183], v[172:175]\n"
    "s_waitcnt lgkmcnt(0)\n"
    "v_mfma_f32_16x16x32_bf16 v[176:179], a[148:151], a[184:187], v[176:179]\n"
    "v_mfma_f32_16x16x32_bf16 v[180:183], a[88:91], a[184:187], v[180:183]\n"
    "v_mfma_f32_16x16x32_bf16 v[184:187], a[120:123], a[184:187], v[184:187]\n"
    "v_mfma_f32_16x16x32_bf16 v[188:191], a[152:155], a[184:187], v[188:191]\n"
    "v_mfma_f32_16x16x32_bf16 v[192:195], a[148:151], a[188:191], v[192:195]\n"
    "v_mfma_f32_16x16x32_bf16 v[196:199], a[88:91], a[188:191], v[196:199]\n"
    "v_mfma_f32_16x16x32_bf16 v[200:203], a[120:123], a[188:191], v[200:203]\n"
    "v_mfma_f32_16x16x32_bf16 v[204:207], a[152:155], a[188:191], v[204:207]\n"
    "v_mfma_f32_16x16x32_bf16 v[128:131], v[154:157], a[248:251], 0\n"
    "v_mfma_f32_16x16x32_bf16 v[132:135], v[152:155], a[248:251], 0\n"
    "v_mfma_f32_16x16x32_bf16 v[136:139], v[154:157], a[252:255], 0\n"
    "v_mfma_f32_16x16x32_bf16 v[140:143], v[152:155], a[252:255], 0\n"
    "s_waitcnt vmcnt(15)\n"
    "v_mfma_f32_16x16x32_bf16 v[128:131], v[154:157], a[224:227], v[128:131]\n"
    "v_mfma_f32_16x16x32_bf16 v[132:135], v[152:155], a[224:227], v[132:135]\n"
    "v_mfma_f32_16x16x32_bf16 v[136:139], v[154:157], a[228:231], v[136:139]\n"
    "buffer_load_dword v144, v145, s[8:11], s52 offen offset:0 sc1\n"
    "v_mfma_f32_16x16x32_bf16 v[140:143], v[152:155], a[228:231], v[140:143]\n"
    "s_waitcnt vmcnt(14)\n"
    "v_mfma_f32_16x16x32_bf16 v[128:131], v[154:157], a[232:235], v[128:131]\n"
    "v_mfma_f32_16x16x32_bf16 v[132:135], v[152:155], a[232:235], v[132:135]\n"
    "v_mfma_f32_16x16x32_bf16 v[136:139], v[154:157], a[236:239], v[136:139]\n"
    "v_mfma_f32_16x16x32_bf16 v[140:143], v[152:155], a[236:239], v[140:143]\n"
    "s_waitcnt vmcnt(12)\n"
    "v_mfma_f32_16x16x32_bf16 v[128:131], v[154:157], a[240:243], v[128:131]\n"
    "v_mfma_f32_16x16x32_bf16 v[132:135], v[152:155], a[240:243], v[132:135]\n"
    "v_mfma_f32_16x16x32_bf16 v[136:139], v[154:157], a[244:247], v[136:139]\n"
    "v_mfma_f32_16x16x32_bf16 v[140:143], v[152:155], a[244:247], v[140:143]\n"
    "s_waitcnt vmcnt(9)\n"
    "v_mfma_f32_16x16x32_bf16 v[0:3], v[154:157], a[192:195], v[0:3]\n"
    "v_cvt_pk_bf16_f32 v128, v128, v129\n"
    "v_mfma_f32_16x16x32_bf16 v[4:7], v[152:155], a[192:195], v[4:7]\n"
    "v_cvt_pk_bf16_f32 v129, v130, v131\n"
    "v_mfma_f32_16x16x32_bf16 v[8:11], v[154:157], a[196:199], v[8:11]\n"
    "v_cvt_pk_bf16_f32 v130, v132, v133\n"
    "v_mfma_f32_16x16x32_bf16 v[12:15], v[152:155], a[196:199], v[12:15]\n"
    "s_waitcnt vmcnt(7)\n"
    "v_cvt_pk_bf16_f32 v131, v134, v135\n"
    "v_mfma_f32_16x16x32_bf16 v[16:19], v[154:157], a[200:203], v[16:19]\n"
    "ds_write_b128 v251, v[128:131] offset:49152\n"
    "v_mfma_f32_16x16x32_bf16 v[20:23], v[152:155], a[200:203], v[20:23]\n"
    "v_cvt_pk_bf16_f32 v136, v136, v137\n"
    "v_mfma_f32_16x16x32_bf16 v[24:27], v[154:157], a[204:207], v[24:27]\n"
    "v_cvt_pk_bf16_f32 v137, v138, v139\n"
    "v_mfma_f32_16x16x32_bf16 v[28:31], v[152:155], a[204:207], v[28:31]\n"
    "s_waitcnt vmcnt(5)\n"
    "v_cvt_pk_bf16_f32 v138, v140, v141\n"
    "v_cvt_pk_bf16_f32 v139, v142, v143\n"
    "v_mfma_f32_16x16x32_bf16 v[0:3], v[154:157], a[208:211], v[0:3]\n"
    "ds_write_b128 v251, v[136:139] offset:50176\n"
    "v_mfma_f32_16x16x32_bf16 v[4:7], v[152:155], a[208:211], v[4:7]\n"
    "v_mfma_f32_16x16x32_bf16 v[8:11], v[154:157], a[212:215], v[8:11]\n"
    "s_waitcnt lgkmcnt(0)\n"
    "s_barrier\n"
    "ds_read_b128 a[128:131], v252 offset:49152\n"
    "ds_read_b128 a[132:135], v252 offset:50176\n"
    "ds_read_b128 a[136:139], v252 offset:51200\n"
    "v_mfma_f32_16x16x32_bf16 v[12:15], v[152:155], a[212:215], v[12:15]\n"
    "s_waitcnt vmcnt(3)\n"
    "v_mfma_f32_16x16x32_bf16 v[16:19], v[154:157], a[216:219], v[16:19]\n"
    "ds_read_b128 a[140:143], v252 offset:52224\n"
    "v_mfma_f32_16x16x32_bf16 v[20:23], v[152:155], a[216:219], v[20:23]\n"
    "v_mfma_f32_16x16x32_bf16 v[24:27], v[154:157], a[220:223], v[24:27]\n"
    "ds_read_b128 a[144:147], v252 offset:53248\n"
    "v_mfma_f32_16x16x32_bf16 v[28:31], v[152:155], a[220:223], v[28:31]\n"
    "s_waitcnt lgkmcnt(3)\n"
    "v_mfma_f32_16x16x32_bf16 v[216:219], v[56:59], a[128:131], 0\n"
    "ds_read_b128 a[148:151], v252 offset:54272\n"
    "v_mfma_f32_16x16x32_bf16 v[220:223], v[60:63], a[128:131], 0\n"
    "ds_read_b128 a[152:155], v252 offset:55296\n"
    "v_mfma_f32_16x16x32_bf16 v[224:227], v[64:67], a[128:131], 0\n"
    "s_waitcnt vmcnt(0)\n"
    "v_cmp_ne_u32_e32 vcc, s39, v144\n"
    "s_nop 3\n"
    "s_cbranch_vccnz .Lat_ypoll\n"
    ".Lat_ypoll_ret:\n"
    "s_add_u32 s61, s46, 49664\n"
    "buffer_load_dwordx4 a[160:163], v253, s[8:11], s61 offen offset:0 sc1\n"
    "buffer_load_dwordx4 a[164:167], v253, s[8:11], s61 offen offset:1024 sc1\n"
    "buffer_load_dwordx4 a[168:171], v253, s[8:11], s61 offen offset:2048 sc1\n"
    "buffer_load_dwordx4 a[172:175], v253, s[8:11], s61 offen offset:3072 sc1\n"
    "s_add_u32 s61, s45, 74240\n"
    "buffer_load_dwordx3 v[244:246], v147, s[8:11], s61 offen offset:0 sc1\n"
    "v_mfma_f32_16x16x32_bf16 v[228:231], v[68:71], a[128:131], 0\n"
    "ds_read_b128 a[156:159], v252 offset:56320\n"
    "v_mfma_f32_16x16x32_bf16 v[216:219], v[72:75], a[132:135], v[216:219]\n"
    "v_mfma_f32_16x16x32_bf16 v[220:223], v[76:79], a[132:135], v[220:223]\n"
    "s_waitcnt vmcnt(6)\n"
    "v_mfma_f32_16x16x32_bf16 v[224:227], v[80:83], a[132:135], v[224:227]\n"
    "v_mfma_f32_16x16x32_bf16 v[228:231], v[84:87], a[132:135], v[228:231]\n"
    "s_waitcnt lgkmcnt(4)\n"
    "v_mfma_f32_16x16x32_bf16 v[216:219], v[88:91], a[136:139], v[216:219]\n"
    "v_mfma_f32_16x16x32_bf16 v[220:223], v[92:95], a[136:139], v[220:223]\n"
    "v_add_f32_e32 v232, v232, v236\n"
    "v_mfma_f32_16x16x32_bf16 v[224:227], v[96:99], a[136:139], v[224:227]\n"
    "v_mfma_f32_16x16x32_bf16 v[228:231], v[100:103], a[136:139], v[228:231]\n"
    "v_mfma_f32_16x16x32_bf16 v[216:219], v[104:107], a[140:143], v[216:219]\n"
    "v_mfma_f32_16x16x32_bf16 v[220:223], v[108:111], a[140:143], v[220:223]\n"
    "v_add_f32_e32 v233, v233, v237\n"
    "v_mfma_f32_16x16x32_bf16 v[224:227], v[112:115], a[140:143], v[224:227]\n"
    "v_mfma_f32_16x16x32_bf16 v[228:231], v[116:119], a[140:143], v[228:231]\n"
    "s_waitcnt lgkmcnt(2)\n"
    "v_mfma_f32_16x16x32_bf16 v[216:219], a[28:31], a[144:147], v[216:219]\n"
    "v_mfma_f32_16x16x32_bf16 v[220:223], a[60:63], a[144:147], v[220:223]\n"
    "v_add_f32_e32 v234, v234, v238\n"
    "v_mfma_f32_16x16x32_bf16 v[224:227], a[0:3], a[144:147], v[224:227]\n"
    "v_mfma_f32_16x16x32_bf16 v[228:231], a[32:35], a[144:147], v[228:231]\n"
    "v_mfma_f32_16x16x32_bf16 v[216:219], a[4:7], a[148:151], v[216:219]\n"
    "v_mfma_f32_16x16x32_bf16 v[220:223], a[36:39], a[148:151], v[220:223]\n"
    "v_add_f32_e32 v232, v232, v248\n"
    "v_mfma_f32_16x16x32_bf16 v[224:227], a[8:11], a[148:151], v[224:227]\n"
    "v_mfma_f32_16x16x32_bf16 v[228:231], a[40:43], a[148:151], v[228:231]\n"
    "s_waitcnt lgkmcnt(0)\n"
    "v_mfma_f32_16x16x32_bf16 v[216:219], a[12:15], a[152:155], v[216:219]\n"
    "v_mfma_f32_16x16x32_bf16 v[220:223], a[44:47], a[152:155], v[220:223]\n"
    "v_add_f32_e32 v233, v233, v249\n"
    "v_mfma_f32_16x16x32_bf16 v[224:227], a[16:19], a[152:155], v[224:227]\n"
    "v_mfma_f32_16x16x32_bf16 v[228:231], a[48:51], a[152:155], v[228:231]\n"
    "v_mfma_f32_16x16x32_bf16 v[216:219], a[20:23], a[156:159], v[216:219]\n"
    "v_mfma_f32_16x16x32_bf16 v[220:223], a[52:55], a[156:159], v[220:223]\n"
    "v_add_f32_e32 v234, v234, v250\n"
    "v_mfma_f32_16x16x32_bf16 v[224:227], a[24:27], a[156:159], v[224:227]\n"
    "v_mfma_f32_16x16x32_bf16 v[228:231], a[56:59], a[156:159], v[228:231]\n"
    "s_waitcnt vmcnt(4)\n"
    "v_mfma_f32_16x16x32_bf16 v[0:3], v[154:157], a[160:163], v[0:3]\n"
    "v_mfma_f32_16x16x32_bf16 v[4:7], v[152:155], a[160:163], v[4:7]\n"
    "s_waitcnt vmcnt(3)\n"
    "v_mfma_f32_16x16x32_bf16 v[8:11], v[154:157], a[164:167], v[8:11]\n"
    "v_mfma_f32_16x16x32_bf16 v[12:15], v[152:155], a[164:167], v[12:15]\n"
    "s_waitcnt vmcnt(2)\n"
    "v_cvt_pk_bf16_f32 v0, v0, v1\n"
    "v_cvt_pk_bf16_f32 v1, v2, v3\n"
    "v_mfma_f32_16x16x32_bf16 v[16:19], v[154:157], a[168:171], v[16:19]\n"
    "v_cvt_pk_bf16_f32 v2, v4, v5\n"
    "v_cvt_pk_bf16_f32 v3, v6, v7\n"
    "v_mfma_f32_16x16x32_bf16 v[20:23], v[152:155], a[168:171], v[20:23]\n"
    "s_waitcnt vmcnt(1)\n"
    "ds_write_b128 v158, v[0:3] offset:32768\n"
    "v_mfma_f32_16x16x32_bf16 v[24:27], v[154:157], a[172:175], v[24:27]\n"
    "v_cvt_pk_bf16_f32 v8, v8, v9\n"
    "v_cvt_pk_bf16_f32 v9, v10, v11\n"
    "v_cvt_pk_bf16_f32 v10, v12, v13\n"
    "v_cvt_pk_bf16_f32 v11, v14, v15\n"
    "v_mfma_f32_16x16x32_bf16 v[28:31], v[152:155], a[172:175], v[28:31]\n"
    "ds_write_b128 v158, v[8:11] offset:33792\n"
    "v_cvt_pk_bf16_f32 v16, v16, v17\n"
    "v_cvt_pk_bf16_f32 v17, v18, v19\n"
    "v_cvt_pk_bf16_f32 v18, v20, v21\n"
    "v_cvt_pk_bf16_f32 v19, v22, v23\n"
    "ds_write_b128 v158, v[16:19] offset:34816\n"
    "v_cvt_pk_bf16_f32 v24, v24, v25\n"
    "v_cvt_pk_bf16_f32 v25, v26, v27\n"
    "v_cvt_pk_bf16_f32 v26, v28, v29\n"
    "v_cvt_pk_bf16_f32 v27, v30, v31\n"
    "ds_write_b128 v158, v[24:27] offset:35840\n"
    "s_waitcnt lgkmcnt(0)\n"
    "s_barrier\n"
    "ds_read_b128 a[96:99], v252 offset:40960\n"
    "ds_read_b128 a[100:103], v252 offset:41984\n"
    "ds_read_b128 a[104:107], v252 offset:43008\n"
    "ds_read_b128 a[108:111], v252 offset:44032\n"
    "ds_read_b128 a[112:115], v252 offset:45056\n"
    "ds_read_b128 a[116:119], v252 offset:46080\n"
    "ds_read_b128 a[120:123], v252 offset:47104\n"
    "ds_read_b128 a[124:127], v252 offset:48128\n"
    "s_waitcnt lgkmcnt(6)\n"
    "v_mfma_f32_16x16x32_bf16 v[120:123], v[56:59], a[96:99], 0\n"
    "s_waitcnt vmcnt(0)\n"
    "v_add_f32_e32 v234, v234, v246\n"
    "v_mfma_f32_16x16x32_bf16 v[124:127], v[60:63], a[96:99], 0\n"
    "ds_read_b128 a[64:67], v252 offset:32768\n"
    "v_mfma_f32_16x16x32_bf16 v[128:131], v[64:67], a[96:99], 0\n"
    "v_add_f32_e32 v233, v233, v245\n"
    "v_add_f32_e32 v232, v232, v244\n"
    "v_mfma_f32_16x16x32_bf16 v[132:135], v[68:71], a[96:99], 0\n"
    "v_rcp_f32_e32 v242, v234\n"
    "v_mfma_f32_16x16x32_bf16 v[120:123], v[72:75], a[100:103], v[120:123]\n"
    "s_add_u32 s48, s55, 16384\n"
    "ds_read_b128 a[68:71], v252 offset:33792\n"
    "v_mfma_f32_16x16x32_bf16 v[124:127], v[76:79], a[100:103], v[124:127]\n"
    "v_fma_f32 v216, v216, v242, v192\n"
    "v_fma_f32 v217, v217, v242, v193\n"
    "v_mfma_f32_16x16x32_bf16 v[128:131], v[80:83], a[100:103], v[128:131]\n"
    "ds_read_b128 a[72:75], v252 offset:34816\n"
    "v_fma_f32 v218, v218, v242, v194\n"
    "v_mfma_f32_16x16x32_bf16 v[132:135], v[84:87], a[100:103], v[132:135]\n"
    "s_waitcnt lgkmcnt(7)\n"
    "v_fma_f32 v219, v219, v242, v195\n"
    "v_fma_f32 v220, v220, v242, v196\n"
    "v_mfma_f32_16x16x32_bf16 v[120:123], v[88:91], a[104:107], v[120:123]\n"
    "v_fma_f32 v221, v221, v242, v197\n"
    "v_mfma_f32_16x16x32_bf16 v[124:127], v[92:95], a[104:107], v[124:127]\n"
    "ds_read_b128 a[76:79], v252 offset:35840\n"
    "v_fma_f32 v222, v222, v242, v198\n"
    "v_mfma_f32_16x16x32_bf16 v[128:131], v[96:99], a[104:107], v[128:131]\n"
    "v_fma_f32 v223, v223, v242, v199\n"
    "v_cvt_pk_bf16_f32 v216, v216, v217\n"
    "v_mfma_f32_16x16x32_bf16 v[132:135], v[100:103], a[104:107], v[132:135]\n"
    "ds_read_b128 a[80:83], v252 offset:36864\n"
    "v_cvt_pk_bf16_f32 v217, v218, v219\n"
    "v_mfma_f32_16x16x32_bf16 v[120:123], v[104:107], a[108:111], v[120:123]\n"
    "v_cvt_pk_bf16_f32 v218, v220, v221\n"
    "v_cvt_pk_bf16_f32 v219, v222, v223\n"
    "v_mfma_f32_16x16x32_bf16 v[124:127], v[108:111], a[108:111], v[124:127]\n"
    "buffer_store_dwordx4 v[216:219], v151, s[16:19], s48 offen offset:0 sc0 sc1\n"
    "v_fma_f32 v224, v224, v242, v200\n"
    "v_mfma_f32_16x16x32_bf16 v[128:131], v[112:115], a[108:111], v[128:131]\n"
    "ds_read_b128 a[84:87], v252 offset:37888\n"
    "v_fma_f32 v225, v225, v242, v201\n"
    "v_mfma_f32_16x16x32_bf16 v[132:135], v[116:119], a[108:111], v[132:135]\n"
    "s_waitcnt lgkmcnt(8)\n"
    "v_fma_f32 v226, v226, v242, v202\n"
    "v_fma_f32 v227, v227, v242, v203\n"
    "v_mfma_f32_16x16x32_bf16 v[120:123], a[28:31], a[112:115], v[120:123]\n"
    "ds_read_b128 a[88:91], v252 offset:38912\n"
    "v_fma_f32 v228, v228, v242, v204\n"
    "v_mfma_f32_16x16x32_bf16 v[124:127], a[60:63], a[112:115], v[124:127]\n"
    "v_fma_f32 v229, v229, v242, v205\n"
    "v_fma_f32 v230, v230, v242, v206\n"
    "v_mfma_f32_16x16x32_bf16 v[128:131], a[0:3], a[112:115], v[128:131]\n"
    "v_fma_f32 v231, v231, v242, v207\n"
    "v_cvt_pk_bf16_f32 v224, v224, v225\n"
    "v_mfma_f32_16x16x32_bf16 v[132:135], a[32:35], a[112:115], v[132:135]\n"
    "ds_read_b128 a[92:95], v252 offset:39936\n"
    "v_cvt_pk_bf16_f32 v225, v226, v227\n"
    "v_mfma_f32_16x16x32_bf16 v[120:123], a[4:7], a[116:119], v[120:123]\n"
    "v_cvt_pk_bf16_f32 v226, v228, v229\n"
    "v_cvt_pk_bf16_f32 v227, v230, v231\n"
    "v_mfma_f32_16x16x32_bf16 v[124:127], a[36:39], a[116:119], v[124:127]\n"
    "buffer_store_dwordx4 v[224:227], v151, s[16:19], s48 offen offset:64 sc0 sc1\n"
    "v_mfma_f32_16x16x32_bf16 v[128:131], a[8:11], a[116:119], v[128:131]\n"
    "s_bitcmp1_b32 %1, 0\n"
    "s_cselect_b64 vcc, -1, 0\n"
    "v_mfma_f32_16x16x32_bf16 v[132:135], a[40:43], a[116:119], v[132:135]\n"
    "s_waitcnt lgkmcnt(8)\n"
    "v_cndmask_b32_e32 v234, v233, v234, vcc\n"
    "v_mfma_f32_16x16x32_bf16 v[120:123], a[12:15], a[120:123], v[120:123]\n"
    "s_mov_b32 s61, 0x12800000\n"
    "v_rcp_f32_e32 v241, v233\n"
    "v_mfma_f32_16x16x32_bf16 v[124:127], a[44:47], a[120:123], v[124:127]\n"
    "v_rcp_f32_e32 v240, v232\n"
    "v_mfma_f32_16x16x32_bf16 v[128:131], a[16:19], a[120:123], v[128:131]\n"
    "v_subrev_u32_e32 v236, s61, v232\n"
    "v_mfma_f32_16x16x32_bf16 v[132:135], a[48:51], a[120:123], v[132:135]\n"
    "v_subrev_u32_e32 v237, s61, v233\n"
    "v_mfma_f32_16x16x32_bf16 v[120:123], a[20:23], a[124:127], v[120:123]\n"
    "v_subrev_u32_e32 v238, s61, v234\n"
    "v_max3_u32 v236, v236, v237, v238\n"
    "v_mfma_f32_16x16x32_bf16 v[124:127], a[52:55], a[124:127], v[124:127]\n"
    "buffer_store_dword v148, v149, s[8:11], s88 offen offset:0 sc1\n"
    "v_cmp_le_u32_e32 vcc, 0x5f000000, v236\n"
    "v_mfma_f32_16x16x32_bf16 v[128:131], a[24:27], a[124:127], v[128:131]\n"
    "v_mfma_f32_16x16x32_bf16 v[132:135], a[56:59], a[124:127], v[132:135]\n"
    "s_waitcnt lgkmcnt(0)\n"
    "v_mfma_f32_16x16x32_bf16 v[32:35], v[56:59], a[64:67], 0\n"
    "s_cbranch_vccnz .Lat_exit\n"
    "v_mfma_f32_16x16x32_bf16 v[36:39], v[60:63], a[64:67], 0\n"
    "s_add_u32 s48, s58, 8192\n"
    "v_fma_f32 v120, v120, v241, v176\n"
    "v_mfma_f32_16x16x32_bf16 v[32:35], v[72:75], a[68:71], v[32:35]\n"
    "v_fma_f32 v121, v121, v241, v177\n"
    "v_mfma_f32_16x16x32_bf16 v[36:39], v[76:79], a[68:71], v[36:39]\n"
    "v_fma_f32 v122, v122, v241, v178\n"
    "v_mfma_f32_16x16x32_bf16 v[32:35], v[88:91], a[72:75], v[32:35]\n"
    "v_fma_f32 v123, v123, v241, v179\n"
    "v_mfma_f32_16x16x32_bf16 v[36:39], v[92:95], a[72:75], v[36:39]\n"
    "v_fma_f32 v124, v124, v241, v180\n"
    "v_mfma_f32_16x16x32_bf16 v[32:35], v[104:107], a[76:79], v[32:35]\n"
    "v_fma_f32 v125, v125, v241, v181\n"
    "v_mfma_f32_16x16x32_bf16 v[36:39], v[108:111], a[76:79], v[36:39]\n"
    "v_fma_f32 v126, v126, v241, v182\n"
    "v_fma_f32 v127, v127, v241, v183\n"
    "v_mfma_f32_16x16x32_bf16 v[32:35], a[28:31], a[80:83], v[32:35]\n"
    "v_cvt_pk_bf16_f32 v120, v120, v121\n"
    "v_mfma_f32_16x16x32_bf16 v[36:39], a[60:63], a[80:83], v[36:39]\n"
    "v_cvt_pk_bf16_f32 v121, v122, v123\n"
    "v_mfma_f32_16x16x32_bf16 v[32:35], a[4:7], a[84:87], v[32:35]\n"
    "v_cvt_pk_bf16_f32 v122, v124, v125\n"
    "v_mfma_f32_16x16x32_bf16 v[36:39], a[36:39], a[84:87], v[36:39]\n"
    "v_cvt_pk_bf16_f32 v123, v126, v127\n"
    "v_mfma_f32_16x16x32_bf16 v[32:35], a[12:15], a[88:91], v[32:35]\n"
    "buffer_store_dwordx4 v[120:123], v151, s[16:19], s48 offen offset:0 sc0 sc1\n"
    "v_fma_f32 v128, v128, v241, v184\n"
    "v_mfma_f32_16x16x32_bf16 v[36:39], a[44:47], a[88:91], v[36:39]\n"
    "v_fma_f32 v129, v129, v241, v185\n"
    "v_fma_f32 v130, v130, v241, v186\n"
    "v_mfma_f32_16x16x32_bf16 v[32:35], a[20:23], a[92:95], v[32:35]\n"
    "v_fma_f32 v131, v131, v241, v187\n"
    "v_mfma_f32_16x16x32_bf16 v[36:39], a[52:55], a[92:95], v[36:39]\n"
    "v_fma_f32 v132, v132, v241, v188\n"
    "v_mfma_f32_16x16x32_bf16 v[40:43], v[64:67], a[64:67], 0\n"
    "v_fma_f32 v133, v133, v241, v189\n"
    "v_mfma_f32_16x16x32_bf16 v[44:47], v[68:71], a[64:67], 0\n"
    "v_fma_f32 v134, v134, v241, v190\n"
    "v_fma_f32 v135, v135, v241, v191\n"
    "v_mfma_f32_16x16x32_bf16 v[40:43], v[80:83], a[68:71], v[40:43]\n"
    "v_cvt_pk_bf16_f32 v128, v128, v129\n"
    "v_mfma_f32_16x16x32_bf16 v[44:47], v[84:87], a[68:71], v[44:47]\n"
    "v_cvt_pk_bf16_f32 v129, v130, v131\n"
    "v_mfma_f32_16x16x32_bf16 v[40:43], v[96:99], a[72:75], v[40:43]\n"
    "v_cvt_pk_bf16_f32 v130, v132, v133\n"
    "v_cvt_pk_bf16_f32 v131, v134, v135\n"
    "v_mfma_f32_16x16x32_bf16 v[44:47], v[100:103], a[72:75], v[44:47]\n"
    "buffer_store_dwordx4 v[128:131], v151, s[16:19], s48 offen offset:64 sc0 sc1\n"
    "v_mfma_f32_16x16x32_bf16 v[40:43], v[112:115], a[76:79], v[40:43]\n"
    "s_add_u32 s48, s58, 0\n"
    "v_fma_f32 v32, v32, v240, v160\n"
    "v_mfma_f32_16x16x32_bf16 v[44:47], v[116:119], a[76:79], v[44:47]\n"
    "v_fma_f32 v33, v33, v240, v161\n"
    "v_mfma_f32_16x16x32_bf16 v[40:43], a[0:3], a[80:83], v[40:43]\n"
    "v_fma_f32 v34, v34, v240, v162\n"
    "v_mfma_f32_16x16x32_bf16 v[44:47], a[32:35], a[80:83], v[44:47]\n"
    "v_fma_f32 v35, v35, v240, v163\n"
    "v_mfma_f32_16x16x32_bf16 v[40:43], a[8:11], a[84:87], v[40:43]\n"
    "v_fma_f32 v36, v36, v240, v164\n"
    "v_fma_f32 v37, v37, v240, v165\n"
    "v_mfma_f32_16x16x32_bf16 v[44:47], a[40:43], a[84:87], v[44:47]\n"
    "v_fma_f32 v38, v38, v240, v166\n"
    "v_mfma_f32_16x16x32_bf16 v[40:43], a[16:19], a[88:91], v[40:43]\n"
    "v_fma_f32 v39, v39, v240, v167\n"
    "v_mfma_f32_16x16x32_bf16 v[44:47], a[48:51], a[88:91], v[44:47]\n"
    "v_cvt_pk_bf16_f32 v32, v32, v33\n"
    "v_cvt_pk_bf16_f32 v33, v34, v35\n"
    "v_mfma_f32_16x16x32_bf16 v[40:43], a[24:27], a[92:95], v[40:43]\n"
    "v_cvt_pk_bf16_f32 v34, v36, v37\n"
    "v_cvt_pk_bf16_f32 v35, v38, v39\n"
    "v_mfma_f32_16x16x32_bf16 v[44:47], a[56:59], a[92:95], v[44:47]\n"
    "buffer_store_dwordx4 v[32:35], v151, s[16:19], s48 offen offset:0 sc0 sc1\n"
    "s_nop 2\n"
    "v_fma_f32 v40, v40, v240, v168\n"
    "v_fma_f32 v41, v41, v240, v169\n"
    "v_fma_f32 v42, v42, v240, v170\n"
    "v_fma_f32 v43, v43, v240, v171\n"
    "v_fma_f32 v44, v44, v240, v172\n"
    "v_fma_f32 v45, v45, v240, v173\n"
    "v_fma_f32 v46, v46, v240, v174\n"
    "v_fma_f32 v47, v47, v240, v175\n"
    "v_cvt_pk_bf16_f32 v40, v40, v41\n"
    "v_cvt_pk_bf16_f32 v41, v42, v43\n"
    "v_cvt_pk_bf16_f32 v42, v44, v45\n"
    "v_cvt_pk_bf16_f32 v43, v46, v47\n"
    "buffer_store_dwordx4 v[40:43], v151, s[16:19], s48 offen offset:64 sc0 sc1\n"
    "s_endpgm\n"
    ".Lat_wpoll:\n"
    "s_mov_b32 s53, 0x400000\n"
    ".Lat_wpoll_l:\n"
    "s_sleep 1\n"
    "buffer_load_dword v255, v254, s[8:11], s45 offen offset:0 sc1\n"
    "s_waitcnt vmcnt(0)\n"
    "v_cmp_ne_u32_e32 vcc, s39, v255\n"
    "s_sub_u32 s53, s53, 1\n"
    "s_cbranch_scc1 .Lat_wpoll_ret\n"
    "s_nop 2\n"
    "s_cbranch_vccnz .Lat_wpoll_l\n"
    "s_branch .Lat_wpoll_ret\n"
    ".Lat_xpoll:\n"
    "s_mov_b32 s53, 0x400000\n"
    ".Lat_xpoll_l:\n"
    "s_sleep 0\n"
    "buffer_load_dword v146, v145, s[8:11], s47 offen offset:128 sc1\n"
    "s_waitcnt vmcnt(0)\n"
    "v_cmp_ne_u32_e32 vcc, s39, v146\n"
    "s_sub_u32 s53, s53, 1\n"
    "s_cbranch_scc1 .Lat_xpoll_ret\n"
    "s_nop 2\n"
    "s_cbranch_vccnz .Lat_xpoll_l\n"
    "s_branch .Lat_xpoll_ret\n"
    ".Lat_ypoll:\n"
    "s_mov_b32 s53, 0x400000\n"
    ".Lat_ypoll_l:\n"
    "s_sleep 0\n"
    "buffer_load_dword v144, v145, s[8:11], s52 offen offset:0 sc1\n"
    "s_waitcnt vmcnt(0)\n"
    "v_cmp_ne_u32_e32 vcc, s39, v144\n"
    "s_sub_u32 s53, s53, 1\n"
    "s_cbranch_scc1 .Lat_ypoll_ret\n"
    "s_nop 2\n"
    "s_cbranch_vccnz .Lat_ypoll_l\n"
    "s_branch .Lat_ypoll_ret\n"
    ".Lat_apoll:\n"
    "s_mov_b32 s53, 0x400000\n"
    ".Lat_apoll_l:\n"
    "s_sleep 1\n"
    "buffer_load_dword v255, v252, s[8:11], 0 offen offset:0 sc1\n"
    "s_waitcnt vmcnt(0)\n"
    "v_cmp_ne_u32_e32 vcc, s39, v255\n"
    "s_sub_u32 s53, s53, 1\n"
    "s_cbranch_scc1 .Lat_apoll_ret\n"
    "s_nop 2\n"
    "s_cbranch_vccnz .Lat_apoll_l\n"
    "s_branch .Lat_apoll_ret\n"
    ".Lat_h1poll:\n"
    "s_mov_b32 s53, 0x400000\n"
    ".Lat_h1poll_l:\n"
    "s_sleep 1\n"
    "buffer_load_dword v0, v3, s[8:11], s59 offen offset:0 sc1\n"
    "s_waitcnt vmcnt(0)\n"
    "v_cmp_ne_u32_e32 vcc, s39, v0\n"
    "s_sub_u32 s53, s53, 1\n"
    "s_cbranch_scc1 .Lat_h1poll_ret\n"
    "s_nop 2\n"
    "s_cbranch_vccnz .Lat_h1poll_l\n"
    "s_branch .Lat_h1poll_ret\n"
    ".Lat_hpoll:\n"
    "s_mov_b32 s53, 0x400000\n"
    ".Lat_hpoll_l:\n"
    "s_sleep 1\n"
    "buffer_load_dword v0, v253, s[8:11], s59 offen offset:0 sc1\n"
    "buffer_load_dword v1, v2, s[8:11], s59 offen offset:0 sc1\n"
    "s_waitcnt vmcnt(0)\n"
    "v_xor_b32_e32 v1, s39, v1\n"
    "v_xor_b32_e32 v0, s39, v0\n"
    "v_or_b32_e32 v0, v0, v1\n"
    "v_cmp_ne_u32_e32 vcc, 0, v0\n"
    "s_sub_u32 s53, s53, 1\n"
    "s_cbranch_scc1 .Lat_hpoll_ret\n"
    "s_nop 2\n"
    "s_cbranch_vccnz .Lat_hpoll_l\n"
    "s_branch .Lat_hpoll_ret\n"
    ".Lat_qpoll:\n"
    "s_mov_b32 s53, 0x400000\n"
    ".Lat_qpoll_l:\n"
    "s_sleep 0\n"
    "buffer_load_dword v11, v5, s[8:11], s47 offen offset:0 sc1\n"
    "s_waitcnt vmcnt(0)\n"
    "v_cmp_ne_u32_e32 vcc, s39, v11\n"
    "s_sub_u32 s53, s53, 1\n"
    "s_cbranch_scc1 .Lat_qpoll_ret\n"
    "s_nop 2\n"
    "s_cbranch_vccnz .Lat_qpoll_l\n"
    "s_branch .Lat_qpoll_ret\n"
    ".Lat_lpoll:\n"
    "s_mov_b32 s53, 0x400000\n"
    ".Lat_lpoll_l:\n"
    "s_sleep 1\n"
    "buffer_load_dword v154, off, s[8:11], s80 sc1\n"
    "s_waitcnt vmcnt(0)\n"
    "v_cmp_ne_u32_e32 vcc, s39, v154\n"
    "s_sub_u32 s53, s53, 1\n"
    "s_cbranch_scc1 .Lat_lpoll_ret\n"
    "s_nop 2\n"
    "s_cbranch_vccnz .Lat_lpoll_l\n"
    ".Lat_lpoll_ret:\n"
    "v_lshlrev_b32_e32 v253, 2, v249\n"
    "s_mov_b32 m0, s82\n"
    "s_nop 0\n"
    "buffer_load_dwordx4 v253, s[8:11], s81 offen offset:0 sc1 lds\n"
    "s_waitcnt vmcnt(0)\n"
    "s_branch .Lat_g\n"
    ".Lat_exit:\n"
    "s_waitcnt vmcnt(0)\n"
    "s_nop 15\n"
    ".Lat_end:\n"
    "s_waitcnt lgkmcnt(0)\n"
    : "+s"(ka), "+s"(b), "+s"(wid) : "s"((unsigned)(size_t)lds)
    : "v0", "v1", "v2", "v3", "v4", "v5", "v6", "v7", "v8", "v9", "v10", "v11", "v12", "v13", "v14", "v15", "v16", "v17", "v18", "v19", "v20", "v21", "v22", "v23", "v24", "v25", "v26", "v27", "v28", "v29", "v30", "v31", "v32", "v33", "v34", "v35", "v36", "v37", "v38", "v39", "v40", "v41", "v42", "v43", "v44", "v45", "v46", "v47", "v48", "v49", "v50", "v51", "v52", "v53", "v54", "v55", "v56", "v57", "v58", "v59", "v60", "v61", "v62", "v63", "v64", "v65", "v66", "v67", "v68", "v69", "v70", "v71", "v72", "v73", "v74", "v75", "v76", "v77", "v78", "v79", "v80", "v81", "v82", "v83", "v84", "v85", "v86", "v87", "v88", "v89", "v90", "v91", "v92", "v93", "v94", "v95", "v96", "v97", "v98", "v99", "v100", "v101", "v102", "v103", "v104", "v105", "v106", "v107", "v108", "v109", "v110", "v111", "v112", "v113", "v114", "v115", "v116", "v117", "v118", "v119", "v120", "v121", "v122", "v123", "v124", "v125", "v126", "v127", "v128", "v129", "v130", "v131", "v132", "v133", "v134", "v135", "v136", "v137", "v138", "v139", "v140", "v141", "v142", "v143", "v144", "v145", "v146", "v147", "v148", "v149", "v150", "v151", "v152", "v153", "v154", "v155", "v156", "v157", "v158", "v159", "v160", "v161", "v162", "v163", "v164", "v165", "v166", "v167", "v168", "v169", "v170", "v171", "v172", "v173", "v174", "v175", "v176", "v177", "v178", "v179", "v180", "v181", "v182", "v183", "v184", "v185", "v186", "v187", "v188", "v189", "v190", "v191", "v192", "v193", "v194", "v195", "v196", "v197", "v198", "v199", "v200", "v201", "v202", "v203", "v204", "v205", "v206", "v207", "v208", "v209", "v210", "v211", "v212", "v213", "v214", "v215", "v216", "v217", "v218", "v219", "v220", "v221", "v222", "v223", "v224", "v225", "v226", "v227", "v228", "v229", "v230", "v231", "v232", "v233", "v234", "v235", "v236", "v237", "v238", "v239", "v240", "v241", "v242", "v243", "v244", "v245", "v246", "v247", "v248", "v249", "v250", "v251", "v252", "v253", "v254", "v255", "a0", "a1", "a2", "a3", "a4", "a5", "a6", "a7", "a8", "a9", "a10", "a11", "a12", "a13", "a14", "a15", "a16", "a17", "a18", "a19", "a20", "a21", "a22", "a23", "a24", "a25", "a26", "a27", "a28", "a29", "a30", "a31", "a32", "a33", "a34", "a35", "a36", "a37", "a38", "a39", "a40", "a41", "a42", "a43", "a44", "a45", "a46", "a47", "a48", "a49", "a50", "a51", "a52", "a53", "a54", "a55", "a56", "a57", "a58", "a59", "a60", "a61", "a62", "a63", "a64", "a65", "a66", "a67", "a68", "a69", "a70", "a71", "a72", "a73", "a74", "a75", "a76", "a77", "a78", "a79", "a80", "a81", "a82", "a83", "a84", "a85", "a86", "a87", "a88", "a89", "a90", "a91", "a92", "a93", "a94", "a95", "a96", "a97", "a98", "a99", "a100", "a101", "a102", "a103", "a104", "a105", "a106", "a107", "a108", "a109", "a110", "a111", "a112", "a113", "a114", "a115", "a116", "a117", "a118", "a119", "a120", "a121", "a122", "a123", "a124", "a125", "a126", "a127", "a128", "a129", "a130", "a131", "a132", "a133", "a134", "a135", "a136", "a137", "a138", "a139", "a140", "a141", "a142", "a143", "a144", "a145", "a146", "a147", "a148", "a149", "a150", "a151", "a152", "a153", "a154", "a155", "a156", "a157", "a158", "a159", "a160", "a161", "a162", "a163", "a164", "a165", "a166", "a167", "a168", "a169", "a170", "a171", "a172", "a173", "a174", "a175", "a176", "a177", "a178", "a179", "a180", "a181", "a182", "a183", "a184", "a185", "a186", "a187", "a188", "a189", "a190", "a191", "a192", "a193", "a194", "a195", "a196", "a197", "a198", "a199", "a200", "a201", "a202", "a203", "a204", "a205", "a206", "a207", "a208", "a209", "a210", "a211", "a212", "a213", "a214", "a215", "a216", "a217", "a218", "a219", "a220", "a221", "a222", "a223", "a224", "a225", "a226", "a227", "a228", "a229", "a230", "a231", "a232", "a233", "a234", "a235", "a236", "a237", "a238", "a239", "a240", "a241", "a242", "a243", "a244", "a245", "a246", "a247", "a248", "a249", "a250", "a251", "a252", "a253", "a254", "a255", "s8", "s9", "s10", "s11", "s12", "s13", "s14", "s15", "s16", "s17", "s18", "s19", "s20", "s21", "s22", "s23", "s24", "s25", "s26", "s27", "s28", "s29", "s30", "s31", "s32", "s33", "s34", "s35", "s36", "s37", "s38", "s39", "s40", "s41", "s42", "s43", "s44", "s45", "s46", "s47", "s48", "s49", "s50", "s51", "s52", "s53", "s54", "s55", "s56", "s57", "s58", "s59", "s60", "s61", "s62", "s63", "s80", "s81", "s82", "s83", "s84", "s85", "s86", "s87", "s88", "s89", "s90", "vcc", "m0", "scc", "memory");
  typedef __attribute__((address_space(1))) char* __attribute__((address_space(4))) const* arg_t;
  const arg_t ap = (arg_t)ka;
  typedef const u16* cu16;
  if (b >= NATT) list_branch(lds, b - NATT, wid, (const int*)ap[1], (const int*)ap[2], (cu16)ap[3], (cu16)ap[8], (cu16)ap[9], (cu16)ap[10], (char*)ap[0]);
  else exact_rows(lds, b, wid, (char*)ap[0], (cu16)ap[4], (u16*)ap[5]);
}

extern "C" void kernel_launch(void* const* d_in, const int* in_sizes, int n_in, void* d_out, int out_size, void* d_ws,
                              size_t ws_size, hipStream_t stream) {
  const u16* x = (const u16*)d_in[0];
  const int* src = (const int*)d_in[1];
  const int* dst = (const int*)d_in[2];
  const u16* deg_inv = (const u16*)d_in[3];
  const u16* wmsg = (const u16*)d_in[4];
  const u16* wq = (const u16*)d_in[5];
  const u16* wk = (const u16*)d_in[6];
  const u16* wv = (const u16*)d_in[7];
  const u16* wout = (const u16*)d_in[8];
  char* ws = (char*)d_ws;
  (void)hipFuncSetAttribute((const void*)attn_kernel, hipFuncAttributeMaxDynamicSharedMemorySize, 163840);
  attn_kernel<<<256, 256, 163840, stream>>>(ws, src, dst, deg_inv, x, (u16*)d_out, wq, wk, wv, wmsg, wout);
}
```

```cpp
#include <hip/hip_runtime.h>
#include <hip/hip_bf16.h>
#include <stdint.h>
#pragma clang diagnostic ignored "-Winline-asm"

typedef unsigned short u16;
typedef __attribute__((ext_vector_type(8))) short bf16x8;
typedef __attribute__((ext_vector_type(4))) short bf16x4;
typedef __attribute__((ext_vector_type(4))) float f32x4;
typedef __attribute__((ext_vector_type(2))) float f32x2;
typedef __attribute__((ext_vector_type(4))) unsigned u32x4;
typedef __attribute__((ext_vector_type(2))) unsigned u32x2;
#define DI __device__ __forceinline__

constexpr int NN = 10000, DD = 256, EE = 160000;
constexpr int NATT = 252, NSP = 4;

constexpr unsigned WS_H = 0;
constexpr unsigned WS_AF = WS_H + 10240 * 512;
constexpr unsigned WS_WV = WS_AF + 131072;
constexpr unsigned WS_WM = WS_WV + 131072;
constexpr unsigned WS_XF = WS_WM + 131072;
constexpr unsigned WS_HF = WS_XF + 256 * 256;
constexpr unsigned WS_BF = WS_HF + 4096;
constexpr unsigned WS_PF = WS_BF + 65536;
constexpr unsigned WS_WF = WS_PF + 4096;
constexpr unsigned WS_LF = WS_WF + 256;
constexpr unsigned WS_EP = WS_LF + 2048;
constexpr unsigned WS_G = WS_EP + 1792;
constexpr unsigned WS_QP = WS_G + 10240 * 512;
constexpr unsigned WS_XB = WS_QP + 10240 * 512;
constexpr unsigned WS_BL = WS_XB + NATT * 3 * 24832;
constexpr unsigned WS_GL = WS_BL + 16 * 5 * 260 * 64;
constexpr unsigned WS_DBG = WS_GL + 256 * 4096;
constexpr unsigned WS_END = WS_DBG + 1024 * 128 + 8192;

DI float bf2f(unsigned x) { return __uint_as_float(x << 16); }
DI float bfhi(unsigned x) { return __uint_as_float(x & 0xffff0000u); }
DI unsigned pk2(float lo, float hi) {
  typedef __attribute__((ext_vector_type(2))) __bf16 bf2_t;
  const bf2_t r = __builtin_convertvector((f32x2){lo, hi}, bf2_t);
  return __builtin_bit_cast(unsigned, r);
}
typedef __attribute__((address_space(3))) bf16x4* lds_bf16x4_p;
DI bf16x4 tr_read(unsigned p) { return __builtin_amdgcn_ds_read_tr16_b64_v4i16((lds_bf16x4_p)p); }
DI bf16x8 as_bf(u32x4 v) { return __builtin_bit_cast(bf16x8, v); }
#define SB() __builtin_amdgcn_sched_barrier(0)
#define MFMA16(a, b, c) __builtin_amdgcn_mfma_f32_16x16x32_bf16(a, b, c, 0, 0, 0)

typedef __amdgpu_buffer_rsrc_t srd_t;
DI void drain() { asm volatile("s_waitcnt vmcnt(0)" ::: "memory"); }
DI void st_flag(srd_t w, unsigned off, unsigned tag) { __builtin_amdgcn_raw_buffer_store_b32(tag, w, off, 0, 16); }
DI void wait_flag(srd_t w, unsigned off, unsigned tag) {
#pragma unroll 1
  for (int it = 0; it < (1 << 22); ++it) {
    unsigned v;
    asm volatile("buffer_load_dword %0, %1, %2, 0 offen sc1\n s_waitcnt vmcnt(0)" : "=v"(v) : "v"(off), "s"(w) : "memory");
    if (__ballot(v != tag) == 0) break;
    __builtin_amdgcn_s_sleep(2);
    asm volatile("" ::: "memory");
  }
  asm volatile("" ::: "memory");
}

DI void prod_store(const f32x4 (&acc)[2][2], int mat, int ks, int J, int lane, srd_t wsrd) {
#pragma unroll
  for (int s = 0; s < 2; ++s) {
    u32x4 o = {pk2(acc[0][s][0], acc[0][s][1]), pk2(acc[0][s][2], acc[0][s][3]), pk2(acc[1][s][0], acc[1][s][1]), pk2(acc[1][s][2], acc[1][s][3])};
    if (ks == 7 && lane >= 48) o[3] &= 0xffffu;
    const int T = 2 * J + s;
    const int at = mat ? 131072 + ((J & 1) ^ 1) * 16384 + ks * 2048 + s * 1024 : ks * 4096 + (T & 3) * 1024;
    __builtin_amdgcn_raw_buffer_store_b128(o, wsrd, WS_WV + (T >> 2) * 32768 + at + lane * 16, 0, 16);
  }
}
DI void corr(unsigned* dl, const unsigned (&lr)[16]) {
  float a0 = 0.f, a1 = 0.f;
#pragma unroll
  for (int r = 0; r < 16; ++r) { a0 += bf2f(lr[r] & 0xffffu); a1 += bfhi(lr[r]); }
  dl[0] = pk2(a0 * -0.0625f, a1 * -0.0625f);
  dl[128] = pk2((bf2f(lr[15] & 0xffffu) - 0.25f * a0) * (1.f / 3.f), (bfhi(lr[15]) - 0.25f * a1) * (1.f / 3.f));
}
DI void product_load(char* lds, int wid, int lane, int job, const u16* __restrict__ wv, const u16* __restrict__ wm, const u16* __restrict__ wo, bool left, bool right) {
  const unsigned ldsb = (unsigned)(size_t)lds;
  const int r = lane >> 3, sw = ((lane & 7) ^ (r & 6)) * 16 + wid * 128;
  const unsigned vp = (8 * (r >> 2) + (r & 3)) * 512 + sw, vn = r * 512 + sw;
  auto image = [&](int n, const u16* w, int row0, bool nat) {
#pragma unroll
    for (int i = 0; i < 8; ++i) {
      const int a = i & 3;
      asm volatile("s_mov_b32 m0, %2\n s_nop 0\n global_load_lds_dwordx4 %0, %1"
                   :: "v"(nat ? vn : vp), "s"(w + (row0 + (i >> 2) * 32 + (nat ? 8 * a : 16 * (a & 1) + 4 * (a >> 1))) * DD),
                      "s"(ldsb + n * 32768 + (i >> 2) * 16384 + wid * 4096 + a * 1024) : "m0", "memory");
    }
  };
  if (left) {
    image(2, wv, (job >> 2) * 64, true);
    image(3, wm, (job >> 2) * 64, false);
  }
  if (right)
#pragma unroll
  for (int k = 0; k < 8; ++k)
    asm volatile("s_mov_b32 m0, %2\n s_nop 0\n global_load_lds_dwordx4 %0, %1"
                 :: "v"((lane >> 3) * 512 + (lane & 7) * 16), "s"(wo + (k * 32 + wid * 8) * DD + (job & 3) * 64), "s"(ldsb + k * 4096 + wid * 1024) : "m0", "memory");
}
DI void product_w(char* lds, int wid, int lane, int job, srd_t wsrd) {
  const int m = lane & 15, g = lane >> 4;
  const int ks = 2 * (job >> 2) + (wid >> 1), J = 2 * (job & 3) + (wid & 1);
  const unsigned ldsb = (unsigned)(size_t)lds;
  unsigned* dl = reinterpret_cast<unsigned*>(lds + 0x26c00);
  const bool hi = (m >> 3) != 0;
  const unsigned ra = (m >> 3) * 1024 + (m & 7) * 128 + (g ^ (m & 6)) * 16;
  auto frag = [&](int n, int tl, int t, int k) {
    return *reinterpret_cast<const bf16x8*>(lds + n * 32768 + tl * 16384 + (k >> 1) * 4096 + t * 2048 + (ra ^ ((k & 1) * 64)));
  };
  f32x4 av[2][2] = {}, am[2][2] = {}, ad[2] = {};
  const unsigned tb = ldsb + (job & 1) * 32768 + (8 * g + ((lane >> 2) & 3)) * 128 + (wid & 1) * 64 + (lane & 3) * 16;
  bf16x8 d0[8], d1[8], d2[8], rr[8][2], vf[8][2], mf[8][2];
#pragma unroll
  for (int kk = 0; kk < 9; ++kk) {
    if (kk < 8) {
      d0[kk] = *reinterpret_cast<const bf16x8*>(dl + 512 + 16 * kk + 4 * g); d1[kk] = *reinterpret_cast<const bf16x8*>(dl + 512 + (ks == 7 ? 128 : 0) + 16 * kk + 4 * g);
      d2[kk] = *reinterpret_cast<const bf16x8*>(dl + 768 + (ks == 7 && hi ? 128 : 0) + 16 * kk + 4 * g);
#pragma unroll
      for (int s = 0; s < 2; ++s) {
        rr[kk][s] = __builtin_shufflevector(tr_read(tb + kk * 4096 + s * 8), tr_read(tb + kk * 4096 + s * 8 + 512), 0, 1, 2, 3, 4, 5, 6, 7);
        vf[kk][s] = frag(2, wid >> 1, s, kk); mf[kk][s] = frag(3, wid >> 1, s, kk);
      }
    }
    SB();
    if (kk) {
      const int k = kk - 1;
#pragma unroll
      for (int s = 0; s < 2; ++s) {
        av[0][s] = MFMA16(d0[k], rr[k][s], av[0][s]); av[1][s] = MFMA16(d1[k], rr[k][s], av[1][s]); ad[s] = MFMA16(d2[k], rr[k][s], ad[s]);
#pragma unroll
        for (int t = 0; t < 2; ++t) { av[t][s] = MFMA16(vf[k][t], rr[k][s], av[t][s]); am[t][s] = MFMA16(mf[k][t], rr[k][s], am[t][s]); }
      }
    }
    SB();
  }
#pragma unroll
  for (int t = 0; t < 2; ++t)
#pragma unroll
    for (int s = 0; s < 2; ++s) am[t][s] += ad[s];
  prod_store(av, 0, ks, J, lane, wsrd);
  prod_store(am, 1, ks, J, lane, wsrd);
}

constexpr unsigned CODE_BYTES = 93440;
DI void code_touch(unsigned i, unsigned ldsdst) {
  unsigned long long a; int o_;
  asm volatile("s_getpc_b64 %0\n s_mov_b32 %1, _Z11attn_kernelPcPKiS1_PKtS3_PtS3_S3_S3_S3_S3_@rel32@lo+4" : "=&s"(a), "=&s"(o_));
  a += (long long)o_;
  asm volatile("s_mov_b32 m0, %2\n s_nop 0\n global_load_lds_dword %0, %1" :: "v"(min(i * 128u, CODE_BYTES - 4u)), "s"(a), "s"(ldsdst) : "m0", "memory");
}

constexpr int LONGS = 1056;
typedef __attribute__((address_space(3))) unsigned* lds_u32_p;
typedef __attribute__((address_space(3))) u16* lds_u16_p;
DI void local_store(srd_t srd, int node, int lane, f32x4 acc, float di) {
  const int n = lane & 15, g = lane >> 4;
  const u32x2 o = {pk2(acc[0] * di, acc[1] * di), pk2(acc[2] * di, acc[3] * di)};
  __builtin_amdgcn_raw_buffer_store_b64(o, srd, WS_G + node * 512 + (g >> 1) * 256 + n * 16 + (g & 1) * 8, 0, 16);
}
DI void list_branch(char* lds, int j, int wid, const int* __restrict__ src, const int* __restrict__ dst, const u16* __restrict__ deg_inv,
                    const u16* wv, const u16* wm, const u16* wo, char* ws) {
  const unsigned rec = (unsigned)(size_t)lds + wid * 33280, ext = (unsigned)(size_t)lds + 0x20800 + wid * 6912;
  int lane; asm volatile("v_mbcnt_lo_u32_b32 %0, -1, 0\n\tv_mbcnt_hi_u32_b32 %0, -1, %0" : "=v"(lane));
  const int tid = wid * 64 + lane, bin = j * 4 + wid, lo = bin * 640;
  code_touch(CODE_BYTES / 128 - tid, (unsigned)(size_t)lds + 0x27f00u);
  const srd_t msrd = __builtin_amdgcn_make_buffer_rsrc(ws + WS_H, 0, 10240 * 512, 0x00020000);
  const srd_t wsrd = __builtin_amdgcn_make_buffer_rsrc(ws, 0, WS_END, 0x00020000);
  const unsigned tag = __builtin_amdgcn_raw_buffer_load_b32(wsrd, WS_EP, 0, 0) + 2u;
  if (lane < 3) st_flag(wsrd, lane == 2 ? WS_WF + 32 : WS_LF + bin * 128 + lane * 64 + 8, 0);
  unsigned lr[16];
#pragma unroll
  for (int r = 0; r < 16; ++r) lr[r] = reinterpret_cast<const unsigned*>((wid >= 2 ? wm : wv) + (240 + r) * DD)[tid & 127];
  unsigned dv[10];
#pragma unroll
  for (int i = 0; i < 10; ++i) dv[i] = (unsigned)deg_inv[min(lo + 64 * i + lane, NN - 1)];
#pragma unroll
  for (int i = 0; i < 10; ++i) ((lds_u32_p)ext)[64 * i + lane] = dv[i] << 16 | (lo + 64 * i + lane < NN ? lo + 64 * i + lane : 0xffff);
  corr(reinterpret_cast<unsigned*>(lds + 0x26c00) + 512 + (wid >> 1) * 256 + (tid & 127), lr);
  const u32x4 none = {~0u, ~0u, ~0u, ~0u};
  auto clear = [&]() {
    unsigned a = rec + lane * 16, r = rec + lane * 52;
    asm volatile("" : "+v"(a), "+v"(r));
#pragma unroll
    for (int i = 0; i < 33; ++i) *(__attribute__((address_space(3))) u32x4*)(a + (i < 32 ? i * 1024 : 32256)) = none;
#pragma unroll
    for (int i = 0; i < 10; ++i) ((lds_u32_p)(r + 64 * 52 * i))[12] = 46;
  };
  clear();
  __builtin_amdgcn_s_sleep(40);
  wait_flag(wsrd, WS_BF + min(tid, NATT - 1) * 128, tag);
  __syncthreads();
  auto take = [&](const u32x4& e0, const u32x4& e1, unsigned (&a)[8], unsigned (&p)[8]) {
#pragma unroll
    for (int r = 0; r < 8; ++r) asm("v_mad_u32_u16 %0, %1, 52, %2 op_sel:[1,0,0,0]" : "=v"(a[r]) : "v"(r < 4 ? e0[r & 3] : e1[r & 3]), "s"(rec));
    SB();
#pragma unroll
    for (int r = 0; r < 8; ++r) p[r] = __atomic_fetch_add((lds_u32_p)a[r] + 12, -2u, __ATOMIC_RELAXED);
    SB();
  };
  auto fill = [&](const u32x4& e0, const u32x4& e1, unsigned (&a)[8], const unsigned (&p)[8]) {
#pragma unroll
    for (int r = 7; r >= 0; --r) a[r] = __builtin_elementwise_add_sat(a[r], p[r]);
    SB();
#pragma unroll
    for (int r = 7; r >= 0; --r) *(lds_u16_p)a[r] = r < 4 ? e0[r & 3] : e1[r & 3];
    SB();
  };
  auto add8 = [&](u32x4 e0, u32x4 e1) { unsigned a[8], p[8]; take(e0, e1, a, p); fill(e0, e1, a, p); };
  const unsigned bo = WS_BL + bin * 5 * 260 * 64 + lane * 16;
  auto ld = [&](int g, int k) { return __builtin_amdgcn_raw_buffer_load_b128(wsrd, bo + (g * 260 + k * 16) * 64, 0, 16); };
  auto fix = [&](u32x4 e, unsigned last) { return e | (unsigned)(__builtin_amdgcn_mov_dpp(last, 0xff, 15, 15, 0) >> 31); };
  bool ovf = false;
  unsigned big = 0; int off[10], tot = 0;
#pragma unroll 1
  for (int pass = 0; pass < 2; ++pass) {
    if (!ovf) {
      u32x4 c[64];
#pragma unroll
      for (int k = 0; k < 16; ++k) c[k] = ld(0, k);
      unsigned m = ~0u, a[2][8], p[2][8];
#pragma unroll
      for (int k = 0; k < 66; k += 2) {
        SB();
        if (k < 64) {
          if (k >= 48) { c[k] = fix(c[k], c[k - 16][3]); c[k + 1] = fix(c[k + 1], c[k - 15][3]); }
          if ((k & 15) == 14 && lane >= 48) c[k + 1] = none;
          if (k >= 48) m &= c[k][3] & c[k + 1][3];
          take(c[k], c[k + 1], a[k >> 1 & 1], p[k >> 1 & 1]);
        }
        if (k < 16) {
#pragma unroll
          for (int i = 16 + 2 * k; i < 20 + 2 * k; ++i) c[i] = ld(i >> 4, i & 15);
        }
        if (k >= 24 && k < 40) {
#pragma unroll
          for (int i = k - 24; i < k - 22; ++i)
            c[48 + i] = __builtin_amdgcn_raw_buffer_load_b128(wsrd, bo + (780 + i * 16) * 64 + (__builtin_amdgcn_mov_dpp(c[32 + i][3], 0xff, 15, 15, 0) & 0x40000000), 0, 16);
        }
        SB();
        if (k) fill(c[k - 2], c[k - 1], a[~k >> 1 & 1], p[~k >> 1 & 1]);
      }
      SB();
      if (__ballot((lane & 3) == 3 && m != ~0u)) {
        unsigned more = 0;
#pragma unroll
        for (int k = 0; k < 16; ++k) more |= __ballot((lane & 3) == 3 && c[48 + k][3] != ~0u) ? 1u << k : 0u;
#pragma unroll 1
        for (; more; more &= more - 1) {
          const int k = __builtin_ctz(more);
          u32x4 e = ld(4, k);
          if (k == 15 && lane >= 48) e = none;
          ovf |= __ballot((lane & 3) == 3 && e[3] != ~0u) != 0;
          add8(e, none);
        }
      }
      if (ovf) clear();
    }
    if (ovf) {
      for (int q = lane; q < EE / 4; q += 64) {
        const u32x4 d = reinterpret_cast<const u32x4*>(dst)[q], s = reinterpret_cast<const u32x4*>(src)[q];
        u32x4 e;
#pragma unroll
        for (int r = 0; r < 4; ++r) e[r] = d[r] - lo < 640u ? (d[r] - lo) << 16 | s[r] : ~0u;
        add8(e, none);
      }
    }
    if (pass) break;
    asm volatile("" : "+v"(lane));
    const int qd = lane & 3, nl = lane >> 2;
    int inc = 0;
#pragma unroll
    for (int i = 0; i < 10; ++i) off[i] = 46 - (int)((lds_u32_p)(rec + (64 * i + lane) * 52))[12];
#pragma unroll
    for (int i = 0; i < 10; ++i) {
      const int cn = off[i];
      off[i] = cn > 48 ? cn : 0; inc += off[i];
      if (cn > 48) { big |= 1u << i; ((lds_u16_p)ext)[2 * (64 * i + lane)] = 0xffff; }
    }
    __builtin_amdgcn_wave_barrier();
    auto put = [&](int odd) {
#pragma unroll 2
      for (int bl = 0; bl < 4; ++bl) {
        u32x4 o[10];
#pragma unroll
        for (int T = 0; T < 10; ++T) if (((2 * T + 1) / 5 & 1) == odd) {
          const lds_u32_p r = (lds_u32_p)(rec + (bl * 160 + T * 16 + nl) * 52);
          o[T] = (u32x4){r[3 * qd], r[1 + 3 * qd], r[2 + 3 * qd], ((lds_u32_p)ext)[bl * 160 + T * 16 + nl]};
        }
#pragma unroll
        for (int T = 0; T < 10; ++T) if (((2 * T + 1) / 5 & 1) == odd) {
          const int c = (2 * T + 1) / 5, ro = 16 * (T - (5 * c >> 1)) + nl, w = c & 1 ? ro * 43 >> 9 : ro >> 3;
          __builtin_amdgcn_raw_buffer_store_b128(o[T], wsrd, WS_GL + c * 4096 + w * (c & 1 ? 256 : 512) + ro * 64 + qd * 16, (bin * 4 + bl) * 16384, 16);
        }
      }
    };
    put(1);
    asm volatile("" ::: "memory");
    put(0);
    asm volatile("s_waitcnt vmcnt(16)" ::: "memory");
    if (lane == 0) st_flag(wsrd, WS_LF + bin * 128 + 64, tag);
#pragma unroll
    for (int o = 1; o < 64; o <<= 1) { const int y = __shfl_up(inc, o); if (lane >= o) inc += y; }
    tot = __builtin_amdgcn_readlane(inc, 63);
    drain();
    if (lane == 0) st_flag(wsrd, WS_LF + bin * 128, tag);
    __syncthreads();
    int j2 = j * 4, w2 = wid, l2 = lane;
    asm volatile("" : "+s"(j2), "+s"(w2), "+v"(l2));
    product_load(lds, w2, l2, j2, wv, wm, wo, true, true);
#pragma unroll 1
    for (int job = 0; job < 4; ++job) {
      if (job) asm volatile("s_waitcnt vmcnt(4)" ::: "memory"); else drain();
      __syncthreads();
      asm volatile("" : "+s"(w2));
      if (job < 3) product_load(lds + (~job & 1) * 32768, w2, l2, j2 + job + 1, wv, wm, wo, false, true);
      product_w(lds, w2, l2, j2 + job, wsrd);
    }
    if (w2 == 0) *(lds_u32_p)(ext - 8) = 0;
    __syncthreads();
    if (tot == 0 || tot > 2 * LONGS) break;
#pragma unroll
    for (int i = 0; i < 10; ++i) {
      const unsigned r = rec + (64 * i + lane) * 52;
      ((lds_u32_p)r)[12] = off[i] ? ext + 2558 + inc - r : -2u;
      inc -= off[i]; off[i] = off[i] << 15 | inc;
    }
  }
  __syncthreads();
  const unsigned ex0 = (unsigned)(size_t)lds + 0x20800;
  const lds_u32_p lng = (lds_u32_p)(size_t)(unsigned)(size_t)lds;
#pragma unroll
  for (int i = 0; i < 10; ++i)
    if (big >> i & 1) {
      const unsigned q = __atomic_fetch_add((lds_u32_p)(ex0 - 8), 1u, __ATOMIC_RELAXED);
      lng[2 * q] = tot > 2 * LONGS ? ~0u : off[i] + wid * 6912 + 2560; lng[2 * q + 1] = (((lds_u32_p)ext)[64 * i + lane] & 0xffff0000u) | (lo + 64 * i + lane);
    }
  __syncthreads();
  const int nl_ = *(lds_u32_p)(ex0 - 8);
  bf16x8 b1;
#pragma unroll
  for (int e = 0; e < 8; ++e) b1[e] = (lane & 15) == e + ((lane >> 1) & 8) ? 0x3f80 : 0;
  const unsigned lo16 = (lane & 31) * 16;
  {
    u32x4 d[3][16]; unsigned nn[3]; int mode[3], q = wid, hf = 0;
    auto issue = [&](u32x4 (&v)[16], unsigned& n, int& md) {
      const unsigned w = __builtin_amdgcn_readfirstlane(lng[2 * q]);
      n = lng[2 * q + 1];
      const int dg = q < nl_ && w >> 16 <= 64 ? w >> 16 : 0, g = dg - hf - (lane >> 5);
      const lds_u16_p f = (lds_u16_p)(ex0 + (w & 0xffffu)) + hf + (lane >> 5);
      md = q >= nl_ ? -1 : dg == 0 ? 0 : dg - hf > 32 ? 1 : 2;
      hf = md == 1 ? 32 : 0; q += md == 1 ? 0 : 4;
      unsigned id[16];
#pragma unroll
      for (int t = 0; t < 16; ++t) id[t] = f[2 * t];
#pragma unroll
      for (int t = 0; t < 16; ++t) { asm volatile("" : "+v"(id[t])); v[t] = __builtin_amdgcn_raw_buffer_load_b128(msrd, (2 * t < g ? id[t] : 0xffffu) * 512u + lo16, 0, 16); }
    };
    issue(d[0], nn[0], mode[0]); issue(d[1], nn[1], mode[1]);
    f32x4 keep = {};
#pragma unroll 1
    for (bool go = true; go;) {
#pragma unroll
      for (int s = 0; s < 3; ++s) {
        if (mode[s] < 0) { go = false; break; }
        issue(d[(s + 2) % 3], nn[(s + 2) % 3], mode[(s + 2) % 3]); SB();
        f32x4 acc = {};
#pragma unroll
        for (int t = 0; t < 16; ++t) acc = MFMA16(b1, as_bf(d[s][t]), acc);
        local_store(wsrd, mode[s] == 2 ? nn[s] & 0xffffu : 0x200000u, lane, acc + keep, bfhi(nn[s]));
        keep = mode[s] == 1 ? acc : (f32x4){0.f, 0.f, 0.f, 0.f}; SB();
      }
    }
  }
#pragma unroll 1
  for (int q0 = 0; q0 < nl_; q0 += 256)
#pragma unroll 1
  for (unsigned long long m = __ballot(q0 + 4 * lane + 3 - wid < nl_ && lng[2 * (q0 + 4 * lane + 3 - wid)] >> 16 > 64); m; m &= m - 1) {
    const int q = q0 + 4 * __builtin_ctzll(m) + 3 - wid;
    const unsigned w = __builtin_amdgcn_readfirstlane(lng[2 * q]), n = __builtin_amdgcn_readfirstlane(lng[2 * q + 1]); const int dg = w >> 16;
    f32x4 acc = {};
    if (w == ~0u) {
      for (int e0 = 0; e0 < EE; e0 += 64) {
        unsigned long long h = __ballot(dst[e0 + lane] == (n & 0xffffu));
        while (h) {
          const int e = e0 + __builtin_ctzll(h); h &= h - 1;
          const unsigned vo = lane < 32 ? (unsigned)src[e] * 512 + lo16 : 0x7fffff00u;
          acc = MFMA16(b1, as_bf(__builtin_amdgcn_raw_buffer_load_b128(msrd, vo, 0, 16)), acc);
        }
      }
    } else {
      const lds_u16_p f = (lds_u16_p)(ex0 + (w & 0xffffu)), u = (lds_u16_p)(ext + 2624 + 2 * LONGS);
      for (int x = lane; x < dg; x += 64) {
        const int key = f[x]; int p = 0;
#pragma unroll 8
        for (int t = 0; t < dg; ++t) { const int v = f[t]; p += v < key || (v == key && t < x); }
        u[p] = key;
      }
      __builtin_amdgcn_wave_barrier();
      for (int x = lane >> 5; x < dg + (lane >> 5); x += 32) {
        u32x4 d[16]; unsigned id[16]; f32x4 a2 = {};
#pragma unroll
        for (int t = 0; t < 16; ++t) id[t] = u[min(x + 2 * t, dg)];
#pragma unroll
        for (int t = 0; t < 16; ++t) { asm volatile("" : "+v"(id[t])); d[t] = __builtin_amdgcn_raw_buffer_load_b128(msrd, (x + 2 * t < dg ? id[t] : 0xffffu) * 512u + lo16, 0, 16); }
#pragma unroll
        for (int t = 0; t < 16; ++t) a2 = MFMA16(b1, as_bf(d[t]), a2);
        acc += a2;
      }
      __builtin_amdgcn_wave_barrier();
    }
    local_store(wsrd, n & 0xffffu, lane, acc, bfhi(n));
  }
  drain();
  __syncthreads();
  if (wid == 0 && lane == 0) st_flag(wsrd, WS_WF + j * 4, tag);
}

DI void exact_rows(char* lds, int b, int wid, char* ws, const u16* x, u16* out) {
  int lane; asm volatile("v_mbcnt_lo_u32_b32 %0, -1, 0\n\tv_mbcnt_hi_u32_b32 %0, -1, %0" : "=v"(lane));
  const srd_t w = __builtin_amdgcn_make_buffer_rsrc(ws, 0, WS_END, 0x00020000);
  const srd_t xs = __builtin_amdgcn_make_buffer_rsrc(const_cast<u16*>(x), 0, NN * 512, 0x00020000);
  const srd_t os = __builtin_amdgcn_make_buffer_rsrc(out, 0, NN * 512, 0x00020000);
  const int n = lane & 15, g = lane >> 4, c = b & 3;
  f32x4* red = reinterpret_cast<f32x4*>(lds);
  float* mx = reinterpret_cast<float*>(lds + 65536);
  auto ld = [&](unsigned off) { return as_bf(__builtin_amdgcn_raw_buffer_load_b128(w, off, 0, 16)); };
#pragma unroll 1
  for (int i = 0; i < 2 + (c & 1); ++i) {
    const int tile = (b >> 2) * 10 + (5 * c >> 1) + i;
    bf16x8 q[8];
#pragma unroll
    for (int ks = 0; ks < 8; ++ks) q[ks] = ld(WS_QP + tile * 8192 + ks * 1024 + lane * 16);
    auto score = [&](int kt) {
      f32x4 s = {};
#pragma unroll
      for (int ks = 0; ks < 8; ++ks) s = MFMA16(ld(WS_H + (kt * 16 + n) * 512 + ks * 64 + g * 16), q[ks], s);
      return s;
    };
    float m = -__builtin_inff();
#pragma unroll 1
    for (int kt = wid; kt < NN / 16; kt += 4) { const f32x4 s = score(kt); m = fmaxf(fmaxf(m, fmaxf(s[0], s[1])), fmaxf(s[2], s[3])); }
    __syncthreads();
    mx[wid * 64 + lane] = m;
    __syncthreads();
#pragma unroll
    for (int r = 0; r < 16; ++r) m = fmaxf(m, mx[r * 16 + n]);
    f32x4 acc[16] = {};
#pragma unroll 1
    for (int kt = wid; kt < NN / 16; kt += 4) {
      const f32x4 s = score(kt);
      const u32x2 p = {pk2(__builtin_amdgcn_exp2f(s[0] - m), __builtin_amdgcn_exp2f(s[1] - m)), pk2(__builtin_amdgcn_exp2f(s[2] - m), __builtin_amdgcn_exp2f(s[3] - m))};
#pragma unroll
      for (int dt = 0; dt < 16; ++dt) {
        bf16x4 v;
#pragma unroll
        for (int e = 0; e < 4; ++e) v[e] = __builtin_amdgcn_raw_buffer_load_b16(w, WS_H + (kt * 16 + 4 * g + e) * 512 + n * 2, dt * 32, 16);
        acc[dt] = __builtin_amdgcn_mfma_f32_16x16x16bf16_1k(v, __builtin_bit_cast(bf16x4, p), acc[dt], 0, 0, 0);
      }
    }
#pragma unroll
    for (int dt = 0; dt < 16; ++dt) red[(wid * 16 + dt) * 64 + lane] = acc[dt];
    __syncthreads();
    float l = 0.f;
#pragma unroll
    for (int r = 0; r < 4; ++r) l += red[(r * 16 + 15) * 64 + 48 + n][3];
    l = 1.f / l;
    f32x4 y[4] = {};
#pragma unroll 1
    for (int ks = 0; ks < 8; ++ks) {
      f32x4 o[2];
#pragma unroll
      for (int h = 0; h < 2; ++h) o[h] = (red[(2 * ks + h) * 64 + lane] + red[(16 + 2 * ks + h) * 64 + lane] + red[(32 + 2 * ks + h) * 64 + lane] + red[(48 + 2 * ks + h) * 64 + lane]) * l;
      const bf16x8 of = as_bf((u32x4){pk2(o[0][0], o[0][1]), pk2(o[0][2], o[0][3]), pk2(o[1][0], o[1][1]), pk2(o[1][2], o[1][3])});
      const bf16x8 gf = ld(WS_G + (tile * 16 + n) * 512 + ks * 64 + g * 16);
#pragma unroll
      for (int et = 0; et < 4; ++et) {
        y[et] = MFMA16(ld(WS_WV + wid * 32768 + ks * 4096 + et * 1024 + lane * 16), of, y[et]);
        y[et] = MFMA16(ld(WS_WM + wid * 32768 + (et < 2 ? 16384 : 0) + ks * 2048 + (et & 1) * 1024 + lane * 16), gf, y[et]);
      }
    }
#pragma unroll
    for (int jj = 0; jj < 2; ++jj) {
      const unsigned at = (tile * 16 + n) * 512 + wid * 128 + jj * 64 + g * 16;
      const u32x4 xv = __builtin_amdgcn_raw_buffer_load_b128(xs, at, 0, 0);
      u32x4 o;
#pragma unroll
      for (int r = 0; r < 4; ++r) o[r] = pk2(y[2 * jj + (r >> 1)][2 * (r & 1)] + bf2f(xv[r] & 0xffffu), y[2 * jj + (r >> 1)][2 * (r & 1) + 1] + bfhi(xv[r]));
      __builtin_amdgcn_raw_buffer_store_b128(o, os, at, 0, 0);
    }
  }
}

__global__ __launch_bounds__(256, 1) void attn_kernel(char* ws, const int* __restrict__ src, const int* __restrict__ dst,
                                                      const u16* __restrict__ deg_inv, const u16* __restrict__ x, u16* out,
                                                      const u16* __restrict__ wq, const u16* __restrict__ wk, const u16* __restrict__ wv,
                                                      const u16* __restrict__ wm, const u16* __restrict__ wo) {
  extern __shared__ __attribute__((aligned(16))) char lds[];
  static_assert(CODE_BYTES > 65536 + 256, "range of the first cache warm-up");
  int b = blockIdx.x;
  int wid = __builtin_amdgcn_readfirstlane(threadIdx.x >> 6);
  static_assert(WS_END == 37557248 && WS_EP == 5777664, "workspace layout of the generated code");
  auto ka = __builtin_amdgcn_kernarg_segment_ptr();
  asm volatile(
    "s_load_dwordx2 s[20:21], %0, 0x30\n"
    "s_load_dwordx2 s[24:25], %0, 0x38\n"
    "s_load_dwordx2 s[12:13], %0, 0x20\n"
    "s_load_dwordx2 s[8:9], %0, 0x0\n"
    "s_load_dwordx2 s[16:17], %0, 0x28\n"
    "s_load_dwordx4 s[28:31], %0, 0x8\n"
    "v_mbcnt_lo_u32_b32 v24, -1, 0\n"
    "v_mbcnt_hi_u32_b32 v24, -1, v24\n"
    "s_and_b32 s45, %1, 6\n"
    "s_lshl_b32 s46, s45, 5\n"
    "s_lshr_b32 s47, %1, 3\n"
    "s_getpc_b64 s[62:63]\n"
    "s_lshl1_add_u32 s46, s47, s46\n"
    "s_and_b32 s47, s47, 1\n"
    "s_lshl2_add_u32 s47, s47, %2\n"
    "s_lshl_b32 s47, s47, 13\n"
    "s_add_u32 m0, %3, 0x27f00\n"
    "v_lshl_add_u32 v29, v24, 7, s47\n"
    "global_load_lds_dword v29, s[62:63]\n"
    "s_add_u32 s48, s45, 2\n"
    "s_lshr_b32 s48, s48, 2\n"
    "s_lshl_b32 s48, s48, 1\n"
    "s_sub_u32 s46, s46, s48\n"
    "s_add_u32 s48, s48, 252\n"
    "s_and_b32 s45, %1, 250\n"
    "s_cmpk_eq_u32 s45, 248\n"
    "s_cselect_b32 s46, s48, s46\n"
    "s_and_b32 %1, %1, 1\n"
    "s_or_b32 %1, %1, s46\n"
    "s_cmpk_ge_u32 %1, 252\n"
    "s_cbranch_scc1 .Lat_end\n"
    "s_and_b32 s50, %2, 1\n"
    "s_lshr_b32 s51, %2, 1\n"
    "v_lshrrev_b32_e32 v25, 3, v24\n"
    "v_and_b32_e32 v26, 6, v25\n"
    "v_and_b32_e32 v27, 7, v24\n"
    "v_xor_b32_e32 v26, v26, v27\n"
    "v_lshlrev_b32_e32 v26, 4, v26\n"
    "v_and_b32_e32 v27, 4, v25\n"
    "v_add_u32_e32 v27, v27, v25\n"
    "v_lshl_add_u32 v254, v27, 9, v26\n"
    "v_add_u32_e32 v255, 0x1c00, v254\n"
    "s_mov_b32 s22, 0x20000\n"
    "s_mov_b32 s23, 0x20000\n"
    "s_mov_b32 s26, 0x20000\n"
    "s_mov_b32 s27, 0x20000\n"
    "s_lshr_b32 s36, %1, 7\n"
    "s_mul_i32 s36, s36, 3\n"
    "s_add_u32 s36, s36, %1\n"
    "s_and_b32 s36, s36, 127\n"
    "s_lshl_b32 s45, %2, 7\n"
    "s_lshl_b32 s46, %2, 12\n"
    "s_add_u32 s46, s46, %3\n"
    "s_and_b32 s47, s36, 7\n"
    "s_lshl_b32 s47, s47, 14\n"
    "s_add_u32 m0, s46, 32768\n"
    "s_add_u32 s47, s47, s45\n"
    "s_and_b32 s32, %1, 3\n"
    "s_lshr_b32 s33, %1, 2\n"
    "s_mul_i32 s38, s32, 5\n"
    "s_lshr_b32 s38, s38, 1\n"
    "s_mul_i32 s35, s33, 10\n"
    "s_add_u32 s35, s35, s38\n"
    "s_lshl_b32 s35, s35, 13\n"
    "s_mov_b32 s14, 5120000\n"
    "s_mov_b32 s15, 0x20000\n"
    "s_waitcnt lgkmcnt(0)\n"
    "buffer_load_dwordx4 v254, s[20:23], s47 offen offset:0 lds\n"
    "s_add_u32 s48, s47, 1024\n"
    "buffer_load_dwordx4 v255, s[20:23], s47 offen offset:3072 lds\n"
    "s_add_u32 s47, s47, 0x1800\n"
    "buffer_load_dwordx4 v254, s[20:23], s48 offen offset:1024 lds\n"
    "buffer_load_dwordx4 v254, s[20:23], s47 offen offset:2048 lds\n"
    "s_lshr_b32 s47, s36, 4\n"
    "s_lshl_b32 s47, s47, 14\n"
    "s_and_b32 s48, s36, 8\n"
    "s_lshl_b32 s48, s48, 8\n"
    "s_add_u32 s47, s47, s48\n"
    "s_add_u32 m0, s46, 49152\n"
    "s_add_u32 s47, s47, s45\n"
    "buffer_load_dwordx4 v254, s[24:27], s47 offen offset:0 lds\n"
    "buffer_load_dwordx4 v255, s[24:27], s47 offen offset:1024 lds\n"
    "s_add_u32 s47, s45, 120832\n"
    "buffer_load_dwordx4 v254, s[24:27], s47 offen offset:2048 lds\n"
    "s_add_u32 s47, s47, 1024\n"
    "buffer_load_dwordx4 v254, s[24:27], s47 offen offset:3072 lds\n"
    "s_mul_i32 s46, %1, 159\n"
    "s_lshl_b32 s45, s50, 6\n"
    "v_add_u32_e32 v111, s45, v24\n"
    "v_add_u32_e32 v108, s46, v111\n"
    "v_min_u32_e32 v108, 39999, v108\n"
    "v_lshlrev_b32_e32 v108, 4, v108\n"
    "s_lshl1_add_u32 s47, s46, 256\n"
    "v_add_u32_e32 v109, s47, v111\n"
    "v_min_u32_e32 v109, 79999, v109\n"
    "v_lshlrev_b32_e32 v109, 3, v109\n"
    "s_cmp_lg_u32 s51, 0\n"
    "s_cselect_b64 exec, -1, 1\n"
    "global_load_dwordx4 v[96:99], v108, s[30:31]\n"
    "global_load_dwordx2 v[92:93], v109, s[30:31]\n"
    "global_load_dwordx4 v[100:103], v108, s[28:29]\n"
    "global_load_dwordx2 v[94:95], v109, s[28:29]\n"
    "s_mov_b64 exec, -1\n"
    "s_lshl_b32 s45, %2, 6\n"
    "v_add_u32_e32 v108, s45, v24\n"
    "s_sub_u32 s59, 40000, s46\n"
    "s_lshl1_add_u32 s61, s59, -256\n"
    "s_min_i32 s61, s61, 62\n"
    "s_lshl_b32 s45, %2, 2\n"
    "s_add_u32 s54, s35, 0x2000\n"
    "s_add_u32 s55, s35, 0x4000\n"
    "v_lshlrev_b32_e32 v253, 4, v24\n"
    "v_lshrrev_b32_e32 v28, 4, v24\n"
    "v_add_u32_e32 v28, s45, v28\n"
    "v_and_b32_e32 v104, 0xf0, v253\n"
    "v_lshl_add_u32 v104, v28, 9, v104\n"
    "buffer_load_dwordx4 v[0:3], v104, s[12:15], s35 offen offset:0\n"
    "buffer_load_dwordx4 v[4:7], v104, s[12:15], s35 offen offset:256\n"
    "s_bitcmp1_b32 %1, 0\n"
    "s_cselect_b32 s60, s55, 2.0\n"
    "buffer_load_dwordx4 v[8:11], v104, s[12:15], s54 offen offset:0\n"
    "buffer_load_dwordx4 v[12:15], v104, s[12:15], s54 offen offset:256\n"
    "buffer_load_dwordx4 v[16:19], v104, s[12:15], s55 offen offset:0\n"
    "buffer_load_dwordx4 v[20:23], v104, s[12:15], s55 offen offset:256\n"
    "s_mov_b32 s10, 37557248\n"
    "s_mov_b32 s11, 0x20000\n"
    "s_mov_b32 s37, 5777664\n"
    "s_buffer_load_dword s39, s[8:11], s37\n"
    "s_bfe_u32 s37, %1, 0x20001\n"
    "v_and_b32_e32 v27, 1, v25\n"
    "v_lshl_add_u32 v248, v27, 9, v26\n"
    "v_lshrrev_b32_e32 v27, 1, v25\n"
    "v_lshl_add_u32 v248, v27, 12, v248\n"
    "s_mul_i32 s40, s32, 39\n"
    "s_and_b32 s34, s32, 1\n"
    "s_add_u32 s40, s40, s34\n"
    "s_lshl1_add_u32 s40, s40, s51\n"
    "s_lshl_b32 s40, s40, 14\n"
    "s_lshl_b32 s47, s50, 8\n"
    "s_add_u32 s40, s40, s47\n"
    "s_add_u32 s41, s40, 128\n"
    "s_xor_b32 s34, s34, 1\n"
    "s_lshl_b32 s34, s34, 15\n"
    "s_xor_b32 s43, s50, s51\n"
    "s_lshl3_add_u32 s43, s50, s43\n"
    "s_lshl_b32 s43, s43, 13\n"
    "s_add_u32 s47, %3, 32768\n"
    "s_add_u32 s43, s43, s47\n"
    "v_bfe_u32 v25, v24, 2, 3\n"
    "v_lshlrev_b32_e32 v244, 7, v25\n"
    "v_lshrrev_b32_e32 v26, 5, v24\n"
    "v_lshl_add_u32 v244, v26, 10, v244\n"
    "v_and_b32_e32 v25, 6, v25\n"
    "v_bfe_u32 v26, v24, 1, 1\n"
    "v_xor_b32_e32 v25, v25, v26\n"
    "v_lshl_add_u32 v244, v25, 4, v244\n"
    "v_and_b32_e32 v26, 1, v24\n"
    "v_lshl_add_u32 v244, v26, 3, v244\n"
    "s_lshl_b32 s48, s51, 16\n"
    "s_add_u32 s48, s48, s47\n"
    "v_add_u32_e32 v244, s48, v244\n"
    "v_xor_b32_e32 v245, 32, v244\n"
    "v_xor_b32_e32 v246, 64, v244\n"
    "v_xor_b32_e32 v247, 0x60, v244\n"
    "v_and_b32_e32 v25, 7, v24\n"
    "v_lshlrev_b32_e32 v27, 7, v25\n"
    "v_bfe_u32 v26, v24, 3, 1\n"
    "v_lshl_add_u32 v27, v26, 10, v27\n"
    "v_and_b32_e32 v25, 6, v24\n"
    "v_lshrrev_b32_e32 v26, 4, v24\n"
    "v_xor_b32_e32 v25, v25, v26\n"
    "v_lshl_add_u32 v27, v25, 4, v27\n"
    "s_lshl_b32 s48, s51, 13\n"
    "s_add_u32 s48, s48, s47\n"
    "v_add_u32_e32 v240, s48, v27\n"
    "v_xor_b32_e32 v241, 64, v240\n"
    "v_and_b32_e32 v29, 15, v24\n"
    "s_cmp_ge_u32 s36, 112\n"
    "s_cselect_b32 s47, 8, 16\n"
    "v_cmp_le_u32_e32 vcc, s47, v29\n"
    "v_mov_b32_e32 v64, 0xbd80bd80\n"
    "v_mov_b32_e32 v65, 0xbdabbdab\n"
    "v_mov_b32_e32 v66, 0x392b392b\n"
    "v_cndmask_b32_e32 v64, v64, v65, vcc\n"
    "v_cndmask_b32_e32 v66, 0, v66, vcc\n"
    "s_cselect_b32 s47, 56, 64\n"
    "v_cmp_le_u32_e32 vcc, s47, v24\n"
    "v_mov_b32_e32 v65, 0x3e80bdab\n"
    "v_mov_b32_e32 v67, 0x392b\n"
    "v_cndmask_b32_e32 v65, v64, v65, vcc\n"
    "v_cndmask_b32_e32 v67, v66, v67, vcc\n"
    "s_cmp_ge_u32 s36, 120\n"
    "s_cselect_b32 s47, 15, 16\n"
    "v_cmp_ne_u32_e32 vcc, s47, v29\n"
    "v_mov_b32_e32 v68, 0x3db8aa3b\n"
    "s_lshr_b32 s47, s36, 5\n"
    "v_cndmask_b32_e32 v68, 0, v68, vcc\n"
    "s_lshl_b32 s47, s47, 15\n"
    "s_and_b32 s48, s36, 7\n"
    "s_lshl_b32 s48, s48, 11\n"
    "s_add_u32 s47, s47, s48\n"
    "s_and_b32 s48, s36, 8\n"
    "s_lshl_b32 s48, s48, 7\n"
    "s_add_u32 s47, s47, s48\n"
    "s_and_b32 s48, s36, 16\n"
    "s_lshl_b32 s48, s48, 10\n"
    "s_add_u32 s47, s47, s48\n"
    "s_add_u32 s52, s47, 5242880\n"
    "s_lshl_b32 s48, s50, 9\n"
    "s_add_u32 s52, s52, s48\n"
    "s_lshl_b32 s48, s50, 11\n"
    "v_add_u32_e32 v72, s48, v240\n"
    "v_add_u32_e32 v73, s48, v241\n"
    "v_and_b32_e32 v74, 0x1f0, v253\n"
    "v_lshrrev_b32_e32 v29, 5, v24\n"
    "v_lshl_add_u32 v74, v29, 3, v74\n"
    "s_lshl_b32 s53, s36, 1\n"
    "s_add_u32 s53, s53, s50\n"
    "s_lshl_b32 s53, s53, 4\n"
    "s_add_u32 s53, s53, 5771264\n"
    "s_cmp_eq_u32 s51, 0\n"
    "s_cselect_b32 s52, s52, 2.0\n"
    "s_cselect_b32 s53, s53, 2.0\n"
    "s_lshl_b32 s47, %1, 4\n"
    "s_add_u32 s47, s47, 5701632\n"
    "s_lshl_b32 s48, %1, 7\n"
    "s_add_u32 s48, s48, 5705728\n"
    "s_cmp_eq_u32 s50, 0\n"
    "s_cselect_b32 s47, s47, s48\n"
    "s_cmp_eq_u32 s51, 0\n"
    "s_cselect_b32 s47, s53, s47\n"
    "buffer_store_dword v24, off, s[8:11], s47 offset:8 sc1\n"
    "v_lshrrev_b32_e32 v109, 2, v108\n"
    "v_mul_u32_u24_e32 v109, 0x4100, v109\n"
    "v_and_b32_e32 v110, 48, v253\n"
    "v_or_b32_e32 v109, v109, v110\n"
    "v_add_u32_e32 v110, 0x104000, v109\n"
    "v_cmp_gt_u32_e32 vcc, 64, v108\n"
    "s_add_u32 s44, %3, 24576\n"
    "v_lshl_add_u32 v108, v108, 4, s44\n"
    "v_cndmask_b32_e32 v110, 2.0, v110, vcc\n"
    "v_mov_b32_e32 v32, -1\n"
    "v_mov_b32_e32 v33, -1\n"
    "v_mov_b32_e32 v34, -1\n"
    "v_mov_b32_e32 v35, -1\n"
    "ds_write_b128 v108, v[32:35] offset:0\n"
    "ds_write_b128 v108, v[32:35] offset:1280\n"
    "v_and_b32_e32 v29, 6, v28\n"
    "v_xor_b32_e32 v29, v29, v24\n"
    "v_and_b32_e32 v29, 7, v29\n"
    "v_lshlrev_b32_e32 v105, 7, v28\n"
    "v_lshl_add_u32 v105, v29, 4, v105\n"
    "v_bfe_u32 v29, v24, 3, 1\n"
    "v_subrev_u32_e32 v29, s37, v29\n"
    "v_and_b32_e32 v29, 3, v29\n"
    "v_lshl_add_u32 v105, v29, 11, v105\n"
    "s_add_u32 s45, %3, 65536\n"
    "v_add_u32_e32 v105, s45, v105\n"
    "v_xor_b32_e32 v106, 0x1000, v105\n"
    "v_and_b32_e32 v29, 15, v24\n"
    "v_cmp_eq_u32_e32 vcc, 15, v29\n"
    "v_mov_b32_e32 v107, 0xffff\n"
    "s_mov_b32 s58, 0x3f803f80\n"
    "s_mov_b32 s57, 0x43c00000\n"
    "v_cndmask_b32_e32 v107, -1, v107, vcc\n"
    "s_mov_b32 s56, 0xc3c00000\n"
    "s_mov_b32 s62, 0xc000c000\n"
    "s_mov_b32 s63, 0xc000c000\n"
    "v_add_u32_e32 v250, s45, v27\n"
    "v_xor_b32_e32 v251, 64, v250\n"
    "s_lshl_b32 s47, %2, 10\n"
    "s_add_u32 s47, s47, 5771264\n"
    "v_lshl_add_u32 v252, v24, 4, s47\n"
    "s_waitcnt vmcnt(11)\n"
    "s_waitcnt lgkmcnt(0)\n"
    "s_barrier\n"
    "s_add_u32 s39, s39, 2\n"
    "s_cmp_lg_u32 s51, 0\n"
    "s_cbranch_scc1 .Lat_bin\n"
    "ds_read_b128 v[112:115], v72 offset:0\n"
    "ds_read_b128 v[208:211], v240 offset:18432\n"
    "ds_read_b128 v[176:179], v240 offset:16384\n"
    "ds_read_b128 v[116:119], v73 offset:0\n"
    "ds_read_b128 v[212:215], v241 offset:18432\n"
    "ds_read_b128 v[180:183], v241 offset:16384\n"
    "ds_read_b128 v[120:123], v72 offset:4096\n"
    "ds_read_b128 v[216:219], v240 offset:22528\n"
    "ds_read_b128 v[184:187], v240 offset:20480\n"
    "ds_read_b128 v[124:127], v73 offset:4096\n"
    "ds_read_b128 v[220:223], v241 offset:22528\n"
    "ds_read_b128 v[188:191], v241 offset:20480\n"
    "ds_read_b128 v[128:131], v72 offset:8192\n"
    "ds_read_b128 v[224:227], v240 offset:26624\n"
    "ds_read_b128 v[192:195], v240 offset:24576\n"
    "s_waitcnt lgkmcnt(12)\n"
    "v_mfma_f32_16x16x32_bf16 v[56:59], v[208:211], v[112:115], 0\n"
    "ds_read_b128 v[132:135], v73 offset:8192\n"
    "ds_read_b128 v[228:231], v241 offset:26624\n"
    "v_mfma_f32_16x16x32_bf16 v[48:51], v[112:115], v[176:179], 0\n"
    "ds_read_b128 v[196:199], v241 offset:24576\n"
    "s_waitcnt lgkmcnt(12)\n"
    "v_mfma_f32_16x16x32_bf16 v[56:59], v[212:215], v[116:119], v[56:59]\n"
    "ds_read_b128 v[136:139], v72 offset:12288\n"
    "ds_read_b128 v[232:235], v240 offset:30720\n"
    "v_mfma_f32_16x16x32_bf16 v[48:51], v[116:119], v[180:183], v[48:51]\n"
    "ds_read_b128 v[200:203], v240 offset:28672\n"
    "s_waitcnt lgkmcnt(12)\n"
    "v_mfma_f32_16x16x32_bf16 v[56:59], v[216:219], v[120:123], v[56:59]\n"
    "ds_read_b128 v[140:143], v73 offset:12288\n"
    "ds_read_b128 v[236:239], v241 offset:30720\n"
    "v_mfma_f32_16x16x32_bf16 v[48:51], v[120:123], v[184:187], v[48:51]\n"
    "ds_read_b128 v[204:207], v241 offset:28672\n"
    "s_waitcnt lgkmcnt(12)\n"
    "v_mfma_f32_16x16x32_bf16 v[56:59], v[220:223], v[124:127], v[56:59]\n"
    "v_mfma_f32_16x16x32_bf16 v[48:51], v[124:127], v[188:191], v[48:51]\n"
    "s_waitcnt lgkmcnt(9)\n"
    "v_mfma_f32_16x16x32_bf16 v[56:59], v[224:227], v[128:131], v[56:59]\n"
    "v_mfma_f32_16x16x32_bf16 v[48:51], v[128:131], v[192:195], v[48:51]\n"
    "s_waitcnt lgkmcnt(6)\n"
    "v_mfma_f32_16x16x32_bf16 v[56:59], v[228:231], v[132:135], v[56:59]\n"
    "v_mfma_f32_16x16x32_bf16 v[48:51], v[132:135], v[196:199], v[48:51]\n"
    "s_waitcnt lgkmcnt(3)\n"
    "v_mfma_f32_16x16x32_bf16 v[56:59], v[232:235], v[136:139], v[56:59]\n"
    "v_mfma_f32_16x16x32_bf16 v[48:51], v[136:139], v[200:203], v[48:51]\n"
    "s_waitcnt lgkmcnt(0)\n"
    "v_mfma_f32_16x16x32_bf16 v[56:59], v[236:239], v[140:143], v[56:59]\n"
    "v_mfma_f32_16x16x32_bf16 v[48:51], v[140:143], v[204:207], v[48:51]\n"
    "s_nop 3\n"
    "v_cvt_pk_bf16_f32 v56, v56, v57\n"
    "v_cvt_pk_bf16_f32 v57, v58, v59\n"
    "s_nop 1\n"
    "v_mfma_f32_16x16x16_bf16 v[48:51], v[56:57], v[64:65], v[48:51]\n"
    "v_mfma_f32_16x16x16_bf16 v[48:51], v[56:57], v[66:67], v[48:51]\n"
    "s_nop 7\n"
    "v_mul_f32_e32 v48, v48, v68\n"
    "v_mul_f32_e32 v49, v49, v68\n"
    "v_mul_f32_e32 v50, v50, v68\n"
    "v_mul_f32_e32 v51, v51, v68\n"
    "v_cvt_pk_bf16_f32 v48, v48, v49\n"
    "v_cvt_pk_bf16_f32 v49, v50, v51\n"
    "s_branch .Lat_noj2\n"
    ".Lat_bin:\n"
    "s_movk_i32 s45, -640\n"
    "s_movk_i32 s48, 80\n"
    "v_mov_b32_e32 v60, 1\n"
    "v_cmp_gt_u32_e32 vcc, s59, v111\n"
    "v_cmp_gt_i32_e64 s[46:47], s61, v111\n"
    "s_waitcnt vmcnt(9)\n"
    "v_mul_u32_u24_e32 v48, 0x6667, v96\n"
    "v_mul_u32_u24_e32 v49, 0x6667, v97\n"
    "v_mul_u32_u24_e32 v50, 0x6667, v98\n"
    "v_mul_u32_u24_e32 v51, 0x6667, v99\n"
    "v_mul_u32_u24_e32 v52, 0x6667, v92\n"
    "v_mul_u32_u24_e32 v53, 0x6667, v93\n"
    "v_lshrrev_b32_e32 v48, 24, v48\n"
    "v_lshrrev_b32_e32 v49, 24, v49\n"
    "v_lshrrev_b32_e32 v50, 24, v50\n"
    "v_lshrrev_b32_e32 v51, 24, v51\n"
    "v_lshrrev_b32_e32 v52, 24, v52\n"
    "v_lshrrev_b32_e32 v53, 24, v53\n"
    "v_min_u32_e32 v48, 15, v48\n"
    "v_min_u32_e32 v49, 15, v49\n"
    "v_min_u32_e32 v50, 15, v50\n"
    "v_min_u32_e32 v51, 15, v51\n"
    "v_min_u32_e32 v52, 15, v52\n"
    "v_min_u32_e32 v53, 15, v53\n"
    "v_lshl_add_u32 v54, v48, 2, s44\n"
    "v_lshl_add_u32 v55, v49, 2, s44\n"
    "v_lshl_add_u32 v56, v50, 2, s44\n"
    "v_lshl_add_u32 v57, v51, 2, s44\n"
    "v_lshl_add_u32 v58, v52, 2, s44\n"
    "v_lshl_add_u32 v59, v53, 2, s44\n"
    "s_mov_b64 exec, vcc\n"
    "ds_add_rtn_u32 v54, v54, v60 offset:5248\n"
    "ds_add_rtn_u32 v55, v55, v60 offset:5248\n"
    "ds_add_rtn_u32 v56, v56, v60 offset:5248\n"
    "ds_add_rtn_u32 v57, v57, v60 offset:5248\n"
    "s_mov_b64 exec, s[46:47]\n"
    "ds_add_rtn_u32 v58, v58, v60 offset:5248\n"
    "ds_add_rtn_u32 v59, v59, v60 offset:5248\n"
    "s_mov_b64 exec, -1\n"
    "s_waitcnt vmcnt(7)\n"
    "v_mad_i32_i24 v96, v48, s45, v96\n"
    "v_mad_i32_i24 v97, v49, s45, v97\n"
    "v_mad_i32_i24 v98, v50, s45, v98\n"
    "v_mad_i32_i24 v99, v51, s45, v99\n"
    "v_mad_i32_i24 v92, v52, s45, v92\n"
    "v_mad_i32_i24 v93, v53, s45, v93\n"
    "v_lshl_or_b32 v96, v96, 16, v100\n"
    "v_lshl_or_b32 v97, v97, 16, v101\n"
    "v_lshl_or_b32 v98, v98, 16, v102\n"
    "v_lshl_or_b32 v99, v99, 16, v103\n"
    "v_lshl_or_b32 v92, v92, 16, v94\n"
    "v_lshl_or_b32 v93, v93, 16, v95\n"
    "s_mov_b64 exec, vcc\n"
    "s_waitcnt lgkmcnt(0)\n"
    "v_min_i32_e32 v54, 78, v54\n"
    "v_min_i32_e32 v55, 78, v55\n"
    "v_min_i32_e32 v56, 78, v56\n"
    "v_min_i32_e32 v57, 78, v57\n"
    "v_mad_u32_u24 v48, v48, s48, v54\n"
    "v_mad_u32_u24 v49, v49, s48, v55\n"
    "v_mad_u32_u24 v50, v50, s48, v56\n"
    "v_mad_u32_u24 v51, v51, s48, v57\n"
    "v_lshl_add_u32 v48, v48, 2, s44\n"
    "v_lshl_add_u32 v49, v49, 2, s44\n"
    "v_lshl_add_u32 v50, v50, 2, s44\n"
    "v_lshl_add_u32 v51, v51, 2, s44\n"
    "ds_write_b32 v48, v96 offset:4\n"
    "ds_write_b32 v49, v97 offset:4\n"
    "ds_write_b32 v50, v98 offset:4\n"
    "ds_write_b32 v51, v99 offset:4\n"
    "s_mov_b64 exec, s[46:47]\n"
    "v_min_i32_e32 v58, 78, v58\n"
    "v_min_i32_e32 v59, 78, v59\n"
    "v_mad_u32_u24 v52, v52, s48, v58\n"
    "v_mad_u32_u24 v53, v53, s48, v59\n"
    "v_lshl_add_u32 v52, v52, 2, s44\n"
    "v_lshl_add_u32 v53, v53, 2, s44\n"
    "ds_write_b32 v52, v92 offset:4\n"
    "ds_write_b32 v53, v93 offset:4\n"
    "s_mov_b64 exec, -1\n"
    ".Lat_noj2:\n"
    "buffer_store_dwordx2 v[48:49], v74, s[8:11], s52 offen offset:0 sc1\n"
    "v_mov_b32_e32 v112, s39\n"
    "v_mov_b32_e32 v113, 0\n"
    "s_waitcnt vmcnt(2)\n"
    "v_dot2_f32_bf16 v24, v0, s58, 0\n"
    "v_dot2_f32_bf16 v32, v8, s58, 0\n"
    "v_dot2_f32_bf16 v40, v16, s58, 0\n"
    "v_dot2_f32_bf16 v25, v4, s58, 0\n"
    "v_dot2_f32_bf16 v33, v12, s58, 0\n"
    "v_dot2_f32_bf16 v41, v20, s58, 0\n"
    "v_dot2_f32_bf16 v26, v0, v0, 0\n"
    "v_dot2_f32_bf16 v34, v8, v8, 0\n"
    "v_dot2_f32_bf16 v42, v16, v16, 0\n"
    "v_dot2_f32_bf16 v27, v4, v4, 0\n"
    "v_dot2_f32_bf16 v35, v12, v12, 0\n"
    "v_dot2_f32_bf16 v43, v20, v20, 0\n"
    "v_dot2_f32_bf16 v24, v1, s58, v24\n"
    "v_dot2_f32_bf16 v32, v9, s58, v32\n"
    "v_dot2_f32_bf16 v40, v17, s58, v40\n"
    "v_dot2_f32_bf16 v25, v5, s58, v25\n"
    "v_dot2_f32_bf16 v33, v13, s58, v33\n"
    "v_dot2_f32_bf16 v41, v21, s58, v41\n"
    "v_dot2_f32_bf16 v26, v1, v1, v26\n"
    "v_dot2_f32_bf16 v34, v9, v9, v34\n"
    "v_dot2_f32_bf16 v42, v17, v17, v42\n"
    "v_dot2_f32_bf16 v27, v5, v5, v27\n"
    "v_dot2_f32_bf16 v35, v13, v13, v35\n"
    "v_dot2_f32_bf16 v43, v21, v21, v43\n"
    "v_dot2_f32_bf16 v24, v2, s58, v24\n"
    "v_dot2_f32_bf16 v32, v10, s58, v32\n"
    "v_dot2_f32_bf16 v40, v18, s58, v40\n"
    "v_dot2_f32_bf16 v25, v6, s58, v25\n"
    "v_dot2_f32_bf16 v33, v14, s58, v33\n"
    "v_dot2_f32_bf16 v41, v22, s58, v41\n"
    "v_dot2_f32_bf16 v26, v2, v2, v26\n"
    "v_dot2_f32_bf16 v34, v10, v10, v34\n"
    "v_dot2_f32_bf16 v42, v18, v18, v42\n"
    "v_dot2_f32_bf16 v27, v6, v6, v27\n"
    "v_dot2_f32_bf16 v35, v14, v14, v35\n"
    "v_dot2_f32_bf16 v43, v22, v22, v43\n"
    "v_dot2_f32_bf16 v24, v3, s58, v24\n"
    "v_dot2_f32_bf16 v32, v11, s58, v32\n"
    "v_dot2_f32_bf16 v40, v19, s58, v40\n"
    "v_dot2_f32_bf16 v25, v7, s58, v25\n"
    "v_dot2_f32_bf16 v33, v15, s58, v33\n"
    "v_dot2_f32_bf16 v41, v23, s58, v41\n"
    "v_dot2_f32_bf16 v26, v3, v3, v26\n"
    "v_dot2_f32_bf16 v34, v11, v11, v34\n"
    "v_dot2_f32_bf16 v42, v19, v19, v42\n"
    "v_dot2_f32_bf16 v27, v7, v7, v27\n"
    "v_dot2_f32_bf16 v35, v15, v15, v35\n"
    "v_dot2_f32_bf16 v43, v23, v23, v43\n"
    "v_and_b32_e32 v30, 0xffff0000, v7\n"
    "v_and_b32_e32 v38, 0xffff0000, v15\n"
    "v_and_b32_e32 v46, 0xffff0000, v23\n"
    "v_add_f32_e32 v24, v24, v25\n"
    "v_add_f32_e32 v32, v32, v33\n"
    "v_add_f32_e32 v40, v40, v41\n"
    "v_add_f32_e32 v26, v26, v27\n"
    "v_add_f32_e32 v34, v34, v35\n"
    "v_add_f32_e32 v42, v42, v43\n"
    "v_add_f32_dpp v25, v25, v25 quad_perm:[1,0,3,2] row_mask:0xf bank_mask:0xf bound_ctrl:1\n"
    "v_lshlrev_b32_e32 v48, 16, v0\n"
    "v_add_f32_dpp v33, v33, v33 quad_perm:[1,0,3,2] row_mask:0xf bank_mask:0xf bound_ctrl:1\n"
    "v_and_b32_e32 v49, 0xffff0000, v0\n"
    "v_add_f32_dpp v41, v41, v41 quad_perm:[1,0,3,2] row_mask:0xf bank_mask:0xf bound_ctrl:1\n"
    "v_lshlrev_b32_e32 v50, 16, v1\n"
    "v_mov_b32_dpp v30, v30 quad_perm:[3,3,3,3] row_mask:0xf bank_mask:0xf bound_ctrl:1\n"
    "v_and_b32_e32 v51, 0xffff0000, v1\n"
    "v_mov_b32_dpp v38, v38 quad_perm:[3,3,3,3] row_mask:0xf bank_mask:0xf bound_ctrl:1\n"
    "v_lshlrev_b32_e32 v52, 16, v2\n"
    "v_mov_b32_dpp v46, v46 quad_perm:[3,3,3,3] row_mask:0xf bank_mask:0xf bound_ctrl:1\n"
    "v_and_b32_e32 v53, 0xffff0000, v2\n"
    "v_add_f32_dpp v24, v24, v24 quad_perm:[1,0,3,2] row_mask:0xf bank_mask:0xf bound_ctrl:1\n"
    "v_lshlrev_b32_e32 v54, 16, v3\n"
    "v_add_f32_dpp v32, v32, v32 quad_perm:[1,0,3,2] row_mask:0xf bank_mask:0xf bound_ctrl:1\n"
    "v_and_b32_e32 v55, 0xffff0000, v3\n"
    "v_add_f32_dpp v40, v40, v40 quad_perm:[1,0,3,2] row_mask:0xf bank_mask:0xf bound_ctrl:1\n"
    "v_lshlrev_b32_e32 v56, 16, v4\n"
    "v_add_f32_dpp v26, v26, v26 quad_perm:[1,0,3,2] row_mask:0xf bank_mask:0xf bound_ctrl:1\n"
    "v_and_b32_e32 v57, 0xffff0000, v4\n"
    "v_add_f32_dpp v34, v34, v34 quad_perm:[1,0,3,2] row_mask:0xf bank_mask:0xf bound_ctrl:1\n"
    "v_lshlrev_b32_e32 v58, 16, v5\n"
    "s_waitcnt vmcnt(0)\n"
    "buffer_store_dword v112, v113, s[8:11], s53 offen offset:0 sc1\n"
    "v_add_f32_dpp v42, v42, v42 quad_perm:[1,0,3,2] row_mask:0xf bank_mask:0xf bound_ctrl:1\n"
    "v_and_b32_e32 v59, 0xffff0000, v5\n"
    "v_add_f32_dpp v24, v24, v24 quad_perm:[2,3,0,1] row_mask:0xf bank_mask:0xf bound_ctrl:1\n"
    "v_lshlrev_b32_e32 v60, 16, v6\n"
    "v_add_f32_dpp v32, v32, v32 quad_perm:[2,3,0,1] row_mask:0xf bank_mask:0xf bound_ctrl:1\n"
    "v_and_b32_e32 v61, 0xffff0000, v6\n"
    "s_waitcnt lgkmcnt(0)\n"
    "s_barrier\n"
    "ds_read_b128 v[96:99], v108 offset:0\n"
    "ds_read_b128 v[100:103], v108 offset:4096\n"
    "s_lshl_b32 s45, %1, 6\n"
    "s_add_u32 s45, s45, 35038208\n"
    "v_add_f32_dpp v40, v40, v40 quad_perm:[2,3,0,1] row_mask:0xf bank_mask:0xf bound_ctrl:1\n"
    "v_lshlrev_b32_e32 v62, 16, v7\n"
    "v_add_f32_dpp v26, v26, v26 quad_perm:[2,3,0,1] row_mask:0xf bank_mask:0xf bound_ctrl:1\n"
    "v_and_b32_e32 v63, 0xffff0000, v7\n"
    "v_add_f32_dpp v34, v34, v34 quad_perm:[2,3,0,1] row_mask:0xf bank_mask:0xf bound_ctrl:1\n"
    "v_lshlrev_b32_e32 v64, 16, v8\n"
    "v_add_f32_dpp v42, v42, v42 quad_perm:[2,3,0,1] row_mask:0xf bank_mask:0xf bound_ctrl:1\n"
    "v_and_b32_e32 v65, 0xffff0000, v8\n"
    "v_add_f32_dpp v24, v24, v24 row_half_mirror row_mask:0xf bank_mask:0xf bound_ctrl:1\n"
    "v_lshlrev_b32_e32 v66, 16, v9\n"
    "v_add_f32_dpp v32, v32, v32 row_half_mirror row_mask:0xf bank_mask:0xf bound_ctrl:1\n"
    "v_and_b32_e32 v67, 0xffff0000, v9\n"
    "v_add_f32_dpp v40, v40, v40 row_half_mirror row_mask:0xf bank_mask:0xf bound_ctrl:1\n"
    "v_lshlrev_b32_e32 v68, 16, v10\n"
    "v_add_f32_dpp v26, v26, v26 row_half_mirror row_mask:0xf bank_mask:0xf bound_ctrl:1\n"
    "v_and_b32_e32 v69, 0xffff0000, v10\n"
    "v_add_f32_dpp v34, v34, v34 row_half_mirror row_mask:0xf bank_mask:0xf bound_ctrl:1\n"
    "v_lshlrev_b32_e32 v70, 16, v11\n"
    "v_add_f32_dpp v42, v42, v42 row_half_mirror row_mask:0xf bank_mask:0xf bound_ctrl:1\n"
    "v_and_b32_e32 v71, 0xffff0000, v11\n"
    "v_add_f32_dpp v24, v24, v24 row_mirror row_mask:0xf bank_mask:0xf bound_ctrl:1\n"
    "v_lshlrev_b32_e32 v72, 16, v12\n"
    "v_add_f32_dpp v32, v32, v32 row_mirror row_mask:0xf bank_mask:0xf bound_ctrl:1\n"
    "v_and_b32_e32 v73, 0xffff0000, v12\n"
    "v_add_f32_dpp v40, v40, v40 row_mirror row_mask:0xf bank_mask:0xf bound_ctrl:1\n"
    "v_lshlrev_b32_e32 v74, 16, v13\n"
    "v_add_f32_dpp v26, v26, v26 row_mirror row_mask:0xf bank_mask:0xf bound_ctrl:1\n"
    "v_and_b32_e32 v75, 0xffff0000, v13\n"
    "v_add_f32_dpp v34, v34, v34 row_mirror row_mask:0xf bank_mask:0xf bound_ctrl:1\n"
    "v_lshlrev_b32_e32 v76, 16, v14\n"
    "s_waitcnt lgkmcnt(0)\n"
    "buffer_store_dwordx4 v[96:99], v109, s[8:11], s45 offen offset:0 sc1\n"
    "buffer_store_dwordx4 v[100:103], v110, s[8:11], s45 offen offset:0 sc1\n"
    "v_add_f32_dpp v42, v42, v42 row_mirror row_mask:0xf bank_mask:0xf bound_ctrl:1\n"
    "v_and_b32_e32 v77, 0xffff0000, v14\n"
    "v_mul_f32_e32 v24, 0x3b800000, v24\n"
    "v_lshlrev_b32_e32 v78, 16, v15\n"
    "v_mul_f32_e32 v32, 0x3b800000, v32\n"
    "v_and_b32_e32 v79, 0xffff0000, v15\n"
    "v_mul_f32_e32 v40, 0x3b800000, v40\n"
    "v_lshlrev_b32_e32 v80, 16, v16\n"
    "v_mul_f32_e32 v25, 0.25, v25\n"
    "v_and_b32_e32 v81, 0xffff0000, v16\n"
    "v_mul_f32_e32 v33, 0.25, v33\n"
    "v_lshlrev_b32_e32 v82, 16, v17\n"
    "v_mul_f32_e32 v41, 0.25, v41\n"
    "v_and_b32_e32 v83, 0xffff0000, v17\n"
    "v_mul_f32_e64 v27, v24, -v24\n"
    "v_lshlrev_b32_e32 v84, 16, v18\n"
    "v_mul_f32_e64 v35, v32, -v32\n"
    "v_and_b32_e32 v85, 0xffff0000, v18\n"
    "v_mul_f32_e64 v43, v40, -v40\n"
    "v_lshlrev_b32_e32 v86, 16, v19\n"
    "v_sub_f32_e32 v25, v25, v30\n"
    "v_and_b32_e32 v87, 0xffff0000, v19\n"
    "v_sub_f32_e32 v33, v33, v38\n"
    "v_lshlrev_b32_e32 v88, 16, v20\n"
    "v_sub_f32_e32 v41, v41, v46\n"
    "v_and_b32_e32 v89, 0xffff0000, v20\n"
    "v_fmac_f32_e32 v27, 0x3b800000, v26\n"
    "v_lshlrev_b32_e32 v90, 16, v21\n"
    "v_fmac_f32_e32 v35, 0x3b800000, v34\n"
    "v_and_b32_e32 v91, 0xffff0000, v21\n"
    "v_fmac_f32_e32 v43, 0x3b800000, v42\n"
    "v_lshlrev_b32_e32 v92, 16, v22\n"
    "v_mul_f32_e32 v25, 0x3eaaaaab, v25\n"
    "v_and_b32_e32 v93, 0xffff0000, v22\n"
    "v_mul_f32_e32 v33, 0x3eaaaaab, v33\n"
    "v_lshlrev_b32_e32 v94, 16, v23\n"
    "v_mul_f32_e32 v41, 0x3eaaaaab, v41\n"
    "v_and_b32_e32 v95, 0xffff0000, v23\n"
    "v_max_f32_e32 v27, 0, v27\n"
    "v_max_f32_e32 v35, 0, v35\n"
    "v_max_f32_e32 v43, 0, v43\n"
    "v_cndmask_b32_e64 v25, v24, v25, s[62:63]\n"
    "v_cndmask_b32_e64 v33, v32, v33, s[62:63]\n"
    "v_cndmask_b32_e64 v41, v40, v41, s[62:63]\n"
    "v_add_f32_e32 v27, 0x3727c5ac, v27\n"
    "v_add_f32_e32 v35, 0x3727c5ac, v35\n"
    "v_add_f32_e32 v43, 0x3727c5ac, v43\n"
    "v_rsq_f32_e32 v28, v27\n"
    "v_rsq_f32_e32 v36, v35\n"
    "v_rsq_f32_e32 v44, v43\n"
    "v_fma_f32 v29, -v24, v28, s57\n"
    "v_fma_f32 v37, -v32, v36, s57\n"
    "v_fma_f32 v45, -v40, v44, s57\n"
    "v_fma_f32 v31, -v25, v28, s57\n"
    "v_fma_f32 v39, -v33, v36, s57\n"
    "v_fma_f32 v47, -v41, v44, s57\n"
    "v_sub_f32_e32 v24, v29, v31\n"
    "v_sub_f32_e32 v32, v37, v39\n"
    "v_sub_f32_e32 v40, v45, v47\n"
    "v_pk_fma_f32 v[48:49], v[48:49], v[28:29], v[28:29] op_sel:[0,0,1] op_sel_hi:[1,0,1]\n"
    "v_pk_fma_f32 v[50:51], v[50:51], v[28:29], v[28:29] op_sel:[0,0,1] op_sel_hi:[1,0,1]\n"
    "v_pk_fma_f32 v[52:53], v[52:53], v[28:29], v[28:29] op_sel:[0,0,1] op_sel_hi:[1,0,1]\n"
    "v_pk_fma_f32 v[54:55], v[54:55], v[28:29], v[28:29] op_sel:[0,0,1] op_sel_hi:[1,0,1]\n"
    "v_pk_fma_f32 v[56:57], v[56:57], v[28:29], v[30:31] op_sel:[0,0,1] op_sel_hi:[1,0,1]\n"
    "v_pk_fma_f32 v[58:59], v[58:59], v[28:29], v[30:31] op_sel:[0,0,1] op_sel_hi:[1,0,1]\n"
    "v_pk_fma_f32 v[60:61], v[60:61], v[28:29], v[30:31] op_sel:[0,0,1] op_sel_hi:[1,0,1]\n"
    "v_pk_fma_f32 v[62:63], v[62:63], v[28:29], v[30:31] op_sel:[0,0,1] op_sel_hi:[1,0,1]\n"
    "v_pk_add_f32 v[48:49], v[48:49], s[56:57] op_sel_hi:[1,0]\n"
    "v_pk_add_f32 v[50:51], v[50:51], s[56:57] op_sel_hi:[1,0]\n"
    "v_pk_add_f32 v[52:53], v[52:53], s[56:57] op_sel_hi:[1,0]\n"
    "v_pk_add_f32 v[54:55], v[54:55], s[56:57] op_sel_hi:[1,0]\n"
    "v_pk_add_f32 v[56:57], v[56:57], s[56:57] op_sel_hi:[1,0]\n"
    "v_pk_add_f32 v[58:59], v[58:59], s[56:57] op_sel_hi:[1,0]\n"
    "v_pk_add_f32 v[60:61], v[60:61], s[56:57] op_sel_hi:[1,0]\n"
    "v_pk_add_f32 v[62:63], v[62:63], s[56:57] op_sel_hi:[1,0]\n"
    "v_cvt_pk_bf16_f32 v0, v48, v49\n"
    "v_cvt_pk_bf16_f32 v1, v50, v51\n"
    "v_cvt_pk_bf16_f32 v2, v52, v53\n"
    "v_cvt_pk_bf16_f32 v3, v54, v55\n"
    "v_cvt_pk_bf16_f32 v4, v56, v57\n"
    "v_cvt_pk_bf16_f32 v5, v58, v59\n"
    "v_cvt_pk_bf16_f32 v6, v60, v61\n"
    "v_cvt_pk_bf16_f32 v7, v62, v63\n"
    "buffer_store_dwordx4 v[0:3], v104, s[8:11], s35 offen offset:0 sc1\n"
    "v_bfi_b32 v7, v107, v7, 1.0\n"
    "ds_write_b128 v105, v[0:3] offset:0\n"
    "buffer_store_dwordx4 v[4:7], v104, s[8:11], s35 offen offset:256 sc1\n"
    "v_pk_fma_f32 v[64:65], v[64:65], v[36:37], v[36:37] op_sel:[0,0,1] op_sel_hi:[1,0,1]\n"
    "v_pk_fma_f32 v[66:67], v[66:67], v[36:37], v[36:37] op_sel:[0,0,1] op_sel_hi:[1,0,1]\n"
    "v_pk_fma_f32 v[68:69], v[68:69], v[36:37], v[36:37] op_sel:[0,0,1] op_sel_hi:[1,0,1]\n"
    "v_pk_fma_f32 v[70:71], v[70:71], v[36:37], v[36:37] op_sel:[0,0,1] op_sel_hi:[1,0,1]\n"
    "v_pk_fma_f32 v[72:73], v[72:73], v[36:37], v[38:39] op_sel:[0,0,1] op_sel_hi:[1,0,1]\n"
    "v_pk_fma_f32 v[74:75], v[74:75], v[36:37], v[38:39] op_sel:[0,0,1] op_sel_hi:[1,0,1]\n"
    "v_pk_fma_f32 v[76:77], v[76:77], v[36:37], v[38:39] op_sel:[0,0,1] op_sel_hi:[1,0,1]\n"
    "v_pk_fma_f32 v[78:79], v[78:79], v[36:37], v[38:39] op_sel:[0,0,1] op_sel_hi:[1,0,1]\n"
    "v_pk_add_f32 v[64:65], v[64:65], s[56:57] op_sel_hi:[1,0]\n"
    "v_pk_add_f32 v[66:67], v[66:67], s[56:57] op_sel_hi:[1,0]\n"
    "v_pk_add_f32 v[68:69], v[68:69], s[56:57] op_sel_hi:[1,0]\n"
    "v_pk_add_f32 v[70:71], v[70:71], s[56:57] op_sel_hi:[1,0]\n"
    "v_pk_add_f32 v[72:73], v[72:73], s[56:57] op_sel_hi:[1,0]\n"
    "v_pk_add_f32 v[74:75], v[74:75], s[56:57] op_sel_hi:[1,0]\n"
    "v_pk_add_f32 v[76:77], v[76:77], s[56:57] op_sel_hi:[1,0]\n"
    "v_pk_add_f32 v[78:79], v[78:79], s[56:57] op_sel_hi:[1,0]\n"
    "v_cvt_pk_bf16_f32 v8, v64, v65\n"
    "v_cvt_pk_bf16_f32 v9, v66, v67\n"
    "v_cvt_pk_bf16_f32 v10, v68, v69\n"
    "v_cvt_pk_bf16_f32 v11, v70, v71\n"
    "v_cvt_pk_bf16_f32 v12, v72, v73\n"
    "v_cvt_pk_bf16_f32 v13, v74, v75\n"
    "v_cvt_pk_bf16_f32 v14, v76, v77\n"
    "v_cvt_pk_bf16_f32 v15, v78, v79\n"
    "buffer_store_dwordx4 v[8:11], v104, s[8:11], s54 offen offset:0 sc1\n"
    "v_bfi_b32 v15, v107, v15, 1.0\n"
    "ds_write_b128 v105, v[8:11] offset:8192\n"
    "buffer_store_dwordx4 v[12:15], v104, s[8:11], s54 offen offset:256 sc1\n"
    "v_pk_fma_f32 v[80:81], v[80:81], v[44:45], v[44:45] op_sel:[0,0,1] op_sel_hi:[1,0,1]\n"
    "v_pk_fma_f32 v[82:83], v[82:83], v[44:45], v[44:45] op_sel:[0,0,1] op_sel_hi:[1,0,1]\n"
    "v_pk_fma_f32 v[84:85], v[84:85], v[44:45], v[44:45] op_sel:[0,0,1] op_sel_hi:[1,0,1]\n"
    "v_pk_fma_f32 v[86:87], v[86:87], v[44:45], v[44:45] op_sel:[0,0,1] op_sel_hi:[1,0,1]\n"
    "v_pk_fma_f32 v[88:89], v[88:89], v[44:45], v[46:47] op_sel:[0,0,1] op_sel_hi:[1,0,1]\n"
    "v_pk_fma_f32 v[90:91], v[90:91], v[44:45], v[46:47] op_sel:[0,0,1] op_sel_hi:[1,0,1]\n"
    "v_pk_fma_f32 v[92:93], v[92:93], v[44:45], v[46:47] op_sel:[0,0,1] op_sel_hi:[1,0,1]\n"
    "v_pk_fma_f32 v[94:95], v[94:95], v[44:45], v[46:47] op_sel:[0,0,1] op_sel_hi:[1,0,1]\n"
    "v_pk_add_f32 v[80:81], v[80:81], s[56:57] op_sel_hi:[1,0]\n"
    "v_pk_add_f32 v[82:83], v[82:83], s[56:57] op_sel_hi:[1,0]\n"
    "v_pk_add_f32 v[84:85], v[84:85], s[56:57] op_sel_hi:[1,0]\n"
    "v_pk_add_f32 v[86:87], v[86:87], s[56:57] op_sel_hi:[1,0]\n"
    "v_pk_add_f32 v[88:89], v[88:89], s[56:57] op_sel_hi:[1,0]\n"
    "v_pk_add_f32 v[90:91], v[90:91], s[56:57] op_sel_hi:[1,0]\n"
    "v_pk_add_f32 v[92:93], v[92:93], s[56:57] op_sel_hi:[1,0]\n"
    "v_pk_add_f32 v[94:95], v[94:95], s[56:57] op_sel_hi:[1,0]\n"
    "v_cvt_pk_bf16_f32 v16, v80, v81\n"
    "v_cvt_pk_bf16_f32 v17, v82, v83\n"
    "v_cvt_pk_bf16_f32 v18, v84, v85\n"
    "v_cvt_pk_bf16_f32 v19, v86, v87\n"
    "v_cvt_pk_bf16_f32 v20, v88, v89\n"
    "v_cvt_pk_bf16_f32 v21, v90, v91\n"
    "v_cvt_pk_bf16_f32 v22, v92, v93\n"
    "v_cvt_pk_bf16_f32 v23, v94, v95\n"
    "buffer_store_dwordx4 v[16:19], v104, s[8:11], s60 offen offset:0 sc1\n"
    "v_bfi_b32 v23, v107, v23, 1.0\n"
    "ds_write_b128 v105, v[16:19] offset:16384\n"
    "buffer_store_dwordx4 v[20:23], v104, s[8:11], s60 offen offset:256 sc1\n"
    "v_pk_add_f32 v[56:57], v[56:57], v[24:25] op_sel_hi:[1,0]\n"
    "v_pk_add_f32 v[58:59], v[58:59], v[24:25] op_sel_hi:[1,0]\n"
    "v_pk_add_f32 v[60:61], v[60:61], v[24:25] op_sel_hi:[1,0]\n"
    "v_pk_add_f32 v[62:63], v[62:63], v[24:25] op_sel_hi:[1,0]\n"
    "v_cvt_pk_bf16_f32 v56, v56, v57\n"
    "v_cvt_pk_bf16_f32 v57, v58, v59\n"
    "v_cvt_pk_bf16_f32 v58, v60, v61\n"
    "v_cvt_pk_bf16_f32 v59, v62, v63\n"
    "ds_write_b128 v106, v[56:59] offset:0\n"
    "v_pk_add_f32 v[72:73], v[72:73], v[32:33] op_sel_hi:[1,0]\n"
    "v_pk_add_f32 v[74:75], v[74:75], v[32:33] op_sel_hi:[1,0]\n"
    "v_pk_add_f32 v[76:77], v[76:77], v[32:33] op_sel_hi:[1,0]\n"
    "v_pk_add_f32 v[78:79], v[78:79], v[32:33] op_sel_hi:[1,0]\n"
    "v_cvt_pk_bf16_f32 v72, v72, v73\n"
    "v_cvt_pk_bf16_f32 v73, v74, v75\n"
    "v_cvt_pk_bf16_f32 v74, v76, v77\n"
    "v_cvt_pk_bf16_f32 v75, v78, v79\n"
    "ds_write_b128 v106, v[72:75] offset:8192\n"
    "v_pk_add_f32 v[88:89], v[88:89], v[40:41] op_sel_hi:[1,0]\n"
    "v_pk_add_f32 v[90:91], v[90:91], v[40:41] op_sel_hi:[1,0]\n"
    "v_pk_add_f32 v[92:93], v[92:93], v[40:41] op_sel_hi:[1,0]\n"
    "v_pk_add_f32 v[94:95], v[94:95], v[40:41] op_sel_hi:[1,0]\n"
    "v_cvt_pk_bf16_f32 v88, v88, v89\n"
    "v_cvt_pk_bf16_f32 v89, v90, v91\n"
    "v_cvt_pk_bf16_f32 v90, v92, v93\n"
    "v_cvt_pk_bf16_f32 v91, v94, v95\n"
    "ds_write_b128 v106, v[88:91] offset:16384\n"
    "s_waitcnt lgkmcnt(0)\n"
    "s_barrier\n"
    "ds_read_b128 a[0:3], v250 offset:0\n"
    "ds_read_b128 a[32:35], v250 offset:8192\n"
    "ds_read_b128 a[64:67], v250 offset:16384\n"
    "ds_read_b128 a[4:7], v251 offset:0\n"
    "ds_read_b128 a[36:39], v251 offset:8192\n"
    "ds_read_b128 a[68:71], v251 offset:16384\n"
    "ds_read_b128 a[8:11], v250 offset:2048\n"
    "ds_read_b128 a[40:43], v250 offset:10240\n"
    "ds_read_b128 a[72:75], v250 offset:18432\n"
    "ds_read_b128 a[12:15], v251 offset:2048\n"
    "ds_read_b128 a[44:47], v251 offset:10240\n"
    "ds_read_b128 a[76:79], v251 offset:18432\n"
    "buffer_load_dword v255, v252, s[8:11], 0 offen offset:0 sc1\n"
    "ds_read_b128 a[16:19], v250 offset:4096\n"
    "ds_read_b128 a[48:51], v250 offset:12288\n"
    "ds_read_b128 a[80:83], v250 offset:20480\n"
    "ds_read_b128 a[20:23], v251 offset:4096\n"
    "ds_read_b128 a[52:55], v251 offset:12288\n"
    "ds_read_b128 a[84:87], v251 offset:20480\n"
    "ds_read_b128 a[24:27], v250 offset:6144\n"
    "ds_read_b128 a[56:59], v250 offset:14336\n"
    "ds_read_b128 a[88:91], v250 offset:22528\n"
    "ds_read_b128 a[28:31], v251 offset:6144\n"
    "ds_read_b128 a[60:63], v251 offset:14336\n"
    "ds_read_b128 a[92:95], v251 offset:22528\n"
    "s_lshl_b32 s52, %2, 15\n"
    "s_add_u32 s52, s52, 5242880\n"
    "s_lshl_b32 s61, s37, 12\n"
    "s_lshl_b32 s46, %1, 4\n"
    "s_lshl_b32 s47, %1, 7\n"
    "s_add_u32 s47, s47, 4096\n"
    "s_cmp_eq_u32 s50, 0\n"
    "s_cselect_b32 s46, s46, s47\n"
    "s_add_u32 s46, s46, 5701632\n"
    "s_cmp_eq_u32 s51, 0\n"
    "s_cselect_b32 s46, s46, 2.0\n"
    "v_mov_b32_e32 v40, s39\n"
    "v_mov_b32_e32 v41, 0\n"
    "s_waitcnt vmcnt(1)\n"
    "s_waitcnt lgkmcnt(0)\n"
    "s_barrier\n"
    "buffer_store_dword v40, v41, s[8:11], s46 offen offset:0 sc1\n"
    "s_waitcnt vmcnt(1)\n"
    "v_cmp_ne_u32_e32 vcc, s39, v255\n"
    "s_nop 4\n"
    "s_cbranch_vccnz .Lat_apoll\n"
    ".Lat_apoll_ret:\n"
    "s_add_u32 s47, s52, s61\n"
    "buffer_load_dwordx4 v[112:115], v253, s[8:11], s47 offen offset:0 sc1\n"
    "buffer_load_dwordx4 v[116:119], v253, s[8:11], s47 offen offset:1024 sc1\n"
    "buffer_load_dwordx4 v[128:131], v253, s[8:11], s47 offen offset:2048 sc1\n"
    "s_add_u32 s61, s61, 0x1000\n"
    "buffer_load_dwordx4 v[132:135], v253, s[8:11], s47 offen offset:3072 sc1\n"
    "s_bitset0_b32 s61, 14\n"
    "s_add_u32 s47, s52, s61\n"
    "buffer_load_dwordx4 v[144:147], v253, s[8:11], s47 offen offset:0 sc1\n"
    "buffer_load_dwordx4 v[148:151], v253, s[8:11], s47 offen offset:1024 sc1\n"
    "buffer_load_dwordx4 v[160:163], v253, s[8:11], s47 offen offset:2048 sc1\n"
    "s_add_u32 s61, s61, 0x1000\n"
    "buffer_load_dwordx4 v[164:167], v253, s[8:11], s47 offen offset:3072 sc1\n"
    "s_bitset0_b32 s61, 14\n"
    "s_add_u32 s47, s52, s61\n"
    "buffer_load_dwordx4 v[176:179], v253, s[8:11], s47 offen offset:0 sc1\n"
    "buffer_load_dwordx4 v[180:183], v253, s[8:11], s47 offen offset:1024 sc1\n"
    "buffer_load_dwordx4 v[192:195], v253, s[8:11], s47 offen offset:2048 sc1\n"
    "s_add_u32 s61, s61, 0x1000\n"
    "s_lshl_b32 s59, s32, 4\n"
    "s_lshr_b32 s48, s32, 1\n"
    "s_sub_u32 s59, s59, s48\n"
    "s_lshl_b32 s59, s59, 6\n"
    "s_sub_u32 s48, 3984, s59\n"
    "v_cmp_le_u32_e32 vcc, 0x40, v253\n"
    "v_mov_b32_e32 v0, s48\n"
    "s_cmp_eq_u32 s32, 2\n"
    "s_cselect_b32 s48, 1024, 0\n"
    "v_mov_b32_e32 v2, s48\n"
    "s_add_u32 s59, s59, 5701632\n"
    "v_cndmask_b32_e32 v3, v253, v0, vcc\n"
    "s_lshl_b32 s45, %2, 11\n"
    "s_add_u32 s48, s45, %3\n"
    "v_add_u32_e32 v9, s48, v253\n"
    "s_add_u32 s45, s45, 11022336\n"
    "v_add_u32_e32 v8, s45, v253\n"
    "v_lshrrev_b32_e32 v13, 7, v253\n"
    "v_and_b32_e32 v12, 0x70, v254\n"
    "v_lshl_add_u32 v12, v13, 9, v12\n"
    "v_add_u32_e32 v13, 0xc00, v12\n"
    "v_add_u32_e32 v14, 0x1800, v12\n"
    "v_add_u32_e32 v15, 0x2400, v12\n"
    "s_lshl_b32 s57, s50, 8\n"
    "s_add_u32 s57, s57, 5111808\n"
    "s_add_u32 s62, s43, s34\n"
    "s_xor_b32 s63, s34, 0xc000\n"
    "s_add_u32 s63, s63, s43\n"
    "s_waitcnt vmcnt(9)\n"
    "v_mfma_f32_16x16x32_bf16 v[24:27], v[112:115], a[0:3], 0\n"
    "buffer_load_dwordx4 v[196:199], v253, s[8:11], s47 offen offset:3072 sc1\n"
    "v_mfma_f32_16x16x32_bf16 v[28:31], v[116:119], a[0:3], 0\n"
    "s_bitset0_b32 s61, 14\n"
    "s_mov_b32 s18, 5120000\n"
    "s_mov_b32 s19, 0x20000\n"
    "v_mfma_f32_16x16x32_bf16 v[48:51], v[112:115], a[32:35], 0\n"
    "s_add_u32 s47, s52, s61\n"
    "v_xor_b32_e32 v242, 0x12000, v240\n"
    "v_xor_b32_e32 v243, 0x12000, v241\n"
    "v_mfma_f32_16x16x32_bf16 v[52:55], v[116:119], a[32:35], 0\n"
    "buffer_load_dwordx4 v[208:211], v253, s[8:11], s47 offen offset:0 sc1\n"
    "v_mfma_f32_16x16x32_bf16 v[64:67], v[112:115], a[64:67], 0\n"
    "s_cmp_eq_u32 %2, 3\n"
    "s_cselect_b32 s88, 0x2ff, -1\n"
    "v_mov_b32_e32 v10, 0xc2980000\n"
    "v_mfma_f32_16x16x32_bf16 v[68:71], v[116:119], a[64:67], 0\n"
    "s_waitcnt vmcnt(9)\n"
    "v_cmp_lt_u32_e32 vcc, s88, v253\n"
    "s_nop 1\n"
    "v_cndmask_b32_e32 v10, 0, v10, vcc\n"
    "v_mfma_f32_16x16x32_bf16 v[24:27], v[128:131], a[4:7], v[24:27]\n"
    "buffer_load_dwordx4 v[212:215], v253, s[8:11], s47 offen offset:1024 sc1\n"
    "v_mfma_f32_16x16x32_bf16 v[28:31], v[132:135], a[4:7], v[28:31]\n"
    "s_add_u32 s52, s52, 0x4000\n"
    "v_lshrrev_b32_e32 v5, 4, v253\n"
    "v_min_u32_e32 v5, 2, v5\n"
    "v_mfma_f32_16x16x32_bf16 v[48:51], v[128:131], a[36:39], v[48:51]\n"
    "s_add_u32 s88, s32, 1\n"
    "v_add_u32_e32 v5, s88, v5\n"
    "v_mov_b32_e32 v80, 0\n"
    "v_mfma_f32_16x16x32_bf16 v[52:55], v[132:135], a[36:39], v[52:55]\n"
    "buffer_load_dwordx4 v[224:227], v253, s[8:11], s47 offen offset:2048 sc1\n"
    "v_mfma_f32_16x16x32_bf16 v[64:67], v[128:131], a[68:71], v[64:67]\n"
    "s_add_u32 s61, s61, 0x1000\n"
    "v_and_b32_e32 v5, 3, v5\n"
    "v_lshlrev_b32_e32 v5, 7, v5\n"
    "v_mfma_f32_16x16x32_bf16 v[68:71], v[132:135], a[68:71], v[68:71]\n"
    "s_waitcnt vmcnt(9)\n"
    "s_lshl_b32 s58, %1, 7\n"
    "s_add_u32 s58, s58, 5738432\n"
    "s_cmp_eq_u32 %2, 2\n"
    "s_cselect_b32 s58, s58, 2.0\n"
    "v_mfma_f32_16x16x32_bf16 v[24:27], v[144:147], a[8:11], v[24:27]\n"
    "buffer_load_dwordx4 v[228:231], v253, s[8:11], s47 offen offset:3072 sc1\n"
    "v_mfma_f32_16x16x32_bf16 v[28:31], v[148:151], a[8:11], v[28:31]\n"
    "s_bitset0_b32 s61, 14\n"
    "s_lshl_b32 s88, s50, 13\n"
    "s_add_u32 s88, s88, %3\n"
    "v_mfma_f32_16x16x32_bf16 v[48:51], v[144:147], a[40:43], v[48:51]\n"
    "s_add_u32 s47, s52, s61\n"
    "v_add_u32_e32 v6, s88, v253\n"
    "v_add_u32_e32 v7, %3, v253\n"
    "v_mfma_f32_16x16x32_bf16 v[52:55], v[148:151], a[40:43], v[52:55]\n"
    "buffer_load_dwordx4 v[120:123], v253, s[8:11], s47 offen offset:0 sc1\n"
    "v_mfma_f32_16x16x32_bf16 v[64:67], v[144:147], a[72:75], v[64:67]\n"
    "s_mul_i32 s88, %2, 1024\n"
    "s_add_u32 s88, s88, %3\n"
    "v_lshrrev_b32_e32 v250, 2, v253\n"
    "v_mfma_f32_16x16x32_bf16 v[68:71], v[148:151], a[72:75], v[68:71]\n"
    "s_waitcnt vmcnt(9)\n"
    "v_add_u32_e32 v249, s88, v250\n"
    "s_mov_b32 s49, s88\n"
    "v_mfma_f32_16x16x32_bf16 v[24:27], v[160:163], a[12:15], v[24:27]\n"
    "buffer_load_dwordx4 v[124:127], v253, s[8:11], s47 offen offset:1024 sc1\n"
    "v_mfma_f32_16x16x32_bf16 v[28:31], v[164:167], a[12:15], v[28:31]\n"
    "s_lshl_b32 s81, s88, 2\n"
    "s_lshl2_add_u32 s90, %1, %2\n"
    "s_lshl_b32 s90, s90, 10\n"
    "v_mfma_f32_16x16x32_bf16 v[48:51], v[160:163], a[44:47], v[48:51]\n"
    "s_sub_u32 s81, s90, s81\n"
    "s_add_u32 s81, s81, 36369408\n"
    "s_xor_b32 s88, %2, 2\n"
    "v_mfma_f32_16x16x32_bf16 v[52:55], v[164:167], a[44:47], v[52:55]\n"
    "buffer_load_dwordx4 v[136:139], v253, s[8:11], s47 offen offset:2048 sc1\n"
    "v_mfma_f32_16x16x32_bf16 v[64:67], v[160:163], a[76:79], v[64:67]\n"
    "s_add_u32 s61, s61, 0x1000\n"
    "s_mul_i32 s88, s88, 1024\n"
    "s_add_u32 s88, s88, %3\n"
    "v_mfma_f32_16x16x32_bf16 v[68:71], v[164:167], a[76:79], v[68:71]\n"
    "s_waitcnt vmcnt(9)\n"
    "v_add_u32_e32 v250, s88, v250\n"
    "v_and_b32_e32 v251, 0x1f0, v253\n"
    "v_mfma_f32_16x16x32_bf16 v[24:27], v[176:179], a[16:19], v[24:27]\n"
    "buffer_load_dwordx4 v[140:143], v253, s[8:11], s47 offen offset:3072 sc1\n"
    "v_mfma_f32_16x16x32_bf16 v[28:31], v[180:183], a[16:19], v[28:31]\n"
    "s_bitset0_b32 s61, 14\n"
    "s_lshl_b32 s82, %2, 10\n"
    "s_add_u32 s82, s82, %3\n"
    "v_mfma_f32_16x16x32_bf16 v[48:51], v[176:179], a[48:51], v[48:51]\n"
    "s_add_u32 s47, s52, s61\n"
    "s_add_u32 s82, s82, 20480\n"
    "v_lshrrev_b32_e32 v252, 9, v253\n"
    "v_mfma_f32_16x16x32_bf16 v[52:55], v[180:183], a[48:51], v[52:55]\n"
    "buffer_load_dwordx4 v[152:155], v253, s[8:11], s47 offen offset:0 sc1\n"
    "v_mfma_f32_16x16x32_bf16 v[64:67], v[176:179], a[80:83], v[64:67]\n"
    "s_lshr_b32 s80, %1, 4\n"
    "s_lshl_b32 s80, s80, 7\n"
    "v_lshl_add_u32 v252, v252, 4, s82\n"
    "v_mfma_f32_16x16x32_bf16 v[68:71], v[180:183], a[80:83], v[68:71]\n"
    "s_waitcnt vmcnt(9)\n"
    "s_add_u32 s80, s80, 5775616\n"
    "s_and_b32 s88, s32, 1\n"
    "v_mfma_f32_16x16x32_bf16 v[24:27], v[192:195], a[20:23], v[24:27]\n"
    "buffer_load_dwordx4 v[156:159], v253, s[8:11], s47 offen offset:1024 sc1\n"
    "v_mfma_f32_16x16x32_bf16 v[28:31], v[196:199], a[20:23], v[28:31]\n"
    "s_lshl_b32 s88, s88, 6\n"
    "s_add_u32 s80, s80, s88\n"
    "s_mov_b64 s[20:21], s[8:9]\n"
    "v_mfma_f32_16x16x32_bf16 v[48:51], v[192:195], a[52:55], v[48:51]\n"
    "s_mov_b32 s22, 5242880\n"
    "s_add_u32 s24, s8, 5779456\n"
    "s_addc_u32 s25, s9, 0\n"
    "v_mfma_f32_16x16x32_bf16 v[52:55], v[196:199], a[52:55], v[52:55]\n"
    "buffer_load_dwordx4 v[168:171], v253, s[8:11], s47 offen offset:2048 sc1\n"
    "v_mfma_f32_16x16x32_bf16 v[64:67], v[192:195], a[84:87], v[64:67]\n"
    "s_add_u32 s61, s61, 0x1000\n"
    "s_mov_b32 s26, 5242880\n"
    "s_movk_i32 s42, 512\n"
    "v_mfma_f32_16x16x32_bf16 v[68:71], v[196:199], a[84:87], v[68:71]\n"
    "s_waitcnt vmcnt(9)\n"
    "s_mov_b32 s37, 0\n"
    "s_add_u32 s38, s38, s50\n"
    "v_mfma_f32_16x16x32_bf16 v[24:27], v[208:211], a[24:27], v[24:27]\n"
    "buffer_load_dwordx4 v[172:175], v253, s[8:11], s47 offen offset:3072 sc1\n"
    "v_mfma_f32_16x16x32_bf16 v[28:31], v[212:215], a[24:27], v[28:31]\n"
    "s_bitset0_b32 s61, 14\n"
    "v_bfe_u32 v16, v253, 4, 2\n"
    "v_bfe_u32 v17, v253, 6, 2\n"
    "v_mov_b32_e32 v18, 0x3f80\n"
    "v_mfma_f32_16x16x32_bf16 v[48:51], v[208:211], a[56:59], v[48:51]\n"
    "s_add_u32 s47, s52, s61\n"
    "v_cmp_eq_u32_e32 vcc, 3, v16\n"
    "s_nop 1\n"
    "v_cndmask_b32_e64 v18, v18, 1.0, vcc\n"
    "v_cndmask_b32_e64 v16, v16, 0, vcc\n"
    "v_mfma_f32_16x16x32_bf16 v[52:55], v[212:215], a[56:59], v[52:55]\n"
    "buffer_load_dwordx4 v[184:187], v253, s[8:11], s47 offen offset:0 sc1\n"
    "v_mfma_f32_16x16x32_bf16 v[64:67], v[208:211], a[88:91], v[64:67]\n"
    "v_or_b32_e32 v16, 12, v16\n"
    "v_mul_u32_u24_e32 v16, 0x1111, v16\n"
    "v_mfma_f32_16x16x32_bf16 v[68:71], v[212:215], a[88:91], v[68:71]\n"
    "s_waitcnt vmcnt(9)\n"
    "v_cmp_eq_u32_e32 vcc, 0, v17\n"
    "s_nop 1\n"
    "v_cndmask_b32_e32 v19, 0, v18, vcc\n"
    "v_mfma_f32_16x16x32_bf16 v[24:27], v[224:227], a[28:31], v[24:27]\n"
    "buffer_load_dwordx4 v[188:191], v253, s[8:11], s47 offen offset:1024 sc1\n"
    "v_mfma_f32_16x16x32_bf16 v[28:31], v[228:231], a[28:31], v[28:31]\n"
    "v_accvgpr_write_b32 a192, v19\n"
    "v_mov_b32_e32 v81, 0\n"
    "v_mfma_f32_16x16x32_bf16 v[48:51], v[224:227], a[60:63], v[48:51]\n"
    "v_cmp_eq_u32_e32 vcc, 1, v17\n"
    "s_nop 1\n"
    "v_cndmask_b32_e32 v19, 0, v18, vcc\n"
    "v_mfma_f32_16x16x32_bf16 v[52:55], v[228:231], a[60:63], v[52:55]\n"
    "buffer_load_dwordx4 v[200:203], v253, s[8:11], s47 offen offset:2048 sc1\n"
    "v_mfma_f32_16x16x32_bf16 v[64:67], v[224:227], a[92:95], v[64:67]\n"
    "s_add_u32 s61, s61, 0x1000\n"
    "v_cvt_pk_bf16_f32 v24, v24, v25\n"
    "v_mov_b32_e32 v82, 0\n"
    "v_mfma_f32_16x16x32_bf16 v[68:71], v[228:231], a[92:95], v[68:71]\n"
    "s_waitcnt vmcnt(9)\n"
    "v_cvt_pk_bf16_f32 v25, v26, v27\n"
    "v_accvgpr_write_b32 a193, v19\n"
    "v_mfma_f32_16x16x32_bf16 v[32:35], v[120:123], a[0:3], 0\n"
    "buffer_load_dwordx4 v[204:207], v253, s[8:11], s47 offen offset:3072 sc1\n"
    "v_mfma_f32_16x16x32_bf16 v[36:39], v[124:127], a[0:3], 0\n"
    "s_bitset0_b32 s61, 14\n"
    "v_cvt_pk_bf16_f32 v26, v28, v29\n"
    "v_cmp_eq_u32_e32 vcc, 2, v17\n"
    "s_nop 1\n"
    "v_cndmask_b32_e32 v19, 0, v18, vcc\n"
    "v_mfma_f32_16x16x32_bf16 v[56:59], v[120:123], a[32:35], 0\n"
    "s_add_u32 s47, s52, s61\n"
    "v_cvt_pk_bf16_f32 v27, v30, v31\n"
    "v_accvgpr_write_b32 a194, v19\n"
    "v_mfma_f32_16x16x32_bf16 v[60:63], v[124:127], a[32:35], 0\n"
    "buffer_load_dwordx4 v[216:219], v253, s[8:11], s47 offen offset:0 sc1\n"
    "v_mfma_f32_16x16x32_bf16 v[72:75], v[120:123], a[64:67], 0\n"
    "buffer_store_dwordx4 v[24:27], v8, s[8:11], s35 offen offset:0 sc1\n"
    "v_cvt_pk_bf16_f32 v48, v48, v49\n"
    "v_cmp_eq_u32_e32 vcc, 3, v17\n"
    "s_nop 1\n"
    "v_cndmask_b32_e32 v19, 0, v18, vcc\n"
    "v_mfma_f32_16x16x32_bf16 v[76:79], v[124:127], a[64:67], 0\n"
    "s_waitcnt vmcnt(10)\n"
    "v_cvt_pk_bf16_f32 v49, v50, v51\n"
    "v_accvgpr_write_b32 a195, v19\n"
    "v_mfma_f32_16x16x32_bf16 v[32:35], v[136:139], a[4:7], v[32:35]\n"
    "buffer_load_dwordx4 v[220:223], v253, s[8:11], s47 offen offset:1024 sc1\n"
    "v_mfma_f32_16x16x32_bf16 v[36:39], v[140:143], a[4:7], v[36:39]\n"
    "s_add_u32 s52, s52, 0x4000\n"
    "v_cvt_pk_bf16_f32 v50, v52, v53\n"
    "v_cvt_pk_bf16_f32 v51, v54, v55\n"
    "v_mfma_f32_16x16x32_bf16 v[56:59], v[136:139], a[36:39], v[56:59]\n"
    "buffer_load_dword v0, v3, s[8:11], s59 offen offset:0 sc1\n"
    "v_mfma_f32_16x16x32_bf16 v[60:63], v[140:143], a[36:39], v[60:63]\n"
    "buffer_load_dwordx4 v[232:235], v253, s[8:11], s47 offen offset:2048 sc1\n"
    "buffer_store_dwordx4 v[48:51], v8, s[8:11], s54 offen offset:0 sc1\n"
    "v_mov_b32_e32 v83, 0\n"
    "v_mfma_f32_16x16x32_bf16 v[72:75], v[136:139], a[68:71], v[72:75]\n"
    "s_add_u32 s61, s61, 0x1000\n"
    "v_cvt_pk_bf16_f32 v64, v64, v65\n"
    "v_mov_b32_e32 v84, 0\n"
    "v_mfma_f32_16x16x32_bf16 v[76:79], v[140:143], a[68:71], v[76:79]\n"
    "s_waitcnt vmcnt(12)\n"
    "v_cvt_pk_bf16_f32 v65, v66, v67\n"
    "v_mov_b32_e32 v85, 0\n"
    "v_mfma_f32_16x16x32_bf16 v[32:35], v[152:155], a[8:11], v[32:35]\n"
    "buffer_load_dwordx4 v[236:239], v253, s[8:11], s47 offen offset:3072 sc1\n"
    "v_mfma_f32_16x16x32_bf16 v[36:39], v[156:159], a[8:11], v[36:39]\n"
    "s_bitset0_b32 s61, 14\n"
    "v_cvt_pk_bf16_f32 v66, v68, v69\n"
    "v_mov_b32_e32 v86, 0\n"
    "v_mfma_f32_16x16x32_bf16 v[56:59], v[152:155], a[40:43], v[56:59]\n"
    "s_add_u32 s28, s38, 2\n"
    "v_cvt_pk_bf16_f32 v67, v70, v71\n"
    "v_mov_b32_e32 v87, 0\n"
    "v_mfma_f32_16x16x32_bf16 v[60:63], v[156:159], a[40:43], v[60:63]\n"
    "buffer_store_dwordx4 v[64:67], v8, s[8:11], s60 offen offset:0 sc1\n"
    "v_mov_b32_e32 v88, 0\n"
    "v_mov_b32_e32 v89, 0\n"
    "v_mfma_f32_16x16x32_bf16 v[72:75], v[152:155], a[72:75], v[72:75]\n"
    "s_sub_u32 s88, s28, 10\n"
    "s_min_u32 s28, s28, s88\n"
    "v_mov_b32_e32 v90, 0\n"
    "v_mfma_f32_16x16x32_bf16 v[76:79], v[156:159], a[72:75], v[76:79]\n"
    "s_waitcnt vmcnt(12)\n"
    "s_lshl_b32 s88, s28, 0\n"
    "v_mov_b32_e32 v91, 0\n"
    "v_mfma_f32_16x16x32_bf16 v[32:35], v[168:171], a[12:15], v[32:35]\n"
    "s_or_b32 s36, s88, s88\n"
    "s_lshl1_add_u32 s88, s28, 1\n"
    "v_mov_b32_e32 v92, 0\n"
    "v_mfma_f32_16x16x32_bf16 v[36:39], v[172:175], a[12:15], v[36:39]\n"
    "s_mul_i32 s88, s88, 52\n"
    "s_lshr_b32 s88, s88, 8\n"
    "v_mov_b32_e32 v93, 0\n"
    "v_mfma_f32_16x16x32_bf16 v[56:59], v[168:171], a[44:47], v[56:59]\n"
    "s_mul_i32 s90, s88, 5\n"
    "ds_write_b128 v9, v[24:27] offset:0\n"
    "v_mfma_f32_16x16x32_bf16 v[60:63], v[172:175], a[44:47], v[60:63]\n"
    "s_lshr_b32 s90, s90, 1\n"
    "s_sub_u32 s28, s28, s90\n"
    "v_mov_b32_e32 v94, 0\n"
    "v_mfma_f32_16x16x32_bf16 v[72:75], v[168:171], a[76:79], v[72:75]\n"
    "s_sub_u32 s90, s32, s88\n"
    "s_sub_u32 s90, s90, 1\n"
    "v_mov_b32_e32 v95, 0\n"
    "v_mfma_f32_16x16x32_bf16 v[76:79], v[172:175], a[76:79], v[76:79]\n"
    "s_waitcnt vmcnt(10)\n"
    "s_and_b32 s90, s90, 3\n"
    "v_mov_b32_e32 v96, 0\n"
    "v_mfma_f32_16x16x32_bf16 v[32:35], v[184:187], a[16:19], v[32:35]\n"
    "s_lshl2_add_u32 s88, s33, s88\n"
    "s_mul_i32 s88, s88, 3\n"
    "v_mov_b32_e32 v97, 0\n"
    "v_mfma_f32_16x16x32_bf16 v[36:39], v[188:191], a[16:19], v[36:39]\n"
    "s_add_u32 s88, s88, s90\n"
    "v_mov_b32_e32 v98, 0\n"
    "v_mov_b32_e32 v99, 0\n"
    "v_mfma_f32_16x16x32_bf16 v[56:59], v[184:187], a[48:51], v[56:59]\n"
    "s_mul_i32 s88, s88, 24832\n"
    "s_add_u32 s88, s88, 16265216\n"
    "v_mov_b32_e32 v100, 0\n"
    "v_mfma_f32_16x16x32_bf16 v[60:63], v[188:191], a[48:51], v[60:63]\n"
    "s_cmp_eq_u32 s90, 3\n"
    "s_cselect_b32 s88, 2.0, s88\n"
    "s_cselect_b32 s89, 2, 3\n"
    "v_mfma_f32_16x16x32_bf16 v[72:75], v[184:187], a[80:83], v[72:75]\n"
    "s_lshl1_add_u32 s83, s28, s51\n"
    "s_lshl_b32 s83, s83, 12\n"
    "v_mov_b32_e32 v101, 0\n"
    "v_mfma_f32_16x16x32_bf16 v[76:79], v[188:191], a[80:83], v[76:79]\n"
    "s_waitcnt vmcnt(8)\n"
    "ds_write_b128 v9, v[48:51] offset:8192\n"
    "v_mfma_f32_16x16x32_bf16 v[32:35], v[200:203], a[20:23], v[32:35]\n"
    "s_add_u32 s83, s83, s88\n"
    "s_lshl2_add_u32 s28, s28, s88\n"
    "v_mov_b32_e32 v102, 0\n"
    "v_mfma_f32_16x16x32_bf16 v[36:39], v[204:207], a[20:23], v[36:39]\n"
    "s_add_u32 s29, s38, 4\n"
    "s_sub_u32 s88, s29, 10\n"
    "v_mov_b32_e32 v103, 0\n"
    "v_mfma_f32_16x16x32_bf16 v[56:59], v[200:203], a[52:55], v[56:59]\n"
    "s_min_u32 s29, s29, s88\n"
    "s_lshl_b32 s88, s29, 4\n"
    "v_mov_b32_e32 v104, 0\n"
    "v_mfma_f32_16x16x32_bf16 v[60:63], v[204:207], a[52:55], v[60:63]\n"
    "s_or_b32 s36, s88, s36\n"
    "s_lshl1_add_u32 s88, s29, 1\n"
    "v_mov_b32_e32 v105, 0\n"
    "v_mfma_f32_16x16x32_bf16 v[72:75], v[200:203], a[84:87], v[72:75]\n"
    "s_waitcnt vmcnt(4)\n"
    "v_cmp_ne_u32_e32 vcc, s39, v0\n"
    "v_mov_b32_e32 v106, 0\n"
    "v_mfma_f32_16x16x32_bf16 v[76:79], v[204:207], a[84:87], v[76:79]\n"
    "s_waitcnt vmcnt(5)\n"
    "s_mul_i32 s88, s88, 52\n"
    "v_mov_b32_e32 v107, 0\n"
    "v_mfma_f32_16x16x32_bf16 v[32:35], v[216:219], a[24:27], v[32:35]\n"
    "s_lshr_b32 s88, s88, 8\n"
    "s_mul_i32 s90, s88, 5\n"
    "v_mfma_f32_16x16x32_bf16 v[36:39], v[220:223], a[24:27], v[36:39]\n"
    "s_lshr_b32 s90, s90, 1\n"
    "ds_write_b128 v9, v[64:67] offset:16384\n"
    "v_mfma_f32_16x16x32_bf16 v[56:59], v[216:219], a[56:59], v[56:59]\n"
    "s_cbranch_vccnz .Lat_h1poll\n"
    ".Lat_h1poll_ret:\n"
    "v_mfma_f32_16x16x32_bf16 v[60:63], v[220:223], a[56:59], v[60:63]\n"
    "s_mov_b32 m0, s63\n"
    "s_sub_u32 s29, s29, s90\n"
    "v_mov_b32_e32 v108, 0\n"
    "v_mfma_f32_16x16x32_bf16 v[72:75], v[216:219], a[88:91], v[72:75]\n"
    "buffer_load_dwordx4 v254, s[8:11], s57 offen offset:0 sc1 lds\n"
    "v_mfma_f32_16x16x32_bf16 v[76:79], v[220:223], a[88:91], v[76:79]\n"
    "s_waitcnt vmcnt(2)\n"
    "s_sub_u32 m0, s63, 1024\n"
    "v_mov_b32_e32 v109, 0\n"
    "v_mfma_f32_16x16x32_bf16 v[32:35], v[232:235], a[28:31], v[32:35]\n"
    "buffer_load_dwordx4 v254, s[8:11], s57 offen offset:2048 sc1 lds\n"
    "v_mfma_f32_16x16x32_bf16 v[36:39], v[236:239], a[28:31], v[36:39]\n"
    "s_add_u32 m0, s63, 3968\n"
    "s_sub_u32 s90, s32, s88\n"
    "v_mov_b32_e32 v110, 0\n"
    "v_mfma_f32_16x16x32_bf16 v[56:59], v[232:235], a[60:63], v[56:59]\n"
    "buffer_load_dwordx4 v254, s[8:11], s57 offen offset:128 sc1 lds\n"
    "v_mfma_f32_16x16x32_bf16 v[60:63], v[236:239], a[60:63], v[60:63]\n"
    "s_add_u32 m0, s63, 2944\n"
    "s_sub_u32 s90, s90, 1\n"
    "s_and_b32 s90, s90, 3\n"
    "v_mfma_f32_16x16x32_bf16 v[72:75], v[232:235], a[92:95], v[72:75]\n"
    "buffer_load_dwordx4 v254, s[8:11], s57 offen offset:2176 sc1 lds\n"
    "v_mfma_f32_16x16x32_bf16 v[76:79], v[236:239], a[92:95], v[76:79]\n"
    "s_lshl2_add_u32 s88, s33, s88\n"
    "s_mul_i32 s88, s88, 3\n"
    "v_mov_b32_e32 v111, 0\n"
    "v_cvt_pk_bf16_f32 v32, v32, v33\n"
    "v_cvt_pk_bf16_f32 v33, v34, v35\n"
    "v_cvt_pk_bf16_f32 v34, v36, v37\n"
    "v_cvt_pk_bf16_f32 v35, v38, v39\n"
    "v_or_b32_e32 v35, v10, v35\n"
    "ds_write_b128 v9, v[32:35] offset:1024\n"
    "buffer_store_dwordx4 v[32:35], v8, s[8:11], s35 offen offset:1024 sc1\n"
    "v_cvt_pk_bf16_f32 v56, v56, v57\n"
    "v_cvt_pk_bf16_f32 v57, v58, v59\n"
    "v_cvt_pk_bf16_f32 v58, v60, v61\n"
    "v_cvt_pk_bf16_f32 v59, v62, v63\n"
    "v_or_b32_e32 v59, v10, v59\n"
    "ds_write_b128 v9, v[56:59] offset:9216\n"
    "buffer_store_dwordx4 v[56:59], v8, s[8:11], s54 offen offset:1024 sc1\n"
    "v_cvt_pk_bf16_f32 v72, v72, v73\n"
    "v_cvt_pk_bf16_f32 v73, v74, v75\n"
    "v_cvt_pk_bf16_f32 v74, v76, v77\n"
    "v_cvt_pk_bf16_f32 v75, v78, v79\n"
    "v_or_b32_e32 v75, v10, v75\n"
    "ds_write_b128 v9, v[72:75] offset:17408\n"
    "buffer_store_dwordx4 v[72:75], v8, s[8:11], s60 offen offset:1024 sc1\n"
    "v_mov_b32_e32 v112, 0\n"
    "v_mov_b32_e32 v113, 0\n"
    "v_mov_b32_e32 v114, 0\n"
    "s_add_u32 m0, s62, 0\n"
    "v_mov_b32_e32 v115, 0\n"
    "v_mov_b32_e32 v116, 0\n"
    "v_mov_b32_e32 v117, 0\n"
    "buffer_load_dwordx4 v12, s[8:11], s40 offen offset:0 sc1 lds\n"
    "v_mov_b32_e32 v118, 0\n"
    "v_mov_b32_e32 v119, 0\n"
    "v_mov_b32_e32 v120, 0\n"
    "buffer_load_dwordx4 v13, s[8:11], s40 offen offset:1024 sc1 lds\n"
    "v_mov_b32_e32 v121, 0\n"
    "v_mov_b32_e32 v122, 0\n"
    "v_mov_b32_e32 v123, 0\n"
    "buffer_load_dwordx4 v14, s[8:11], s40 offen offset:2048 sc1 lds\n"
    "v_mov_b32_e32 v124, 0\n"
    "v_mov_b32_e32 v125, 0\n"
    "v_mov_b32_e32 v126, 0\n"
    "buffer_load_dwordx4 v15, s[8:11], s40 offen offset:3072 sc1 lds\n"
    "v_mov_b32_e32 v127, 0\n"
    "v_mov_b32_e32 v128, 0\n"
    "v_mov_b32_e32 v129, 0\n"
    "s_add_u32 s40, s40, 0x8000\n"
    "v_mov_b32_e32 v130, 0\n"
    "v_mov_b32_e32 v131, 0\n"
    "v_mov_b32_e32 v132, 0\n"
    "s_add_u32 m0, s62, 4096\n"
    "v_mov_b32_e32 v133, 0\n"
    "v_mov_b32_e32 v134, 0\n"
    "v_mov_b32_e32 v135, 0\n"
    "buffer_load_dwordx4 v12, s[8:11], s41 offen offset:0 sc1 lds\n"
    "v_mov_b32_e32 v136, 0\n"
    "v_mov_b32_e32 v137, 0\n"
    "v_mov_b32_e32 v138, 0\n"
    "buffer_load_dwordx4 v13, s[8:11], s41 offen offset:1024 sc1 lds\n"
    "v_mov_b32_e32 v139, 0\n"
    "v_mov_b32_e32 v140, 0\n"
    "v_mov_b32_e32 v141, 0\n"
    "buffer_load_dwordx4 v14, s[8:11], s41 offen offset:2048 sc1 lds\n"
    "v_mov_b32_e32 v142, 0\n"
    "v_mov_b32_e32 v143, 0\n"
    "v_mov_b32_e32 v144, 0\n"
    "buffer_load_dwordx4 v15, s[8:11], s41 offen offset:3072 sc1 lds\n"
    "v_mov_b32_e32 v145, 0\n"
    "v_mov_b32_e32 v146, 0\n"
    "v_mov_b32_e32 v147, 0\n"
    "s_add_u32 s41, s41, 0x8000\n"
    "v_mov_b32_e32 v148, 0\n"
    "v_mov_b32_e32 v149, 0\n"
    "v_mov_b32_e32 v150, 0\n"
    "v_mov_b32_e32 v151, 0\n"
    "s_xor_b32 s45, s34, 0xc000\n"
    "v_add_u32_e32 v204, s45, v240\n"
    "v_add_u32_e32 v205, s45, v241\n"
    "v_add_u32_e32 v206, s45, v242\n"
    "v_add_u32_e32 v207, s45, v243\n"
    "v_add_u32_e32 v208, s45, v244\n"
    "v_add_u32_e32 v209, s45, v245\n"
    "v_add_u32_e32 v210, s45, v246\n"
    "buffer_load_dword v0, v253, s[8:11], s59 offen offset:0 sc1\n"
    "buffer_load_dword v1, v2, s[8:11], s59 offen offset:0 sc1\n"
    "v_add_u32_e32 v211, s45, v247\n"
    "v_add_u32_e32 v200, s34, v240\n"
    "v_add_u32_e32 v201, s34, v241\n"
    "v_add_u32_e32 v202, s34, v242\n"
    "v_add_u32_e32 v203, s34, v243\n"
    "v_mov_b32_e32 v152, v16\n"
    "v_and_b32_e32 v153, 0xf0, v253\n"
    "v_lshrrev_b32_e32 v154, 9, v253\n"
    "v_lshl_add_u32 v153, v154, 8, v153\n"
    "v_bfe_u32 v154, v253, 8, 1\n"
    "v_lshl_add_u32 v153, v154, 3, v153\n"
    "v_mov_b32_e32 v156, 0\n"
    "v_mov_b32_e32 v157, 0\n"
    "v_mov_b32_e32 v158, 0\n"
    "v_mov_b32_e32 v159, 0\n"
    "v_mov_b32_e32 v56, 0\n"
    "v_mov_b32_e32 v57, 0\n"
    "v_mov_b32_e32 v58, 0\n"
    "v_mov_b32_e32 v59, 0\n"
    "v_mov_b32_e32 v60, 0\n"
    "v_mov_b32_e32 v61, 0\n"
    "v_mov_b32_e32 v62, 0\n"
    "v_mov_b32_e32 v63, 0\n"
    "v_mov_b32_e32 v64, 0\n"
    "v_mov_b32_e32 v65, 0\n"
    "v_mov_b32_e32 v66, 0\n"
    "v_mov_b32_e32 v67, 0\n"
    "v_mov_b32_e32 v68, 0\n"
    "v_mov_b32_e32 v69, 0\n"
    "v_mov_b32_e32 v70, 0\n"
    "v_mov_b32_e32 v71, 0\n"
    "v_mov_b32_e32 v72, 0\n"
    "v_mov_b32_e32 v73, 0\n"
    "v_mov_b32_e32 v74, 0\n"
    "v_mov_b32_e32 v75, 0\n"
    "v_mov_b32_e32 v76, 0\n"
    "v_mov_b32_e32 v77, 0\n"
    "v_mov_b32_e32 v78, 0\n"
    "v_mov_b32_e32 v79, 0\n"
    "s_add_u32 s88, s88, s90\n"
    "s_mul_i32 s88, s88, 24832\n"
    "s_add_u32 s88, s88, 16265216\n"
    "s_lshl1_add_u32 s84, s29, s51\n"
    "s_lshl_b32 s84, s84, 12\n"
    "s_add_u32 s84, s84, s88\n"
    "s_lshl2_add_u32 s29, s29, s88\n"
    "s_add_u32 s30, s38, 6\n"
    "s_sub_u32 s88, s30, 10\n"
    "s_min_u32 s30, s30, s88\n"
    "s_lshl_b32 s88, s30, 8\n"
    "s_or_b32 s36, s88, s36\n"
    "s_lshl1_add_u32 s88, s30, 1\n"
    "s_mul_i32 s88, s88, 52\n"
    "s_lshr_b32 s88, s88, 8\n"
    "s_mul_i32 s90, s88, 5\n"
    "s_lshr_b32 s90, s90, 1\n"
    "s_sub_u32 s30, s30, s90\n"
    "s_sub_u32 s90, s32, s88\n"
    "s_sub_u32 s90, s90, 1\n"
    "s_and_b32 s90, s90, 3\n"
    "s_lshl2_add_u32 s88, s33, s88\n"
    "s_mul_i32 s88, s88, 3\n"
    "s_add_u32 s88, s88, s90\n"
    "s_mul_i32 s88, s88, 24832\n"
    "s_add_u32 s88, s88, 16265216\n"
    "s_lshl1_add_u32 s85, s30, s51\n"
    "s_lshl_b32 s85, s85, 12\n"
    "s_add_u32 s85, s85, s88\n"
    "s_lshl2_add_u32 s30, s30, s88\n"
    "s_add_u32 s31, s38, 8\n"
    "s_sub_u32 s88, s31, 10\n"
    "s_min_u32 s31, s31, s88\n"
    "s_lshl_b32 s88, s31, 12\n"
    "s_or_b32 s36, s88, s36\n"
    "s_lshl1_add_u32 s88, s31, 1\n"
    "s_mul_i32 s88, s88, 52\n"
    "s_lshr_b32 s88, s88, 8\n"
    "s_mul_i32 s90, s88, 5\n"
    "s_lshr_b32 s90, s90, 1\n"
    "s_sub_u32 s31, s31, s90\n"
    "s_sub_u32 s90, s32, s88\n"
    "s_sub_u32 s90, s90, 1\n"
    "s_and_b32 s90, s90, 3\n"
    "s_lshl2_add_u32 s88, s33, s88\n"
    "s_mul_i32 s88, s88, 3\n"
    "s_add_u32 s88, s88, s90\n"
    "s_mul_i32 s88, s88, 24832\n"
    "s_add_u32 s88, s88, 16265216\n"
    "s_lshl1_add_u32 s86, s31, s51\n"
    "s_lshl_b32 s86, s86, 12\n"
    "s_add_u32 s86, s86, s88\n"
    "s_lshl2_add_u32 s31, s31, s88\n"
    "s_lshl_b32 s87, %1, 6\n"
    "s_add_u32 s87, s87, %2\n"
    "s_lshl2_add_u32 s87, s87, 5636096\n"
    "s_or_b32 s88, %1, %2\n"
    "s_cselect_b32 s88, 2.0, 5777664\n"
    "s_lshl_b32 s47, s33, 9\n"
    "s_add_u32 s47, s47, 5738496\n"
    "v_lshlrev_b32_e32 v9, 3, v253\n"
    "s_cmp_eq_u32 s50, 0\n"
    "s_cselect_b32 s46, s35, s54\n"
    "s_cselect_b32 s45, s60, 2.0\n"
    "s_cmp_eq_u32 s51, 0\n"
    "s_cselect_b32 s46, s46, s45\n"
    "s_add_u32 s46, s46, 11022336\n"
    "s_waitcnt vmcnt(10)\n"
    "s_waitcnt lgkmcnt(0)\n"
    "s_barrier\n"
    "buffer_store_dword v40, v41, s[8:11], s58 offen offset:64 sc1\n"
    "buffer_load_dword v228, v9, s[8:11], s46 offen offset:0 sc1\n"
    "ds_read_b128 a[0:3], v6 offset:0\n"
    "ds_read_b128 a[4:7], v6 offset:1024\n"
    "ds_read_b128 a[8:11], v6 offset:2048\n"
    "ds_read_b128 a[12:15], v6 offset:3072\n"
    "ds_read_b128 a[16:19], v6 offset:4096\n"
    "ds_read_b128 a[20:23], v6 offset:5120\n"
    "ds_read_b128 a[24:27], v6 offset:6144\n"
    "ds_read_b128 a[28:31], v6 offset:7168\n"
    "s_waitcnt vmcnt(4)\n"
    "s_barrier\n"
    "ds_read_b128 a[160:163], v204 offset:0\n"
    "ds_read_b128 a[164:167], v205 offset:0\n"
    "ds_read_b128 a[168:171], v204 offset:4096\n"
    "ds_read_b128 a[172:175], v205 offset:4096\n"
    "ds_read_b128 a[176:179], v206 offset:0\n"
    "ds_read_b128 a[180:183], v207 offset:0\n"
    "ds_read_b128 a[184:187], v206 offset:4096\n"
    "ds_read_b128 a[188:191], v207 offset:4096\n"
    "s_add_u32 m0, s62, 16384\n"
    "s_nop 0\n"
    "buffer_load_dwordx4 v12, s[8:11], s40 offen offset:0 sc1 lds\n"
    "buffer_load_dwordx4 v13, s[8:11], s40 offen offset:1024 sc1 lds\n"
    "buffer_load_dwordx4 v14, s[8:11], s40 offen offset:2048 sc1 lds\n"
    "buffer_load_dwordx4 v15, s[8:11], s40 offen offset:3072 sc1 lds\n"
    "s_add_u32 s40, s40, 0x8000\n"
    "s_add_u32 m0, s62, 20480\n"
    "s_nop 0\n"
    "buffer_load_dwordx4 v12, s[8:11], s41 offen offset:0 sc1 lds\n"
    "buffer_load_dwordx4 v13, s[8:11], s41 offen offset:1024 sc1 lds\n"
    "buffer_load_dwordx4 v14, s[8:11], s41 offen offset:2048 sc1 lds\n"
    "buffer_load_dwordx4 v15, s[8:11], s41 offen offset:3072 sc1 lds\n"
    "s_add_u32 s41, s41, 0x8000\n"
    "s_waitcnt lgkmcnt(4)\n"
    "v_mfma_f32_16x16x32_bf16 v[220:223], a[160:163], a[0:3], 0\n"
    "ds_read_b128 a[32:35], v7 offset:16384\n"
    "ds_read_b128 a[36:39], v7 offset:17408\n"
    "v_mfma_f32_16x16x32_bf16 v[220:223], a[164:167], a[4:7], v[220:223]\n"
    "ds_read_b128 a[40:43], v7 offset:18432\n"
    "ds_read_b128 a[44:47], v7 offset:19456\n"
    "v_mfma_f32_16x16x32_bf16 v[220:223], a[168:171], a[8:11], v[220:223]\n"
    "ds_read_b128 a[48:51], v7 offset:20480\n"
    "ds_read_b128 a[52:55], v7 offset:21504\n"
    "v_mfma_f32_16x16x32_bf16 v[220:223], a[172:175], a[12:15], v[220:223]\n"
    "ds_read_b128 a[56:59], v7 offset:22528\n"
    "ds_read_b128 a[60:63], v7 offset:23552\n"
    "s_waitcnt lgkmcnt(8)\n"
    "v_mfma_f32_16x16x32_bf16 v[220:223], a[176:179], a[16:19], v[220:223]\n"
    "ds_read_b64_tr_b16 a[232:233], v208 offset:0\n"
    "ds_read_b64_tr_b16 a[234:235], v209 offset:0\n"
    "v_mfma_f32_16x16x32_bf16 v[220:223], a[180:183], a[20:23], v[220:223]\n"
    "ds_read_b64_tr_b16 a[236:237], v210 offset:0\n"
    "ds_read_b64_tr_b16 a[238:239], v211 offset:0\n"
    "v_mfma_f32_16x16x32_bf16 v[220:223], a[184:187], a[24:27], v[220:223]\n"
    "ds_read_b64_tr_b16 a[240:241], v208 offset:4096\n"
    "ds_read_b64_tr_b16 a[242:243], v209 offset:4096\n"
    "v_mfma_f32_16x16x32_bf16 v[220:223], a[188:191], a[28:31], v[220:223]\n"
    "buffer_load_dword v11, v5, s[8:11], s47 offen offset:0 sc1\n"
    "ds_read_b64_tr_b16 a[244:245], v210 offset:4096\n"
    "s_waitcnt lgkmcnt(11)\n"
    "v_mfma_f32_16x16x32_bf16 v[224:227], a[160:163], a[32:35], 0\n"
    "ds_read_b64_tr_b16 a[246:247], v211 offset:4096\n"
    "ds_read_b128 a[200:203], v203 offset:6144\n"
    "v_mfma_f32_16x16x32_bf16 v[224:227], a[164:167], a[36:39], v[224:227]\n"
    "ds_read_b128 a[204:207], v200 offset:2048\n"
    "ds_read_b128 a[208:211], v201 offset:2048\n"
    "v_mfma_f32_16x16x32_bf16 v[224:227], a[168:171], a[40:43], v[224:227]\n"
    "ds_read_b128 a[212:215], v200 offset:6144\n"
    "ds_read_b128 a[216:219], v201 offset:6144\n"
    "v_mfma_f32_16x16x32_bf16 v[224:227], a[172:175], a[44:47], v[224:227]\n"
    "ds_read_b128 a[220:223], v202 offset:2048\n"
    "ds_read_b128 a[224:227], v203 offset:2048\n"
    "s_waitcnt lgkmcnt(15)\n"
    "v_mfma_f32_16x16x32_bf16 v[224:227], a[176:179], a[48:51], v[224:227]\n"
    "ds_read_b128 a[228:231], v202 offset:6144\n"
    "v_mfma_f32_16x16x32_bf16 v[224:227], a[180:183], a[52:55], v[224:227]\n"
    "v_mfma_f32_16x16x32_bf16 v[224:227], a[184:187], a[56:59], v[224:227]\n"
    "v_mfma_f32_16x16x32_bf16 v[224:227], a[188:191], a[60:63], v[224:227]\n"
    "s_andn2_b32 s61, s32, s50\n"
    "s_bfe_i32 s61, s61, 0x10000\n"
    "ds_read_b128 a[160:163], v203 offset:4096\n"
    "ds_read_b128 a[164:167], v200 offset:0\n"
    "ds_read_b128 a[168:171], v201 offset:0\n"
    "ds_read_b128 a[172:175], v200 offset:4096\n"
    "ds_read_b128 a[176:179], v201 offset:4096\n"
    "ds_read_b128 a[180:183], v202 offset:0\n"
    "ds_read_b128 a[184:187], v203 offset:0\n"
    "ds_read_b128 a[188:191], v202 offset:4096\n"
    "s_and_b32 s57, s63, 0xffffdfff\n"
    "s_sub_u32 s57, s57, 16384\n"
    "v_add_u32_e32 v216, s57, v253\n"
    "s_lshl_b32 s45, s51, 12\n"
    "s_add_u32 s57, s57, s45\n"
    "s_mul_i32 s48, s33, 81920\n"
    "s_add_u32 s48, s48, s45\n"
    "s_add_u32 s48, s48, 11022336\n"
    "v_exp_f32_e32 v220, v220\n"
    "v_exp_f32_e32 v221, v221\n"
    "v_exp_f32_e32 v222, v222\n"
    "v_exp_f32_e32 v223, v223\n"
    "v_exp_f32_e32 v224, v224\n"
    "v_exp_f32_e32 v225, v225\n"
    "v_exp_f32_e32 v226, v226\n"
    "v_exp_f32_e32 v227, v227\n"
    "v_cvt_pk_bf16_f32 v220, v220, v221\n"
    "v_cvt_pk_bf16_f32 v221, v222, v223\n"
    "v_cvt_pk_bf16_f32 v222, v224, v225\n"
    "v_cvt_pk_bf16_f32 v223, v226, v227\n"
    "v_and_b32_e32 v222, s61, v222\n"
    "v_and_b32_e32 v223, s61, v223\n"
    "s_waitcnt vmcnt(11)\n"
    "s_waitcnt lgkmcnt(0)\n"
    "s_barrier\n"
    "v_xor_b32_e32 v1, s39, v1\n"
    "v_xor_b32_e32 v0, s39, v0\n"
    "v_or_b32_e32 v0, v0, v1\n"
    "v_cmp_ne_u32_e32 vcc, 0, v0\n"
    "s_nop 3\n"
    "s_cbranch_vccnz .Lat_hpoll\n"
    ".Lat_hpoll_ret:\n"
    "s_waitcnt vmcnt(0)\n"
    "v_cmp_ne_u32_e32 vcc, s39, v11\n"
    "s_nop 3\n"
    "s_cbranch_vccnz .Lat_qpoll\n"
    ".Lat_qpoll_ret:\n"
    "s_cmp_eq_u32 s50, 0\n"
    "s_cbranch_scc1 .Lat_qx\n"
    "s_bfe_u32 s45, s36, 0x40000\n"
    "s_add_u32 m0, s57, 0\n"
    "s_lshl_b32 s45, s45, 13\n"
    "s_add_u32 s45, s45, s48\n"
    "buffer_load_dwordx4 v253, s[8:11], s45 offen offset:0 sc1 lds\n"
    "buffer_load_dwordx4 v253, s[8:11], s45 offen offset:1024 sc1 lds\n"
    "buffer_load_dwordx4 v253, s[8:11], s45 offen offset:2048 sc1 lds\n"
    "buffer_load_dwordx4 v253, s[8:11], s45 offen offset:3072 sc1 lds\n"
    ".Lat_qx:\n"
    "s_bfe_u32 s45, s36, 0x40004\n"
    "s_add_u32 m0, s57, 8192\n"
    "s_lshl_b32 s45, s45, 13\n"
    "s_add_u32 s45, s45, s48\n"
    "buffer_load_dwordx4 v253, s[8:11], s45 offen offset:0 sc1 lds\n"
    "buffer_load_dwordx4 v253, s[8:11], s45 offen offset:1024 sc1 lds\n"
    "buffer_load_dwordx4 v253, s[8:11], s45 offen offset:2048 sc1 lds\n"
    "buffer_load_dwordx4 v253, s[8:11], s45 offen offset:3072 sc1 lds\n"
    "v_mfma_f32_16x16x16_bf16 v[52:55], a[246:247], v[222:223], 0\n"
    "v_mfma_f32_16x16x16_bf16 v[48:51], a[244:245], v[222:223], 0\n"
    "s_bfe_u32 s45, s36, 0x40008\n"
    "s_add_u32 m0, s57, 16384\n"
    "s_lshl_b32 s45, s45, 13\n"
    "s_add_u32 s45, s45, s48\n"
    "v_mfma_f32_16x16x16_bf16 v[44:47], a[242:243], v[222:223], 0\n"
    "buffer_load_dwordx4 v253, s[8:11], s45 offen offset:0 sc1 lds\n"
    "v_mfma_f32_16x16x16_bf16 v[40:43], a[240:241], v[222:223], 0\n"
    "v_mfma_f32_16x16x16_bf16 v[36:39], a[238:239], v[222:223], 0\n"
    "buffer_load_dwordx4 v253, s[8:11], s45 offen offset:1024 sc1 lds\n"
    "v_mfma_f32_16x16x16_bf16 v[32:35], a[236:237], v[222:223], 0\n"
    "v_mfma_f32_16x16x16_bf16 a[252:255], a[234:235], v[222:223], 0\n"
    "buffer_load_dwordx4 v253, s[8:11], s45 offen offset:2048 sc1 lds\n"
    "v_mfma_f32_16x16x16_bf16 a[248:251], a[232:233], v[222:223], 0\n"
    "v_mfma_f32_16x16x16_bf16 v[28:31], a[246:247], v[220:221], 0\n"
    "buffer_load_dwordx4 v253, s[8:11], s45 offen offset:3072 sc1 lds\n"
    "v_mfma_f32_16x16x16_bf16 v[24:27], a[244:245], v[220:221], 0\n"
    "s_bfe_u32 s45, s36, 0x4000c\n"
    "s_add_u32 m0, s57, 24576\n"
    "s_lshl_b32 s45, s45, 13\n"
    "s_add_u32 s45, s45, s48\n"
    "v_mfma_f32_16x16x16_bf16 v[20:23], a[242:243], v[220:221], 0\n"
    "buffer_load_dwordx4 v253, s[8:11], s45 offen offset:0 sc1 lds\n"
    "v_mfma_f32_16x16x16_bf16 v[16:19], a[240:241], v[220:221], 0\n"
    "v_mfma_f32_16x16x16_bf16 v[12:15], a[238:239], v[220:221], 0\n"
    "buffer_load_dwordx4 v253, s[8:11], s45 offen offset:1024 sc1 lds\n"
    "v_mfma_f32_16x16x16_bf16 v[8:11], a[236:237], v[220:221], 0\n"
    "v_mfma_f32_16x16x16_bf16 v[4:7], a[234:235], v[220:221], 0\n"
    "buffer_load_dwordx4 v253, s[8:11], s45 offen offset:2048 sc1 lds\n"
    "v_mfma_f32_16x16x16_bf16 v[0:3], a[232:233], v[220:221], 0\n"
    "v_mfma_f32_16x16x32_bf16 v[160:163], a[160:163], a[28:31], 0\n"
    "buffer_load_dwordx4 v253, s[8:11], s45 offen offset:3072 sc1 lds\n"
    "v_mfma_f32_16x16x32_bf16 v[160:163], a[164:167], a[0:3], v[160:163]\n"
    "s_mov_b32 m0, s49\n"
    "v_mfma_f32_16x16x32_bf16 v[160:163], a[168:171], a[4:7], v[160:163]\n"
    "v_mfma_f32_16x16x32_bf16 v[160:163], a[172:175], a[8:11], v[160:163]\n"
    "v_mfma_f32_16x16x32_bf16 v[160:163], a[176:179], a[12:15], v[160:163]\n"
    "v_mfma_f32_16x16x32_bf16 v[160:163], a[180:183], a[16:19], v[160:163]\n"
    "v_mfma_f32_16x16x32_bf16 v[160:163], a[184:187], a[20:23], v[160:163]\n"
    "v_mfma_f32_16x16x32_bf16 v[160:163], a[188:191], a[24:27], v[160:163]\n"
    "v_mfma_f32_16x16x32_bf16 v[164:167], a[200:203], a[28:31], 0\n"
    "v_mfma_f32_16x16x32_bf16 v[164:167], a[204:207], a[0:3], v[164:167]\n"
    "v_mfma_f32_16x16x32_bf16 v[164:167], a[208:211], a[4:7], v[164:167]\n"
    "v_exp_f32_e32 v160, v160\n"
    "v_mfma_f32_16x16x32_bf16 v[164:167], a[212:215], a[8:11], v[164:167]\n"
    "v_exp_f32_e32 v161, v161\n"
    "v_mfma_f32_16x16x32_bf16 v[164:167], a[216:219], a[12:15], v[164:167]\n"
    "v_exp_f32_e32 v162, v162\n"
    "v_mfma_f32_16x16x32_bf16 v[164:167], a[220:223], a[16:19], v[164:167]\n"
    "v_exp_f32_e32 v163, v163\n"
    "v_mfma_f32_16x16x32_bf16 v[164:167], a[224:227], a[20:23], v[164:167]\n"
    "v_cvt_pk_bf16_f32 v160, v160, v161\n"
    "v_mfma_f32_16x16x32_bf16 v[164:167], a[228:231], a[24:27], v[164:167]\n"
    "s_waitcnt vmcnt(8)\n"
    "s_barrier\n"
    "s_cmp_eq_u32 s50, 0\n"
    "s_cbranch_scc1 .Lat_qy\n"
    "ds_read_b128 a[60:63], v216 offset:7168\n"
    "ds_read_b128 a[32:35], v216 offset:0\n"
    "ds_read_b128 a[36:39], v216 offset:1024\n"
    "ds_read_b128 a[40:43], v216 offset:2048\n"
    "ds_read_b128 a[44:47], v216 offset:3072\n"
    "ds_read_b128 a[48:51], v216 offset:4096\n"
    "ds_read_b128 a[52:55], v216 offset:5120\n"
    "ds_read_b128 a[56:59], v216 offset:6144\n"
    ".Lat_qy:\n"
    "ds_read_b128 a[92:95], v216 offset:15360\n"
    "ds_read_b128 a[64:67], v216 offset:8192\n"
    "s_waitcnt lgkmcnt(8)\n"
    "v_mfma_f32_16x16x32_bf16 v[168:171], a[160:163], a[60:63], 0\n"
    "v_cvt_pk_bf16_f32 v161, v162, v163\n"
    "v_mfma_f32_16x16x32_bf16 v[168:171], a[164:167], a[32:35], v[168:171]\n"
    "s_waitcnt lgkmcnt(6)\n"
    "ds_read_b128 a[68:71], v216 offset:9216\n"
    "v_mfma_f32_16x16x32_bf16 v[168:171], a[168:171], a[36:39], v[168:171]\n"
    "ds_read_b128 a[72:75], v216 offset:10240\n"
    "v_mfma_f32_16x16x32_bf16 v[168:171], a[172:175], a[40:43], v[168:171]\n"
    "s_waitcnt lgkmcnt(6)\n"
    "ds_read_b128 a[76:79], v216 offset:11264\n"
    "ds_write_addtid_b32 v160 offset:0\n"
    "v_mfma_f32_16x16x32_bf16 v[168:171], a[176:179], a[44:47], v[168:171]\n"
    "v_exp_f32_e32 v164, v164\n"
    "v_mfma_f32_16x16x32_bf16 v[168:171], a[180:183], a[48:51], v[168:171]\n"
    "s_waitcnt lgkmcnt(6)\n"
    "ds_read_b128 a[80:83], v216 offset:12288\n"
    "v_mfma_f32_16x16x32_bf16 v[168:171], a[184:187], a[52:55], v[168:171]\n"
    "ds_read_b128 a[84:87], v216 offset:13312\n"
    "v_mfma_f32_16x16x32_bf16 v[168:171], a[188:191], a[56:59], v[168:171]\n"
    "ds_read_b128 a[88:91], v216 offset:14336\n"
    "ds_write_addtid_b32 v161 offset:256\n"
    "v_mfma_f32_16x16x32_bf16 v[172:175], a[200:203], a[60:63], 0\n"
    "v_exp_f32_e32 v165, v165\n"
    "v_mfma_f32_16x16x32_bf16 v[172:175], a[204:207], a[32:35], v[172:175]\n"
    "v_exp_f32_e32 v166, v166\n"
    "v_mfma_f32_16x16x32_bf16 v[172:175], a[208:211], a[36:39], v[172:175]\n"
    "v_exp_f32_e32 v167, v167\n"
    "v_mfma_f32_16x16x32_bf16 v[172:175], a[212:215], a[40:43], v[172:175]\n"
    "v_cvt_pk_bf16_f32 v162, v164, v165\n"
    "v_cvt_pk_bf16_f32 v163, v166, v167\n"
    "v_mfma_f32_16x16x32_bf16 v[172:175], a[216:219], a[44:47], v[172:175]\n"
    "ds_write_addtid_b32 v162 offset:512\n"
    "v_exp_f32_e32 v168, v168\n"
    "v_mfma_f32_16x16x32_bf16 v[172:175], a[220:223], a[48:51], v[172:175]\n"
    "v_exp_f32_e32 v169, v169\n"
    "v_mfma_f32_16x16x32_bf16 v[172:175], a[224:227], a[52:55], v[172:175]\n"
    "v_exp_f32_e32 v170, v170\n"
    "v_mfma_f32_16x16x32_bf16 v[172:175], a[228:231], a[56:59], v[172:175]\n"
    "s_waitcnt lgkmcnt(9)\n"
    "v_exp_f32_e32 v171, v171\n"
    "v_mfma_f32_16x16x32_bf16 v[176:179], a[160:163], a[92:95], 0\n"
    "ds_write_addtid_b32 v163 offset:768\n"
    "v_cvt_pk_bf16_f32 v168, v168, v169\n"
    "v_mfma_f32_16x16x32_bf16 v[176:179], a[164:167], a[64:67], v[176:179]\n"
    "s_waitcnt lgkmcnt(8)\n"
    "ds_write_addtid_b32 v168 offset:4096\n"
    "v_cvt_pk_bf16_f32 v169, v170, v171\n"
    "v_mfma_f32_16x16x32_bf16 v[176:179], a[168:171], a[68:71], v[176:179]\n"
    "v_exp_f32_e32 v172, v172\n"
    "v_mfma_f32_16x16x32_bf16 v[176:179], a[172:175], a[72:75], v[176:179]\n"
    "s_waitcnt lgkmcnt(6)\n"
    "v_exp_f32_e32 v173, v173\n"
    "v_mfma_f32_16x16x32_bf16 v[176:179], a[176:179], a[76:79], v[176:179]\n"
    "v_exp_f32_e32 v174, v174\n"
    "v_mfma_f32_16x16x32_bf16 v[176:179], a[180:183], a[80:83], v[176:179]\n"
    "s_waitcnt vmcnt(0)\n"
    "s_barrier\n"
    "s_waitcnt lgkmcnt(4)\n"
    "v_mfma_f32_16x16x32_bf16 v[176:179], a[184:187], a[84:87], v[176:179]\n"
    "ds_write_addtid_b32 v169 offset:4352\n"
    "v_exp_f32_e32 v175, v175\n"
    "v_mfma_f32_16x16x32_bf16 v[176:179], a[188:191], a[88:91], v[176:179]\n"
    "ds_read_b128 a[124:127], v216 offset:23552\n"
    "v_cvt_pk_bf16_f32 v170, v172, v173\n"
    "v_mfma_f32_16x16x32_bf16 v[180:183], a[200:203], a[92:95], 0\n"
    "ds_read_b128 a[96:99], v216 offset:16384\n"
    "v_cvt_pk_bf16_f32 v171, v174, v175\n"
    "v_mfma_f32_16x16x32_bf16 v[180:183], a[204:207], a[64:67], v[180:183]\n"
    "ds_read_b128 a[100:103], v216 offset:17408\n"
    "v_exp_f32_e32 v176, v176\n"
    "v_mfma_f32_16x16x32_bf16 v[180:183], a[208:211], a[68:71], v[180:183]\n"
    "ds_read_b128 a[104:107], v216 offset:18432\n"
    "ds_write_addtid_b32 v170 offset:4608\n"
    "v_mfma_f32_16x16x32_bf16 v[180:183], a[212:215], a[72:75], v[180:183]\n"
    "ds_read_b128 a[108:111], v216 offset:19456\n"
    "v_exp_f32_e32 v177, v177\n"
    "v_mfma_f32_16x16x32_bf16 v[180:183], a[216:219], a[76:79], v[180:183]\n"
    "ds_read_b128 a[112:115], v216 offset:20480\n"
    "v_exp_f32_e32 v178, v178\n"
    "v_mfma_f32_16x16x32_bf16 v[180:183], a[220:223], a[80:83], v[180:183]\n"
    "ds_read_b128 a[116:119], v216 offset:21504\n"
    "ds_write_addtid_b32 v171 offset:4864\n"
    "v_mfma_f32_16x16x32_bf16 v[180:183], a[224:227], a[84:87], v[180:183]\n"
    "ds_read_b128 a[120:123], v216 offset:22528\n"
    "v_mfma_f32_16x16x32_bf16 v[180:183], a[228:231], a[88:91], v[180:183]\n"
    "s_waitcnt lgkmcnt(8)\n"
    "s_sub_u32 m0, s63, 16384\n"
    "v_exp_f32_e32 v179, v179\n"
    "v_mfma_f32_16x16x32_bf16 v[184:187], a[160:163], a[124:127], 0\n"
    "ds_read_b128 a[156:159], v216 offset:31744\n"
    "buffer_load_dwordx4 v248, s[8:11], s40 offen offset:0 sc1 lds\n"
    "v_cvt_pk_bf16_f32 v176, v176, v177\n"
    "v_mfma_f32_16x16x32_bf16 v[184:187], a[164:167], a[96:99], v[184:187]\n"
    "s_waitcnt lgkmcnt(7)\n"
    "ds_read_b128 a[128:131], v216 offset:24576\n"
    "v_cvt_pk_bf16_f32 v177, v178, v179\n"
    "v_mfma_f32_16x16x32_bf16 v[184:187], a[168:171], a[100:103], v[184:187]\n"
    "ds_read_b128 a[132:135], v216 offset:25600\n"
    "v_mfma_f32_16x16x32_bf16 v[184:187], a[172:175], a[104:107], v[184:187]\n"
    "s_waitcnt lgkmcnt(6)\n"
    "ds_read_b128 a[136:139], v216 offset:26624\n"
    "v_mfma_f32_16x16x32_bf16 v[184:187], a[176:179], a[108:111], v[184:187]\n"
    "ds_read_b128 a[140:143], v216 offset:27648\n"
    "buffer_load_dwordx4 v248, s[8:11], s40 offen offset:1024 sc1 lds\n"
    "v_mfma_f32_16x16x32_bf16 v[184:187], a[180:183], a[112:115], v[184:187]\n"
    "s_waitcnt lgkmcnt(5)\n"
    "ds_read_b128 a[144:147], v216 offset:28672\n"
    "v_mfma_f32_16x16x32_bf16 v[184:187], a[184:187], a[116:119], v[184:187]\n"
    "ds_read_b128 a[148:151], v216 offset:29696\n"
    "v_mfma_f32_16x16x32_bf16 v[184:187], a[188:191], a[120:123], v[184:187]\n"
    "ds_read_b128 a[152:155], v216 offset:30720\n"
    "v_mfma_f32_16x16x32_bf16 v[188:191], a[200:203], a[124:127], 0\n"
    "buffer_load_dwordx4 v248, s[8:11], s40 offen offset:2048 sc1 lds\n"
    "v_exp_f32_e32 v180, v180\n"
    "v_mfma_f32_16x16x32_bf16 v[188:191], a[204:207], a[96:99], v[188:191]\n"
    "ds_write_b32 v249, v176 offset:8192\n"
    "v_exp_f32_e32 v181, v181\n"
    "v_mfma_f32_16x16x32_bf16 v[188:191], a[208:211], a[100:103], v[188:191]\n"
    "ds_write_b32 v249, v177 offset:8448\n"
    "v_exp_f32_e32 v182, v182\n"
    "v_mfma_f32_16x16x32_bf16 v[188:191], a[212:215], a[104:107], v[188:191]\n"
    "v_exp_f32_e32 v183, v183\n"
    "v_mfma_f32_16x16x32_bf16 v[188:191], a[216:219], a[108:111], v[188:191]\n"
    "buffer_load_dwordx4 v248, s[8:11], s40 offen offset:3072 sc1 lds\n"
    "v_cvt_pk_bf16_f32 v178, v180, v181\n"
    "v_cvt_pk_bf16_f32 v179, v182, v183\n"
    "v_mfma_f32_16x16x32_bf16 v[188:191], a[220:223], a[112:115], v[188:191]\n"
    "s_add_u32 s40, s40, 0x8000\n"
    "ds_write_b32 v249, v178 offset:8704\n"
    "v_exp_f32_e32 v184, v184\n"
    "v_mfma_f32_16x16x32_bf16 v[188:191], a[224:227], a[116:119], v[188:191]\n"
    "s_sub_u32 m0, s63, 12288\n"
    "ds_write_b32 v249, v179 offset:8960\n"
    "v_exp_f32_e32 v185, v185\n"
    "v_mfma_f32_16x16x32_bf16 v[188:191], a[228:231], a[120:123], v[188:191]\n"
    "s_waitcnt lgkmcnt(10)\n"
    "v_exp_f32_e32 v186, v186\n"
    "v_mfma_f32_16x16x32_bf16 v[192:195], a[160:163], a[156:159], 0\n"
    "buffer_load_dwordx4 v248, s[8:11], s41 offen offset:0 sc1 lds\n"
    "v_exp_f32_e32 v187, v187\n"
    "v_mfma_f32_16x16x32_bf16 v[192:195], a[164:167], a[128:131], v[192:195]\n"
    "s_waitcnt lgkmcnt(8)\n"
    "ds_read_b128 a[160:163], v203 offset:20480\n"
    "v_cvt_pk_bf16_f32 v184, v184, v185\n"
    "v_mfma_f32_16x16x32_bf16 v[192:195], a[168:171], a[132:135], v[192:195]\n"
    "ds_read_b128 a[164:167], v200 offset:16384\n"
    "v_cvt_pk_bf16_f32 v185, v186, v187\n"
    "v_mfma_f32_16x16x32_bf16 v[192:195], a[172:175], a[136:139], v[192:195]\n"
    "s_waitcnt lgkmcnt(8)\n"
    "ds_read_b128 a[168:171], v201 offset:16384\n"
    "v_exp_f32_e32 v188, v188\n"
    "v_mfma_f32_16x16x32_bf16 v[192:195], a[176:179], a[140:143], v[192:195]\n"
    "ds_read_b128 a[172:175], v200 offset:20480\n"
    "buffer_load_dwordx4 v248, s[8:11], s41 offen offset:1024 sc1 lds\n"
    "v_exp_f32_e32 v189, v189\n"
    "v_mfma_f32_16x16x32_bf16 v[192:195], a[180:183], a[144:147], v[192:195]\n"
    "s_waitcnt lgkmcnt(8)\n"
    "ds_read_b128 a[176:179], v201 offset:20480\n"
    "v_exp_f32_e32 v190, v190\n"
    "v_mfma_f32_16x16x32_bf16 v[192:195], a[184:187], a[148:151], v[192:195]\n"
    "ds_read_b128 a[180:183], v202 offset:16384\n"
    "v_mfma_f32_16x16x32_bf16 v[192:195], a[188:191], a[152:155], v[192:195]\n"
    "ds_read_b128 a[184:187], v203 offset:16384\n"
    "v_mfma_f32_16x16x32_bf16 v[196:199], a[200:203], a[156:159], 0\n"
    "ds_read_b128 a[188:191], v202 offset:20480\n"
    "buffer_load_dwordx4 v248, s[8:11], s41 offen offset:2048 sc1 lds\n"
    "v_mfma_f32_16x16x32_bf16 v[196:199], a[204:207], a[128:131], v[196:199]\n"
    "ds_write_b32 v249, v184 offset:12288\n"
    "v_exp_f32_e32 v191, v191\n"
    "v_mfma_f32_16x16x32_bf16 v[196:199], a[208:211], a[132:135], v[196:199]\n"
    "ds_write_b32 v249, v185 offset:12544\n"
    "v_cvt_pk_bf16_f32 v186, v188, v189\n"
    "v_cvt_pk_bf16_f32 v187, v190, v191\n"
    "v_mfma_f32_16x16x32_bf16 v[196:199], a[212:215], a[136:139], v[196:199]\n"
    "ds_write_b32 v249, v186 offset:12800\n"
    "v_exp_f32_e32 v192, v192\n"
    "v_mfma_f32_16x16x32_bf16 v[196:199], a[216:219], a[140:143], v[196:199]\n"
    "buffer_load_dwordx4 v248, s[8:11], s41 offen offset:3072 sc1 lds\n"
    "v_exp_f32_e32 v193, v193\n"
    "v_mfma_f32_16x16x32_bf16 v[196:199], a[220:223], a[144:147], v[196:199]\n"
    "s_add_u32 s41, s41, 0x8000\n"
    "ds_write_b32 v249, v187 offset:13056\n"
    "v_exp_f32_e32 v194, v194\n"
    "v_mfma_f32_16x16x32_bf16 v[196:199], a[224:227], a[148:151], v[196:199]\n"
    "v_exp_f32_e32 v195, v195\n"
    "v_mfma_f32_16x16x32_bf16 v[196:199], a[228:231], a[152:155], v[196:199]\n"
    "v_cvt_pk_bf16_f32 v192, v192, v193\n"
    "ds_write_b32 v249, v192 offset:16384\n"
    "v_cvt_pk_bf16_f32 v193, v194, v195\n"
    "ds_write_b32 v249, v193 offset:16640\n"
    "s_cmp_lg_u32 s34, 0\n"
    "s_cselect_b32 s44, 2, 1\n"
    "s_waitcnt lgkmcnt(4)\n"
    "s_nop 0\n"
    "v_exp_f32_e32 v196, v196\n"
    "v_exp_f32_e32 v197, v197\n"
    "v_exp_f32_e32 v198, v198\n"
    "v_exp_f32_e32 v199, v199\n"
    "v_cvt_pk_bf16_f32 v194, v196, v197\n"
    "v_cvt_pk_bf16_f32 v195, v198, v199\n"
    "ds_write_b32 v249, v194 offset:16896\n"
    "ds_write_b32 v249, v195 offset:17152\n"
    "s_cbranch_scc1 .Lat_p2e\n"
    ".p2align 6\n"
    ".Lat_p:\n"
    "v_mfma_f32_16x16x32_bf16 v[200:203], a[160:163], a[28:31], 0\n"
    "v_mfma_f32_16x16x32_bf16 v[200:203], a[164:167], a[0:3], v[200:203]\n"
    "v_mfma_f32_16x16x32_bf16 v[208:211], a[164:167], a[32:35], 0\n"
    "v_mfma_f32_16x16x32_bf16 v[208:211], a[160:163], a[60:63], v[208:211]\n"
    "v_mfma_f32_16x16x32_bf16 v[216:219], a[160:163], a[92:95], 0\n"
    "v_mfma_f32_16x16x32_bf16 v[216:219], a[164:167], a[64:67], v[216:219]\n"
    "s_waitcnt lgkmcnt(0)\n"
    "v_mfma_f32_16x16x32_bf16 v[224:227], a[164:167], a[96:99], 0\n"
    "s_barrier\n"
    "v_mfma_f32_16x16x32_bf16 v[224:227], a[160:163], a[124:127], v[224:227]\n"
    "s_add_u32 m0, s43, 49152\n"
    "ds_read2st64_b32 v[164:165], v250 offset0:0 offset1:1\n"
    "v_mfma_f32_16x16x32_bf16 v[232:235], a[160:163], a[156:159], 0\n"
    "ds_read_b128 a[160:163], v243 offset:22528\n"
    "v_mfma_f32_16x16x32_bf16 v[232:235], a[164:167], a[128:131], v[232:235]\n"
    "ds_read_b128 a[164:167], v240 offset:18432\n"
    "v_mfma_f32_16x16x32_bf16 v[232:235], a[168:171], a[132:135], v[232:235]\n"
    "buffer_load_dwordx4 v248, s[8:11], s40 offen offset:0 sc1 lds\n"
    "v_mfma_f32_16x16x32_bf16 v[232:235], a[172:175], a[136:139], v[232:235]\n"
    "v_mfma_f32_16x16x32_bf16 v[224:227], a[172:175], a[104:107], v[224:227]\n"
    "v_mfma_f32_16x16x32_bf16 v[224:227], a[168:171], a[100:103], v[224:227]\n"
    "ds_read2st64_b32 v[166:167], v250 offset0:2 offset1:3\n"
    "v_mfma_f32_16x16x32_bf16 v[216:219], a[168:171], a[68:71], v[216:219]\n"
    "v_mfma_f32_16x16x32_bf16 v[216:219], a[172:175], a[72:75], v[216:219]\n"
    "buffer_load_dwordx4 v248, s[8:11], s40 offen offset:1024 sc1 lds\n"
    "v_mfma_f32_16x16x32_bf16 v[208:211], a[172:175], a[40:43], v[208:211]\n"
    "v_mfma_f32_16x16x32_bf16 v[208:211], a[168:171], a[36:39], v[208:211]\n"
    "ds_read2st64_b32 v[172:173], v250 offset0:16 offset1:17\n"
    "v_mfma_f32_16x16x32_bf16 v[200:203], a[168:171], a[4:7], v[200:203]\n"
    "ds_read_b128 a[168:171], v241 offset:18432\n"
    "v_mfma_f32_16x16x32_bf16 v[200:203], a[172:175], a[8:11], v[200:203]\n"
    "ds_read_b128 a[172:175], v240 offset:22528\n"
    "v_mfma_f32_16x16x32_bf16 v[200:203], a[176:179], a[12:15], v[200:203]\n"
    "buffer_load_dwordx4 v248, s[8:11], s40 offen offset:2048 sc1 lds\n"
    "v_mfma_f32_16x16x32_bf16 v[200:203], a[180:183], a[16:19], v[200:203]\n"
    "ds_read2st64_b32 v[174:175], v250 offset0:18 offset1:19\n"
    "v_mfma_f32_16x16x32_bf16 v[208:211], a[180:183], a[48:51], v[208:211]\n"
    "v_mfma_f32_16x16x32_bf16 v[208:211], a[176:179], a[44:47], v[208:211]\n"
    "v_mfma_f32_16x16x32_bf16 v[216:219], a[176:179], a[76:79], v[216:219]\n"
    "ds_read2st64_b32 v[180:181], v250 offset0:32 offset1:33\n"
    "v_mfma_f32_16x16x32_bf16 v[216:219], a[180:183], a[80:83], v[216:219]\n"
    "buffer_load_dwordx4 v248, s[8:11], s40 offen offset:3072 sc1 lds\n"
    "v_mfma_f32_16x16x32_bf16 v[224:227], a[180:183], a[112:115], v[224:227]\n"
    "s_add_u32 m0, s43, 53248\n"
    "v_mfma_f32_16x16x32_bf16 v[224:227], a[176:179], a[108:111], v[224:227]\n"
    "s_add_u32 s40, s40, 0x8000\n"
    "ds_read2st64_b32 v[182:183], v250 offset0:34 offset1:35\n"
    "v_mfma_f32_16x16x32_bf16 v[232:235], a[176:179], a[140:143], v[232:235]\n"
    "ds_read_b128 a[176:179], v241 offset:22528\n"
    "v_mfma_f32_16x16x32_bf16 v[232:235], a[180:183], a[144:147], v[232:235]\n"
    "ds_read_b128 a[180:183], v242 offset:18432\n"
    "v_mfma_f32_16x16x32_bf16 v[232:235], a[184:187], a[148:151], v[232:235]\n"
    "buffer_load_dwordx4 v248, s[8:11], s41 offen offset:0 sc1 lds\n"
    "v_mfma_f32_16x16x32_bf16 v[232:235], a[188:191], a[152:155], v[232:235]\n"
    "v_mfma_f32_16x16x32_bf16 v[224:227], a[188:191], a[120:123], v[224:227]\n"
    "ds_read2st64_b32 v[188:189], v250 offset0:48 offset1:49\n"
    "v_mfma_f32_16x16x32_bf16 v[224:227], a[184:187], a[116:119], v[224:227]\n"
    "v_mfma_f32_16x16x32_bf16 v[216:219], a[184:187], a[84:87], v[216:219]\n"
    "v_exp_f32_e32 v232, v232\n"
    "v_mfma_f32_16x16x32_bf16 v[216:219], a[188:191], a[88:91], v[216:219]\n"
    "buffer_load_dwordx4 v248, s[8:11], s41 offen offset:1024 sc1 lds\n"
    "v_mfma_f32_16x16x32_bf16 v[208:211], a[188:191], a[56:59], v[208:211]\n"
    "ds_read2st64_b32 v[190:191], v250 offset0:50 offset1:51\n"
    "v_mfma_f32_16x16x32_bf16 v[208:211], a[184:187], a[52:55], v[208:211]\n"
    "v_exp_f32_e32 v233, v233\n"
    "v_mfma_f32_16x16x32_bf16 v[200:203], a[184:187], a[20:23], v[200:203]\n"
    "ds_read_b128 a[184:187], v243 offset:18432\n"
    "v_mfma_f32_16x16x32_bf16 v[200:203], a[188:191], a[24:27], v[200:203]\n"
    "s_waitcnt lgkmcnt(8)\n"
    "ds_read_b128 a[188:191], v242 offset:22528\n"
    "v_mfma_f32_16x16x32_bf16 v[204:207], a[160:163], a[28:31], 0\n"
    "buffer_load_dwordx4 v248, s[8:11], s41 offen offset:2048 sc1 lds\n"
    "v_mfma_f32_16x16x32_bf16 v[204:207], a[164:167], a[0:3], v[204:207]\n"
    "ds_read2st64_b32 v[196:197], v250 offset0:64 offset1:65\n"
    "v_mfma_f32_16x16x32_bf16 v[212:215], a[164:167], a[32:35], 0\n"
    "v_exp_f32_e32 v234, v234\n"
    "v_mfma_f32_16x16x32_bf16 v[212:215], a[160:163], a[60:63], v[212:215]\n"
    "ds_read2st64_b32 v[198:199], v250 offset0:66 offset1:67\n"
    "v_mfma_f32_16x16x32_bf16 v[220:223], a[160:163], a[92:95], 0\n"
    "v_exp_f32_e32 v235, v235\n"
    "v_mfma_f32_16x16x32_bf16 v[220:223], a[164:167], a[64:67], v[220:223]\n"
    "buffer_load_dwordx4 v248, s[8:11], s41 offen offset:3072 sc1 lds\n"
    "v_mfma_f32_16x16x32_bf16 v[228:231], a[164:167], a[96:99], 0\n"
    "s_add_u32 s41, s41, 0x8000\n"
    "v_cvt_pk_bf16_f32 v232, v232, v233\n"
    "v_mfma_f32_16x16x32_bf16 v[228:231], a[160:163], a[124:127], v[228:231]\n"
    "v_exp_f32_e32 v224, v224\n"
    "v_mfma_f32_16x16x32_bf16 v[236:239], a[160:163], a[156:159], 0\n"
    "ds_read_b64_tr_b16 a[160:161], v244 offset:0\n"
    "v_mfma_f32_16x16x32_bf16 v[236:239], a[164:167], a[128:131], v[236:239]\n"
    "ds_read_b64_tr_b16 a[162:163], v244 offset:2048\n"
    "v_mfma_f32_16x16x32_bf16 v[236:239], a[168:171], a[132:135], v[236:239]\n"
    "ds_read_b64_tr_b16 a[164:165], v244 offset:8192\n"
    "v_mfma_f32_16x16x32_bf16 v[236:239], a[172:175], a[136:139], v[236:239]\n"
    "ds_read_b64_tr_b16 a[166:167], v244 offset:10240\n"
    "v_mfma_f32_16x16x32_bf16 v[228:231], a[172:175], a[104:107], v[228:231]\n"
    "v_exp_f32_e32 v225, v225\n"
    "v_mfma_f32_16x16x32_bf16 v[228:231], a[168:171], a[100:103], v[228:231]\n"
    "v_cvt_pk_bf16_f32 v233, v234, v235\n"
    "v_mfma_f32_16x16x32_bf16 v[220:223], a[168:171], a[68:71], v[220:223]\n"
    "v_exp_f32_e32 v226, v226\n"
    "v_mfma_f32_16x16x32_bf16 v[220:223], a[172:175], a[72:75], v[220:223]\n"
    "v_exp_f32_e32 v227, v227\n"
    "v_mfma_f32_16x16x32_bf16 v[212:215], a[172:175], a[40:43], v[212:215]\n"
    "v_cvt_pk_bf16_f32 v224, v224, v225\n"
    "v_mfma_f32_16x16x32_bf16 v[212:215], a[168:171], a[36:39], v[212:215]\n"
    "v_exp_f32_e32 v216, v216\n"
    "v_mfma_f32_16x16x32_bf16 v[204:207], a[168:171], a[4:7], v[204:207]\n"
    "ds_read_b64_tr_b16 a[168:169], v245 offset:0\n"
    "v_mfma_f32_16x16x32_bf16 v[204:207], a[172:175], a[8:11], v[204:207]\n"
    "s_waitcnt lgkmcnt(7)\n"
    "ds_read_b64_tr_b16 a[170:171], v245 offset:2048\n"
    "v_mfma_f32_16x16x32_bf16 v[204:207], a[176:179], a[12:15], v[204:207]\n"
    "ds_read_b64_tr_b16 a[172:173], v245 offset:8192\n"
    "v_mfma_f32_16x16x32_bf16 v[204:207], a[180:183], a[16:19], v[204:207]\n"
    "ds_read_b64_tr_b16 a[174:175], v245 offset:10240\n"
    "v_mfma_f32_16x16x32_bf16 v[212:215], a[180:183], a[48:51], v[212:215]\n"
    "v_exp_f32_e32 v217, v217\n"
    "v_mfma_f32_16x16x32_bf16 v[212:215], a[176:179], a[44:47], v[212:215]\n"
    "v_cvt_pk_bf16_f32 v225, v226, v227\n"
    "v_mfma_f32_16x16x32_bf16 v[220:223], a[176:179], a[76:79], v[220:223]\n"
    "v_exp_f32_e32 v218, v218\n"
    "v_mfma_f32_16x16x32_bf16 v[220:223], a[180:183], a[80:83], v[220:223]\n"
    "v_exp_f32_e32 v219, v219\n"
    "v_mfma_f32_16x16x32_bf16 v[228:231], a[180:183], a[112:115], v[228:231]\n"
    "v_cvt_pk_bf16_f32 v216, v216, v217\n"
    "v_mfma_f32_16x16x32_bf16 v[228:231], a[176:179], a[108:111], v[228:231]\n"
    "v_exp_f32_e32 v208, v208\n"
    "v_mfma_f32_16x16x32_bf16 v[236:239], a[176:179], a[140:143], v[236:239]\n"
    "ds_read_b64_tr_b16 a[176:177], v246 offset:0\n"
    "v_mfma_f32_16x16x32_bf16 v[236:239], a[180:183], a[144:147], v[236:239]\n"
    "ds_read_b64_tr_b16 a[178:179], v246 offset:2048\n"
    "v_mfma_f32_16x16x32_bf16 v[236:239], a[184:187], a[148:151], v[236:239]\n"
    "ds_read_b64_tr_b16 a[180:181], v246 offset:8192\n"
    "v_mfma_f32_16x16x32_bf16 v[236:239], a[188:191], a[152:155], v[236:239]\n"
    "ds_read_b64_tr_b16 a[182:183], v246 offset:10240\n"
    "v_mfma_f32_16x16x32_bf16 v[228:231], a[188:191], a[120:123], v[228:231]\n"
    "v_exp_f32_e32 v209, v209\n"
    "v_mfma_f32_16x16x32_bf16 v[228:231], a[184:187], a[116:119], v[228:231]\n"
    "v_cvt_pk_bf16_f32 v217, v218, v219\n"
    "v_mfma_f32_16x16x32_bf16 v[220:223], a[184:187], a[84:87], v[220:223]\n"
    "v_exp_f32_e32 v210, v210\n"
    "v_mfma_f32_16x16x32_bf16 v[220:223], a[188:191], a[88:91], v[220:223]\n"
    "v_exp_f32_e32 v211, v211\n"
    "v_mfma_f32_16x16x32_bf16 v[212:215], a[188:191], a[56:59], v[212:215]\n"
    "v_cvt_pk_bf16_f32 v208, v208, v209\n"
    "v_mfma_f32_16x16x32_bf16 v[212:215], a[184:187], a[52:55], v[212:215]\n"
    "v_exp_f32_e32 v200, v200\n"
    "v_mfma_f32_16x16x32_bf16 v[204:207], a[184:187], a[20:23], v[204:207]\n"
    "ds_read_b64_tr_b16 a[184:185], v247 offset:0\n"
    "v_mfma_f32_16x16x32_bf16 v[204:207], a[188:191], a[24:27], v[204:207]\n"
    "s_waitcnt lgkmcnt(5)\n"
    "ds_read_b64_tr_b16 a[186:187], v247 offset:2048\n"
    "v_mfma_f32_16x16x32_bf16 v[0:3], a[160:163], v[160:163], v[0:3]\n"
    "ds_read_b64_tr_b16 a[188:189], v247 offset:8192\n"
    "v_mfma_f32_16x16x32_bf16 v[0:3], a[164:167], v[164:167], v[0:3]\n"
    "ds_read_b64_tr_b16 a[190:191], v247 offset:10240\n"
    "v_mfma_f32_16x16x32_bf16 a[248:251], a[164:167], v[172:175], a[248:251]\n"
    "v_exp_f32_e32 v201, v201\n"
    "v_mfma_f32_16x16x32_bf16 a[248:251], a[160:163], v[168:171], a[248:251]\n"
    "v_cvt_pk_bf16_f32 v209, v210, v211\n"
    "v_mfma_f32_16x16x32_bf16 v[56:59], a[160:163], v[176:179], v[56:59]\n"
    "v_exp_f32_e32 v202, v202\n"
    "v_mfma_f32_16x16x32_bf16 v[56:59], a[164:167], v[180:183], v[56:59]\n"
    "v_exp_f32_e32 v203, v203\n"
    "v_mfma_f32_16x16x32_bf16 v[60:63], a[164:167], v[188:191], v[60:63]\n"
    "v_cvt_pk_bf16_f32 v200, v200, v201\n"
    "v_mfma_f32_16x16x32_bf16 v[60:63], a[160:163], v[184:187], v[60:63]\n"
    "v_cvt_pk_bf16_f32 v201, v202, v203\n"
    "v_mfma_f32_16x16x32_bf16 v[64:67], a[160:163], v[192:195], v[64:67]\n"
    "ds_read_b64_tr_b16 a[160:161], v244 offset:4096\n"
    "v_mfma_f32_16x16x32_bf16 v[64:67], a[164:167], v[196:199], v[64:67]\n"
    "ds_read_b64_tr_b16 a[162:163], v244 offset:6144\n"
    "v_mfma_f32_16x16x32_bf16 v[76:79], a[168:171], v[192:195], v[76:79]\n"
    "ds_read_b64_tr_b16 a[164:165], v244 offset:12288\n"
    "v_mfma_f32_16x16x32_bf16 v[76:79], a[172:175], v[196:199], v[76:79]\n"
    "ds_read_b64_tr_b16 a[166:167], v244 offset:14336\n"
    "v_mfma_f32_16x16x32_bf16 v[72:75], a[172:175], v[188:191], v[72:75]\n"
    "v_exp_f32_e32 v236, v236\n"
    "v_mfma_f32_16x16x32_bf16 v[72:75], a[168:171], v[184:187], v[72:75]\n"
    "v_exp_f32_e32 v237, v237\n"
    "v_mfma_f32_16x16x32_bf16 v[68:71], a[168:171], v[176:179], v[68:71]\n"
    "v_exp_f32_e32 v238, v238\n"
    "v_mfma_f32_16x16x32_bf16 v[68:71], a[172:175], v[180:183], v[68:71]\n"
    "v_exp_f32_e32 v239, v239\n"
    "v_mfma_f32_16x16x32_bf16 a[252:255], a[172:175], v[172:175], a[252:255]\n"
    "v_cvt_pk_bf16_f32 v234, v236, v237\n"
    "v_mfma_f32_16x16x32_bf16 a[252:255], a[168:171], v[168:171], a[252:255]\n"
    "v_exp_f32_e32 v228, v228\n"
    "v_mfma_f32_16x16x32_bf16 v[4:7], a[168:171], v[160:163], v[4:7]\n"
    "ds_read_b64_tr_b16 a[168:169], v245 offset:4096\n"
    "v_mfma_f32_16x16x32_bf16 v[4:7], a[172:175], v[164:167], v[4:7]\n"
    "s_waitcnt lgkmcnt(5)\n"
    "ds_read_b64_tr_b16 a[170:171], v245 offset:6144\n"
    "v_mfma_f32_16x16x32_bf16 v[8:11], a[176:179], v[160:163], v[8:11]\n"
    "ds_read_b64_tr_b16 a[172:173], v245 offset:12288\n"
    "v_mfma_f32_16x16x32_bf16 v[8:11], a[180:183], v[164:167], v[8:11]\n"
    "ds_read_b64_tr_b16 a[174:175], v245 offset:14336\n"
    "v_mfma_f32_16x16x32_bf16 v[32:35], a[180:183], v[172:175], v[32:35]\n"
    "v_exp_f32_e32 v229, v229\n"
    "v_mfma_f32_16x16x32_bf16 v[32:35], a[176:179], v[168:171], v[32:35]\n"
    "v_cvt_pk_bf16_f32 v235, v238, v239\n"
    "v_mfma_f32_16x16x32_bf16 v[80:83], a[176:179], v[176:179], v[80:83]\n"
    "v_exp_f32_e32 v230, v230\n"
    "v_mfma_f32_16x16x32_bf16 v[80:83], a[180:183], v[180:183], v[80:83]\n"
    "v_exp_f32_e32 v231, v231\n"
    "v_mfma_f32_16x16x32_bf16 v[84:87], a[180:183], v[188:191], v[84:87]\n"
    "v_cvt_pk_bf16_f32 v226, v228, v229\n"
    "v_mfma_f32_16x16x32_bf16 v[84:87], a[176:179], v[184:187], v[84:87]\n"
    "v_exp_f32_e32 v220, v220\n"
    "v_mfma_f32_16x16x32_bf16 v[88:91], a[176:179], v[192:195], v[88:91]\n"
    "ds_read_b64_tr_b16 a[176:177], v246 offset:4096\n"
    "v_mfma_f32_16x16x32_bf16 v[88:91], a[180:183], v[196:199], v[88:91]\n"
    "ds_read_b64_tr_b16 a[178:179], v246 offset:6144\n"
    "v_mfma_f32_16x16x32_bf16 v[100:103], a[184:187], v[192:195], v[100:103]\n"
    "ds_read_b64_tr_b16 a[180:181], v246 offset:12288\n"
    "v_mfma_f32_16x16x32_bf16 v[100:103], a[188:191], v[196:199], v[100:103]\n"
    "ds_read_b64_tr_b16 a[182:183], v246 offset:14336\n"
    "v_mfma_f32_16x16x32_bf16 v[96:99], a[188:191], v[188:191], v[96:99]\n"
    "v_exp_f32_e32 v221, v221\n"
    "v_mfma_f32_16x16x32_bf16 v[96:99], a[184:187], v[184:187], v[96:99]\n"
    "v_cvt_pk_bf16_f32 v227, v230, v231\n"
    "v_mfma_f32_16x16x32_bf16 v[92:95], a[184:187], v[176:179], v[92:95]\n"
    "v_exp_f32_e32 v222, v222\n"
    "v_mfma_f32_16x16x32_bf16 v[92:95], a[188:191], v[180:183], v[92:95]\n"
    "v_exp_f32_e32 v223, v223\n"
    "v_mfma_f32_16x16x32_bf16 v[36:39], a[188:191], v[172:175], v[36:39]\n"
    "v_cvt_pk_bf16_f32 v218, v220, v221\n"
    "v_mfma_f32_16x16x32_bf16 v[36:39], a[184:187], v[168:171], v[36:39]\n"
    "v_exp_f32_e32 v212, v212\n"
    "v_mfma_f32_16x16x32_bf16 v[12:15], a[184:187], v[160:163], v[12:15]\n"
    "s_mov_b32 m0, s49\n"
    "ds_read_b64_tr_b16 a[184:185], v247 offset:4096\n"
    "v_mfma_f32_16x16x32_bf16 v[12:15], a[188:191], v[164:167], v[12:15]\n"
    "s_waitcnt lgkmcnt(5)\n"
    "ds_read_b64_tr_b16 a[186:187], v247 offset:6144\n"
    "v_mfma_f32_16x16x32_bf16 v[16:19], a[160:163], v[160:163], v[16:19]\n"
    "ds_read_b64_tr_b16 a[188:189], v247 offset:12288\n"
    "v_mfma_f32_16x16x32_bf16 v[16:19], a[164:167], v[164:167], v[16:19]\n"
    "ds_read_b64_tr_b16 a[190:191], v247 offset:14336\n"
    "v_mfma_f32_16x16x32_bf16 v[40:43], a[164:167], v[172:175], v[40:43]\n"
    "v_exp_f32_e32 v213, v213\n"
    "v_mfma_f32_16x16x32_bf16 v[40:43], a[160:163], v[168:171], v[40:43]\n"
    "v_cvt_pk_bf16_f32 v219, v222, v223\n"
    "v_mfma_f32_16x16x32_bf16 v[104:107], a[160:163], v[176:179], v[104:107]\n"
    "v_exp_f32_e32 v214, v214\n"
    "v_mfma_f32_16x16x32_bf16 v[104:107], a[164:167], v[180:183], v[104:107]\n"
    "s_waitcnt vmcnt(8)\n"
    "v_mfma_f32_16x16x32_bf16 v[108:111], a[164:167], v[188:191], v[108:111]\n"
    "s_barrier\n"
    "v_mfma_f32_16x16x32_bf16 v[108:111], a[160:163], v[184:187], v[108:111]\n"
    "v_exp_f32_e32 v215, v215\n"
    "v_mfma_f32_16x16x32_bf16 v[112:115], a[160:163], v[192:195], v[112:115]\n"
    "ds_read_b128 a[160:163], v243 offset:36864\n"
    "v_mfma_f32_16x16x32_bf16 v[112:115], a[164:167], v[196:199], v[112:115]\n"
    "ds_read_b128 a[164:167], v240 offset:32768\n"
    "v_mfma_f32_16x16x32_bf16 v[124:127], a[168:171], v[192:195], v[124:127]\n"
    "ds_write_addtid_b32 v232 offset:16384\n"
    "v_cvt_pk_bf16_f32 v210, v212, v213\n"
    "v_mfma_f32_16x16x32_bf16 v[124:127], a[172:175], v[196:199], v[124:127]\n"
    "v_exp_f32_e32 v204, v204\n"
    "v_mfma_f32_16x16x32_bf16 v[120:123], a[172:175], v[188:191], v[120:123]\n"
    "v_exp_f32_e32 v205, v205\n"
    "v_mfma_f32_16x16x32_bf16 v[120:123], a[168:171], v[184:187], v[120:123]\n"
    "ds_write_addtid_b32 v233 offset:16640\n"
    "v_cvt_pk_bf16_f32 v211, v214, v215\n"
    "v_mfma_f32_16x16x32_bf16 v[116:119], a[168:171], v[176:179], v[116:119]\n"
    "v_exp_f32_e32 v206, v206\n"
    "v_mfma_f32_16x16x32_bf16 v[116:119], a[172:175], v[180:183], v[116:119]\n"
    "v_exp_f32_e32 v207, v207\n"
    "v_mfma_f32_16x16x32_bf16 v[44:47], a[172:175], v[172:175], v[44:47]\n"
    "ds_write_addtid_b32 v224 offset:12288\n"
    "v_cvt_pk_bf16_f32 v202, v204, v205\n"
    "v_mfma_f32_16x16x32_bf16 v[44:47], a[168:171], v[168:171], v[44:47]\n"
    "ds_write_addtid_b32 v225 offset:12544\n"
    "v_cvt_pk_bf16_f32 v203, v206, v207\n"
    "v_mfma_f32_16x16x32_bf16 v[20:23], a[168:171], v[160:163], v[20:23]\n"
    "ds_read_b128 a[168:171], v241 offset:32768\n"
    "v_mfma_f32_16x16x32_bf16 v[20:23], a[172:175], v[164:167], v[20:23]\n"
    "s_waitcnt lgkmcnt(7)\n"
    "ds_read_b128 a[172:175], v240 offset:36864\n"
    "v_mfma_f32_16x16x32_bf16 v[24:27], a[176:179], v[160:163], v[24:27]\n"
    "ds_write_addtid_b32 v216 offset:8192\n"
    "v_mfma_f32_16x16x32_bf16 v[24:27], a[180:183], v[164:167], v[24:27]\n"
    "ds_write_addtid_b32 v217 offset:8448\n"
    "v_mfma_f32_16x16x32_bf16 v[48:51], a[180:183], v[172:175], v[48:51]\n"
    "ds_write_addtid_b32 v208 offset:4096\n"
    "v_mfma_f32_16x16x32_bf16 v[48:51], a[176:179], v[168:171], v[48:51]\n"
    "ds_write_addtid_b32 v209 offset:4352\n"
    "v_mfma_f32_16x16x32_bf16 v[128:131], a[176:179], v[176:179], v[128:131]\n"
    "ds_write_addtid_b32 v200 offset:0\n"
    "v_mfma_f32_16x16x32_bf16 v[128:131], a[180:183], v[180:183], v[128:131]\n"
    "ds_write_addtid_b32 v201 offset:256\n"
    "v_mfma_f32_16x16x32_bf16 v[132:135], a[180:183], v[188:191], v[132:135]\n"
    "ds_write_addtid_b32 v234 offset:16896\n"
    "v_mfma_f32_16x16x32_bf16 v[132:135], a[176:179], v[184:187], v[132:135]\n"
    "ds_write_addtid_b32 v235 offset:17152\n"
    "v_mfma_f32_16x16x32_bf16 v[136:139], a[176:179], v[192:195], v[136:139]\n"
    "ds_read_b128 a[176:179], v241 offset:36864\n"
    "v_mfma_f32_16x16x32_bf16 v[136:139], a[180:183], v[196:199], v[136:139]\n"
    "ds_read_b128 a[180:183], v242 offset:32768\n"
    "v_mfma_f32_16x16x32_bf16 v[148:151], a[184:187], v[192:195], v[148:151]\n"
    "ds_write_addtid_b32 v226 offset:12800\n"
    "v_mfma_f32_16x16x32_bf16 v[148:151], a[188:191], v[196:199], v[148:151]\n"
    "ds_write_addtid_b32 v227 offset:13056\n"
    "v_mfma_f32_16x16x32_bf16 v[144:147], a[188:191], v[188:191], v[144:147]\n"
    "ds_write_addtid_b32 v218 offset:8704\n"
    "v_mfma_f32_16x16x32_bf16 v[144:147], a[184:187], v[184:187], v[144:147]\n"
    "ds_write_addtid_b32 v219 offset:8960\n"
    "v_mfma_f32_16x16x32_bf16 v[140:143], a[184:187], v[176:179], v[140:143]\n"
    "ds_write_addtid_b32 v210 offset:4608\n"
    "v_mfma_f32_16x16x32_bf16 v[140:143], a[188:191], v[180:183], v[140:143]\n"
    "ds_write_addtid_b32 v211 offset:4864\n"
    "v_mfma_f32_16x16x32_bf16 v[52:55], a[188:191], v[172:175], v[52:55]\n"
    "ds_write_addtid_b32 v202 offset:512\n"
    "v_mfma_f32_16x16x32_bf16 v[52:55], a[184:187], v[168:171], v[52:55]\n"
    "ds_write_addtid_b32 v203 offset:768\n"
    "v_mfma_f32_16x16x32_bf16 v[28:31], a[184:187], v[160:163], v[28:31]\n"
    "ds_read_b128 a[184:187], v243 offset:32768\n"
    "v_mfma_f32_16x16x32_bf16 v[28:31], a[188:191], v[164:167], v[28:31]\n"
    "s_waitcnt lgkmcnt(15)\n"
    "ds_read_b128 a[188:191], v242 offset:36864\n"
    "v_mfma_f32_16x16x32_bf16 v[160:163], a[160:163], a[28:31], 0\n"
    "v_mfma_f32_16x16x32_bf16 v[160:163], a[164:167], a[0:3], v[160:163]\n"
    "v_mfma_f32_16x16x32_bf16 v[168:171], a[164:167], a[32:35], 0\n"
    "v_mfma_f32_16x16x32_bf16 v[168:171], a[160:163], a[60:63], v[168:171]\n"
    "v_mfma_f32_16x16x32_bf16 v[176:179], a[160:163], a[92:95], 0\n"
    "v_mfma_f32_16x16x32_bf16 v[176:179], a[164:167], a[64:67], v[176:179]\n"
    "s_waitcnt lgkmcnt(0)\n"
    "v_mfma_f32_16x16x32_bf16 v[184:187], a[164:167], a[96:99], 0\n"
    "s_barrier\n"
    "v_mfma_f32_16x16x32_bf16 v[184:187], a[160:163], a[124:127], v[184:187]\n"
    "s_add_u32 m0, s43, 0\n"
    "ds_read2st64_b32 v[204:205], v250 offset0:0 offset1:1\n"
    "v_mfma_f32_16x16x32_bf16 v[192:195], a[160:163], a[156:159], 0\n"
    "ds_read_b128 a[160:163], v243 offset:38912\n"
    "v_mfma_f32_16x16x32_bf16 v[192:195], a[164:167], a[128:131], v[192:195]\n"
    "ds_read_b128 a[164:167], v240 offset:34816\n"
    "v_mfma_f32_16x16x32_bf16 v[192:195], a[168:171], a[132:135], v[192:195]\n"
    "buffer_load_dwordx4 v248, s[8:11], s40 offen offset:0 sc1 lds\n"
    "v_mfma_f32_16x16x32_bf16 v[192:195], a[172:175], a[136:139], v[192:195]\n"
    "v_mfma_f32_16x16x32_bf16 v[184:187], a[172:175], a[104:107], v[184:187]\n"
    "v_mfma_f32_16x16x32_bf16 v[184:187], a[168:171], a[100:103], v[184:187]\n"
    "ds_read2st64_b32 v[206:207], v250 offset0:2 offset1:3\n"
    "v_mfma_f32_16x16x32_bf16 v[176:179], a[168:171], a[68:71], v[176:179]\n"
    "v_mfma_f32_16x16x32_bf16 v[176:179], a[172:175], a[72:75], v[176:179]\n"
    "buffer_load_dwordx4 v248, s[8:11], s40 offen offset:1024 sc1 lds\n"
    "v_mfma_f32_16x16x32_bf16 v[168:171], a[172:175], a[40:43], v[168:171]\n"
    "v_mfma_f32_16x16x32_bf16 v[168:171], a[168:171], a[36:39], v[168:171]\n"
    "ds_read2st64_b32 v[212:213], v250 offset0:16 offset1:17\n"
    "v_mfma_f32_16x16x32_bf16 v[160:163], a[168:171], a[4:7], v[160:163]\n"
    "ds_read_b128 a[168:171], v241 offset:34816\n"
    "v_mfma_f32_16x16x32_bf16 v[160:163], a[172:175], a[8:11], v[160:163]\n"
    "ds_read_b128 a[172:175], v240 offset:38912\n"
    "v_mfma_f32_16x16x32_bf16 v[160:163], a[176:179], a[12:15], v[160:163]\n"
    "buffer_load_dwordx4 v248, s[8:11], s40 offen offset:2048 sc1 lds\n"
    "v_mfma_f32_16x16x32_bf16 v[160:163], a[180:183], a[16:19], v[160:163]\n"
    "ds_read2st64_b32 v[214:215], v250 offset0:18 offset1:19\n"
    "v_mfma_f32_16x16x32_bf16 v[168:171], a[180:183], a[48:51], v[168:171]\n"
    "v_mfma_f32_16x16x32_bf16 v[168:171], a[176:179], a[44:47], v[168:171]\n"
    "v_mfma_f32_16x16x32_bf16 v[176:179], a[176:179], a[76:79], v[176:179]\n"
    "ds_read2st64_b32 v[220:221], v250 offset0:32 offset1:33\n"
    "v_mfma_f32_16x16x32_bf16 v[176:179], a[180:183], a[80:83], v[176:179]\n"
    "buffer_load_dwordx4 v248, s[8:11], s40 offen offset:3072 sc1 lds\n"
    "v_mfma_f32_16x16x32_bf16 v[184:187], a[180:183], a[112:115], v[184:187]\n"
    "s_add_u32 m0, s43, 4096\n"
    "v_mfma_f32_16x16x32_bf16 v[184:187], a[176:179], a[108:111], v[184:187]\n"
    "s_add_u32 s40, s40, 0x8000\n"
    "ds_read2st64_b32 v[222:223], v250 offset0:34 offset1:35\n"
    "v_mfma_f32_16x16x32_bf16 v[192:195], a[176:179], a[140:143], v[192:195]\n"
    "ds_read_b128 a[176:179], v241 offset:38912\n"
    "v_mfma_f32_16x16x32_bf16 v[192:195], a[180:183], a[144:147], v[192:195]\n"
    "ds_read_b128 a[180:183], v242 offset:34816\n"
    "v_mfma_f32_16x16x32_bf16 v[192:195], a[184:187], a[148:151], v[192:195]\n"
    "buffer_load_dwordx4 v248, s[8:11], s41 offen offset:0 sc1 lds\n"
    "v_mfma_f32_16x16x32_bf16 v[192:195], a[188:191], a[152:155], v[192:195]\n"
    "v_mfma_f32_16x16x32_bf16 v[184:187], a[188:191], a[120:123], v[184:187]\n"
    "ds_read2st64_b32 v[228:229], v250 offset0:48 offset1:49\n"
    "v_mfma_f32_16x16x32_bf16 v[184:187], a[184:187], a[116:119], v[184:187]\n"
    "v_mfma_f32_16x16x32_bf16 v[176:179], a[184:187], a[84:87], v[176:179]\n"
    "v_exp_f32_e32 v192, v192\n"
    "v_mfma_f32_16x16x32_bf16 v[176:179], a[188:191], a[88:91], v[176:179]\n"
    "buffer_load_dwordx4 v248, s[8:11], s41 offen offset:1024 sc1 lds\n"
    "v_mfma_f32_16x16x32_bf16 v[168:171], a[188:191], a[56:59], v[168:171]\n"
    "ds_read2st64_b32 v[230:231], v250 offset0:50 offset1:51\n"
    "v_mfma_f32_16x16x32_bf16 v[168:171], a[184:187], a[52:55], v[168:171]\n"
    "v_exp_f32_e32 v193, v193\n"
    "v_mfma_f32_16x16x32_bf16 v[160:163], a[184:187], a[20:23], v[160:163]\n"
    "ds_read_b128 a[184:187], v243 offset:34816\n"
    "v_mfma_f32_16x16x32_bf16 v[160:163], a[188:191], a[24:27], v[160:163]\n"
    "s_waitcnt lgkmcnt(8)\n"
    "ds_read_b128 a[188:191], v242 offset:38912\n"
    "v_mfma_f32_16x16x32_bf16 v[164:167], a[160:163], a[28:31], 0\n"
    "buffer_load_dwordx4 v248, s[8:11], s41 offen offset:2048 sc1 lds\n"
    "v_mfma_f32_16x16x32_bf16 v[164:167], a[164:167], a[0:3], v[164:167]\n"
    "ds_read2st64_b32 v[236:237], v250 offset0:64 offset1:65\n"
    "v_mfma_f32_16x16x32_bf16 v[172:175], a[164:167], a[32:35], 0\n"
    "v_exp_f32_e32 v194, v194\n"
    "v_mfma_f32_16x16x32_bf16 v[172:175], a[160:163], a[60:63], v[172:175]\n"
    "ds_read2st64_b32 v[238:239], v250 offset0:66 offset1:67\n"
    "v_mfma_f32_16x16x32_bf16 v[180:183], a[160:163], a[92:95], 0\n"
    "v_exp_f32_e32 v195, v195\n"
    "v_mfma_f32_16x16x32_bf16 v[180:183], a[164:167], a[64:67], v[180:183]\n"
    "buffer_load_dwordx4 v248, s[8:11], s41 offen offset:3072 sc1 lds\n"
    "v_mfma_f32_16x16x32_bf16 v[188:191], a[164:167], a[96:99], 0\n"
    "s_add_u32 s41, s41, 0x8000\n"
    "v_cvt_pk_bf16_f32 v192, v192, v193\n"
    "v_mfma_f32_16x16x32_bf16 v[188:191], a[160:163], a[124:127], v[188:191]\n"
    "v_exp_f32_e32 v184, v184\n"
    "v_mfma_f32_16x16x32_bf16 v[196:199], a[160:163], a[156:159], 0\n"
    "ds_read_b64_tr_b16 a[160:161], v244 offset:16384\n"
    "v_mfma_f32_16x16x32_bf16 v[196:199], a[164:167], a[128:131], v[196:199]\n"
    "ds_read_b64_tr_b16 a[162:163], v244 offset:18432\n"
    "v_mfma_f32_16x16x32_bf16 v[196:199], a[168:171], a[132:135], v[196:199]\n"
    "ds_read_b64_tr_b16 a[164:165], v244 offset:24576\n"
    "v_mfma_f32_16x16x32_bf16 v[196:199], a[172:175], a[136:139], v[196:199]\n"
    "ds_read_b64_tr_b16 a[166:167], v244 offset:26624\n"
    "v_mfma_f32_16x16x32_bf16 v[188:191], a[172:175], a[104:107], v[188:191]\n"
    "v_exp_f32_e32 v185, v185\n"
    "v_mfma_f32_16x16x32_bf16 v[188:191], a[168:171], a[100:103], v[188:191]\n"
    "v_cvt_pk_bf16_f32 v193, v194, v195\n"
    "v_mfma_f32_16x16x32_bf16 v[180:183], a[168:171], a[68:71], v[180:183]\n"
    "v_exp_f32_e32 v186, v186\n"
    "v_mfma_f32_16x16x32_bf16 v[180:183], a[172:175], a[72:75], v[180:183]\n"
    "v_exp_f32_e32 v187, v187\n"
    "v_mfma_f32_16x16x32_bf16 v[172:175], a[172:175], a[40:43], v[172:175]\n"
    "v_cvt_pk_bf16_f32 v184, v184, v185\n"
    "v_mfma_f32_16x16x32_bf16 v[172:175], a[168:171], a[36:39], v[172:175]\n"
    "v_exp_f32_e32 v176, v176\n"
    "v_mfma_f32_16x16x32_bf16 v[164:167], a[168:171], a[4:7], v[164:167]\n"
    "ds_read_b64_tr_b16 a[168:169], v245 offset:16384\n"
    "v_mfma_f32_16x16x32_bf16 v[164:167], a[172:175], a[8:11], v[164:167]\n"
    "s_waitcnt lgkmcnt(7)\n"
    "ds_read_b64_tr_b16 a[170:171], v245 offset:18432\n"
    "v_mfma_f32_16x16x32_bf16 v[164:167], a[176:179], a[12:15], v[164:167]\n"
    "ds_read_b64_tr_b16 a[172:173], v245 offset:24576\n"
    "v_mfma_f32_16x16x32_bf16 v[164:167], a[180:183], a[16:19], v[164:167]\n"
    "ds_read_b64_tr_b16 a[174:175], v245 offset:26624\n"
    "v_mfma_f32_16x16x32_bf16 v[172:175], a[180:183], a[48:51], v[172:175]\n"
    "v_exp_f32_e32 v177, v177\n"
    "v_mfma_f32_16x16x32_bf16 v[172:175], a[176:179], a[44:47], v[172:175]\n"
    "v_cvt_pk_bf16_f32 v185, v186, v187\n"
    "v_mfma_f32_16x16x32_bf16 v[180:183], a[176:179], a[76:79], v[180:183]\n"
    "v_exp_f32_e32 v178, v178\n"
    "v_mfma_f32_16x16x32_bf16 v[180:183], a[180:183], a[80:83], v[180:183]\n"
    "v_exp_f32_e32 v179, v179\n"
    "v_mfma_f32_16x16x32_bf16 v[188:191], a[180:183], a[112:115], v[188:191]\n"
    "v_cvt_pk_bf16_f32 v176, v176, v177\n"
    "v_mfma_f32_16x16x32_bf16 v[188:191], a[176:179], a[108:111], v[188:191]\n"
    "v_exp_f32_e32 v168, v168\n"
    "v_mfma_f32_16x16x32_bf16 v[196:199], a[176:179], a[140:143], v[196:199]\n"
    "ds_read_b64_tr_b16 a[176:177], v246 offset:16384\n"
    "v_mfma_f32_16x16x32_bf16 v[196:199], a[180:183], a[144:147], v[196:199]\n"
    "ds_read_b64_tr_b16 a[178:179], v246 offset:18432\n"
    "v_mfma_f32_16x16x32_bf16 v[196:199], a[184:187], a[148:151], v[196:199]\n"
    "ds_read_b64_tr_b16 a[180:181], v246 offset:24576\n"
    "v_mfma_f32_16x16x32_bf16 v[196:199], a[188:191], a[152:155], v[196:199]\n"
    "ds_read_b64_tr_b16 a[182:183], v246 offset:26624\n"
    "v_mfma_f32_16x16x32_bf16 v[188:191], a[188:191], a[120:123], v[188:191]\n"
    "v_exp_f32_e32 v169, v169\n"
    "v_mfma_f32_16x16x32_bf16 v[188:191], a[184:187], a[116:119], v[188:191]\n"
    "v_cvt_pk_bf16_f32 v177, v178, v179\n"
    "v_mfma_f32_16x16x32_bf16 v[180:183], a[184:187], a[84:87], v[180:183]\n"
    "v_exp_f32_e32 v170, v170\n"
    "v_mfma_f32_16x16x32_bf16 v[180:183], a[188:191], a[88:91], v[180:183]\n"
    "v_exp_f32_e32 v171, v171\n"
    "v_mfma_f32_16x16x32_bf16 v[172:175], a[188:191], a[56:59], v[172:175]\n"
    "v_cvt_pk_bf16_f32 v168, v168, v169\n"
    "v_mfma_f32_16x16x32_bf16 v[172:175], a[184:187], a[52:55], v[172:175]\n"
    "v_exp_f32_e32 v160, v160\n"
    "v_mfma_f32_16x16x32_bf16 v[164:167], a[184:187], a[20:23], v[164:167]\n"
    "ds_read_b64_tr_b16 a[184:185], v247 offset:16384\n"
    "v_mfma_f32_16x16x32_bf16 v[164:167], a[188:191], a[24:27], v[164:167]\n"
    "s_waitcnt lgkmcnt(5)\n"
    "ds_read_b64_tr_b16 a[186:187], v247 offset:18432\n"
    "v_mfma_f32_16x16x32_bf16 v[0:3], a[160:163], v[200:203], v[0:3]\n"
    "ds_read_b64_tr_b16 a[188:189], v247 offset:24576\n"
    "v_mfma_f32_16x16x32_bf16 v[0:3], a[164:167], v[204:207], v[0:3]\n"
    "ds_read_b64_tr_b16 a[190:191], v247 offset:26624\n"
    "v_mfma_f32_16x16x32_bf16 a[248:251], a[164:167], v[212:215], a[248:251]\n"
    "v_exp_f32_e32 v161, v161\n"
    "v_mfma_f32_16x16x32_bf16 a[248:251], a[160:163], v[208:211], a[248:251]\n"
    "v_cvt_pk_bf16_f32 v169, v170, v171\n"
    "v_mfma_f32_16x16x32_bf16 v[56:59], a[160:163], v[216:219], v[56:59]\n"
    "v_exp_f32_e32 v162, v162\n"
    "v_mfma_f32_16x16x32_bf16 v[56:59], a[164:167], v[220:223], v[56:59]\n"
    "v_exp_f32_e32 v163, v163\n"
    "v_mfma_f32_16x16x32_bf16 v[60:63], a[164:167], v[228:231], v[60:63]\n"
    "v_cvt_pk_bf16_f32 v160, v160, v161\n"
    "v_mfma_f32_16x16x32_bf16 v[60:63], a[160:163], v[224:227], v[60:63]\n"
    "v_cvt_pk_bf16_f32 v161, v162, v163\n"
    "v_mfma_f32_16x16x32_bf16 v[64:67], a[160:163], v[232:235], v[64:67]\n"
    "ds_read_b64_tr_b16 a[160:161], v244 offset:20480\n"
    "v_mfma_f32_16x16x32_bf16 v[64:67], a[164:167], v[236:239], v[64:67]\n"
    "ds_read_b64_tr_b16 a[162:163], v244 offset:22528\n"
    "v_mfma_f32_16x16x32_bf16 v[76:79], a[168:171], v[232:235], v[76:79]\n"
    "ds_read_b64_tr_b16 a[164:165], v244 offset:28672\n"
    "v_mfma_f32_16x16x32_bf16 v[76:79], a[172:175], v[236:239], v[76:79]\n"
    "ds_read_b64_tr_b16 a[166:167], v244 offset:30720\n"
    "v_mfma_f32_16x16x32_bf16 v[72:75], a[172:175], v[228:231], v[72:75]\n"
    "v_exp_f32_e32 v196, v196\n"
    "v_mfma_f32_16x16x32_bf16 v[72:75], a[168:171], v[224:227], v[72:75]\n"
    "v_exp_f32_e32 v197, v197\n"
    "v_mfma_f32_16x16x32_bf16 v[68:71], a[168:171], v[216:219], v[68:71]\n"
    "v_exp_f32_e32 v198, v198\n"
    "v_mfma_f32_16x16x32_bf16 v[68:71], a[172:175], v[220:223], v[68:71]\n"
    "v_exp_f32_e32 v199, v199\n"
    "v_mfma_f32_16x16x32_bf16 a[252:255], a[172:175], v[212:215], a[252:255]\n"
    "v_cvt_pk_bf16_f32 v194, v196, v197\n"
    "v_mfma_f32_16x16x32_bf16 a[252:255], a[168:171], v[208:211], a[252:255]\n"
    "v_exp_f32_e32 v188, v188\n"
    "v_mfma_f32_16x16x32_bf16 v[4:7], a[168:171], v[200:203], v[4:7]\n"
    "ds_read_b64_tr_b16 a[168:169], v245 offset:20480\n"
    "v_mfma_f32_16x16x32_bf16 v[4:7], a[172:175], v[204:207], v[4:7]\n"
    "s_waitcnt lgkmcnt(5)\n"
    "ds_read_b64_tr_b16 a[170:171], v245 offset:22528\n"
    "v_mfma_f32_16x16x32_bf16 v[8:11], a[176:179], v[200:203], v[8:11]\n"
    "ds_read_b64_tr_b16 a[172:173], v245 offset:28672\n"
    "v_mfma_f32_16x16x32_bf16 v[8:11], a[180:183], v[204:207], v[8:11]\n"
    "ds_read_b64_tr_b16 a[174:175], v245 offset:30720\n"
    "v_mfma_f32_16x16x32_bf16 v[32:35], a[180:183], v[212:215], v[32:35]\n"
    "v_exp_f32_e32 v189, v189\n"
    "v_mfma_f32_16x16x32_bf16 v[32:35], a[176:179], v[208:211], v[32:35]\n"
    "v_cvt_pk_bf16_f32 v195, v198, v199\n"
    "v_mfma_f32_16x16x32_bf16 v[80:83], a[176:179], v[216:219], v[80:83]\n"
    "v_exp_f32_e32 v190, v190\n"
    "v_mfma_f32_16x16x32_bf16 v[80:83], a[180:183], v[220:223], v[80:83]\n"
    "v_exp_f32_e32 v191, v191\n"
    "v_mfma_f32_16x16x32_bf16 v[84:87], a[180:183], v[228:231], v[84:87]\n"
    "v_cvt_pk_bf16_f32 v186, v188, v189\n"
    "v_mfma_f32_16x16x32_bf16 v[84:87], a[176:179], v[224:227], v[84:87]\n"
    "v_exp_f32_e32 v180, v180\n"
    "v_mfma_f32_16x16x32_bf16 v[88:91], a[176:179], v[232:235], v[88:91]\n"
    "ds_read_b64_tr_b16 a[176:177], v246 offset:20480\n"
    "v_mfma_f32_16x16x32_bf16 v[88:91], a[180:183], v[236:239], v[88:91]\n"
    "ds_read_b64_tr_b16 a[178:179], v246 offset:22528\n"
    "v_mfma_f32_16x16x32_bf16 v[100:103], a[184:187], v[232:235], v[100:103]\n"
    "ds_read_b64_tr_b16 a[180:181], v246 offset:28672\n"
    "v_mfma_f32_16x16x32_bf16 v[100:103], a[188:191], v[236:239], v[100:103]\n"
    "ds_read_b64_tr_b16 a[182:183], v246 offset:30720\n"
    "v_mfma_f32_16x16x32_bf16 v[96:99], a[188:191], v[228:231], v[96:99]\n"
    "v_exp_f32_e32 v181, v181\n"
    "v_mfma_f32_16x16x32_bf16 v[96:99], a[184:187], v[224:227], v[96:99]\n"
    "v_cvt_pk_bf16_f32 v187, v190, v191\n"
    "v_mfma_f32_16x16x32_bf16 v[92:95], a[184:187], v[216:219], v[92:95]\n"
    "v_exp_f32_e32 v182, v182\n"
    "v_mfma_f32_16x16x32_bf16 v[92:95], a[188:191], v[220:223], v[92:95]\n"
    "v_exp_f32_e32 v183, v183\n"
    "v_mfma_f32_16x16x32_bf16 v[36:39], a[188:191], v[212:215], v[36:39]\n"
    "v_cvt_pk_bf16_f32 v178, v180, v181\n"
    "v_mfma_f32_16x16x32_bf16 v[36:39], a[184:187], v[208:211], v[36:39]\n"
    "v_exp_f32_e32 v172, v172\n"
    "v_mfma_f32_16x16x32_bf16 v[12:15], a[184:187], v[200:203], v[12:15]\n"
    "s_mov_b32 m0, s49\n"
    "ds_read_b64_tr_b16 a[184:185], v247 offset:20480\n"
    "v_mfma_f32_16x16x32_bf16 v[12:15], a[188:191], v[204:207], v[12:15]\n"
    "s_waitcnt lgkmcnt(5)\n"
    "ds_read_b64_tr_b16 a[186:187], v247 offset:22528\n"
    "v_mfma_f32_16x16x32_bf16 v[16:19], a[160:163], v[200:203], v[16:19]\n"
    "ds_read_b64_tr_b16 a[188:189], v247 offset:28672\n"
    "v_mfma_f32_16x16x32_bf16 v[16:19], a[164:167], v[204:207], v[16:19]\n"
    "ds_read_b64_tr_b16 a[190:191], v247 offset:30720\n"
    "v_mfma_f32_16x16x32_bf16 v[40:43], a[164:167], v[212:215], v[40:43]\n"
    "v_exp_f32_e32 v173, v173\n"
    "v_mfma_f32_16x16x32_bf16 v[40:43], a[160:163], v[208:211], v[40:43]\n"
    "v_cvt_pk_bf16_f32 v179, v182, v183\n"
    "v_mfma_f32_16x16x32_bf16 v[104:107], a[160:163], v[216:219], v[104:107]\n"
    "v_exp_f32_e32 v174, v174\n"
    "v_mfma_f32_16x16x32_bf16 v[104:107], a[164:167], v[220:223], v[104:107]\n"
    "s_waitcnt vmcnt(8)\n"
    "v_mfma_f32_16x16x32_bf16 v[108:111], a[164:167], v[228:231], v[108:111]\n"
    "s_barrier\n"
    "v_mfma_f32_16x16x32_bf16 v[108:111], a[160:163], v[224:227], v[108:111]\n"
    "v_exp_f32_e32 v175, v175\n"
    "v_mfma_f32_16x16x32_bf16 v[112:115], a[160:163], v[232:235], v[112:115]\n"
    "ds_read_b128 a[160:163], v243 offset:53248\n"
    "v_mfma_f32_16x16x32_bf16 v[112:115], a[164:167], v[236:239], v[112:115]\n"
    "ds_read_b128 a[164:167], v240 offset:49152\n"
    "v_mfma_f32_16x16x32_bf16 v[124:127], a[168:171], v[232:235], v[124:127]\n"
    "ds_write_addtid_b32 v192 offset:16384\n"
    "v_cvt_pk_bf16_f32 v170, v172, v173\n"
    "v_mfma_f32_16x16x32_bf16 v[124:127], a[172:175], v[236:239], v[124:127]\n"
    "v_exp_f32_e32 v164, v164\n"
    "v_mfma_f32_16x16x32_bf16 v[120:123], a[172:175], v[228:231], v[120:123]\n"
    "v_exp_f32_e32 v165, v165\n"
    "v_mfma_f32_16x16x32_bf16 v[120:123], a[168:171], v[224:227], v[120:123]\n"
    "ds_write_addtid_b32 v193 offset:16640\n"
    "v_cvt_pk_bf16_f32 v171, v174, v175\n"
    "v_mfma_f32_16x16x32_bf16 v[116:119], a[168:171], v[216:219], v[116:119]\n"
    "v_exp_f32_e32 v166, v166\n"
    "v_mfma_f32_16x16x32_bf16 v[116:119], a[172:175], v[220:223], v[116:119]\n"
    "v_exp_f32_e32 v167, v167\n"
    "v_mfma_f32_16x16x32_bf16 v[44:47], a[172:175], v[212:215], v[44:47]\n"
    "ds_write_addtid_b32 v184 offset:12288\n"
    "v_cvt_pk_bf16_f32 v162, v164, v165\n"
    "v_mfma_f32_16x16x32_bf16 v[44:47], a[168:171], v[208:211], v[44:47]\n"
    "ds_write_addtid_b32 v185 offset:12544\n"
    "v_cvt_pk_bf16_f32 v163, v166, v167\n"
    "v_mfma_f32_16x16x32_bf16 v[20:23], a[168:171], v[200:203], v[20:23]\n"
    "ds_read_b128 a[168:171], v241 offset:49152\n"
    "v_mfma_f32_16x16x32_bf16 v[20:23], a[172:175], v[204:207], v[20:23]\n"
    "s_waitcnt lgkmcnt(7)\n"
    "ds_read_b128 a[172:175], v240 offset:53248\n"
    "v_mfma_f32_16x16x32_bf16 v[24:27], a[176:179], v[200:203], v[24:27]\n"
    "ds_write_addtid_b32 v176 offset:8192\n"
    "v_mfma_f32_16x16x32_bf16 v[24:27], a[180:183], v[204:207], v[24:27]\n"
    "ds_write_addtid_b32 v177 offset:8448\n"
    "v_mfma_f32_16x16x32_bf16 v[48:51], a[180:183], v[212:215], v[48:51]\n"
    "ds_write_addtid_b32 v168 offset:4096\n"
    "v_mfma_f32_16x16x32_bf16 v[48:51], a[176:179], v[208:211], v[48:51]\n"
    "ds_write_addtid_b32 v169 offset:4352\n"
    "v_mfma_f32_16x16x32_bf16 v[128:131], a[176:179], v[216:219], v[128:131]\n"
    "ds_write_addtid_b32 v160 offset:0\n"
    "v_mfma_f32_16x16x32_bf16 v[128:131], a[180:183], v[220:223], v[128:131]\n"
    "ds_write_addtid_b32 v161 offset:256\n"
    "v_mfma_f32_16x16x32_bf16 v[132:135], a[180:183], v[228:231], v[132:135]\n"
    "ds_write_addtid_b32 v194 offset:16896\n"
    "v_mfma_f32_16x16x32_bf16 v[132:135], a[176:179], v[224:227], v[132:135]\n"
    "ds_write_addtid_b32 v195 offset:17152\n"
    "v_mfma_f32_16x16x32_bf16 v[136:139], a[176:179], v[232:235], v[136:139]\n"
    "ds_read_b128 a[176:179], v241 offset:53248\n"
    "v_mfma_f32_16x16x32_bf16 v[136:139], a[180:183], v[236:239], v[136:139]\n"
    "ds_read_b128 a[180:183], v242 offset:49152\n"
    "v_mfma_f32_16x16x32_bf16 v[148:151], a[184:187], v[232:235], v[148:151]\n"
    "ds_write_addtid_b32 v186 offset:12800\n"
    "v_mfma_f32_16x16x32_bf16 v[148:151], a[188:191], v[236:239], v[148:151]\n"
    "ds_write_addtid_b32 v187 offset:13056\n"
    "v_mfma_f32_16x16x32_bf16 v[144:147], a[188:191], v[228:231], v[144:147]\n"
    "ds_write_addtid_b32 v178 offset:8704\n"
    "v_mfma_f32_16x16x32_bf16 v[144:147], a[184:187], v[224:227], v[144:147]\n"
    "ds_write_addtid_b32 v179 offset:8960\n"
    "v_mfma_f32_16x16x32_bf16 v[140:143], a[184:187], v[216:219], v[140:143]\n"
    "ds_write_addtid_b32 v170 offset:4608\n"
    "v_mfma_f32_16x16x32_bf16 v[140:143], a[188:191], v[220:223], v[140:143]\n"
    "ds_write_addtid_b32 v171 offset:4864\n"
    "v_mfma_f32_16x16x32_bf16 v[52:55], a[188:191], v[212:215], v[52:55]\n"
    "ds_write_addtid_b32 v162 offset:512\n"
    "v_mfma_f32_16x16x32_bf16 v[52:55], a[184:187], v[208:211], v[52:55]\n"
    "ds_write_addtid_b32 v163 offset:768\n"
    "v_mfma_f32_16x16x32_bf16 v[28:31], a[184:187], v[200:203], v[28:31]\n"
    "ds_read_b128 a[184:187], v243 offset:49152\n"
    "v_mfma_f32_16x16x32_bf16 v[28:31], a[188:191], v[204:207], v[28:31]\n"
    "s_waitcnt lgkmcnt(15)\n"
    "ds_read_b128 a[188:191], v242 offset:53248\n"
    ".Lat_p2e:\n"
    "v_mfma_f32_16x16x32_bf16 v[200:203], a[160:163], a[28:31], 0\n"
    "v_mfma_f32_16x16x32_bf16 v[200:203], a[164:167], a[0:3], v[200:203]\n"
    "v_mfma_f32_16x16x32_bf16 v[208:211], a[164:167], a[32:35], 0\n"
    "v_mfma_f32_16x16x32_bf16 v[208:211], a[160:163], a[60:63], v[208:211]\n"
    "v_mfma_f32_16x16x32_bf16 v[216:219], a[160:163], a[92:95], 0\n"
    "v_mfma_f32_16x16x32_bf16 v[216:219], a[164:167], a[64:67], v[216:219]\n"
    "s_waitcnt lgkmcnt(0)\n"
    "v_mfma_f32_16x16x32_bf16 v[224:227], a[164:167], a[96:99], 0\n"
    "s_barrier\n"
    "v_mfma_f32_16x16x32_bf16 v[224:227], a[160:163], a[124:127], v[224:227]\n"
    "s_add_u32 m0, s43, 16384\n"
    "ds_read2st64_b32 v[164:165], v250 offset0:0 offset1:1\n"
    "v_mfma_f32_16x16x32_bf16 v[232:235], a[160:163], a[156:159], 0\n"
    "ds_read_b128 a[160:163], v243 offset:55296\n"
    "v_mfma_f32_16x16x32_bf16 v[232:235], a[164:167], a[128:131], v[232:235]\n"
    "ds_read_b128 a[164:167], v240 offset:51200\n"
    "v_mfma_f32_16x16x32_bf16 v[232:235], a[168:171], a[132:135], v[232:235]\n"
    "buffer_load_dwordx4 v248, s[8:11], s40 offen offset:0 sc1 lds\n"
    "v_mfma_f32_16x16x32_bf16 v[232:235], a[172:175], a[136:139], v[232:235]\n"
    "v_mfma_f32_16x16x32_bf16 v[224:227], a[172:175], a[104:107], v[224:227]\n"
    "v_mfma_f32_16x16x32_bf16 v[224:227], a[168:171], a[100:103], v[224:227]\n"
    "ds_read2st64_b32 v[166:167], v250 offset0:2 offset1:3\n"
    "v_mfma_f32_16x16x32_bf16 v[216:219], a[168:171], a[68:71], v[216:219]\n"
    "v_mfma_f32_16x16x32_bf16 v[216:219], a[172:175], a[72:75], v[216:219]\n"
    "buffer_load_dwordx4 v248, s[8:11], s40 offen offset:1024 sc1 lds\n"
    "v_mfma_f32_16x16x32_bf16 v[208:211], a[172:175], a[40:43], v[208:211]\n"
    "v_mfma_f32_16x16x32_bf16 v[208:211], a[168:171], a[36:39], v[208:211]\n"
    "ds_read2st64_b32 v[172:173], v250 offset0:16 offset1:17\n"
    "v_mfma_f32_16x16x32_bf16 v[200:203], a[168:171], a[4:7], v[200:203]\n"
    "ds_read_b128 a[168:171], v241 offset:51200\n"
    "v_mfma_f32_16x16x32_bf16 v[200:203], a[172:175], a[8:11], v[200:203]\n"
    "ds_read_b128 a[172:175], v240 offset:55296\n"
    "v_mfma_f32_16x16x32_bf16 v[200:203], a[176:179], a[12:15], v[200:203]\n"
    "buffer_load_dwordx4 v248, s[8:11], s40 offen offset:2048 sc1 lds\n"
    "v_mfma_f32_16x16x32_bf16 v[200:203], a[180:183], a[16:19], v[200:203]\n"
    "ds_read2st64_b32 v[174:175], v250 offset0:18 offset1:19\n"
    "v_mfma_f32_16x16x32_bf16 v[208:211], a[180:183], a[48:51], v[208:211]\n"
    "v_mfma_f32_16x16x32_bf16 v[208:211], a[176:179], a[44:47], v[208:211]\n"
    "v_mfma_f32_16x16x32_bf16 v[216:219], a[176:179], a[76:79], v[216:219]\n"
    "ds_read2st64_b32 v[180:181], v250 offset0:32 offset1:33\n"
    "v_mfma_f32_16x16x32_bf16 v[216:219], a[180:183], a[80:83], v[216:219]\n"
    "buffer_load_dwordx4 v248, s[8:11], s40 offen offset:3072 sc1 lds\n"
    "v_mfma_f32_16x16x32_bf16 v[224:227], a[180:183], a[112:115], v[224:227]\n"
    "s_add_u32 m0, s43, 20480\n"
    "v_mfma_f32_16x16x32_bf16 v[224:227], a[176:179], a[108:111], v[224:227]\n"
    "s_add_u32 s40, s40, 0x8000\n"
    "ds_read2st64_b32 v[182:183], v250 offset0:34 offset1:35\n"
    "v_mfma_f32_16x16x32_bf16 v[232:235], a[176:179], a[140:143], v[232:235]\n"
    "ds_read_b128 a[176:179], v241 offset:55296\n"
    "v_mfma_f32_16x16x32_bf16 v[232:235], a[180:183], a[144:147], v[232:235]\n"
    "ds_read_b128 a[180:183], v242 offset:51200\n"
    "v_mfma_f32_16x16x32_bf16 v[232:235], a[184:187], a[148:151], v[232:235]\n"
    "buffer_load_dwordx4 v248, s[8:11], s41 offen offset:0 sc1 lds\n"
    "v_mfma_f32_16x16x32_bf16 v[232:235], a[188:191], a[152:155], v[232:235]\n"
    "v_mfma_f32_16x16x32_bf16 v[224:227], a[188:191], a[120:123], v[224:227]\n"
    "ds_read2st64_b32 v[188:189], v250 offset0:48 offset1:49\n"
    "v_mfma_f32_16x16x32_bf16 v[224:227], a[184:187], a[116:119], v[224:227]\n"
    "v_mfma_f32_16x16x32_bf16 v[216:219], a[184:187], a[84:87], v[216:219]\n"
    "v_exp_f32_e32 v232, v232\n"
    "v_mfma_f32_16x16x32_bf16 v[216:219], a[188:191], a[88:91], v[216:219]\n"
    "buffer_load_dwordx4 v248, s[8:11], s41 offen offset:1024 sc1 lds\n"
    "v_mfma_f32_16x16x32_bf16 v[208:211], a[188:191], a[56:59], v[208:211]\n"
    "ds_read2st64_b32 v[190:191], v250 offset0:50 offset1:51\n"
    "v_mfma_f32_16x16x32_bf16 v[208:211], a[184:187], a[52:55], v[208:211]\n"
    "v_exp_f32_e32 v233, v233\n"
    "v_mfma_f32_16x16x32_bf16 v[200:203], a[184:187], a[20:23], v[200:203]\n"
    "ds_read_b128 a[184:187], v243 offset:51200\n"
    "v_mfma_f32_16x16x32_bf16 v[200:203], a[188:191], a[24:27], v[200:203]\n"
    "s_waitcnt lgkmcnt(8)\n"
    "ds_read_b128 a[188:191], v242 offset:55296\n"
    "v_mfma_f32_16x16x32_bf16 v[204:207], a[160:163], a[28:31], 0\n"
    "buffer_load_dwordx4 v248, s[8:11], s41 offen offset:2048 sc1 lds\n"
    "v_mfma_f32_16x16x32_bf16 v[204:207], a[164:167], a[0:3], v[204:207]\n"
    "ds_read2st64_b32 v[196:197], v250 offset0:64 offset1:65\n"
    "v_mfma_f32_16x16x32_bf16 v[212:215], a[164:167], a[32:35], 0\n"
    "v_exp_f32_e32 v234, v234\n"
    "v_mfma_f32_16x16x32_bf16 v[212:215], a[160:163], a[60:63], v[212:215]\n"
    "ds_read2st64_b32 v[198:199], v250 offset0:66 offset1:67\n"
    "v_mfma_f32_16x16x32_bf16 v[220:223], a[160:163], a[92:95], 0\n"
    "v_exp_f32_e32 v235, v235\n"
    "v_mfma_f32_16x16x32_bf16 v[220:223], a[164:167], a[64:67], v[220:223]\n"
    "buffer_load_dwordx4 v248, s[8:11], s41 offen offset:3072 sc1 lds\n"
    "v_cvt_pk_bf16_f32 v232, v232, v233\n"
    "v_mfma_f32_16x16x32_bf16 v[228:231], a[164:167], a[96:99], 0\n"
    "s_add_u32 s41, s41, 0x8000\n"
    "v_exp_f32_e32 v224, v224\n"
    "v_mfma_f32_16x16x32_bf16 v[228:231], a[160:163], a[124:127], v[228:231]\n"
    "v_exp_f32_e32 v225, v225\n"
    "v_mfma_f32_16x16x32_bf16 v[236:239], a[160:163], a[156:159], 0\n"
    "ds_read_b64_tr_b16 a[160:161], v244 offset:32768\n"
    "v_cvt_pk_bf16_f32 v233, v234, v235\n"
    "v_mfma_f32_16x16x32_bf16 v[236:239], a[164:167], a[128:131], v[236:239]\n"
    "ds_read_b64_tr_b16 a[162:163], v244 offset:34816\n"
    "v_mfma_f32_16x16x32_bf16 v[236:239], a[168:171], a[132:135], v[236:239]\n"
    "ds_read_b64_tr_b16 a[164:165], v244 offset:40960\n"
    "v_mfma_f32_16x16x32_bf16 v[236:239], a[172:175], a[136:139], v[236:239]\n"
    "ds_read_b64_tr_b16 a[166:167], v244 offset:43008\n"
    "v_mfma_f32_16x16x32_bf16 v[228:231], a[172:175], a[104:107], v[228:231]\n"
    "v_exp_f32_e32 v226, v226\n"
    "v_mfma_f32_16x16x32_bf16 v[228:231], a[168:171], a[100:103], v[228:231]\n"
    "v_exp_f32_e32 v227, v227\n"
    "v_mfma_f32_16x16x32_bf16 v[220:223], a[168:171], a[68:71], v[220:223]\n"
    "v_cvt_pk_bf16_f32 v224, v224, v225\n"
    "v_mfma_f32_16x16x32_bf16 v[220:223], a[172:175], a[72:75], v[220:223]\n"
    "v_exp_f32_e32 v216, v216\n"
    "v_mfma_f32_16x16x32_bf16 v[212:215], a[172:175], a[40:43], v[212:215]\n"
    "v_exp_f32_e32 v217, v217\n"
    "v_mfma_f32_16x16x32_bf16 v[212:215], a[168:171], a[36:39], v[212:215]\n"
    "v_cvt_pk_bf16_f32 v225, v226, v227\n"
    "v_mfma_f32_16x16x32_bf16 v[204:207], a[168:171], a[4:7], v[204:207]\n"
    "ds_read_b64_tr_b16 a[168:169], v245 offset:32768\n"
    "v_mfma_f32_16x16x32_bf16 v[204:207], a[172:175], a[8:11], v[204:207]\n"
    "s_waitcnt lgkmcnt(7)\n"
    "ds_read_b64_tr_b16 a[170:171], v245 offset:34816\n"
    "v_mfma_f32_16x16x32_bf16 v[204:207], a[176:179], a[12:15], v[204:207]\n"
    "ds_read_b64_tr_b16 a[172:173], v245 offset:40960\n"
    "v_mfma_f32_16x16x32_bf16 v[204:207], a[180:183], a[16:19], v[204:207]\n"
    "ds_read_b64_tr_b16 a[174:175], v245 offset:43008\n"
    "v_mfma_f32_16x16x32_bf16 v[212:215], a[180:183], a[48:51], v[212:215]\n"
    "v_exp_f32_e32 v218, v218\n"
    "v_mfma_f32_16x16x32_bf16 v[212:215], a[176:179], a[44:47], v[212:215]\n"
    "v_exp_f32_e32 v219, v219\n"
    "v_mfma_f32_16x16x32_bf16 v[220:223], a[176:179], a[76:79], v[220:223]\n"
    "v_cvt_pk_bf16_f32 v216, v216, v217\n"
    "v_mfma_f32_16x16x32_bf16 v[220:223], a[180:183], a[80:83], v[220:223]\n"
    "v_exp_f32_e32 v208, v208\n"
    "v_mfma_f32_16x16x32_bf16 v[228:231], a[180:183], a[112:115], v[228:231]\n"
    "v_exp_f32_e32 v209, v209\n"
    "v_mfma_f32_16x16x32_bf16 v[228:231], a[176:179], a[108:111], v[228:231]\n"
    "v_cvt_pk_bf16_f32 v217, v218, v219\n"
    "v_mfma_f32_16x16x32_bf16 v[236:239], a[176:179], a[140:143], v[236:239]\n"
    "ds_read_b64_tr_b16 a[176:177], v246 offset:32768\n"
    "v_mfma_f32_16x16x32_bf16 v[236:239], a[180:183], a[144:147], v[236:239]\n"
    "ds_read_b64_tr_b16 a[178:179], v246 offset:34816\n"
    "v_mfma_f32_16x16x32_bf16 v[236:239], a[184:187], a[148:151], v[236:239]\n"
    "ds_read_b64_tr_b16 a[180:181], v246 offset:40960\n"
    "v_mfma_f32_16x16x32_bf16 v[236:239], a[188:191], a[152:155], v[236:239]\n"
    "ds_read_b64_tr_b16 a[182:183], v246 offset:43008\n"
    "v_mfma_f32_16x16x32_bf16 v[228:231], a[188:191], a[120:123], v[228:231]\n"
    "v_exp_f32_e32 v210, v210\n"
    "v_mfma_f32_16x16x32_bf16 v[228:231], a[184:187], a[116:119], v[228:231]\n"
    "v_exp_f32_e32 v211, v211\n"
    "v_mfma_f32_16x16x32_bf16 v[220:223], a[184:187], a[84:87], v[220:223]\n"
    "v_cvt_pk_bf16_f32 v208, v208, v209\n"
    "v_mfma_f32_16x16x32_bf16 v[220:223], a[188:191], a[88:91], v[220:223]\n"
    "v_exp_f32_e32 v200, v200\n"
    "v_mfma_f32_16x16x32_bf16 v[212:215], a[188:191], a[56:59], v[212:215]\n"
    "v_exp_f32_e32 v201, v201\n"
    "v_mfma_f32_16x16x32_bf16 v[212:215], a[184:187], a[52:55], v[212:215]\n"
    "v_cvt_pk_bf16_f32 v209, v210, v211\n"
    "v_mfma_f32_16x16x32_bf16 v[204:207], a[184:187], a[20:23], v[204:207]\n"
    "ds_read_b64_tr_b16 a[184:185], v247 offset:32768\n"
    "v_mfma_f32_16x16x32_bf16 v[204:207], a[188:191], a[24:27], v[204:207]\n"
    "s_waitcnt lgkmcnt(5)\n"
    "ds_read_b64_tr_b16 a[186:187], v247 offset:34816\n"
    "v_mfma_f32_16x16x32_bf16 v[0:3], a[160:163], v[160:163], v[0:3]\n"
    "ds_read_b64_tr_b16 a[188:189], v247 offset:40960\n"
    "v_mfma_f32_16x16x32_bf16 v[0:3], a[164:167], v[164:167], v[0:3]\n"
    "ds_read_b64_tr_b16 a[190:191], v247 offset:43008\n"
    "v_mfma_f32_16x16x32_bf16 a[248:251], a[164:167], v[172:175], a[248:251]\n"
    "v_exp_f32_e32 v202, v202\n"
    "v_mfma_f32_16x16x32_bf16 a[248:251], a[160:163], v[168:171], a[248:251]\n"
    "v_exp_f32_e32 v203, v203\n"
    "v_mfma_f32_16x16x32_bf16 v[56:59], a[160:163], v[176:179], v[56:59]\n"
    "v_cvt_pk_bf16_f32 v200, v200, v201\n"
    "v_mfma_f32_16x16x32_bf16 v[56:59], a[164:167], v[180:183], v[56:59]\n"
    "v_cvt_pk_bf16_f32 v201, v202, v203\n"
    "v_mfma_f32_16x16x32_bf16 v[60:63], a[164:167], v[188:191], v[60:63]\n"
    "v_exp_f32_e32 v236, v236\n"
    "v_mfma_f32_16x16x32_bf16 v[60:63], a[160:163], v[184:187], v[60:63]\n"
    "v_exp_f32_e32 v237, v237\n"
    "v_mfma_f32_16x16x32_bf16 v[64:67], a[160:163], v[192:195], v[64:67]\n"
    "ds_read_b64_tr_b16 a[160:161], v244 offset:36864\n"
    "v_mfma_f32_16x16x32_bf16 v[64:67], a[164:167], v[196:199], v[64:67]\n"
    "ds_read_b64_tr_b16 a[162:163], v244 offset:38912\n"
    "v_mfma_f32_16x16x32_bf16 v[76:79], a[168:171], v[192:195], v[76:79]\n"
    "ds_read_b64_tr_b16 a[164:165], v244 offset:45056\n"
    "v_mfma_f32_16x16x32_bf16 v[76:79], a[172:175], v[196:199], v[76:79]\n"
    "ds_read_b64_tr_b16 a[166:167], v244 offset:47104\n"
    "v_mfma_f32_16x16x32_bf16 v[72:75], a[172:175], v[188:191], v[72:75]\n"
    "v_exp_f32_e32 v238, v238\n"
    "v_mfma_f32_16x16x32_bf16 v[72:75], a[168:171], v[184:187], v[72:75]\n"
    "v_exp_f32_e32 v239, v239\n"
    "v_mfma_f32_16x16x32_bf16 v[68:71], a[168:171], v[176:179], v[68:71]\n"
    "v_cvt_pk_bf16_f32 v234, v236, v237\n"
    "v_mfma_f32_16x16x32_bf16 v[68:71], a[172:175], v[180:183], v[68:71]\n"
    "v_exp_f32_e32 v228, v228\n"
    "v_mfma_f32_16x16x32_bf16 a[252:255], a[172:175], v[172:175], a[252:255]\n"
    "v_exp_f32_e32 v229, v229\n"
    "v_mfma_f32_16x16x32_bf16 a[252:255], a[168:171], v[168:171], a[252:255]\n"
    "v_cvt_pk_bf16_f32 v235, v238, v239\n"
    "v_mfma_f32_16x16x32_bf16 v[4:7], a[168:171], v[160:163], v[4:7]\n"
    "ds_read_b64_tr_b16 a[168:169], v245 offset:36864\n"
    "v_mfma_f32_16x16x32_bf16 v[4:7], a[172:175], v[164:167], v[4:7]\n"
    "s_waitcnt lgkmcnt(5)\n"
    "ds_read_b64_tr_b16 a[170:171], v245 offset:38912\n"
    "v_mfma_f32_16x16x32_bf16 v[8:11], a[176:179], v[160:163], v[8:11]\n"
    "ds_read_b64_tr_b16 a[172:173], v245 offset:45056\n"
    "v_mfma_f32_16x16x32_bf16 v[8:11], a[180:183], v[164:167], v[8:11]\n"
    "ds_read_b64_tr_b16 a[174:175], v245 offset:47104\n"
    "v_mfma_f32_16x16x32_bf16 v[32:35], a[180:183], v[172:175], v[32:35]\n"
    "s_or_b32 s45, s44, s37\n"
    "s_cselect_b32 s46, 2.0, s81\n"
    "s_cselect_b32 s45, 2.0, s80\n"
    "v_mfma_f32_16x16x32_bf16 v[32:35], a[176:179], v[168:171], v[32:35]\n"
    "v_exp_f32_e32 v230, v230\n"
    "v_mfma_f32_16x16x32_bf16 v[80:83], a[176:179], v[176:179], v[80:83]\n"
    "v_exp_f32_e32 v231, v231\n"
    "v_mfma_f32_16x16x32_bf16 v[80:83], a[180:183], v[180:183], v[80:83]\n"
    "v_cvt_pk_bf16_f32 v226, v228, v229\n"
    "v_mfma_f32_16x16x32_bf16 v[84:87], a[180:183], v[188:191], v[84:87]\n"
    "v_exp_f32_e32 v220, v220\n"
    "v_mfma_f32_16x16x32_bf16 v[84:87], a[176:179], v[184:187], v[84:87]\n"
    "v_exp_f32_e32 v221, v221\n"
    "v_mfma_f32_16x16x32_bf16 v[88:91], a[176:179], v[192:195], v[88:91]\n"
    "ds_read_b64_tr_b16 a[176:177], v246 offset:36864\n"
    "v_mfma_f32_16x16x32_bf16 v[88:91], a[180:183], v[196:199], v[88:91]\n"
    "ds_read_b64_tr_b16 a[178:179], v246 offset:38912\n"
    "v_mfma_f32_16x16x32_bf16 v[100:103], a[184:187], v[192:195], v[100:103]\n"
    "ds_read_b64_tr_b16 a[180:181], v246 offset:45056\n"
    "v_mfma_f32_16x16x32_bf16 v[100:103], a[188:191], v[196:199], v[100:103]\n"
    "ds_read_b64_tr_b16 a[182:183], v246 offset:47104\n"
    "v_mfma_f32_16x16x32_bf16 v[96:99], a[188:191], v[188:191], v[96:99]\n"
    "v_cvt_pk_bf16_f32 v227, v230, v231\n"
    "v_mfma_f32_16x16x32_bf16 v[96:99], a[184:187], v[184:187], v[96:99]\n"
    "v_exp_f32_e32 v222, v222\n"
    "v_mfma_f32_16x16x32_bf16 v[92:95], a[184:187], v[176:179], v[92:95]\n"
    "v_exp_f32_e32 v223, v223\n"
    "v_mfma_f32_16x16x32_bf16 v[92:95], a[188:191], v[180:183], v[92:95]\n"
    "v_cvt_pk_bf16_f32 v218, v220, v221\n"
    "v_mfma_f32_16x16x32_bf16 v[36:39], a[188:191], v[172:175], v[36:39]\n"
    "v_exp_f32_e32 v212, v212\n"
    "v_mfma_f32_16x16x32_bf16 v[36:39], a[184:187], v[168:171], v[36:39]\n"
    "v_exp_f32_e32 v213, v213\n"
    "v_mfma_f32_16x16x32_bf16 v[12:15], a[184:187], v[160:163], v[12:15]\n"
    "s_mov_b32 m0, s49\n"
    "ds_read_b64_tr_b16 a[184:185], v247 offset:36864\n"
    "v_mfma_f32_16x16x32_bf16 v[12:15], a[188:191], v[164:167], v[12:15]\n"
    "s_waitcnt lgkmcnt(5)\n"
    "ds_read_b64_tr_b16 a[186:187], v247 offset:38912\n"
    "v_mfma_f32_16x16x32_bf16 v[16:19], a[160:163], v[160:163], v[16:19]\n"
    "ds_read_b64_tr_b16 a[188:189], v247 offset:45056\n"
    "v_mfma_f32_16x16x32_bf16 v[16:19], a[164:167], v[164:167], v[16:19]\n"
    "ds_read_b64_tr_b16 a[190:191], v247 offset:47104\n"
    "v_mfma_f32_16x16x32_bf16 v[40:43], a[164:167], v[172:175], v[40:43]\n"
    "buffer_load_dword v154, off, s[8:11], s45 sc1\n"
    "v_mfma_f32_16x16x32_bf16 v[40:43], a[160:163], v[168:171], v[40:43]\n"
    "v_cvt_pk_bf16_f32 v219, v222, v223\n"
    "v_mfma_f32_16x16x32_bf16 v[104:107], a[160:163], v[176:179], v[104:107]\n"
    "v_exp_f32_e32 v214, v214\n"
    "v_mfma_f32_16x16x32_bf16 v[104:107], a[164:167], v[180:183], v[104:107]\n"
    "s_waitcnt vmcnt(9)\n"
    "v_mfma_f32_16x16x32_bf16 v[108:111], a[164:167], v[188:191], v[108:111]\n"
    "s_barrier\n"
    "v_mfma_f32_16x16x32_bf16 v[108:111], a[160:163], v[184:187], v[108:111]\n"
    "v_exp_f32_e32 v215, v215\n"
    "v_mfma_f32_16x16x32_bf16 v[112:115], a[160:163], v[192:195], v[112:115]\n"
    "ds_read_b128 a[160:163], v243 offset:4096\n"
    "v_mfma_f32_16x16x32_bf16 v[112:115], a[164:167], v[196:199], v[112:115]\n"
    "ds_read_b128 a[164:167], v240 offset:0\n"
    "v_mfma_f32_16x16x32_bf16 v[124:127], a[168:171], v[192:195], v[124:127]\n"
    "ds_write_addtid_b32 v232 offset:16384\n"
    "v_cvt_pk_bf16_f32 v210, v212, v213\n"
    "v_mfma_f32_16x16x32_bf16 v[124:127], a[172:175], v[196:199], v[124:127]\n"
    "v_exp_f32_e32 v204, v204\n"
    "v_mfma_f32_16x16x32_bf16 v[120:123], a[172:175], v[188:191], v[120:123]\n"
    "v_exp_f32_e32 v205, v205\n"
    "v_mfma_f32_16x16x32_bf16 v[120:123], a[168:171], v[184:187], v[120:123]\n"
    "ds_write_addtid_b32 v233 offset:16640\n"
    "v_cvt_pk_bf16_f32 v211, v214, v215\n"
    "v_mfma_f32_16x16x32_bf16 v[116:119], a[168:171], v[176:179], v[116:119]\n"
    "v_exp_f32_e32 v206, v206\n"
    "v_mfma_f32_16x16x32_bf16 v[116:119], a[172:175], v[180:183], v[116:119]\n"
    "v_exp_f32_e32 v207, v207\n"
    "v_mfma_f32_16x16x32_bf16 v[44:47], a[172:175], v[172:175], v[44:47]\n"
    "ds_write_addtid_b32 v224 offset:12288\n"
    "v_cvt_pk_bf16_f32 v202, v204, v205\n"
    "v_mfma_f32_16x16x32_bf16 v[44:47], a[168:171], v[168:171], v[44:47]\n"
    "ds_write_addtid_b32 v225 offset:12544\n"
    "v_cvt_pk_bf16_f32 v203, v206, v207\n"
    "v_mfma_f32_16x16x32_bf16 v[20:23], a[168:171], v[160:163], v[20:23]\n"
    "ds_read_b128 a[168:171], v241 offset:0\n"
    "v_mfma_f32_16x16x32_bf16 v[20:23], a[172:175], v[164:167], v[20:23]\n"
    "s_waitcnt lgkmcnt(7)\n"
    "ds_read_b128 a[172:175], v240 offset:4096\n"
    "v_mfma_f32_16x16x32_bf16 v[24:27], a[176:179], v[160:163], v[24:27]\n"
    "ds_write_addtid_b32 v216 offset:8192\n"
    "v_mfma_f32_16x16x32_bf16 v[24:27], a[180:183], v[164:167], v[24:27]\n"
    "ds_write_addtid_b32 v217 offset:8448\n"
    "v_mfma_f32_16x16x32_bf16 v[48:51], a[180:183], v[172:175], v[48:51]\n"
    "ds_write_addtid_b32 v208 offset:4096\n"
    "v_mfma_f32_16x16x32_bf16 v[48:51], a[176:179], v[168:171], v[48:51]\n"
    "ds_write_addtid_b32 v209 offset:4352\n"
    "v_mfma_f32_16x16x32_bf16 v[128:131], a[176:179], v[176:179], v[128:131]\n"
    "ds_write_addtid_b32 v200 offset:0\n"
    "v_mfma_f32_16x16x32_bf16 v[128:131], a[180:183], v[180:183], v[128:131]\n"
    "ds_write_addtid_b32 v201 offset:256\n"
    "v_mfma_f32_16x16x32_bf16 v[132:135], a[180:183], v[188:191], v[132:135]\n"
    "ds_write_addtid_b32 v234 offset:16896\n"
    "v_mfma_f32_16x16x32_bf16 v[132:135], a[176:179], v[184:187], v[132:135]\n"
    "ds_write_addtid_b32 v235 offset:17152\n"
    "v_mfma_f32_16x16x32_bf16 v[136:139], a[176:179], v[192:195], v[136:139]\n"
    "ds_read_b128 a[176:179], v241 offset:4096\n"
    "v_mfma_f32_16x16x32_bf16 v[136:139], a[180:183], v[196:199], v[136:139]\n"
    "ds_read_b128 a[180:183], v242 offset:0\n"
    "v_mfma_f32_16x16x32_bf16 v[148:151], a[184:187], v[192:195], v[148:151]\n"
    "ds_write_addtid_b32 v226 offset:12800\n"
    "v_mfma_f32_16x16x32_bf16 v[148:151], a[188:191], v[196:199], v[148:151]\n"
    "ds_write_addtid_b32 v227 offset:13056\n"
    "v_mfma_f32_16x16x32_bf16 v[144:147], a[188:191], v[188:191], v[144:147]\n"
    "ds_write_addtid_b32 v218 offset:8704\n"
    "v_mfma_f32_16x16x32_bf16 v[144:147], a[184:187], v[184:187], v[144:147]\n"
    "ds_write_addtid_b32 v219 offset:8960\n"
    "v_mfma_f32_16x16x32_bf16 v[140:143], a[184:187], v[176:179], v[140:143]\n"
    "ds_write_addtid_b32 v210 offset:4608\n"
    "v_mfma_f32_16x16x32_bf16 v[140:143], a[188:191], v[180:183], v[140:143]\n"
    "ds_write_addtid_b32 v211 offset:4864\n"
    "v_mfma_f32_16x16x32_bf16 v[52:55], a[188:191], v[172:175], v[52:55]\n"
    "ds_write_addtid_b32 v202 offset:512\n"
    "v_mfma_f32_16x16x32_bf16 v[52:55], a[184:187], v[168:171], v[52:55]\n"
    "ds_write_addtid_b32 v203 offset:768\n"
    "v_mfma_f32_16x16x32_bf16 v[28:31], a[184:187], v[160:163], v[28:31]\n"
    "ds_read_b128 a[184:187], v243 offset:0\n"
    "v_mfma_f32_16x16x32_bf16 v[28:31], a[188:191], v[164:167], v[28:31]\n"
    "s_waitcnt lgkmcnt(15)\n"
    "ds_read_b128 a[188:191], v242 offset:4096\n"
    "v_mfma_f32_16x16x32_bf16 v[160:163], a[160:163], a[28:31], 0\n"
    "v_lshlrev_b32_e32 v253, 2, v249\n"
    "v_mfma_f32_16x16x32_bf16 v[160:163], a[164:167], a[0:3], v[160:163]\n"
    "v_mfma_f32_16x16x32_bf16 v[168:171], a[164:167], a[32:35], 0\n"
    "v_mfma_f32_16x16x32_bf16 v[168:171], a[160:163], a[60:63], v[168:171]\n"
    "v_mfma_f32_16x16x32_bf16 v[176:179], a[160:163], a[92:95], 0\n"
    "v_mfma_f32_16x16x32_bf16 v[176:179], a[164:167], a[64:67], v[176:179]\n"
    "s_waitcnt lgkmcnt(0)\n"
    "v_mfma_f32_16x16x32_bf16 v[184:187], a[164:167], a[96:99], 0\n"
    "s_barrier\n"
    "v_mfma_f32_16x16x32_bf16 v[184:187], a[160:163], a[124:127], v[184:187]\n"
    "s_add_u32 m0, s43, 32768\n"
    "ds_read2st64_b32 v[204:205], v250 offset0:0 offset1:1\n"
    "v_mfma_f32_16x16x32_bf16 v[192:195], a[160:163], a[156:159], 0\n"
    "ds_read_b128 a[160:163], v243 offset:6144\n"
    "v_mfma_f32_16x16x32_bf16 v[192:195], a[164:167], a[128:131], v[192:195]\n"
    "ds_read_b128 a[164:167], v240 offset:2048\n"
    "v_mfma_f32_16x16x32_bf16 v[192:195], a[168:171], a[132:135], v[192:195]\n"
    "buffer_load_dwordx4 v248, s[8:11], s40 offen offset:0 sc1 lds\n"
    "v_mfma_f32_16x16x32_bf16 v[192:195], a[172:175], a[136:139], v[192:195]\n"
    "v_mfma_f32_16x16x32_bf16 v[184:187], a[172:175], a[104:107], v[184:187]\n"
    "v_mfma_f32_16x16x32_bf16 v[184:187], a[168:171], a[100:103], v[184:187]\n"
    "ds_read2st64_b32 v[206:207], v250 offset0:2 offset1:3\n"
    "v_mfma_f32_16x16x32_bf16 v[176:179], a[168:171], a[68:71], v[176:179]\n"
    "v_mfma_f32_16x16x32_bf16 v[176:179], a[172:175], a[72:75], v[176:179]\n"
    "buffer_load_dwordx4 v248, s[8:11], s40 offen offset:1024 sc1 lds\n"
    "v_mfma_f32_16x16x32_bf16 v[168:171], a[172:175], a[40:43], v[168:171]\n"
    "v_mfma_f32_16x16x32_bf16 v[168:171], a[168:171], a[36:39], v[168:171]\n"
    "ds_read2st64_b32 v[212:213], v250 offset0:16 offset1:17\n"
    "v_mfma_f32_16x16x32_bf16 v[160:163], a[168:171], a[4:7], v[160:163]\n"
    "ds_read_b128 a[168:171], v241 offset:2048\n"
    "v_mfma_f32_16x16x32_bf16 v[160:163], a[172:175], a[8:11], v[160:163]\n"
    "ds_read_b128 a[172:175], v240 offset:6144\n"
    "v_mfma_f32_16x16x32_bf16 v[160:163], a[176:179], a[12:15], v[160:163]\n"
    "buffer_load_dwordx4 v248, s[8:11], s40 offen offset:2048 sc1 lds\n"
    "v_mfma_f32_16x16x32_bf16 v[160:163], a[180:183], a[16:19], v[160:163]\n"
    "ds_read2st64_b32 v[214:215], v250 offset0:18 offset1:19\n"
    "v_mfma_f32_16x16x32_bf16 v[168:171], a[180:183], a[48:51], v[168:171]\n"
    "v_mfma_f32_16x16x32_bf16 v[168:171], a[176:179], a[44:47], v[168:171]\n"
    "v_mfma_f32_16x16x32_bf16 v[176:179], a[176:179], a[76:79], v[176:179]\n"
    "ds_read2st64_b32 v[220:221], v250 offset0:32 offset1:33\n"
    "v_mfma_f32_16x16x32_bf16 v[176:179], a[180:183], a[80:83], v[176:179]\n"
    "buffer_load_dwordx4 v248, s[8:11], s40 offen offset:3072 sc1 lds\n"
    "v_mfma_f32_16x16x32_bf16 v[184:187], a[180:183], a[112:115], v[184:187]\n"
    "s_add_u32 m0, s43, 36864\n"
    "v_mfma_f32_16x16x32_bf16 v[184:187], a[176:179], a[108:111], v[184:187]\n"
    "s_add_u32 s40, s40, 0x8000\n"
    "ds_read2st64_b32 v[222:223], v250 offset0:34 offset1:35\n"
    "v_mfma_f32_16x16x32_bf16 v[192:195], a[176:179], a[140:143], v[192:195]\n"
    "ds_read_b128 a[176:179], v241 offset:6144\n"
    "v_mfma_f32_16x16x32_bf16 v[192:195], a[180:183], a[144:147], v[192:195]\n"
    "ds_read_b128 a[180:183], v242 offset:2048\n"
    "v_mfma_f32_16x16x32_bf16 v[192:195], a[184:187], a[148:151], v[192:195]\n"
    "buffer_load_dwordx4 v248, s[8:11], s41 offen offset:0 sc1 lds\n"
    "v_mfma_f32_16x16x32_bf16 v[192:195], a[188:191], a[152:155], v[192:195]\n"
    "v_mfma_f32_16x16x32_bf16 v[184:187], a[188:191], a[120:123], v[184:187]\n"
    "ds_read2st64_b32 v[228:229], v250 offset0:48 offset1:49\n"
    "v_mfma_f32_16x16x32_bf16 v[184:187], a[184:187], a[116:119], v[184:187]\n"
    "v_mfma_f32_16x16x32_bf16 v[176:179], a[184:187], a[84:87], v[176:179]\n"
    "v_exp_f32_e32 v192, v192\n"
    "v_mfma_f32_16x16x32_bf16 v[176:179], a[188:191], a[88:91], v[176:179]\n"
    "buffer_load_dwordx4 v248, s[8:11], s41 offen offset:1024 sc1 lds\n"
    "v_mfma_f32_16x16x32_bf16 v[168:171], a[188:191], a[56:59], v[168:171]\n"
    "ds_read2st64_b32 v[230:231], v250 offset0:50 offset1:51\n"
    "v_mfma_f32_16x16x32_bf16 v[168:171], a[184:187], a[52:55], v[168:171]\n"
    "v_exp_f32_e32 v193, v193\n"
    "v_mfma_f32_16x16x32_bf16 v[160:163], a[184:187], a[20:23], v[160:163]\n"
    "ds_read_b128 a[184:187], v243 offset:2048\n"
    "v_mfma_f32_16x16x32_bf16 v[160:163], a[188:191], a[24:27], v[160:163]\n"
    "s_waitcnt lgkmcnt(8)\n"
    "ds_read_b128 a[188:191], v242 offset:6144\n"
    "v_mfma_f32_16x16x32_bf16 v[164:167], a[160:163], a[28:31], 0\n"
    "buffer_load_dwordx4 v248, s[8:11], s41 offen offset:2048 sc1 lds\n"
    "v_mfma_f32_16x16x32_bf16 v[164:167], a[164:167], a[0:3], v[164:167]\n"
    "ds_read2st64_b32 v[236:237], v250 offset0:64 offset1:65\n"
    "v_mfma_f32_16x16x32_bf16 v[172:175], a[164:167], a[32:35], 0\n"
    "v_exp_f32_e32 v194, v194\n"
    "v_mfma_f32_16x16x32_bf16 v[172:175], a[160:163], a[60:63], v[172:175]\n"
    "ds_read2st64_b32 v[238:239], v250 offset0:66 offset1:67\n"
    "v_mfma_f32_16x16x32_bf16 v[180:183], a[160:163], a[92:95], 0\n"
    "v_exp_f32_e32 v195, v195\n"
    "v_mfma_f32_16x16x32_bf16 v[180:183], a[164:167], a[64:67], v[180:183]\n"
    "buffer_load_dwordx4 v248, s[8:11], s41 offen offset:3072 sc1 lds\n"
    "v_cvt_pk_bf16_f32 v192, v192, v193\n"
    "v_mfma_f32_16x16x32_bf16 v[188:191], a[164:167], a[96:99], 0\n"
    "s_add_u32 s41, s41, 0x8000\n"
    "v_exp_f32_e32 v184, v184\n"
    "v_mfma_f32_16x16x32_bf16 v[188:191], a[160:163], a[124:127], v[188:191]\n"
    "v_exp_f32_e32 v185, v185\n"
    "v_mfma_f32_16x16x32_bf16 v[196:199], a[160:163], a[156:159], 0\n"
    "ds_read_b64_tr_b16 a[160:161], v244 offset:49152\n"
    "v_mfma_f32_16x16x32_bf16 v[196:199], a[164:167], a[128:131], v[196:199]\n"
    "ds_read_b64_tr_b16 a[162:163], v244 offset:51200\n"
    "v_mfma_f32_16x16x32_bf16 v[196:199], a[168:171], a[132:135], v[196:199]\n"
    "ds_read_b64_tr_b16 a[164:165], v244 offset:57344\n"
    "v_mfma_f32_16x16x32_bf16 v[196:199], a[172:175], a[136:139], v[196:199]\n"
    "ds_read_b64_tr_b16 a[166:167], v244 offset:59392\n"
    "v_mfma_f32_16x16x32_bf16 v[188:191], a[172:175], a[104:107], v[188:191]\n"
    "v_cvt_pk_bf16_f32 v193, v194, v195\n"
    "v_mfma_f32_16x16x32_bf16 v[188:191], a[168:171], a[100:103], v[188:191]\n"
    "v_exp_f32_e32 v186, v186\n"
    "v_mfma_f32_16x16x32_bf16 v[180:183], a[168:171], a[68:71], v[180:183]\n"
    "v_exp_f32_e32 v187, v187\n"
    "v_mfma_f32_16x16x32_bf16 v[180:183], a[172:175], a[72:75], v[180:183]\n"
    "v_cvt_pk_bf16_f32 v184, v184, v185\n"
    "v_mfma_f32_16x16x32_bf16 v[172:175], a[172:175], a[40:43], v[172:175]\n"
    "v_exp_f32_e32 v176, v176\n"
    "v_mfma_f32_16x16x32_bf16 v[172:175], a[168:171], a[36:39], v[172:175]\n"
    "v_exp_f32_e32 v177, v177\n"
    "v_mfma_f32_16x16x32_bf16 v[164:167], a[168:171], a[4:7], v[164:167]\n"
    "ds_read_b64_tr_b16 a[168:169], v245 offset:49152\n"
    "v_mfma_f32_16x16x32_bf16 v[164:167], a[172:175], a[8:11], v[164:167]\n"
    "s_waitcnt lgkmcnt(7)\n"
    "ds_read_b64_tr_b16 a[170:171], v245 offset:51200\n"
    "v_mfma_f32_16x16x32_bf16 v[164:167], a[176:179], a[12:15], v[164:167]\n"
    "s_mov_b32 m0, s82\n"
    "ds_read_b64_tr_b16 a[172:173], v245 offset:57344\n"
    "v_mfma_f32_16x16x32_bf16 v[164:167], a[180:183], a[16:19], v[164:167]\n"
    "s_waitcnt vmcnt(8)\n"
    "ds_read_b64_tr_b16 a[174:175], v245 offset:59392\n"
    "v_mfma_f32_16x16x32_bf16 v[172:175], a[180:183], a[48:51], v[172:175]\n"
    "buffer_load_dwordx4 v253, s[8:11], s46 offen offset:0 sc1 lds\n"
    "v_mfma_f32_16x16x32_bf16 v[172:175], a[176:179], a[44:47], v[172:175]\n"
    "v_cvt_pk_bf16_f32 v185, v186, v187\n"
    "v_mfma_f32_16x16x32_bf16 v[180:183], a[176:179], a[76:79], v[180:183]\n"
    "v_exp_f32_e32 v178, v178\n"
    "v_mfma_f32_16x16x32_bf16 v[180:183], a[180:183], a[80:83], v[180:183]\n"
    "v_exp_f32_e32 v179, v179\n"
    "v_mfma_f32_16x16x32_bf16 v[188:191], a[180:183], a[112:115], v[188:191]\n"
    "v_cvt_pk_bf16_f32 v176, v176, v177\n"
    "v_mfma_f32_16x16x32_bf16 v[188:191], a[176:179], a[108:111], v[188:191]\n"
    "v_exp_f32_e32 v168, v168\n"
    "v_mfma_f32_16x16x32_bf16 v[196:199], a[176:179], a[140:143], v[196:199]\n"
    "ds_read_b64_tr_b16 a[176:177], v246 offset:49152\n"
    "v_mfma_f32_16x16x32_bf16 v[196:199], a[180:183], a[144:147], v[196:199]\n"
    "ds_read_b64_tr_b16 a[178:179], v246 offset:51200\n"
    "v_mfma_f32_16x16x32_bf16 v[196:199], a[184:187], a[148:151], v[196:199]\n"
    "ds_read_b64_tr_b16 a[180:181], v246 offset:57344\n"
    "v_mfma_f32_16x16x32_bf16 v[196:199], a[188:191], a[152:155], v[196:199]\n"
    "ds_read_b64_tr_b16 a[182:183], v246 offset:59392\n"
    "v_mfma_f32_16x16x32_bf16 v[188:191], a[188:191], a[120:123], v[188:191]\n"
    "v_exp_f32_e32 v169, v169\n"
    "v_mfma_f32_16x16x32_bf16 v[188:191], a[184:187], a[116:119], v[188:191]\n"
    "v_cvt_pk_bf16_f32 v177, v178, v179\n"
    "v_mfma_f32_16x16x32_bf16 v[180:183], a[184:187], a[84:87], v[180:183]\n"
    "v_exp_f32_e32 v170, v170\n"
    "v_mfma_f32_16x16x32_bf16 v[180:183], a[188:191], a[88:91], v[180:183]\n"
    "v_exp_f32_e32 v171, v171\n"
    "v_mfma_f32_16x16x32_bf16 v[172:175], a[188:191], a[56:59], v[172:175]\n"
    "v_cvt_pk_bf16_f32 v168, v168, v169\n"
    "v_mfma_f32_16x16x32_bf16 v[172:175], a[184:187], a[52:55], v[172:175]\n"
    "v_exp_f32_e32 v160, v160\n"
    "v_mfma_f32_16x16x32_bf16 v[164:167], a[184:187], a[20:23], v[164:167]\n"
    "ds_read_b64_tr_b16 a[184:185], v247 offset:49152\n"
    "v_mfma_f32_16x16x32_bf16 v[164:167], a[188:191], a[24:27], v[164:167]\n"
    "s_waitcnt lgkmcnt(5)\n"
    "ds_read_b64_tr_b16 a[186:187], v247 offset:51200\n"
    "v_mfma_f32_16x16x32_bf16 v[0:3], a[160:163], v[200:203], v[0:3]\n"
    "ds_read_b64_tr_b16 a[188:189], v247 offset:57344\n"
    "v_mfma_f32_16x16x32_bf16 v[0:3], a[164:167], v[204:207], v[0:3]\n"
    "ds_read_b64_tr_b16 a[190:191], v247 offset:59392\n"
    "v_mfma_f32_16x16x32_bf16 a[248:251], a[164:167], v[212:215], a[248:251]\n"
    "v_exp_f32_e32 v161, v161\n"
    "v_mfma_f32_16x16x32_bf16 a[248:251], a[160:163], v[208:211], a[248:251]\n"
    "v_cvt_pk_bf16_f32 v169, v170, v171\n"
    "v_mfma_f32_16x16x32_bf16 v[56:59], a[160:163], v[216:219], v[56:59]\n"
    "v_exp_f32_e32 v162, v162\n"
    "v_mfma_f32_16x16x32_bf16 v[56:59], a[164:167], v[220:223], v[56:59]\n"
    "v_exp_f32_e32 v163, v163\n"
    "v_mfma_f32_16x16x32_bf16 v[60:63], a[164:167], v[228:231], v[60:63]\n"
    "v_cvt_pk_bf16_f32 v160, v160, v161\n"
    "v_mfma_f32_16x16x32_bf16 v[60:63], a[160:163], v[224:227], v[60:63]\n"
    "v_cvt_pk_bf16_f32 v161, v162, v163\n"
    "v_mfma_f32_16x16x32_bf16 v[64:67], a[160:163], v[232:235], v[64:67]\n"
    "ds_read_b64_tr_b16 a[160:161], v244 offset:53248\n"
    "v_mfma_f32_16x16x32_bf16 v[64:67], a[164:167], v[236:239], v[64:67]\n"
    "ds_read_b64_tr_b16 a[162:163], v244 offset:55296\n"
    "v_mfma_f32_16x16x32_bf16 v[76:79], a[168:171], v[232:235], v[76:79]\n"
    "ds_read_b64_tr_b16 a[164:165], v244 offset:61440\n"
    "v_mfma_f32_16x16x32_bf16 v[76:79], a[172:175], v[236:239], v[76:79]\n"
    "ds_read_b64_tr_b16 a[166:167], v244 offset:63488\n"
    "v_mfma_f32_16x16x32_bf16 v[72:75], a[172:175], v[228:231], v[72:75]\n"
    "v_exp_f32_e32 v196, v196\n"
    "v_mfma_f32_16x16x32_bf16 v[72:75], a[168:171], v[224:227], v[72:75]\n"
    "v_exp_f32_e32 v197, v197\n"
    "v_mfma_f32_16x16x32_bf16 v[68:71], a[168:171], v[216:219], v[68:71]\n"
    "v_exp_f32_e32 v198, v198\n"
    "v_mfma_f32_16x16x32_bf16 v[68:71], a[172:175], v[220:223], v[68:71]\n"
    "v_exp_f32_e32 v199, v199\n"
    "v_mfma_f32_16x16x32_bf16 a[252:255], a[172:175], v[212:215], a[252:255]\n"
    "v_cvt_pk_bf16_f32 v194, v196, v197\n"
    "v_mfma_f32_16x16x32_bf16 a[252:255], a[168:171], v[208:211], a[252:255]\n"
    "v_exp_f32_e32 v188, v188\n"
    "v_mfma_f32_16x16x32_bf16 v[4:7], a[168:171], v[200:203], v[4:7]\n"
    "ds_read_b64_tr_b16 a[168:169], v245 offset:53248\n"
    "v_mfma_f32_16x16x32_bf16 v[4:7], a[172:175], v[204:207], v[4:7]\n"
    "s_waitcnt lgkmcnt(5)\n"
    "ds_read_b64_tr_b16 a[170:171], v245 offset:55296\n"
    "v_mfma_f32_16x16x32_bf16 v[8:11], a[176:179], v[200:203], v[8:11]\n"
    "ds_read_b64_tr_b16 a[172:173], v245 offset:61440\n"
    "v_mfma_f32_16x16x32_bf16 v[8:11], a[180:183], v[204:207], v[8:11]\n"
    "ds_read_b64_tr_b16 a[174:175], v245 offset:63488\n"
    "v_mfma_f32_16x16x32_bf16 v[32:35], a[180:183], v[212:215], v[32:35]\n"
    "v_exp_f32_e32 v189, v189\n"
    "v_mfma_f32_16x16x32_bf16 v[32:35], a[176:179], v[208:211], v[32:35]\n"
    "v_cvt_pk_bf16_f32 v195, v198, v199\n"
    "v_mfma_f32_16x16x32_bf16 v[80:83], a[176:179], v[216:219], v[80:83]\n"
    "v_exp_f32_e32 v190, v190\n"
    "v_mfma_f32_16x16x32_bf16 v[80:83], a[180:183], v[220:223], v[80:83]\n"
    "v_exp_f32_e32 v191, v191\n"
    "v_mfma_f32_16x16x32_bf16 v[84:87], a[180:183], v[228:231], v[84:87]\n"
    "v_cvt_pk_bf16_f32 v186, v188, v189\n"
    "v_mfma_f32_16x16x32_bf16 v[84:87], a[176:179], v[224:227], v[84:87]\n"
    "v_exp_f32_e32 v180, v180\n"
    "v_mfma_f32_16x16x32_bf16 v[88:91], a[176:179], v[232:235], v[88:91]\n"
    "ds_read_b64_tr_b16 a[176:177], v246 offset:53248\n"
    "v_mfma_f32_16x16x32_bf16 v[88:91], a[180:183], v[236:239], v[88:91]\n"
    "ds_read_b64_tr_b16 a[178:179], v246 offset:55296\n"
    "v_mfma_f32_16x16x32_bf16 v[100:103], a[184:187], v[232:235], v[100:103]\n"
    "ds_read_b64_tr_b16 a[180:181], v246 offset:61440\n"
    "v_mfma_f32_16x16x32_bf16 v[100:103], a[188:191], v[236:239], v[100:103]\n"
    "ds_read_b64_tr_b16 a[182:183], v246 offset:63488\n"
    "v_mfma_f32_16x16x32_bf16 v[96:99], a[188:191], v[228:231], v[96:99]\n"
    "v_exp_f32_e32 v181, v181\n"
    "v_mfma_f32_16x16x32_bf16 v[96:99], a[184:187], v[224:227], v[96:99]\n"
    "v_cvt_pk_bf16_f32 v187, v190, v191\n"
    "v_mfma_f32_16x16x32_bf16 v[92:95], a[184:187], v[216:219], v[92:95]\n"
    "v_exp_f32_e32 v182, v182\n"
    "v_mfma_f32_16x16x32_bf16 v[92:95], a[188:191], v[220:223], v[92:95]\n"
    "v_exp_f32_e32 v183, v183\n"
    "v_mfma_f32_16x16x32_bf16 v[36:39], a[188:191], v[212:215], v[36:39]\n"
    "v_cvt_pk_bf16_f32 v178, v180, v181\n"
    "v_mfma_f32_16x16x32_bf16 v[36:39], a[184:187], v[208:211], v[36:39]\n"
    "v_exp_f32_e32 v172, v172\n"
    "v_mfma_f32_16x16x32_bf16 v[12:15], a[184:187], v[200:203], v[12:15]\n"
    "s_mov_b32 m0, s49\n"
    "ds_read_b64_tr_b16 a[184:185], v247 offset:53248\n"
    "v_mfma_f32_16x16x32_bf16 v[12:15], a[188:191], v[204:207], v[12:15]\n"
    "s_waitcnt lgkmcnt(5)\n"
    "ds_read_b64_tr_b16 a[186:187], v247 offset:55296\n"
    "v_mfma_f32_16x16x32_bf16 v[16:19], a[160:163], v[200:203], v[16:19]\n"
    "ds_read_b64_tr_b16 a[188:189], v247 offset:61440\n"
    "v_mfma_f32_16x16x32_bf16 v[16:19], a[164:167], v[204:207], v[16:19]\n"
    "ds_read_b64_tr_b16 a[190:191], v247 offset:63488\n"
    "v_mfma_f32_16x16x32_bf16 v[40:43], a[164:167], v[212:215], v[40:43]\n"
    "v_exp_f32_e32 v173, v173\n"
    "v_mfma_f32_16x16x32_bf16 v[40:43], a[160:163], v[208:211], v[40:43]\n"
    "v_cvt_pk_bf16_f32 v179, v182, v183\n"
    "v_mfma_f32_16x16x32_bf16 v[104:107], a[160:163], v[216:219], v[104:107]\n"
    "v_exp_f32_e32 v174, v174\n"
    "v_mfma_f32_16x16x32_bf16 v[104:107], a[164:167], v[220:223], v[104:107]\n"
    "s_waitcnt vmcnt(9)\n"
    "v_mfma_f32_16x16x32_bf16 v[108:111], a[164:167], v[228:231], v[108:111]\n"
    "s_barrier\n"
    "v_mfma_f32_16x16x32_bf16 v[108:111], a[160:163], v[224:227], v[108:111]\n"
    "v_exp_f32_e32 v175, v175\n"
    "v_mfma_f32_16x16x32_bf16 v[112:115], a[160:163], v[232:235], v[112:115]\n"
    "ds_read_b128 a[160:163], v243 offset:20480\n"
    "v_mfma_f32_16x16x32_bf16 v[112:115], a[164:167], v[236:239], v[112:115]\n"
    "ds_read_b128 a[164:167], v240 offset:16384\n"
    "v_mfma_f32_16x16x32_bf16 v[124:127], a[168:171], v[232:235], v[124:127]\n"
    "ds_write_addtid_b32 v192 offset:16384\n"
    "v_cvt_pk_bf16_f32 v170, v172, v173\n"
    "v_mfma_f32_16x16x32_bf16 v[124:127], a[172:175], v[236:239], v[124:127]\n"
    "v_exp_f32_e32 v164, v164\n"
    "v_mfma_f32_16x16x32_bf16 v[120:123], a[172:175], v[228:231], v[120:123]\n"
    "v_exp_f32_e32 v165, v165\n"
    "v_mfma_f32_16x16x32_bf16 v[120:123], a[168:171], v[224:227], v[120:123]\n"
    "ds_write_addtid_b32 v193 offset:16640\n"
    "v_cvt_pk_bf16_f32 v171, v174, v175\n"
    "v_mfma_f32_16x16x32_bf16 v[116:119], a[168:171], v[216:219], v[116:119]\n"
    "v_exp_f32_e32 v166, v166\n"
    "v_mfma_f32_16x16x32_bf16 v[116:119], a[172:175], v[220:223], v[116:119]\n"
    "v_exp_f32_e32 v167, v167\n"
    "v_mfma_f32_16x16x32_bf16 v[44:47], a[172:175], v[212:215], v[44:47]\n"
    "ds_write_addtid_b32 v184 offset:12288\n"
    "v_cvt_pk_bf16_f32 v162, v164, v165\n"
    "v_mfma_f32_16x16x32_bf16 v[44:47], a[168:171], v[208:211], v[44:47]\n"
    "ds_write_addtid_b32 v185 offset:12544\n"
    "v_cvt_pk_bf16_f32 v163, v166, v167\n"
    "v_mfma_f32_16x16x32_bf16 v[20:23], a[168:171], v[200:203], v[20:23]\n"
    "ds_read_b128 a[168:171], v241 offset:16384\n"
    "v_mfma_f32_16x16x32_bf16 v[20:23], a[172:175], v[204:207], v[20:23]\n"
    "s_waitcnt lgkmcnt(7)\n"
    "ds_read_b128 a[172:175], v240 offset:20480\n"
    "v_mfma_f32_16x16x32_bf16 v[24:27], a[176:179], v[200:203], v[24:27]\n"
    "ds_write_addtid_b32 v176 offset:8192\n"
    "v_mfma_f32_16x16x32_bf16 v[24:27], a[180:183], v[204:207], v[24:27]\n"
    "ds_write_addtid_b32 v177 offset:8448\n"
    "v_mfma_f32_16x16x32_bf16 v[48:51], a[180:183], v[212:215], v[48:51]\n"
    "ds_write_addtid_b32 v168 offset:4096\n"
    "v_mfma_f32_16x16x32_bf16 v[48:51], a[176:179], v[208:211], v[48:51]\n"
    "ds_write_addtid_b32 v169 offset:4352\n"
    "v_mfma_f32_16x16x32_bf16 v[128:131], a[176:179], v[216:219], v[128:131]\n"
    "ds_write_addtid_b32 v160 offset:0\n"
    "v_mfma_f32_16x16x32_bf16 v[128:131], a[180:183], v[220:223], v[128:131]\n"
    "ds_write_addtid_b32 v161 offset:256\n"
    "v_mfma_f32_16x16x32_bf16 v[132:135], a[180:183], v[228:231], v[132:135]\n"
    "ds_write_addtid_b32 v194 offset:16896\n"
    "v_mfma_f32_16x16x32_bf16 v[132:135], a[176:179], v[224:227], v[132:135]\n"
    "ds_write_addtid_b32 v195 offset:17152\n"
    "v_mfma_f32_16x16x32_bf16 v[136:139], a[176:179], v[232:235], v[136:139]\n"
    "ds_read_b128 a[176:179], v241 offset:20480\n"
    "v_mfma_f32_16x16x32_bf16 v[136:139], a[180:183], v[236:239], v[136:139]\n"
    "ds_read_b128 a[180:183], v242 offset:16384\n"
    "v_mfma_f32_16x16x32_bf16 v[148:151], a[184:187], v[232:235], v[148:151]\n"
    "ds_write_addtid_b32 v186 offset:12800\n"
    "v_mfma_f32_16x16x32_bf16 v[148:151], a[188:191], v[236:239], v[148:151]\n"
    "ds_write_addtid_b32 v187 offset:13056\n"
    "v_mfma_f32_16x16x32_bf16 v[144:147], a[188:191], v[228:231], v[144:147]\n"
    "ds_write_addtid_b32 v178 offset:8704\n"
    "v_mfma_f32_16x16x32_bf16 v[144:147], a[184:187], v[224:227], v[144:147]\n"
    "ds_write_addtid_b32 v179 offset:8960\n"
    "v_mfma_f32_16x16x32_bf16 v[140:143], a[184:187], v[216:219], v[140:143]\n"
    "s_sub_u32 s44, s44, 1\n"
    "ds_write_addtid_b32 v170 offset:4608\n"
    "v_mfma_f32_16x16x32_bf16 v[140:143], a[188:191], v[220:223], v[140:143]\n"
    "ds_write_addtid_b32 v171 offset:4864\n"
    "v_mfma_f32_16x16x32_bf16 v[52:55], a[188:191], v[212:215], v[52:55]\n"
    "ds_write_addtid_b32 v162 offset:512\n"
    "v_mfma_f32_16x16x32_bf16 v[52:55], a[184:187], v[208:211], v[52:55]\n"
    "ds_write_addtid_b32 v163 offset:768\n"
    "v_mfma_f32_16x16x32_bf16 v[28:31], a[184:187], v[200:203], v[28:31]\n"
    "ds_read_b128 a[184:187], v243 offset:16384\n"
    "v_mfma_f32_16x16x32_bf16 v[28:31], a[188:191], v[204:207], v[28:31]\n"
    "s_waitcnt lgkmcnt(15)\n"
    "ds_read_b128 a[188:191], v242 offset:20480\n"
    "s_cbranch_scc0 .Lat_p\n"
    "s_cmp_lg_u32 s37, 0\n"
    "s_cbranch_scc1 .Lat_tail\n"
    "s_waitcnt vmcnt(0)\n"
    "v_cmp_ne_u32_e32 vcc, s39, v154\n"
    "s_mov_b32 s37, 1\n"
    "s_bitcmp1_b32 %1, 0\n"
    "s_cselect_b32 s44, 5, 3\n"
    "s_nop 1\n"
    "s_cbranch_vccnz .Lat_lpoll\n"
    ".p2align 4\n"
    ".Lat_g:\n"
    "v_mfma_f32_16x16x32_bf16 v[200:203], a[160:163], a[28:31], 0\n"
    "v_mfma_f32_16x16x32_bf16 v[200:203], a[164:167], a[0:3], v[200:203]\n"
    "v_mfma_f32_16x16x32_bf16 v[208:211], a[164:167], a[32:35], 0\n"
    "v_mfma_f32_16x16x32_bf16 v[208:211], a[160:163], a[60:63], v[208:211]\n"
    "v_mfma_f32_16x16x32_bf16 v[216:219], a[160:163], a[92:95], 0\n"
    "v_mfma_f32_16x16x32_bf16 v[216:219], a[164:167], a[64:67], v[216:219]\n"
    "s_waitcnt lgkmcnt(0)\n"
    "v_mfma_f32_16x16x32_bf16 v[224:227], a[164:167], a[96:99], 0\n"
    "s_barrier\n"
    "v_mfma_f32_16x16x32_bf16 v[224:227], a[160:163], a[124:127], v[224:227]\n"
    "s_add_u32 m0, s43, 49152\n"
    "ds_read2st64_b32 v[164:165], v250 offset0:0 offset1:1\n"
    "v_mfma_f32_16x16x32_bf16 v[232:235], a[160:163], a[156:159], 0\n"
    "ds_read_b128 a[160:163], v243 offset:22528\n"
    "v_mfma_f32_16x16x32_bf16 v[232:235], a[164:167], a[128:131], v[232:235]\n"
    "ds_read_b128 a[164:167], v240 offset:18432\n"
    "v_mfma_f32_16x16x32_bf16 v[232:235], a[168:171], a[132:135], v[232:235]\n"
    "buffer_load_dwordx4 v248, s[8:11], s40 offen offset:0 sc1 lds\n"
    "v_mfma_f32_16x16x32_bf16 v[232:235], a[172:175], a[136:139], v[232:235]\n"
    "v_mfma_f32_16x16x32_bf16 v[224:227], a[172:175], a[104:107], v[224:227]\n"
    "v_mfma_f32_16x16x32_bf16 v[224:227], a[168:171], a[100:103], v[224:227]\n"
    "ds_read2st64_b32 v[166:167], v250 offset0:2 offset1:3\n"
    "v_mfma_f32_16x16x32_bf16 v[216:219], a[168:171], a[68:71], v[216:219]\n"
    "buffer_load_dwordx4 v248, s[8:11], s40 offen offset:1024 sc1 lds\n"
    "v_mfma_f32_16x16x32_bf16 v[216:219], a[172:175], a[72:75], v[216:219]\n"
    "v_mfma_f32_16x16x32_bf16 v[208:211], a[172:175], a[40:43], v[208:211]\n"
    "v_mfma_f32_16x16x32_bf16 v[208:211], a[168:171], a[36:39], v[208:211]\n"
    "ds_read2st64_b32 v[172:173], v250 offset0:16 offset1:17\n"
    "v_mfma_f32_16x16x32_bf16 v[200:203], a[168:171], a[4:7], v[200:203]\n"
    "ds_read_b128 a[168:171], v241 offset:18432\n"
    "buffer_load_dwordx4 v248, s[8:11], s40 offen offset:2048 sc1 lds\n"
    "v_mfma_f32_16x16x32_bf16 v[200:203], a[172:175], a[8:11], v[200:203]\n"
    "ds_read_b128 a[172:175], v240 offset:22528\n"
    "v_mfma_f32_16x16x32_bf16 v[200:203], a[176:179], a[12:15], v[200:203]\n"
    "ds_read2st64_b32 v[174:175], v250 offset0:18 offset1:19\n"
    "v_mfma_f32_16x16x32_bf16 v[200:203], a[180:183], a[16:19], v[200:203]\n"
    "v_mfma_f32_16x16x32_bf16 v[208:211], a[180:183], a[48:51], v[208:211]\n"
    "buffer_load_dwordx4 v248, s[8:11], s40 offen offset:3072 sc1 lds\n"
    "v_mfma_f32_16x16x32_bf16 v[208:211], a[176:179], a[44:47], v[208:211]\n"
    "s_add_u32 m0, s43, 53248\n"
    "v_mfma_f32_16x16x32_bf16 v[216:219], a[176:179], a[76:79], v[216:219]\n"
    "s_add_u32 s40, s40, 0x8000\n"
    "ds_read2st64_b32 v[180:181], v250 offset0:32 offset1:33\n"
    "v_mfma_f32_16x16x32_bf16 v[216:219], a[180:183], a[80:83], v[216:219]\n"
    "v_mfma_f32_16x16x32_bf16 v[224:227], a[180:183], a[112:115], v[224:227]\n"
    "buffer_load_dwordx4 v248, s[8:11], s41 offen offset:0 sc1 lds\n"
    "v_mfma_f32_16x16x32_bf16 v[224:227], a[176:179], a[108:111], v[224:227]\n"
    "ds_read2st64_b32 v[182:183], v250 offset0:34 offset1:35\n"
    "v_mfma_f32_16x16x32_bf16 v[232:235], a[176:179], a[140:143], v[232:235]\n"
    "ds_read_b128 a[176:179], v241 offset:22528\n"
    "v_mfma_f32_16x16x32_bf16 v[232:235], a[180:183], a[144:147], v[232:235]\n"
    "ds_read_b128 a[180:183], v242 offset:18432\n"
    "v_mfma_f32_16x16x32_bf16 v[232:235], a[184:187], a[148:151], v[232:235]\n"
    "buffer_load_dwordx4 v248, s[8:11], s41 offen offset:1024 sc1 lds\n"
    "v_mfma_f32_16x16x32_bf16 v[232:235], a[188:191], a[152:155], v[232:235]\n"
    "v_mfma_f32_16x16x32_bf16 v[224:227], a[188:191], a[120:123], v[224:227]\n"
    "ds_read2st64_b32 v[188:189], v250 offset0:48 offset1:49\n"
    "v_mfma_f32_16x16x32_bf16 v[224:227], a[184:187], a[116:119], v[224:227]\n"
    "v_mfma_f32_16x16x32_bf16 v[216:219], a[184:187], a[84:87], v[216:219]\n"
    "buffer_load_dwordx4 v248, s[8:11], s41 offen offset:2048 sc1 lds\n"
    "v_mfma_f32_16x16x32_bf16 v[216:219], a[188:191], a[88:91], v[216:219]\n"
    "ds_read2st64_b32 v[190:191], v250 offset0:50 offset1:51\n"
    "v_mfma_f32_16x16x32_bf16 v[208:211], a[188:191], a[56:59], v[208:211]\n"
    "ds_read_b64 v[254:255], v252 offset:0\n"
    "v_mfma_f32_16x16x32_bf16 v[208:211], a[184:187], a[52:55], v[208:211]\n"
    "ds_read2st64_b32 v[196:197], v250 offset0:64 offset1:65\n"
    "v_mfma_f32_16x16x32_bf16 v[200:203], a[184:187], a[20:23], v[200:203]\n"
    "ds_read_b128 a[184:187], v243 offset:18432\n"
    "buffer_load_dwordx4 v248, s[8:11], s41 offen offset:3072 sc1 lds\n"
    "v_mfma_f32_16x16x32_bf16 v[200:203], a[188:191], a[24:27], v[200:203]\n"
    "s_waitcnt lgkmcnt(15)\n"
    "s_add_u32 s41, s41, 0x8000\n"
    "ds_read_b128 a[188:191], v242 offset:22528\n"
    "v_mfma_f32_16x16x32_bf16 v[204:207], a[160:163], a[28:31], 0\n"
    "s_waitcnt lgkmcnt(3)\n"
    "v_mad_u32_u16 v253, v254, s42, v251 op_sel:[0,0,0,0]\n"
    "v_mfma_f32_16x16x32_bf16 v[204:207], a[164:167], a[0:3], v[204:207]\n"
    "buffer_load_dwordx4 a[200:203], v253, s[20:23], 0 offen offset:0 sc1\n"
    "v_mfma_f32_16x16x32_bf16 v[212:215], a[164:167], a[32:35], 0\n"
    "v_exp_f32_e32 v232, v232\n"
    "v_mfma_f32_16x16x32_bf16 v[212:215], a[160:163], a[60:63], v[212:215]\n"
    "ds_read2st64_b32 v[198:199], v250 offset0:66 offset1:67\n"
    "v_mfma_f32_16x16x32_bf16 v[220:223], a[160:163], a[92:95], 0\n"
    "v_mad_u32_u16 v253, v254, s42, v251 op_sel:[1,0,0,0]\n"
    "v_mfma_f32_16x16x32_bf16 v[220:223], a[164:167], a[64:67], v[220:223]\n"
    "buffer_load_dwordx4 a[204:207], v253, s[20:23], 0 offen offset:0 sc1\n"
    "v_mfma_f32_16x16x32_bf16 v[228:231], a[164:167], a[96:99], 0\n"
    "v_exp_f32_e32 v233, v233\n"
    "v_mfma_f32_16x16x32_bf16 v[228:231], a[160:163], a[124:127], v[228:231]\n"
    "v_exp_f32_e32 v234, v234\n"
    "v_mfma_f32_16x16x32_bf16 v[236:239], a[160:163], a[156:159], 0\n"
    "ds_read_b64_tr_b16 a[160:161], v244 offset:0\n"
    "v_mad_u32_u16 v253, v255, s42, v251 op_sel:[0,0,0,0]\n"
    "v_mfma_f32_16x16x32_bf16 v[236:239], a[164:167], a[128:131], v[236:239]\n"
    "ds_read_b64_tr_b16 a[162:163], v244 offset:2048\n"
    "buffer_load_dwordx4 a[208:211], v253, s[20:23], 0 offen offset:0 sc1\n"
    "v_mfma_f32_16x16x32_bf16 v[236:239], a[168:171], a[132:135], v[236:239]\n"
    "ds_read_b64_tr_b16 a[164:165], v244 offset:8192\n"
    "v_mfma_f32_16x16x32_bf16 v[236:239], a[172:175], a[136:139], v[236:239]\n"
    "ds_read_b64_tr_b16 a[166:167], v244 offset:10240\n"
    "v_mfma_f32_16x16x32_bf16 v[228:231], a[172:175], a[104:107], v[228:231]\n"
    "v_mad_u32_u16 v253, v255, s42, v251 op_sel:[1,0,0,0]\n"
    "v_mfma_f32_16x16x32_bf16 v[228:231], a[168:171], a[100:103], v[228:231]\n"
    "buffer_load_dwordx4 a[212:215], v253, s[20:23], 0 offen offset:0 sc1\n"
    "ds_read_b64 v[254:255], v252 offset:8\n"
    "v_mfma_f32_16x16x32_bf16 v[220:223], a[168:171], a[68:71], v[220:223]\n"
    "v_exp_f32_e32 v235, v235\n"
    "v_mfma_f32_16x16x32_bf16 v[220:223], a[172:175], a[72:75], v[220:223]\n"
    "v_cvt_pk_bf16_f32 v232, v232, v233\n"
    "v_mfma_f32_16x16x32_bf16 v[212:215], a[172:175], a[40:43], v[212:215]\n"
    "v_exp_f32_e32 v224, v224\n"
    "v_mfma_f32_16x16x32_bf16 v[212:215], a[168:171], a[36:39], v[212:215]\n"
    "v_exp_f32_e32 v225, v225\n"
    "v_mfma_f32_16x16x32_bf16 v[204:207], a[168:171], a[4:7], v[204:207]\n"
    "ds_read_b64_tr_b16 a[168:169], v245 offset:0\n"
    "v_cvt_pk_bf16_f32 v233, v234, v235\n"
    "v_mfma_f32_16x16x32_bf16 v[204:207], a[172:175], a[8:11], v[204:207]\n"
    "ds_read_b64_tr_b16 a[170:171], v245 offset:2048\n"
    "v_mfma_f32_16x16x32_bf16 v[204:207], a[176:179], a[12:15], v[204:207]\n"
    "s_waitcnt lgkmcnt(2)\n"
    "ds_read_b64_tr_b16 a[172:173], v245 offset:8192\n"
    "v_mad_u32_u16 v253, v254, s42, v251 op_sel:[0,0,0,0]\n"
    "v_mfma_f32_16x16x32_bf16 v[204:207], a[180:183], a[16:19], v[204:207]\n"
    "ds_read_b64_tr_b16 a[174:175], v245 offset:10240\n"
    "buffer_load_dwordx4 a[216:219], v253, s[20:23], 0 offen offset:0 sc1\n"
    "v_mfma_f32_16x16x32_bf16 v[212:215], a[180:183], a[48:51], v[212:215]\n"
    "v_exp_f32_e32 v226, v226\n"
    "v_mfma_f32_16x16x32_bf16 v[212:215], a[176:179], a[44:47], v[212:215]\n"
    "v_exp_f32_e32 v227, v227\n"
    "v_mfma_f32_16x16x32_bf16 v[220:223], a[176:179], a[76:79], v[220:223]\n"
    "v_mad_u32_u16 v253, v254, s42, v251 op_sel:[1,0,0,0]\n"
    "v_cvt_pk_bf16_f32 v224, v224, v225\n"
    "v_mfma_f32_16x16x32_bf16 v[220:223], a[180:183], a[80:83], v[220:223]\n"
    "buffer_load_dwordx4 a[220:223], v253, s[20:23], 0 offen offset:0 sc1\n"
    "v_mfma_f32_16x16x32_bf16 v[228:231], a[180:183], a[112:115], v[228:231]\n"
    "v_exp_f32_e32 v216, v216\n"
    "v_mfma_f32_16x16x32_bf16 v[228:231], a[176:179], a[108:111], v[228:231]\n"
    "v_exp_f32_e32 v217, v217\n"
    "v_mfma_f32_16x16x32_bf16 v[236:239], a[176:179], a[140:143], v[236:239]\n"
    "ds_read_b64_tr_b16 a[176:177], v246 offset:0\n"
    "v_cvt_pk_bf16_f32 v225, v226, v227\n"
    "v_mfma_f32_16x16x32_bf16 v[236:239], a[180:183], a[144:147], v[236:239]\n"
    "ds_read_b64_tr_b16 a[178:179], v246 offset:2048\n"
    "v_mfma_f32_16x16x32_bf16 v[236:239], a[184:187], a[148:151], v[236:239]\n"
    "ds_read_b64_tr_b16 a[180:181], v246 offset:8192\n"
    "v_mfma_f32_16x16x32_bf16 v[236:239], a[188:191], a[152:155], v[236:239]\n"
    "ds_read_b64_tr_b16 a[182:183], v246 offset:10240\n"
    "v_mfma_f32_16x16x32_bf16 v[228:231], a[188:191], a[120:123], v[228:231]\n"
    "ds_read_b64 v[254:255], v252 offset:32\n"
    "v_mfma_f32_16x16x32_bf16 v[228:231], a[184:187], a[116:119], v[228:231]\n"
    "v_exp_f32_e32 v218, v218\n"
    "v_mfma_f32_16x16x32_bf16 v[220:223], a[184:187], a[84:87], v[220:223]\n"
    "v_exp_f32_e32 v219, v219\n"
    "v_mfma_f32_16x16x32_bf16 v[220:223], a[188:191], a[88:91], v[220:223]\n"
    "v_cvt_pk_bf16_f32 v216, v216, v217\n"
    "v_mfma_f32_16x16x32_bf16 v[212:215], a[188:191], a[56:59], v[212:215]\n"
    "s_waitcnt lgkmcnt(0)\n"
    "v_mad_u32_u16 v253, v254, s42, v251 op_sel:[0,0,0,0]\n"
    "v_mfma_f32_16x16x32_bf16 v[212:215], a[184:187], a[52:55], v[212:215]\n"
    "buffer_load_dwordx4 a[224:227], v253, s[20:23], 0 offen offset:0 sc1\n"
    "v_mfma_f32_16x16x32_bf16 v[204:207], a[184:187], a[20:23], v[204:207]\n"
    "ds_read_b64_tr_b16 a[184:185], v247 offset:0\n"
    "v_mfma_f32_16x16x32_bf16 v[204:207], a[188:191], a[24:27], v[204:207]\n"
    "ds_read_b64_tr_b16 a[186:187], v247 offset:2048\n"
    "v_mfma_f32_16x16x32_bf16 v[0:3], a[160:163], v[160:163], v[0:3]\n"
    "ds_read_b64_tr_b16 a[188:189], v247 offset:8192\n"
    "v_mad_u32_u16 v253, v254, s42, v251 op_sel:[1,0,0,0]\n"
    "v_mfma_f32_16x16x32_bf16 v[0:3], a[164:167], v[164:167], v[0:3]\n"
    "ds_read_b64_tr_b16 a[190:191], v247 offset:10240\n"
    "buffer_load_dwordx4 a[228:231], v253, s[20:23], 0 offen offset:0 sc1\n"
    "v_mfma_f32_16x16x32_bf16 a[248:251], a[164:167], v[172:175], a[248:251]\n"
    "v_exp_f32_e32 v208, v208\n"
    "v_mfma_f32_16x16x32_bf16 a[248:251], a[160:163], v[168:171], a[248:251]\n"
    "v_exp_f32_e32 v209, v209\n"
    "v_mfma_f32_16x16x32_bf16 v[56:59], a[160:163], v[176:179], v[56:59]\n"
    "v_mad_u32_u16 v253, v255, s42, v251 op_sel:[0,0,0,0]\n"
    "v_cvt_pk_bf16_f32 v217, v218, v219\n"
    "v_mfma_f32_16x16x32_bf16 v[56:59], a[164:167], v[180:183], v[56:59]\n"
    "buffer_load_dwordx4 a[232:235], v253, s[20:23], 0 offen offset:0 sc1\n"
    "v_mfma_f32_16x16x32_bf16 v[60:63], a[164:167], v[188:191], v[60:63]\n"
    "v_exp_f32_e32 v210, v210\n"
    "v_mfma_f32_16x16x32_bf16 v[60:63], a[160:163], v[184:187], v[60:63]\n"
    "v_exp_f32_e32 v211, v211\n"
    "v_mfma_f32_16x16x32_bf16 v[64:67], a[160:163], v[192:195], v[64:67]\n"
    "ds_read_b64_tr_b16 a[160:161], v244 offset:4096\n"
    "v_mad_u32_u16 v253, v255, s42, v251 op_sel:[1,0,0,0]\n"
    "v_mfma_f32_16x16x32_bf16 v[64:67], a[164:167], v[196:199], v[64:67]\n"
    "ds_read_b64_tr_b16 a[162:163], v244 offset:6144\n"
    "buffer_load_dwordx4 a[236:239], v253, s[20:23], 0 offen offset:0 sc1\n"
    "v_mfma_f32_16x16x32_bf16 v[76:79], a[168:171], v[192:195], v[76:79]\n"
    "ds_read_b64_tr_b16 a[164:165], v244 offset:12288\n"
    "v_cvt_pk_bf16_f32 v208, v208, v209\n"
    "v_mfma_f32_16x16x32_bf16 v[76:79], a[172:175], v[196:199], v[76:79]\n"
    "ds_read_b64_tr_b16 a[166:167], v244 offset:14336\n"
    "v_mfma_f32_16x16x32_bf16 v[72:75], a[172:175], v[188:191], v[72:75]\n"
    "ds_read_b64 v[254:255], v252 offset:40\n"
    "v_mfma_f32_16x16x32_bf16 v[72:75], a[168:171], v[184:187], v[72:75]\n"
    "v_exp_f32_e32 v200, v200\n"
    "v_mfma_f32_16x16x32_bf16 v[68:71], a[168:171], v[176:179], v[68:71]\n"
    "v_exp_f32_e32 v201, v201\n"
    "v_mfma_f32_16x16x32_bf16 v[68:71], a[172:175], v[180:183], v[68:71]\n"
    "v_cvt_pk_bf16_f32 v209, v210, v211\n"
    "v_mfma_f32_16x16x32_bf16 a[252:255], a[172:175], v[172:175], a[252:255]\n"
    "v_exp_f32_e32 v202, v202\n"
    "v_mfma_f32_16x16x32_bf16 a[252:255], a[168:171], v[168:171], a[252:255]\n"
    "v_exp_f32_e32 v203, v203\n"
    "v_mfma_f32_16x16x32_bf16 v[4:7], a[168:171], v[160:163], v[4:7]\n"
    "s_waitcnt lgkmcnt(0)\n"
    "ds_read_b64_tr_b16 a[168:169], v245 offset:4096\n"
    "v_mad_u32_u16 v253, v254, s42, v251 op_sel:[0,0,0,0]\n"
    "v_mfma_f32_16x16x32_bf16 v[4:7], a[172:175], v[164:167], v[4:7]\n"
    "ds_read_b64_tr_b16 a[170:171], v245 offset:6144\n"
    "v_cvt_pk_bf16_f32 v200, v200, v201\n"
    "v_mfma_f32_16x16x32_bf16 v[8:11], a[176:179], v[160:163], v[8:11]\n"
    "ds_read_b64_tr_b16 a[172:173], v245 offset:12288\n"
    "buffer_load_dwordx4 a[240:243], v253, s[20:23], 0 offen offset:0 sc1\n"
    "v_mfma_f32_16x16x32_bf16 v[8:11], a[180:183], v[164:167], v[8:11]\n"
    "ds_read_b64_tr_b16 a[174:175], v245 offset:14336\n"
    "v_cvt_pk_bf16_f32 v201, v202, v203\n"
    "v_mfma_f32_16x16x32_bf16 v[32:35], a[180:183], v[172:175], v[32:35]\n"
    "v_exp_f32_e32 v236, v236\n"
    "v_mfma_f32_16x16x32_bf16 v[32:35], a[176:179], v[168:171], v[32:35]\n"
    "v_mad_u32_u16 v253, v254, s42, v251 op_sel:[1,0,0,0]\n"
    "v_mfma_f32_16x16x32_bf16 v[80:83], a[176:179], v[176:179], v[80:83]\n"
    "buffer_load_dwordx4 a[244:247], v253, s[20:23], 0 offen offset:0 sc1\n"
    "v_mfma_f32_16x16x32_bf16 v[80:83], a[180:183], v[180:183], v[80:83]\n"
    "v_exp_f32_e32 v237, v237\n"
    "v_mfma_f32_16x16x32_bf16 v[84:87], a[180:183], v[188:191], v[84:87]\n"
    "v_exp_f32_e32 v238, v238\n"
    "v_mfma_f32_16x16x32_bf16 v[84:87], a[176:179], v[184:187], v[84:87]\n"
    "v_exp_f32_e32 v239, v239\n"
    "v_mfma_f32_16x16x32_bf16 v[88:91], a[176:179], v[192:195], v[88:91]\n"
    "ds_read_b64_tr_b16 a[176:177], v246 offset:4096\n"
    "v_cvt_pk_bf16_f32 v234, v236, v237\n"
    "v_mfma_f32_16x16x32_bf16 v[88:91], a[180:183], v[196:199], v[88:91]\n"
    "ds_read_b64_tr_b16 a[178:179], v246 offset:6144\n"
    "v_exp_f32_e32 v228, v228\n"
    "v_mfma_f32_16x16x32_bf16 v[100:103], a[184:187], v[192:195], v[100:103]\n"
    "ds_read_b64_tr_b16 a[180:181], v246 offset:12288\n"
    "v_exp_f32_e32 v229, v229\n"
    "v_mfma_f32_16x16x32_bf16 v[100:103], a[188:191], v[196:199], v[100:103]\n"
    "ds_read_b64_tr_b16 a[182:183], v246 offset:14336\n"
    "v_cvt_pk_bf16_f32 v235, v238, v239\n"
    "v_mfma_f32_16x16x32_bf16 v[96:99], a[188:191], v[188:191], v[96:99]\n"
    "v_exp_f32_e32 v230, v230\n"
    "v_mfma_f32_16x16x32_bf16 v[96:99], a[184:187], v[184:187], v[96:99]\n"
    "v_exp_f32_e32 v231, v231\n"
    "v_cvt_pk_bf16_f32 v226, v228, v229\n"
    "v_mfma_f32_16x16x32_bf16 v[92:95], a[184:187], v[176:179], v[92:95]\n"
    "v_exp_f32_e32 v220, v220\n"
    "v_mfma_f32_16x16x32_bf16 v[92:95], a[188:191], v[180:183], v[92:95]\n"
    "v_exp_f32_e32 v221, v221\n"
    "v_mfma_f32_16x16x32_bf16 v[36:39], a[188:191], v[172:175], v[36:39]\n"
    "v_cvt_pk_bf16_f32 v227, v230, v231\n"
    "v_mfma_f32_16x16x32_bf16 v[36:39], a[184:187], v[168:171], v[36:39]\n"
    "v_exp_f32_e32 v222, v222\n"
    "v_mfma_f32_16x16x32_bf16 v[12:15], a[184:187], v[160:163], v[12:15]\n"
    "s_mov_b32 m0, s49\n"
    "ds_read_b64_tr_b16 a[184:185], v247 offset:4096\n"
    "v_mfma_f32_16x16x32_bf16 v[12:15], a[188:191], v[164:167], v[12:15]\n"
    "s_waitcnt lgkmcnt(5)\n"
    "ds_read_b64_tr_b16 a[186:187], v247 offset:6144\n"
    "v_mfma_f32_16x16x32_bf16 v[16:19], a[160:163], v[160:163], v[16:19]\n"
    "ds_read_b64_tr_b16 a[188:189], v247 offset:12288\n"
    "v_mfma_f32_16x16x32_bf16 v[16:19], a[164:167], v[164:167], v[16:19]\n"
    "ds_read_b64_tr_b16 a[190:191], v247 offset:14336\n"
    "v_mfma_f32_16x16x32_bf16 v[40:43], a[164:167], v[172:175], v[40:43]\n"
    "v_exp_f32_e32 v223, v223\n"
    "v_mfma_f32_16x16x32_bf16 v[40:43], a[160:163], v[168:171], v[40:43]\n"
    "v_cvt_pk_bf16_f32 v218, v220, v221\n"
    "v_mfma_f32_16x16x32_bf16 v[104:107], a[160:163], v[176:179], v[104:107]\n"
    "v_exp_f32_e32 v212, v212\n"
    "v_mfma_f32_16x16x32_bf16 v[104:107], a[164:167], v[180:183], v[104:107]\n"
    "s_waitcnt vmcnt(20)\n"
    "v_mfma_f32_16x16x32_bf16 v[108:111], a[164:167], v[188:191], v[108:111]\n"
    "s_barrier\n"
    "v_mfma_f32_16x16x32_bf16 v[108:111], a[160:163], v[184:187], v[108:111]\n"
    "v_exp_f32_e32 v213, v213\n"
    "v_mfma_f32_16x16x32_bf16 v[112:115], a[160:163], v[192:195], v[112:115]\n"
    "ds_read_b128 a[160:163], v243 offset:36864\n"
    "v_cvt_pk_bf16_f32 v219, v222, v223\n"
    "v_mfma_f32_16x16x32_bf16 v[112:115], a[164:167], v[196:199], v[112:115]\n"
    "ds_read_b128 a[164:167], v240 offset:32768\n"
    "ds_write_addtid_b32 v232 offset:16384\n"
    "v_mfma_f32_16x16x32_bf16 v[124:127], a[168:171], v[192:195], v[124:127]\n"
    "v_exp_f32_e32 v214, v214\n"
    "v_mfma_f32_16x16x32_bf16 v[124:127], a[172:175], v[196:199], v[124:127]\n"
    "v_exp_f32_e32 v215, v215\n"
    "v_mfma_f32_16x16x32_bf16 v[120:123], a[172:175], v[188:191], v[120:123]\n"
    "ds_write_addtid_b32 v233 offset:16640\n"
    "v_cvt_pk_bf16_f32 v210, v212, v213\n"
    "v_mfma_f32_16x16x32_bf16 v[120:123], a[168:171], v[184:187], v[120:123]\n"
    "v_exp_f32_e32 v204, v204\n"
    "v_mfma_f32_16x16x32_bf16 v[116:119], a[168:171], v[176:179], v[116:119]\n"
    "v_exp_f32_e32 v205, v205\n"
    "v_mfma_f32_16x16x32_bf16 v[116:119], a[172:175], v[180:183], v[116:119]\n"
    "ds_write_addtid_b32 v224 offset:12288\n"
    "v_cvt_pk_bf16_f32 v211, v214, v215\n"
    "v_mfma_f32_16x16x32_bf16 v[44:47], a[172:175], v[172:175], v[44:47]\n"
    "v_exp_f32_e32 v206, v206\n"
    "v_mfma_f32_16x16x32_bf16 v[44:47], a[168:171], v[168:171], v[44:47]\n"
    "v_exp_f32_e32 v207, v207\n"
    "v_mfma_f32_16x16x32_bf16 v[20:23], a[168:171], v[160:163], v[20:23]\n"
    "ds_read_b128 a[168:171], v241 offset:32768\n"
    "v_cvt_pk_bf16_f32 v202, v204, v205\n"
    "v_mfma_f32_16x16x32_bf16 v[20:23], a[172:175], v[164:167], v[20:23]\n"
    "s_waitcnt lgkmcnt(6)\n"
    "ds_read_b128 a[172:175], v240 offset:36864\n"
    "v_cvt_pk_bf16_f32 v203, v206, v207\n"
    "v_mfma_f32_16x16x32_bf16 v[24:27], a[176:179], v[160:163], v[24:27]\n"
    "ds_write_addtid_b32 v225 offset:12544\n"
    "ds_write_addtid_b32 v216 offset:8192\n"
    "v_mfma_f32_16x16x32_bf16 v[24:27], a[180:183], v[164:167], v[24:27]\n"
    "ds_write_addtid_b32 v217 offset:8448\n"
    "v_mfma_f32_16x16x32_bf16 v[48:51], a[180:183], v[172:175], v[48:51]\n"
    "ds_write_addtid_b32 v208 offset:4096\n"
    "v_mfma_f32_16x16x32_bf16 v[48:51], a[176:179], v[168:171], v[48:51]\n"
    "ds_write_addtid_b32 v209 offset:4352\n"
    "v_mfma_f32_16x16x32_bf16 v[128:131], a[176:179], v[176:179], v[128:131]\n"
    "ds_write_addtid_b32 v200 offset:0\n"
    "v_mfma_f32_16x16x32_bf16 v[128:131], a[180:183], v[180:183], v[128:131]\n"
    "ds_write_addtid_b32 v201 offset:256\n"
    "v_mfma_f32_16x16x32_bf16 v[132:135], a[180:183], v[188:191], v[132:135]\n"
    "ds_write_addtid_b32 v234 offset:16896\n"
    "v_mfma_f32_16x16x32_bf16 v[132:135], a[176:179], v[184:187], v[132:135]\n"
    "ds_write_addtid_b32 v235 offset:17152\n"
    "v_mfma_f32_16x16x32_bf16 v[136:139], a[176:179], v[192:195], v[136:139]\n"
    "ds_read_b128 a[176:179], v241 offset:36864\n"
    "v_mfma_f32_16x16x32_bf16 v[136:139], a[180:183], v[196:199], v[136:139]\n"
    "ds_read_b128 a[180:183], v242 offset:32768\n"
    "v_mfma_f32_16x16x32_bf16 v[148:151], a[184:187], v[192:195], v[148:151]\n"
    "ds_write_addtid_b32 v226 offset:12800\n"
    "v_mfma_f32_16x16x32_bf16 v[148:151], a[188:191], v[196:199], v[148:151]\n"
    "ds_write_addtid_b32 v227 offset:13056\n"
    "v_mfma_f32_16x16x32_bf16 v[144:147], a[188:191], v[188:191], v[144:147]\n"
    "ds_write_addtid_b32 v218 offset:8704\n"
    "v_mfma_f32_16x16x32_bf16 v[144:147], a[184:187], v[184:187], v[144:147]\n"
    "ds_write_addtid_b32 v219 offset:8960\n"
    "v_mfma_f32_16x16x32_bf16 v[140:143], a[184:187], v[176:179], v[140:143]\n"
    "ds_write_addtid_b32 v210 offset:4608\n"
    "v_mfma_f32_16x16x32_bf16 v[140:143], a[188:191], v[180:183], v[140:143]\n"
    "ds_write_addtid_b32 v211 offset:4864\n"
    "v_mfma_f32_16x16x32_bf16 v[52:55], a[188:191], v[172:175], v[52:55]\n"
    "ds_write_addtid_b32 v202 offset:512\n"
    "v_mfma_f32_16x16x32_bf16 v[52:55], a[184:187], v[168:171], v[52:55]\n"
    "ds_write_addtid_b32 v203 offset:768\n"
    "v_mfma_f32_16x16x32_bf16 v[28:31], a[184:187], v[160:163], v[28:31]\n"
    "ds_read_b128 a[184:187], v243 offset:32768\n"
    "v_mfma_f32_16x16x32_bf16 v[28:31], a[188:191], v[164:167], v[28:31]\n"
    "s_waitcnt lgkmcnt(15)\n"
    "ds_read_b128 a[188:191], v242 offset:36864\n"
    "v_mfma_f32_16x16x32_bf16 v[160:163], a[160:163], a[28:31], 0\n"
    "v_mfma_f32_16x16x32_bf16 v[160:163], a[164:167], a[0:3], v[160:163]\n"
    "v_mfma_f32_16x16x32_bf16 v[168:171], a[164:167], a[32:35], 0\n"
    "v_mfma_f32_16x16x32_bf16 v[168:171], a[160:163], a[60:63], v[168:171]\n"
    "v_mfma_f32_16x16x32_bf16 v[176:179], a[160:163], a[92:95], 0\n"
    "v_mfma_f32_16x16x32_bf16 v[176:179], a[164:167], a[64:67], v[176:179]\n"
    "s_waitcnt lgkmcnt(0)\n"
    "v_mfma_f32_16x16x32_bf16 v[184:187], a[164:167], a[96:99], 0\n"
    "s_barrier\n"
    "v_mfma_f32_16x16x32_bf16 v[184:187], a[160:163], a[124:127], v[184:187]\n"
    "s_add_u32 m0, s43, 0\n"
    "v_mfma_f32_16x16x32_bf16 v[192:195], a[160:163], a[156:159], 0\n"
    "ds_read_b128 a[160:163], v243 offset:38912\n"
    "v_mfma_f32_16x16x32_bf16 v[192:195], a[164:167], a[128:131], v[192:195]\n"
    "ds_read_b128 a[164:167], v240 offset:34816\n"
    "v_mfma_f32_16x16x32_bf16 v[192:195], a[168:171], a[132:135], v[192:195]\n"
    "buffer_load_dwordx4 v248, s[8:11], s40 offen offset:0 sc1 lds\n"
    "v_mfma_f32_16x16x32_bf16 v[192:195], a[172:175], a[136:139], v[192:195]\n"
    "ds_read2st64_b32 v[204:205], v250 offset0:0 offset1:1\n"
    "v_mfma_f32_16x16x32_bf16 v[184:187], a[172:175], a[104:107], v[184:187]\n"
    "s_waitcnt vmcnt(11)\n"
    "v_smfmac_f32_16x16x64_bf16 v[156:159], a[192:195], a[200:207], v152\n"
    "v_mfma_f32_16x16x32_bf16 v[184:187], a[168:171], a[100:103], v[184:187]\n"
    "ds_read2st64_b32 v[206:207], v250 offset0:2 offset1:3\n"
    "v_mfma_f32_16x16x32_bf16 v[176:179], a[168:171], a[68:71], v[176:179]\n"
    "buffer_load_dwordx4 v248, s[8:11], s40 offen offset:1024 sc1 lds\n"
    "v_mfma_f32_16x16x32_bf16 v[176:179], a[172:175], a[72:75], v[176:179]\n"
    "v_mfma_f32_16x16x32_bf16 v[168:171], a[172:175], a[40:43], v[168:171]\n"
    "s_waitcnt vmcnt(10)\n"
    "v_smfmac_f32_16x16x64_bf16 v[156:159], a[192:195], a[208:215], v152\n"
    "ds_read2st64_b32 v[212:213], v250 offset0:16 offset1:17\n"
    "v_mfma_f32_16x16x32_bf16 v[168:171], a[168:171], a[36:39], v[168:171]\n"
    "v_mfma_f32_16x16x32_bf16 v[160:163], a[168:171], a[4:7], v[160:163]\n"
    "ds_read_b128 a[168:171], v241 offset:34816\n"
    "buffer_load_dwordx4 v248, s[8:11], s40 offen offset:2048 sc1 lds\n"
    "v_mfma_f32_16x16x32_bf16 v[160:163], a[172:175], a[8:11], v[160:163]\n"
    "ds_read_b128 a[172:175], v240 offset:38912\n"
    "v_mfma_f32_16x16x32_bf16 v[160:163], a[176:179], a[12:15], v[160:163]\n"
    "s_waitcnt vmcnt(9)\n"
    "ds_read2st64_b32 v[214:215], v250 offset0:18 offset1:19\n"
    "v_smfmac_f32_16x16x64_bf16 v[156:159], a[192:195], a[216:223], v152\n"
    "v_mfma_f32_16x16x32_bf16 v[160:163], a[180:183], a[16:19], v[160:163]\n"
    "v_mfma_f32_16x16x32_bf16 v[168:171], a[180:183], a[48:51], v[168:171]\n"
    "buffer_load_dwordx4 v248, s[8:11], s40 offen offset:3072 sc1 lds\n"
    "v_mfma_f32_16x16x32_bf16 v[168:171], a[176:179], a[44:47], v[168:171]\n"
    "s_add_u32 m0, s43, 4096\n"
    "v_mfma_f32_16x16x32_bf16 v[176:179], a[176:179], a[76:79], v[176:179]\n"
    "s_add_u32 s40, s40, 0x8000\n"
    "ds_read2st64_b32 v[220:221], v250 offset0:32 offset1:33\n"
    "v_mfma_f32_16x16x32_bf16 v[176:179], a[180:183], a[80:83], v[176:179]\n"
    "v_mfma_f32_16x16x32_bf16 v[184:187], a[180:183], a[112:115], v[184:187]\n"
    "v_mfma_f32_16x16x32_bf16 v[184:187], a[176:179], a[108:111], v[184:187]\n"
    "buffer_load_dwordx4 v248, s[8:11], s41 offen offset:0 sc1 lds\n"
    "v_mfma_f32_16x16x32_bf16 v[192:195], a[176:179], a[140:143], v[192:195]\n"
    "ds_read_b128 a[176:179], v241 offset:38912\n"
    "v_mfma_f32_16x16x32_bf16 v[192:195], a[180:183], a[144:147], v[192:195]\n"
    "ds_read_b128 a[180:183], v242 offset:34816\n"
    "v_mfma_f32_16x16x32_bf16 v[192:195], a[184:187], a[148:151], v[192:195]\n"
    "ds_read2st64_b32 v[222:223], v250 offset0:34 offset1:35\n"
    "v_mfma_f32_16x16x32_bf16 v[192:195], a[188:191], a[152:155], v[192:195]\n"
    "v_mfma_f32_16x16x32_bf16 v[184:187], a[188:191], a[120:123], v[184:187]\n"
    "buffer_load_dwordx4 v248, s[8:11], s41 offen offset:1024 sc1 lds\n"
    "v_mfma_f32_16x16x32_bf16 v[184:187], a[184:187], a[116:119], v[184:187]\n"
    "ds_read2st64_b32 v[228:229], v250 offset0:48 offset1:49\n"
    "v_mfma_f32_16x16x32_bf16 v[176:179], a[184:187], a[84:87], v[176:179]\n"
    "s_waitcnt vmcnt(10)\n"
    "v_exp_f32_e32 v192, v192\n"
    "v_smfmac_f32_16x16x64_bf16 v[156:159], a[192:195], a[224:231], v152\n"
    "v_exp_f32_e32 v193, v193\n"
    "v_mfma_f32_16x16x32_bf16 v[176:179], a[188:191], a[88:91], v[176:179]\n"
    "v_exp_f32_e32 v194, v194\n"
    "v_mfma_f32_16x16x32_bf16 v[168:171], a[188:191], a[56:59], v[168:171]\n"
    "buffer_load_dwordx4 v248, s[8:11], s41 offen offset:2048 sc1 lds\n"
    "v_mfma_f32_16x16x32_bf16 v[168:171], a[184:187], a[52:55], v[168:171]\n"
    "ds_read2st64_b32 v[230:231], v250 offset0:50 offset1:51\n"
    "v_mfma_f32_16x16x32_bf16 v[160:163], a[184:187], a[20:23], v[160:163]\n"
    "s_waitcnt vmcnt(9)\n"
    "ds_read_b128 a[184:187], v243 offset:34816\n"
    "v_smfmac_f32_16x16x64_bf16 v[156:159], a[192:195], a[232:239], v152\n"
    "ds_read2st64_b32 v[236:237], v250 offset0:64 offset1:65\n"
    "v_mfma_f32_16x16x32_bf16 v[160:163], a[188:191], a[24:27], v[160:163]\n"
    "s_waitcnt lgkmcnt(9)\n"
    "ds_read_b128 a[188:191], v242 offset:38912\n"
    "v_mfma_f32_16x16x32_bf16 v[164:167], a[160:163], a[28:31], 0\n"
    "buffer_load_dwordx4 v248, s[8:11], s41 offen offset:3072 sc1 lds\n"
    "v_mfma_f32_16x16x32_bf16 v[164:167], a[164:167], a[0:3], v[164:167]\n"
    "s_add_u32 s41, s41, 0x8000\n"
    "v_exp_f32_e32 v195, v195\n"
    "v_mfma_f32_16x16x32_bf16 v[172:175], a[164:167], a[32:35], 0\n"
    "s_waitcnt vmcnt(8)\n"
    "v_cvt_pk_bf16_f32 v192, v192, v193\n"
    "v_smfmac_f32_16x16x64_bf16 v[156:159], a[192:195], a[240:247], v152\n"
    "v_exp_f32_e32 v184, v184\n"
    "v_mfma_f32_16x16x32_bf16 v[172:175], a[160:163], a[60:63], v[172:175]\n"
    "ds_read2st64_b32 v[238:239], v250 offset0:66 offset1:67\n"
    "v_mfma_f32_16x16x32_bf16 v[180:183], a[160:163], a[92:95], 0\n"
    "v_exp_f32_e32 v185, v185\n"
    "v_mfma_f32_16x16x32_bf16 v[180:183], a[164:167], a[64:67], v[180:183]\n"
    "v_cvt_pk_bf16_f32 v193, v194, v195\n"
    "v_mfma_f32_16x16x32_bf16 v[188:191], a[164:167], a[96:99], 0\n"
    "v_exp_f32_e32 v186, v186\n"
    "v_mfma_f32_16x16x32_bf16 v[188:191], a[160:163], a[124:127], v[188:191]\n"
    "v_exp_f32_e32 v187, v187\n"
    "v_mfma_f32_16x16x32_bf16 v[196:199], a[160:163], a[156:159], 0\n"
    "ds_read_b64_tr_b16 a[160:161], v244 offset:16384\n"
    "v_and_b32_e32 v253, 0xffff0000, v255\n"
    "v_mfma_f32_16x16x32_bf16 v[196:199], a[164:167], a[128:131], v[196:199]\n"
    "ds_read_b64_tr_b16 a[162:163], v244 offset:18432\n"
    "v_mfma_f32_16x16x32_bf16 v[196:199], a[168:171], a[132:135], v[196:199]\n"
    "ds_read_b64_tr_b16 a[164:165], v244 offset:24576\n"
    "v_mfma_f32_16x16x32_bf16 v[196:199], a[172:175], a[136:139], v[196:199]\n"
    "ds_read_b64_tr_b16 a[166:167], v244 offset:26624\n"
    "v_mfma_f32_16x16x32_bf16 v[188:191], a[172:175], a[104:107], v[188:191]\n"
    "v_cvt_pk_bf16_f32 v184, v184, v185\n"
    "v_mfma_f32_16x16x32_bf16 v[188:191], a[168:171], a[100:103], v[188:191]\n"
    "v_exp_f32_e32 v176, v176\n"
    "v_mfma_f32_16x16x32_bf16 v[180:183], a[168:171], a[68:71], v[180:183]\n"
    "v_exp_f32_e32 v177, v177\n"
    "v_mfma_f32_16x16x32_bf16 v[180:183], a[172:175], a[72:75], v[180:183]\n"
    "v_cvt_pk_bf16_f32 v185, v186, v187\n"
    "v_mul_f32_e32 v156, v253, v156\n"
    "v_mfma_f32_16x16x32_bf16 v[172:175], a[172:175], a[40:43], v[172:175]\n"
    "v_exp_f32_e32 v178, v178\n"
    "v_mfma_f32_16x16x32_bf16 v[172:175], a[168:171], a[36:39], v[172:175]\n"
    "v_exp_f32_e32 v179, v179\n"
    "v_mfma_f32_16x16x32_bf16 v[164:167], a[168:171], a[4:7], v[164:167]\n"
    "ds_read_b64_tr_b16 a[168:169], v245 offset:16384\n"
    "v_mfma_f32_16x16x32_bf16 v[164:167], a[172:175], a[8:11], v[164:167]\n"
    "s_waitcnt lgkmcnt(6)\n"
    "ds_read_b64_tr_b16 a[170:171], v245 offset:18432\n"
    "v_mfma_f32_16x16x32_bf16 v[164:167], a[176:179], a[12:15], v[164:167]\n"
    "ds_read_b64_tr_b16 a[172:173], v245 offset:24576\n"
    "v_mfma_f32_16x16x32_bf16 v[164:167], a[180:183], a[16:19], v[164:167]\n"
    "ds_read_b64_tr_b16 a[174:175], v245 offset:26624\n"
    "v_mfma_f32_16x16x32_bf16 v[172:175], a[180:183], a[48:51], v[172:175]\n"
    "v_cvt_pk_bf16_f32 v176, v176, v177\n"
    "v_mfma_f32_16x16x32_bf16 v[172:175], a[176:179], a[44:47], v[172:175]\n"
    "v_exp_f32_e32 v168, v168\n"
    "v_mfma_f32_16x16x32_bf16 v[180:183], a[176:179], a[76:79], v[180:183]\n"
    "v_mul_f32_e32 v157, v253, v157\n"
    "v_mfma_f32_16x16x32_bf16 v[180:183], a[180:183], a[80:83], v[180:183]\n"
    "v_exp_f32_e32 v169, v169\n"
    "v_mfma_f32_16x16x32_bf16 v[188:191], a[180:183], a[112:115], v[188:191]\n"
    "v_cvt_pk_bf16_f32 v177, v178, v179\n"
    "v_mfma_f32_16x16x32_bf16 v[188:191], a[176:179], a[108:111], v[188:191]\n"
    "v_exp_f32_e32 v170, v170\n"
    "v_mfma_f32_16x16x32_bf16 v[196:199], a[176:179], a[140:143], v[196:199]\n"
    "ds_read_b64_tr_b16 a[176:177], v246 offset:16384\n"
    "v_mul_f32_e32 v158, v253, v158\n"
    "v_mfma_f32_16x16x32_bf16 v[196:199], a[180:183], a[144:147], v[196:199]\n"
    "ds_read_b64_tr_b16 a[178:179], v246 offset:18432\n"
    "v_mfma_f32_16x16x32_bf16 v[196:199], a[184:187], a[148:151], v[196:199]\n"
    "ds_read_b64_tr_b16 a[180:181], v246 offset:24576\n"
    "v_mfma_f32_16x16x32_bf16 v[196:199], a[188:191], a[152:155], v[196:199]\n"
    "ds_read_b64_tr_b16 a[182:183], v246 offset:26624\n"
    "v_mfma_f32_16x16x32_bf16 v[188:191], a[188:191], a[120:123], v[188:191]\n"
    "v_exp_f32_e32 v171, v171\n"
    "v_mfma_f32_16x16x32_bf16 v[188:191], a[184:187], a[116:119], v[188:191]\n"
    "v_cvt_pk_bf16_f32 v168, v168, v169\n"
    "v_mul_f32_e32 v159, v253, v159\n"
    "v_mfma_f32_16x16x32_bf16 v[180:183], a[184:187], a[84:87], v[180:183]\n"
    "v_exp_f32_e32 v160, v160\n"
    "v_mfma_f32_16x16x32_bf16 v[180:183], a[188:191], a[88:91], v[180:183]\n"
    "v_exp_f32_e32 v161, v161\n"
    "v_mfma_f32_16x16x32_bf16 v[172:175], a[188:191], a[56:59], v[172:175]\n"
    "v_cvt_pk_bf16_f32 v169, v170, v171\n"
    "v_mfma_f32_16x16x32_bf16 v[172:175], a[184:187], a[52:55], v[172:175]\n"
    "v_exp_f32_e32 v162, v162\n"
    "v_mfma_f32_16x16x32_bf16 v[164:167], a[184:187], a[20:23], v[164:167]\n"
    "ds_read_b64_tr_b16 a[184:185], v247 offset:16384\n"
    "v_mfma_f32_16x16x32_bf16 v[164:167], a[188:191], a[24:27], v[164:167]\n"
    "s_waitcnt lgkmcnt(5)\n"
    "ds_read_b64_tr_b16 a[186:187], v247 offset:18432\n"
    "v_mfma_f32_16x16x32_bf16 v[0:3], a[160:163], v[200:203], v[0:3]\n"
    "ds_read_b64_tr_b16 a[188:189], v247 offset:24576\n"
    "v_mad_u32_u16 v253, v255, s42, v153 op_sel:[0,0,0,0]\n"
    "v_mfma_f32_16x16x32_bf16 v[0:3], a[164:167], v[204:207], v[0:3]\n"
    "ds_read_b64_tr_b16 a[190:191], v247 offset:26624\n"
    "v_mfma_f32_16x16x32_bf16 a[248:251], a[164:167], v[212:215], a[248:251]\n"
    "v_exp_f32_e32 v163, v163\n"
    "v_mfma_f32_16x16x32_bf16 a[248:251], a[160:163], v[208:211], a[248:251]\n"
    "v_cvt_pk_bf16_f32 v160, v160, v161\n"
    "v_mfma_f32_16x16x32_bf16 v[56:59], a[160:163], v[216:219], v[56:59]\n"
    "v_cvt_pk_bf16_f32 v154, v156, v157\n"
    "v_mfma_f32_16x16x32_bf16 v[56:59], a[164:167], v[220:223], v[56:59]\n"
    "v_cvt_pk_bf16_f32 v161, v162, v163\n"
    "v_mfma_f32_16x16x32_bf16 v[60:63], a[164:167], v[228:231], v[60:63]\n"
    "v_exp_f32_e32 v196, v196\n"
    "v_mfma_f32_16x16x32_bf16 v[60:63], a[160:163], v[224:227], v[60:63]\n"
    "v_exp_f32_e32 v197, v197\n"
    "v_mfma_f32_16x16x32_bf16 v[64:67], a[160:163], v[232:235], v[64:67]\n"
    "ds_read_b64_tr_b16 a[160:161], v244 offset:20480\n"
    "v_mfma_f32_16x16x32_bf16 v[64:67], a[164:167], v[236:239], v[64:67]\n"
    "ds_read_b64_tr_b16 a[162:163], v244 offset:22528\n"
    "v_mfma_f32_16x16x32_bf16 v[76:79], a[168:171], v[232:235], v[76:79]\n"
    "ds_read_b64_tr_b16 a[164:165], v244 offset:28672\n"
    "v_mfma_f32_16x16x32_bf16 v[76:79], a[172:175], v[236:239], v[76:79]\n"
    "ds_read_b64_tr_b16 a[166:167], v244 offset:30720\n"
    "v_mfma_f32_16x16x32_bf16 v[72:75], a[172:175], v[228:231], v[72:75]\n"
    "v_exp_f32_e32 v198, v198\n"
    "v_mfma_f32_16x16x32_bf16 v[72:75], a[168:171], v[224:227], v[72:75]\n"
    "v_exp_f32_e32 v199, v199\n"
    "v_mfma_f32_16x16x32_bf16 v[68:71], a[168:171], v[216:219], v[68:71]\n"
    "v_cvt_pk_bf16_f32 v194, v196, v197\n"
    "v_cvt_pk_bf16_f32 v155, v158, v159\n"
    "v_mfma_f32_16x16x32_bf16 v[68:71], a[172:175], v[220:223], v[68:71]\n"
    "v_exp_f32_e32 v188, v188\n"
    "v_mfma_f32_16x16x32_bf16 a[252:255], a[172:175], v[212:215], a[252:255]\n"
    "v_exp_f32_e32 v189, v189\n"
    "v_mfma_f32_16x16x32_bf16 a[252:255], a[168:171], v[208:211], a[252:255]\n"
    "v_cvt_pk_bf16_f32 v195, v198, v199\n"
    "v_mfma_f32_16x16x32_bf16 v[4:7], a[168:171], v[200:203], v[4:7]\n"
    "ds_read_b64_tr_b16 a[168:169], v245 offset:20480\n"
    "v_mfma_f32_16x16x32_bf16 v[4:7], a[172:175], v[204:207], v[4:7]\n"
    "s_waitcnt lgkmcnt(5)\n"
    "ds_read_b64_tr_b16 a[170:171], v245 offset:22528\n"
    "v_mfma_f32_16x16x32_bf16 v[8:11], a[176:179], v[200:203], v[8:11]\n"
    "ds_read_b64_tr_b16 a[172:173], v245 offset:28672\n"
    "v_mov_b32_e32 v156, 0\n"
    "v_mfma_f32_16x16x32_bf16 v[8:11], a[180:183], v[204:207], v[8:11]\n"
    "ds_read_b64_tr_b16 a[174:175], v245 offset:30720\n"
    "v_mfma_f32_16x16x32_bf16 v[32:35], a[180:183], v[212:215], v[32:35]\n"
    "v_exp_f32_e32 v190, v190\n"
    "v_mfma_f32_16x16x32_bf16 v[32:35], a[176:179], v[208:211], v[32:35]\n"
    "v_exp_f32_e32 v191, v191\n"
    "v_mfma_f32_16x16x32_bf16 v[80:83], a[176:179], v[216:219], v[80:83]\n"
    "v_mov_b32_e32 v157, 0\n"
    "v_mfma_f32_16x16x32_bf16 v[80:83], a[180:183], v[220:223], v[80:83]\n"
    "v_cvt_pk_bf16_f32 v186, v188, v189\n"
    "v_mfma_f32_16x16x32_bf16 v[84:87], a[180:183], v[228:231], v[84:87]\n"
    "v_exp_f32_e32 v180, v180\n"
    "v_mfma_f32_16x16x32_bf16 v[84:87], a[176:179], v[224:227], v[84:87]\n"
    "v_exp_f32_e32 v181, v181\n"
    "v_mfma_f32_16x16x32_bf16 v[88:91], a[176:179], v[232:235], v[88:91]\n"
    "ds_read_b64_tr_b16 a[176:177], v246 offset:20480\n"
    "v_mfma_f32_16x16x32_bf16 v[88:91], a[180:183], v[236:239], v[88:91]\n"
    "ds_read_b64_tr_b16 a[178:179], v246 offset:22528\n"
    "v_mfma_f32_16x16x32_bf16 v[100:103], a[184:187], v[232:235], v[100:103]\n"
    "ds_read_b64_tr_b16 a[180:181], v246 offset:28672\n"
    "v_mfma_f32_16x16x32_bf16 v[100:103], a[188:191], v[236:239], v[100:103]\n"
    "ds_read_b64_tr_b16 a[182:183], v246 offset:30720\n"
    "v_mfma_f32_16x16x32_bf16 v[96:99], a[188:191], v[228:231], v[96:99]\n"
    "v_cvt_pk_bf16_f32 v187, v190, v191\n"
    "v_mov_b32_e32 v158, 0\n"
    "v_mfma_f32_16x16x32_bf16 v[96:99], a[184:187], v[224:227], v[96:99]\n"
    "v_exp_f32_e32 v182, v182\n"
    "v_mfma_f32_16x16x32_bf16 v[92:95], a[184:187], v[216:219], v[92:95]\n"
    "v_exp_f32_e32 v183, v183\n"
    "v_mfma_f32_16x16x32_bf16 v[92:95], a[188:191], v[220:223], v[92:95]\n"
    "v_cvt_pk_bf16_f32 v178, v180, v181\n"
    "v_mfma_f32_16x16x32_bf16 v[36:39], a[188:191], v[212:215], v[36:39]\n"
    "v_exp_f32_e32 v172, v172\n"
    "v_mfma_f32_16x16x32_bf16 v[36:39], a[184:187], v[208:211], v[36:39]\n"
    "v_exp_f32_e32 v173, v173\n"
    "v_mfma_f32_16x16x32_bf16 v[12:15], a[184:187], v[200:203], v[12:15]\n"
    "s_mov_b32 m0, s49\n"
    "ds_read_b64_tr_b16 a[184:185], v247 offset:20480\n"
    "v_mfma_f32_16x16x32_bf16 v[12:15], a[188:191], v[204:207], v[12:15]\n"
    "s_waitcnt lgkmcnt(5)\n"
    "ds_read_b64_tr_b16 a[186:187], v247 offset:22528\n"
    "v_mov_b32_e32 v159, 0\n"
    "v_mfma_f32_16x16x32_bf16 v[16:19], a[160:163], v[200:203], v[16:19]\n"
    "ds_read_b64_tr_b16 a[188:189], v247 offset:28672\n"
    "v_mfma_f32_16x16x32_bf16 v[16:19], a[164:167], v[204:207], v[16:19]\n"
    "ds_read_b64_tr_b16 a[190:191], v247 offset:30720\n"
    "v_mfma_f32_16x16x32_bf16 v[40:43], a[164:167], v[212:215], v[40:43]\n"
    "buffer_store_dwordx2 v[154:155], v253, s[24:27], 0 offen offset:0 sc1\n"
    "v_cvt_pk_bf16_f32 v179, v182, v183\n"
    "v_mfma_f32_16x16x32_bf16 v[40:43], a[160:163], v[208:211], v[40:43]\n"
    "v_exp_f32_e32 v174, v174\n"
    "v_mfma_f32_16x16x32_bf16 v[104:107], a[160:163], v[216:219], v[104:107]\n"
    "v_exp_f32_e32 v175, v175\n"
    "v_mfma_f32_16x16x32_bf16 v[104:107], a[164:167], v[220:223], v[104:107]\n"
    "s_waitcnt vmcnt(9)\n"
    "v_mfma_f32_16x16x32_bf16 v[108:111], a[164:167], v[228:231], v[108:111]\n"
    "s_barrier\n"
    "v_mfma_f32_16x16x32_bf16 v[108:111], a[160:163], v[224:227], v[108:111]\n"
    "ds_write_addtid_b32 v192 offset:16384\n"
    "v_cvt_pk_bf16_f32 v170, v172, v173\n"
    "v_mfma_f32_16x16x32_bf16 v[112:115], a[160:163], v[232:235], v[112:115]\n"
    "ds_read_b128 a[160:163], v243 offset:53248\n"
    "v_mfma_f32_16x16x32_bf16 v[112:115], a[164:167], v[236:239], v[112:115]\n"
    "ds_read_b128 a[164:167], v240 offset:49152\n"
    "v_mfma_f32_16x16x32_bf16 v[124:127], a[168:171], v[232:235], v[124:127]\n"
    "v_exp_f32_e32 v164, v164\n"
    "v_mfma_f32_16x16x32_bf16 v[124:127], a[172:175], v[236:239], v[124:127]\n"
    "v_exp_f32_e32 v165, v165\n"
    "v_mfma_f32_16x16x32_bf16 v[120:123], a[172:175], v[228:231], v[120:123]\n"
    "ds_write_addtid_b32 v193 offset:16640\n"
    "v_cvt_pk_bf16_f32 v171, v174, v175\n"
    "v_mfma_f32_16x16x32_bf16 v[120:123], a[168:171], v[224:227], v[120:123]\n"
    "ds_write_addtid_b32 v184 offset:12288\n"
    "v_mfma_f32_16x16x32_bf16 v[116:119], a[168:171], v[216:219], v[116:119]\n"
    "v_exp_f32_e32 v166, v166\n"
    "v_mfma_f32_16x16x32_bf16 v[116:119], a[172:175], v[220:223], v[116:119]\n"
    "v_exp_f32_e32 v167, v167\n"
    "v_mfma_f32_16x16x32_bf16 v[44:47], a[172:175], v[212:215], v[44:47]\n"
    "ds_write_addtid_b32 v185 offset:12544\n"
    "v_cvt_pk_bf16_f32 v162, v164, v165\n"
    "v_mfma_f32_16x16x32_bf16 v[44:47], a[168:171], v[208:211], v[44:47]\n"
    "v_cvt_pk_bf16_f32 v163, v166, v167\n"
    "v_mfma_f32_16x16x32_bf16 v[20:23], a[168:171], v[200:203], v[20:23]\n"
    "ds_read_b128 a[168:171], v241 offset:49152\n"
    "v_mfma_f32_16x16x32_bf16 v[20:23], a[172:175], v[204:207], v[20:23]\n"
    "s_waitcnt lgkmcnt(7)\n"
    "ds_read_b128 a[172:175], v240 offset:53248\n"
    "v_mfma_f32_16x16x32_bf16 v[24:27], a[176:179], v[200:203], v[24:27]\n"
    "ds_write_addtid_b32 v176 offset:8192\n"
    "v_mfma_f32_16x16x32_bf16 v[24:27], a[180:183], v[204:207], v[24:27]\n"
    "ds_write_addtid_b32 v177 offset:8448\n"
    "v_mfma_f32_16x16x32_bf16 v[48:51], a[180:183], v[212:215], v[48:51]\n"
    "ds_write_addtid_b32 v168 offset:4096\n"
    "v_mfma_f32_16x16x32_bf16 v[48:51], a[176:179], v[208:211], v[48:51]\n"
    "ds_write_addtid_b32 v169 offset:4352\n"
    "v_mfma_f32_16x16x32_bf16 v[128:131], a[176:179], v[216:219], v[128:131]\n"
    "ds_write_addtid_b32 v160 offset:0\n"
    "v_mfma_f32_16x16x32_bf16 v[128:131], a[180:183], v[220:223], v[128:131]\n"
    "ds_write_addtid_b32 v161 offset:256\n"
    "v_mfma_f32_16x16x32_bf16 v[132:135], a[180:183], v[228:231], v[132:135]\n"
    "ds_write_addtid_b32 v194 offset:16896\n"
    "v_mfma_f32_16x16x32_bf16 v[132:135], a[176:179], v[224:227], v[132:135]\n"
    "ds_write_addtid_b32 v195 offset:17152\n"
    "v_mfma_f32_16x16x32_bf16 v[136:139], a[176:179], v[232:235], v[136:139]\n"
    "ds_read_b128 a[176:179], v241 offset:53248\n"
    "v_mfma_f32_16x16x32_bf16 v[136:139], a[180:183], v[236:239], v[136:139]\n"
    "ds_read_b128 a[180:183], v242 offset:49152\n"
    "v_mfma_f32_16x16x32_bf16 v[148:151], a[184:187], v[232:235], v[148:151]\n"
    "ds_write_addtid_b32 v186 offset:12800\n"
    "v_mfma_f32_16x16x32_bf16 v[148:151], a[188:191], v[236:239], v[148:151]\n"
    "ds_write_addtid_b32 v187 offset:13056\n"
    "v_mfma_f32_16x16x32_bf16 v[144:147], a[188:191], v[228:231], v[144:147]\n"
    "ds_write_addtid_b32 v178 offset:8704\n"
    "v_mfma_f32_16x16x32_bf16 v[144:147], a[184:187], v[224:227], v[144:147]\n"
    "ds_write_addtid_b32 v179 offset:8960\n"
    "v_mfma_f32_16x16x32_bf16 v[140:143], a[184:187], v[216:219], v[140:143]\n"
    "ds_write_addtid_b32 v170 offset:4608\n"
    "v_mfma_f32_16x16x32_bf16 v[140:143], a[188:191], v[220:223], v[140:143]\n"
    "ds_write_addtid_b32 v171 offset:4864\n"
    "v_mfma_f32_16x16x32_bf16 v[52:55], a[188:191], v[212:215], v[52:55]\n"
    "ds_write_addtid_b32 v162 offset:512\n"
    "v_mfma_f32_16x16x32_bf16 v[52:55], a[184:187], v[208:211], v[52:55]\n"
    "ds_write_addtid_b32 v163 offset:768\n"
    "v_mfma_f32_16x16x32_bf16 v[28:31], a[184:187], v[200:203], v[28:31]\n"
    "ds_read_b128 a[184:187], v243 offset:49152\n"
    "v_mfma_f32_16x16x32_bf16 v[28:31], a[188:191], v[204:207], v[28:31]\n"
    "s_waitcnt lgkmcnt(15)\n"
    "ds_read_b128 a[188:191], v242 offset:53248\n"
    "v_mfma_f32_16x16x32_bf16 v[200:203], a[160:163], a[28:31], 0\n"
    "v_mfma_f32_16x16x32_bf16 v[200:203], a[164:167], a[0:3], v[200:203]\n"
    "v_mfma_f32_16x16x32_bf16 v[208:211], a[164:167], a[32:35], 0\n"
    "v_mfma_f32_16x16x32_bf16 v[208:211], a[160:163], a[60:63], v[208:211]\n"
    "v_mfma_f32_16x16x32_bf16 v[216:219], a[160:163], a[92:95], 0\n"
    "v_mfma_f32_16x16x32_bf16 v[216:219], a[164:167], a[64:67], v[216:219]\n"
    "s_waitcnt lgkmcnt(0)\n"
    "v_mfma_f32_16x16x32_bf16 v[224:227], a[164:167], a[96:99], 0\n"
    "s_barrier\n"
    "v_mfma_f32_16x16x32_bf16 v[224:227], a[160:163], a[124:127], v[224:227]\n"
    "s_add_u32 m0, s43, 16384\n"
    "ds_read2st64_b32 v[164:165], v250 offset0:0 offset1:1\n"
    "v_mfma_f32_16x16x32_bf16 v[232:235], a[160:163], a[156:159], 0\n"
    "ds_read_b128 a[160:163], v243 offset:55296\n"
    "v_mfma_f32_16x16x32_bf16 v[232:235], a[164:167], a[128:131], v[232:235]\n"
    "ds_read_b128 a[164:167], v240 offset:51200\n"
    "v_mfma_f32_16x16x32_bf16 v[232:235], a[168:171], a[132:135], v[232:235]\n"
    "buffer_load_dwordx4 v248, s[8:11], s40 offen offset:0 sc1 lds\n"
    "v_mfma_f32_16x16x32_bf16 v[232:235], a[172:175], a[136:139], v[232:235]\n"
    "v_mfma_f32_16x16x32_bf16 v[224:227], a[172:175], a[104:107], v[224:227]\n"
    "v_mfma_f32_16x16x32_bf16 v[224:227], a[168:171], a[100:103], v[224:227]\n"
    "ds_read2st64_b32 v[166:167], v250 offset0:2 offset1:3\n"
    "v_mfma_f32_16x16x32_bf16 v[216:219], a[168:171], a[68:71], v[216:219]\n"
    "buffer_load_dwordx4 v248, s[8:11], s40 offen offset:1024 sc1 lds\n"
    "v_mfma_f32_16x16x32_bf16 v[216:219], a[172:175], a[72:75], v[216:219]\n"
    "v_mfma_f32_16x16x32_bf16 v[208:211], a[172:175], a[40:43], v[208:211]\n"
    "v_mfma_f32_16x16x32_bf16 v[208:211], a[168:171], a[36:39], v[208:211]\n"
    "ds_read2st64_b32 v[172:173], v250 offset0:16 offset1:17\n"
    "v_mfma_f32_16x16x32_bf16 v[200:203], a[168:171], a[4:7], v[200:203]\n"
    "ds_read_b128 a[168:171], v241 offset:51200\n"
    "buffer_load_dwordx4 v248, s[8:11], s40 offen offset:2048 sc1 lds\n"
    "v_mfma_f32_16x16x32_bf16 v[200:203], a[172:175], a[8:11], v[200:203]\n"
    "ds_read_b128 a[172:175], v240 offset:55296\n"
    "v_mfma_f32_16x16x32_bf16 v[200:203], a[176:179], a[12:15], v[200:203]\n"
    "ds_read2st64_b32 v[174:175], v250 offset0:18 offset1:19\n"
    "v_mfma_f32_16x16x32_bf16 v[200:203], a[180:183], a[16:19], v[200:203]\n"
    "v_mfma_f32_16x16x32_bf16 v[208:211], a[180:183], a[48:51], v[208:211]\n"
    "buffer_load_dwordx4 v248, s[8:11], s40 offen offset:3072 sc1 lds\n"
    "v_mfma_f32_16x16x32_bf16 v[208:211], a[176:179], a[44:47], v[208:211]\n"
    "s_add_u32 m0, s43, 20480\n"
    "v_mfma_f32_16x16x32_bf16 v[216:219], a[176:179], a[76:79], v[216:219]\n"
    "s_add_u32 s40, s40, 0x8000\n"
    "ds_read2st64_b32 v[180:181], v250 offset0:32 offset1:33\n"
    "v_mfma_f32_16x16x32_bf16 v[216:219], a[180:183], a[80:83], v[216:219]\n"
    "v_mfma_f32_16x16x32_bf16 v[224:227], a[180:183], a[112:115], v[224:227]\n"
    "buffer_load_dwordx4 v248, s[8:11], s41 offen offset:0 sc1 lds\n"
    "v_mfma_f32_16x16x32_bf16 v[224:227], a[176:179], a[108:111], v[224:227]\n"
    "ds_read2st64_b32 v[182:183], v250 offset0:34 offset1:35\n"
    "v_mfma_f32_16x16x32_bf16 v[232:235], a[176:179], a[140:143], v[232:235]\n"
    "ds_read_b128 a[176:179], v241 offset:55296\n"
    "v_mfma_f32_16x16x32_bf16 v[232:235], a[180:183], a[144:147], v[232:235]\n"
    "ds_read_b128 a[180:183], v242 offset:51200\n"
    "v_mfma_f32_16x16x32_bf16 v[232:235], a[184:187], a[148:151], v[232:235]\n"
    "buffer_load_dwordx4 v248, s[8:11], s41 offen offset:1024 sc1 lds\n"
    "v_mfma_f32_16x16x32_bf16 v[232:235], a[188:191], a[152:155], v[232:235]\n"
    "v_mfma_f32_16x16x32_bf16 v[224:227], a[188:191], a[120:123], v[224:227]\n"
    "ds_read2st64_b32 v[188:189], v250 offset0:48 offset1:49\n"
    "v_mfma_f32_16x16x32_bf16 v[224:227], a[184:187], a[116:119], v[224:227]\n"
    "v_mfma_f32_16x16x32_bf16 v[216:219], a[184:187], a[84:87], v[216:219]\n"
    "buffer_load_dwordx4 v248, s[8:11], s41 offen offset:2048 sc1 lds\n"
    "v_mfma_f32_16x16x32_bf16 v[216:219], a[188:191], a[88:91], v[216:219]\n"
    "ds_read2st64_b32 v[190:191], v250 offset0:50 offset1:51\n"
    "v_mfma_f32_16x16x32_bf16 v[208:211], a[188:191], a[56:59], v[208:211]\n"
    "ds_read_b64 v[254:255], v252 offset:64\n"
    "v_mfma_f32_16x16x32_bf16 v[208:211], a[184:187], a[52:55], v[208:211]\n"
    "ds_read2st64_b32 v[196:197], v250 offset0:64 offset1:65\n"
    "v_mfma_f32_16x16x32_bf16 v[200:203], a[184:187], a[20:23], v[200:203]\n"
    "ds_read_b128 a[184:187], v243 offset:51200\n"
    "buffer_load_dwordx4 v248, s[8:11], s41 offen offset:3072 sc1 lds\n"
    "v_mfma_f32_16x16x32_bf16 v[200:203], a[188:191], a[24:27], v[200:203]\n"
    "s_waitcnt lgkmcnt(15)\n"
    "s_add_u32 s41, s41, 0x8000\n"
    "ds_read_b128 a[188:191], v242 offset:55296\n"
    "v_mfma_f32_16x16x32_bf16 v[204:207], a[160:163], a[28:31], 0\n"
    "s_waitcnt lgkmcnt(3)\n"
    "v_mad_u32_u16 v253, v254, s42, v251 op_sel:[0,0,0,0]\n"
    "v_mfma_f32_16x16x32_bf16 v[204:207], a[164:167], a[0:3], v[204:207]\n"
    "buffer_load_dwordx4 a[200:203], v253, s[20:23], 0 offen offset:0 sc1\n"
    "v_mfma_f32_16x16x32_bf16 v[212:215], a[164:167], a[32:35], 0\n"
    "v_exp_f32_e32 v232, v232\n"
    "v_mfma_f32_16x16x32_bf16 v[212:215], a[160:163], a[60:63], v[212:215]\n"
    "ds_read2st64_b32 v[198:199], v250 offset0:66 offset1:67\n"
    "v_mfma_f32_16x16x32_bf16 v[220:223], a[160:163], a[92:95], 0\n"
    "v_mad_u32_u16 v253, v254, s42, v251 op_sel:[1,0,0,0]\n"
    "v_mfma_f32_16x16x32_bf16 v[220:223], a[164:167], a[64:67], v[220:223]\n"
    "buffer_load_dwordx4 a[204:207], v253, s[20:23], 0 offen offset:0 sc1\n"
    "v_mfma_f32_16x16x32_bf16 v[228:231], a[164:167], a[96:99], 0\n"
    "v_exp_f32_e32 v233, v233\n"
    "v_mfma_f32_16x16x32_bf16 v[228:231], a[160:163], a[124:127], v[228:231]\n"
    "v_exp_f32_e32 v234, v234\n"
    "v_mfma_f32_16x16x32_bf16 v[236:239], a[160:163], a[156:159], 0\n"
    "ds_read_b64_tr_b16 a[160:161], v244 offset:32768\n"
    "v_mad_u32_u16 v253, v255, s42, v251 op_sel:[0,0,0,0]\n"
    "v_mfma_f32_16x16x32_bf16 v[236:239], a[164:167], a[128:131], v[236:239]\n"
    "ds_read_b64_tr_b16 a[162:163], v244 offset:34816\n"
    "buffer_load_dwordx4 a[208:211], v253, s[20:23], 0 offen offset:0 sc1\n"
    "v_mfma_f32_16x16x32_bf16 v[236:239], a[168:171], a[132:135], v[236:239]\n"
    "ds_read_b64_tr_b16 a[164:165], v244 offset:40960\n"
    "v_mfma_f32_16x16x32_bf16 v[236:239], a[172:175], a[136:139], v[236:239]\n"
    "ds_read_b64_tr_b16 a[166:167], v244 offset:43008\n"
    "v_mfma_f32_16x16x32_bf16 v[228:231], a[172:175], a[104:107], v[228:231]\n"
    "v_mad_u32_u16 v253, v255, s42, v251 op_sel:[1,0,0,0]\n"
    "v_mfma_f32_16x16x32_bf16 v[228:231], a[168:171], a[100:103], v[228:231]\n"
    "buffer_load_dwordx4 a[212:215], v253, s[20:23], 0 offen offset:0 sc1\n"
    "ds_read_b64 v[254:255], v252 offset:72\n"
    "v_mfma_f32_16x16x32_bf16 v[220:223], a[168:171], a[68:71], v[220:223]\n"
    "v_exp_f32_e32 v235, v235\n"
    "v_mfma_f32_16x16x32_bf16 v[220:223], a[172:175], a[72:75], v[220:223]\n"
    "v_cvt_pk_bf16_f32 v232, v232, v233\n"
    "v_mfma_f32_16x16x32_bf16 v[212:215], a[172:175], a[40:43], v[212:215]\n"
    "v_exp_f32_e32 v224, v224\n"
    "v_mfma_f32_16x16x32_bf16 v[212:215], a[168:171], a[36:39], v[212:215]\n"
    "v_exp_f32_e32 v225, v225\n"
    "v_mfma_f32_16x16x32_bf16 v[204:207], a[168:171], a[4:7], v[204:207]\n"
    "ds_read_b64_tr_b16 a[168:169], v245 offset:32768\n"
    "v_cvt_pk_bf16_f32 v233, v234, v235\n"
    "v_mfma_f32_16x16x32_bf16 v[204:207], a[172:175], a[8:11], v[204:207]\n"
    "ds_read_b64_tr_b16 a[170:171], v245 offset:34816\n"
    "v_mfma_f32_16x16x32_bf16 v[204:207], a[176:179], a[12:15], v[204:207]\n"
    "s_waitcnt lgkmcnt(2)\n"
    "ds_read_b64_tr_b16 a[172:173], v245 offset:40960\n"
    "v_mad_u32_u16 v253, v254, s42, v251 op_sel:[0,0,0,0]\n"
    "v_mfma_f32_16x16x32_bf16 v[204:207], a[180:183], a[16:19], v[204:207]\n"
    "ds_read_b64_tr_b16 a[174:175], v245 offset:43008\n"
    "buffer_load_dwordx4 a[216:219], v253, s[20:23], 0 offen offset:0 sc1\n"
    "v_mfma_f32_16x16x32_bf16 v[212:215], a[180:183], a[48:51], v[212:215]\n"
    "v_exp_f32_e32 v226, v226\n"
    "v_mfma_f32_16x16x32_bf16 v[212:215], a[176:179], a[44:47], v[212:215]\n"
    "v_exp_f32_e32 v227, v227\n"
    "v_mfma_f32_16x16x32_bf16 v[220:223], a[176:179], a[76:79], v[220:223]\n"
    "v_mad_u32_u16 v253, v254, s42, v251 op_sel:[1,0,0,0]\n"
    "v_cvt_pk_bf16_f32 v224, v224, v225\n"
    "v_mfma_f32_16x16x32_bf16 v[220:223], a[180:183], a[80:83], v[220:223]\n"
    "buffer_load_dwordx4 a[220:223], v253, s[20:23], 0 offen offset:0 sc1\n"
    "v_mfma_f32_16x16x32_bf16 v[228:231], a[180:183], a[112:115], v[228:231]\n"
    "v_exp_f32_e32 v216, v216\n"
    "v_mfma_f32_16x16x32_bf16 v[228:231], a[176:179], a[108:111], v[228:231]\n"
    "v_exp_f32_e32 v217, v217\n"
    "v_mfma_f32_16x16x32_bf16 v[236:239], a[176:179], a[140:143], v[236:239]\n"
    "ds_read_b64_tr_b16 a[176:177], v246 offset:32768\n"
    "v_cvt_pk_bf16_f32 v225, v226, v227\n"
    "v_mfma_f32_16x16x32_bf16 v[236:239], a[180:183], a[144:147], v[236:239]\n"
    "ds_read_b64_tr_b16 a[178:179], v246 offset:34816\n"
    "v_mfma_f32_16x16x32_bf16 v[236:239], a[184:187], a[148:151], v[236:239]\n"
    "ds_read_b64_tr_b16 a[180:181], v246 offset:40960\n"
    "v_mfma_f32_16x16x32_bf16 v[236:239], a[188:191], a[152:155], v[236:239]\n"
    "ds_read_b64_tr_b16 a[182:183], v246 offset:43008\n"
    "v_mfma_f32_16x16x32_bf16 v[228:231], a[188:191], a[120:123], v[228:231]\n"
    "ds_read_b64 v[254:255], v252 offset:96\n"
    "v_mfma_f32_16x16x32_bf16 v[228:231], a[184:187], a[116:119], v[228:231]\n"
    "v_exp_f32_e32 v218, v218\n"
    "v_mfma_f32_16x16x32_bf16 v[220:223], a[184:187], a[84:87], v[220:223]\n"
    "v_exp_f32_e32 v219, v219\n"
    "v_mfma_f32_16x16x32_bf16 v[220:223], a[188:191], a[88:91], v[220:223]\n"
    "v_cvt_pk_bf16_f32 v216, v216, v217\n"
    "v_mfma_f32_16x16x32_bf16 v[212:215], a[188:191], a[56:59], v[212:215]\n"
    "s_waitcnt lgkmcnt(0)\n"
    "v_mad_u32_u16 v253, v254, s42, v251 op_sel:[0,0,0,0]\n"
    "v_mfma_f32_16x16x32_bf16 v[212:215], a[184:187], a[52:55], v[212:215]\n"
    "buffer_load_dwordx4 a[224:227], v253, s[20:23], 0 offen offset:0 sc1\n"
    "v_mfma_f32_16x16x32_bf16 v[204:207], a[184:187], a[20:23], v[204:207]\n"
    "ds_read_b64_tr_b16 a[184:185], v247 offset:32768\n"
    "v_mfma_f32_16x16x32_bf16 v[204:207], a[188:191], a[24:27], v[204:207]\n"
    "ds_read_b64_tr_b16 a[186:187], v247 offset:34816\n"
    "v_mfma_f32_16x16x32_bf16 v[0:3], a[160:163], v[160:163], v[0:3]\n"
    "ds_read_b64_tr_b16 a[188:189], v247 offset:40960\n"
    "v_mad_u32_u16 v253, v254, s42, v251 op_sel:[1,0,0,0]\n"
    "v_mfma_f32_16x16x32_bf16 v[0:3], a[164:167], v[164:167], v[0:3]\n"
    "ds_read_b64_tr_b16 a[190:191], v247 offset:43008\n"
    "buffer_load_dwordx4 a[228:231], v253, s[20:23], 0 offen offset:0 sc1\n"
    "v_mfma_f32_16x16x32_bf16 a[248:251], a[164:167], v[172:175], a[248:251]\n"
    "v_exp_f32_e32 v208, v208\n"
    "v_mfma_f32_16x16x32_bf16 a[248:251], a[160:163], v[168:171], a[248:251]\n"
    "v_exp_f32_e32 v209, v209\n"
    "v_mfma_f32_16x16x32_bf16 v[56:59], a[160:163], v[176:179], v[56:59]\n"
    "v_mad_u32_u16 v253, v255, s42, v251 op_sel:[0,0,0,0]\n"
    "v_cvt_pk_bf16_f32 v217, v218, v219\n"
    "v_mfma_f32_16x16x32_bf16 v[56:59], a[164:167], v[180:183], v[56:59]\n"
    "buffer_load_dwordx4 a[232:235], v253, s[20:23], 0 offen offset:0 sc1\n"
    "v_mfma_f32_16x16x32_bf16 v[60:63], a[164:167], v[188:191], v[60:63]\n"
    "v_exp_f32_e32 v210, v210\n"
    "v_mfma_f32_16x16x32_bf16 v[60:63], a[160:163], v[184:187], v[60:63]\n"
    "v_exp_f32_e32 v211, v211\n"
    "v_mfma_f32_16x16x32_bf16 v[64:67], a[160:163], v[192:195], v[64:67]\n"
    "ds_read_b64_tr_b16 a[160:161], v244 offset:36864\n"
    "v_mad_u32_u16 v253, v255, s42, v251 op_sel:[1,0,0,0]\n"
    "v_mfma_f32_16x16x32_bf16 v[64:67], a[164:167], v[196:199], v[64:67]\n"
    "ds_read_b64_tr_b16 a[162:163], v244 offset:38912\n"
    "buffer_load_dwordx4 a[236:239], v253, s[20:23], 0 offen offset:0 sc1\n"
    "v_mfma_f32_16x16x32_bf16 v[76:79], a[168:171], v[192:195], v[76:79]\n"
    "ds_read_b64_tr_b16 a[164:165], v244 offset:45056\n"
    "v_cvt_pk_bf16_f32 v208, v208, v209\n"
    "v_mfma_f32_16x16x32_bf16 v[76:79], a[172:175], v[196:199], v[76:79]\n"
    "ds_read_b64_tr_b16 a[166:167], v244 offset:47104\n"
    "v_mfma_f32_16x16x32_bf16 v[72:75], a[172:175], v[188:191], v[72:75]\n"
    "ds_read_b64 v[254:255], v252 offset:104\n"
    "v_mfma_f32_16x16x32_bf16 v[72:75], a[168:171], v[184:187], v[72:75]\n"
    "v_exp_f32_e32 v200, v200\n"
    "v_mfma_f32_16x16x32_bf16 v[68:71], a[168:171], v[176:179], v[68:71]\n"
    "v_exp_f32_e32 v201, v201\n"
    "v_mfma_f32_16x16x32_bf16 v[68:71], a[172:175], v[180:183], v[68:71]\n"
    "v_cvt_pk_bf16_f32 v209, v210, v211\n"
    "v_mfma_f32_16x16x32_bf16 a[252:255], a[172:175], v[172:175], a[252:255]\n"
    "v_exp_f32_e32 v202, v202\n"
    "v_mfma_f32_16x16x32_bf16 a[252:255], a[168:171], v[168:171], a[252:255]\n"
    "v_exp_f32_e32 v203, v203\n"
    "v_mfma_f32_16x16x32_bf16 v[4:7], a[168:171], v[160:163], v[4:7]\n"
    "s_waitcnt lgkmcnt(0)\n"
    "ds_read_b64_tr_b16 a[168:169], v245 offset:36864\n"
    "v_mad_u32_u16 v253, v254, s42, v251 op_sel:[0,0,0,0]\n"
    "v_mfma_f32_16x16x32_bf16 v[4:7], a[172:175], v[164:167], v[4:7]\n"
    "ds_read_b64_tr_b16 a[170:171], v245 offset:38912\n"
    "v_cvt_pk_bf16_f32 v200, v200, v201\n"
    "v_mfma_f32_16x16x32_bf16 v[8:11], a[176:179], v[160:163], v[8:11]\n"
    "ds_read_b64_tr_b16 a[172:173], v245 offset:45056\n"
    "buffer_load_dwordx4 a[240:243], v253, s[20:23], 0 offen offset:0 sc1\n"
    "v_mfma_f32_16x16x32_bf16 v[8:11], a[180:183], v[164:167], v[8:11]\n"
    "ds_read_b64_tr_b16 a[174:175], v245 offset:47104\n"
    "v_cvt_pk_bf16_f32 v201, v202, v203\n"
    "v_mfma_f32_16x16x32_bf16 v[32:35], a[180:183], v[172:175], v[32:35]\n"
    "v_exp_f32_e32 v236, v236\n"
    "v_mfma_f32_16x16x32_bf16 v[32:35], a[176:179], v[168:171], v[32:35]\n"
    "v_mad_u32_u16 v253, v254, s42, v251 op_sel:[1,0,0,0]\n"
    "v_mfma_f32_16x16x32_bf16 v[80:83], a[176:179], v[176:179], v[80:83]\n"
    "buffer_load_dwordx4 a[244:247], v253, s[20:23], 0 offen offset:0 sc1\n"
    "v_mfma_f32_16x16x32_bf16 v[80:83], a[180:183], v[180:183], v[80:83]\n"
    "v_exp_f32_e32 v237, v237\n"
    "v_mfma_f32_16x16x32_bf16 v[84:87], a[180:183], v[188:191], v[84:87]\n"
    "v_exp_f32_e32 v238, v238\n"
    "v_mfma_f32_16x16x32_bf16 v[84:87], a[176:179], v[184:187], v[84:87]\n"
    "v_exp_f32_e32 v239, v239\n"
    "v_mfma_f32_16x16x32_bf16 v[88:91], a[176:179], v[192:195], v[88:91]\n"
    "ds_read_b64_tr_b16 a[176:177], v246 offset:36864\n"
    "v_cvt_pk_bf16_f32 v234, v236, v237\n"
    "v_mfma_f32_16x16x32_bf16 v[88:91], a[180:183], v[196:199], v[88:91]\n"
    "ds_read_b64_tr_b16 a[178:179], v246 offset:38912\n"
    "v_exp_f32_e32 v228, v228\n"
    "v_mfma_f32_16x16x32_bf16 v[100:103], a[184:187], v[192:195], v[100:103]\n"
    "ds_read_b64_tr_b16 a[180:181], v246 offset:45056\n"
    "v_exp_f32_e32 v229, v229\n"
    "v_mfma_f32_16x16x32_bf16 v[100:103], a[188:191], v[196:199], v[100:103]\n"
    "ds_read_b64_tr_b16 a[182:183], v246 offset:47104\n"
    "v_cvt_pk_bf16_f32 v235, v238, v239\n"
    "v_mfma_f32_16x16x32_bf16 v[96:99], a[188:191], v[188:191], v[96:99]\n"
    "v_exp_f32_e32 v230, v230\n"
    "v_mfma_f32_16x16x32_bf16 v[96:99], a[184:187], v[184:187], v[96:99]\n"
    "v_exp_f32_e32 v231, v231\n"
    "v_cvt_pk_bf16_f32 v226, v228, v229\n"
    "v_mfma_f32_16x16x32_bf16 v[92:95], a[184:187], v[176:179], v[92:95]\n"
    "v_exp_f32_e32 v220, v220\n"
    "v_mfma_f32_16x16x32_bf16 v[92:95], a[188:191], v[180:183], v[92:95]\n"
    "v_exp_f32_e32 v221, v221\n"
    "v_mfma_f32_16x16x32_bf16 v[36:39], a[188:191], v[172:175], v[36:39]\n"
    "v_cvt_pk_bf16_f32 v227, v230, v231\n"
    "v_mfma_f32_16x16x32_bf16 v[36:39], a[184:187], v[168:171], v[36:39]\n"
    "v_exp_f32_e32 v222, v222\n"
    "v_mfma_f32_16x16x32_bf16 v[12:15], a[184:187], v[160:163], v[12:15]\n"
    "s_mov_b32 m0, s49\n"
    "ds_read_b64_tr_b16 a[184:185], v247 offset:36864\n"
    "v_mfma_f32_16x16x32_bf16 v[12:15], a[188:191], v[164:167], v[12:15]\n"
    "s_waitcnt lgkmcnt(5)\n"
    "ds_read_b64_tr_b16 a[186:187], v247 offset:38912\n"
    "v_mfma_f32_16x16x32_bf16 v[16:19], a[160:163], v[160:163], v[16:19]\n"
    "ds_read_b64_tr_b16 a[188:189], v247 offset:45056\n"
    "v_mfma_f32_16x16x32_bf16 v[16:19], a[164:167], v[164:167], v[16:19]\n"
    "ds_read_b64_tr_b16 a[190:191], v247 offset:47104\n"
    "v_mfma_f32_16x16x32_bf16 v[40:43], a[164:167], v[172:175], v[40:43]\n"
    "v_exp_f32_e32 v223, v223\n"
    "v_mfma_f32_16x16x32_bf16 v[40:43], a[160:163], v[168:171], v[40:43]\n"
    "v_cvt_pk_bf16_f32 v218, v220, v221\n"
    "v_mfma_f32_16x16x32_bf16 v[104:107], a[160:163], v[176:179], v[104:107]\n"
    "v_exp_f32_e32 v212, v212\n"
    "v_mfma_f32_16x16x32_bf16 v[104:107], a[164:167], v[180:183], v[104:107]\n"
    "s_waitcnt vmcnt(20)\n"
    "v_mfma_f32_16x16x32_bf16 v[108:111], a[164:167], v[188:191], v[108:111]\n"
    "s_barrier\n"
    "v_mfma_f32_16x16x32_bf16 v[108:111], a[160:163], v[184:187], v[108:111]\n"
    "v_exp_f32_e32 v213, v213\n"
    "v_mfma_f32_16x16x32_bf16 v[112:115], a[160:163], v[192:195], v[112:115]\n"
    "ds_read_b128 a[160:163], v243 offset:4096\n"
    "v_cvt_pk_bf16_f32 v219, v222, v223\n"
    "v_mfma_f32_16x16x32_bf16 v[112:115], a[164:167], v[196:199], v[112:115]\n"
    "ds_read_b128 a[164:167], v240 offset:0\n"
    "ds_write_addtid_b32 v232 offset:16384\n"
    "v_mfma_f32_16x16x32_bf16 v[124:127], a[168:171], v[192:195], v[124:127]\n"
    "v_exp_f32_e32 v214, v214\n"
    "v_mfma_f32_16x16x32_bf16 v[124:127], a[172:175], v[196:199], v[124:127]\n"
    "v_exp_f32_e32 v215, v215\n"
    "v_mfma_f32_16x16x32_bf16 v[120:123], a[172:175], v[188:191], v[120:123]\n"
    "ds_write_addtid_b32 v233 offset:16640\n"
    "v_cvt_pk_bf16_f32 v210, v212, v213\n"
    "v_mfma_f32_16x16x32_bf16 v[120:123], a[168:171], v[184:187], v[120:123]\n"
    "v_exp_f32_e32 v204, v204\n"
    "v_mfma_f32_16x16x32_bf16 v[116:119], a[168:171], v[176:179], v[116:119]\n"
    "v_exp_f32_e32 v205, v205\n"
    "v_mfma_f32_16x16x32_bf16 v[116:119], a[172:175], v[180:183], v[116:119]\n"
    "ds_write_addtid_b32 v224 offset:12288\n"
    "v_cvt_pk_bf16_f32 v211, v214, v215\n"
    "v_mfma_f32_16x16x32_bf16 v[44:47], a[172:175], v[172:175], v[44:47]\n"
    "v_exp_f32_e32 v206, v206\n"
    "v_mfma_f32_16x16x32_bf16 v[44:47], a[168:171], v[168:171], v[44:47]\n"
    "v_exp_f32_e32 v207, v207\n"
    "v_mfma_f32_16x16x32_bf16 v[20:23], a[168:171], v[160:163], v[20:23]\n"
    "ds_read_b128 a[168:171], v241 offset:0\n"
    "v_cvt_pk_bf16_f32 v202, v204, v205\n"
    "v_mfma_f32_16x16x32_bf16 v[20:23], a[172:175], v[164:167], v[20:23]\n"
    "s_waitcnt lgkmcnt(6)\n"
    "ds_read_b128 a[172:175], v240 offset:4096\n"
    "v_cvt_pk_bf16_f32 v203, v206, v207\n"
    "v_mfma_f32_16x16x32_bf16 v[24:27], a[176:179], v[160:163], v[24:27]\n"
    "ds_write_addtid_b32 v225 offset:12544\n"
    "ds_write_addtid_b32 v216 offset:8192\n"
    "v_mfma_f32_16x16x32_bf16 v[24:27], a[180:183], v[164:167], v[24:27]\n"
    "ds_write_addtid_b32 v217 offset:8448\n"
    "v_mfma_f32_16x16x32_bf16 v[48:51], a[180:183], v[172:175], v[48:51]\n"
    "ds_write_addtid_b32 v208 offset:4096\n"
    "v_mfma_f32_16x16x32_bf16 v[48:51], a[176:179], v[168:171], v[48:51]\n"
    "ds_write_addtid_b32 v209 offset:4352\n"
    "v_mfma_f32_16x16x32_bf16 v[128:131], a[176:179], v[176:179], v[128:131]\n"
    "ds_write_addtid_b32 v200 offset:0\n"
    "v_mfma_f32_16x16x32_bf16 v[128:131], a[180:183], v[180:183], v[128:131]\n"
    "ds_write_addtid_b32 v201 offset:256\n"
    "v_mfma_f32_16x16x32_bf16 v[132:135], a[180:183], v[188:191], v[132:135]\n"
    "ds_write_addtid_b32 v234 offset:16896\n"
    "v_mfma_f32_16x16x32_bf16 v[132:135], a[176:179], v[184:187], v[132:135]\n"
    "ds_write_addtid_b32 v235 offset:17152\n"
    "v_mfma_f32_16x16x32_bf16 v[136:139], a[176:179], v[192:195], v[136:139]\n"
    "ds_read_b128 a[176:179], v241 offset:4096\n"
    "v_mfma_f32_16x16x32_bf16 v[136:139], a[180:183], v[196:199], v[136:139]\n"
    "ds_read_b128 a[180:183], v242 offset:0\n"
    "v_mfma_f32_16x16x32_bf16 v[148:151], a[184:187], v[192:195], v[148:151]\n"
    "ds_write_addtid_b32 v226 offset:12800\n"
    "v_mfma_f32_16x16x32_bf16 v[148:151], a[188:191], v[196:199], v[148:151]\n"
    "ds_write_addtid_b32 v227 offset:13056\n"
    "v_mfma_f32_16x16x32_bf16 v[144:147], a[188:191], v[188:191], v[144:147]\n"
    "ds_write_addtid_b32 v218 offset:8704\n"
    "v_mfma_f32_16x16x32_bf16 v[144:147], a[184:187], v[184:187], v[144:147]\n"
    "ds_write_addtid_b32 v219 offset:8960\n"
    "v_mfma_f32_16x16x32_bf16 v[140:143], a[184:187], v[176:179], v[140:143]\n"
    "ds_write_addtid_b32 v210 offset:4608\n"
    "v_mfma_f32_16x16x32_bf16 v[140:143], a[188:191], v[180:183], v[140:143]\n"
    "ds_write_addtid_b32 v211 offset:4864\n"
    "v_mfma_f32_16x16x32_bf16 v[52:55], a[188:191], v[172:175], v[52:55]\n"
    "ds_write_addtid_b32 v202 offset:512\n"
    "v_mfma_f32_16x16x32_bf16 v[52:55], a[184:187], v[168:171], v[52:55]\n"
    "ds_write_addtid_b32 v203 offset:768\n"
    "v_mfma_f32_16x16x32_bf16 v[28:31], a[184:187], v[160:163], v[28:31]\n"
    "ds_read_b128 a[184:187], v243 offset:0\n"
    "v_mfma_f32_16x16x32_bf16 v[28:31], a[188:191], v[164:167], v[28:31]\n"
    "s_waitcnt lgkmcnt(15)\n"
    "ds_read_b128 a[188:191], v242 offset:4096\n"
    "v_mfma_f32_16x16x32_bf16 v[160:163], a[160:163], a[28:31], 0\n"
    "v_mfma_f32_16x16x32_bf16 v[160:163], a[164:167], a[0:3], v[160:163]\n"
    "v_mfma_f32_16x16x32_bf16 v[168:171], a[164:167], a[32:35], 0\n"
    "v_mfma_f32_16x16x32_bf16 v[168:171], a[160:163], a[60:63], v[168:171]\n"
    "v_mfma_f32_16x16x32_bf16 v[176:179], a[160:163], a[92:95], 0\n"
    "v_mfma_f32_16x16x32_bf16 v[176:179], a[164:167], a[64:67], v[176:179]\n"
    "s_waitcnt lgkmcnt(0)\n"
    "v_mfma_f32_16x16x32_bf16 v[184:187], a[164:167], a[96:99], 0\n"
    "s_barrier\n"
    "v_mfma_f32_16x16x32_bf16 v[184:187], a[160:163], a[124:127], v[184:187]\n"
    "s_add_u32 m0, s43, 32768\n"
    "v_mfma_f32_16x16x32_bf16 v[192:195], a[160:163], a[156:159], 0\n"
    "ds_read_b128 a[160:163], v243 offset:6144\n"
    "v_mfma_f32_16x16x32_bf16 v[192:195], a[164:167], a[128:131], v[192:195]\n"
    "ds_read_b128 a[164:167], v240 offset:2048\n"
    "v_mfma_f32_16x16x32_bf16 v[192:195], a[168:171], a[132:135], v[192:195]\n"
    "buffer_load_dwordx4 v248, s[8:11], s40 offen offset:0 sc1 lds\n"
    "v_mfma_f32_16x16x32_bf16 v[192:195], a[172:175], a[136:139], v[192:195]\n"
    "ds_read2st64_b32 v[204:205], v250 offset0:0 offset1:1\n"
    "v_mfma_f32_16x16x32_bf16 v[184:187], a[172:175], a[104:107], v[184:187]\n"
    "s_waitcnt vmcnt(11)\n"
    "v_smfmac_f32_16x16x64_bf16 v[156:159], a[192:195], a[200:207], v152\n"
    "v_mfma_f32_16x16x32_bf16 v[184:187], a[168:171], a[100:103], v[184:187]\n"
    "ds_read2st64_b32 v[206:207], v250 offset0:2 offset1:3\n"
    "v_mfma_f32_16x16x32_bf16 v[176:179], a[168:171], a[68:71], v[176:179]\n"
    "buffer_load_dwordx4 v248, s[8:11], s40 offen offset:1024 sc1 lds\n"
    "v_mfma_f32_16x16x32_bf16 v[176:179], a[172:175], a[72:75], v[176:179]\n"
    "v_mfma_f32_16x16x32_bf16 v[168:171], a[172:175], a[40:43], v[168:171]\n"
    "s_waitcnt vmcnt(10)\n"
    "v_smfmac_f32_16x16x64_bf16 v[156:159], a[192:195], a[208:215], v152\n"
    "ds_read2st64_b32 v[212:213], v250 offset0:16 offset1:17\n"
    "v_mfma_f32_16x16x32_bf16 v[168:171], a[168:171], a[36:39], v[168:171]\n"
    "v_mfma_f32_16x16x32_bf16 v[160:163], a[168:171], a[4:7], v[160:163]\n"
    "ds_read_b128 a[168:171], v241 offset:2048\n"
    "buffer_load_dwordx4 v248, s[8:11], s40 offen offset:2048 sc1 lds\n"
    "v_mfma_f32_16x16x32_bf16 v[160:163], a[172:175], a[8:11], v[160:163]\n"
    "ds_read_b128 a[172:175], v240 offset:6144\n"
    "v_mfma_f32_16x16x32_bf16 v[160:163], a[176:179], a[12:15], v[160:163]\n"
    "s_waitcnt vmcnt(9)\n"
    "ds_read2st64_b32 v[214:215], v250 offset0:18 offset1:19\n"
    "v_smfmac_f32_16x16x64_bf16 v[156:159], a[192:195], a[216:223], v152\n"
    "v_mfma_f32_16x16x32_bf16 v[160:163], a[180:183], a[16:19], v[160:163]\n"
    "v_mfma_f32_16x16x32_bf16 v[168:171], a[180:183], a[48:51], v[168:171]\n"
    "buffer_load_dwordx4 v248, s[8:11], s40 offen offset:3072 sc1 lds\n"
    "v_mfma_f32_16x16x32_bf16 v[168:171], a[176:179], a[44:47], v[168:171]\n"
    "s_add_u32 m0, s43, 36864\n"
    "v_mfma_f32_16x16x32_bf16 v[176:179], a[176:179], a[76:79], v[176:179]\n"
    "s_add_u32 s40, s40, 0x8000\n"
    "ds_read2st64_b32 v[220:221], v250 offset0:32 offset1:33\n"
    "v_mfma_f32_16x16x32_bf16 v[176:179], a[180:183], a[80:83], v[176:179]\n"
    "v_mfma_f32_16x16x32_bf16 v[184:187], a[180:183], a[112:115], v[184:187]\n"
    "v_mfma_f32_16x16x32_bf16 v[184:187], a[176:179], a[108:111], v[184:187]\n"
    "buffer_load_dwordx4 v248, s[8:11], s41 offen offset:0 sc1 lds\n"
    "v_mfma_f32_16x16x32_bf16 v[192:195], a[176:179], a[140:143], v[192:195]\n"
    "ds_read_b128 a[176:179], v241 offset:6144\n"
    "v_mfma_f32_16x16x32_bf16 v[192:195], a[180:183], a[144:147], v[192:195]\n"
    "ds_read_b128 a[180:183], v242 offset:2048\n"
    "v_mfma_f32_16x16x32_bf16 v[192:195], a[184:187], a[148:151], v[192:195]\n"
    "ds_read2st64_b32 v[222:223], v250 offset0:34 offset1:35\n"
    "v_mfma_f32_16x16x32_bf16 v[192:195], a[188:191], a[152:155], v[192:195]\n"
    "v_mfma_f32_16x16x32_bf16 v[184:187], a[188:191], a[120:123], v[184:187]\n"
    "buffer_load_dwordx4 v248, s[8:11], s41 offen offset:1024 sc1 lds\n"
    "v_mfma_f32_16x16x32_bf16 v[184:187], a[184:187], a[116:119], v[184:187]\n"
    "ds_read2st64_b32 v[228:229], v250 offset0:48 offset1:49\n"
    "v_mfma_f32_16x16x32_bf16 v[176:179], a[184:187], a[84:87], v[176:179]\n"
    "s_waitcnt vmcnt(10)\n"
    "v_exp_f32_e32 v192, v192\n"
    "v_smfmac_f32_16x16x64_bf16 v[156:159], a[192:195], a[224:231], v152\n"
    "v_exp_f32_e32 v193, v193\n"
    "v_mfma_f32_16x16x32_bf16 v[176:179], a[188:191], a[88:91], v[176:179]\n"
    "v_exp_f32_e32 v194, v194\n"
    "v_mfma_f32_16x16x32_bf16 v[168:171], a[188:191], a[56:59], v[168:171]\n"
    "buffer_load_dwordx4 v248, s[8:11], s41 offen offset:2048 sc1 lds\n"
    "v_mfma_f32_16x16x32_bf16 v[168:171], a[184:187], a[52:55], v[168:171]\n"
    "ds_read2st64_b32 v[230:231], v250 offset0:50 offset1:51\n"
    "v_mfma_f32_16x16x32_bf16 v[160:163], a[184:187], a[20:23], v[160:163]\n"
    "s_waitcnt vmcnt(9)\n"
    "ds_read_b128 a[184:187], v243 offset:2048\n"
    "v_smfmac_f32_16x16x64_bf16 v[156:159], a[192:195], a[232:239], v152\n"
    "ds_read2st64_b32 v[236:237], v250 offset0:64 offset1:65\n"
    "v_mfma_f32_16x16x32_bf16 v[160:163], a[188:191], a[24:27], v[160:163]\n"
    "s_waitcnt lgkmcnt(9)\n"
    "ds_read_b128 a[188:191], v242 offset:6144\n"
    "v_mfma_f32_16x16x32_bf16 v[164:167], a[160:163], a[28:31], 0\n"
    "buffer_load_dwordx4 v248, s[8:11], s41 offen offset:3072 sc1 lds\n"
    "v_mfma_f32_16x16x32_bf16 v[164:167], a[164:167], a[0:3], v[164:167]\n"
    "s_add_u32 s41, s41, 0x8000\n"
    "v_exp_f32_e32 v195, v195\n"
    "v_mfma_f32_16x16x32_bf16 v[172:175], a[164:167], a[32:35], 0\n"
    "s_waitcnt vmcnt(8)\n"
    "v_cvt_pk_bf16_f32 v192, v192, v193\n"
    "v_smfmac_f32_16x16x64_bf16 v[156:159], a[192:195], a[240:247], v152\n"
    "v_exp_f32_e32 v184, v184\n"
    "v_mfma_f32_16x16x32_bf16 v[172:175], a[160:163], a[60:63], v[172:175]\n"
    "ds_read2st64_b32 v[238:239], v250 offset0:66 offset1:67\n"
    "v_mfma_f32_16x16x32_bf16 v[180:183], a[160:163], a[92:95], 0\n"
    "v_exp_f32_e32 v185, v185\n"
    "v_mfma_f32_16x16x32_bf16 v[180:183], a[164:167], a[64:67], v[180:183]\n"
    "v_cvt_pk_bf16_f32 v193, v194, v195\n"
    "v_mfma_f32_16x16x32_bf16 v[188:191], a[164:167], a[96:99], 0\n"
    "v_exp_f32_e32 v186, v186\n"
    "v_mfma_f32_16x16x32_bf16 v[188:191], a[160:163], a[124:127], v[188:191]\n"
    "v_exp_f32_e32 v187, v187\n"
    "v_mfma_f32_16x16x32_bf16 v[196:199], a[160:163], a[156:159], 0\n"
    "ds_read_b64_tr_b16 a[160:161], v244 offset:49152\n"
    "v_and_b32_e32 v253, 0xffff0000, v255\n"
    "v_mfma_f32_16x16x32_bf16 v[196:199], a[164:167], a[128:131], v[196:199]\n"
    "ds_read_b64_tr_b16 a[162:163], v244 offset:51200\n"
    "v_mfma_f32_16x16x32_bf16 v[196:199], a[168:171], a[132:135], v[196:199]\n"
    "ds_read_b64_tr_b16 a[164:165], v244 offset:57344\n"
    "v_mfma_f32_16x16x32_bf16 v[196:199], a[172:175], a[136:139], v[196:199]\n"
    "ds_read_b64_tr_b16 a[166:167], v244 offset:59392\n"
    "v_mfma_f32_16x16x32_bf16 v[188:191], a[172:175], a[104:107], v[188:191]\n"
    "v_cvt_pk_bf16_f32 v184, v184, v185\n"
    "v_mfma_f32_16x16x32_bf16 v[188:191], a[168:171], a[100:103], v[188:191]\n"
    "v_exp_f32_e32 v176, v176\n"
    "v_mfma_f32_16x16x32_bf16 v[180:183], a[168:171], a[68:71], v[180:183]\n"
    "v_exp_f32_e32 v177, v177\n"
    "v_mfma_f32_16x16x32_bf16 v[180:183], a[172:175], a[72:75], v[180:183]\n"
    "v_cvt_pk_bf16_f32 v185, v186, v187\n"
    "v_mul_f32_e32 v156, v253, v156\n"
    "v_mfma_f32_16x16x32_bf16 v[172:175], a[172:175], a[40:43], v[172:175]\n"
    "v_exp_f32_e32 v178, v178\n"
    "v_mfma_f32_16x16x32_bf16 v[172:175], a[168:171], a[36:39], v[172:175]\n"
    "v_exp_f32_e32 v179, v179\n"
    "v_mfma_f32_16x16x32_bf16 v[164:167], a[168:171], a[4:7], v[164:167]\n"
    "ds_read_b64_tr_b16 a[168:169], v245 offset:49152\n"
    "v_mfma_f32_16x16x32_bf16 v[164:167], a[172:175], a[8:11], v[164:167]\n"
    "s_waitcnt lgkmcnt(6)\n"
    "ds_read_b64_tr_b16 a[170:171], v245 offset:51200\n"
    "v_mfma_f32_16x16x32_bf16 v[164:167], a[176:179], a[12:15], v[164:167]\n"
    "ds_read_b64_tr_b16 a[172:173], v245 offset:57344\n"
    "v_mul_f32_e32 v157, v253, v157\n"
    "v_mfma_f32_16x16x32_bf16 v[164:167], a[180:183], a[16:19], v[164:167]\n"
    "ds_read_b64_tr_b16 a[174:175], v245 offset:59392\n"
    "v_mfma_f32_16x16x32_bf16 v[172:175], a[180:183], a[48:51], v[172:175]\n"
    "v_cvt_pk_bf16_f32 v176, v176, v177\n"
    "v_mfma_f32_16x16x32_bf16 v[172:175], a[176:179], a[44:47], v[172:175]\n"
    "v_exp_f32_e32 v168, v168\n"
    "v_mfma_f32_16x16x32_bf16 v[180:183], a[176:179], a[76:79], v[180:183]\n"
    "v_mul_f32_e32 v158, v253, v158\n"
    "v_mfma_f32_16x16x32_bf16 v[180:183], a[180:183], a[80:83], v[180:183]\n"
    "v_exp_f32_e32 v169, v169\n"
    "v_mfma_f32_16x16x32_bf16 v[188:191], a[180:183], a[112:115], v[188:191]\n"
    "v_cvt_pk_bf16_f32 v177, v178, v179\n"
    "v_mfma_f32_16x16x32_bf16 v[188:191], a[176:179], a[108:111], v[188:191]\n"
    "v_exp_f32_e32 v170, v170\n"
    "v_mfma_f32_16x16x32_bf16 v[196:199], a[176:179], a[140:143], v[196:199]\n"
    "ds_read_b64_tr_b16 a[176:177], v246 offset:49152\n"
    "v_mfma_f32_16x16x32_bf16 v[196:199], a[180:183], a[144:147], v[196:199]\n"
    "ds_read_b64_tr_b16 a[178:179], v246 offset:51200\n"
    "v_mfma_f32_16x16x32_bf16 v[196:199], a[184:187], a[148:151], v[196:199]\n"
    "ds_read_b64_tr_b16 a[180:181], v246 offset:57344\n"
    "v_mfma_f32_16x16x32_bf16 v[196:199], a[188:191], a[152:155], v[196:199]\n"
    "ds_read_b64_tr_b16 a[182:183], v246 offset:59392\n"
    "v_mul_f32_e32 v159, v253, v159\n"
    "v_mfma_f32_16x16x32_bf16 v[188:191], a[188:191], a[120:123], v[188:191]\n"
    "v_exp_f32_e32 v171, v171\n"
    "v_mfma_f32_16x16x32_bf16 v[188:191], a[184:187], a[116:119], v[188:191]\n"
    "v_cvt_pk_bf16_f32 v168, v168, v169\n"
    "v_mfma_f32_16x16x32_bf16 v[180:183], a[184:187], a[84:87], v[180:183]\n"
    "v_exp_f32_e32 v160, v160\n"
    "v_mfma_f32_16x16x32_bf16 v[180:183], a[188:191], a[88:91], v[180:183]\n"
    "v_exp_f32_e32 v161, v161\n"
    "v_mfma_f32_16x16x32_bf16 v[172:175], a[188:191], a[56:59], v[172:175]\n"
    "v_cvt_pk_bf16_f32 v169, v170, v171\n"
    "v_mfma_f32_16x16x32_bf16 v[172:175], a[184:187], a[52:55], v[172:175]\n"
    "v_exp_f32_e32 v162, v162\n"
    "v_mfma_f32_16x16x32_bf16 v[164:167], a[184:187], a[20:23], v[164:167]\n"
    "ds_read_b64_tr_b16 a[184:185], v247 offset:49152\n"
    "v_mad_u32_u16 v253, v255, s42, v153 op_sel:[0,0,0,0]\n"
    "v_mfma_f32_16x16x32_bf16 v[164:167], a[188:191], a[24:27], v[164:167]\n"
    "s_waitcnt lgkmcnt(5)\n"
    "ds_read_b64_tr_b16 a[186:187], v247 offset:51200\n"
    "v_mfma_f32_16x16x32_bf16 v[0:3], a[160:163], v[200:203], v[0:3]\n"
    "ds_read_b64_tr_b16 a[188:189], v247 offset:57344\n"
    "v_mfma_f32_16x16x32_bf16 v[0:3], a[164:167], v[204:207], v[0:3]\n"
    "ds_read_b64_tr_b16 a[190:191], v247 offset:59392\n"
    "v_mfma_f32_16x16x32_bf16 a[248:251], a[164:167], v[212:215], a[248:251]\n"
    "v_exp_f32_e32 v163, v163\n"
    "v_mfma_f32_16x16x32_bf16 a[248:251], a[160:163], v[208:211], a[248:251]\n"
    "v_cvt_pk_bf16_f32 v160, v160, v161\n"
    "v_mfma_f32_16x16x32_bf16 v[56:59], a[160:163], v[216:219], v[56:59]\n"
    "v_cvt_pk_bf16_f32 v154, v156, v157\n"
    "v_mfma_f32_16x16x32_bf16 v[56:59], a[164:167], v[220:223], v[56:59]\n"
    "v_cvt_pk_bf16_f32 v161, v162, v163\n"
    "v_mfma_f32_16x16x32_bf16 v[60:63], a[164:167], v[228:231], v[60:63]\n"
    "v_exp_f32_e32 v196, v196\n"
    "v_mfma_f32_16x16x32_bf16 v[60:63], a[160:163], v[224:227], v[60:63]\n"
    "v_exp_f32_e32 v197, v197\n"
    "v_mfma_f32_16x16x32_bf16 v[64:67], a[160:163], v[232:235], v[64:67]\n"
    "ds_read_b64_tr_b16 a[160:161], v244 offset:53248\n"
    "v_mfma_f32_16x16x32_bf16 v[64:67], a[164:167], v[236:239], v[64:67]\n"
    "ds_read_b64_tr_b16 a[162:163], v244 offset:55296\n"
    "v_cvt_pk_bf16_f32 v155, v158, v159\n"
    "v_mfma_f32_16x16x32_bf16 v[76:79], a[168:171], v[232:235], v[76:79]\n"
    "ds_read_b64_tr_b16 a[164:165], v244 offset:61440\n"
    "v_mfma_f32_16x16x32_bf16 v[76:79], a[172:175], v[236:239], v[76:79]\n"
    "ds_read_b64_tr_b16 a[166:167], v244 offset:63488\n"
    "v_mfma_f32_16x16x32_bf16 v[72:75], a[172:175], v[228:231], v[72:75]\n"
    "v_exp_f32_e32 v198, v198\n"
    "v_mfma_f32_16x16x32_bf16 v[72:75], a[168:171], v[224:227], v[72:75]\n"
    "v_exp_f32_e32 v199, v199\n"
    "v_mfma_f32_16x16x32_bf16 v[68:71], a[168:171], v[216:219], v[68:71]\n"
    "v_cvt_pk_bf16_f32 v194, v196, v197\n"
    "v_mov_b32_e32 v156, 0\n"
    "v_mfma_f32_16x16x32_bf16 v[68:71], a[172:175], v[220:223], v[68:71]\n"
    "v_exp_f32_e32 v188, v188\n"
    "v_mfma_f32_16x16x32_bf16 a[252:255], a[172:175], v[212:215], a[252:255]\n"
    "v_exp_f32_e32 v189, v189\n"
    "v_mfma_f32_16x16x32_bf16 a[252:255], a[168:171], v[208:211], a[252:255]\n"
    "v_cvt_pk_bf16_f32 v195, v198, v199\n"
    "v_mfma_f32_16x16x32_bf16 v[4:7], a[168:171], v[200:203], v[4:7]\n"
    "ds_read_b64_tr_b16 a[168:169], v245 offset:53248\n"
    "v_mfma_f32_16x16x32_bf16 v[4:7], a[172:175], v[204:207], v[4:7]\n"
    "s_waitcnt lgkmcnt(5)\n"
    "ds_read_b64_tr_b16 a[170:171], v245 offset:55296\n"
    "v_mfma_f32_16x16x32_bf16 v[8:11], a[176:179], v[200:203], v[8:11]\n"
    "ds_read_b64_tr_b16 a[172:173], v245 offset:61440\n"
    "v_mfma_f32_16x16x32_bf16 v[8:11], a[180:183], v[204:207], v[8:11]\n"
    "ds_read_b64_tr_b16 a[174:175], v245 offset:63488\n"
    "v_mov_b32_e32 v157, 0\n"
    "v_mfma_f32_16x16x32_bf16 v[32:35], a[180:183], v[212:215], v[32:35]\n"
    "v_exp_f32_e32 v190, v190\n"
    "v_mfma_f32_16x16x32_bf16 v[32:35], a[176:179], v[208:211], v[32:35]\n"
    "v_exp_f32_e32 v191, v191\n"
    "v_mfma_f32_16x16x32_bf16 v[80:83], a[176:179], v[216:219], v[80:83]\n"
    "v_mov_b32_e32 v158, 0\n"
    "v_mfma_f32_16x16x32_bf16 v[80:83], a[180:183], v[220:223], v[80:83]\n"
    "v_cvt_pk_bf16_f32 v186, v188, v189\n"
    "v_mfma_f32_16x16x32_bf16 v[84:87], a[180:183], v[228:231], v[84:87]\n"
    "v_exp_f32_e32 v180, v180\n"
    "v_mfma_f32_16x16x32_bf16 v[84:87], a[176:179], v[224:227], v[84:87]\n"
    "v_exp_f32_e32 v181, v181\n"
    "v_mfma_f32_16x16x32_bf16 v[88:91], a[176:179], v[232:235], v[88:91]\n"
    "ds_read_b64_tr_b16 a[176:177], v246 offset:53248\n"
    "v_mfma_f32_16x16x32_bf16 v[88:91], a[180:183], v[236:239], v[88:91]\n"
    "ds_read_b64_tr_b16 a[178:179], v246 offset:55296\n"
    "v_mfma_f32_16x16x32_bf16 v[100:103], a[184:187], v[232:235], v[100:103]\n"
    "ds_read_b64_tr_b16 a[180:181], v246 offset:61440\n"
    "v_mfma_f32_16x16x32_bf16 v[100:103], a[188:191], v[236:239], v[100:103]\n"
    "ds_read_b64_tr_b16 a[182:183], v246 offset:63488\n"
    "v_mfma_f32_16x16x32_bf16 v[96:99], a[188:191], v[228:231], v[96:99]\n"
    "v_cvt_pk_bf16_f32 v187, v190, v191\n"
    "v_mov_b32_e32 v159, 0\n"
    "v_mfma_f32_16x16x32_bf16 v[96:99], a[184:187], v[224:227], v[96:99]\n"
    "v_exp_f32_e32 v182, v182\n"
    "v_mfma_f32_16x16x32_bf16 v[92:95], a[184:187], v[216:219], v[92:95]\n"
    "v_exp_f32_e32 v183, v183\n"
    "v_mfma_f32_16x16x32_bf16 v[92:95], a[188:191], v[220:223], v[92:95]\n"
    "v_cvt_pk_bf16_f32 v178, v180, v181\n"
    "v_mfma_f32_16x16x32_bf16 v[36:39], a[188:191], v[212:215], v[36:39]\n"
    "v_exp_f32_e32 v172, v172\n"
    "v_mfma_f32_16x16x32_bf16 v[36:39], a[184:187], v[208:211], v[36:39]\n"
    "v_exp_f32_e32 v173, v173\n"
    "v_mfma_f32_16x16x32_bf16 v[12:15], a[184:187], v[200:203], v[12:15]\n"
    "s_mov_b32 m0, s49\n"
    "ds_read_b64_tr_b16 a[184:185], v247 offset:53248\n"
    "v_mfma_f32_16x16x32_bf16 v[12:15], a[188:191], v[204:207], v[12:15]\n"
    "s_waitcnt lgkmcnt(5)\n"
    "ds_read_b64_tr_b16 a[186:187], v247 offset:55296\n"
    "v_add_u32_e32 v252, 0x80, v252\n"
    "v_mfma_f32_16x16x32_bf16 v[16:19], a[160:163], v[200:203], v[16:19]\n"
    "ds_read_b64_tr_b16 a[188:189], v247 offset:61440\n"
    "v_mfma_f32_16x16x32_bf16 v[16:19], a[164:167], v[204:207], v[16:19]\n"
    "ds_read_b64_tr_b16 a[190:191], v247 offset:63488\n"
    "v_mfma_f32_16x16x32_bf16 v[40:43], a[164:167], v[212:215], v[40:43]\n"
    "buffer_store_dwordx2 v[154:155], v253, s[24:27], 0 offen offset:0 sc1\n"
    "v_cvt_pk_bf16_f32 v179, v182, v183\n"
    "v_mfma_f32_16x16x32_bf16 v[40:43], a[160:163], v[208:211], v[40:43]\n"
    "v_exp_f32_e32 v174, v174\n"
    "v_mfma_f32_16x16x32_bf16 v[104:107], a[160:163], v[216:219], v[104:107]\n"
    "v_exp_f32_e32 v175, v175\n"
    "v_mfma_f32_16x16x32_bf16 v[104:107], a[164:167], v[220:223], v[104:107]\n"
    "s_waitcnt vmcnt(9)\n"
    "v_mfma_f32_16x16x32_bf16 v[108:111], a[164:167], v[228:231], v[108:111]\n"
    "s_barrier\n"
    "v_mfma_f32_16x16x32_bf16 v[108:111], a[160:163], v[224:227], v[108:111]\n"
    "ds_write_addtid_b32 v192 offset:16384\n"
    "v_cvt_pk_bf16_f32 v170, v172, v173\n"
    "v_mfma_f32_16x16x32_bf16 v[112:115], a[160:163], v[232:235], v[112:115]\n"
    "ds_read_b128 a[160:163], v243 offset:20480\n"
    "v_mfma_f32_16x16x32_bf16 v[112:115], a[164:167], v[236:239], v[112:115]\n"
    "ds_read_b128 a[164:167], v240 offset:16384\n"
    "v_mfma_f32_16x16x32_bf16 v[124:127], a[168:171], v[232:235], v[124:127]\n"
    "v_exp_f32_e32 v164, v164\n"
    "v_mfma_f32_16x16x32_bf16 v[124:127], a[172:175], v[236:239], v[124:127]\n"
    "v_exp_f32_e32 v165, v165\n"
    "v_mfma_f32_16x16x32_bf16 v[120:123], a[172:175], v[228:231], v[120:123]\n"
    "ds_write_addtid_b32 v193 offset:16640\n"
    "v_cvt_pk_bf16_f32 v171, v174, v175\n"
    "v_mfma_f32_16x16x32_bf16 v[120:123], a[168:171], v[224:227], v[120:123]\n"
    "ds_write_addtid_b32 v184 offset:12288\n"
    "v_mfma_f32_16x16x32_bf16 v[116:119], a[168:171], v[216:219], v[116:119]\n"
    "v_exp_f32_e32 v166, v166\n"
    "v_mfma_f32_16x16x32_bf16 v[116:119], a[172:175], v[220:223], v[116:119]\n"
    "v_exp_f32_e32 v167, v167\n"
    "v_mfma_f32_16x16x32_bf16 v[44:47], a[172:175], v[212:215], v[44:47]\n"
    "ds_write_addtid_b32 v185 offset:12544\n"
    "v_cvt_pk_bf16_f32 v162, v164, v165\n"
    "v_mfma_f32_16x16x32_bf16 v[44:47], a[168:171], v[208:211], v[44:47]\n"
    "v_cvt_pk_bf16_f32 v163, v166, v167\n"
    "v_mfma_f32_16x16x32_bf16 v[20:23], a[168:171], v[200:203], v[20:23]\n"
    "ds_read_b128 a[168:171], v241 offset:16384\n"
    "v_mfma_f32_16x16x32_bf16 v[20:23], a[172:175], v[204:207], v[20:23]\n"
    "s_waitcnt lgkmcnt(7)\n"
    "ds_read_b128 a[172:175], v240 offset:20480\n"
    "v_mfma_f32_16x16x32_bf16 v[24:27], a[176:179], v[200:203], v[24:27]\n"
    "ds_write_addtid_b32 v176 offset:8192\n"
    "v_mfma_f32_16x16x32_bf16 v[24:27], a[180:183], v[204:207], v[24:27]\n"
    "ds_write_addtid_b32 v177 offset:8448\n"
    "v_mfma_f32_16x16x32_bf16 v[48:51], a[180:183], v[212:215], v[48:51]\n"
    "ds_write_addtid_b32 v168 offset:4096\n"
    "v_mfma_f32_16x16x32_bf16 v[48:51], a[176:179], v[208:211], v[48:51]\n"
    "ds_write_addtid_b32 v169 offset:4352\n"
    "v_mfma_f32_16x16x32_bf16 v[128:131], a[176:179], v[216:219], v[128:131]\n"
    "ds_write_addtid_b32 v160 offset:0\n"
    "v_mfma_f32_16x16x32_bf16 v[128:131], a[180:183], v[220:223], v[128:131]\n"
    "ds_write_addtid_b32 v161 offset:256\n"
    "v_mfma_f32_16x16x32_bf16 v[132:135], a[180:183], v[228:231], v[132:135]\n"
    "ds_write_addtid_b32 v194 offset:16896\n"
    "v_mfma_f32_16x16x32_bf16 v[132:135], a[176:179], v[224:227], v[132:135]\n"
    "ds_write_addtid_b32 v195 offset:17152\n"
    "v_mfma_f32_16x16x32_bf16 v[136:139], a[176:179], v[232:235], v[136:139]\n"
    "ds_read_b128 a[176:179], v241 offset:20480\n"
    "v_mfma_f32_16x16x32_bf16 v[136:139], a[180:183], v[236:239], v[136:139]\n"
    "ds_read_b128 a[180:183], v242 offset:16384\n"
    "v_mfma_f32_16x16x32_bf16 v[148:151], a[184:187], v[232:235], v[148:151]\n"
    "ds_write_addtid_b32 v186 offset:12800\n"
    "v_mfma_f32_16x16x32_bf16 v[148:151], a[188:191], v[236:239], v[148:151]\n"
    "ds_write_addtid_b32 v187 offset:13056\n"
    "v_mfma_f32_16x16x32_bf16 v[144:147], a[188:191], v[228:231], v[144:147]\n"
    "ds_write_addtid_b32 v178 offset:8704\n"
    "v_mfma_f32_16x16x32_bf16 v[144:147], a[184:187], v[224:227], v[144:147]\n"
    "ds_write_addtid_b32 v179 offset:8960\n"
    "v_mfma_f32_16x16x32_bf16 v[140:143], a[184:187], v[216:219], v[140:143]\n"
    "s_sub_u32 s44, s44, 1\n"
    "ds_write_addtid_b32 v170 offset:4608\n"
    "v_mfma_f32_16x16x32_bf16 v[140:143], a[188:191], v[220:223], v[140:143]\n"
    "ds_write_addtid_b32 v171 offset:4864\n"
    "v_mfma_f32_16x16x32_bf16 v[52:55], a[188:191], v[212:215], v[52:55]\n"
    "ds_write_addtid_b32 v162 offset:512\n"
    "v_mfma_f32_16x16x32_bf16 v[52:55], a[184:187], v[208:211], v[52:55]\n"
    "ds_write_addtid_b32 v163 offset:768\n"
    "v_mfma_f32_16x16x32_bf16 v[28:31], a[184:187], v[200:203], v[28:31]\n"
    "ds_read_b128 a[184:187], v243 offset:16384\n"
    "v_mfma_f32_16x16x32_bf16 v[28:31], a[188:191], v[204:207], v[28:31]\n"
    "s_waitcnt lgkmcnt(15)\n"
    "ds_read_b128 a[188:191], v242 offset:20480\n"
    "s_cbranch_scc0 .Lat_g\n"
    "s_mov_b32 s44, 1\n"
    "s_bitcmp0_b32 %1, 0\n"
    "s_cbranch_scc1 .Lat_p\n"
    ".Lat_tail:\n"
    "v_mfma_f32_16x16x32_bf16 v[216:219], a[160:163], a[92:95], 0\n"
    "s_mov_b32 s45, 5775360\n"
    "v_mbcnt_lo_u32_b32 v253, -1, 0\n"
    "v_mfma_f32_16x16x32_bf16 v[216:219], a[164:167], a[64:67], v[216:219]\n"
    "s_add_u32 m0, s43, 49152\n"
    "v_mbcnt_hi_u32_b32 v253, -1, v253\n"
    "v_mfma_f32_16x16x32_bf16 v[224:227], a[164:167], a[96:99], 0\n"
    "buffer_load_dwordx4 v248, s[8:11], s40 offen offset:0 sc1 lds\n"
    "v_mfma_f32_16x16x32_bf16 v[224:227], a[160:163], a[124:127], v[224:227]\n"
    "s_lshl_b32 s56, %2, 15\n"
    "v_and_b32_e32 v254, 3, v253\n"
    "v_mfma_f32_16x16x32_bf16 v[232:235], a[160:163], a[156:159], 0\n"
    "ds_read_b128 a[160:163], v243 offset:22528\n"
    "v_mfma_f32_16x16x32_bf16 v[232:235], a[164:167], a[128:131], v[232:235]\n"
    "s_waitcnt lgkmcnt(1)\n"
    "ds_read_b128 a[164:167], v240 offset:18432\n"
    "v_mfma_f32_16x16x32_bf16 v[232:235], a[168:171], a[132:135], v[232:235]\n"
    "s_barrier\n"
    "v_mfma_f32_16x16x32_bf16 v[232:235], a[172:175], a[136:139], v[232:235]\n"
    "buffer_load_dwordx4 v248, s[8:11], s40 offen offset:1024 sc1 lds\n"
    "v_mfma_f32_16x16x32_bf16 v[224:227], a[172:175], a[104:107], v[224:227]\n"
    "s_add_u32 s57, s56, 5505024\n"
    "v_lshlrev_b32_e32 v254, 2, v254\n"
    "v_mfma_f32_16x16x32_bf16 v[224:227], a[168:171], a[100:103], v[224:227]\n"
    "s_add_u32 s56, s56, 5373952\n"
    "ds_read2st64_b32 v[164:165], v250 offset0:0 offset1:1\n"
    "v_and_b32_e32 v158, 15, v253\n"
    "v_mfma_f32_16x16x32_bf16 v[216:219], a[168:171], a[68:71], v[216:219]\n"
    "ds_read_b128 a[168:171], v241 offset:18432\n"
    "v_mfma_f32_16x16x32_bf16 v[216:219], a[172:175], a[72:75], v[216:219]\n"
    "ds_read_b128 a[172:175], v240 offset:22528\n"
    "v_mfma_f32_16x16x32_bf16 v[216:219], a[176:179], a[76:79], v[216:219]\n"
    "buffer_load_dwordx4 v248, s[8:11], s40 offen offset:2048 sc1 lds\n"
    "ds_read2st64_b32 v[166:167], v250 offset0:2 offset1:3\n"
    "v_mfma_f32_16x16x32_bf16 v[216:219], a[180:183], a[80:83], v[216:219]\n"
    "s_lshl_b32 s47, %2, 7\n"
    "v_lshlrev_b32_e32 v158, 9, v158\n"
    "v_mfma_f32_16x16x32_bf16 v[224:227], a[180:183], a[112:115], v[224:227]\n"
    "s_add_u32 s47, s47, s35\n"
    "v_and_or_b32 v158, v253, 48, v158\n"
    "v_mfma_f32_16x16x32_bf16 v[224:227], a[176:179], a[108:111], v[224:227]\n"
    "ds_read2st64_b32 v[172:173], v250 offset0:16 offset1:17\n"
    "v_bfe_u32 v252, v253, 4, 2\n"
    "v_mfma_f32_16x16x32_bf16 v[232:235], a[176:179], a[140:143], v[232:235]\n"
    "ds_read_b128 a[176:179], v241 offset:22528\n"
    "buffer_load_dwordx4 v248, s[8:11], s40 offen offset:3072 sc1 lds\n"
    "v_mfma_f32_16x16x32_bf16 v[232:235], a[180:183], a[144:147], v[232:235]\n"
    "s_add_u32 s40, s40, 32768\n"
    "ds_read_b128 a[180:183], v242 offset:18432\n"
    "v_mfma_f32_16x16x32_bf16 v[232:235], a[184:187], a[148:151], v[232:235]\n"
    "s_add_u32 m0, s43, 53248\n"
    "v_lshlrev_b32_e32 v252, 1, v252\n"
    "v_mfma_f32_16x16x32_bf16 v[232:235], a[188:191], a[152:155], v[232:235]\n"
    "ds_read2st64_b32 v[174:175], v250 offset0:18 offset1:19\n"
    "v_xor_b32_e32 v252, v252, v253\n"
    "v_mfma_f32_16x16x32_bf16 v[224:227], a[188:191], a[120:123], v[224:227]\n"
    "buffer_load_dwordx4 v248, s[8:11], s41 offen offset:0 sc1 lds\n"
    "v_mfma_f32_16x16x32_bf16 v[224:227], a[184:187], a[116:119], v[224:227]\n"
    "v_and_b32_e32 v252, 7, v252\n"
    "v_mfma_f32_16x16x32_bf16 v[216:219], a[184:187], a[84:87], v[216:219]\n"
    "ds_read_b128 a[184:187], v243 offset:18432\n"
    "v_mfma_f32_16x16x32_bf16 v[216:219], a[188:191], a[88:91], v[216:219]\n"
    "s_waitcnt lgkmcnt(9)\n"
    "ds_read_b128 a[188:191], v242 offset:22528\n"
    "v_mfma_f32_16x16x32_bf16 v[220:223], a[160:163], a[92:95], 0\n"
    "buffer_load_dwordx4 v248, s[8:11], s41 offen offset:1024 sc1 lds\n"
    "ds_read2st64_b32 v[180:181], v250 offset0:32 offset1:33\n"
    "v_mfma_f32_16x16x32_bf16 v[220:223], a[164:167], a[64:67], v[220:223]\n"
    "v_lshlrev_b32_e32 v252, 4, v252\n"
    "v_mfma_f32_16x16x32_bf16 v[228:231], a[164:167], a[96:99], 0\n"
    "ds_read2st64_b32 v[182:183], v250 offset0:34 offset1:35\n"
    "v_lshrrev_b32_e32 v255, 3, v253\n"
    "v_mfma_f32_16x16x32_bf16 v[228:231], a[160:163], a[124:127], v[228:231]\n"
    "ds_read2st64_b32 v[188:189], v250 offset0:48 offset1:49\n"
    "v_lshl_add_u32 v252, v255, 9, v252\n"
    "v_mfma_f32_16x16x32_bf16 v[236:239], a[160:163], a[156:159], 0\n"
    "ds_read_b64_tr_b16 a[160:161], v244 offset:0\n"
    "buffer_load_dwordx4 v248, s[8:11], s41 offen offset:2048 sc1 lds\n"
    "v_mfma_f32_16x16x32_bf16 v[236:239], a[164:167], a[128:131], v[236:239]\n"
    "s_waitcnt lgkmcnt(11)\n"
    "ds_read_b64_tr_b16 a[162:163], v244 offset:2048\n"
    "v_mfma_f32_16x16x32_bf16 v[236:239], a[168:171], a[132:135], v[236:239]\n"
    "ds_read_b64_tr_b16 a[164:165], v244 offset:8192\n"
    "v_mfma_f32_16x16x32_bf16 v[236:239], a[172:175], a[136:139], v[236:239]\n"
    "ds_read_b64_tr_b16 a[166:167], v244 offset:10240\n"
    "v_mfma_f32_16x16x32_bf16 v[228:231], a[172:175], a[104:107], v[228:231]\n"
    "buffer_load_dwordx4 v248, s[8:11], s41 offen offset:3072 sc1 lds\n"
    "ds_read2st64_b32 v[190:191], v250 offset0:50 offset1:51\n"
    "v_mfma_f32_16x16x32_bf16 v[228:231], a[168:171], a[100:103], v[228:231]\n"
    "s_add_u32 s41, s41, 32768\n"
    "s_add_u32 s61, s47, 0\n"
    "v_lshlrev_b32_e32 v253, 4, v253\n"
    "v_mfma_f32_16x16x32_bf16 v[220:223], a[168:171], a[68:71], v[220:223]\n"
    "s_mov_b32 m0, s49\n"
    "ds_read_b64_tr_b16 a[168:169], v245 offset:0\n"
    "v_mfma_f32_16x16x32_bf16 v[220:223], a[172:175], a[72:75], v[220:223]\n"
    "s_waitcnt lgkmcnt(12)\n"
    "ds_read_b64_tr_b16 a[170:171], v245 offset:2048\n"
    "v_mfma_f32_16x16x32_bf16 v[220:223], a[176:179], a[76:79], v[220:223]\n"
    "ds_read_b64_tr_b16 a[172:173], v245 offset:8192\n"
    "buffer_load_dwordx4 a[224:227], v158, s[12:15], s61 offen offset:0\n"
    "v_mfma_f32_16x16x32_bf16 v[220:223], a[180:183], a[80:83], v[220:223]\n"
    "ds_read_b64_tr_b16 a[174:175], v245 offset:10240\n"
    "v_mfma_f32_16x16x32_bf16 v[228:231], a[180:183], a[112:115], v[228:231]\n"
    "ds_read2st64_b32 v[196:197], v250 offset0:64 offset1:65\n"
    "v_mfma_f32_16x16x32_bf16 v[228:231], a[176:179], a[108:111], v[228:231]\n"
    "ds_read2st64_b32 v[198:199], v250 offset0:66 offset1:67\n"
    "v_mfma_f32_16x16x32_bf16 v[236:239], a[176:179], a[140:143], v[236:239]\n"
    "ds_read_b64_tr_b16 a[176:177], v246 offset:0\n"
    "v_mfma_f32_16x16x32_bf16 v[236:239], a[180:183], a[144:147], v[236:239]\n"
    "s_waitcnt lgkmcnt(15)\n"
    "ds_read_b64_tr_b16 a[178:179], v246 offset:2048\n"
    "v_mfma_f32_16x16x32_bf16 v[236:239], a[184:187], a[148:151], v[236:239]\n"
    "ds_read_b64_tr_b16 a[180:181], v246 offset:8192\n"
    "v_mfma_f32_16x16x32_bf16 v[236:239], a[188:191], a[152:155], v[236:239]\n"
    "ds_read_b64_tr_b16 a[182:183], v246 offset:10240\n"
    "v_mfma_f32_16x16x32_bf16 v[228:231], a[188:191], a[120:123], v[228:231]\n"
    "v_exp_f32_e32 v232, v232\n"
    "v_mfma_f32_16x16x32_bf16 v[228:231], a[184:187], a[116:119], v[228:231]\n"
    "v_exp_f32_e32 v233, v233\n"
    "v_mfma_f32_16x16x32_bf16 v[220:223], a[184:187], a[84:87], v[220:223]\n"
    "ds_read_b64_tr_b16 a[184:185], v247 offset:0\n"
    "v_mfma_f32_16x16x32_bf16 v[220:223], a[188:191], a[88:91], v[220:223]\n"
    "s_waitcnt lgkmcnt(5)\n"
    "ds_read_b64_tr_b16 a[186:187], v247 offset:2048\n"
    "v_mfma_f32_16x16x32_bf16 v[0:3], a[160:163], v[160:163], v[0:3]\n"
    "ds_read_b64_tr_b16 a[188:189], v247 offset:8192\n"
    "buffer_load_dwordx4 a[228:231], v158, s[12:15], s61 offen offset:64\n"
    "v_mfma_f32_16x16x32_bf16 v[0:3], a[164:167], v[164:167], v[0:3]\n"
    "s_add_u32 s61, s47, 8192\n"
    "ds_read_b64_tr_b16 a[190:191], v247 offset:10240\n"
    "v_mfma_f32_16x16x32_bf16 a[248:251], a[164:167], v[172:175], a[248:251]\n"
    "v_exp_f32_e32 v234, v234\n"
    "v_mfma_f32_16x16x32_bf16 a[248:251], a[160:163], v[168:171], a[248:251]\n"
    "v_exp_f32_e32 v235, v235\n"
    "v_mfma_f32_16x16x32_bf16 v[56:59], a[160:163], v[176:179], v[56:59]\n"
    "v_cvt_pk_bf16_f32 v232, v232, v233\n"
    "v_mfma_f32_16x16x32_bf16 v[56:59], a[164:167], v[180:183], v[56:59]\n"
    "v_exp_f32_e32 v224, v224\n"
    "v_mfma_f32_16x16x32_bf16 v[60:63], a[164:167], v[188:191], v[60:63]\n"
    "v_exp_f32_e32 v225, v225\n"
    "v_mfma_f32_16x16x32_bf16 v[60:63], a[160:163], v[184:187], v[60:63]\n"
    "v_cvt_pk_bf16_f32 v233, v234, v235\n"
    "v_mfma_f32_16x16x32_bf16 v[64:67], a[160:163], v[192:195], v[64:67]\n"
    "ds_read_b64_tr_b16 a[160:161], v244 offset:4096\n"
    "v_mfma_f32_16x16x32_bf16 v[64:67], a[164:167], v[196:199], v[64:67]\n"
    "ds_read_b64_tr_b16 a[162:163], v244 offset:6144\n"
    "v_mfma_f32_16x16x32_bf16 v[76:79], a[168:171], v[192:195], v[76:79]\n"
    "ds_read_b64_tr_b16 a[164:165], v244 offset:12288\n"
    "v_mfma_f32_16x16x32_bf16 v[76:79], a[172:175], v[196:199], v[76:79]\n"
    "ds_read_b64_tr_b16 a[166:167], v244 offset:14336\n"
    "v_mfma_f32_16x16x32_bf16 v[72:75], a[172:175], v[188:191], v[72:75]\n"
    "buffer_load_dwordx4 a[232:235], v158, s[12:15], s61 offen offset:0\n"
    "v_mfma_f32_16x16x32_bf16 v[72:75], a[168:171], v[184:187], v[72:75]\n"
    "s_waitcnt vmcnt(11)\n"
    "v_exp_f32_e32 v226, v226\n"
    "v_mfma_f32_16x16x32_bf16 v[68:71], a[168:171], v[176:179], v[68:71]\n"
    "s_barrier\n"
    "v_mfma_f32_16x16x32_bf16 v[68:71], a[172:175], v[180:183], v[68:71]\n"
    "v_exp_f32_e32 v227, v227\n"
    "v_mfma_f32_16x16x32_bf16 a[252:255], a[172:175], v[172:175], a[252:255]\n"
    "v_cvt_pk_bf16_f32 v224, v224, v225\n"
    "v_mfma_f32_16x16x32_bf16 a[252:255], a[168:171], v[168:171], a[252:255]\n"
    "v_exp_f32_e32 v216, v216\n"
    "v_mfma_f32_16x16x32_bf16 v[4:7], a[168:171], v[160:163], v[4:7]\n"
    "ds_read_b64_tr_b16 a[168:169], v245 offset:4096\n"
    "v_mfma_f32_16x16x32_bf16 v[4:7], a[172:175], v[164:167], v[4:7]\n"
    "s_waitcnt lgkmcnt(9)\n"
    "ds_read_b64_tr_b16 a[170:171], v245 offset:6144\n"
    "v_mfma_f32_16x16x32_bf16 v[8:11], a[176:179], v[160:163], v[8:11]\n"
    "ds_read_b64_tr_b16 a[172:173], v245 offset:12288\n"
    "v_mfma_f32_16x16x32_bf16 v[8:11], a[180:183], v[164:167], v[8:11]\n"
    "ds_read_b64_tr_b16 a[174:175], v245 offset:14336\n"
    "v_mfma_f32_16x16x32_bf16 v[32:35], a[180:183], v[172:175], v[32:35]\n"
    "v_exp_f32_e32 v217, v217\n"
    "v_mfma_f32_16x16x32_bf16 v[32:35], a[176:179], v[168:171], v[32:35]\n"
    "v_cvt_pk_bf16_f32 v225, v226, v227\n"
    "v_mfma_f32_16x16x32_bf16 v[80:83], a[176:179], v[176:179], v[80:83]\n"
    "buffer_load_dwordx4 a[236:239], v158, s[12:15], s61 offen offset:64\n"
    "v_mfma_f32_16x16x32_bf16 v[80:83], a[180:183], v[180:183], v[80:83]\n"
    "s_add_u32 s61, s47, 16384\n"
    "v_exp_f32_e32 v218, v218\n"
    "v_mfma_f32_16x16x32_bf16 v[84:87], a[180:183], v[188:191], v[84:87]\n"
    "v_exp_f32_e32 v219, v219\n"
    "v_mfma_f32_16x16x32_bf16 v[84:87], a[176:179], v[184:187], v[84:87]\n"
    "v_cvt_pk_bf16_f32 v216, v216, v217\n"
    "v_mfma_f32_16x16x32_bf16 v[88:91], a[176:179], v[192:195], v[88:91]\n"
    "ds_read_b64_tr_b16 a[176:177], v246 offset:4096\n"
    "v_mfma_f32_16x16x32_bf16 v[88:91], a[180:183], v[196:199], v[88:91]\n"
    "s_waitcnt lgkmcnt(9)\n"
    "ds_read_b64_tr_b16 a[178:179], v246 offset:6144\n"
    "v_mfma_f32_16x16x32_bf16 v[100:103], a[184:187], v[192:195], v[100:103]\n"
    "ds_read_b64_tr_b16 a[180:181], v246 offset:12288\n"
    "v_mfma_f32_16x16x32_bf16 v[100:103], a[188:191], v[196:199], v[100:103]\n"
    "ds_read_b64_tr_b16 a[182:183], v246 offset:14336\n"
    "v_mfma_f32_16x16x32_bf16 v[96:99], a[188:191], v[188:191], v[96:99]\n"
    "v_cvt_pk_bf16_f32 v217, v218, v219\n"
    "v_mfma_f32_16x16x32_bf16 v[96:99], a[184:187], v[184:187], v[96:99]\n"
    "v_exp_f32_e32 v236, v236\n"
    "v_mfma_f32_16x16x32_bf16 v[92:95], a[184:187], v[176:179], v[92:95]\n"
    "v_exp_f32_e32 v237, v237\n"
    "v_mfma_f32_16x16x32_bf16 v[92:95], a[188:191], v[180:183], v[92:95]\n"
    "v_exp_f32_e32 v238, v238\n"
    "v_mfma_f32_16x16x32_bf16 v[36:39], a[188:191], v[172:175], v[36:39]\n"
    "buffer_load_dwordx4 a[240:243], v158, s[12:15], s61 offen offset:0\n"
    "v_mfma_f32_16x16x32_bf16 v[36:39], a[184:187], v[168:171], v[36:39]\n"
    "v_exp_f32_e32 v239, v239\n"
    "v_mfma_f32_16x16x32_bf16 v[12:15], a[184:187], v[160:163], v[12:15]\n"
    "ds_read_b64_tr_b16 a[184:185], v247 offset:4096\n"
    "v_mfma_f32_16x16x32_bf16 v[12:15], a[188:191], v[164:167], v[12:15]\n"
    "s_waitcnt lgkmcnt(9)\n"
    "ds_read_b64_tr_b16 a[186:187], v247 offset:6144\n"
    "v_mfma_f32_16x16x32_bf16 v[16:19], a[160:163], v[160:163], v[16:19]\n"
    "ds_read_b64_tr_b16 a[188:189], v247 offset:12288\n"
    "v_mfma_f32_16x16x32_bf16 v[16:19], a[164:167], v[164:167], v[16:19]\n"
    "ds_read_b64_tr_b16 a[190:191], v247 offset:14336\n"
    "v_mfma_f32_16x16x32_bf16 v[40:43], a[164:167], v[172:175], v[40:43]\n"
    "v_cvt_pk_bf16_f32 v234, v236, v237\n"
    "v_mfma_f32_16x16x32_bf16 v[40:43], a[160:163], v[168:171], v[40:43]\n"
    "v_exp_f32_e32 v228, v228\n"
    "v_mfma_f32_16x16x32_bf16 v[104:107], a[160:163], v[176:179], v[104:107]\n"
    "ds_write_addtid_b32 v232 offset:16384\n"
    "v_mfma_f32_16x16x32_bf16 v[104:107], a[164:167], v[180:183], v[104:107]\n"
    "ds_write_addtid_b32 v233 offset:16640\n"
    "v_mfma_f32_16x16x32_bf16 v[108:111], a[164:167], v[188:191], v[108:111]\n"
    "v_exp_f32_e32 v229, v229\n"
    "v_mfma_f32_16x16x32_bf16 v[108:111], a[160:163], v[184:187], v[108:111]\n"
    "ds_write_addtid_b32 v224 offset:12288\n"
    "v_mfma_f32_16x16x32_bf16 v[112:115], a[160:163], v[192:195], v[112:115]\n"
    "ds_read_b128 a[160:163], v243 offset:36864\n"
    "buffer_load_dwordx4 a[244:247], v158, s[12:15], s61 offen offset:64\n"
    "v_mfma_f32_16x16x32_bf16 v[112:115], a[164:167], v[196:199], v[112:115]\n"
    "s_waitcnt lgkmcnt(12)\n"
    "ds_read_b128 a[164:167], v240 offset:32768\n"
    "v_mfma_f32_16x16x32_bf16 v[124:127], a[168:171], v[192:195], v[124:127]\n"
    "v_cvt_pk_bf16_f32 v235, v238, v239\n"
    "v_mfma_f32_16x16x32_bf16 v[124:127], a[172:175], v[196:199], v[124:127]\n"
    "ds_write_addtid_b32 v225 offset:12544\n"
    "v_mfma_f32_16x16x32_bf16 v[120:123], a[172:175], v[188:191], v[120:123]\n"
    "v_exp_f32_e32 v230, v230\n"
    "v_mfma_f32_16x16x32_bf16 v[120:123], a[168:171], v[184:187], v[120:123]\n"
    "ds_write_addtid_b32 v216 offset:8192\n"
    "v_mfma_f32_16x16x32_bf16 v[116:119], a[168:171], v[176:179], v[116:119]\n"
    "ds_write_addtid_b32 v217 offset:8448\n"
    "v_mfma_f32_16x16x32_bf16 v[116:119], a[172:175], v[180:183], v[116:119]\n"
    "v_exp_f32_e32 v231, v231\n"
    "v_mfma_f32_16x16x32_bf16 v[44:47], a[172:175], v[172:175], v[44:47]\n"
    "ds_write_addtid_b32 v234 offset:16896\n"
    "v_mfma_f32_16x16x32_bf16 v[44:47], a[168:171], v[168:171], v[44:47]\n"
    "v_cvt_pk_bf16_f32 v226, v228, v229\n"
    "v_mfma_f32_16x16x32_bf16 v[20:23], a[168:171], v[160:163], v[20:23]\n"
    "ds_read_b128 a[168:171], v241 offset:32768\n"
    "v_mfma_f32_16x16x32_bf16 v[20:23], a[172:175], v[164:167], v[20:23]\n"
    "s_waitcnt lgkmcnt(14)\n"
    "ds_read_b128 a[172:175], v240 offset:36864\n"
    "v_mfma_f32_16x16x32_bf16 v[24:27], a[176:179], v[160:163], v[24:27]\n"
    "v_mfma_f32_16x16x32_bf16 v[24:27], a[180:183], v[164:167], v[24:27]\n"
    "v_exp_f32_e32 v220, v220\n"
    "v_mfma_f32_16x16x32_bf16 v[48:51], a[180:183], v[172:175], v[48:51]\n"
    "v_mfma_f32_16x16x32_bf16 v[48:51], a[176:179], v[168:171], v[48:51]\n"
    "ds_write_addtid_b32 v235 offset:17152\n"
    "v_mfma_f32_16x16x32_bf16 v[128:131], a[176:179], v[176:179], v[128:131]\n"
    "v_exp_f32_e32 v221, v221\n"
    "v_mfma_f32_16x16x32_bf16 v[128:131], a[180:183], v[180:183], v[128:131]\n"
    "v_mfma_f32_16x16x32_bf16 v[132:135], a[180:183], v[188:191], v[132:135]\n"
    "ds_write_addtid_b32 v226 offset:12800\n"
    "v_mfma_f32_16x16x32_bf16 v[132:135], a[176:179], v[184:187], v[132:135]\n"
    "v_cvt_pk_bf16_f32 v227, v230, v231\n"
    "v_mfma_f32_16x16x32_bf16 v[136:139], a[176:179], v[192:195], v[136:139]\n"
    "ds_read_b128 a[176:179], v241 offset:36864\n"
    "v_mfma_f32_16x16x32_bf16 v[136:139], a[180:183], v[196:199], v[136:139]\n"
    "s_waitcnt lgkmcnt(14)\n"
    "ds_read_b128 a[180:183], v242 offset:32768\n"
    "v_mfma_f32_16x16x32_bf16 v[148:151], a[184:187], v[192:195], v[148:151]\n"
    "v_exp_f32_e32 v222, v222\n"
    "v_mfma_f32_16x16x32_bf16 v[148:151], a[188:191], v[196:199], v[148:151]\n"
    "v_mfma_f32_16x16x32_bf16 v[144:147], a[188:191], v[188:191], v[144:147]\n"
    "ds_write_addtid_b32 v227 offset:13056\n"
    "v_mfma_f32_16x16x32_bf16 v[144:147], a[184:187], v[184:187], v[144:147]\n"
    "v_exp_f32_e32 v223, v223\n"
    "v_mfma_f32_16x16x32_bf16 v[140:143], a[184:187], v[176:179], v[140:143]\n"
    "v_mfma_f32_16x16x32_bf16 v[140:143], a[188:191], v[180:183], v[140:143]\n"
    "v_mfma_f32_16x16x32_bf16 v[52:55], a[188:191], v[172:175], v[52:55]\n"
    "v_cvt_pk_bf16_f32 v218, v220, v221\n"
    "v_mfma_f32_16x16x32_bf16 v[52:55], a[184:187], v[168:171], v[52:55]\n"
    "v_cvt_pk_bf16_f32 v219, v222, v223\n"
    "v_mfma_f32_16x16x32_bf16 v[28:31], a[184:187], v[160:163], v[28:31]\n"
    "ds_read_b128 a[184:187], v243 offset:32768\n"
    "v_mfma_f32_16x16x32_bf16 v[28:31], a[188:191], v[164:167], v[28:31]\n"
    "s_waitcnt lgkmcnt(12)\n"
    "ds_read_b128 a[188:191], v242 offset:36864\n"
    "v_mfma_f32_16x16x32_bf16 v[176:179], a[160:163], a[92:95], 0\n"
    "ds_write_addtid_b32 v218 offset:8704\n"
    "v_mfma_f32_16x16x32_bf16 v[176:179], a[164:167], a[64:67], v[176:179]\n"
    "v_mfma_f32_16x16x32_bf16 v[184:187], a[164:167], a[96:99], 0\n"
    "v_mfma_f32_16x16x32_bf16 v[184:187], a[160:163], a[124:127], v[184:187]\n"
    "ds_write_addtid_b32 v219 offset:8960\n"
    "v_mfma_f32_16x16x32_bf16 v[192:195], a[160:163], a[156:159], 0\n"
    "ds_read_b128 a[160:163], v243 offset:38912\n"
    "v_mfma_f32_16x16x32_bf16 v[192:195], a[164:167], a[128:131], v[192:195]\n"
    "s_waitcnt lgkmcnt(10)\n"
    "ds_read_b128 a[164:167], v240 offset:34816\n"
    "v_mfma_f32_16x16x32_bf16 v[192:195], a[168:171], a[132:135], v[192:195]\n"
    "v_mfma_f32_16x16x32_bf16 v[192:195], a[172:175], a[136:139], v[192:195]\n"
    "s_waitcnt lgkmcnt(2)\n"
    "v_mfma_f32_16x16x32_bf16 v[184:187], a[172:175], a[104:107], v[184:187]\n"
    "s_barrier\n"
    "v_mfma_f32_16x16x32_bf16 v[184:187], a[168:171], a[100:103], v[184:187]\n"
    "s_add_u32 m0, s43, 0\n"
    "v_mfma_f32_16x16x32_bf16 v[176:179], a[168:171], a[68:71], v[176:179]\n"
    "ds_read_b128 a[168:171], v241 offset:34816\n"
    "buffer_load_dwordx4 v248, s[8:11], s40 offen offset:0 sc1 lds\n"
    "v_mfma_f32_16x16x32_bf16 v[176:179], a[172:175], a[72:75], v[176:179]\n"
    "ds_read_b128 a[172:175], v240 offset:38912\n"
    "v_mfma_f32_16x16x32_bf16 v[176:179], a[176:179], a[76:79], v[176:179]\n"
    "ds_read2st64_b32 v[220:221], v250 offset0:32 offset1:33\n"
    "v_mfma_f32_16x16x32_bf16 v[176:179], a[180:183], a[80:83], v[176:179]\n"
    "v_mfma_f32_16x16x32_bf16 v[184:187], a[180:183], a[112:115], v[184:187]\n"
    "buffer_load_dwordx4 v248, s[8:11], s40 offen offset:1024 sc1 lds\n"
    "v_mfma_f32_16x16x32_bf16 v[184:187], a[176:179], a[108:111], v[184:187]\n"
    "v_mfma_f32_16x16x32_bf16 v[192:195], a[176:179], a[140:143], v[192:195]\n"
    "ds_read_b128 a[176:179], v241 offset:38912\n"
    "v_mfma_f32_16x16x32_bf16 v[192:195], a[180:183], a[144:147], v[192:195]\n"
    "ds_read_b128 a[180:183], v242 offset:34816\n"
    "v_mfma_f32_16x16x32_bf16 v[192:195], a[184:187], a[148:151], v[192:195]\n"
    "buffer_load_dwordx4 v248, s[8:11], s40 offen offset:2048 sc1 lds\n"
    "v_mfma_f32_16x16x32_bf16 v[192:195], a[188:191], a[152:155], v[192:195]\n"
    "ds_read2st64_b32 v[222:223], v250 offset0:34 offset1:35\n"
    "v_mfma_f32_16x16x32_bf16 v[184:187], a[188:191], a[120:123], v[184:187]\n"
    "v_mfma_f32_16x16x32_bf16 v[184:187], a[184:187], a[116:119], v[184:187]\n"
    "v_exp_f32_e32 v192, v192\n"
    "v_mfma_f32_16x16x32_bf16 v[176:179], a[184:187], a[84:87], v[176:179]\n"
    "ds_read_b128 a[184:187], v243 offset:34816\n"
    "buffer_load_dwordx4 v248, s[8:11], s40 offen offset:3072 sc1 lds\n"
    "v_mfma_f32_16x16x32_bf16 v[176:179], a[188:191], a[88:91], v[176:179]\n"
    "s_waitcnt lgkmcnt(7)\n"
    "s_add_u32 s40, s40, 32768\n"
    "ds_read_b128 a[188:191], v242 offset:38912\n"
    "v_mfma_f32_16x16x32_bf16 v[180:183], a[160:163], a[92:95], 0\n"
    "s_add_u32 m0, s43, 4096\n"
    "ds_read2st64_b32 v[228:229], v250 offset0:48 offset1:49\n"
    "v_mfma_f32_16x16x32_bf16 v[180:183], a[164:167], a[64:67], v[180:183]\n"
    "v_exp_f32_e32 v193, v193\n"
    "v_mfma_f32_16x16x32_bf16 v[188:191], a[164:167], a[96:99], 0\n"
    "buffer_load_dwordx4 v248, s[8:11], s41 offen offset:0 sc1 lds\n"
    "v_exp_f32_e32 v194, v194\n"
    "v_mfma_f32_16x16x32_bf16 v[188:191], a[160:163], a[124:127], v[188:191]\n"
    "ds_read2st64_b32 v[230:231], v250 offset0:50 offset1:51\n"
    "v_mfma_f32_16x16x32_bf16 v[196:199], a[160:163], a[156:159], 0\n"
    "ds_read_b64_tr_b16 a[160:161], v244 offset:16384\n"
    "v_mfma_f32_16x16x32_bf16 v[196:199], a[164:167], a[128:131], v[196:199]\n"
    "s_waitcnt lgkmcnt(9)\n"
    "ds_read_b64_tr_b16 a[162:163], v244 offset:18432\n"
    "v_mfma_f32_16x16x32_bf16 v[196:199], a[168:171], a[132:135], v[196:199]\n"
    "ds_read_b64_tr_b16 a[164:165], v244 offset:24576\n"
    "buffer_load_dwordx4 v248, s[8:11], s41 offen offset:1024 sc1 lds\n"
    "v_mfma_f32_16x16x32_bf16 v[196:199], a[172:175], a[136:139], v[196:199]\n"
    "ds_read_b64_tr_b16 a[166:167], v244 offset:26624\n"
    "v_mfma_f32_16x16x32_bf16 v[188:191], a[172:175], a[104:107], v[188:191]\n"
    "v_exp_f32_e32 v195, v195\n"
    "v_mfma_f32_16x16x32_bf16 v[188:191], a[168:171], a[100:103], v[188:191]\n"
    "ds_read2st64_b32 v[236:237], v250 offset0:64 offset1:65\n"
    "v_cvt_pk_bf16_f32 v192, v192, v193\n"
    "v_mfma_f32_16x16x32_bf16 v[180:183], a[168:171], a[68:71], v[180:183]\n"
    "ds_read_b64_tr_b16 a[168:169], v245 offset:16384\n"
    "buffer_load_dwordx4 v248, s[8:11], s41 offen offset:2048 sc1 lds\n"
    "v_mfma_f32_16x16x32_bf16 v[180:183], a[172:175], a[72:75], v[180:183]\n"
    "s_waitcnt lgkmcnt(11)\n"
    "ds_read_b64_tr_b16 a[170:171], v245 offset:18432\n"
    "v_mfma_f32_16x16x32_bf16 v[180:183], a[176:179], a[76:79], v[180:183]\n"
    "ds_read_b64_tr_b16 a[172:173], v245 offset:24576\n"
    "v_mfma_f32_16x16x32_bf16 v[180:183], a[180:183], a[80:83], v[180:183]\n"
    "ds_read_b64_tr_b16 a[174:175], v245 offset:26624\n"
    "v_mfma_f32_16x16x32_bf16 v[188:191], a[180:183], a[112:115], v[188:191]\n"
    "buffer_load_dwordx4 v248, s[8:11], s41 offen offset:3072 sc1 lds\n"
    "v_mfma_f32_16x16x32_bf16 v[188:191], a[176:179], a[108:111], v[188:191]\n"
    "s_add_u32 s41, s41, 32768\n"
    "ds_read2st64_b32 v[238:239], v250 offset0:66 offset1:67\n"
    "v_mfma_f32_16x16x32_bf16 v[196:199], a[176:179], a[140:143], v[196:199]\n"
    "s_mov_b32 m0, s49\n"
    "ds_read_b64_tr_b16 a[176:177], v246 offset:16384\n"
    "v_mfma_f32_16x16x32_bf16 v[196:199], a[180:183], a[144:147], v[196:199]\n"
    "s_waitcnt lgkmcnt(13)\n"
    "ds_read_b64_tr_b16 a[178:179], v246 offset:18432\n"
    "v_mfma_f32_16x16x32_bf16 v[196:199], a[184:187], a[148:151], v[196:199]\n"
    "ds_read_b64_tr_b16 a[180:181], v246 offset:24576\n"
    "v_mfma_f32_16x16x32_bf16 v[196:199], a[188:191], a[152:155], v[196:199]\n"
    "ds_read_b64_tr_b16 a[182:183], v246 offset:26624\n"
    "v_mfma_f32_16x16x32_bf16 v[188:191], a[188:191], a[120:123], v[188:191]\n"
    "v_exp_f32_e32 v184, v184\n"
    "v_mfma_f32_16x16x32_bf16 v[188:191], a[184:187], a[116:119], v[188:191]\n"
    "v_exp_f32_e32 v185, v185\n"
    "v_mfma_f32_16x16x32_bf16 v[180:183], a[184:187], a[84:87], v[180:183]\n"
    "ds_read_b64_tr_b16 a[184:185], v247 offset:16384\n"
    "v_cvt_pk_bf16_f32 v193, v194, v195\n"
    "v_mfma_f32_16x16x32_bf16 v[180:183], a[188:191], a[88:91], v[180:183]\n"
    "s_waitcnt lgkmcnt(5)\n"
    "ds_read_b64_tr_b16 a[186:187], v247 offset:18432\n"
    "v_mfma_f32_16x16x32_bf16 v[56:59], a[160:163], v[216:219], v[56:59]\n"
    "ds_read_b64_tr_b16 a[188:189], v247 offset:24576\n"
    "v_mfma_f32_16x16x32_bf16 v[56:59], a[164:167], v[220:223], v[56:59]\n"
    "ds_read_b64_tr_b16 a[190:191], v247 offset:26624\n"
    "v_mfma_f32_16x16x32_bf16 v[60:63], a[164:167], v[228:231], v[60:63]\n"
    "v_exp_f32_e32 v186, v186\n"
    "v_mfma_f32_16x16x32_bf16 v[60:63], a[160:163], v[224:227], v[60:63]\n"
    "v_exp_f32_e32 v187, v187\n"
    "v_mfma_f32_16x16x32_bf16 v[64:67], a[160:163], v[232:235], v[64:67]\n"
    "ds_read_b64_tr_b16 a[160:161], v244 offset:20480\n"
    "v_cvt_pk_bf16_f32 v184, v184, v185\n"
    "v_mfma_f32_16x16x32_bf16 v[64:67], a[164:167], v[236:239], v[64:67]\n"
    "ds_read_b64_tr_b16 a[162:163], v244 offset:22528\n"
    "v_mfma_f32_16x16x32_bf16 v[76:79], a[168:171], v[232:235], v[76:79]\n"
    "ds_read_b64_tr_b16 a[164:165], v244 offset:28672\n"
    "v_mfma_f32_16x16x32_bf16 v[76:79], a[172:175], v[236:239], v[76:79]\n"
    "ds_read_b64_tr_b16 a[166:167], v244 offset:30720\n"
    "v_mfma_f32_16x16x32_bf16 v[72:75], a[172:175], v[228:231], v[72:75]\n"
    "v_exp_f32_e32 v176, v176\n"
    "v_mfma_f32_16x16x32_bf16 v[72:75], a[168:171], v[224:227], v[72:75]\n"
    "v_exp_f32_e32 v177, v177\n"
    "v_mfma_f32_16x16x32_bf16 v[68:71], a[168:171], v[216:219], v[68:71]\n"
    "ds_read_b64_tr_b16 a[168:169], v245 offset:20480\n"
    "v_cvt_pk_bf16_f32 v185, v186, v187\n"
    "v_mfma_f32_16x16x32_bf16 v[68:71], a[172:175], v[220:223], v[68:71]\n"
    "s_waitcnt lgkmcnt(9)\n"
    "ds_read_b64_tr_b16 a[170:171], v245 offset:22528\n"
    "v_mfma_f32_16x16x32_bf16 v[80:83], a[176:179], v[216:219], v[80:83]\n"
    "ds_read_b64_tr_b16 a[172:173], v245 offset:28672\n"
    "v_mfma_f32_16x16x32_bf16 v[80:83], a[180:183], v[220:223], v[80:83]\n"
    "ds_read_b64_tr_b16 a[174:175], v245 offset:30720\n"
    "v_mfma_f32_16x16x32_bf16 v[84:87], a[180:183], v[228:231], v[84:87]\n"
    "v_exp_f32_e32 v178, v178\n"
    "v_mfma_f32_16x16x32_bf16 v[84:87], a[176:179], v[224:227], v[84:87]\n"
    "s_waitcnt vmcnt(14)\n"
    "v_exp_f32_e32 v179, v179\n"
    "v_mfma_f32_16x16x32_bf16 v[88:91], a[176:179], v[232:235], v[88:91]\n"
    "s_barrier\n"
    "v_mfma_f32_16x16x32_bf16 v[88:91], a[180:183], v[236:239], v[88:91]\n"
    "s_waitcnt lgkmcnt(8)\n"
    "ds_read_b64_tr_b16 a[176:177], v246 offset:20480\n"
    "v_cvt_pk_bf16_f32 v176, v176, v177\n"
    "v_mfma_f32_16x16x32_bf16 v[100:103], a[184:187], v[232:235], v[100:103]\n"
    "ds_read_b64_tr_b16 a[178:179], v246 offset:22528\n"
    "v_cvt_pk_bf16_f32 v177, v178, v179\n"
    "v_mfma_f32_16x16x32_bf16 v[100:103], a[188:191], v[236:239], v[100:103]\n"
    "ds_read_b64_tr_b16 a[180:181], v246 offset:28672\n"
    "v_mfma_f32_16x16x32_bf16 v[96:99], a[188:191], v[228:231], v[96:99]\n"
    "ds_read_b64_tr_b16 a[182:183], v246 offset:30720\n"
    "v_mfma_f32_16x16x32_bf16 v[96:99], a[184:187], v[224:227], v[96:99]\n"
    "v_exp_f32_e32 v196, v196\n"
    "v_mfma_f32_16x16x32_bf16 v[92:95], a[184:187], v[216:219], v[92:95]\n"
    "ds_read_b64_tr_b16 a[184:185], v247 offset:20480\n"
    "ds_write_addtid_b32 v192 offset:16384\n"
    "v_mfma_f32_16x16x32_bf16 v[92:95], a[188:191], v[220:223], v[92:95]\n"
    "s_waitcnt lgkmcnt(10)\n"
    "ds_read_b64_tr_b16 a[186:187], v247 offset:22528\n"
    "v_mfma_f32_16x16x32_bf16 v[104:107], a[160:163], v[216:219], v[104:107]\n"
    "ds_read_b64_tr_b16 a[188:189], v247 offset:28672\n"
    "ds_write_addtid_b32 v193 offset:16640\n"
    "v_mfma_f32_16x16x32_bf16 v[104:107], a[164:167], v[220:223], v[104:107]\n"
    "ds_read_b64_tr_b16 a[190:191], v247 offset:30720\n"
    "v_mfma_f32_16x16x32_bf16 v[108:111], a[164:167], v[228:231], v[108:111]\n"
    "v_exp_f32_e32 v197, v197\n"
    "v_mfma_f32_16x16x32_bf16 v[108:111], a[160:163], v[224:227], v[108:111]\n"
    "v_exp_f32_e32 v198, v198\n"
    "v_mfma_f32_16x16x32_bf16 v[112:115], a[160:163], v[232:235], v[112:115]\n"
    "ds_read_b128 a[160:163], v243 offset:53248\n"
    "ds_write_addtid_b32 v184 offset:12288\n"
    "v_mfma_f32_16x16x32_bf16 v[112:115], a[164:167], v[236:239], v[112:115]\n"
    "s_waitcnt lgkmcnt(12)\n"
    "ds_read_b128 a[164:167], v240 offset:49152\n"
    "ds_write_addtid_b32 v185 offset:12544\n"
    "v_mfma_f32_16x16x32_bf16 v[124:127], a[168:171], v[232:235], v[124:127]\n"
    "v_exp_f32_e32 v199, v199\n"
    "v_mfma_f32_16x16x32_bf16 v[124:127], a[172:175], v[236:239], v[124:127]\n"
    "ds_write_addtid_b32 v176 offset:8192\n"
    "v_cvt_pk_bf16_f32 v194, v196, v197\n"
    "v_mfma_f32_16x16x32_bf16 v[120:123], a[172:175], v[228:231], v[120:123]\n"
    "v_exp_f32_e32 v188, v188\n"
    "v_mfma_f32_16x16x32_bf16 v[120:123], a[168:171], v[224:227], v[120:123]\n"
    "v_exp_f32_e32 v189, v189\n"
    "v_mfma_f32_16x16x32_bf16 v[116:119], a[168:171], v[216:219], v[116:119]\n"
    "ds_read_b128 a[168:171], v241 offset:49152\n"
    "v_cvt_pk_bf16_f32 v195, v198, v199\n"
    "v_mfma_f32_16x16x32_bf16 v[116:119], a[172:175], v[220:223], v[116:119]\n"
    "s_waitcnt lgkmcnt(12)\n"
    "ds_read_b128 a[172:175], v240 offset:53248\n"
    "ds_write_addtid_b32 v177 offset:8448\n"
    "v_mfma_f32_16x16x32_bf16 v[128:131], a[176:179], v[216:219], v[128:131]\n"
    "v_exp_f32_e32 v190, v190\n"
    "v_mfma_f32_16x16x32_bf16 v[128:131], a[180:183], v[220:223], v[128:131]\n"
    "v_exp_f32_e32 v191, v191\n"
    "v_mfma_f32_16x16x32_bf16 v[132:135], a[180:183], v[228:231], v[132:135]\n"
    "ds_write_addtid_b32 v194 offset:16896\n"
    "v_cvt_pk_bf16_f32 v186, v188, v189\n"
    "v_mfma_f32_16x16x32_bf16 v[132:135], a[176:179], v[224:227], v[132:135]\n"
    "v_exp_f32_e32 v180, v180\n"
    "v_mfma_f32_16x16x32_bf16 v[136:139], a[176:179], v[232:235], v[136:139]\n"
    "ds_read_b128 a[176:179], v241 offset:53248\n"
    "v_mfma_f32_16x16x32_bf16 v[136:139], a[180:183], v[236:239], v[136:139]\n"
    "s_waitcnt lgkmcnt(10)\n"
    "ds_read_b128 a[180:183], v242 offset:49152\n"
    "v_mfma_f32_16x16x32_bf16 v[148:151], a[184:187], v[232:235], v[148:151]\n"
    "v_exp_f32_e32 v181, v181\n"
    "v_mfma_f32_16x16x32_bf16 v[148:151], a[188:191], v[236:239], v[148:151]\n"
    "ds_write_addtid_b32 v195 offset:17152\n"
    "v_cvt_pk_bf16_f32 v187, v190, v191\n"
    "v_mfma_f32_16x16x32_bf16 v[144:147], a[188:191], v[228:231], v[144:147]\n"
    "v_exp_f32_e32 v182, v182\n"
    "v_mfma_f32_16x16x32_bf16 v[144:147], a[184:187], v[224:227], v[144:147]\n"
    "v_exp_f32_e32 v183, v183\n"
    "v_mfma_f32_16x16x32_bf16 v[140:143], a[184:187], v[216:219], v[140:143]\n"
    "ds_read_b128 a[184:187], v243 offset:49152\n"
    "v_cvt_pk_bf16_f32 v178, v180, v181\n"
    "v_mfma_f32_16x16x32_bf16 v[140:143], a[188:191], v[220:223], v[140:143]\n"
    "s_waitcnt lgkmcnt(10)\n"
    "ds_read_b128 a[188:191], v242 offset:53248\n"
    "v_cvt_pk_bf16_f32 v179, v182, v183\n"
    "v_mfma_f32_16x16x32_bf16 v[216:219], a[160:163], a[92:95], 0\n"
    "ds_write_addtid_b32 v186 offset:12800\n"
    "v_mfma_f32_16x16x32_bf16 v[216:219], a[164:167], a[64:67], v[216:219]\n"
    "ds_write_addtid_b32 v187 offset:13056\n"
    "v_mfma_f32_16x16x32_bf16 v[224:227], a[164:167], a[96:99], 0\n"
    "ds_write_addtid_b32 v178 offset:8704\n"
    "v_mfma_f32_16x16x32_bf16 v[224:227], a[160:163], a[124:127], v[224:227]\n"
    "ds_write_addtid_b32 v179 offset:8960\n"
    "v_mfma_f32_16x16x32_bf16 v[232:235], a[160:163], a[156:159], 0\n"
    "ds_read_b128 a[160:163], v243 offset:55296\n"
    "v_mfma_f32_16x16x32_bf16 v[232:235], a[164:167], a[128:131], v[232:235]\n"
    "s_waitcnt lgkmcnt(12)\n"
    "ds_read_b128 a[164:167], v240 offset:51200\n"
    "v_mfma_f32_16x16x32_bf16 v[232:235], a[168:171], a[132:135], v[232:235]\n"
    "v_mfma_f32_16x16x32_bf16 v[232:235], a[172:175], a[136:139], v[232:235]\n"
    "s_waitcnt lgkmcnt(2)\n"
    "v_mfma_f32_16x16x32_bf16 v[224:227], a[172:175], a[104:107], v[224:227]\n"
    "s_barrier\n"
    "v_mfma_f32_16x16x32_bf16 v[224:227], a[168:171], a[100:103], v[224:227]\n"
    "s_add_u32 m0, s43, 16384\n"
    "v_mfma_f32_16x16x32_bf16 v[216:219], a[168:171], a[68:71], v[216:219]\n"
    "ds_read_b128 a[168:171], v241 offset:51200\n"
    "buffer_load_dwordx4 v248, s[8:11], s40 offen offset:0 sc1 lds\n"
    "v_mfma_f32_16x16x32_bf16 v[216:219], a[172:175], a[72:75], v[216:219]\n"
    "ds_read_b128 a[172:175], v240 offset:55296\n"
    "v_mfma_f32_16x16x32_bf16 v[216:219], a[176:179], a[76:79], v[216:219]\n"
    "ds_read2st64_b32 v[180:181], v250 offset0:32 offset1:33\n"
    "v_mfma_f32_16x16x32_bf16 v[216:219], a[180:183], a[80:83], v[216:219]\n"
    "v_mfma_f32_16x16x32_bf16 v[224:227], a[180:183], a[112:115], v[224:227]\n"
    "buffer_load_dwordx4 v248, s[8:11], s40 offen offset:1024 sc1 lds\n"
    "v_mfma_f32_16x16x32_bf16 v[224:227], a[176:179], a[108:111], v[224:227]\n"
    "v_mfma_f32_16x16x32_bf16 v[232:235], a[176:179], a[140:143], v[232:235]\n"
    "ds_read_b128 a[176:179], v241 offset:55296\n"
    "v_mfma_f32_16x16x32_bf16 v[232:235], a[180:183], a[144:147], v[232:235]\n"
    "ds_read_b128 a[180:183], v242 offset:51200\n"
    "v_mfma_f32_16x16x32_bf16 v[232:235], a[184:187], a[148:151], v[232:235]\n"
    "buffer_load_dwordx4 v248, s[8:11], s40 offen offset:2048 sc1 lds\n"
    "v_mfma_f32_16x16x32_bf16 v[232:235], a[188:191], a[152:155], v[232:235]\n"
    "ds_read2st64_b32 v[182:183], v250 offset0:34 offset1:35\n"
    "v_mfma_f32_16x16x32_bf16 v[224:227], a[188:191], a[120:123], v[224:227]\n"
    "v_mfma_f32_16x16x32_bf16 v[224:227], a[184:187], a[116:119], v[224:227]\n"
    "v_exp_f32_e32 v232, v232\n"
    "v_mfma_f32_16x16x32_bf16 v[216:219], a[184:187], a[84:87], v[216:219]\n"
    "ds_read_b128 a[184:187], v243 offset:51200\n"
    "buffer_load_dwordx4 v248, s[8:11], s40 offen offset:3072 sc1 lds\n"
    "v_mfma_f32_16x16x32_bf16 v[216:219], a[188:191], a[88:91], v[216:219]\n"
    "s_waitcnt lgkmcnt(7)\n"
    "s_add_u32 s40, s40, -131072\n"
    "ds_read_b128 a[188:191], v242 offset:55296\n"
    "v_mfma_f32_16x16x32_bf16 v[220:223], a[160:163], a[92:95], 0\n"
    "s_add_u32 m0, s43, 20480\n"
    "ds_read2st64_b32 v[188:189], v250 offset0:48 offset1:49\n"
    "v_mfma_f32_16x16x32_bf16 v[220:223], a[164:167], a[64:67], v[220:223]\n"
    "v_exp_f32_e32 v233, v233\n"
    "v_mfma_f32_16x16x32_bf16 v[228:231], a[164:167], a[96:99], 0\n"
    "buffer_load_dwordx4 v248, s[8:11], s41 offen offset:0 sc1 lds\n"
    "v_exp_f32_e32 v234, v234\n"
    "v_mfma_f32_16x16x32_bf16 v[228:231], a[160:163], a[124:127], v[228:231]\n"
    "ds_read2st64_b32 v[190:191], v250 offset0:50 offset1:51\n"
    "v_mfma_f32_16x16x32_bf16 v[236:239], a[160:163], a[156:159], 0\n"
    "ds_read_b64_tr_b16 a[160:161], v244 offset:32768\n"
    "v_mfma_f32_16x16x32_bf16 v[236:239], a[164:167], a[128:131], v[236:239]\n"
    "s_waitcnt lgkmcnt(9)\n"
    "ds_read_b64_tr_b16 a[162:163], v244 offset:34816\n"
    "v_mfma_f32_16x16x32_bf16 v[236:239], a[168:171], a[132:135], v[236:239]\n"
    "ds_read_b64_tr_b16 a[164:165], v244 offset:40960\n"
    "buffer_load_dwordx4 v248, s[8:11], s41 offen offset:1024 sc1 lds\n"
    "v_mfma_f32_16x16x32_bf16 v[236:239], a[172:175], a[136:139], v[236:239]\n"
    "ds_read_b64_tr_b16 a[166:167], v244 offset:43008\n"
    "v_mfma_f32_16x16x32_bf16 v[228:231], a[172:175], a[104:107], v[228:231]\n"
    "v_exp_f32_e32 v235, v235\n"
    "v_mfma_f32_16x16x32_bf16 v[228:231], a[168:171], a[100:103], v[228:231]\n"
    "ds_read2st64_b32 v[196:197], v250 offset0:64 offset1:65\n"
    "v_cvt_pk_bf16_f32 v232, v232, v233\n"
    "v_mfma_f32_16x16x32_bf16 v[220:223], a[168:171], a[68:71], v[220:223]\n"
    "ds_read_b64_tr_b16 a[168:169], v245 offset:32768\n"
    "buffer_load_dwordx4 v248, s[8:11], s41 offen offset:2048 sc1 lds\n"
    "v_mfma_f32_16x16x32_bf16 v[220:223], a[172:175], a[72:75], v[220:223]\n"
    "s_waitcnt lgkmcnt(11)\n"
    "ds_read_b64_tr_b16 a[170:171], v245 offset:34816\n"
    "v_mfma_f32_16x16x32_bf16 v[220:223], a[176:179], a[76:79], v[220:223]\n"
    "ds_read_b64_tr_b16 a[172:173], v245 offset:40960\n"
    "v_mfma_f32_16x16x32_bf16 v[220:223], a[180:183], a[80:83], v[220:223]\n"
    "ds_read_b64_tr_b16 a[174:175], v245 offset:43008\n"
    "v_mfma_f32_16x16x32_bf16 v[228:231], a[180:183], a[112:115], v[228:231]\n"
    "buffer_load_dwordx4 v248, s[8:11], s41 offen offset:3072 sc1 lds\n"
    "v_mfma_f32_16x16x32_bf16 v[228:231], a[176:179], a[108:111], v[228:231]\n"
    "s_add_u32 s41, s41, -131072\n"
    "ds_read2st64_b32 v[198:199], v250 offset0:66 offset1:67\n"
    "v_mfma_f32_16x16x32_bf16 v[236:239], a[176:179], a[140:143], v[236:239]\n"
    "s_mov_b32 m0, s49\n"
    "ds_read_b64_tr_b16 a[176:177], v246 offset:32768\n"
    "v_mfma_f32_16x16x32_bf16 v[236:239], a[180:183], a[144:147], v[236:239]\n"
    "s_waitcnt lgkmcnt(13)\n"
    "ds_read_b64_tr_b16 a[178:179], v246 offset:34816\n"
    "v_mfma_f32_16x16x32_bf16 v[236:239], a[184:187], a[148:151], v[236:239]\n"
    "ds_read_b64_tr_b16 a[180:181], v246 offset:40960\n"
    "buffer_load_dword v255, v254, s[8:11], s45 offen offset:0 sc1\n"
    "v_mfma_f32_16x16x32_bf16 v[236:239], a[188:191], a[152:155], v[236:239]\n"
    "ds_read_b64_tr_b16 a[182:183], v246 offset:43008\n"
    "v_mfma_f32_16x16x32_bf16 v[228:231], a[188:191], a[120:123], v[228:231]\n"
    "v_exp_f32_e32 v224, v224\n"
    "v_mfma_f32_16x16x32_bf16 v[228:231], a[184:187], a[116:119], v[228:231]\n"
    "v_exp_f32_e32 v225, v225\n"
    "v_mfma_f32_16x16x32_bf16 v[220:223], a[184:187], a[84:87], v[220:223]\n"
    "ds_read_b64_tr_b16 a[184:185], v247 offset:32768\n"
    "v_cvt_pk_bf16_f32 v233, v234, v235\n"
    "v_mfma_f32_16x16x32_bf16 v[220:223], a[188:191], a[88:91], v[220:223]\n"
    "s_waitcnt lgkmcnt(5)\n"
    "ds_read_b64_tr_b16 a[186:187], v247 offset:34816\n"
    "v_mfma_f32_16x16x32_bf16 v[56:59], a[160:163], v[176:179], v[56:59]\n"
    "ds_read_b64_tr_b16 a[188:189], v247 offset:40960\n"
    "v_mfma_f32_16x16x32_bf16 v[56:59], a[164:167], v[180:183], v[56:59]\n"
    "ds_read_b64_tr_b16 a[190:191], v247 offset:43008\n"
    "v_mfma_f32_16x16x32_bf16 v[60:63], a[164:167], v[188:191], v[60:63]\n"
    "v_exp_f32_e32 v226, v226\n"
    "v_mfma_f32_16x16x32_bf16 v[60:63], a[160:163], v[184:187], v[60:63]\n"
    "v_exp_f32_e32 v227, v227\n"
    "v_mfma_f32_16x16x32_bf16 v[64:67], a[160:163], v[192:195], v[64:67]\n"
    "ds_read_b64_tr_b16 a[160:161], v244 offset:36864\n"
    "v_cvt_pk_bf16_f32 v224, v224, v225\n"
    "v_mfma_f32_16x16x32_bf16 v[64:67], a[164:167], v[196:199], v[64:67]\n"
    "ds_read_b64_tr_b16 a[162:163], v244 offset:38912\n"
    "v_mfma_f32_16x16x32_bf16 v[76:79], a[168:171], v[192:195], v[76:79]\n"
    "ds_read_b64_tr_b16 a[164:165], v244 offset:45056\n"
    "v_mfma_f32_16x16x32_bf16 v[76:79], a[172:175], v[196:199], v[76:79]\n"
    "ds_read_b64_tr_b16 a[166:167], v244 offset:47104\n"
    "v_mfma_f32_16x16x32_bf16 v[72:75], a[172:175], v[188:191], v[72:75]\n"
    "v_exp_f32_e32 v216, v216\n"
    "v_mfma_f32_16x16x32_bf16 v[72:75], a[168:171], v[184:187], v[72:75]\n"
    "v_exp_f32_e32 v217, v217\n"
    "v_mfma_f32_16x16x32_bf16 v[68:71], a[168:171], v[176:179], v[68:71]\n"
    "ds_read_b64_tr_b16 a[168:169], v245 offset:36864\n"
    "v_cvt_pk_bf16_f32 v225, v226, v227\n"
    "v_mfma_f32_16x16x32_bf16 v[68:71], a[172:175], v[180:183], v[68:71]\n"
    "s_waitcnt lgkmcnt(9)\n"
    "ds_read_b64_tr_b16 a[170:171], v245 offset:38912\n"
    "v_mfma_f32_16x16x32_bf16 v[80:83], a[176:179], v[176:179], v[80:83]\n"
    "ds_read_b64_tr_b16 a[172:173], v245 offset:45056\n"
    "v_mfma_f32_16x16x32_bf16 v[80:83], a[180:183], v[180:183], v[80:83]\n"
    "ds_read_b64_tr_b16 a[174:175], v245 offset:47104\n"
    "v_mfma_f32_16x16x32_bf16 v[84:87], a[180:183], v[188:191], v[84:87]\n"
    "v_exp_f32_e32 v218, v218\n"
    "v_mfma_f32_16x16x32_bf16 v[84:87], a[176:179], v[184:187], v[84:87]\n"
    "s_waitcnt vmcnt(9)\n"
    "v_exp_f32_e32 v219, v219\n"
    "v_mfma_f32_16x16x32_bf16 v[88:91], a[176:179], v[192:195], v[88:91]\n"
    "s_barrier\n"
    "v_mfma_f32_16x16x32_bf16 v[88:91], a[180:183], v[196:199], v[88:91]\n"
    "s_waitcnt lgkmcnt(8)\n"
    "ds_read_b64_tr_b16 a[176:177], v246 offset:36864\n"
    "v_cvt_pk_bf16_f32 v216, v216, v217\n"
    "v_mfma_f32_16x16x32_bf16 v[100:103], a[184:187], v[192:195], v[100:103]\n"
    "ds_read_b64_tr_b16 a[178:179], v246 offset:38912\n"
    "v_cvt_pk_bf16_f32 v217, v218, v219\n"
    "v_mfma_f32_16x16x32_bf16 v[100:103], a[188:191], v[196:199], v[100:103]\n"
    "ds_read_b64_tr_b16 a[180:181], v246 offset:45056\n"
    "v_mfma_f32_16x16x32_bf16 v[96:99], a[188:191], v[188:191], v[96:99]\n"
    "ds_read_b64_tr_b16 a[182:183], v246 offset:47104\n"
    "v_mfma_f32_16x16x32_bf16 v[96:99], a[184:187], v[184:187], v[96:99]\n"
    "v_exp_f32_e32 v236, v236\n"
    "v_mfma_f32_16x16x32_bf16 v[92:95], a[184:187], v[176:179], v[92:95]\n"
    "ds_read_b64_tr_b16 a[184:185], v247 offset:36864\n"
    "ds_write_addtid_b32 v232 offset:16384\n"
    "v_mfma_f32_16x16x32_bf16 v[92:95], a[188:191], v[180:183], v[92:95]\n"
    "s_waitcnt lgkmcnt(10)\n"
    "ds_read_b64_tr_b16 a[186:187], v247 offset:38912\n"
    "v_mfma_f32_16x16x32_bf16 v[104:107], a[160:163], v[176:179], v[104:107]\n"
    "ds_read_b64_tr_b16 a[188:189], v247 offset:45056\n"
    "ds_write_addtid_b32 v233 offset:16640\n"
    "v_mfma_f32_16x16x32_bf16 v[104:107], a[164:167], v[180:183], v[104:107]\n"
    "ds_read_b64_tr_b16 a[190:191], v247 offset:47104\n"
    "v_mfma_f32_16x16x32_bf16 v[108:111], a[164:167], v[188:191], v[108:111]\n"
    "v_exp_f32_e32 v237, v237\n"
    "v_mfma_f32_16x16x32_bf16 v[108:111], a[160:163], v[184:187], v[108:111]\n"
    "v_exp_f32_e32 v238, v238\n"
    "v_mfma_f32_16x16x32_bf16 v[112:115], a[160:163], v[192:195], v[112:115]\n"
    "ds_read_b128 a[160:163], v243 offset:4096\n"
    "ds_write_addtid_b32 v224 offset:12288\n"
    "v_mfma_f32_16x16x32_bf16 v[112:115], a[164:167], v[196:199], v[112:115]\n"
    "s_waitcnt lgkmcnt(12)\n"
    "ds_read_b128 a[164:167], v240 offset:0\n"
    "ds_write_addtid_b32 v225 offset:12544\n"
    "v_mfma_f32_16x16x32_bf16 v[124:127], a[168:171], v[192:195], v[124:127]\n"
    "v_exp_f32_e32 v239, v239\n"
    "v_mfma_f32_16x16x32_bf16 v[124:127], a[172:175], v[196:199], v[124:127]\n"
    "ds_write_addtid_b32 v216 offset:8192\n"
    "v_cvt_pk_bf16_f32 v234, v236, v237\n"
    "v_mfma_f32_16x16x32_bf16 v[120:123], a[172:175], v[188:191], v[120:123]\n"
    "v_exp_f32_e32 v228, v228\n"
    "v_mfma_f32_16x16x32_bf16 v[120:123], a[168:171], v[184:187], v[120:123]\n"
    "v_exp_f32_e32 v229, v229\n"
    "v_mfma_f32_16x16x32_bf16 v[116:119], a[168:171], v[176:179], v[116:119]\n"
    "ds_read_b128 a[168:171], v241 offset:0\n"
    "v_cvt_pk_bf16_f32 v235, v238, v239\n"
    "v_mfma_f32_16x16x32_bf16 v[116:119], a[172:175], v[180:183], v[116:119]\n"
    "s_waitcnt lgkmcnt(12)\n"
    "ds_read_b128 a[172:175], v240 offset:4096\n"
    "ds_write_addtid_b32 v217 offset:8448\n"
    "v_mfma_f32_16x16x32_bf16 v[128:131], a[176:179], v[176:179], v[128:131]\n"
    "v_exp_f32_e32 v230, v230\n"
    "v_mfma_f32_16x16x32_bf16 v[128:131], a[180:183], v[180:183], v[128:131]\n"
    "v_exp_f32_e32 v231, v231\n"
    "v_mfma_f32_16x16x32_bf16 v[132:135], a[180:183], v[188:191], v[132:135]\n"
    "ds_write_addtid_b32 v234 offset:16896\n"
    "v_cvt_pk_bf16_f32 v226, v228, v229\n"
    "v_mfma_f32_16x16x32_bf16 v[132:135], a[176:179], v[184:187], v[132:135]\n"
    "v_exp_f32_e32 v220, v220\n"
    "v_mfma_f32_16x16x32_bf16 v[136:139], a[176:179], v[192:195], v[136:139]\n"
    "ds_read_b128 a[176:179], v241 offset:4096\n"
    "v_mfma_f32_16x16x32_bf16 v[136:139], a[180:183], v[196:199], v[136:139]\n"
    "s_waitcnt lgkmcnt(10)\n"
    "ds_read_b128 a[180:183], v242 offset:0\n"
    "ds_write_addtid_b32 v235 offset:17152\n"
    "v_mfma_f32_16x16x32_bf16 v[148:151], a[184:187], v[192:195], v[148:151]\n"
    "v_exp_f32_e32 v221, v221\n"
    "v_mfma_f32_16x16x32_bf16 v[148:151], a[188:191], v[196:199], v[148:151]\n"
    "ds_write_addtid_b32 v226 offset:12800\n"
    "v_cvt_pk_bf16_f32 v227, v230, v231\n"
    "v_mfma_f32_16x16x32_bf16 v[144:147], a[188:191], v[188:191], v[144:147]\n"
    "v_exp_f32_e32 v222, v222\n"
    "v_mfma_f32_16x16x32_bf16 v[144:147], a[184:187], v[184:187], v[144:147]\n"
    "v_exp_f32_e32 v223, v223\n"
    "v_mfma_f32_16x16x32_bf16 v[140:143], a[184:187], v[176:179], v[140:143]\n"
    "ds_read_b128 a[184:187], v243 offset:0\n"
    "v_cvt_pk_bf16_f32 v218, v220, v221\n"
    "v_mfma_f32_16x16x32_bf16 v[140:143], a[188:191], v[180:183], v[140:143]\n"
    "s_waitcnt lgkmcnt(11)\n"
    "ds_read_b128 a[188:191], v242 offset:4096\n"
    "v_cvt_pk_bf16_f32 v219, v222, v223\n"
    "v_mfma_f32_16x16x32_bf16 v[176:179], a[160:163], a[92:95], 0\n"
    "s_waitcnt vmcnt(0)\n"
    "ds_write_addtid_b32 v227 offset:13056\n"
    "v_cmp_ne_u32_e32 vcc, s39, v255\n"
    "v_mfma_f32_16x16x32_bf16 v[176:179], a[164:167], a[64:67], v[176:179]\n"
    "ds_write_addtid_b32 v218 offset:8704\n"
    "v_mfma_f32_16x16x32_bf16 v[184:187], a[164:167], a[96:99], 0\n"
    "s_cbranch_vccnz .Lat_wpoll\n"
    ".Lat_wpoll_ret:\n"
    "v_mfma_f32_16x16x32_bf16 v[184:187], a[160:163], a[124:127], v[184:187]\n"
    "ds_write_addtid_b32 v219 offset:8960\n"
    "v_mfma_f32_16x16x32_bf16 v[192:195], a[160:163], a[156:159], 0\n"
    "ds_read_b128 a[160:163], v243 offset:6144\n"
    "v_mfma_f32_16x16x32_bf16 v[192:195], a[164:167], a[128:131], v[192:195]\n"
    "s_waitcnt lgkmcnt(12)\n"
    "ds_read_b128 a[164:167], v240 offset:2048\n"
    "v_mfma_f32_16x16x32_bf16 v[192:195], a[168:171], a[132:135], v[192:195]\n"
    "v_mfma_f32_16x16x32_bf16 v[192:195], a[172:175], a[136:139], v[192:195]\n"
    "s_waitcnt lgkmcnt(2)\n"
    "v_mfma_f32_16x16x32_bf16 v[184:187], a[172:175], a[104:107], v[184:187]\n"
    "s_barrier\n"
    "v_mfma_f32_16x16x32_bf16 v[184:187], a[168:171], a[100:103], v[184:187]\n"
    "s_add_u32 s61, s47, 5791744\n"
    "v_mfma_f32_16x16x32_bf16 v[176:179], a[168:171], a[68:71], v[176:179]\n"
    "s_add_u32 m0, s49, 20480\n"
    "ds_read_b128 a[168:171], v241 offset:2048\n"
    "v_mfma_f32_16x16x32_bf16 v[176:179], a[172:175], a[72:75], v[176:179]\n"
    "ds_read_b128 a[172:175], v240 offset:6144\n"
    "v_mfma_f32_16x16x32_bf16 v[176:179], a[176:179], a[76:79], v[176:179]\n"
    "buffer_load_dwordx4 v252, s[8:11], s61 offen offset:0 sc1 lds\n"
    "v_mfma_f32_16x16x32_bf16 v[176:179], a[180:183], a[80:83], v[176:179]\n"
    "s_add_u32 s61, s47, 5795840\n"
    "ds_read2st64_b32 v[220:221], v250 offset0:32 offset1:33\n"
    "v_mfma_f32_16x16x32_bf16 v[184:187], a[180:183], a[112:115], v[184:187]\n"
    "s_add_u32 m0, s49, 24576\n"
    "v_mfma_f32_16x16x32_bf16 v[184:187], a[176:179], a[108:111], v[184:187]\n"
    "v_mfma_f32_16x16x32_bf16 v[192:195], a[176:179], a[140:143], v[192:195]\n"
    "ds_read_b128 a[176:179], v241 offset:6144\n"
    "buffer_load_dwordx4 v252, s[8:11], s61 offen offset:0 sc1 lds\n"
    "v_mfma_f32_16x16x32_bf16 v[192:195], a[180:183], a[144:147], v[192:195]\n"
    "s_add_u32 s61, s47, 5799936\n"
    "ds_read_b128 a[180:183], v242 offset:2048\n"
    "v_mfma_f32_16x16x32_bf16 v[192:195], a[184:187], a[148:151], v[192:195]\n"
    "s_add_u32 m0, s49, 28672\n"
    "ds_read2st64_b32 v[222:223], v250 offset0:34 offset1:35\n"
    "v_mfma_f32_16x16x32_bf16 v[192:195], a[188:191], a[152:155], v[192:195]\n"
    "v_mfma_f32_16x16x32_bf16 v[184:187], a[188:191], a[120:123], v[184:187]\n"
    "buffer_load_dwordx4 v252, s[8:11], s61 offen offset:0 sc1 lds\n"
    "v_mfma_f32_16x16x32_bf16 v[184:187], a[184:187], a[116:119], v[184:187]\n"
    "s_mov_b32 m0, s49\n"
    "ds_read2st64_b32 v[228:229], v250 offset0:48 offset1:49\n"
    "v_mfma_f32_16x16x32_bf16 v[176:179], a[184:187], a[84:87], v[176:179]\n"
    "s_add_u32 s48, s57, 30720\n"
    "ds_read_b128 a[184:187], v243 offset:2048\n"
    "v_exp_f32_e32 v192, v192\n"
    "v_mfma_f32_16x16x32_bf16 v[176:179], a[188:191], a[88:91], v[176:179]\n"
    "s_waitcnt lgkmcnt(8)\n"
    "ds_read_b128 a[188:191], v242 offset:6144\n"
    "v_mfma_f32_16x16x32_bf16 v[180:183], a[160:163], a[92:95], 0\n"
    "buffer_load_dwordx4 a[192:195], v253, s[8:11], s48 offen offset:0 sc1\n"
    "v_mfma_f32_16x16x32_bf16 v[180:183], a[164:167], a[64:67], v[180:183]\n"
    "v_exp_f32_e32 v193, v193\n"
    "v_mfma_f32_16x16x32_bf16 v[188:191], a[164:167], a[96:99], 0\n"
    "v_exp_f32_e32 v194, v194\n"
    "v_mfma_f32_16x16x32_bf16 v[188:191], a[160:163], a[124:127], v[188:191]\n"
    "ds_read2st64_b32 v[230:231], v250 offset0:50 offset1:51\n"
    "v_exp_f32_e32 v195, v195\n"
    "v_mfma_f32_16x16x32_bf16 v[196:199], a[160:163], a[156:159], 0\n"
    "ds_read_b64_tr_b16 a[160:161], v244 offset:49152\n"
    "buffer_load_dwordx4 a[196:199], v253, s[8:11], s48 offen offset:1024 sc1\n"
    "v_mfma_f32_16x16x32_bf16 v[196:199], a[164:167], a[128:131], v[196:199]\n"
    "s_waitcnt lgkmcnt(9)\n"
    "s_add_u32 s48, s57, 14336\n"
    "ds_read_b64_tr_b16 a[162:163], v244 offset:51200\n"
    "v_mfma_f32_16x16x32_bf16 v[196:199], a[168:171], a[132:135], v[196:199]\n"
    "ds_read_b64_tr_b16 a[164:165], v244 offset:57344\n"
    "v_cvt_pk_bf16_f32 v192, v192, v193\n"
    "v_mfma_f32_16x16x32_bf16 v[196:199], a[172:175], a[136:139], v[196:199]\n"
    "ds_read_b64_tr_b16 a[166:167], v244 offset:59392\n"
    "v_mfma_f32_16x16x32_bf16 v[188:191], a[172:175], a[104:107], v[188:191]\n"
    "buffer_load_dwordx4 a[200:203], v253, s[8:11], s48 offen offset:0 sc1\n"
    "v_mfma_f32_16x16x32_bf16 v[188:191], a[168:171], a[100:103], v[188:191]\n"
    "ds_read2st64_b32 v[236:237], v250 offset0:64 offset1:65\n"
    "v_mfma_f32_16x16x32_bf16 v[180:183], a[168:171], a[68:71], v[180:183]\n"
    "ds_read_b64_tr_b16 a[168:169], v245 offset:49152\n"
    "v_mfma_f32_16x16x32_bf16 v[180:183], a[172:175], a[72:75], v[180:183]\n"
    "s_waitcnt lgkmcnt(11)\n"
    "ds_read_b64_tr_b16 a[170:171], v245 offset:51200\n"
    "v_mfma_f32_16x16x32_bf16 v[180:183], a[176:179], a[76:79], v[180:183]\n"
    "ds_read_b64_tr_b16 a[172:173], v245 offset:57344\n"
    "buffer_load_dwordx4 a[204:207], v253, s[8:11], s48 offen offset:1024 sc1\n"
    "v_mfma_f32_16x16x32_bf16 v[180:183], a[180:183], a[80:83], v[180:183]\n"
    "s_add_u32 s48, s57, 16384\n"
    "ds_read_b64_tr_b16 a[174:175], v245 offset:59392\n"
    "v_mfma_f32_16x16x32_bf16 v[188:191], a[180:183], a[112:115], v[188:191]\n"
    "ds_read2st64_b32 v[238:239], v250 offset0:66 offset1:67\n"
    "v_mfma_f32_16x16x32_bf16 v[188:191], a[176:179], a[108:111], v[188:191]\n"
    "v_exp_f32_e32 v184, v184\n"
    "v_mfma_f32_16x16x32_bf16 v[196:199], a[176:179], a[140:143], v[196:199]\n"
    "ds_read_b64_tr_b16 a[176:177], v246 offset:49152\n"
    "buffer_load_dwordx4 a[208:211], v253, s[8:11], s48 offen offset:0 sc1\n"
    "v_mfma_f32_16x16x32_bf16 v[196:199], a[180:183], a[144:147], v[196:199]\n"
    "s_waitcnt lgkmcnt(12)\n"
    "ds_read_b64_tr_b16 a[178:179], v246 offset:51200\n"
    "v_mfma_f32_16x16x32_bf16 v[196:199], a[184:187], a[148:151], v[196:199]\n"
    "ds_read_b64_tr_b16 a[180:181], v246 offset:57344\n"
    "v_mfma_f32_16x16x32_bf16 v[196:199], a[188:191], a[152:155], v[196:199]\n"
    "ds_read_b64_tr_b16 a[182:183], v246 offset:59392\n"
    "v_mfma_f32_16x16x32_bf16 v[188:191], a[188:191], a[120:123], v[188:191]\n"
    "buffer_load_dwordx4 a[212:215], v253, s[8:11], s48 offen offset:1024 sc1\n"
    "v_mfma_f32_16x16x32_bf16 v[188:191], a[184:187], a[116:119], v[188:191]\n"
    "s_add_u32 s48, s57, 0\n"
    "v_exp_f32_e32 v185, v185\n"
    "v_mfma_f32_16x16x32_bf16 v[180:183], a[184:187], a[84:87], v[180:183]\n"
    "ds_read_b64_tr_b16 a[184:185], v247 offset:49152\n"
    "v_cvt_pk_bf16_f32 v193, v194, v195\n"
    "v_mfma_f32_16x16x32_bf16 v[180:183], a[188:191], a[88:91], v[180:183]\n"
    "s_waitcnt lgkmcnt(5)\n"
    "ds_read_b64_tr_b16 a[186:187], v247 offset:51200\n"
    "v_mfma_f32_16x16x32_bf16 v[56:59], a[160:163], v[216:219], v[56:59]\n"
    "ds_read_b64_tr_b16 a[188:189], v247 offset:57344\n"
    "buffer_load_dwordx4 a[216:219], v253, s[8:11], s48 offen offset:0 sc1\n"
    "v_mfma_f32_16x16x32_bf16 v[56:59], a[164:167], v[220:223], v[56:59]\n"
    "ds_read_b64_tr_b16 a[190:191], v247 offset:59392\n"
    "v_mfma_f32_16x16x32_bf16 v[60:63], a[164:167], v[228:231], v[60:63]\n"
    "v_exp_f32_e32 v186, v186\n"
    "v_mfma_f32_16x16x32_bf16 v[60:63], a[160:163], v[224:227], v[60:63]\n"
    "v_exp_f32_e32 v187, v187\n"
    "v_mfma_f32_16x16x32_bf16 v[64:67], a[160:163], v[232:235], v[64:67]\n"
    "ds_read_b64_tr_b16 a[160:161], v244 offset:53248\n"
    "buffer_load_dwordx4 a[220:223], v253, s[8:11], s48 offen offset:1024 sc1\n"
    "v_mfma_f32_16x16x32_bf16 v[64:67], a[164:167], v[236:239], v[64:67]\n"
    "ds_read_b64_tr_b16 a[162:163], v244 offset:55296\n"
    "v_cvt_pk_bf16_f32 v184, v184, v185\n"
    "v_mfma_f32_16x16x32_bf16 v[76:79], a[168:171], v[232:235], v[76:79]\n"
    "ds_read_b64_tr_b16 a[164:165], v244 offset:61440\n"
    "v_exp_f32_e32 v176, v176\n"
    "v_mfma_f32_16x16x32_bf16 v[76:79], a[172:175], v[236:239], v[76:79]\n"
    "s_add_u32 s61, s28, 24576\n"
    "ds_read_b64_tr_b16 a[166:167], v244 offset:63488\n"
    "v_mfma_f32_16x16x32_bf16 v[72:75], a[172:175], v[228:231], v[72:75]\n"
    "v_and_b32_e32 v254, 0x80, v253\n"
    "v_mfma_f32_16x16x32_bf16 v[72:75], a[168:171], v[224:227], v[72:75]\n"
    "v_exp_f32_e32 v177, v177\n"
    "v_mfma_f32_16x16x32_bf16 v[68:71], a[168:171], v[216:219], v[68:71]\n"
    "ds_read_b64_tr_b16 a[168:169], v245 offset:53248\n"
    "buffer_load_dword v249, v254, s[8:11], s61 offen offset:0 sc1\n"
    "v_mfma_f32_16x16x32_bf16 v[68:71], a[172:175], v[220:223], v[68:71]\n"
    "s_waitcnt lgkmcnt(9)\n"
    "s_add_u32 s61, s29, 24576\n"
    "ds_read_b64_tr_b16 a[170:171], v245 offset:55296\n"
    "v_mfma_f32_16x16x32_bf16 v[80:83], a[176:179], v[216:219], v[80:83]\n"
    "ds_read_b64_tr_b16 a[172:173], v245 offset:61440\n"
    "v_cvt_pk_bf16_f32 v185, v186, v187\n"
    "v_mfma_f32_16x16x32_bf16 v[80:83], a[180:183], v[220:223], v[80:83]\n"
    "ds_read_b64_tr_b16 a[174:175], v245 offset:63488\n"
    "buffer_load_dword v249, v254, s[8:11], s61 offen offset:0 sc1\n"
    "v_mfma_f32_16x16x32_bf16 v[84:87], a[180:183], v[228:231], v[84:87]\n"
    "s_add_u32 s61, s30, 24576\n"
    "v_exp_f32_e32 v178, v178\n"
    "v_mfma_f32_16x16x32_bf16 v[84:87], a[176:179], v[224:227], v[84:87]\n"
    "s_waitcnt vmcnt(14)\n"
    "v_exp_f32_e32 v179, v179\n"
    "v_mfma_f32_16x16x32_bf16 v[88:91], a[176:179], v[232:235], v[88:91]\n"
    "s_barrier\n"
    "v_mfma_f32_16x16x32_bf16 v[88:91], a[180:183], v[236:239], v[88:91]\n"
    "s_waitcnt lgkmcnt(8)\n"
    "ds_read_b64_tr_b16 a[176:177], v246 offset:53248\n"
    "buffer_load_dword v249, v254, s[8:11], s61 offen offset:0 sc1\n"
    "v_mfma_f32_16x16x32_bf16 v[100:103], a[184:187], v[232:235], v[100:103]\n"
    "s_add_u32 s61, s31, 24576\n"
    "ds_read_b64_tr_b16 a[178:179], v246 offset:55296\n"
    "v_cvt_pk_bf16_f32 v176, v176, v177\n"
    "v_mfma_f32_16x16x32_bf16 v[100:103], a[188:191], v[236:239], v[100:103]\n"
    "ds_read_b64_tr_b16 a[180:181], v246 offset:61440\n"
    "v_cvt_pk_bf16_f32 v177, v178, v179\n"
    "v_mfma_f32_16x16x32_bf16 v[96:99], a[188:191], v[228:231], v[96:99]\n"
    "ds_read_b64_tr_b16 a[182:183], v246 offset:63488\n"
    "buffer_load_dword v249, v254, s[8:11], s61 offen offset:0 sc1\n"
    "v_mfma_f32_16x16x32_bf16 v[96:99], a[184:187], v[224:227], v[96:99]\n"
    "v_exp_f32_e32 v196, v196\n"
    "v_mfma_f32_16x16x32_bf16 v[92:95], a[184:187], v[216:219], v[92:95]\n"
    "ds_read_b64_tr_b16 a[184:185], v247 offset:53248\n"
    "ds_write_addtid_b32 v192 offset:16384\n"
    "v_mfma_f32_16x16x32_bf16 v[92:95], a[188:191], v[220:223], v[92:95]\n"
    "s_waitcnt lgkmcnt(10)\n"
    "ds_read_b64_tr_b16 a[186:187], v247 offset:55296\n"
    "ds_write_addtid_b32 v193 offset:16640\n"
    "v_mfma_f32_16x16x32_bf16 v[104:107], a[160:163], v[216:219], v[104:107]\n"
    "ds_read_b64_tr_b16 a[188:189], v247 offset:61440\n"
    "buffer_load_dword v249, v254, s[8:11], s87 offen offset:0 sc1\n"
    "v_mfma_f32_16x16x32_bf16 v[104:107], a[164:167], v[220:223], v[104:107]\n"
    "ds_read_b64_tr_b16 a[190:191], v247 offset:63488\n"
    "ds_write_addtid_b32 v184 offset:12288\n"
    "v_mfma_f32_16x16x32_bf16 v[108:111], a[164:167], v[228:231], v[108:111]\n"
    "v_exp_f32_e32 v197, v197\n"
    "v_mfma_f32_16x16x32_bf16 v[108:111], a[160:163], v[224:227], v[108:111]\n"
    "v_exp_f32_e32 v198, v198\n"
    "v_mfma_f32_16x16x32_bf16 v[112:115], a[160:163], v[232:235], v[112:115]\n"
    "ds_read_b128 a[160:163], v243 offset:20480\n"
    "ds_write_addtid_b32 v185 offset:12544\n"
    "v_mfma_f32_16x16x32_bf16 v[112:115], a[164:167], v[236:239], v[112:115]\n"
    "s_waitcnt lgkmcnt(13)\n"
    "ds_read_b128 a[164:167], v240 offset:16384\n"
    "v_mfma_f32_16x16x32_bf16 v[124:127], a[168:171], v[232:235], v[124:127]\n"
    "v_exp_f32_e32 v199, v199\n"
    "v_mfma_f32_16x16x32_bf16 v[124:127], a[172:175], v[236:239], v[124:127]\n"
    "ds_write_addtid_b32 v176 offset:8192\n"
    "v_cvt_pk_bf16_f32 v194, v196, v197\n"
    "v_mfma_f32_16x16x32_bf16 v[120:123], a[172:175], v[228:231], v[120:123]\n"
    "v_exp_f32_e32 v188, v188\n"
    "v_mfma_f32_16x16x32_bf16 v[120:123], a[168:171], v[224:227], v[120:123]\n"
    "v_exp_f32_e32 v189, v189\n"
    "v_mfma_f32_16x16x32_bf16 v[116:119], a[168:171], v[216:219], v[116:119]\n"
    "ds_read_b128 a[168:171], v241 offset:16384\n"
    "v_cvt_pk_bf16_f32 v195, v198, v199\n"
    "v_mfma_f32_16x16x32_bf16 v[116:119], a[172:175], v[220:223], v[116:119]\n"
    "s_waitcnt lgkmcnt(12)\n"
    "ds_read_b128 a[172:175], v240 offset:20480\n"
    "ds_write_addtid_b32 v177 offset:8448\n"
    "v_mfma_f32_16x16x32_bf16 v[128:131], a[176:179], v[216:219], v[128:131]\n"
    "v_exp_f32_e32 v190, v190\n"
    "v_mfma_f32_16x16x32_bf16 v[128:131], a[180:183], v[220:223], v[128:131]\n"
    "v_exp_f32_e32 v191, v191\n"
    "v_mfma_f32_16x16x32_bf16 v[132:135], a[180:183], v[228:231], v[132:135]\n"
    "ds_write_addtid_b32 v194 offset:16896\n"
    "v_cvt_pk_bf16_f32 v186, v188, v189\n"
    "v_mfma_f32_16x16x32_bf16 v[132:135], a[176:179], v[224:227], v[132:135]\n"
    "v_exp_f32_e32 v180, v180\n"
    "v_mfma_f32_16x16x32_bf16 v[136:139], a[176:179], v[232:235], v[136:139]\n"
    "ds_read_b128 a[176:179], v241 offset:20480\n"
    "v_mfma_f32_16x16x32_bf16 v[136:139], a[180:183], v[236:239], v[136:139]\n"
    "s_waitcnt lgkmcnt(10)\n"
    "ds_read_b128 a[180:183], v242 offset:16384\n"
    "v_mfma_f32_16x16x32_bf16 v[148:151], a[184:187], v[232:235], v[148:151]\n"
    "v_exp_f32_e32 v181, v181\n"
    "v_mfma_f32_16x16x32_bf16 v[148:151], a[188:191], v[236:239], v[148:151]\n"
    "ds_write_addtid_b32 v195 offset:17152\n"
    "v_cvt_pk_bf16_f32 v187, v190, v191\n"
    "v_mfma_f32_16x16x32_bf16 v[144:147], a[188:191], v[228:231], v[144:147]\n"
    "v_exp_f32_e32 v182, v182\n"
    "v_mfma_f32_16x16x32_bf16 v[144:147], a[184:187], v[224:227], v[144:147]\n"
    "v_exp_f32_e32 v183, v183\n"
    "v_mfma_f32_16x16x32_bf16 v[140:143], a[184:187], v[216:219], v[140:143]\n"
    "ds_read_b128 a[184:187], v243 offset:16384\n"
    "v_cvt_pk_bf16_f32 v178, v180, v181\n"
    "v_mfma_f32_16x16x32_bf16 v[140:143], a[188:191], v[220:223], v[140:143]\n"
    "s_waitcnt lgkmcnt(9)\n"
    "ds_read_b128 a[188:191], v242 offset:20480\n"
    "v_cvt_pk_bf16_f32 v179, v182, v183\n"
    "v_mfma_f32_16x16x32_bf16 v[216:219], a[160:163], a[92:95], 0\n"
    "ds_write_addtid_b32 v186 offset:12800\n"
    "v_mfma_f32_16x16x32_bf16 v[216:219], a[164:167], a[64:67], v[216:219]\n"
    "ds_write_addtid_b32 v187 offset:13056\n"
    "v_mfma_f32_16x16x32_bf16 v[224:227], a[164:167], a[96:99], 0\n"
    "ds_write_addtid_b32 v178 offset:8704\n"
    "v_mfma_f32_16x16x32_bf16 v[224:227], a[160:163], a[124:127], v[224:227]\n"
    "ds_write_addtid_b32 v179 offset:8960\n"
    "v_mfma_f32_16x16x32_bf16 v[232:235], a[160:163], a[156:159], 0\n"
    "ds_read_b128 a[160:163], v243 offset:22528\n"
    "v_mfma_f32_16x16x32_bf16 v[232:235], a[164:167], a[128:131], v[232:235]\n"
    "s_waitcnt lgkmcnt(12)\n"
    "ds_read_b128 a[164:167], v240 offset:18432\n"
    "v_mfma_f32_16x16x32_bf16 v[232:235], a[168:171], a[132:135], v[232:235]\n"
    "v_mfma_f32_16x16x32_bf16 v[232:235], a[172:175], a[136:139], v[232:235]\n"
    "s_waitcnt lgkmcnt(2)\n"
    "v_mfma_f32_16x16x32_bf16 v[224:227], a[172:175], a[104:107], v[224:227]\n"
    "s_barrier\n"
    "v_mfma_f32_16x16x32_bf16 v[224:227], a[168:171], a[100:103], v[224:227]\n"
    "v_mfma_f32_16x16x32_bf16 v[216:219], a[168:171], a[68:71], v[216:219]\n"
    "ds_read_b128 a[168:171], v241 offset:18432\n"
    "v_mfma_f32_16x16x32_bf16 v[216:219], a[172:175], a[72:75], v[216:219]\n"
    "ds_read_b128 a[172:175], v240 offset:22528\n"
    "v_mfma_f32_16x16x32_bf16 v[216:219], a[176:179], a[76:79], v[216:219]\n"
    "ds_read2st64_b32 v[180:181], v250 offset0:32 offset1:33\n"
    "v_mfma_f32_16x16x32_bf16 v[216:219], a[180:183], a[80:83], v[216:219]\n"
    "v_mfma_f32_16x16x32_bf16 v[224:227], a[180:183], a[112:115], v[224:227]\n"
    "v_mfma_f32_16x16x32_bf16 v[224:227], a[176:179], a[108:111], v[224:227]\n"
    "v_mfma_f32_16x16x32_bf16 v[232:235], a[176:179], a[140:143], v[232:235]\n"
    "ds_read_b128 a[176:179], v241 offset:22528\n"
    "v_mfma_f32_16x16x32_bf16 v[232:235], a[180:183], a[144:147], v[232:235]\n"
    "ds_read_b128 a[180:183], v242 offset:18432\n"
    "v_mfma_f32_16x16x32_bf16 v[232:235], a[184:187], a[148:151], v[232:235]\n"
    "ds_read2st64_b32 v[182:183], v250 offset0:34 offset1:35\n"
    "v_mfma_f32_16x16x32_bf16 v[232:235], a[188:191], a[152:155], v[232:235]\n"
    "v_mfma_f32_16x16x32_bf16 v[224:227], a[188:191], a[120:123], v[224:227]\n"
    "v_mfma_f32_16x16x32_bf16 v[224:227], a[184:187], a[116:119], v[224:227]\n"
    "ds_read2st64_b32 v[188:189], v250 offset0:48 offset1:49\n"
    "v_mfma_f32_16x16x32_bf16 v[216:219], a[184:187], a[84:87], v[216:219]\n"
    "ds_read_b128 a[184:187], v243 offset:18432\n"
    "v_mfma_f32_16x16x32_bf16 v[216:219], a[188:191], a[88:91], v[216:219]\n"
    "s_waitcnt lgkmcnt(8)\n"
    "ds_read_b128 a[188:191], v242 offset:22528\n"
    "v_mfma_f32_16x16x32_bf16 v[220:223], a[160:163], a[92:95], 0\n"
    "v_exp_f32_e32 v232, v232\n"
    "v_mfma_f32_16x16x32_bf16 v[220:223], a[164:167], a[64:67], v[220:223]\n"
    "v_exp_f32_e32 v233, v233\n"
    "v_mfma_f32_16x16x32_bf16 v[228:231], a[164:167], a[96:99], 0\n"
    "s_add_u32 s48, s57, 18432\n"
    "v_exp_f32_e32 v234, v234\n"
    "v_mfma_f32_16x16x32_bf16 v[228:231], a[160:163], a[124:127], v[228:231]\n"
    "ds_read2st64_b32 v[190:191], v250 offset0:50 offset1:51\n"
    "v_mfma_f32_16x16x32_bf16 v[236:239], a[160:163], a[156:159], 0\n"
    "ds_read_b64_tr_b16 a[160:161], v244 offset:0\n"
    "v_exp_f32_e32 v235, v235\n"
    "v_mfma_f32_16x16x32_bf16 v[236:239], a[164:167], a[128:131], v[236:239]\n"
    "s_waitcnt lgkmcnt(9)\n"
    "ds_read_b64_tr_b16 a[162:163], v244 offset:2048\n"
    "v_mfma_f32_16x16x32_bf16 v[236:239], a[168:171], a[132:135], v[236:239]\n"
    "ds_read_b64_tr_b16 a[164:165], v244 offset:8192\n"
    "buffer_load_dwordx4 a[92:95], v253, s[8:11], s48 offen offset:0 sc1\n"
    "v_mfma_f32_16x16x32_bf16 v[236:239], a[172:175], a[136:139], v[236:239]\n"
    "ds_read_b64_tr_b16 a[166:167], v244 offset:10240\n"
    "v_cvt_pk_bf16_f32 v232, v232, v233\n"
    "v_mfma_f32_16x16x32_bf16 v[228:231], a[172:175], a[104:107], v[228:231]\n"
    "ds_read2st64_b32 v[196:197], v250 offset0:64 offset1:65\n"
    "v_mfma_f32_16x16x32_bf16 v[228:231], a[168:171], a[100:103], v[228:231]\n"
    "v_exp_f32_e32 v224, v224\n"
    "v_mfma_f32_16x16x32_bf16 v[220:223], a[168:171], a[68:71], v[220:223]\n"
    "ds_read_b64_tr_b16 a[168:169], v245 offset:0\n"
    "buffer_load_dwordx4 a[124:127], v253, s[8:11], s48 offen offset:1024 sc1\n"
    "v_mfma_f32_16x16x32_bf16 v[220:223], a[172:175], a[72:75], v[220:223]\n"
    "s_waitcnt lgkmcnt(11)\n"
    "s_add_u32 s48, s57, 2048\n"
    "ds_read_b64_tr_b16 a[170:171], v245 offset:2048\n"
    "v_mfma_f32_16x16x32_bf16 v[220:223], a[176:179], a[76:79], v[220:223]\n"
    "ds_read_b64_tr_b16 a[172:173], v245 offset:8192\n"
    "v_exp_f32_e32 v225, v225\n"
    "v_mfma_f32_16x16x32_bf16 v[220:223], a[180:183], a[80:83], v[220:223]\n"
    "ds_read_b64_tr_b16 a[174:175], v245 offset:10240\n"
    "v_cvt_pk_bf16_f32 v233, v234, v235\n"
    "v_mfma_f32_16x16x32_bf16 v[228:231], a[180:183], a[112:115], v[228:231]\n"
    "buffer_load_dwordx4 a[156:159], v253, s[8:11], s48 offen offset:0 sc1\n"
    "v_mfma_f32_16x16x32_bf16 v[228:231], a[176:179], a[108:111], v[228:231]\n"
    "ds_read2st64_b32 v[198:199], v250 offset0:66 offset1:67\n"
    "v_exp_f32_e32 v226, v226\n"
    "v_mfma_f32_16x16x32_bf16 v[236:239], a[176:179], a[140:143], v[236:239]\n"
    "ds_read_b64_tr_b16 a[176:177], v246 offset:0\n"
    "v_mfma_f32_16x16x32_bf16 v[236:239], a[180:183], a[144:147], v[236:239]\n"
    "s_waitcnt lgkmcnt(12)\n"
    "ds_read_b64_tr_b16 a[178:179], v246 offset:2048\n"
    "v_exp_f32_e32 v227, v227\n"
    "v_mfma_f32_16x16x32_bf16 v[236:239], a[184:187], a[148:151], v[236:239]\n"
    "ds_read_b64_tr_b16 a[180:181], v246 offset:8192\n"
    "buffer_load_dwordx4 a[64:67], v253, s[8:11], s48 offen offset:1024 sc1\n"
    "v_mfma_f32_16x16x32_bf16 v[236:239], a[188:191], a[152:155], v[236:239]\n"
    "s_add_u32 s48, s57, 20480\n"
    "ds_read_b64_tr_b16 a[182:183], v246 offset:10240\n"
    "v_cvt_pk_bf16_f32 v224, v224, v225\n"
    "v_mfma_f32_16x16x32_bf16 v[228:231], a[188:191], a[120:123], v[228:231]\n"
    "v_exp_f32_e32 v216, v216\n"
    "v_mfma_f32_16x16x32_bf16 v[228:231], a[184:187], a[116:119], v[228:231]\n"
    "v_exp_f32_e32 v217, v217\n"
    "v_mfma_f32_16x16x32_bf16 v[220:223], a[184:187], a[84:87], v[220:223]\n"
    "ds_read_b64_tr_b16 a[184:185], v247 offset:0\n"
    "buffer_load_dwordx4 a[96:99], v253, s[8:11], s48 offen offset:0 sc1\n"
    "v_mfma_f32_16x16x32_bf16 v[220:223], a[188:191], a[88:91], v[220:223]\n"
    "s_waitcnt lgkmcnt(5)\n"
    "ds_read_b64_tr_b16 a[186:187], v247 offset:2048\n"
    "v_mfma_f32_16x16x32_bf16 v[56:59], a[160:163], v[176:179], v[56:59]\n"
    "ds_read_b64_tr_b16 a[188:189], v247 offset:8192\n"
    "v_mfma_f32_16x16x32_bf16 v[56:59], a[164:167], v[180:183], v[56:59]\n"
    "ds_read_b64_tr_b16 a[190:191], v247 offset:10240\n"
    "v_mfma_f32_16x16x32_bf16 v[60:63], a[164:167], v[188:191], v[60:63]\n"
    "buffer_load_dwordx4 a[128:131], v253, s[8:11], s48 offen offset:1024 sc1\n"
    "v_cvt_pk_bf16_f32 v225, v226, v227\n"
    "v_mfma_f32_16x16x32_bf16 v[60:63], a[160:163], v[184:187], v[60:63]\n"
    "s_add_u32 s48, s57, 4096\n"
    "v_exp_f32_e32 v218, v218\n"
    "v_mfma_f32_16x16x32_bf16 v[64:67], a[160:163], v[192:195], v[64:67]\n"
    "ds_read_b64_tr_b16 a[160:161], v244 offset:4096\n"
    "v_mfma_f32_16x16x32_bf16 v[64:67], a[164:167], v[196:199], v[64:67]\n"
    "ds_read_b64_tr_b16 a[162:163], v244 offset:6144\n"
    "v_mfma_f32_16x16x32_bf16 v[76:79], a[168:171], v[192:195], v[76:79]\n"
    "ds_read_b64_tr_b16 a[164:165], v244 offset:12288\n"
    "buffer_load_dwordx4 a[68:71], v253, s[8:11], s48 offen offset:0 sc1\n"
    "v_mfma_f32_16x16x32_bf16 v[76:79], a[172:175], v[196:199], v[76:79]\n"
    "ds_read_b64_tr_b16 a[166:167], v244 offset:14336\n"
    "v_mfma_f32_16x16x32_bf16 v[72:75], a[172:175], v[188:191], v[72:75]\n"
    "s_barrier\n"
    "v_mfma_f32_16x16x32_bf16 v[72:75], a[168:171], v[184:187], v[72:75]\n"
    "v_exp_f32_e32 v219, v219\n"
    "v_mfma_f32_16x16x32_bf16 v[68:71], a[168:171], v[176:179], v[68:71]\n"
    "ds_read_b64_tr_b16 a[168:169], v245 offset:4096\n"
    "buffer_load_dwordx4 a[100:103], v253, s[8:11], s48 offen offset:1024 sc1\n"
    "v_mfma_f32_16x16x32_bf16 v[68:71], a[172:175], v[180:183], v[68:71]\n"
    "s_waitcnt lgkmcnt(9)\n"
    "s_add_u32 s48, s57, 22528\n"
    "ds_read_b64_tr_b16 a[170:171], v245 offset:6144\n"
    "v_mfma_f32_16x16x32_bf16 v[80:83], a[176:179], v[176:179], v[80:83]\n"
    "ds_read_b64_tr_b16 a[172:173], v245 offset:12288\n"
    "v_cvt_pk_bf16_f32 v216, v216, v217\n"
    "v_mfma_f32_16x16x32_bf16 v[80:83], a[180:183], v[180:183], v[80:83]\n"
    "ds_read_b64_tr_b16 a[174:175], v245 offset:14336\n"
    "v_cvt_pk_bf16_f32 v217, v218, v219\n"
    "v_mfma_f32_16x16x32_bf16 v[84:87], a[180:183], v[188:191], v[84:87]\n"
    "buffer_load_dwordx4 a[132:135], v253, s[8:11], s48 offen offset:0 sc1\n"
    "ds_write_addtid_b32 v232 offset:16384\n"
    "v_mfma_f32_16x16x32_bf16 v[84:87], a[176:179], v[184:187], v[84:87]\n"
    "v_exp_f32_e32 v236, v236\n"
    "v_mfma_f32_16x16x32_bf16 v[88:91], a[176:179], v[192:195], v[88:91]\n"
    "ds_read_b64_tr_b16 a[176:177], v246 offset:4096\n"
    "ds_write_addtid_b32 v233 offset:16640\n"
    "v_mfma_f32_16x16x32_bf16 v[88:91], a[180:183], v[196:199], v[88:91]\n"
    "s_waitcnt lgkmcnt(11)\n"
    "ds_read_b64_tr_b16 a[178:179], v246 offset:6144\n"
    "v_mfma_f32_16x16x32_bf16 v[100:103], a[184:187], v[192:195], v[100:103]\n"
    "ds_read_b64_tr_b16 a[180:181], v246 offset:12288\n"
    "buffer_load_dwordx4 a[72:75], v253, s[8:11], s48 offen offset:1024 sc1\n"
    "v_mfma_f32_16x16x32_bf16 v[100:103], a[188:191], v[196:199], v[100:103]\n"
    "s_add_u32 s48, s57, 6144\n"
    "ds_read_b64_tr_b16 a[182:183], v246 offset:14336\n"
    "ds_write_addtid_b32 v224 offset:12288\n"
    "v_mfma_f32_16x16x32_bf16 v[96:99], a[188:191], v[188:191], v[96:99]\n"
    "v_exp_f32_e32 v237, v237\n"
    "v_mfma_f32_16x16x32_bf16 v[96:99], a[184:187], v[184:187], v[96:99]\n"
    "v_exp_f32_e32 v238, v238\n"
    "v_mfma_f32_16x16x32_bf16 v[92:95], a[184:187], v[176:179], v[92:95]\n"
    "ds_read_b64_tr_b16 a[184:185], v247 offset:4096\n"
    "buffer_load_dwordx4 a[104:107], v253, s[8:11], s48 offen offset:0 sc1\n"
    "v_mfma_f32_16x16x32_bf16 v[92:95], a[188:191], v[180:183], v[92:95]\n"
    "s_waitcnt lgkmcnt(12)\n"
    "ds_read_b64_tr_b16 a[186:187], v247 offset:6144\n"
    "ds_write_addtid_b32 v225 offset:12544\n"
    "v_mfma_f32_16x16x32_bf16 v[104:107], a[160:163], v[176:179], v[104:107]\n"
    "ds_read_b64_tr_b16 a[188:189], v247 offset:12288\n"
    "v_mfma_f32_16x16x32_bf16 v[104:107], a[164:167], v[180:183], v[104:107]\n"
    "ds_read_b64_tr_b16 a[190:191], v247 offset:14336\n"
    "v_mfma_f32_16x16x32_bf16 v[108:111], a[164:167], v[188:191], v[108:111]\n"
    "buffer_load_dwordx4 a[136:139], v253, s[8:11], s48 offen offset:1024 sc1\n"
    "ds_write_addtid_b32 v216 offset:8192\n"
    "v_mfma_f32_16x16x32_bf16 v[108:111], a[160:163], v[184:187], v[108:111]\n"
    "s_add_u32 s48, s57, 24576\n"
    "v_exp_f32_e32 v239, v239\n"
    "v_mfma_f32_16x16x32_bf16 v[112:115], a[160:163], v[192:195], v[112:115]\n"
    "ds_read_b128 a[160:163], v243 offset:36864\n"
    "v_cvt_pk_bf16_f32 v234, v236, v237\n"
    "v_mfma_f32_16x16x32_bf16 v[112:115], a[164:167], v[196:199], v[112:115]\n"
    "s_waitcnt lgkmcnt(14)\n"
    "ds_read_b128 a[164:167], v240 offset:32768\n"
    "v_mfma_f32_16x16x32_bf16 v[124:127], a[168:171], v[192:195], v[124:127]\n"
    "buffer_load_dwordx4 a[76:79], v253, s[8:11], s48 offen offset:0 sc1\n"
    "ds_write_addtid_b32 v217 offset:8448\n"
    "v_mfma_f32_16x16x32_bf16 v[124:127], a[172:175], v[196:199], v[124:127]\n"
    "v_exp_f32_e32 v228, v228\n"
    "v_mfma_f32_16x16x32_bf16 v[120:123], a[172:175], v[188:191], v[120:123]\n"
    "v_exp_f32_e32 v229, v229\n"
    "v_mfma_f32_16x16x32_bf16 v[120:123], a[168:171], v[184:187], v[120:123]\n"
    "ds_write_addtid_b32 v234 offset:16896\n"
    "v_cvt_pk_bf16_f32 v235, v238, v239\n"
    "v_mfma_f32_16x16x32_bf16 v[116:119], a[168:171], v[176:179], v[116:119]\n"
    "ds_read_b128 a[168:171], v241 offset:32768\n"
    "buffer_load_dwordx4 a[108:111], v253, s[8:11], s48 offen offset:1024 sc1\n"
    "v_mfma_f32_16x16x32_bf16 v[116:119], a[172:175], v[180:183], v[116:119]\n"
    "s_waitcnt lgkmcnt(12)\n"
    "s_add_u32 s48, s57, 8192\n"
    "ds_read_b128 a[172:175], v240 offset:36864\n"
    "v_mfma_f32_16x16x32_bf16 v[128:131], a[176:179], v[176:179], v[128:131]\n"
    "v_exp_f32_e32 v230, v230\n"
    "v_mfma_f32_16x16x32_bf16 v[128:131], a[180:183], v[180:183], v[128:131]\n"
    "v_exp_f32_e32 v231, v231\n"
    "v_mfma_f32_16x16x32_bf16 v[132:135], a[180:183], v[188:191], v[132:135]\n"
    "buffer_load_dwordx4 a[140:143], v253, s[8:11], s48 offen offset:0 sc1\n"
    "v_cvt_pk_bf16_f32 v226, v228, v229\n"
    "v_mfma_f32_16x16x32_bf16 v[132:135], a[176:179], v[184:187], v[132:135]\n"
    "v_exp_f32_e32 v220, v220\n"
    "v_mfma_f32_16x16x32_bf16 v[136:139], a[176:179], v[192:195], v[136:139]\n"
    "ds_read_b128 a[176:179], v241 offset:36864\n"
    "v_mfma_f32_16x16x32_bf16 v[136:139], a[180:183], v[196:199], v[136:139]\n"
    "s_waitcnt lgkmcnt(8)\n"
    "ds_read_b128 a[180:183], v242 offset:32768\n"
    "v_mfma_f32_16x16x32_bf16 v[148:151], a[184:187], v[192:195], v[148:151]\n"
    "buffer_load_dwordx4 a[80:83], v253, s[8:11], s48 offen offset:1024 sc1\n"
    "ds_write_addtid_b32 v235 offset:17152\n"
    "v_mfma_f32_16x16x32_bf16 v[148:151], a[188:191], v[196:199], v[148:151]\n"
    "s_add_u32 s48, s57, 26624\n"
    "v_exp_f32_e32 v221, v221\n"
    "v_mfma_f32_16x16x32_bf16 v[144:147], a[188:191], v[188:191], v[144:147]\n"
    "ds_write_addtid_b32 v226 offset:12800\n"
    "v_cvt_pk_bf16_f32 v227, v230, v231\n"
    "v_mfma_f32_16x16x32_bf16 v[144:147], a[184:187], v[184:187], v[144:147]\n"
    "v_exp_f32_e32 v222, v222\n"
    "v_mfma_f32_16x16x32_bf16 v[140:143], a[184:187], v[176:179], v[140:143]\n"
    "ds_read_b128 a[184:187], v243 offset:32768\n"
    "buffer_load_dwordx4 a[112:115], v253, s[8:11], s48 offen offset:0 sc1\n"
    "v_mfma_f32_16x16x32_bf16 v[140:143], a[188:191], v[180:183], v[140:143]\n"
    "s_waitcnt lgkmcnt(5)\n"
    "ds_read_b128 a[188:191], v242 offset:36864\n"
    "v_mfma_f32_16x16x32_bf16 v[160:163], a[160:163], a[28:31], 0\n"
    "v_exp_f32_e32 v223, v223\n"
    "v_mfma_f32_16x16x32_bf16 v[160:163], a[164:167], a[0:3], v[160:163]\n"
    "v_cvt_pk_bf16_f32 v218, v220, v221\n"
    "v_cvt_pk_bf16_f32 v219, v222, v223\n"
    "v_mfma_f32_16x16x32_bf16 v[168:171], a[164:167], a[32:35], 0\n"
    "buffer_load_dwordx4 a[144:147], v253, s[8:11], s48 offen offset:1024 sc1\n"
    "ds_write_addtid_b32 v227 offset:13056\n"
    "v_mfma_f32_16x16x32_bf16 v[168:171], a[160:163], a[60:63], v[168:171]\n"
    "s_add_u32 s48, s57, 10240\n"
    "ds_read_b128 a[160:163], v243 offset:38912\n"
    "v_mfma_f32_16x16x32_bf16 v[168:171], a[168:171], a[36:39], v[168:171]\n"
    "ds_read_b128 a[164:167], v240 offset:34816\n"
    "v_mfma_f32_16x16x32_bf16 v[168:171], a[172:175], a[40:43], v[168:171]\n"
    "ds_write_addtid_b32 v218 offset:8704\n"
    "v_mfma_f32_16x16x32_bf16 v[160:163], a[172:175], a[8:11], v[160:163]\n"
    "buffer_load_dwordx4 a[84:87], v253, s[8:11], s48 offen offset:0 sc1\n"
    "ds_write_addtid_b32 v219 offset:8960\n"
    "v_mfma_f32_16x16x32_bf16 v[160:163], a[168:171], a[4:7], v[160:163]\n"
    "s_waitcnt lgkmcnt(5)\n"
    "ds_read_b128 a[168:171], v241 offset:34816\n"
    "v_mfma_f32_16x16x32_bf16 v[160:163], a[176:179], a[12:15], v[160:163]\n"
    "ds_read_b128 a[172:175], v240 offset:38912\n"
    "v_mfma_f32_16x16x32_bf16 v[160:163], a[180:183], a[16:19], v[160:163]\n"
    "v_mfma_f32_16x16x32_bf16 v[168:171], a[180:183], a[48:51], v[168:171]\n"
    "buffer_load_dwordx4 a[116:119], v253, s[8:11], s48 offen offset:1024 sc1\n"
    "v_mfma_f32_16x16x32_bf16 v[168:171], a[176:179], a[44:47], v[168:171]\n"
    "s_waitcnt lgkmcnt(2)\n"
    "s_add_u32 s48, s57, 28672\n"
    "ds_read_b128 a[176:179], v241 offset:38912\n"
    "v_mfma_f32_16x16x32_bf16 v[168:171], a[184:187], a[52:55], v[168:171]\n"
    "s_barrier\n"
    "v_mfma_f32_16x16x32_bf16 v[168:171], a[188:191], a[56:59], v[168:171]\n"
    "ds_read_b128 a[180:183], v242 offset:34816\n"
    "v_mfma_f32_16x16x32_bf16 v[160:163], a[188:191], a[24:27], v[160:163]\n"
    "buffer_load_dwordx4 a[148:151], v253, s[8:11], s48 offen offset:0 sc1\n"
    "ds_read2st64_b32 v[220:221], v250 offset0:32 offset1:33\n"
    "v_mfma_f32_16x16x32_bf16 v[160:163], a[184:187], a[20:23], v[160:163]\n"
    "s_waitcnt lgkmcnt(3)\n"
    "ds_read_b128 a[184:187], v243 offset:34816\n"
    "v_mfma_f32_16x16x32_bf16 v[164:167], a[160:163], a[28:31], 0\n"
    "ds_read_b128 a[188:191], v242 offset:38912\n"
    "v_mfma_f32_16x16x32_bf16 v[164:167], a[164:167], a[0:3], v[164:167]\n"
    "ds_read2st64_b32 v[222:223], v250 offset0:34 offset1:35\n"
    "v_mfma_f32_16x16x32_bf16 v[172:175], a[164:167], a[32:35], 0\n"
    "buffer_load_dwordx4 a[88:91], v253, s[8:11], s48 offen offset:1024 sc1\n"
    "ds_read2st64_b32 v[228:229], v250 offset0:48 offset1:49\n"
    "v_mfma_f32_16x16x32_bf16 v[172:175], a[160:163], a[60:63], v[172:175]\n"
    "s_add_u32 s48, s57, 12288\n"
    "ds_read_b64_tr_b16 a[160:161], v244 offset:16384\n"
    "ds_read2st64_b32 v[230:231], v250 offset0:50 offset1:51\n"
    "v_mfma_f32_16x16x32_bf16 v[172:175], a[168:171], a[36:39], v[172:175]\n"
    "ds_read_b64_tr_b16 a[162:163], v244 offset:18432\n"
    "v_mfma_f32_16x16x32_bf16 v[172:175], a[172:175], a[40:43], v[172:175]\n"
    "ds_read_b64_tr_b16 a[164:165], v244 offset:24576\n"
    "ds_read2st64_b32 v[236:237], v250 offset0:64 offset1:65\n"
    "v_mfma_f32_16x16x32_bf16 v[164:167], a[172:175], a[8:11], v[164:167]\n"
    "ds_read_b64_tr_b16 a[166:167], v244 offset:26624\n"
    "buffer_load_dwordx4 a[120:123], v253, s[8:11], s48 offen offset:0 sc1\n"
    "v_mfma_f32_16x16x32_bf16 v[164:167], a[168:171], a[4:7], v[164:167]\n"
    "s_waitcnt lgkmcnt(8)\n"
    "ds_read_b64_tr_b16 a[168:169], v245 offset:16384\n"
    "ds_read2st64_b32 v[238:239], v250 offset0:66 offset1:67\n"
    "v_mfma_f32_16x16x32_bf16 v[164:167], a[176:179], a[12:15], v[164:167]\n"
    "ds_read_b64_tr_b16 a[170:171], v245 offset:18432\n"
    "v_mfma_f32_16x16x32_bf16 v[164:167], a[180:183], a[16:19], v[164:167]\n"
    "ds_read_b64_tr_b16 a[172:173], v245 offset:24576\n"
    "v_mfma_f32_16x16x32_bf16 v[172:175], a[180:183], a[48:51], v[172:175]\n"
    "ds_read_b64_tr_b16 a[174:175], v245 offset:26624\n"
    "buffer_load_dwordx4 a[152:155], v253, s[8:11], s48 offen offset:1024 sc1\n"
    "v_mfma_f32_16x16x32_bf16 v[172:175], a[176:179], a[44:47], v[172:175]\n"
    "s_waitcnt lgkmcnt(3)\n"
    "ds_read_b64_tr_b16 a[176:177], v246 offset:16384\n"
    "v_mfma_f32_16x16x32_bf16 v[172:175], a[184:187], a[52:55], v[172:175]\n"
    "s_barrier\n"
    "v_mfma_f32_16x16x32_bf16 v[172:175], a[188:191], a[56:59], v[172:175]\n"
    "s_add_u32 s61, s47, 5779456\n"
    "ds_read_b64_tr_b16 a[178:179], v246 offset:18432\n"
    "v_mfma_f32_16x16x32_bf16 v[164:167], a[188:191], a[24:27], v[164:167]\n"
    "s_add_u32 m0, s49, 8192\n"
    "ds_read_b64_tr_b16 a[180:181], v246 offset:24576\n"
    "v_mfma_f32_16x16x32_bf16 v[164:167], a[184:187], a[20:23], v[164:167]\n"
    "ds_read_b64_tr_b16 a[182:183], v246 offset:26624\n"
    "buffer_load_dwordx4 v252, s[8:11], s61 offen offset:0 sc1 lds\n"
    "v_mfma_f32_16x16x32_bf16 v[56:59], a[160:163], v[216:219], v[56:59]\n"
    "s_add_u32 s61, s47, 5783552\n"
    "ds_read_b64_tr_b16 a[184:185], v247 offset:16384\n"
    "v_mfma_f32_16x16x32_bf16 v[56:59], a[164:167], v[220:223], v[56:59]\n"
    "s_add_u32 m0, s49, 12288\n"
    "ds_read_b64_tr_b16 a[186:187], v247 offset:18432\n"
    "v_mfma_f32_16x16x32_bf16 v[60:63], a[164:167], v[228:231], v[60:63]\n"
    "ds_read_b64_tr_b16 a[188:189], v247 offset:24576\n"
    "buffer_load_dwordx4 v252, s[8:11], s61 offen offset:0 sc1 lds\n"
    "v_mfma_f32_16x16x32_bf16 v[60:63], a[160:163], v[224:227], v[60:63]\n"
    "s_add_u32 s61, s47, 5787648\n"
    "ds_read_b64_tr_b16 a[190:191], v247 offset:26624\n"
    "v_mfma_f32_16x16x32_bf16 v[64:67], a[160:163], v[232:235], v[64:67]\n"
    "s_add_u32 m0, s49, 16384\n"
    "ds_read_b64_tr_b16 a[160:161], v244 offset:20480\n"
    "v_lshrrev_b32_e32 v158, 4, v253\n"
    "v_mfma_f32_16x16x32_bf16 v[64:67], a[164:167], v[236:239], v[64:67]\n"
    "s_waitcnt lgkmcnt(9)\n"
    "ds_read_b64_tr_b16 a[162:163], v244 offset:22528\n"
    "buffer_load_dwordx4 v252, s[8:11], s61 offen offset:0 sc1 lds\n"
    "v_mfma_f32_16x16x32_bf16 v[76:79], a[168:171], v[232:235], v[76:79]\n"
    "s_mov_b32 m0, s49\n"
    "ds_read_b64_tr_b16 a[164:165], v244 offset:28672\n"
    "v_lshrrev_b32_e32 v159, 4, v158\n"
    "v_mfma_f32_16x16x32_bf16 v[76:79], a[172:175], v[236:239], v[76:79]\n"
    "ds_read_b64_tr_b16 a[166:167], v244 offset:30720\n"
    "v_cvt_pk_bf16_f32 v64, v64, v65\n"
    "v_mfma_f32_16x16x32_bf16 v[72:75], a[172:175], v[228:231], v[72:75]\n"
    "v_exp_f32_e32 v168, v168\n"
    "v_mfma_f32_16x16x32_bf16 v[72:75], a[168:171], v[224:227], v[72:75]\n"
    "v_exp_f32_e32 v169, v169\n"
    "v_mfma_f32_16x16x32_bf16 v[68:71], a[168:171], v[216:219], v[68:71]\n"
    "ds_read_b64_tr_b16 a[168:169], v245 offset:20480\n"
    "v_cvt_pk_bf16_f32 v65, v66, v67\n"
    "v_mfma_f32_16x16x32_bf16 v[68:71], a[172:175], v[220:223], v[68:71]\n"
    "s_waitcnt lgkmcnt(9)\n"
    "ds_read_b64_tr_b16 a[170:171], v245 offset:22528\n"
    "v_cvt_pk_bf16_f32 v66, v76, v77\n"
    "v_mfma_f32_16x16x32_bf16 v[80:83], a[176:179], v[216:219], v[80:83]\n"
    "ds_read_b64_tr_b16 a[172:173], v245 offset:28672\n"
    "v_cvt_pk_bf16_f32 v67, v78, v79\n"
    "v_mfma_f32_16x16x32_bf16 v[80:83], a[180:183], v[220:223], v[80:83]\n"
    "ds_read_b64_tr_b16 a[174:175], v245 offset:30720\n"
    "v_cvt_pk_bf16_f32 v60, v60, v61\n"
    "v_mfma_f32_16x16x32_bf16 v[84:87], a[180:183], v[228:231], v[84:87]\n"
    "v_exp_f32_e32 v170, v170\n"
    "v_mfma_f32_16x16x32_bf16 v[84:87], a[176:179], v[224:227], v[84:87]\n"
    "v_exp_f32_e32 v171, v171\n"
    "v_mfma_f32_16x16x32_bf16 v[88:91], a[176:179], v[232:235], v[88:91]\n"
    "ds_read_b64_tr_b16 a[176:177], v246 offset:20480\n"
    "buffer_store_dwordx4 v[64:67], v253, s[8:11], s86 offen offset:0 sc1\n"
    "v_cvt_pk_bf16_f32 v168, v168, v169\n"
    "v_cvt_pk_bf16_f32 v61, v62, v63\n"
    "v_bfe_u32 v255, v158, 2, 2\n"
    "v_mfma_f32_16x16x32_bf16 v[88:91], a[180:183], v[236:239], v[88:91]\n"
    "s_waitcnt lgkmcnt(9)\n"
    "ds_read_b64_tr_b16 a[178:179], v246 offset:22528\n"
    "v_cvt_pk_bf16_f32 v62, v72, v73\n"
    "v_mfma_f32_16x16x32_bf16 v[100:103], a[184:187], v[232:235], v[100:103]\n"
    "ds_read_b64_tr_b16 a[180:181], v246 offset:28672\n"
    "v_cvt_pk_bf16_f32 v63, v74, v75\n"
    "v_mfma_f32_16x16x32_bf16 v[100:103], a[188:191], v[236:239], v[100:103]\n"
    "ds_read_b64_tr_b16 a[182:183], v246 offset:30720\n"
    "v_cvt_pk_bf16_f32 v56, v56, v57\n"
    "v_mfma_f32_16x16x32_bf16 v[96:99], a[188:191], v[228:231], v[96:99]\n"
    "v_exp_f32_e32 v160, v160\n"
    "v_mfma_f32_16x16x32_bf16 v[96:99], a[184:187], v[224:227], v[96:99]\n"
    "v_exp_f32_e32 v161, v161\n"
    "v_and_b32_e32 v158, 3, v158\n"
    "v_mfma_f32_16x16x32_bf16 v[92:95], a[184:187], v[216:219], v[92:95]\n"
    "ds_read_b64_tr_b16 a[184:185], v247 offset:20480\n"
    "v_cvt_pk_bf16_f32 v169, v170, v171\n"
    "v_mfma_f32_16x16x32_bf16 v[92:95], a[188:191], v[220:223], v[92:95]\n"
    "s_waitcnt lgkmcnt(9)\n"
    "ds_read_b64_tr_b16 a[186:187], v247 offset:22528\n"
    "v_cvt_pk_bf16_f32 v57, v58, v59\n"
    "v_mfma_f32_16x16x32_bf16 v[104:107], a[160:163], v[216:219], v[104:107]\n"
    "ds_read_b64_tr_b16 a[188:189], v247 offset:28672\n"
    "v_cvt_pk_bf16_f32 v58, v68, v69\n"
    "v_mfma_f32_16x16x32_bf16 v[104:107], a[164:167], v[220:223], v[104:107]\n"
    "ds_read_b64_tr_b16 a[190:191], v247 offset:30720\n"
    "v_cvt_pk_bf16_f32 v59, v70, v71\n"
    "v_xor_b32_e32 v159, v159, v255\n"
    "v_mfma_f32_16x16x32_bf16 v[108:111], a[164:167], v[228:231], v[108:111]\n"
    "buffer_store_dwordx4 v[60:63], v253, s[8:11], s85 offen offset:0 sc1\n"
    "v_exp_f32_e32 v162, v162\n"
    "v_mfma_f32_16x16x32_bf16 v[108:111], a[160:163], v[224:227], v[108:111]\n"
    "v_exp_f32_e32 v163, v163\n"
    "v_mfma_f32_16x16x32_bf16 v[112:115], a[160:163], v[232:235], v[112:115]\n"
    "ds_read_b128 a[160:163], v243 offset:53248\n"
    "v_cvt_pk_bf16_f32 v160, v160, v161\n"
    "v_mfma_f32_16x16x32_bf16 v[112:115], a[164:167], v[236:239], v[112:115]\n"
    "s_waitcnt lgkmcnt(9)\n"
    "ds_read_b128 a[164:167], v240 offset:49152\n"
    "v_cvt_pk_bf16_f32 v161, v162, v163\n"
    "v_mfma_f32_16x16x32_bf16 v[124:127], a[168:171], v[232:235], v[124:127]\n"
    "v_exp_f32_e32 v172, v172\n"
    "v_lshl_or_b32 v158, v159, 2, v158\n"
    "v_mfma_f32_16x16x32_bf16 v[124:127], a[172:175], v[236:239], v[124:127]\n"
    "v_exp_f32_e32 v173, v173\n"
    "v_mfma_f32_16x16x32_bf16 v[120:123], a[172:175], v[228:231], v[120:123]\n"
    "v_exp_f32_e32 v174, v174\n"
    "v_mfma_f32_16x16x32_bf16 v[120:123], a[168:171], v[224:227], v[120:123]\n"
    "v_exp_f32_e32 v175, v175\n"
    "v_mfma_f32_16x16x32_bf16 v[116:119], a[168:171], v[216:219], v[116:119]\n"
    "ds_read_b128 a[168:171], v241 offset:49152\n"
    "ds_write_addtid_b32 v168 offset:4096\n"
    "v_mfma_f32_16x16x32_bf16 v[116:119], a[172:175], v[220:223], v[116:119]\n"
    "s_waitcnt lgkmcnt(8)\n"
    "ds_read_b128 a[172:175], v240 offset:53248\n"
    "v_cvt_pk_bf16_f32 v88, v88, v89\n"
    "v_mov_b32_e32 v159, 0x3f80\n"
    "v_mfma_f32_16x16x32_bf16 v[128:131], a[176:179], v[216:219], v[128:131]\n"
    "ds_write_addtid_b32 v169 offset:4352\n"
    "v_cvt_pk_bf16_f32 v170, v172, v173\n"
    "v_mfma_f32_16x16x32_bf16 v[128:131], a[180:183], v[220:223], v[128:131]\n"
    "buffer_store_dwordx4 v[56:59], v253, s[8:11], s84 offen offset:0 sc1\n"
    "v_exp_f32_e32 v164, v164\n"
    "v_mfma_f32_16x16x32_bf16 v[132:135], a[180:183], v[228:231], v[132:135]\n"
    "v_exp_f32_e32 v165, v165\n"
    "v_mfma_f32_16x16x32_bf16 v[132:135], a[176:179], v[224:227], v[132:135]\n"
    "ds_write_addtid_b32 v160 offset:0\n"
    "v_cvt_pk_bf16_f32 v171, v174, v175\n"
    "v_mfma_f32_16x16x32_bf16 v[136:139], a[176:179], v[232:235], v[136:139]\n"
    "ds_read_b128 a[176:179], v241 offset:53248\n"
    "v_cvt_pk_bf16_f32 v89, v90, v91\n"
    "v_mfma_f32_16x16x32_bf16 v[136:139], a[180:183], v[236:239], v[136:139]\n"
    "s_waitcnt lgkmcnt(8)\n"
    "ds_read_b128 a[180:183], v242 offset:49152\n"
    "v_cvt_pk_bf16_f32 v90, v100, v101\n"
    "v_mfma_f32_16x16x32_bf16 v[148:151], a[184:187], v[232:235], v[148:151]\n"
    "v_exp_f32_e32 v166, v166\n"
    "v_mfma_f32_16x16x32_bf16 v[148:151], a[188:191], v[236:239], v[148:151]\n"
    "v_exp_f32_e32 v167, v167\n"
    "v_mfma_f32_16x16x32_bf16 v[144:147], a[188:191], v[228:231], v[144:147]\n"
    "ds_write_addtid_b32 v161 offset:256\n"
    "v_cvt_pk_bf16_f32 v162, v164, v165\n"
    "v_mfma_f32_16x16x32_bf16 v[144:147], a[184:187], v[224:227], v[144:147]\n"
    "ds_write_addtid_b32 v170 offset:4608\n"
    "v_cvt_pk_bf16_f32 v163, v166, v167\n"
    "v_cvt_pk_bf16_f32 v91, v102, v103\n"
    "v_mfma_f32_16x16x32_bf16 v[140:143], a[184:187], v[216:219], v[140:143]\n"
    "ds_read_b128 a[184:187], v243 offset:49152\n"
    "buffer_store_dwordx4 v[88:91], v253, s[8:11], s86 offen offset:1024 sc1\n"
    "v_cvt_pk_bf16_f32 v84, v84, v85\n"
    "v_mov_b32_e32 v152, 0\n"
    "v_mfma_f32_16x16x32_bf16 v[140:143], a[188:191], v[220:223], v[140:143]\n"
    "s_waitcnt lgkmcnt(7)\n"
    "ds_read_b128 a[188:191], v242 offset:53248\n"
    "v_mov_b32_e32 v153, 0\n"
    "v_mfma_f32_16x16x32_bf16 v[200:203], a[160:163], a[28:31], 0\n"
    "ds_write_addtid_b32 v171 offset:4864\n"
    "v_mov_b32_e32 v156, 0\n"
    "v_mfma_f32_16x16x32_bf16 v[200:203], a[164:167], a[0:3], v[200:203]\n"
    "ds_write_addtid_b32 v162 offset:512\n"
    "v_mov_b32_e32 v157, 0\n"
    "v_mfma_f32_16x16x32_bf16 v[208:211], a[164:167], a[32:35], 0\n"
    "ds_write_addtid_b32 v163 offset:768\n"
    "v_cmp_eq_u32_e32 vcc, 0, v158\n"
    "v_mfma_f32_16x16x32_bf16 v[208:211], a[160:163], a[60:63], v[208:211]\n"
    "ds_read_b128 a[160:163], v243 offset:55296\n"
    "v_cndmask_b32_e32 v154, 0, v159, vcc\n"
    "v_mfma_f32_16x16x32_bf16 v[208:211], a[168:171], a[36:39], v[208:211]\n"
    "ds_read_b128 a[164:167], v240 offset:51200\n"
    "v_cmp_eq_u32_e32 vcc, 1, v158\n"
    "v_mfma_f32_16x16x32_bf16 v[208:211], a[172:175], a[40:43], v[208:211]\n"
    "s_waitcnt lgkmcnt(2)\n"
    "v_cvt_pk_bf16_f32 v85, v86, v87\n"
    "v_cndmask_b32_e64 v154, v154, 1.0, vcc\n"
    "v_mfma_f32_16x16x32_bf16 v[200:203], a[172:175], a[8:11], v[200:203]\n"
    "s_barrier\n"
    "v_mfma_f32_16x16x32_bf16 v[200:203], a[168:171], a[4:7], v[200:203]\n"
    "ds_read_b128 a[168:171], v241 offset:51200\n"
    "v_cmp_eq_u32_e32 vcc, 2, v158\n"
    "v_mfma_f32_16x16x32_bf16 v[200:203], a[176:179], a[12:15], v[200:203]\n"
    "ds_read_b128 a[172:175], v240 offset:55296\n"
    "v_cndmask_b32_e32 v155, 0, v159, vcc\n"
    "v_mfma_f32_16x16x32_bf16 v[200:203], a[180:183], a[16:19], v[200:203]\n"
    "ds_read2st64_b32 v[164:165], v250 offset0:0 offset1:1\n"
    "v_cmp_eq_u32_e32 vcc, 3, v158\n"
    "s_nop 1\n"
    "v_cndmask_b32_e64 v155, v155, 1.0, vcc\n"
    "v_mfma_f32_16x16x32_bf16 v[208:211], a[180:183], a[48:51], v[208:211]\n"
    "v_cvt_pk_bf16_f32 v86, v96, v97\n"
    "v_and_b32_e32 v251, 0xf0, v253\n"
    "v_mov_b32_e32 v254, 2.0\n"
    "v_mfma_f32_16x16x32_bf16 v[208:211], a[176:179], a[44:47], v[208:211]\n"
    "ds_read_b128 a[176:179], v241 offset:55296\n"
    "v_cmp_le_u32_e32 vcc, 0x100, v253\n"
    "v_mov_b32_e32 v255, s29\n"
    "v_mfma_f32_16x16x32_bf16 v[208:211], a[184:187], a[52:55], v[208:211]\n"
    "ds_read_b128 a[180:183], v242 offset:51200\n"
    "v_cvt_pk_bf16_f32 v87, v98, v99\n"
    "v_mfma_f32_16x16x32_bf16 v[208:211], a[188:191], a[56:59], v[208:211]\n"
    "v_cvt_pk_bf16_f32 v80, v80, v81\n"
    "v_cvt_pk_bf16_f32 v81, v82, v83\n"
    "v_mfma_f32_16x16x32_bf16 v[200:203], a[188:191], a[24:27], v[200:203]\n"
    "ds_read2st64_b32 v[166:167], v250 offset0:2 offset1:3\n"
    "v_cvt_pk_bf16_f32 v82, v92, v93\n"
    "v_mfma_f32_16x16x32_bf16 v[200:203], a[184:187], a[20:23], v[200:203]\n"
    "s_waitcnt lgkmcnt(4)\n"
    "ds_read_b128 a[184:187], v243 offset:51200\n"
    "v_cvt_pk_bf16_f32 v83, v94, v95\n"
    "v_mfma_f32_16x16x32_bf16 v[204:207], a[160:163], a[28:31], 0\n"
    "ds_read_b128 a[188:191], v242 offset:55296\n"
    "v_cvt_pk_bf16_f32 v112, v112, v113\n"
    "v_mfma_f32_16x16x32_bf16 v[204:207], a[164:167], a[0:3], v[204:207]\n"
    "v_exp_f32_e32 v208, v208\n"
    "v_mfma_f32_16x16x32_bf16 v[212:215], a[164:167], a[32:35], 0\n"
    "ds_read2st64_b32 v[172:173], v250 offset0:16 offset1:17\n"
    "v_cvt_pk_bf16_f32 v113, v114, v115\n"
    "v_mfma_f32_16x16x32_bf16 v[212:215], a[160:163], a[60:63], v[212:215]\n"
    "ds_read_b64_tr_b16 a[160:161], v244 offset:32768\n"
    "ds_read_b64_tr_b16 a[162:163], v244 offset:34816\n"
    "v_mfma_f32_16x16x32_bf16 v[212:215], a[168:171], a[36:39], v[212:215]\n"
    "v_exp_f32_e32 v209, v209\n"
    "v_mfma_f32_16x16x32_bf16 v[212:215], a[172:175], a[40:43], v[212:215]\n"
    "ds_read_b64_tr_b16 a[164:165], v244 offset:40960\n"
    "ds_read_b64_tr_b16 a[166:167], v244 offset:43008\n"
    "v_mfma_f32_16x16x32_bf16 v[204:207], a[172:175], a[8:11], v[204:207]\n"
    "v_exp_f32_e32 v210, v210\n"
    "v_mfma_f32_16x16x32_bf16 v[204:207], a[168:171], a[4:7], v[204:207]\n"
    "s_waitcnt lgkmcnt(5)\n"
    "ds_read_b64_tr_b16 a[168:169], v245 offset:32768\n"
    "ds_read_b64_tr_b16 a[170:171], v245 offset:34816\n"
    "v_mfma_f32_16x16x32_bf16 v[204:207], a[176:179], a[12:15], v[204:207]\n"
    "ds_read2st64_b32 v[174:175], v250 offset0:18 offset1:19\n"
    "v_cvt_pk_bf16_f32 v114, v124, v125\n"
    "v_mfma_f32_16x16x32_bf16 v[204:207], a[180:183], a[16:19], v[204:207]\n"
    "ds_read_b64_tr_b16 a[172:173], v245 offset:40960\n"
    "ds_read_b64_tr_b16 a[174:175], v245 offset:43008\n"
    "v_mfma_f32_16x16x32_bf16 v[212:215], a[180:183], a[48:51], v[212:215]\n"
    "v_exp_f32_e32 v211, v211\n"
    "v_mfma_f32_16x16x32_bf16 v[212:215], a[176:179], a[44:47], v[212:215]\n"
    "ds_read_b64_tr_b16 a[176:177], v246 offset:32768\n"
    "ds_read_b64_tr_b16 a[178:179], v246 offset:34816\n"
    "v_mfma_f32_16x16x32_bf16 v[212:215], a[184:187], a[52:55], v[212:215]\n"
    "v_cvt_pk_bf16_f32 v208, v208, v209\n"
    "v_cvt_pk_bf16_f32 v115, v126, v127\n"
    "v_mfma_f32_16x16x32_bf16 v[212:215], a[188:191], a[56:59], v[212:215]\n"
    "ds_read_b64_tr_b16 a[180:181], v246 offset:40960\n"
    "ds_read_b64_tr_b16 a[182:183], v246 offset:43008\n"
    "v_mfma_f32_16x16x32_bf16 v[204:207], a[188:191], a[24:27], v[204:207]\n"
    "v_exp_f32_e32 v200, v200\n"
    "v_mfma_f32_16x16x32_bf16 v[204:207], a[184:187], a[20:23], v[204:207]\n"
    "s_waitcnt lgkmcnt(4)\n"
    "ds_read_b64_tr_b16 a[184:185], v247 offset:32768\n"
    "ds_read_b64_tr_b16 a[186:187], v247 offset:34816\n"
    "v_mfma_f32_16x16x32_bf16 v[0:3], a[160:163], v[160:163], v[0:3]\n"
    "v_exp_f32_e32 v201, v201\n"
    "v_mfma_f32_16x16x32_bf16 v[0:3], a[164:167], v[164:167], v[0:3]\n"
    "ds_read_b64_tr_b16 a[188:189], v247 offset:40960\n"
    "ds_read_b64_tr_b16 a[190:191], v247 offset:43008\n"
    "v_mfma_f32_16x16x32_bf16 a[248:251], a[164:167], v[172:175], a[248:251]\n"
    "v_cvt_pk_bf16_f32 v209, v210, v211\n"
    "v_cvt_pk_bf16_f32 v108, v108, v109\n"
    "v_mfma_f32_16x16x32_bf16 a[248:251], a[160:163], v[168:171], a[248:251]\n"
    "ds_read_b64_tr_b16 a[160:161], v244 offset:36864\n"
    "ds_read_b64_tr_b16 a[162:163], v244 offset:38912\n"
    "v_mfma_f32_16x16x32_bf16 a[252:255], a[168:171], v[168:171], a[252:255]\n"
    "v_exp_f32_e32 v202, v202\n"
    "v_mfma_f32_16x16x32_bf16 a[252:255], a[172:175], v[172:175], a[252:255]\n"
    "s_barrier\n"
    "v_mfma_f32_16x16x32_bf16 v[4:7], a[172:175], v[164:167], v[4:7]\n"
    "ds_read_b64_tr_b16 a[164:165], v244 offset:45056\n"
    "ds_read_b64_tr_b16 a[166:167], v244 offset:47104\n"
    "v_mfma_f32_16x16x32_bf16 v[4:7], a[168:171], v[160:163], v[4:7]\n"
    "s_waitcnt lgkmcnt(4)\n"
    "v_exp_f32_e32 v203, v203\n"
    "v_mfma_f32_16x16x32_bf16 v[8:11], a[176:179], v[160:163], v[8:11]\n"
    "ds_read_b64_tr_b16 a[168:169], v245 offset:36864\n"
    "ds_read_b64_tr_b16 a[170:171], v245 offset:38912\n"
    "v_mfma_f32_16x16x32_bf16 v[8:11], a[180:183], v[164:167], v[8:11]\n"
    "buffer_store_dwordx4 v[84:87], v253, s[8:11], s85 offen offset:1024 sc1\n"
    "v_cvt_pk_bf16_f32 v200, v200, v201\n"
    "v_cvt_pk_bf16_f32 v201, v202, v203\n"
    "v_mfma_f32_16x16x32_bf16 v[32:35], a[180:183], v[172:175], v[32:35]\n"
    "ds_read_b64_tr_b16 a[172:173], v245 offset:45056\n"
    "ds_read_b64_tr_b16 a[174:175], v245 offset:47104\n"
    "v_mfma_f32_16x16x32_bf16 v[32:35], a[176:179], v[168:171], v[32:35]\n"
    "v_exp_f32_e32 v212, v212\n"
    "v_mfma_f32_16x16x32_bf16 v[36:39], a[184:187], v[168:171], v[36:39]\n"
    "ds_read_b64_tr_b16 a[176:177], v246 offset:36864\n"
    "ds_read_b64_tr_b16 a[178:179], v246 offset:38912\n"
    "v_mfma_f32_16x16x32_bf16 v[36:39], a[188:191], v[172:175], v[36:39]\n"
    "buffer_store_dwordx4 v[80:83], v253, s[8:11], s84 offen offset:1024 sc1\n"
    "v_exp_f32_e32 v213, v213\n"
    "v_mfma_f32_16x16x32_bf16 v[12:15], a[188:191], v[164:167], v[12:15]\n"
    "ds_read_b64_tr_b16 a[180:181], v246 offset:45056\n"
    "ds_read_b64_tr_b16 a[182:183], v246 offset:47104\n"
    "v_mfma_f32_16x16x32_bf16 v[12:15], a[184:187], v[160:163], v[12:15]\n"
    "s_waitcnt lgkmcnt(4)\n"
    "v_exp_f32_e32 v214, v214\n"
    "v_mfma_f32_16x16x32_bf16 v[16:19], a[160:163], v[160:163], v[16:19]\n"
    "ds_read_b64_tr_b16 a[184:185], v247 offset:36864\n"
    "ds_read_b64_tr_b16 a[186:187], v247 offset:38912\n"
    "v_mfma_f32_16x16x32_bf16 v[16:19], a[164:167], v[164:167], v[16:19]\n"
    "v_exp_f32_e32 v215, v215\n"
    "v_mfma_f32_16x16x32_bf16 v[40:43], a[164:167], v[172:175], v[40:43]\n"
    "ds_read_b64_tr_b16 a[188:189], v247 offset:45056\n"
    "ds_read_b64_tr_b16 a[190:191], v247 offset:47104\n"
    "v_mfma_f32_16x16x32_bf16 v[40:43], a[160:163], v[168:171], v[40:43]\n"
    "ds_write_addtid_b32 v208 offset:4096\n"
    "buffer_store_dwordx4 v[112:115], v253, s[8:11], s86 offen offset:2048 sc1\n"
    "v_cvt_pk_bf16_f32 v210, v212, v213\n"
    "v_cvt_pk_bf16_f32 v109, v110, v111\n"
    "v_mfma_f32_16x16x32_bf16 v[44:47], a[168:171], v[168:171], v[44:47]\n"
    "ds_read_b128 a[160:163], v243 offset:4096\n"
    "ds_write_addtid_b32 v209 offset:4352\n"
    "v_mfma_f32_16x16x32_bf16 v[44:47], a[172:175], v[172:175], v[44:47]\n"
    "ds_read_b128 a[164:167], v240 offset:0\n"
    "ds_write_addtid_b32 v200 offset:0\n"
    "v_mfma_f32_16x16x32_bf16 v[20:23], a[172:175], v[164:167], v[20:23]\n"
    "v_exp_f32_e32 v204, v204\n"
    "v_mfma_f32_16x16x32_bf16 v[20:23], a[168:171], v[160:163], v[20:23]\n"
    "s_waitcnt lgkmcnt(5)\n"
    "ds_read_b128 a[168:171], v241 offset:0\n"
    "v_cvt_pk_bf16_f32 v110, v120, v121\n"
    "v_mfma_f32_16x16x32_bf16 v[24:27], a[176:179], v[160:163], v[24:27]\n"
    "ds_read_b128 a[172:175], v240 offset:4096\n"
    "v_cvt_pk_bf16_f32 v111, v122, v123\n"
    "v_mfma_f32_16x16x32_bf16 v[24:27], a[180:183], v[164:167], v[24:27]\n"
    "buffer_store_dwordx4 v[108:111], v253, s[8:11], s85 offen offset:2048 sc1\n"
    "v_exp_f32_e32 v205, v205\n"
    "v_mfma_f32_16x16x32_bf16 v[48:51], a[180:183], v[172:175], v[48:51]\n"
    "ds_write_addtid_b32 v201 offset:256\n"
    "v_cvt_pk_bf16_f32 v211, v214, v215\n"
    "v_mfma_f32_16x16x32_bf16 v[48:51], a[176:179], v[168:171], v[48:51]\n"
    "ds_read_b128 a[176:179], v241 offset:4096\n"
    "v_cndmask_b32_e32 v254, v254, v255, vcc\n"
    "v_mfma_f32_16x16x32_bf16 v[52:55], a[184:187], v[168:171], v[52:55]\n"
    "ds_read_b128 a[180:183], v242 offset:0\n"
    "v_cmp_le_u32_e32 vcc, 0x200, v253\n"
    "v_mov_b32_e32 v255, s30\n"
    "v_mfma_f32_16x16x32_bf16 v[52:55], a[188:191], v[172:175], v[52:55]\n"
    "v_exp_f32_e32 v206, v206\n"
    "v_mfma_f32_16x16x32_bf16 v[28:31], a[188:191], v[164:167], v[28:31]\n"
    "v_exp_f32_e32 v207, v207\n"
    "v_mfma_f32_16x16x32_bf16 v[28:31], a[184:187], v[160:163], v[28:31]\n"
    "s_waitcnt lgkmcnt(3)\n"
    "ds_read_b128 a[184:187], v243 offset:0\n"
    "v_cvt_pk_bf16_f32 v202, v204, v205\n"
    "v_cndmask_b32_e32 v254, v254, v255, vcc\n"
    "v_mfma_f32_16x16x32_bf16 v[160:163], a[160:163], a[28:31], 0\n"
    "ds_read_b128 a[188:191], v242 offset:4096\n"
    "v_cmp_le_u32_e32 vcc, 0x300, v253\n"
    "v_mov_b32_e32 v255, s31\n"
    "v_mfma_f32_16x16x32_bf16 v[160:163], a[164:167], a[0:3], v[160:163]\n"
    "ds_write_addtid_b32 v210 offset:4608\n"
    "v_cvt_pk_bf16_f32 v203, v206, v207\n"
    "v_cndmask_b32_e32 v254, v254, v255, vcc\n"
    "v_mfma_f32_16x16x32_bf16 v[168:171], a[164:167], a[32:35], 0\n"
    "s_cmp_eq_u32 s51, 0\n"
    "s_cselect_b32 s60, 2.0, 24576\n"
    "ds_write_addtid_b32 v211 offset:4864\n"
    "v_add_u32_e32 v251, v251, v254\n"
    "v_mfma_f32_16x16x32_bf16 v[168:171], a[160:163], a[60:63], v[168:171]\n"
    "s_lshl_b32 s45, s51, 16\n"
    "s_add_u32 s45, s45, %3\n"
    "s_add_u32 s45, s45, 73728\n"
    "ds_read_b128 a[160:163], v243 offset:6144\n"
    "v_mfma_f32_16x16x32_bf16 v[168:171], a[168:171], a[36:39], v[168:171]\n"
    "s_lshl_b32 s46, s89, 12\n"
    "s_add_u32 s46, s46, s45\n"
    "ds_read_b128 a[164:167], v240 offset:2048\n"
    "v_add_u32_e32 v159, s46, v253\n"
    "v_mfma_f32_16x16x32_bf16 v[168:171], a[172:175], a[40:43], v[168:171]\n"
    "ds_write_addtid_b32 v202 offset:512\n"
    "v_cvt_pk_bf16_f32 v104, v104, v105\n"
    "v_mfma_f32_16x16x32_bf16 v[160:163], a[172:175], a[8:11], v[160:163]\n"
    "ds_write_addtid_b32 v203 offset:768\n"
    "v_cvt_pk_bf16_f32 v105, v106, v107\n"
    "v_mfma_f32_16x16x32_bf16 v[160:163], a[168:171], a[4:7], v[160:163]\n"
    "s_waitcnt lgkmcnt(6)\n"
    "ds_read_b128 a[168:171], v241 offset:2048\n"
    "v_cvt_pk_bf16_f32 v106, v116, v117\n"
    "v_mfma_f32_16x16x32_bf16 v[160:163], a[176:179], a[12:15], v[160:163]\n"
    "ds_read_b128 a[172:175], v240 offset:6144\n"
    "v_cvt_pk_bf16_f32 v107, v118, v119\n"
    "v_mfma_f32_16x16x32_bf16 v[160:163], a[180:183], a[16:19], v[160:163]\n"
    "s_waitcnt lgkmcnt(2)\n"
    "v_cvt_pk_bf16_f32 v136, v136, v137\n"
    "v_cvt_pk_bf16_f32 v137, v138, v139\n"
    "v_mfma_f32_16x16x32_bf16 v[168:171], a[180:183], a[48:51], v[168:171]\n"
    "s_barrier\n"
    "v_mfma_f32_16x16x32_bf16 v[168:171], a[176:179], a[44:47], v[168:171]\n"
    "s_add_u32 m0, s43, 32768\n"
    "ds_read_b128 a[176:179], v241 offset:6144\n"
    "v_cvt_pk_bf16_f32 v138, v148, v149\n"
    "v_mfma_f32_16x16x32_bf16 v[168:171], a[184:187], a[52:55], v[168:171]\n"
    "ds_read_b128 a[180:183], v242 offset:2048\n"
    "buffer_load_dwordx4 v248, s[8:11], s40 offen offset:0 sc1 lds\n"
    "v_mfma_f32_16x16x32_bf16 v[168:171], a[188:191], a[56:59], v[168:171]\n"
    "ds_read2st64_b32 v[204:205], v250 offset0:0 offset1:1\n"
    "v_cvt_pk_bf16_f32 v139, v150, v151\n"
    "v_mfma_f32_16x16x32_bf16 v[160:163], a[188:191], a[24:27], v[160:163]\n"
    "v_cvt_pk_bf16_f32 v132, v132, v133\n"
    "v_cvt_pk_bf16_f32 v133, v134, v135\n"
    "v_mfma_f32_16x16x32_bf16 v[160:163], a[184:187], a[20:23], v[160:163]\n"
    "s_waitcnt lgkmcnt(3)\n"
    "ds_read_b128 a[184:187], v243 offset:2048\n"
    "buffer_load_dwordx4 v248, s[8:11], s40 offen offset:1024 sc1 lds\n"
    "v_mfma_f32_16x16x32_bf16 v[164:167], a[160:163], a[28:31], 0\n"
    "ds_read_b128 a[188:191], v242 offset:6144\n"
    "v_exp_f32_e32 v168, v168\n"
    "v_mfma_f32_16x16x32_bf16 v[164:167], a[164:167], a[0:3], v[164:167]\n"
    "ds_read2st64_b32 v[206:207], v250 offset0:2 offset1:3\n"
    "v_cvt_pk_bf16_f32 v134, v144, v145\n"
    "v_mfma_f32_16x16x32_bf16 v[172:175], a[164:167], a[32:35], 0\n"
    "buffer_load_dwordx4 v248, s[8:11], s40 offen offset:2048 sc1 lds\n"
    "v_exp_f32_e32 v169, v169\n"
    "v_mfma_f32_16x16x32_bf16 v[172:175], a[160:163], a[60:63], v[172:175]\n"
    "ds_read_b64_tr_b16 a[160:161], v244 offset:49152\n"
    "ds_read_b64_tr_b16 a[162:163], v244 offset:51200\n"
    "v_mfma_f32_16x16x32_bf16 v[172:175], a[168:171], a[36:39], v[172:175]\n"
    "ds_read2st64_b32 v[212:213], v250 offset0:16 offset1:17\n"
    "v_cvt_pk_bf16_f32 v135, v146, v147\n"
    "v_mfma_f32_16x16x32_bf16 v[172:175], a[172:175], a[40:43], v[172:175]\n"
    "ds_read_b64_tr_b16 a[164:165], v244 offset:57344\n"
    "ds_read_b64_tr_b16 a[166:167], v244 offset:59392\n"
    "buffer_load_dwordx4 v248, s[8:11], s40 offen offset:3072 sc1 lds\n"
    "v_mfma_f32_16x16x32_bf16 v[164:167], a[172:175], a[8:11], v[164:167]\n"
    "s_add_u32 s40, s40, 32768\n"
    "ds_read2st64_b32 v[214:215], v250 offset0:18 offset1:19\n"
    "v_cvt_pk_bf16_f32 v128, v128, v129\n"
    "v_mfma_f32_16x16x32_bf16 v[164:167], a[168:171], a[4:7], v[164:167]\n"
    "s_waitcnt lgkmcnt(7)\n"
    "s_add_u32 m0, s43, 36864\n"
    "ds_read_b64_tr_b16 a[168:169], v245 offset:49152\n"
    "ds_read_b64_tr_b16 a[170:171], v245 offset:51200\n"
    "v_mfma_f32_16x16x32_bf16 v[164:167], a[176:179], a[12:15], v[164:167]\n"
    "buffer_load_dwordx4 v248, s[8:11], s41 offen offset:0 sc1 lds\n"
    "v_cvt_pk_bf16_f32 v129, v130, v131\n"
    "v_mfma_f32_16x16x32_bf16 v[164:167], a[180:183], a[16:19], v[164:167]\n"
    "ds_read_b64_tr_b16 a[172:173], v245 offset:57344\n"
    "ds_read_b64_tr_b16 a[174:175], v245 offset:59392\n"
    "v_mfma_f32_16x16x32_bf16 v[172:175], a[180:183], a[48:51], v[172:175]\n"
    "v_exp_f32_e32 v170, v170\n"
    "v_mfma_f32_16x16x32_bf16 v[172:175], a[176:179], a[44:47], v[172:175]\n"
    "ds_read_b64_tr_b16 a[176:177], v246 offset:49152\n"
    "ds_read_b64_tr_b16 a[178:179], v246 offset:51200\n"
    "buffer_load_dwordx4 v248, s[8:11], s41 offen offset:1024 sc1 lds\n"
    "v_mfma_f32_16x16x32_bf16 v[172:175], a[184:187], a[52:55], v[172:175]\n"
    "v_exp_f32_e32 v171, v171\n"
    "v_mfma_f32_16x16x32_bf16 v[172:175], a[188:191], a[56:59], v[172:175]\n"
    "ds_read_b64_tr_b16 a[180:181], v246 offset:57344\n"
    "ds_read_b64_tr_b16 a[182:183], v246 offset:59392\n"
    "v_mfma_f32_16x16x32_bf16 v[164:167], a[188:191], a[24:27], v[164:167]\n"
    "buffer_load_dwordx4 v248, s[8:11], s41 offen offset:2048 sc1 lds\n"
    "v_cvt_pk_bf16_f32 v168, v168, v169\n"
    "v_mfma_f32_16x16x32_bf16 v[164:167], a[184:187], a[20:23], v[164:167]\n"
    "s_waitcnt lgkmcnt(4)\n"
    "ds_read_b64_tr_b16 a[184:185], v247 offset:49152\n"
    "ds_read_b64_tr_b16 a[186:187], v247 offset:51200\n"
    "v_mfma_f32_16x16x32_bf16 v[0:3], a[160:163], v[200:203], v[0:3]\n"
    "v_exp_f32_e32 v160, v160\n"
    "v_mfma_f32_16x16x32_bf16 v[0:3], a[164:167], v[204:207], v[0:3]\n"
    "ds_read_b64_tr_b16 a[188:189], v247 offset:57344\n"
    "ds_read_b64_tr_b16 a[190:191], v247 offset:59392\n"
    "buffer_load_dwordx4 v248, s[8:11], s41 offen offset:3072 sc1 lds\n"
    "v_mfma_f32_16x16x32_bf16 a[248:251], a[164:167], v[212:215], a[248:251]\n"
    "s_add_u32 s41, s41, 32768\n"
    "v_exp_f32_e32 v161, v161\n"
    "v_mfma_f32_16x16x32_bf16 a[248:251], a[160:163], v[208:211], a[248:251]\n"
    "s_mov_b32 m0, s49\n"
    "ds_read_b64_tr_b16 a[160:161], v244 offset:53248\n"
    "ds_read_b64_tr_b16 a[162:163], v244 offset:55296\n"
    "v_mfma_f32_16x16x32_bf16 a[252:255], a[168:171], v[208:211], a[252:255]\n"
    "v_cvt_pk_bf16_f32 v169, v170, v171\n"
    "v_cvt_pk_bf16_f32 v130, v140, v141\n"
    "v_mfma_f32_16x16x32_bf16 a[252:255], a[172:175], v[212:215], a[252:255]\n"
    "s_barrier\n"
    "v_mfma_f32_16x16x32_bf16 v[4:7], a[172:175], v[204:207], v[4:7]\n"
    "ds_read_b64_tr_b16 a[164:165], v244 offset:61440\n"
    "ds_read_b64_tr_b16 a[166:167], v244 offset:63488\n"
    "v_mfma_f32_16x16x32_bf16 v[4:7], a[168:171], v[200:203], v[4:7]\n"
    "s_waitcnt lgkmcnt(4)\n"
    "v_exp_f32_e32 v162, v162\n"
    "v_mfma_f32_16x16x32_bf16 v[8:11], a[176:179], v[200:203], v[8:11]\n"
    "s_add_u32 s45, s45, 16384\n"
    "ds_read_b64_tr_b16 a[168:169], v245 offset:53248\n"
    "ds_read_b64_tr_b16 a[170:171], v245 offset:55296\n"
    "v_lshrrev_b32_e32 v255, 2, v253\n"
    "v_mfma_f32_16x16x32_bf16 v[8:11], a[180:183], v[204:207], v[8:11]\n"
    "buffer_store_dwordx4 v[104:107], v253, s[8:11], s84 offen offset:2048 sc1\n"
    "v_exp_f32_e32 v163, v163\n"
    "v_mfma_f32_16x16x32_bf16 v[32:35], a[180:183], v[212:215], v[32:35]\n"
    "ds_read_b64_tr_b16 a[172:173], v245 offset:61440\n"
    "ds_read_b64_tr_b16 a[174:175], v245 offset:63488\n"
    "v_mfma_f32_16x16x32_bf16 v[32:35], a[176:179], v[208:211], v[32:35]\n"
    "v_cvt_pk_bf16_f32 v160, v160, v161\n"
    "v_cvt_pk_bf16_f32 v161, v162, v163\n"
    "v_mfma_f32_16x16x32_bf16 v[36:39], a[184:187], v[208:211], v[36:39]\n"
    "ds_read_b64_tr_b16 a[176:177], v246 offset:53248\n"
    "ds_read_b64_tr_b16 a[178:179], v246 offset:55296\n"
    "v_mfma_f32_16x16x32_bf16 v[36:39], a[188:191], v[212:215], v[36:39]\n"
    "buffer_store_dwordx4 v[136:139], v253, s[8:11], s86 offen offset:3072 sc1\n"
    "v_exp_f32_e32 v172, v172\n"
    "v_add_u32_e32 v255, s45, v255\n"
    "v_mfma_f32_16x16x32_bf16 v[12:15], a[188:191], v[204:207], v[12:15]\n"
    "ds_read_b64_tr_b16 a[180:181], v246 offset:61440\n"
    "ds_read_b64_tr_b16 a[182:183], v246 offset:63488\n"
    "v_mfma_f32_16x16x32_bf16 v[12:15], a[184:187], v[200:203], v[12:15]\n"
    "s_waitcnt lgkmcnt(4)\n"
    "v_exp_f32_e32 v173, v173\n"
    "v_mfma_f32_16x16x32_bf16 v[16:19], a[160:163], v[200:203], v[16:19]\n"
    "ds_read_b64_tr_b16 a[184:185], v247 offset:53248\n"
    "ds_read_b64_tr_b16 a[186:187], v247 offset:55296\n"
    "v_mfma_f32_16x16x32_bf16 v[16:19], a[164:167], v[204:207], v[16:19]\n"
    "v_exp_f32_e32 v174, v174\n"
    "v_mfma_f32_16x16x32_bf16 v[40:43], a[164:167], v[212:215], v[40:43]\n"
    "s_lshl_b32 s45, s89, 8\n"
    "ds_read_b64_tr_b16 a[188:189], v247 offset:61440\n"
    "ds_read_b64_tr_b16 a[190:191], v247 offset:63488\n"
    "v_add_u32_e32 v254, s45, v255\n"
    "v_mfma_f32_16x16x32_bf16 v[40:43], a[160:163], v[208:211], v[40:43]\n"
    "buffer_store_dwordx4 v[132:135], v253, s[8:11], s85 offen offset:3072 sc1\n"
    "v_exp_f32_e32 v175, v175\n"
    "v_mfma_f32_16x16x32_bf16 v[44:47], a[168:171], v[208:211], v[44:47]\n"
    "ds_read_b128 a[160:163], v243 offset:20480\n"
    "v_cvt_pk_bf16_f32 v170, v172, v173\n"
    "v_mfma_f32_16x16x32_bf16 v[44:47], a[172:175], v[212:215], v[44:47]\n"
    "ds_read_b128 a[164:167], v240 offset:16384\n"
    "ds_write_addtid_b32 v168 offset:4096\n"
    "v_mfma_f32_16x16x32_bf16 v[20:23], a[172:175], v[204:207], v[20:23]\n"
    "s_lshl_b32 s45, s50, 8\n"
    "v_exp_f32_e32 v164, v164\n"
    "v_add_u32_e32 v255, s45, v255\n"
    "v_mfma_f32_16x16x32_bf16 v[20:23], a[168:171], v[200:203], v[20:23]\n"
    "s_waitcnt lgkmcnt(3)\n"
    "ds_read_b128 a[168:171], v241 offset:16384\n"
    "ds_write_addtid_b32 v169 offset:4352\n"
    "v_mfma_f32_16x16x32_bf16 v[24:27], a[176:179], v[200:203], v[24:27]\n"
    "ds_read_b128 a[172:175], v240 offset:20480\n"
    "v_cvt_pk_bf16_f32 v131, v142, v143\n"
    "v_mfma_f32_16x16x32_bf16 v[24:27], a[180:183], v[204:207], v[24:27]\n"
    "buffer_store_dwordx4 v[128:131], v253, s[8:11], s84 offen offset:3072 sc1\n"
    "v_exp_f32_e32 v165, v165\n"
    "v_mfma_f32_16x16x32_bf16 v[48:51], a[180:183], v[212:215], v[48:51]\n"
    "ds_write_addtid_b32 v160 offset:0\n"
    "v_cvt_pk_bf16_f32 v171, v174, v175\n"
    "v_permlane16_swap_b32_e32 v147, v151\n"
    "v_mfma_f32_16x16x32_bf16 v[48:51], a[176:179], v[208:211], v[48:51]\n"
    "ds_read_b128 a[176:179], v241 offset:20480\n"
    "v_mov_b32_e32 v148, s39\n"
    "v_mfma_f32_16x16x32_bf16 v[52:55], a[184:187], v[208:211], v[52:55]\n"
    "ds_read_b128 a[180:183], v242 offset:16384\n"
    "ds_write_addtid_b32 v161 offset:256\n"
    "v_mfma_f32_16x16x32_bf16 v[52:55], a[188:191], v[212:215], v[52:55]\n"
    "v_exp_f32_e32 v166, v166\n"
    "v_mov_b32_e32 v149, 0\n"
    "v_mfma_f32_16x16x32_bf16 v[28:31], a[188:191], v[204:207], v[28:31]\n"
    "v_exp_f32_e32 v167, v167\n"
    "v_permlane32_swap_b32_e32 v143, v151\n"
    "v_mfma_f32_16x16x32_bf16 v[28:31], a[184:187], v[200:203], v[28:31]\n"
    "s_waitcnt lgkmcnt(4)\n"
    "ds_read_b128 a[184:187], v243 offset:16384\n"
    "v_cvt_pk_bf16_f32 v162, v164, v165\n"
    "v_mfma_f32_16x16x32_bf16 v[200:203], a[160:163], a[28:31], 0\n"
    "ds_read_b128 a[188:191], v242 offset:20480\n"
    "v_mfma_f32_16x16x32_bf16 v[200:203], a[164:167], a[0:3], v[200:203]\n"
    "ds_write_addtid_b32 v170 offset:4608\n"
    "buffer_store_dword v151, v251, s[8:11], s60 offen offset:0 sc1\n"
    "v_cvt_pk_bf16_f32 v163, v166, v167\n"
    "v_mfma_f32_16x16x32_bf16 v[208:211], a[164:167], a[32:35], 0\n"
    "ds_write_addtid_b32 v171 offset:4864\n"
    "v_mfma_f32_16x16x32_bf16 v[208:211], a[160:163], a[60:63], v[208:211]\n"
    "ds_read_b128 a[160:163], v243 offset:22528\n"
    "v_lshrrev_b32_e32 v147, 4, v253\n"
    "v_mfma_f32_16x16x32_bf16 v[208:211], a[168:171], a[36:39], v[208:211]\n"
    "ds_read_b128 a[164:167], v240 offset:18432\n"
    "v_mfma_f32_16x16x32_bf16 v[208:211], a[172:175], a[40:43], v[208:211]\n"
    "ds_write_addtid_b32 v162 offset:512\n"
    "v_mfma_f32_16x16x32_bf16 v[200:203], a[172:175], a[8:11], v[200:203]\n"
    "ds_write_addtid_b32 v163 offset:768\n"
    "v_bfe_u32 v145, v147, 2, 2\n"
    "v_mfma_f32_16x16x32_bf16 v[200:203], a[168:171], a[4:7], v[200:203]\n"
    "s_waitcnt lgkmcnt(6)\n"
    "ds_read_b128 a[168:171], v241 offset:18432\n"
    "v_mfma_f32_16x16x32_bf16 v[200:203], a[176:179], a[12:15], v[200:203]\n"
    "ds_read_b128 a[172:175], v240 offset:22528\n"
    "v_mfma_f32_16x16x32_bf16 v[200:203], a[180:183], a[16:19], v[200:203]\n"
    "s_waitcnt lgkmcnt(2)\n"
    "v_min_u32_e32 v145, 2, v145\n"
    "v_mfma_f32_16x16x32_bf16 v[208:211], a[180:183], a[48:51], v[208:211]\n"
    "s_barrier\n"
    "v_mfma_f32_16x16x32_bf16 v[208:211], a[176:179], a[44:47], v[208:211]\n"
    "s_add_u32 s46, s32, 1\n"
    "ds_read_b128 a[176:179], v241 offset:22528\n"
    "v_mfma_f32_16x16x32_bf16 v[208:211], a[184:187], a[52:55], v[208:211]\n"
    "ds_read_b128 a[180:183], v242 offset:18432\n"
    "v_mfma_f32_16x16x32_bf16 v[208:211], a[188:191], a[56:59], v[208:211]\n"
    "ds_read2st64_b32 v[164:165], v250 offset0:0 offset1:1\n"
    "v_mfma_f32_16x16x32_bf16 v[200:203], a[188:191], a[24:27], v[200:203]\n"
    "v_add_u32_e32 v145, s46, v145\n"
    "v_mfma_f32_16x16x32_bf16 v[200:203], a[184:187], a[20:23], v[200:203]\n"
    "s_waitcnt lgkmcnt(3)\n"
    "ds_read_b128 a[184:187], v243 offset:18432\n"
    "v_and_b32_e32 v145, 3, v145\n"
    "v_mfma_f32_16x16x32_bf16 v[204:207], a[160:163], a[28:31], 0\n"
    "ds_read_b128 a[188:191], v242 offset:22528\n"
    "v_lshlrev_b32_e32 v145, 6, v145\n"
    "v_mfma_f32_16x16x32_bf16 v[204:207], a[164:167], a[0:3], v[204:207]\n"
    "ds_read2st64_b32 v[166:167], v250 offset0:2 offset1:3\n"
    "v_and_or_b32 v145, v147, 3, v145\n"
    "v_mfma_f32_16x16x32_bf16 v[212:215], a[164:167], a[32:35], 0\n"
    "v_exp_f32_e32 v208, v208\n"
    "v_mfma_f32_16x16x32_bf16 v[212:215], a[160:163], a[60:63], v[212:215]\n"
    "ds_read_b64_tr_b16 a[160:161], v244 offset:0\n"
    "ds_read_b64_tr_b16 a[162:163], v244 offset:2048\n"
    "v_mfma_f32_16x16x32_bf16 v[212:215], a[168:171], a[36:39], v[212:215]\n"
    "ds_read2st64_b32 v[172:173], v250 offset0:16 offset1:17\n"
    "v_lshlrev_b32_e32 v145, 2, v145\n"
    "v_mfma_f32_16x16x32_bf16 v[212:215], a[172:175], a[40:43], v[212:215]\n"
    "ds_read_b64_tr_b16 a[164:165], v244 offset:8192\n"
    "ds_read_b64_tr_b16 a[166:167], v244 offset:10240\n"
    "v_mfma_f32_16x16x32_bf16 v[204:207], a[172:175], a[8:11], v[204:207]\n"
    "s_lshl_b32 s47, s33, 10\n"
    "v_exp_f32_e32 v209, v209\n"
    "v_mfma_f32_16x16x32_bf16 v[204:207], a[168:171], a[4:7], v[204:207]\n"
    "s_waitcnt lgkmcnt(6)\n"
    "ds_read_b64_tr_b16 a[168:169], v245 offset:0\n"
    "ds_read_b64_tr_b16 a[170:171], v245 offset:2048\n"
    "v_mfma_f32_16x16x32_bf16 v[204:207], a[176:179], a[12:15], v[204:207]\n"
    "s_add_u32 s47, s47, 5636096\n"
    "ds_read2st64_b32 v[174:175], v250 offset0:18 offset1:19\n"
    "v_cmp_gt_u32_e32 vcc, 0x300, v253\n"
    "v_mfma_f32_16x16x32_bf16 v[204:207], a[180:183], a[16:19], v[204:207]\n"
    "s_add_u32 s48, s56, 0\n"
    "ds_read_b64_tr_b16 a[172:173], v245 offset:8192\n"
    "ds_read_b64_tr_b16 a[174:175], v245 offset:10240\n"
    "v_mfma_f32_16x16x32_bf16 v[212:215], a[180:183], a[48:51], v[212:215]\n"
    "v_exp_f32_e32 v210, v210\n"
    "v_mfma_f32_16x16x32_bf16 v[212:215], a[176:179], a[44:47], v[212:215]\n"
    "ds_read_b64_tr_b16 a[176:177], v246 offset:0\n"
    "ds_read_b64_tr_b16 a[178:179], v246 offset:2048\n"
    "v_mfma_f32_16x16x32_bf16 v[212:215], a[184:187], a[52:55], v[212:215]\n"
    "buffer_load_dwordx4 v[56:59], v253, s[8:11], s48 offen offset:0 sc1\n"
    "v_exp_f32_e32 v211, v211\n"
    "v_mfma_f32_16x16x32_bf16 v[212:215], a[188:191], a[56:59], v[212:215]\n"
    "ds_read_b64_tr_b16 a[180:181], v246 offset:8192\n"
    "ds_read_b64_tr_b16 a[182:183], v246 offset:10240\n"
    "v_mfma_f32_16x16x32_bf16 v[204:207], a[188:191], a[24:27], v[204:207]\n"
    "s_add_u32 s60, s60, s28\n"
    "v_cvt_pk_bf16_f32 v208, v208, v209\n"
    "v_and_b32_e32 v251, 0xf0, v253\n"
    "v_mfma_f32_16x16x32_bf16 v[204:207], a[184:187], a[20:23], v[204:207]\n"
    "s_waitcnt lgkmcnt(4)\n"
    "ds_read_b64_tr_b16 a[184:185], v247 offset:0\n"
    "ds_read_b64_tr_b16 a[186:187], v247 offset:2048\n"
    "v_mfma_f32_16x16x32_bf16 v[0:3], a[160:163], v[160:163], v[0:3]\n"
    "buffer_load_dwordx4 v[60:63], v253, s[8:11], s48 offen offset:1024 sc1\n"
    "v_exp_f32_e32 v200, v200\n"
    "v_mfma_f32_16x16x32_bf16 v[0:3], a[164:167], v[164:167], v[0:3]\n"
    "ds_read_b64_tr_b16 a[188:189], v247 offset:8192\n"
    "ds_read_b64_tr_b16 a[190:191], v247 offset:10240\n"
    "v_mfma_f32_16x16x32_bf16 a[248:251], a[164:167], v[172:175], a[248:251]\n"
    "v_exp_f32_e32 v201, v201\n"
    "v_mfma_f32_16x16x32_bf16 a[248:251], a[160:163], v[168:171], a[248:251]\n"
    "ds_read_b64_tr_b16 a[160:161], v244 offset:4096\n"
    "ds_read_b64_tr_b16 a[162:163], v244 offset:6144\n"
    "v_mfma_f32_16x16x32_bf16 a[252:255], a[168:171], v[168:171], a[252:255]\n"
    "buffer_load_dwordx4 v[64:67], v253, s[8:11], s48 offen offset:2048 sc1\n"
    "v_cvt_pk_bf16_f32 v209, v210, v211\n"
    "v_cndmask_b32_e64 v251, v251, 2.0, vcc\n"
    "v_mfma_f32_16x16x32_bf16 a[252:255], a[172:175], v[172:175], a[252:255]\n"
    "s_waitcnt vmcnt(8)\n"
    "s_barrier\n"
    "v_mfma_f32_16x16x32_bf16 v[4:7], a[172:175], v[164:167], v[4:7]\n"
    "ds_read_b64_tr_b16 a[164:165], v244 offset:12288\n"
    "ds_read_b64_tr_b16 a[166:167], v244 offset:14336\n"
    "v_mfma_f32_16x16x32_bf16 v[4:7], a[168:171], v[160:163], v[4:7]\n"
    "s_waitcnt lgkmcnt(4)\n"
    "v_exp_f32_e32 v202, v202\n"
    "v_mfma_f32_16x16x32_bf16 v[8:11], a[176:179], v[160:163], v[8:11]\n"
    "ds_read_b64_tr_b16 a[168:169], v245 offset:4096\n"
    "ds_read_b64_tr_b16 a[170:171], v245 offset:6144\n"
    "buffer_load_dwordx4 v[68:71], v253, s[8:11], s48 offen offset:3072 sc1\n"
    "v_mfma_f32_16x16x32_bf16 v[8:11], a[180:183], v[164:167], v[8:11]\n"
    "s_add_u32 s48, s56, 4096\n"
    "v_exp_f32_e32 v203, v203\n"
    "v_mfma_f32_16x16x32_bf16 v[32:35], a[180:183], v[172:175], v[32:35]\n"
    "ds_read_b64_tr_b16 a[172:173], v245 offset:12288\n"
    "ds_read_b64_tr_b16 a[174:175], v245 offset:14336\n"
    "v_mfma_f32_16x16x32_bf16 v[32:35], a[176:179], v[168:171], v[32:35]\n"
    "v_cvt_pk_bf16_f32 v200, v200, v201\n"
    "v_cvt_pk_bf16_f32 v201, v202, v203\n"
    "v_mfma_f32_16x16x32_bf16 v[36:39], a[184:187], v[168:171], v[36:39]\n"
    "ds_read_b64_tr_b16 a[176:177], v246 offset:4096\n"
    "ds_read_b64_tr_b16 a[178:179], v246 offset:6144\n"
    "buffer_load_dwordx4 v[72:75], v253, s[8:11], s48 offen offset:0 sc1\n"
    "v_mfma_f32_16x16x32_bf16 v[36:39], a[188:191], v[172:175], v[36:39]\n"
    "v_exp_f32_e32 v212, v212\n"
    "v_mfma_f32_16x16x32_bf16 v[12:15], a[188:191], v[164:167], v[12:15]\n"
    "ds_read_b64_tr_b16 a[180:181], v246 offset:12288\n"
    "ds_read_b64_tr_b16 a[182:183], v246 offset:14336\n"
    "v_mfma_f32_16x16x32_bf16 v[12:15], a[184:187], v[160:163], v[12:15]\n"
    "s_waitcnt lgkmcnt(4)\n"
    "v_exp_f32_e32 v213, v213\n"
    "v_mfma_f32_16x16x32_bf16 v[16:19], a[160:163], v[160:163], v[16:19]\n"
    "ds_read_b64_tr_b16 a[184:185], v247 offset:4096\n"
    "ds_read_b64_tr_b16 a[186:187], v247 offset:6144\n"
    "buffer_load_dwordx4 v[76:79], v253, s[8:11], s48 offen offset:1024 sc1\n"
    "v_mfma_f32_16x16x32_bf16 v[16:19], a[164:167], v[164:167], v[16:19]\n"
    "v_exp_f32_e32 v214, v214\n"
    "v_mfma_f32_16x16x32_bf16 v[40:43], a[164:167], v[172:175], v[40:43]\n"
    "ds_read_b64_tr_b16 a[188:189], v247 offset:12288\n"
    "ds_read_b64_tr_b16 a[190:191], v247 offset:14336\n"
    "v_mfma_f32_16x16x32_bf16 v[40:43], a[160:163], v[168:171], v[40:43]\n"
    "v_exp_f32_e32 v215, v215\n"
    "v_mfma_f32_16x16x32_bf16 v[44:47], a[168:171], v[168:171], v[44:47]\n"
    "ds_read_b128 a[160:163], v243 offset:36864\n"
    "buffer_load_dwordx4 v[80:83], v253, s[8:11], s48 offen offset:2048 sc1\n"
    "v_cvt_pk_bf16_f32 v210, v212, v213\n"
    "v_mfma_f32_16x16x32_bf16 v[44:47], a[172:175], v[172:175], v[44:47]\n"
    "ds_read_b128 a[164:167], v240 offset:32768\n"
    "ds_write_addtid_b32 v208 offset:4096\n"
    "v_mfma_f32_16x16x32_bf16 v[20:23], a[172:175], v[164:167], v[20:23]\n"
    "v_exp_f32_e32 v204, v204\n"
    "v_mfma_f32_16x16x32_bf16 v[20:23], a[168:171], v[160:163], v[20:23]\n"
    "s_waitcnt lgkmcnt(3)\n"
    "ds_read_b128 a[168:171], v241 offset:32768\n"
    "ds_write_addtid_b32 v209 offset:4352\n"
    "v_mfma_f32_16x16x32_bf16 v[24:27], a[176:179], v[160:163], v[24:27]\n"
    "ds_read_b128 a[172:175], v240 offset:36864\n"
    "ds_write_addtid_b32 v200 offset:0\n"
    "buffer_load_dwordx4 v[84:87], v253, s[8:11], s48 offen offset:3072 sc1\n"
    "v_mfma_f32_16x16x32_bf16 v[24:27], a[180:183], v[164:167], v[24:27]\n"
    "s_add_u32 s48, s56, 8192\n"
    "v_exp_f32_e32 v205, v205\n"
    "v_mfma_f32_16x16x32_bf16 v[48:51], a[180:183], v[172:175], v[48:51]\n"
    "s_waitcnt vmcnt(8)\n"
    "ds_write_addtid_b32 v201 offset:256\n"
    "buffer_store_dword v148, v149, s[8:11], s87 offen offset:128 sc1\n"
    "v_cvt_pk_bf16_f32 v211, v214, v215\n"
    "v_mfma_f32_16x16x32_bf16 v[48:51], a[176:179], v[168:171], v[48:51]\n"
    "ds_read_b128 a[176:179], v241 offset:36864\n"
    "v_mfma_f32_16x16x32_bf16 v[52:55], a[184:187], v[168:171], v[52:55]\n"
    "ds_read_b128 a[180:183], v242 offset:32768\n"
    "buffer_load_dwordx4 v[88:91], v253, s[8:11], s48 offen offset:0 sc1\n"
    "v_mfma_f32_16x16x32_bf16 v[52:55], a[188:191], v[172:175], v[52:55]\n"
    "v_exp_f32_e32 v206, v206\n"
    "v_mfma_f32_16x16x32_bf16 v[28:31], a[188:191], v[164:167], v[28:31]\n"
    "v_exp_f32_e32 v207, v207\n"
    "v_mfma_f32_16x16x32_bf16 v[28:31], a[184:187], v[160:163], v[28:31]\n"
    "s_waitcnt lgkmcnt(4)\n"
    "ds_read_b128 a[184:187], v243 offset:32768\n"
    "v_cvt_pk_bf16_f32 v202, v204, v205\n"
    "v_mfma_f32_16x16x32_bf16 v[160:163], a[160:163], a[28:31], 0\n"
    "ds_read_b128 a[188:191], v242 offset:36864\n"
    "buffer_load_dwordx4 v[92:95], v253, s[8:11], s48 offen offset:1024 sc1\n"
    "v_mfma_f32_16x16x32_bf16 v[160:163], a[164:167], a[0:3], v[160:163]\n"
    "ds_write_addtid_b32 v210 offset:4608\n"
    "v_cvt_pk_bf16_f32 v203, v206, v207\n"
    "v_mfma_f32_16x16x32_bf16 v[168:171], a[164:167], a[32:35], 0\n"
    "ds_write_addtid_b32 v211 offset:4864\n"
    "v_mfma_f32_16x16x32_bf16 v[168:171], a[160:163], a[60:63], v[168:171]\n"
    "ds_read_b128 a[160:163], v243 offset:38912\n"
    "v_mfma_f32_16x16x32_bf16 v[168:171], a[168:171], a[36:39], v[168:171]\n"
    "ds_read_b128 a[164:167], v240 offset:34816\n"
    "buffer_load_dwordx4 v[96:99], v253, s[8:11], s48 offen offset:2048 sc1\n"
    "v_mfma_f32_16x16x32_bf16 v[168:171], a[172:175], a[40:43], v[168:171]\n"
    "ds_write_addtid_b32 v202 offset:512\n"
    "v_mfma_f32_16x16x32_bf16 v[160:163], a[172:175], a[8:11], v[160:163]\n"
    "ds_write_addtid_b32 v203 offset:768\n"
    "v_mfma_f32_16x16x32_bf16 v[160:163], a[168:171], a[4:7], v[160:163]\n"
    "s_waitcnt lgkmcnt(6)\n"
    "ds_read_b128 a[168:171], v241 offset:34816\n"
    "v_mfma_f32_16x16x32_bf16 v[160:163], a[176:179], a[12:15], v[160:163]\n"
    "ds_read_b128 a[172:175], v240 offset:38912\n"
    "buffer_load_dwordx4 v[100:103], v253, s[8:11], s48 offen offset:3072 sc1\n"
    "v_mfma_f32_16x16x32_bf16 v[160:163], a[180:183], a[16:19], v[160:163]\n"
    "s_waitcnt lgkmcnt(2)\n"
    "s_add_u32 s48, s56, 12288\n"
    "v_mfma_f32_16x16x32_bf16 v[168:171], a[180:183], a[48:51], v[168:171]\n"
    "s_barrier\n"
    "v_mfma_f32_16x16x32_bf16 v[168:171], a[176:179], a[44:47], v[168:171]\n"
    "ds_read_b128 a[176:179], v241 offset:38912\n"
    "v_mfma_f32_16x16x32_bf16 v[168:171], a[184:187], a[52:55], v[168:171]\n"
    "ds_read_b128 a[180:183], v242 offset:34816\n"
    "buffer_load_dwordx4 v[104:107], v253, s[8:11], s48 offen offset:0 sc1\n"
    "v_mfma_f32_16x16x32_bf16 v[168:171], a[188:191], a[56:59], v[168:171]\n"
    "ds_read2st64_b32 v[204:205], v250 offset0:0 offset1:1\n"
    "v_mfma_f32_16x16x32_bf16 v[160:163], a[188:191], a[24:27], v[160:163]\n"
    "v_mfma_f32_16x16x32_bf16 v[160:163], a[184:187], a[20:23], v[160:163]\n"
    "s_waitcnt lgkmcnt(3)\n"
    "ds_read_b128 a[184:187], v243 offset:34816\n"
    "v_exp_f32_e32 v168, v168\n"
    "v_mfma_f32_16x16x32_bf16 v[164:167], a[160:163], a[28:31], 0\n"
    "ds_read_b128 a[188:191], v242 offset:38912\n"
    "buffer_load_dwordx4 v[108:111], v253, s[8:11], s48 offen offset:1024 sc1\n"
    "v_exp_f32_e32 v169, v169\n"
    "v_mfma_f32_16x16x32_bf16 v[164:167], a[164:167], a[0:3], v[164:167]\n"
    "ds_read2st64_b32 v[206:207], v250 offset0:2 offset1:3\n"
    "v_exp_f32_e32 v170, v170\n"
    "v_mfma_f32_16x16x32_bf16 v[172:175], a[164:167], a[32:35], 0\n"
    "v_exp_f32_e32 v171, v171\n"
    "v_mfma_f32_16x16x32_bf16 v[172:175], a[160:163], a[60:63], v[172:175]\n"
    "ds_read_b64_tr_b16 a[160:161], v244 offset:16384\n"
    "ds_read_b64_tr_b16 a[162:163], v244 offset:18432\n"
    "v_mfma_f32_16x16x32_bf16 v[172:175], a[168:171], a[36:39], v[172:175]\n"
    "ds_read2st64_b32 v[212:213], v250 offset0:16 offset1:17\n"
    "buffer_load_dwordx4 v[112:115], v253, s[8:11], s48 offen offset:2048 sc1\n"
    "v_cvt_pk_bf16_f32 v168, v168, v169\n"
    "v_mfma_f32_16x16x32_bf16 v[172:175], a[172:175], a[40:43], v[172:175]\n"
    "ds_read_b64_tr_b16 a[164:165], v244 offset:24576\n"
    "ds_read_b64_tr_b16 a[166:167], v244 offset:26624\n"
    "v_mfma_f32_16x16x32_bf16 v[164:167], a[172:175], a[8:11], v[164:167]\n"
    "v_exp_f32_e32 v160, v160\n"
    "v_mfma_f32_16x16x32_bf16 v[164:167], a[168:171], a[4:7], v[164:167]\n"
    "s_waitcnt lgkmcnt(6)\n"
    "ds_read_b64_tr_b16 a[168:169], v245 offset:16384\n"
    "ds_read_b64_tr_b16 a[170:171], v245 offset:18432\n"
    "v_mfma_f32_16x16x32_bf16 v[164:167], a[176:179], a[12:15], v[164:167]\n"
    "ds_read2st64_b32 v[214:215], v250 offset0:18 offset1:19\n"
    "buffer_load_dwordx4 v[116:119], v253, s[8:11], s48 offen offset:3072 sc1\n"
    "v_mfma_f32_16x16x32_bf16 v[164:167], a[180:183], a[16:19], v[164:167]\n"
    "s_add_u32 s48, s56, 16384\n"
    "ds_read_b64_tr_b16 a[172:173], v245 offset:24576\n"
    "ds_read_b64_tr_b16 a[174:175], v245 offset:26624\n"
    "v_mfma_f32_16x16x32_bf16 v[172:175], a[180:183], a[48:51], v[172:175]\n"
    "v_exp_f32_e32 v161, v161\n"
    "v_mfma_f32_16x16x32_bf16 v[172:175], a[176:179], a[44:47], v[172:175]\n"
    "ds_read_b64_tr_b16 a[176:177], v246 offset:16384\n"
    "ds_read_b64_tr_b16 a[178:179], v246 offset:18432\n"
    "v_cvt_pk_bf16_f32 v169, v170, v171\n"
    "v_mfma_f32_16x16x32_bf16 v[172:175], a[184:187], a[52:55], v[172:175]\n"
    "buffer_load_dwordx4 a[28:31], v253, s[8:11], s48 offen offset:0 sc1\n"
    "v_exp_f32_e32 v162, v162\n"
    "v_mfma_f32_16x16x32_bf16 v[172:175], a[188:191], a[56:59], v[172:175]\n"
    "ds_read_b64_tr_b16 a[180:181], v246 offset:24576\n"
    "ds_read_b64_tr_b16 a[182:183], v246 offset:26624\n"
    "v_mfma_f32_16x16x32_bf16 v[164:167], a[188:191], a[24:27], v[164:167]\n"
    "v_exp_f32_e32 v163, v163\n"
    "v_mfma_f32_16x16x32_bf16 v[164:167], a[184:187], a[20:23], v[164:167]\n"
    "s_waitcnt lgkmcnt(4)\n"
    "ds_read_b64_tr_b16 a[184:185], v247 offset:16384\n"
    "ds_read_b64_tr_b16 a[186:187], v247 offset:18432\n"
    "v_mfma_f32_16x16x32_bf16 v[0:3], a[160:163], v[200:203], v[0:3]\n"
    "buffer_load_dwordx4 a[60:63], v253, s[8:11], s48 offen offset:1024 sc1\n"
    "v_cvt_pk_bf16_f32 v160, v160, v161\n"
    "v_cvt_pk_bf16_f32 v161, v162, v163\n"
    "v_mfma_f32_16x16x32_bf16 v[0:3], a[164:167], v[204:207], v[0:3]\n"
    "ds_read_b64_tr_b16 a[188:189], v247 offset:24576\n"
    "ds_read_b64_tr_b16 a[190:191], v247 offset:26624\n"
    "v_mfma_f32_16x16x32_bf16 a[248:251], a[164:167], v[212:215], a[248:251]\n"
    "v_exp_f32_e32 v172, v172\n"
    "v_mfma_f32_16x16x32_bf16 a[248:251], a[160:163], v[208:211], a[248:251]\n"
    "ds_read_b64_tr_b16 a[160:161], v244 offset:20480\n"
    "ds_read_b64_tr_b16 a[162:163], v244 offset:22528\n"
    "v_mfma_f32_16x16x32_bf16 a[252:255], a[168:171], v[208:211], a[252:255]\n"
    "buffer_load_dwordx4 a[0:3], v253, s[8:11], s48 offen offset:2048 sc1\n"
    "v_exp_f32_e32 v173, v173\n"
    "v_mfma_f32_16x16x32_bf16 a[252:255], a[172:175], v[212:215], a[252:255]\n"
    "s_waitcnt vmcnt(41)\n"
    "s_barrier\n"
    "v_mfma_f32_16x16x32_bf16 v[4:7], a[172:175], v[204:207], v[4:7]\n"
    "ds_read_b64_tr_b16 a[164:165], v244 offset:28672\n"
    "ds_read_b64_tr_b16 a[166:167], v244 offset:30720\n"
    "v_mfma_f32_16x16x32_bf16 v[4:7], a[168:171], v[200:203], v[4:7]\n"
    "s_waitcnt lgkmcnt(4)\n"
    "ds_write_addtid_b32 v168 offset:4096\n"
    "v_exp_f32_e32 v174, v174\n"
    "v_mfma_f32_16x16x32_bf16 v[8:11], a[176:179], v[200:203], v[8:11]\n"
    "ds_read_b64_tr_b16 a[168:169], v245 offset:20480\n"
    "ds_read_b64_tr_b16 a[170:171], v245 offset:22528\n"
    "buffer_load_dwordx4 a[32:35], v253, s[8:11], s48 offen offset:3072 sc1\n"
    "v_mfma_f32_16x16x32_bf16 v[8:11], a[180:183], v[204:207], v[8:11]\n"
    "s_add_u32 s48, s56, 20480\n"
    "ds_write_addtid_b32 v169 offset:4352\n"
    "v_exp_f32_e32 v175, v175\n"
    "v_mfma_f32_16x16x32_bf16 v[32:35], a[180:183], v[212:215], v[32:35]\n"
    "ds_read_b64_tr_b16 a[172:173], v245 offset:28672\n"
    "ds_read_b64_tr_b16 a[174:175], v245 offset:30720\n"
    "v_cvt_pk_bf16_f32 v170, v172, v173\n"
    "v_mfma_f32_16x16x32_bf16 v[32:35], a[176:179], v[208:211], v[32:35]\n"
    "v_exp_f32_e32 v164, v164\n"
    "v_mfma_f32_16x16x32_bf16 v[36:39], a[184:187], v[208:211], v[36:39]\n"
    "ds_read_b64_tr_b16 a[176:177], v246 offset:20480\n"
    "ds_read_b64_tr_b16 a[178:179], v246 offset:22528\n"
    "buffer_load_dwordx4 a[4:7], v253, s[8:11], s48 offen offset:0 sc1\n"
    "v_mfma_f32_16x16x32_bf16 v[36:39], a[188:191], v[212:215], v[36:39]\n"
    "v_exp_f32_e32 v165, v165\n"
    "v_mfma_f32_16x16x32_bf16 v[12:15], a[188:191], v[204:207], v[12:15]\n"
    "ds_read_b64_tr_b16 a[180:181], v246 offset:28672\n"
    "ds_read_b64_tr_b16 a[182:183], v246 offset:30720\n"
    "v_mfma_f32_16x16x32_bf16 v[12:15], a[184:187], v[200:203], v[12:15]\n"
    "s_waitcnt lgkmcnt(4)\n"
    "ds_write_addtid_b32 v160 offset:0\n"
    "v_cvt_pk_bf16_f32 v171, v174, v175\n"
    "v_mfma_f32_16x16x32_bf16 v[16:19], a[160:163], v[200:203], v[16:19]\n"
    "ds_read_b64_tr_b16 a[184:185], v247 offset:20480\n"
    "ds_read_b64_tr_b16 a[186:187], v247 offset:22528\n"
    "buffer_load_dwordx4 a[36:39], v253, s[8:11], s48 offen offset:1024 sc1\n"
    "v_mfma_f32_16x16x32_bf16 v[16:19], a[164:167], v[204:207], v[16:19]\n"
    "v_exp_f32_e32 v166, v166\n"
    "v_mfma_f32_16x16x32_bf16 v[40:43], a[164:167], v[212:215], v[40:43]\n"
    "ds_read_b64_tr_b16 a[188:189], v247 offset:28672\n"
    "ds_read_b64_tr_b16 a[190:191], v247 offset:30720\n"
    "v_mfma_f32_16x16x32_bf16 v[40:43], a[160:163], v[208:211], v[40:43]\n"
    "v_exp_f32_e32 v167, v167\n"
    "v_mfma_f32_16x16x32_bf16 v[44:47], a[168:171], v[208:211], v[44:47]\n"
    "ds_read_b64_tr_b16 a[160:161], v244 offset:32768\n"
    "buffer_load_dwordx4 a[8:11], v253, s[8:11], s48 offen offset:2048 sc1\n"
    "v_cvt_pk_bf16_f32 v162, v164, v165\n"
    "v_mfma_f32_16x16x32_bf16 v[44:47], a[172:175], v[212:215], v[44:47]\n"
    "ds_read_b64_tr_b16 a[162:163], v244 offset:34816\n"
    "v_cvt_pk_bf16_f32 v163, v166, v167\n"
    "v_mfma_f32_16x16x32_bf16 v[20:23], a[172:175], v[204:207], v[20:23]\n"
    "ds_read_b64_tr_b16 a[164:165], v244 offset:40960\n"
    "ds_write_addtid_b32 v161 offset:256\n"
    "v_mfma_f32_16x16x32_bf16 v[20:23], a[168:171], v[200:203], v[20:23]\n"
    "s_waitcnt lgkmcnt(4)\n"
    "ds_read_b64_tr_b16 a[166:167], v244 offset:43008\n"
    "ds_write_addtid_b32 v170 offset:4608\n"
    "v_mfma_f32_16x16x32_bf16 v[24:27], a[176:179], v[200:203], v[24:27]\n"
    "ds_read_b64_tr_b16 a[168:169], v245 offset:32768\n"
    "ds_write_addtid_b32 v171 offset:4864\n"
    "buffer_load_dwordx4 a[40:43], v253, s[8:11], s48 offen offset:3072 sc1\n"
    "v_mfma_f32_16x16x32_bf16 v[24:27], a[180:183], v[204:207], v[24:27]\n"
    "s_add_u32 s48, s56, 24576\n"
    "ds_read_b64_tr_b16 a[170:171], v245 offset:34816\n"
    "ds_write_addtid_b32 v162 offset:512\n"
    "v_mfma_f32_16x16x32_bf16 v[48:51], a[180:183], v[212:215], v[48:51]\n"
    "ds_read_b64_tr_b16 a[172:173], v245 offset:40960\n"
    "ds_write_addtid_b32 v163 offset:768\n"
    "v_mfma_f32_16x16x32_bf16 v[48:51], a[176:179], v[208:211], v[48:51]\n"
    "ds_read_b64_tr_b16 a[174:175], v245 offset:43008\n"
    "v_and_b32_e32 v242, 0x400, v240\n"
    "v_mfma_f32_16x16x32_bf16 v[52:55], a[184:187], v[208:211], v[52:55]\n"
    "ds_read_b64_tr_b16 a[176:177], v246 offset:32768\n"
    "buffer_load_dwordx4 a[12:15], v253, s[8:11], s48 offen offset:0 sc1\n"
    "v_and_b32_e32 v240, 0x3ff, v240\n"
    "v_mfma_f32_16x16x32_bf16 v[52:55], a[188:191], v[212:215], v[52:55]\n"
    "s_waitcnt lgkmcnt(2)\n"
    "s_barrier\n"
    "v_mfma_f32_16x16x32_bf16 v[28:31], a[188:191], v[204:207], v[28:31]\n"
    "ds_read_b64_tr_b16 a[178:179], v246 offset:34816\n"
    "v_lshl_or_b32 v240, v242, 2, v240\n"
    "v_mfma_f32_16x16x32_bf16 v[28:31], a[184:187], v[200:203], v[28:31]\n"
    "s_waitcnt lgkmcnt(0)\n"
    "ds_read_b64_tr_b16 a[180:181], v246 offset:40960\n"
    "ds_read2st64_b32 v[164:165], v250 offset0:0 offset1:1\n"
    "v_mfma_f32_16x16x32_bf16 v[0:3], a[160:163], v[160:163], v[0:3]\n"
    "ds_read_b64_tr_b16 a[182:183], v246 offset:43008\n"
    "ds_read2st64_b32 v[166:167], v250 offset0:2 offset1:3\n"
    "buffer_load_dwordx4 a[44:47], v253, s[8:11], s48 offen offset:1024 sc1\n"
    "v_mfma_f32_16x16x32_bf16 a[248:251], a[160:163], v[168:171], a[248:251]\n"
    "ds_read_b64_tr_b16 a[184:185], v247 offset:32768\n"
    "ds_read2st64_b32 v[172:173], v250 offset0:16 offset1:17\n"
    "v_mfma_f32_16x16x32_bf16 a[252:255], a[168:171], v[168:171], a[252:255]\n"
    "ds_read_b64_tr_b16 a[186:187], v247 offset:34816\n"
    "ds_read2st64_b32 v[174:175], v250 offset0:18 offset1:19\n"
    "v_mfma_f32_16x16x32_bf16 v[4:7], a[168:171], v[160:163], v[4:7]\n"
    "ds_read_b64_tr_b16 a[188:189], v247 offset:40960\n"
    "v_mfma_f32_16x16x32_bf16 v[8:11], a[176:179], v[160:163], v[8:11]\n"
    "ds_read_b64_tr_b16 a[190:191], v247 offset:43008\n"
    "buffer_load_dwordx4 a[16:19], v253, s[8:11], s48 offen offset:2048 sc1\n"
    "v_mfma_f32_16x16x32_bf16 v[32:35], a[176:179], v[168:171], v[32:35]\n"
    "s_waitcnt lgkmcnt(2)\n"
    "ds_read_b64_tr_b16 a[160:161], v244 offset:36864\n"
    "v_xor_b32_e32 v241, 64, v240\n"
    "v_mfma_f32_16x16x32_bf16 v[0:3], a[164:167], v[164:167], v[0:3]\n"
    "ds_read_b64_tr_b16 a[162:163], v244 offset:38912\n"
    "v_mfma_f32_16x16x32_bf16 a[248:251], a[164:167], v[172:175], a[248:251]\n"
    "s_waitcnt lgkmcnt(2)\n"
    "ds_read_b64_tr_b16 a[164:165], v244 offset:45056\n"
    "v_mfma_f32_16x16x32_bf16 a[252:255], a[172:175], v[172:175], a[252:255]\n"
    "ds_read_b64_tr_b16 a[166:167], v244 offset:47104\n"
    "buffer_load_dwordx4 a[48:51], v253, s[8:11], s48 offen offset:3072 sc1\n"
    "v_mfma_f32_16x16x32_bf16 v[4:7], a[172:175], v[164:167], v[4:7]\n"
    "s_add_u32 s48, s56, 28672\n"
    "ds_read_b64_tr_b16 a[168:169], v245 offset:36864\n"
    "v_mfma_f32_16x16x32_bf16 v[8:11], a[180:183], v[164:167], v[8:11]\n"
    "ds_read_b64_tr_b16 a[170:171], v245 offset:38912\n"
    "v_mfma_f32_16x16x32_bf16 v[32:35], a[180:183], v[172:175], v[32:35]\n"
    "ds_read_b64_tr_b16 a[172:173], v245 offset:45056\n"
    "v_accvgpr_read_b32 v216, a248\n"
    "v_mfma_f32_16x16x32_bf16 v[36:39], a[184:187], v[168:171], v[36:39]\n"
    "ds_read_b64_tr_b16 a[174:175], v245 offset:47104\n"
    "buffer_load_dwordx4 a[20:23], v253, s[8:11], s48 offen offset:0 sc1\n"
    "v_mfma_f32_16x16x32_bf16 v[36:39], a[188:191], v[172:175], v[36:39]\n"
    "s_lshl_b32 s45, s51, 16\n"
    "ds_read_b64_tr_b16 a[176:177], v246 offset:36864\n"
    "v_accvgpr_read_b32 v217, a249\n"
    "v_mfma_f32_16x16x32_bf16 v[12:15], a[188:191], v[164:167], v[12:15]\n"
    "ds_read_b64_tr_b16 a[178:179], v246 offset:38912\n"
    "buffer_load_dword v146, v145, s[8:11], s47 offen offset:128 sc1\n"
    "v_accvgpr_read_b32 v218, a250\n"
    "v_mfma_f32_16x16x32_bf16 v[12:15], a[184:187], v[160:163], v[12:15]\n"
    "s_waitcnt lgkmcnt(2)\n"
    "ds_read_b64_tr_b16 a[180:181], v246 offset:45056\n"
    "v_accvgpr_read_b32 v219, a251\n"
    "v_mfma_f32_16x16x32_bf16 v[16:19], a[160:163], v[160:163], v[16:19]\n"
    "ds_read_b64_tr_b16 a[182:183], v246 offset:47104\n"
    "buffer_load_dwordx4 a[52:55], v253, s[8:11], s48 offen offset:1024 sc1\n"
    "v_mfma_f32_16x16x32_bf16 v[16:19], a[164:167], v[164:167], v[16:19]\n"
    "s_lshl_b32 s46, s50, 11\n"
    "ds_read_b64_tr_b16 a[184:185], v247 offset:36864\n"
    "v_accvgpr_read_b32 v220, a252\n"
    "v_mfma_f32_16x16x32_bf16 v[40:43], a[164:167], v[172:175], v[40:43]\n"
    "s_add_u32 s46, s46, s45\n"
    "ds_read_b64_tr_b16 a[186:187], v247 offset:38912\n"
    "v_accvgpr_read_b32 v221, a253\n"
    "v_mfma_f32_16x16x32_bf16 v[40:43], a[160:163], v[168:171], v[40:43]\n"
    "s_add_u32 s46, s46, %3\n"
    "ds_read_b64_tr_b16 a[188:189], v247 offset:45056\n"
    "v_accvgpr_read_b32 v222, a254\n"
    "v_mfma_f32_16x16x32_bf16 v[44:47], a[168:171], v[168:171], v[44:47]\n"
    "ds_read_b64_tr_b16 a[190:191], v247 offset:47104\n"
    "buffer_load_dwordx4 a[24:27], v253, s[8:11], s48 offen offset:2048 sc1\n"
    "v_mfma_f32_16x16x32_bf16 v[44:47], a[172:175], v[172:175], v[44:47]\n"
    "s_bitcmp0_b32 %1, 0\n"
    "s_cbranch_scc1 .Lat_two\n"
    "s_add_u32 s46, s46, 81920\n"
    "ds_read_b128 a[160:163], v241 offset:11264\n"
    "v_accvgpr_read_b32 v223, a255\n"
    "v_mfma_f32_16x16x32_bf16 v[20:23], a[172:175], v[164:167], v[20:23]\n"
    "ds_read_b128 a[164:167], v241 offset:19456\n"
    "v_cvt_pk_bf16_f32 v216, v216, v217\n"
    "v_mfma_f32_16x16x32_bf16 v[20:23], a[168:171], v[160:163], v[20:23]\n"
    "s_waitcnt lgkmcnt(2)\n"
    "ds_read_b128 a[168:171], v241 offset:27648\n"
    "v_cvt_pk_bf16_f32 v217, v218, v219\n"
    "v_mfma_f32_16x16x32_bf16 v[24:27], a[176:179], v[160:163], v[24:27]\n"
    "ds_read_b128 a[172:175], v240 offset:8192\n"
    "buffer_load_dwordx4 a[56:59], v253, s[8:11], s48 offen offset:3072 sc1\n"
    "v_mfma_f32_16x16x32_bf16 v[24:27], a[180:183], v[164:167], v[24:27]\n"
    "v_cvt_pk_bf16_f32 v218, v220, v221\n"
    "v_cvt_pk_bf16_f32 v219, v222, v223\n"
    "v_mfma_f32_16x16x32_bf16 v[48:51], a[180:183], v[172:175], v[48:51]\n"
    "buffer_store_dwordx4 v[216:219], v253, s[8:11], s83 offen offset:0 sc1\n"
    "v_cvt_pk_bf16_f32 v32, v32, v33\n"
    "v_cvt_pk_bf16_f32 v33, v34, v35\n"
    "v_mfma_f32_16x16x32_bf16 v[48:51], a[176:179], v[168:171], v[48:51]\n"
    "ds_read_b128 a[176:179], v240 offset:16384\n"
    "v_cvt_pk_bf16_f32 v34, v36, v37\n"
    "v_mfma_f32_16x16x32_bf16 v[52:55], a[184:187], v[168:171], v[52:55]\n"
    "ds_read_b128 a[180:183], v240 offset:24576\n"
    "v_cvt_pk_bf16_f32 v35, v38, v39\n"
    "v_mfma_f32_16x16x32_bf16 v[52:55], a[188:191], v[172:175], v[52:55]\n"
    "buffer_store_dwordx4 v[32:35], v253, s[8:11], s83 offen offset:1024 sc1\n"
    "v_cvt_pk_bf16_f32 v40, v40, v41\n"
    "v_cvt_pk_bf16_f32 v41, v42, v43\n"
    "v_mfma_f32_16x16x32_bf16 v[28:31], a[188:191], v[164:167], v[28:31]\n"
    "v_cvt_pk_bf16_f32 v42, v44, v45\n"
    "v_cvt_pk_bf16_f32 v43, v46, v47\n"
    "v_mfma_f32_16x16x32_bf16 v[28:31], a[184:187], v[160:163], v[28:31]\n"
    "ds_read_b128 a[184:187], v241 offset:8192\n"
    "buffer_store_dwordx4 v[40:43], v253, s[8:11], s83 offen offset:2048 sc1\n"
    "v_cvt_pk_bf16_f32 v48, v48, v49\n"
    "v_add_u32_e32 v158, s46, v253\n"
    "v_mfma_f32_16x16x32_bf16 v[160:163], v[154:157], a[224:227], 0\n"
    "ds_read_b128 a[188:191], v241 offset:16384\n"
    "v_cvt_pk_bf16_f32 v49, v50, v51\n"
    "v_mfma_f32_16x16x32_bf16 v[164:167], v[152:155], a[224:227], 0\n"
    "v_cvt_pk_bf16_f32 v50, v52, v53\n"
    "v_cvt_pk_bf16_f32 v51, v54, v55\n"
    "v_mfma_f32_16x16x32_bf16 v[168:171], v[154:157], a[228:231], 0\n"
    "s_add_u32 s45, %3, 155840\n"
    "buffer_store_dwordx4 v[48:51], v253, s[8:11], s83 offen offset:3072 sc1\n"
    "v_and_b32_e32 v150, 15, v147\n"
    "v_lshlrev_b32_e32 v151, 9, v150\n"
    "v_mfma_f32_16x16x32_bf16 v[172:175], v[152:155], a[228:231], 0\n"
    "buffer_store_dword v55, v251, s[8:11], s60 offen offset:0 sc1\n"
    "v_lshl_add_u32 v150, v150, 2, s45\n"
    "v_and_or_b32 v151, v147, 48, v151\n"
    "v_mfma_f32_16x16x32_bf16 v[176:179], v[154:157], a[232:235], 0\n"
    "s_lshl_b32 s46, %2, 7\n"
    "s_add_u32 s58, s35, s46\n"
    "s_mul_i32 s45, %1, 74496\n"
    "v_mfma_f32_16x16x32_bf16 v[180:183], v[152:155], a[232:235], 0\n"
    "s_add_u32 s45, s45, 16265216\n"
    "ds_write_b128 v159, v[216:219] offset:0\n"
    "v_mfma_f32_16x16x32_bf16 v[184:187], v[154:157], a[236:239], 0\n"
    "s_lshl_b32 s54, %2, 11\n"
    "v_and_b32_e32 v147, 0xf0, v253\n"
    "v_add_u32_e32 v252, %3, v253\n"
    "v_mfma_f32_16x16x32_bf16 v[188:191], v[152:155], a[236:239], 0\n"
    "s_add_u32 s54, s54, s45\n"
    "ds_write_b128 v159, v[32:35] offset:1024\n"
    "v_mfma_f32_16x16x32_bf16 v[192:195], v[154:157], a[240:243], 0\n"
    "s_add_u32 s54, s54, 16384\n"
    "ds_write_b128 v159, v[40:43] offset:2048\n"
    "v_mfma_f32_16x16x32_bf16 v[196:199], v[152:155], a[240:243], 0\n"
    "s_lshl1_add_u32 s59, s50, s51\n"
    "s_lshl_b32 s59, s59, 12\n"
    "v_mfma_f32_16x16x32_bf16 v[200:203], v[154:157], a[244:247], 0\n"
    "s_add_u32 s46, s59, s45\n"
    "ds_write_b128 v159, v[48:51] offset:3072\n"
    "v_mfma_f32_16x16x32_bf16 v[204:207], v[152:155], a[244:247], 0\n"
    "s_waitcnt lgkmcnt(10)\n"
    "s_bitcmp1_b32 %1, 0\n"
    "s_cselect_b32 s54, s54, 2.0\n"
    "s_cselect_b32 s55, s58, 2.0\n"
    "v_mfma_f32_16x16x32_bf16 v[160:163], a[192:195], a[160:163], v[160:163]\n"
    "ds_write_b32 v254, v55 offset:0\n"
    "v_mfma_f32_16x16x32_bf16 v[164:167], a[196:199], a[160:163], v[164:167]\n"
    "s_add_u32 s61, s54, 0\n"
    "ds_write_b32 v255, v31 offset:0\n"
    "v_mfma_f32_16x16x32_bf16 v[168:171], a[200:203], a[160:163], v[168:171]\n"
    "s_waitcnt vmcnt(8)\n"
    "v_cmp_ne_u32_e32 vcc, s39, v146\n"
    "v_mfma_f32_16x16x32_bf16 v[172:175], a[204:207], a[160:163], v[172:175]\n"
    "ds_read_b128 a[160:163], v241 offset:24576\n"
    "v_mfma_f32_16x16x32_bf16 v[176:179], a[192:195], a[164:167], v[176:179]\n"
    "s_cbranch_vccnz .Lat_xpoll\n"
    ".Lat_xpoll_ret:\n"
    "v_mfma_f32_16x16x32_bf16 v[180:183], a[196:199], a[164:167], v[180:183]\n"
    "v_mfma_f32_16x16x32_bf16 v[184:187], a[200:203], a[164:167], v[184:187]\n"
    "buffer_load_dwordx4 a[224:227], v253, s[8:11], s61 offen offset:0 sc1\n"
    "v_mfma_f32_16x16x32_bf16 v[188:191], a[204:207], a[164:167], v[188:191]\n"
    "s_waitcnt lgkmcnt(11)\n"
    "ds_read_b128 a[164:167], v240 offset:9216\n"
    "v_mfma_f32_16x16x32_bf16 v[192:195], a[192:195], a[168:171], v[192:195]\n"
    "v_mfma_f32_16x16x32_bf16 v[196:199], a[196:199], a[168:171], v[196:199]\n"
    "v_mfma_f32_16x16x32_bf16 v[200:203], a[200:203], a[168:171], v[200:203]\n"
    "v_mfma_f32_16x16x32_bf16 v[204:207], a[204:207], a[168:171], v[204:207]\n"
    "ds_read_b128 a[168:171], v240 offset:17408\n"
    "buffer_load_dwordx4 a[228:231], v253, s[8:11], s61 offen offset:1024 sc1\n"
    "v_mfma_f32_16x16x32_bf16 v[160:163], a[208:211], a[172:175], v[160:163]\n"
    "s_waitcnt lgkmcnt(3)\n"
    "s_barrier\n"
    "v_mfma_f32_16x16x32_bf16 v[164:167], a[212:215], a[172:175], v[164:167]\n"
    "s_add_u32 s61, s54, 24832\n"
    "v_mfma_f32_16x16x32_bf16 v[168:171], a[216:219], a[172:175], v[168:171]\n"
    "v_mfma_f32_16x16x32_bf16 v[172:175], a[220:223], a[172:175], v[172:175]\n"
    "ds_read_b128 a[172:175], v240 offset:25600\n"
    "v_mfma_f32_16x16x32_bf16 v[176:179], a[208:211], a[176:179], v[176:179]\n"
    "buffer_load_dwordx4 a[232:235], v253, s[8:11], s61 offen offset:0 sc1\n"
    "v_mfma_f32_16x16x32_bf16 v[180:183], a[212:215], a[176:179], v[180:183]\n"
    "ds_read_b32 v232, v150 offset:0\n"
    "v_mfma_f32_16x16x32_bf16 v[184:187], a[216:219], a[176:179], v[184:187]\n"
    "v_mfma_f32_16x16x32_bf16 v[188:191], a[220:223], a[176:179], v[188:191]\n"
    "ds_read_b128 a[176:179], v241 offset:9216\n"
    "v_mfma_f32_16x16x32_bf16 v[192:195], a[208:211], a[180:183], v[192:195]\n"
    "v_mfma_f32_16x16x32_bf16 v[196:199], a[212:215], a[180:183], v[196:199]\n"
    "buffer_load_dwordx4 a[236:239], v253, s[8:11], s61 offen offset:1024 sc1\n"
    "v_mfma_f32_16x16x32_bf16 v[200:203], a[216:219], a[180:183], v[200:203]\n"
    "s_add_u32 s61, s54, 49664\n"
    "ds_read_b32 v233, v150 offset:256\n"
    "v_mfma_f32_16x16x32_bf16 v[204:207], a[220:223], a[180:183], v[204:207]\n"
    "ds_read_b128 a[180:183], v241 offset:17408\n"
    "v_mfma_f32_16x16x32_bf16 v[160:163], a[92:95], a[184:187], v[160:163]\n"
    "v_mfma_f32_16x16x32_bf16 v[164:167], a[124:127], a[184:187], v[164:167]\n"
    "v_mfma_f32_16x16x32_bf16 v[168:171], a[156:159], a[184:187], v[168:171]\n"
    "buffer_load_dwordx4 a[240:243], v253, s[8:11], s61 offen offset:0 sc1\n"
    "v_mfma_f32_16x16x32_bf16 v[172:175], a[64:67], a[184:187], v[172:175]\n"
    "ds_read_b128 a[184:187], v241 offset:25600\n"
    "v_mfma_f32_16x16x32_bf16 v[176:179], a[92:95], a[188:191], v[176:179]\n"
    "ds_read_b32 v234, v150 offset:512\n"
    "v_mfma_f32_16x16x32_bf16 v[180:183], a[124:127], a[188:191], v[180:183]\n"
    "v_mfma_f32_16x16x32_bf16 v[184:187], a[156:159], a[188:191], v[184:187]\n"
    "v_mfma_f32_16x16x32_bf16 v[188:191], a[64:67], a[188:191], v[188:191]\n"
    "s_waitcnt lgkmcnt(8)\n"
    "ds_read_b128 a[188:191], v240 offset:10240\n"
    "v_mfma_f32_16x16x32_bf16 v[192:195], a[92:95], a[160:163], v[192:195]\n"
    "buffer_load_dwordx4 a[244:247], v253, s[8:11], s61 offen offset:1024 sc1\n"
    "v_mfma_f32_16x16x32_bf16 v[196:199], a[124:127], a[160:163], v[196:199]\n"
    "s_add_u32 s61, s46, 0\n"
    "ds_read_b128 a[248:251], v158 offset:0\n"
    "v_mfma_f32_16x16x32_bf16 v[200:203], a[156:159], a[160:163], v[200:203]\n"
    "v_mfma_f32_16x16x32_bf16 v[204:207], a[64:67], a[160:163], v[204:207]\n"
    "ds_read_b128 a[160:163], v240 offset:18432\n"
    "v_mfma_f32_16x16x32_bf16 v[160:163], a[96:99], a[164:167], v[160:163]\n"
    "s_waitcnt vmcnt(6)\n"
    "buffer_store_dword v148, v149, s[8:11], s87 offen offset:0 sc1\n"
    "v_mfma_f32_16x16x32_bf16 v[164:167], a[128:131], a[164:167], v[164:167]\n"
    "buffer_load_dwordx4 a[192:195], v253, s[8:11], s61 offen offset:0 sc1\n"
    "v_mfma_f32_16x16x32_bf16 v[168:171], a[68:71], a[164:167], v[168:171]\n"
    "ds_read_b128 a[252:255], v158 offset:1024\n"
    "v_mfma_f32_16x16x32_bf16 v[172:175], a[100:103], a[164:167], v[172:175]\n"
    "s_waitcnt lgkmcnt(10)\n"
    "ds_read_b128 a[164:167], v240 offset:26624\n"
    "v_mfma_f32_16x16x32_bf16 v[176:179], a[96:99], a[168:171], v[176:179]\n"
    "v_mfma_f32_16x16x32_bf16 v[180:183], a[128:131], a[168:171], v[180:183]\n"
    "v_mfma_f32_16x16x32_bf16 v[184:187], a[68:71], a[168:171], v[184:187]\n"
    "buffer_load_dwordx4 a[196:199], v253, s[8:11], s61 offen offset:1024 sc1\n"
    "v_mfma_f32_16x16x32_bf16 v[188:191], a[100:103], a[168:171], v[188:191]\n"
    "s_lshl_b32 s52, %2, 11\n"
    "ds_read_b128 a[168:171], v241 offset:10240\n"
    "v_mfma_f32_16x16x32_bf16 v[192:195], a[96:99], a[172:175], v[192:195]\n"
    "v_mfma_f32_16x16x32_bf16 v[196:199], a[128:131], a[172:175], v[196:199]\n"
    "v_mfma_f32_16x16x32_bf16 v[200:203], a[68:71], a[172:175], v[200:203]\n"
    "v_mfma_f32_16x16x32_bf16 v[204:207], a[100:103], a[172:175], v[204:207]\n"
    "s_waitcnt lgkmcnt(8)\n"
    "ds_read_b128 a[172:175], v241 offset:18432\n"
    "v_mfma_f32_16x16x32_bf16 v[160:163], a[132:135], a[176:179], v[160:163]\n"
    "buffer_load_dwordx4 a[200:203], v253, s[8:11], s61 offen offset:2048 sc1\n"
    "v_mfma_f32_16x16x32_bf16 v[164:167], a[72:75], a[176:179], v[164:167]\n"
    "v_add_u32_e32 v251, s52, v252\n"
    "v_mfma_f32_16x16x32_bf16 v[168:171], a[104:107], a[176:179], v[168:171]\n"
    "v_mfma_f32_16x16x32_bf16 v[172:175], a[136:139], a[176:179], v[172:175]\n"
    "ds_read_b128 a[176:179], v241 offset:26624\n"
    "v_mfma_f32_16x16x32_bf16 v[176:179], a[132:135], a[180:183], v[176:179]\n"
    "v_mfma_f32_16x16x32_bf16 v[180:183], a[72:75], a[180:183], v[180:183]\n"
    "buffer_load_dwordx4 a[204:207], v253, s[8:11], s61 offen offset:3072 sc1\n"
    "v_mfma_f32_16x16x32_bf16 v[184:187], a[104:107], a[180:183], v[184:187]\n"
    "s_add_u32 s61, s46, 24832\n"
    "v_add_u32_e32 v158, s59, v252\n"
    "v_mfma_f32_16x16x32_bf16 v[188:191], a[136:139], a[180:183], v[188:191]\n"
    "s_waitcnt lgkmcnt(7)\n"
    "ds_read_b128 a[180:183], v240 offset:11264\n"
    "v_mfma_f32_16x16x32_bf16 v[192:195], a[132:135], a[184:187], v[192:195]\n"
    "v_mfma_f32_16x16x32_bf16 v[196:199], a[72:75], a[184:187], v[196:199]\n"
    "v_mfma_f32_16x16x32_bf16 v[200:203], a[104:107], a[184:187], v[200:203]\n"
    "buffer_load_dwordx4 a[208:211], v253, s[8:11], s61 offen offset:0 sc1\n"
    "v_mfma_f32_16x16x32_bf16 v[204:207], a[136:139], a[184:187], v[204:207]\n"
    "ds_read_b128 a[184:187], v240 offset:19456\n"
    "v_mfma_f32_16x16x32_bf16 v[160:163], a[76:79], a[188:191], v[160:163]\n"
    "v_and_b32_e32 v145, 12, v145\n"
    "v_mfma_f32_16x16x32_bf16 v[164:167], a[108:111], a[188:191], v[164:167]\n"
    "v_mfma_f32_16x16x32_bf16 v[168:171], a[140:143], a[188:191], v[168:171]\n"
    "v_mfma_f32_16x16x32_bf16 v[172:175], a[80:83], a[188:191], v[172:175]\n"
    "s_waitcnt lgkmcnt(5)\n"
    "ds_read_b128 a[188:191], v240 offset:27648\n"
    "v_mfma_f32_16x16x32_bf16 v[176:179], a[76:79], a[160:163], v[176:179]\n"
    "buffer_load_dwordx4 a[212:215], v253, s[8:11], s61 offen offset:1024 sc1\n"
    "v_mfma_f32_16x16x32_bf16 v[180:183], a[108:111], a[160:163], v[180:183]\n"
    "s_add_u32 s52, s32, 3\n"
    "v_mfma_f32_16x16x32_bf16 v[184:187], a[140:143], a[160:163], v[184:187]\n"
    "v_mfma_f32_16x16x32_bf16 v[188:191], a[80:83], a[160:163], v[188:191]\n"
    "v_mfma_f32_16x16x32_bf16 v[192:195], a[76:79], a[164:167], v[192:195]\n"
    "v_mfma_f32_16x16x32_bf16 v[196:199], a[108:111], a[164:167], v[196:199]\n"
    "buffer_load_dwordx4 a[216:219], v253, s[8:11], s61 offen offset:2048 sc1\n"
    "v_mfma_f32_16x16x32_bf16 v[200:203], a[140:143], a[164:167], v[200:203]\n"
    "s_and_b32 s52, s52, 3\n"
    "v_mfma_f32_16x16x32_bf16 v[204:207], a[80:83], a[164:167], v[204:207]\n"
    "s_waitcnt lgkmcnt(4)\n"
    "v_mfma_f32_16x16x32_bf16 v[160:163], a[112:115], a[168:171], v[160:163]\n"
    "v_mfma_f32_16x16x32_bf16 v[164:167], a[144:147], a[168:171], v[164:167]\n"
    "v_mfma_f32_16x16x32_bf16 v[168:171], a[84:87], a[168:171], v[168:171]\n"
    "buffer_load_dwordx4 a[220:223], v253, s[8:11], s61 offen offset:3072 sc1\n"
    "v_mfma_f32_16x16x32_bf16 v[172:175], a[116:119], a[168:171], v[172:175]\n"
    "s_add_u32 s61, s45, 24576\n"
    "v_mfma_f32_16x16x32_bf16 v[176:179], a[112:115], a[172:175], v[176:179]\n"
    "s_lshl_b32 s52, s52, 8\n"
    "v_mfma_f32_16x16x32_bf16 v[180:183], a[144:147], a[172:175], v[180:183]\n"
    "v_mfma_f32_16x16x32_bf16 v[184:187], a[84:87], a[172:175], v[184:187]\n"
    "v_mfma_f32_16x16x32_bf16 v[188:191], a[116:119], a[172:175], v[188:191]\n"
    "s_waitcnt lgkmcnt(2)\n"
    "buffer_load_dwordx3 v[236:238], v147, s[8:11], s61 offen offset:0 sc1\n"
    "v_mfma_f32_16x16x32_bf16 v[192:195], a[112:115], a[176:179], v[192:195]\n"
    "s_add_u32 s61, s45, 49408\n"
    "v_mfma_f32_16x16x32_bf16 v[196:199], a[144:147], a[176:179], v[196:199]\n"
    "s_add_u32 s52, s52, s47\n"
    "v_mfma_f32_16x16x32_bf16 v[200:203], a[84:87], a[176:179], v[200:203]\n"
    "v_mfma_f32_16x16x32_bf16 v[204:207], a[116:119], a[176:179], v[204:207]\n"
    "v_mfma_f32_16x16x32_bf16 v[160:163], a[148:151], a[180:183], v[160:163]\n"
    "buffer_load_dwordx3 v[248:250], v147, s[8:11], s61 offen offset:0 sc1\n"
    "v_mfma_f32_16x16x32_bf16 v[164:167], a[88:91], a[180:183], v[164:167]\n"
    "v_mfma_f32_16x16x32_bf16 v[168:171], a[120:123], a[180:183], v[168:171]\n"
    "v_mfma_f32_16x16x32_bf16 v[172:175], a[152:155], a[180:183], v[172:175]\n"
    "s_waitcnt lgkmcnt(0)\n"
    "v_mfma_f32_16x16x32_bf16 v[176:179], a[148:151], a[184:187], v[176:179]\n"
    "v_mfma_f32_16x16x32_bf16 v[180:183], a[88:91], a[184:187], v[180:183]\n"
    "v_mfma_f32_16x16x32_bf16 v[184:187], a[120:123], a[184:187], v[184:187]\n"
    "v_mfma_f32_16x16x32_bf16 v[188:191], a[152:155], a[184:187], v[188:191]\n"
    "v_mfma_f32_16x16x32_bf16 v[192:195], a[148:151], a[188:191], v[192:195]\n"
    "v_mfma_f32_16x16x32_bf16 v[196:199], a[88:91], a[188:191], v[196:199]\n"
    "v_mfma_f32_16x16x32_bf16 v[200:203], a[120:123], a[188:191], v[200:203]\n"
    "v_mfma_f32_16x16x32_bf16 v[204:207], a[152:155], a[188:191], v[204:207]\n"
    "v_mfma_f32_16x16x32_bf16 v[128:131], v[154:157], a[248:251], 0\n"
    "v_mfma_f32_16x16x32_bf16 v[132:135], v[152:155], a[248:251], 0\n"
    "v_mfma_f32_16x16x32_bf16 v[136:139], v[154:157], a[252:255], 0\n"
    "v_mfma_f32_16x16x32_bf16 v[140:143], v[152:155], a[252:255], 0\n"
    "s_waitcnt vmcnt(15)\n"
    "v_mfma_f32_16x16x32_bf16 v[128:131], v[154:157], a[224:227], v[128:131]\n"
    "v_mfma_f32_16x16x32_bf16 v[132:135], v[152:155], a[224:227], v[132:135]\n"
    "v_mfma_f32_16x16x32_bf16 v[136:139], v[154:157], a[228:231], v[136:139]\n"
    "buffer_load_dword v144, v145, s[8:11], s52 offen offset:0 sc1\n"
    "v_mfma_f32_16x16x32_bf16 v[140:143], v[152:155], a[228:231], v[140:143]\n"
    "s_waitcnt vmcnt(14)\n"
    "v_mfma_f32_16x16x32_bf16 v[128:131], v[154:157], a[232:235], v[128:131]\n"
    "v_mfma_f32_16x16x32_bf16 v[132:135], v[152:155], a[232:235], v[132:135]\n"
    "v_mfma_f32_16x16x32_bf16 v[136:139], v[154:157], a[236:239], v[136:139]\n"
    "v_mfma_f32_16x16x32_bf16 v[140:143], v[152:155], a[236:239], v[140:143]\n"
    "s_waitcnt vmcnt(12)\n"
    "v_mfma_f32_16x16x32_bf16 v[128:131], v[154:157], a[240:243], v[128:131]\n"
    "v_mfma_f32_16x16x32_bf16 v[132:135], v[152:155], a[240:243], v[132:135]\n"
    "v_mfma_f32_16x16x32_bf16 v[136:139], v[154:157], a[244:247], v[136:139]\n"
    "v_mfma_f32_16x16x32_bf16 v[140:143], v[152:155], a[244:247], v[140:143]\n"
    "s_waitcnt vmcnt(9)\n"
    "v_mfma_f32_16x16x32_bf16 v[0:3], v[154:157], a[192:195], v[0:3]\n"
    "v_cvt_pk_bf16_f32 v128, v128, v129\n"
    "v_mfma_f32_16x16x32_bf16 v[4:7], v[152:155], a[192:195], v[4:7]\n"
    "v_cvt_pk_bf16_f32 v129, v130, v131\n"
    "v_mfma_f32_16x16x32_bf16 v[8:11], v[154:157], a[196:199], v[8:11]\n"
    "v_cvt_pk_bf16_f32 v130, v132, v133\n"
    "v_mfma_f32_16x16x32_bf16 v[12:15], v[152:155], a[196:199], v[12:15]\n"
    "s_waitcnt vmcnt(7)\n"
    "v_cvt_pk_bf16_f32 v131, v134, v135\n"
    "v_mfma_f32_16x16x32_bf16 v[16:19], v[154:157], a[200:203], v[16:19]\n"
    "ds_write_b128 v251, v[128:131] offset:49152\n"
    "v_mfma_f32_16x16x32_bf16 v[20:23], v[152:155], a[200:203], v[20:23]\n"
    "v_cvt_pk_bf16_f32 v136, v136, v137\n"
    "v_mfma_f32_16x16x32_bf16 v[24:27], v[154:157], a[204:207], v[24:27]\n"
    "v_cvt_pk_bf16_f32 v137, v138, v139\n"
    "v_mfma_f32_16x16x32_bf16 v[28:31], v[152:155], a[204:207], v[28:31]\n"
    "s_waitcnt vmcnt(5)\n"
    "v_cvt_pk_bf16_f32 v138, v140, v141\n"
    "v_cvt_pk_bf16_f32 v139, v142, v143\n"
    "v_mfma_f32_16x16x32_bf16 v[0:3], v[154:157], a[208:211], v[0:3]\n"
    "ds_write_b128 v251, v[136:139] offset:50176\n"
    "v_mfma_f32_16x16x32_bf16 v[4:7], v[152:155], a[208:211], v[4:7]\n"
    "v_mfma_f32_16x16x32_bf16 v[8:11], v[154:157], a[212:215], v[8:11]\n"
    "s_waitcnt lgkmcnt(0)\n"
    "s_barrier\n"
    "ds_read_b128 a[128:131], v252 offset:49152\n"
    "ds_read_b128 a[132:135], v252 offset:50176\n"
    "ds_read_b128 a[136:139], v252 offset:51200\n"
    "v_mfma_f32_16x16x32_bf16 v[12:15], v[152:155], a[212:215], v[12:15]\n"
    "s_waitcnt vmcnt(3)\n"
    "v_mfma_f32_16x16x32_bf16 v[16:19], v[154:157], a[216:219], v[16:19]\n"
    "ds_read_b128 a[140:143], v252 offset:52224\n"
    "v_mfma_f32_16x16x32_bf16 v[20:23], v[152:155], a[216:219], v[20:23]\n"
    "v_mfma_f32_16x16x32_bf16 v[24:27], v[154:157], a[220:223], v[24:27]\n"
    "ds_read_b128 a[144:147], v252 offset:53248\n"
    "v_mfma_f32_16x16x32_bf16 v[28:31], v[152:155], a[220:223], v[28:31]\n"
    "s_waitcnt lgkmcnt(3)\n"
    "v_mfma_f32_16x16x32_bf16 v[216:219], v[56:59], a[128:131], 0\n"
    "ds_read_b128 a[148:151], v252 offset:54272\n"
    "v_mfma_f32_16x16x32_bf16 v[220:223], v[60:63], a[128:131], 0\n"
    "ds_read_b128 a[152:155], v252 offset:55296\n"
    "v_mfma_f32_16x16x32_bf16 v[224:227], v[64:67], a[128:131], 0\n"
    "s_waitcnt vmcnt(0)\n"
    "v_cmp_ne_u32_e32 vcc, s39, v144\n"
    "s_nop 3\n"
    "s_cbranch_vccnz .Lat_ypoll\n"
    ".Lat_ypoll_ret:\n"
    "s_add_u32 s61, s46, 49664\n"
    "buffer_load_dwordx4 a[160:163], v253, s[8:11], s61 offen offset:0 sc1\n"
    "buffer_load_dwordx4 a[164:167], v253, s[8:11], s61 offen offset:1024 sc1\n"
    "buffer_load_dwordx4 a[168:171], v253, s[8:11], s61 offen offset:2048 sc1\n"
    "buffer_load_dwordx4 a[172:175], v253, s[8:11], s61 offen offset:3072 sc1\n"
    "s_add_u32 s61, s45, 74240\n"
    "buffer_load_dwordx3 v[244:246], v147, s[8:11], s61 offen offset:0 sc1\n"
    "v_mfma_f32_16x16x32_bf16 v[228:231], v[68:71], a[128:131], 0\n"
    "ds_read_b128 a[156:159], v252 offset:56320\n"
    "v_mfma_f32_16x16x32_bf16 v[216:219], v[72:75], a[132:135], v[216:219]\n"
    "v_mfma_f32_16x16x32_bf16 v[220:223], v[76:79], a[132:135], v[220:223]\n"
    "s_waitcnt vmcnt(6)\n"
    "v_mfma_f32_16x16x32_bf16 v[224:227], v[80:83], a[132:135], v[224:227]\n"
    "v_mfma_f32_16x16x32_bf16 v[228:231], v[84:87], a[132:135], v[228:231]\n"
    "s_waitcnt lgkmcnt(4)\n"
    "v_mfma_f32_16x16x32_bf16 v[216:219], v[88:91], a[136:139], v[216:219]\n"
    "v_mfma_f32_16x16x32_bf16 v[220:223], v[92:95], a[136:139], v[220:223]\n"
    "v_add_f32_e32 v232, v232, v236\n"
    "v_mfma_f32_16x16x32_bf16 v[224:227], v[96:99], a[136:139], v[224:227]\n"
    "v_mfma_f32_16x16x32_bf16 v[228:231], v[100:103], a[136:139], v[228:231]\n"
    "v_mfma_f32_16x16x32_bf16 v[216:219], v[104:107], a[140:143], v[216:219]\n"
    "v_mfma_f32_16x16x32_bf16 v[220:223], v[108:111], a[140:143], v[220:223]\n"
    "v_add_f32_e32 v233, v233, v237\n"
    "v_mfma_f32_16x16x32_bf16 v[224:227], v[112:115], a[140:143], v[224:227]\n"
    "v_mfma_f32_16x16x32_bf16 v[228:231], v[116:119], a[140:143], v[228:231]\n"
    "s_waitcnt lgkmcnt(2)\n"
    "v_mfma_f32_16x16x32_bf16 v[216:219], a[28:31], a[144:147], v[216:219]\n"
    "v_mfma_f32_16x16x32_bf16 v[220:223], a[60:63], a[144:147], v[220:223]\n"
    "v_add_f32_e32 v234, v234, v238\n"
    "v_mfma_f32_16x16x32_bf16 v[224:227], a[0:3], a[144:147], v[224:227]\n"
    "v_mfma_f32_16x16x32_bf16 v[228:231], a[32:35], a[144:147], v[228:231]\n"
    "v_mfma_f32_16x16x32_bf16 v[216:219], a[4:7], a[148:151], v[216:219]\n"
    "v_mfma_f32_16x16x32_bf16 v[220:223], a[36:39], a[148:151], v[220:223]\n"
    "v_add_f32_e32 v232, v232, v248\n"
    "v_mfma_f32_16x16x32_bf16 v[224:227], a[8:11], a[148:151], v[224:227]\n"
    "v_mfma_f32_16x16x32_bf16 v[228:231], a[40:43], a[148:151], v[228:231]\n"
    "s_waitcnt lgkmcnt(0)\n"
    "v_mfma_f32_16x16x32_bf16 v[216:219], a[12:15], a[152:155], v[216:219]\n"
    "v_mfma_f32_16x16x32_bf16 v[220:223], a[44:47], a[152:155], v[220:223]\n"
    "v_add_f32_e32 v233, v233, v249\n"
    "v_mfma_f32_16x16x32_bf16 v[224:227], a[16:19], a[152:155], v[224:227]\n"
    "v_mfma_f32_16x16x32_bf16 v[228:231], a[48:51], a[152:155], v[228:231]\n"
    "v_mfma_f32_16x16x32_bf16 v[216:219], a[20:23], a[156:159], v[216:219]\n"
    "v_mfma_f32_16x16x32_bf16 v[220:223], a[52:55], a[156:159], v[220:223]\n"
    "v_add_f32_e32 v234, v234, v250\n"
    "v_mfma_f32_16x16x32_bf16 v[224:227], a[24:27], a[156:159], v[224:227]\n"
    "v_mfma_f32_16x16x32_bf16 v[228:231], a[56:59], a[156:159], v[228:231]\n"
    "s_waitcnt vmcnt(4)\n"
    "v_mfma_f32_16x16x32_bf16 v[0:3], v[154:157], a[160:163], v[0:3]\n"
    "v_mfma_f32_16x16x32_bf16 v[4:7], v[152:155], a[160:163], v[4:7]\n"
    "s_waitcnt vmcnt(3)\n"
    "v_mfma_f32_16x16x32_bf16 v[8:11], v[154:157], a[164:167], v[8:11]\n"
    "v_mfma_f32_16x16x32_bf16 v[12:15], v[152:155], a[164:167], v[12:15]\n"
    "s_waitcnt vmcnt(2)\n"
    "v_cvt_pk_bf16_f32 v0, v0, v1\n"
    "v_cvt_pk_bf16_f32 v1, v2, v3\n"
    "v_mfma_f32_16x16x32_bf16 v[16:19], v[154:157], a[168:171], v[16:19]\n"
    "v_cvt_pk_bf16_f32 v2, v4, v5\n"
    "v_cvt_pk_bf16_f32 v3, v6, v7\n"
    "v_mfma_f32_16x16x32_bf16 v[20:23], v[152:155], a[168:171], v[20:23]\n"
    "s_waitcnt vmcnt(1)\n"
    "ds_write_b128 v158, v[0:3] offset:32768\n"
    "v_mfma_f32_16x16x32_bf16 v[24:27], v[154:157], a[172:175], v[24:27]\n"
    "v_cvt_pk_bf16_f32 v8, v8, v9\n"
    "v_cvt_pk_bf16_f32 v9, v10, v11\n"
    "v_cvt_pk_bf16_f32 v10, v12, v13\n"
    "v_cvt_pk_bf16_f32 v11, v14, v15\n"
    "v_mfma_f32_16x16x32_bf16 v[28:31], v[152:155], a[172:175], v[28:31]\n"
    "ds_write_b128 v158, v[8:11] offset:33792\n"
    "v_cvt_pk_bf16_f32 v16, v16, v17\n"
    "v_cvt_pk_bf16_f32 v17, v18, v19\n"
    "v_cvt_pk_bf16_f32 v18, v20, v21\n"
    "v_cvt_pk_bf16_f32 v19, v22, v23\n"
    "ds_write_b128 v158, v[16:19] offset:34816\n"
    "v_cvt_pk_bf16_f32 v24, v24, v25\n"
    "v_cvt_pk_bf16_f32 v25, v26, v27\n"
    "v_cvt_pk_bf16_f32 v26, v28, v29\n"
    "v_cvt_pk_bf16_f32 v27, v30, v31\n"
    "ds_write_b128 v158, v[24:27] offset:35840\n"
    "s_waitcnt lgkmcnt(0)\n"
    "s_barrier\n"
    "ds_read_b128 a[96:99], v252 offset:40960\n"
    "ds_read_b128 a[100:103], v252 offset:41984\n"
    "ds_read_b128 a[104:107], v252 offset:43008\n"
    "ds_read_b128 a[108:111], v252 offset:44032\n"
    "ds_read_b128 a[112:115], v252 offset:45056\n"
    "ds_read_b128 a[116:119], v252 offset:46080\n"
    "ds_read_b128 a[120:123], v252 offset:47104\n"
    "ds_read_b128 a[124:127], v252 offset:48128\n"
    "s_waitcnt lgkmcnt(6)\n"
    "v_mfma_f32_16x16x32_bf16 v[120:123], v[56:59], a[96:99], 0\n"
    "s_waitcnt vmcnt(0)\n"
    "v_add_f32_e32 v234, v234, v246\n"
    "v_mfma_f32_16x16x32_bf16 v[124:127], v[60:63], a[96:99], 0\n"
    "ds_read_b128 a[64:67], v252 offset:32768\n"
    "v_mfma_f32_16x16x32_bf16 v[128:131], v[64:67], a[96:99], 0\n"
    "v_add_f32_e32 v233, v233, v245\n"
    "v_add_f32_e32 v232, v232, v244\n"
    "v_mfma_f32_16x16x32_bf16 v[132:135], v[68:71], a[96:99], 0\n"
    "v_rcp_f32_e32 v242, v234\n"
    "v_mfma_f32_16x16x32_bf16 v[120:123], v[72:75], a[100:103], v[120:123]\n"
    "s_add_u32 s48, s55, 16384\n"
    "ds_read_b128 a[68:71], v252 offset:33792\n"
    "v_mfma_f32_16x16x32_bf16 v[124:127], v[76:79], a[100:103], v[124:127]\n"
    "v_fma_f32 v216, v216, v242, v192\n"
    "v_fma_f32 v217, v217, v242, v193\n"
    "v_mfma_f32_16x16x32_bf16 v[128:131], v[80:83], a[100:103], v[128:131]\n"
    "ds_read_b128 a[72:75], v252 offset:34816\n"
    "v_fma_f32 v218, v218, v242, v194\n"
    "v_mfma_f32_16x16x32_bf16 v[132:135], v[84:87], a[100:103], v[132:135]\n"
    "s_waitcnt lgkmcnt(7)\n"
    "v_fma_f32 v219, v219, v242, v195\n"
    "v_fma_f32 v220, v220, v242, v196\n"
    "v_mfma_f32_16x16x32_bf16 v[120:123], v[88:91], a[104:107], v[120:123]\n"
    "v_fma_f32 v221, v221, v242, v197\n"
    "v_mfma_f32_16x16x32_bf16 v[124:127], v[92:95], a[104:107], v[124:127]\n"
    "ds_read_b128 a[76:79], v252 offset:35840\n"
    "v_fma_f32 v222, v222, v242, v198\n"
    "v_mfma_f32_16x16x32_bf16 v[128:131], v[96:99], a[104:107], v[128:131]\n"
    "v_fma_f32 v223, v223, v242, v199\n"
    "v_cvt_pk_bf16_f32 v216, v216, v217\n"
    "v_mfma_f32_16x16x32_bf16 v[132:135], v[100:103], a[104:107], v[132:135]\n"
    "ds_read_b128 a[80:83], v252 offset:36864\n"
    "v_cvt_pk_bf16_f32 v217, v218, v219\n"
    "v_mfma_f32_16x16x32_bf16 v[120:123], v[104:107], a[108:111], v[120:123]\n"
    "v_cvt_pk_bf16_f32 v218, v220, v221\n"
    "v_cvt_pk_bf16_f32 v219, v222, v223\n"
    "v_mfma_f32_16x16x32_bf16 v[124:127], v[108:111], a[108:111], v[124:127]\n"
    "buffer_store_dwordx4 v[216:219], v151, s[16:19], s48 offen offset:0 sc0 sc1\n"
    "v_fma_f32 v224, v224, v242, v200\n"
    "v_mfma_f32_16x16x32_bf16 v[128:131], v[112:115], a[108:111], v[128:131]\n"
    "ds_read_b128 a[84:87], v252 offset:37888\n"
    "v_fma_f32 v225, v225, v242, v201\n"
    "v_mfma_f32_16x16x32_bf16 v[132:135], v[116:119], a[108:111], v[132:135]\n"
    "s_waitcnt lgkmcnt(8)\n"
    "v_fma_f32 v226, v226, v242, v202\n"
    "v_fma_f32 v227, v227, v242, v203\n"
    "v_mfma_f32_16x16x32_bf16 v[120:123], a[28:31], a[112:115], v[120:123]\n"
    "ds_read_b128 a[88:91], v252 offset:38912\n"
    "v_fma_f32 v228, v228, v242, v204\n"
    "v_mfma_f32_16x16x32_bf16 v[124:127], a[60:63], a[112:115], v[124:127]\n"
    "v_fma_f32 v229, v229, v242, v205\n"
    "v_fma_f32 v230, v230, v242, v206\n"
    "v_mfma_f32_16x16x32_bf16 v[128:131], a[0:3], a[112:115], v[128:131]\n"
    "v_fma_f32 v231, v231, v242, v207\n"
    "v_cvt_pk_bf16_f32 v224, v224, v225\n"
    "v_mfma_f32_16x16x32_bf16 v[132:135], a[32:35], a[112:115], v[132:135]\n"
    "ds_read_b128 a[92:95], v252 offset:39936\n"
    "v_cvt_pk_bf16_f32 v225, v226, v227\n"
    "v_mfma_f32_16x16x32_bf16 v[120:123], a[4:7], a[116:119], v[120:123]\n"
    "v_cvt_pk_bf16_f32 v226, v228, v229\n"
    "v_cvt_pk_bf16_f32 v227, v230, v231\n"
    "v_mfma_f32_16x16x32_bf16 v[124:127], a[36:39], a[116:119], v[124:127]\n"
    "buffer_store_dwordx4 v[224:227], v151, s[16:19], s48 offen offset:64 sc0 sc1\n"
    "v_mfma_f32_16x16x32_bf16 v[128:131], a[8:11], a[116:119], v[128:131]\n"
    "s_bitcmp1_b32 %1, 0\n"
    "s_cselect_b64 vcc, -1, 0\n"
    "v_mfma_f32_16x16x32_bf16 v[132:135], a[40:43], a[116:119], v[132:135]\n"
    "s_waitcnt lgkmcnt(8)\n"
    "v_cndmask_b32_e32 v234, v233, v234, vcc\n"
    "v_mfma_f32_16x16x32_bf16 v[120:123], a[12:15], a[120:123], v[120:123]\n"
    "s_mov_b32 s61, 0x12800000\n"
    "v_rcp_f32_e32 v241, v233\n"
    "v_mfma_f32_16x16x32_bf16 v[124:127], a[44:47], a[120:123], v[124:127]\n"
    "v_rcp_f32_e32 v240, v232\n"
    "v_mfma_f32_16x16x32_bf16 v[128:131], a[16:19], a[120:123], v[128:131]\n"
    "v_subrev_u32_e32 v236, s61, v232\n"
    "v_mfma_f32_16x16x32_bf16 v[132:135], a[48:51], a[120:123], v[132:135]\n"
    "v_subrev_u32_e32 v237, s61, v233\n"
    "v_mfma_f32_16x16x32_bf16 v[120:123], a[20:23], a[124:127], v[120:123]\n"
    "v_subrev_u32_e32 v238, s61, v234\n"
    "v_max3_u32 v236, v236, v237, v238\n"
    "v_mfma_f32_16x16x32_bf16 v[124:127], a[52:55], a[124:127], v[124:127]\n"
    "buffer_store_dword v148, v149, s[8:11], s88 offen offset:0 sc1\n"
    "v_cmp_le_u32_e32 vcc, 0x5f000000, v236\n"
    "v_mfma_f32_16x16x32_bf16 v[128:131], a[24:27], a[124:127], v[128:131]\n"
    "v_mfma_f32_16x16x32_bf16 v[132:135], a[56:59], a[124:127], v[132:135]\n"
    "s_waitcnt lgkmcnt(0)\n"
    "v_mfma_f32_16x16x32_bf16 v[32:35], v[56:59], a[64:67], 0\n"
    "s_cbranch_vccnz .Lat_exit\n"
    "v_mfma_f32_16x16x32_bf16 v[36:39], v[60:63], a[64:67], 0\n"
    "s_add_u32 s48, s58, 8192\n"
    "v_fma_f32 v120, v120, v241, v176\n"
    "v_mfma_f32_16x16x32_bf16 v[32:35], v[72:75], a[68:71], v[32:35]\n"
    "v_fma_f32 v121, v121, v241, v177\n"
    "v_mfma_f32_16x16x32_bf16 v[36:39], v[76:79], a[68:71], v[36:39]\n"
    "v_fma_f32 v122, v122, v241, v178\n"
    "v_mfma_f32_16x16x32_bf16 v[32:35], v[88:91], a[72:75], v[32:35]\n"
    "v_fma_f32 v123, v123, v241, v179\n"
    "v_mfma_f32_16x16x32_bf16 v[36:39], v[92:95], a[72:75], v[36:39]\n"
    "v_fma_f32 v124, v124, v241, v180\n"
    "v_mfma_f32_16x16x32_bf16 v[32:35], v[104:107], a[76:79], v[32:35]\n"
    "v_fma_f32 v125, v125, v241, v181\n"
    "v_mfma_f32_16x16x32_bf16 v[36:39], v[108:111], a[76:79], v[36:39]\n"
    "v_fma_f32 v126, v126, v241, v182\n"
    "v_fma_f32 v127, v127, v241, v183\n"
    "v_mfma_f32_16x16x32_bf16 v[32:35], a[28:31], a[80:83], v[32:35]\n"
    "v_cvt_pk_bf16_f32 v120, v120, v121\n"
    "v_mfma_f32_16x16x32_bf16 v[36:39], a[60:63], a[80:83], v[36:39]\n"
    "v_cvt_pk_bf16_f32 v121, v122, v123\n"
    "v_mfma_f32_16x16x32_bf16 v[32:35], a[4:7], a[84:87], v[32:35]\n"
    "v_cvt_pk_bf16_f32 v122, v124, v125\n"
    "v_mfma_f32_16x16x32_bf16 v[36:39], a[36:39], a[84:87], v[36:39]\n"
    "v_cvt_pk_bf16_f32 v123, v126, v127\n"
    "v_mfma_f32_16x16x32_bf16 v[32:35], a[12:15], a[88:91], v[32:35]\n"
    "buffer_store_dwordx4 v[120:123], v151, s[16:19], s48 offen offset:0 sc0 sc1\n"
    "v_fma_f32 v128, v128, v241, v184\n"
    "v_mfma_f32_16x16x32_bf16 v[36:39], a[44:47], a[88:91], v[36:39]\n"
    "v_fma_f32 v129, v129, v241, v185\n"
    "v_fma_f32 v130, v130, v241, v186\n"
    "v_mfma_f32_16x16x32_bf16 v[32:35], a[20:23], a[92:95], v[32:35]\n"
    "v_fma_f32 v131, v131, v241, v187\n"
    "v_mfma_f32_16x16x32_bf16 v[36:39], a[52:55], a[92:95], v[36:39]\n"
    "v_fma_f32 v132, v132, v241, v188\n"
    "v_mfma_f32_16x16x32_bf16 v[40:43], v[64:67], a[64:67], 0\n"
    "v_fma_f32 v133, v133, v241, v189\n"
    "v_mfma_f32_16x16x32_bf16 v[44:47], v[68:71], a[64:67], 0\n"
    "v_fma_f32 v134, v134, v241, v190\n"
    "v_fma_f32 v135, v135, v241, v191\n"
    "v_mfma_f32_16x16x32_bf16 v[40:43], v[80:83], a[68:71], v[40:43]\n"
    "v_cvt_pk_bf16_f32 v128, v128, v129\n"
    "v_mfma_f32_16x16x32_bf16 v[44:47], v[84:87], a[68:71], v[44:47]\n"
    "v_cvt_pk_bf16_f32 v129, v130, v131\n"
    "v_mfma_f32_16x16x32_bf16 v[40:43], v[96:99], a[72:75], v[40:43]\n"
    "v_cvt_pk_bf16_f32 v130, v132, v133\n"
    "v_cvt_pk_bf16_f32 v131, v134, v135\n"
    "v_mfma_f32_16x16x32_bf16 v[44:47], v[100:103], a[72:75], v[44:47]\n"
    "buffer_store_dwordx4 v[128:131], v151, s[16:19], s48 offen offset:64 sc0 sc1\n"
    "v_mfma_f32_16x16x32_bf16 v[40:43], v[112:115], a[76:79], v[40:43]\n"
    "s_add_u32 s48, s58, 0\n"
    "v_fma_f32 v32, v32, v240, v160\n"
    "v_mfma_f32_16x16x32_bf16 v[44:47], v[116:119], a[76:79], v[44:47]\n"
    "v_fma_f32 v33, v33, v240, v161\n"
    "v_mfma_f32_16x16x32_bf16 v[40:43], a[0:3], a[80:83], v[40:43]\n"
    "v_fma_f32 v34, v34, v240, v162\n"
    "v_mfma_f32_16x16x32_bf16 v[44:47], a[32:35], a[80:83], v[44:47]\n"
    "v_fma_f32 v35, v35, v240, v163\n"
    "v_mfma_f32_16x16x32_bf16 v[40:43], a[8:11], a[84:87], v[40:43]\n"
    "v_fma_f32 v36, v36, v240, v164\n"
    "v_fma_f32 v37, v37, v240, v165\n"
    "v_mfma_f32_16x16x32_bf16 v[44:47], a[40:43], a[84:87], v[44:47]\n"
    "v_fma_f32 v38, v38, v240, v166\n"
    "v_mfma_f32_16x16x32_bf16 v[40:43], a[16:19], a[88:91], v[40:43]\n"
    "v_fma_f32 v39, v39, v240, v167\n"
    "v_mfma_f32_16x16x32_bf16 v[44:47], a[48:51], a[88:91], v[44:47]\n"
    "v_cvt_pk_bf16_f32 v32, v32, v33\n"
    "v_cvt_pk_bf16_f32 v33, v34, v35\n"
    "v_mfma_f32_16x16x32_bf16 v[40:43], a[24:27], a[92:95], v[40:43]\n"
    "v_cvt_pk_bf16_f32 v34, v36, v37\n"
    "v_cvt_pk_bf16_f32 v35, v38, v39\n"
    "v_mfma_f32_16x16x32_bf16 v[44:47], a[56:59], a[92:95], v[44:47]\n"
    "buffer_store_dwordx4 v[32:35], v151, s[16:19], s48 offen offset:0 sc0 sc1\n"
    "s_nop 2\n"
    "v_fma_f32 v40, v40, v240, v168\n"
    "v_fma_f32 v41, v41, v240, v169\n"
    "v_fma_f32 v42, v42, v240, v170\n"
    "v_fma_f32 v43, v43, v240, v171\n"
    "v_fma_f32 v44, v44, v240, v172\n"
    "v_fma_f32 v45, v45, v240, v173\n"
    "v_fma_f32 v46, v46, v240, v174\n"
    "v_fma_f32 v47, v47, v240, v175\n"
    "v_cvt_pk_bf16_f32 v40, v40, v41\n"
    "v_cvt_pk_bf16_f32 v41, v42, v43\n"
    "v_cvt_pk_bf16_f32 v42, v44, v45\n"
    "v_cvt_pk_bf16_f32 v43, v46, v47\n"
    "buffer_store_dwordx4 v[40:43], v151, s[16:19], s48 offen offset:64 sc0 sc1\n"
    "s_endpgm\n"
    ".Lat_two:\n"
    "s_add_u32 s46, s46, 81920\n"
    "ds_read_b128 a[160:163], v241 offset:11264\n"
    "v_accvgpr_read_b32 v223, a255\n"
    "v_add_u32_e32 v158, s46, v253\n"
    "v_mfma_f32_16x16x32_bf16 v[20:23], a[172:175], v[164:167], v[20:23]\n"
    "s_add_u32 s45, %3, 155840\n"
    "ds_read_b128 a[164:167], v241 offset:19456\n"
    "v_cvt_pk_bf16_f32 v216, v216, v217\n"
    "v_and_b32_e32 v150, 15, v147\n"
    "v_mfma_f32_16x16x32_bf16 v[20:23], a[168:171], v[160:163], v[20:23]\n"
    "s_waitcnt lgkmcnt(2)\n"
    "ds_read_b128 a[168:171], v240 offset:8192\n"
    "v_cvt_pk_bf16_f32 v217, v218, v219\n"
    "v_lshlrev_b32_e32 v151, 9, v150\n"
    "v_mfma_f32_16x16x32_bf16 v[24:27], a[176:179], v[160:163], v[24:27]\n"
    "ds_read_b128 a[172:175], v240 offset:16384\n"
    "buffer_load_dwordx4 a[56:59], v253, s[8:11], s48 offen offset:3072 sc1\n"
    "v_lshl_add_u32 v150, v150, 2, s45\n"
    "v_mfma_f32_16x16x32_bf16 v[24:27], a[180:183], v[164:167], v[24:27]\n"
    "s_lshl_b32 s46, %2, 7\n"
    "v_cvt_pk_bf16_f32 v218, v220, v221\n"
    "v_cvt_pk_bf16_f32 v219, v222, v223\n"
    "v_and_or_b32 v151, v147, 48, v151\n"
    "v_mfma_f32_16x16x32_bf16 v[48:51], a[180:183], v[172:175], v[48:51]\n"
    "buffer_store_dwordx4 v[216:219], v253, s[8:11], s83 offen offset:0 sc1\n"
    "v_cvt_pk_bf16_f32 v32, v32, v33\n"
    "v_cvt_pk_bf16_f32 v33, v34, v35\n"
    "v_mfma_f32_16x16x32_bf16 v[48:51], a[176:179], v[168:171], v[48:51]\n"
    "s_add_u32 s58, s35, s46\n"
    "ds_read_b128 a[176:179], v241 offset:8192\n"
    "v_cvt_pk_bf16_f32 v34, v36, v37\n"
    "v_mfma_f32_16x16x32_bf16 v[52:55], a[184:187], v[168:171], v[52:55]\n"
    "s_mul_i32 s45, %1, 74496\n"
    "ds_read_b128 a[180:183], v241 offset:16384\n"
    "v_cvt_pk_bf16_f32 v35, v38, v39\n"
    "v_mfma_f32_16x16x32_bf16 v[52:55], a[188:191], v[172:175], v[52:55]\n"
    "buffer_store_dwordx4 v[32:35], v253, s[8:11], s83 offen offset:1024 sc1\n"
    "v_cvt_pk_bf16_f32 v40, v40, v41\n"
    "v_cvt_pk_bf16_f32 v41, v42, v43\n"
    "v_mfma_f32_16x16x32_bf16 v[28:31], a[188:191], v[164:167], v[28:31]\n"
    "s_add_u32 s45, s45, 16265216\n"
    "v_cvt_pk_bf16_f32 v42, v44, v45\n"
    "v_cvt_pk_bf16_f32 v43, v46, v47\n"
    "v_mfma_f32_16x16x32_bf16 v[28:31], a[184:187], v[160:163], v[28:31]\n"
    "ds_read_b128 a[184:187], v240 offset:9216\n"
    "buffer_store_dwordx4 v[40:43], v253, s[8:11], s83 offen offset:2048 sc1\n"
    "v_cvt_pk_bf16_f32 v48, v48, v49\n"
    "v_mfma_f32_16x16x32_bf16 v[160:163], v[154:157], a[224:227], 0\n"
    "s_lshl_b32 s54, %2, 11\n"
    "ds_read_b128 a[188:191], v240 offset:17408\n"
    "v_cvt_pk_bf16_f32 v49, v50, v51\n"
    "v_mfma_f32_16x16x32_bf16 v[164:167], v[152:155], a[224:227], 0\n"
    "v_cvt_pk_bf16_f32 v50, v52, v53\n"
    "v_cvt_pk_bf16_f32 v51, v54, v55\n"
    "v_mfma_f32_16x16x32_bf16 v[168:171], v[154:157], a[228:231], 0\n"
    "buffer_store_dwordx4 v[48:51], v253, s[8:11], s83 offen offset:3072 sc1\n"
    "v_and_b32_e32 v147, 0xf0, v253\n"
    "v_mfma_f32_16x16x32_bf16 v[172:175], v[152:155], a[228:231], 0\n"
    "buffer_store_dword v55, v251, s[8:11], s60 offen offset:0 sc1\n"
    "v_add_u32_e32 v252, %3, v253\n"
    "v_mfma_f32_16x16x32_bf16 v[176:179], v[154:157], a[232:235], 0\n"
    "s_add_u32 s54, s54, s45\n"
    "s_add_u32 s54, s54, 16384\n"
    "s_lshl1_add_u32 s59, s50, s51\n"
    "v_mfma_f32_16x16x32_bf16 v[180:183], v[152:155], a[232:235], 0\n"
    "s_lshl_b32 s59, s59, 12\n"
    "s_add_u32 s46, s59, s45\n"
    "v_mfma_f32_16x16x32_bf16 v[184:187], v[154:157], a[236:239], 0\n"
    "s_bitcmp1_b32 %1, 0\n"
    "s_cselect_b32 s54, s54, 2.0\n"
    "s_cselect_b32 s55, s58, 2.0\n"
    "v_mfma_f32_16x16x32_bf16 v[188:191], v[152:155], a[236:239], 0\n"
    "s_waitcnt lgkmcnt(6)\n"
    "ds_write_b32 v255, v31 offset:0\n"
    "v_mfma_f32_16x16x32_bf16 v[160:163], a[192:195], a[160:163], v[160:163]\n"
    "s_waitcnt vmcnt(8)\n"
    "v_cmp_ne_u32_e32 vcc, s39, v146\n"
    "v_mfma_f32_16x16x32_bf16 v[164:167], a[196:199], a[160:163], v[164:167]\n"
    "s_add_u32 s61, s46, 0\n"
    "v_mfma_f32_16x16x32_bf16 v[168:171], a[200:203], a[160:163], v[168:171]\n"
    "s_cbranch_vccnz .Lat_two_xpoll\n"
    ".Lat_two_xpoll_ret:\n"
    "v_mfma_f32_16x16x32_bf16 v[172:175], a[204:207], a[160:163], v[172:175]\n"
    "ds_read_b128 a[160:163], v241 offset:9216\n"
    "v_mfma_f32_16x16x32_bf16 v[176:179], a[192:195], a[164:167], v[176:179]\n"
    "buffer_load_dwordx4 a[224:227], v253, s[8:11], s61 offen offset:0 sc1\n"
    "v_mfma_f32_16x16x32_bf16 v[180:183], a[196:199], a[164:167], v[180:183]\n"
    "v_mfma_f32_16x16x32_bf16 v[184:187], a[200:203], a[164:167], v[184:187]\n"
    "v_mfma_f32_16x16x32_bf16 v[188:191], a[204:207], a[164:167], v[188:191]\n"
    "s_waitcnt lgkmcnt(6)\n"
    "ds_read_b128 a[164:167], v241 offset:17408\n"
    "v_mfma_f32_16x16x32_bf16 v[160:163], a[208:211], a[168:171], v[160:163]\n"
    "buffer_load_dwordx4 a[228:231], v253, s[8:11], s61 offen offset:1024 sc1\n"
    "v_mfma_f32_16x16x32_bf16 v[164:167], a[212:215], a[168:171], v[164:167]\n"
    "v_mfma_f32_16x16x32_bf16 v[168:171], a[216:219], a[168:171], v[168:171]\n"
    "v_mfma_f32_16x16x32_bf16 v[172:175], a[220:223], a[168:171], v[172:175]\n"
    "ds_read_b128 a[168:171], v240 offset:10240\n"
    "buffer_load_dwordx4 a[232:235], v253, s[8:11], s61 offen offset:2048 sc1\n"
    "v_mfma_f32_16x16x32_bf16 v[176:179], a[208:211], a[172:175], v[176:179]\n"
    "s_waitcnt lgkmcnt(3)\n"
    "s_barrier\n"
    "v_mfma_f32_16x16x32_bf16 v[180:183], a[212:215], a[172:175], v[180:183]\n"
    "ds_read_b32 v232, v150 offset:0\n"
    "v_mfma_f32_16x16x32_bf16 v[184:187], a[216:219], a[172:175], v[184:187]\n"
    "buffer_load_dwordx4 a[236:239], v253, s[8:11], s61 offen offset:3072 sc1\n"
    "v_mfma_f32_16x16x32_bf16 v[188:191], a[220:223], a[172:175], v[188:191]\n"
    "s_add_u32 s61, s46, 24832\n"
    "ds_read_b128 a[172:175], v240 offset:18432\n"
    "v_mfma_f32_16x16x32_bf16 v[160:163], a[92:95], a[176:179], v[160:163]\n"
    "ds_read_b32 v233, v150 offset:256\n"
    "v_mfma_f32_16x16x32_bf16 v[164:167], a[124:127], a[176:179], v[164:167]\n"
    "buffer_load_dwordx4 a[240:243], v253, s[8:11], s61 offen offset:0 sc1\n"
    "v_mfma_f32_16x16x32_bf16 v[168:171], a[156:159], a[176:179], v[168:171]\n"
    "ds_read_b32 v234, v150 offset:512\n"
    "v_mfma_f32_16x16x32_bf16 v[172:175], a[64:67], a[176:179], v[172:175]\n"
    "ds_read_b128 a[176:179], v241 offset:10240\n"
    "v_mfma_f32_16x16x32_bf16 v[176:179], a[92:95], a[180:183], v[176:179]\n"
    "buffer_load_dwordx4 a[244:247], v253, s[8:11], s61 offen offset:1024 sc1\n"
    "v_mfma_f32_16x16x32_bf16 v[180:183], a[124:127], a[180:183], v[180:183]\n"
    "s_lshl_b32 s52, %2, 11\n"
    "v_mfma_f32_16x16x32_bf16 v[184:187], a[156:159], a[180:183], v[184:187]\n"
    "v_mfma_f32_16x16x32_bf16 v[188:191], a[64:67], a[180:183], v[188:191]\n"
    "ds_read_b128 a[180:183], v241 offset:18432\n"
    "v_mfma_f32_16x16x32_bf16 v[160:163], a[96:99], a[184:187], v[160:163]\n"
    "buffer_load_dwordx4 a[248:251], v253, s[8:11], s61 offen offset:2048 sc1\n"
    "v_mfma_f32_16x16x32_bf16 v[164:167], a[128:131], a[184:187], v[164:167]\n"
    "v_add_u32_e32 v251, s52, v252\n"
    "v_mfma_f32_16x16x32_bf16 v[168:171], a[68:71], a[184:187], v[168:171]\n"
    "v_add_u32_e32 v158, s59, v252\n"
    "v_mfma_f32_16x16x32_bf16 v[172:175], a[100:103], a[184:187], v[172:175]\n"
    "ds_read_b128 a[184:187], v240 offset:11264\n"
    "buffer_load_dwordx4 a[252:255], v253, s[8:11], s61 offen offset:3072 sc1\n"
    "v_mfma_f32_16x16x32_bf16 v[176:179], a[96:99], a[188:191], v[176:179]\n"
    "s_add_u32 s61, s45, 24576\n"
    "v_and_b32_e32 v145, 12, v145\n"
    "v_mfma_f32_16x16x32_bf16 v[180:183], a[128:131], a[188:191], v[180:183]\n"
    "v_mfma_f32_16x16x32_bf16 v[184:187], a[68:71], a[188:191], v[184:187]\n"
    "buffer_load_dwordx3 v[236:238], v147, s[8:11], s61 offen offset:0 sc1\n"
    "v_mfma_f32_16x16x32_bf16 v[188:191], a[100:103], a[188:191], v[188:191]\n"
    "s_waitcnt lgkmcnt(8)\n"
    "s_add_u32 s61, s45, 49408\n"
    "ds_read_b128 a[188:191], v240 offset:19456\n"
    "v_mfma_f32_16x16x32_bf16 v[160:163], a[132:135], a[160:163], v[160:163]\n"
    "s_add_u32 s52, s32, 3\n"
    "v_mfma_f32_16x16x32_bf16 v[164:167], a[72:75], a[160:163], v[164:167]\n"
    "buffer_load_dwordx3 v[248:250], v147, s[8:11], s61 offen offset:0 sc1\n"
    "v_mfma_f32_16x16x32_bf16 v[168:171], a[104:107], a[160:163], v[168:171]\n"
    "s_and_b32 s52, s52, 3\n"
    "v_mfma_f32_16x16x32_bf16 v[172:175], a[136:139], a[160:163], v[172:175]\n"
    "s_lshl_b32 s52, s52, 8\n"
    "v_mfma_f32_16x16x32_bf16 v[176:179], a[132:135], a[164:167], v[176:179]\n"
    "s_waitcnt vmcnt(10)\n"
    "buffer_store_dword v148, v149, s[8:11], s87 offen offset:0 sc1\n"
    "v_mfma_f32_16x16x32_bf16 v[180:183], a[72:75], a[164:167], v[180:183]\n"
    "v_mfma_f32_16x16x32_bf16 v[184:187], a[104:107], a[164:167], v[184:187]\n"
    "s_add_u32 s52, s52, s47\n"
    "v_mfma_f32_16x16x32_bf16 v[188:191], a[136:139], a[164:167], v[188:191]\n"
    "s_waitcnt lgkmcnt(6)\n"
    "v_mfma_f32_16x16x32_bf16 v[160:163], a[76:79], a[168:171], v[160:163]\n"
    "buffer_load_dword v144, v145, s[8:11], s52 offen offset:0 sc1\n"
    "v_mfma_f32_16x16x32_bf16 v[164:167], a[108:111], a[168:171], v[164:167]\n"
    "v_mfma_f32_16x16x32_bf16 v[168:171], a[140:143], a[168:171], v[168:171]\n"
    "v_mfma_f32_16x16x32_bf16 v[172:175], a[80:83], a[168:171], v[172:175]\n"
    "v_mfma_f32_16x16x32_bf16 v[176:179], a[76:79], a[172:175], v[176:179]\n"
    "v_mfma_f32_16x16x32_bf16 v[180:183], a[108:111], a[172:175], v[180:183]\n"
    "v_mfma_f32_16x16x32_bf16 v[184:187], a[140:143], a[172:175], v[184:187]\n"
    "v_mfma_f32_16x16x32_bf16 v[188:191], a[80:83], a[172:175], v[188:191]\n"
    "s_waitcnt lgkmcnt(2)\n"
    "v_mfma_f32_16x16x32_bf16 v[160:163], a[112:115], a[176:179], v[160:163]\n"
    "v_mfma_f32_16x16x32_bf16 v[164:167], a[144:147], a[176:179], v[164:167]\n"
    "v_mfma_f32_16x16x32_bf16 v[168:171], a[84:87], a[176:179], v[168:171]\n"
    "v_mfma_f32_16x16x32_bf16 v[172:175], a[116:119], a[176:179], v[172:175]\n"
    "v_mfma_f32_16x16x32_bf16 v[176:179], a[112:115], a[180:183], v[176:179]\n"
    "v_mfma_f32_16x16x32_bf16 v[180:183], a[144:147], a[180:183], v[180:183]\n"
    "v_mfma_f32_16x16x32_bf16 v[184:187], a[84:87], a[180:183], v[184:187]\n"
    "v_mfma_f32_16x16x32_bf16 v[188:191], a[116:119], a[180:183], v[188:191]\n"
    "s_waitcnt lgkmcnt(0)\n"
    "v_mfma_f32_16x16x32_bf16 v[160:163], a[148:151], a[184:187], v[160:163]\n"
    "v_mfma_f32_16x16x32_bf16 v[164:167], a[88:91], a[184:187], v[164:167]\n"
    "v_mfma_f32_16x16x32_bf16 v[168:171], a[120:123], a[184:187], v[168:171]\n"
    "v_mfma_f32_16x16x32_bf16 v[172:175], a[152:155], a[184:187], v[172:175]\n"
    "v_mfma_f32_16x16x32_bf16 v[176:179], a[148:151], a[188:191], v[176:179]\n"
    "v_mfma_f32_16x16x32_bf16 v[180:183], a[88:91], a[188:191], v[180:183]\n"
    "v_mfma_f32_16x16x32_bf16 v[184:187], a[120:123], a[188:191], v[184:187]\n"
    "v_mfma_f32_16x16x32_bf16 v[188:191], a[152:155], a[188:191], v[188:191]\n"
    "s_waitcnt vmcnt(10)\n"
    "v_mfma_f32_16x16x32_bf16 v[0:3], v[154:157], a[224:227], v[0:3]\n"
    "s_waitcnt vmcnt(0)\n"
    "v_cmp_ne_u32_e32 vcc, s39, v144\n"
    "s_nop 3\n"
    "s_cbranch_vccnz .Lat_two_ypoll\n"
    ".Lat_two_ypoll_ret:\n"
    "s_add_u32 s61, s46, 49664\n"
    "buffer_load_dwordx4 a[192:195], v253, s[8:11], s61 offen offset:0 sc1\n"
    "buffer_load_dwordx4 a[196:199], v253, s[8:11], s61 offen offset:1024 sc1\n"
    "buffer_load_dwordx4 a[200:203], v253, s[8:11], s61 offen offset:2048 sc1\n"
    "buffer_load_dwordx4 a[204:207], v253, s[8:11], s61 offen offset:3072 sc1\n"
    "s_add_u32 s61, s45, 74240\n"
    "buffer_load_dwordx3 v[244:246], v147, s[8:11], s61 offen offset:0 sc1\n"
    "v_mfma_f32_16x16x32_bf16 v[4:7], v[152:155], a[224:227], v[4:7]\n"
    "v_mfma_f32_16x16x32_bf16 v[8:11], v[154:157], a[228:231], v[8:11]\n"
    "s_waitcnt vmcnt(7)\n"
    "v_mfma_f32_16x16x32_bf16 v[12:15], v[152:155], a[228:231], v[12:15]\n"
    "s_waitcnt vmcnt(13)\n"
    "v_mfma_f32_16x16x32_bf16 v[16:19], v[154:157], a[232:235], v[16:19]\n"
    "v_add_f32_e32 v232, v232, v236\n"
    "v_mfma_f32_16x16x32_bf16 v[20:23], v[152:155], a[232:235], v[20:23]\n"
    "v_mfma_f32_16x16x32_bf16 v[24:27], v[154:157], a[236:239], v[24:27]\n"
    "v_add_f32_e32 v233, v233, v237\n"
    "v_mfma_f32_16x16x32_bf16 v[28:31], v[152:155], a[236:239], v[28:31]\n"
    "s_waitcnt vmcnt(11)\n"
    "v_mfma_f32_16x16x32_bf16 v[0:3], v[154:157], a[240:243], v[0:3]\n"
    "v_add_f32_e32 v234, v234, v238\n"
    "v_mfma_f32_16x16x32_bf16 v[4:7], v[152:155], a[240:243], v[4:7]\n"
    "v_mfma_f32_16x16x32_bf16 v[8:11], v[154:157], a[244:247], v[8:11]\n"
    "v_add_f32_e32 v232, v232, v248\n"
    "v_mfma_f32_16x16x32_bf16 v[12:15], v[152:155], a[244:247], v[12:15]\n"
    "s_waitcnt vmcnt(9)\n"
    "v_mfma_f32_16x16x32_bf16 v[16:19], v[154:157], a[248:251], v[16:19]\n"
    "v_add_f32_e32 v233, v233, v249\n"
    "v_mfma_f32_16x16x32_bf16 v[20:23], v[152:155], a[248:251], v[20:23]\n"
    "v_mfma_f32_16x16x32_bf16 v[24:27], v[154:157], a[252:255], v[24:27]\n"
    "v_add_f32_e32 v234, v234, v250\n"
    "v_mfma_f32_16x16x32_bf16 v[28:31], v[152:155], a[252:255], v[28:31]\n"
    "s_waitcnt vmcnt(4)\n"
    "v_mfma_f32_16x16x32_bf16 v[0:3], v[154:157], a[192:195], v[0:3]\n"
    "v_mfma_f32_16x16x32_bf16 v[4:7], v[152:155], a[192:195], v[4:7]\n"
    "s_waitcnt vmcnt(3)\n"
    "v_mfma_f32_16x16x32_bf16 v[8:11], v[154:157], a[196:199], v[8:11]\n"
    "v_mfma_f32_16x16x32_bf16 v[12:15], v[152:155], a[196:199], v[12:15]\n"
    "s_waitcnt vmcnt(2)\n"
    "v_cvt_pk_bf16_f32 v0, v0, v1\n"
    "v_cvt_pk_bf16_f32 v1, v2, v3\n"
    "v_mfma_f32_16x16x32_bf16 v[16:19], v[154:157], a[200:203], v[16:19]\n"
    "v_cvt_pk_bf16_f32 v2, v4, v5\n"
    "v_cvt_pk_bf16_f32 v3, v6, v7\n"
    "v_mfma_f32_16x16x32_bf16 v[20:23], v[152:155], a[200:203], v[20:23]\n"
    "s_waitcnt vmcnt(1)\n"
    "ds_write_b128 v158, v[0:3] offset:32768\n"
    "v_mfma_f32_16x16x32_bf16 v[24:27], v[154:157], a[204:207], v[24:27]\n"
    "v_cvt_pk_bf16_f32 v8, v8, v9\n"
    "v_cvt_pk_bf16_f32 v9, v10, v11\n"
    "v_cvt_pk_bf16_f32 v10, v12, v13\n"
    "v_cvt_pk_bf16_f32 v11, v14, v15\n"
    "v_mfma_f32_16x16x32_bf16 v[28:31], v[152:155], a[204:207], v[28:31]\n"
    "ds_write_b128 v158, v[8:11] offset:33792\n"
    "v_cvt_pk_bf16_f32 v16, v16, v17\n"
    "v_cvt_pk_bf16_f32 v17, v18, v19\n"
    "v_cvt_pk_bf16_f32 v18, v20, v21\n"
    "v_cvt_pk_bf16_f32 v19, v22, v23\n"
    "ds_write_b128 v158, v[16:19] offset:34816\n"
    "v_cvt_pk_bf16_f32 v24, v24, v25\n"
    "v_cvt_pk_bf16_f32 v25, v26, v27\n"
    "v_cvt_pk_bf16_f32 v26, v28, v29\n"
    "v_cvt_pk_bf16_f32 v27, v30, v31\n"
    "ds_write_b128 v158, v[24:27] offset:35840\n"
    "s_waitcnt lgkmcnt(0)\n"
    "s_barrier\n"
    "ds_read_b128 a[96:99], v252 offset:40960\n"
    "ds_read_b128 a[100:103], v252 offset:41984\n"
    "ds_read_b128 a[104:107], v252 offset:43008\n"
    "ds_read_b128 a[108:111], v252 offset:44032\n"
    "ds_read_b128 a[112:115], v252 offset:45056\n"
    "ds_read_b128 a[116:119], v252 offset:46080\n"
    "ds_read_b128 a[120:123], v252 offset:47104\n"
    "ds_read_b128 a[124:127], v252 offset:48128\n"
    "s_waitcnt lgkmcnt(6)\n"
    "v_mfma_f32_16x16x32_bf16 v[120:123], v[56:59], a[96:99], 0\n"
    "v_mfma_f32_16x16x32_bf16 v[124:127], v[60:63], a[96:99], 0\n"
    "s_waitcnt vmcnt(0)\n"
    "ds_read_b128 a[64:67], v252 offset:32768\n"
    "v_mfma_f32_16x16x32_bf16 v[128:131], v[64:67], a[96:99], 0\n"
    "v_mfma_f32_16x16x32_bf16 v[132:135], v[68:71], a[96:99], 0\n"
    "v_add_f32_e32 v234, v234, v246\n"
    "v_mfma_f32_16x16x32_bf16 v[120:123], v[72:75], a[100:103], v[120:123]\n"
    "ds_read_b128 a[68:71], v252 offset:33792\n"
    "v_mfma_f32_16x16x32_bf16 v[124:127], v[76:79], a[100:103], v[124:127]\n"
    "v_add_f32_e32 v233, v233, v245\n"
    "v_mfma_f32_16x16x32_bf16 v[128:131], v[80:83], a[100:103], v[128:131]\n"
    "ds_read_b128 a[72:75], v252 offset:34816\n"
    "v_mfma_f32_16x16x32_bf16 v[132:135], v[84:87], a[100:103], v[132:135]\n"
    "s_waitcnt lgkmcnt(7)\n"
    "v_add_f32_e32 v232, v232, v244\n"
    "v_mfma_f32_16x16x32_bf16 v[120:123], v[88:91], a[104:107], v[120:123]\n"
    "v_mfma_f32_16x16x32_bf16 v[124:127], v[92:95], a[104:107], v[124:127]\n"
    "ds_read_b128 a[76:79], v252 offset:35840\n"
    "v_mfma_f32_16x16x32_bf16 v[128:131], v[96:99], a[104:107], v[128:131]\n"
    "s_bitcmp1_b32 %1, 0\n"
    "s_cselect_b64 vcc, -1, 0\n"
    "v_mfma_f32_16x16x32_bf16 v[132:135], v[100:103], a[104:107], v[132:135]\n"
    "ds_read_b128 a[80:83], v252 offset:36864\n"
    "v_mfma_f32_16x16x32_bf16 v[120:123], v[104:107], a[108:111], v[120:123]\n"
    "v_cndmask_b32_e32 v234, v233, v234, vcc\n"
    "v_mfma_f32_16x16x32_bf16 v[124:127], v[108:111], a[108:111], v[124:127]\n"
    "s_mov_b32 s61, 0x12800000\n"
    "v_mfma_f32_16x16x32_bf16 v[128:131], v[112:115], a[108:111], v[128:131]\n"
    "ds_read_b128 a[84:87], v252 offset:37888\n"
    "v_mfma_f32_16x16x32_bf16 v[132:135], v[116:119], a[108:111], v[132:135]\n"
    "s_waitcnt lgkmcnt(8)\n"
    "v_rcp_f32_e32 v241, v233\n"
    "v_mfma_f32_16x16x32_bf16 v[120:123], a[28:31], a[112:115], v[120:123]\n"
    "ds_read_b128 a[88:91], v252 offset:38912\n"
    "v_mfma_f32_16x16x32_bf16 v[124:127], a[60:63], a[112:115], v[124:127]\n"
    "v_rcp_f32_e32 v240, v232\n"
    "v_mfma_f32_16x16x32_bf16 v[128:131], a[0:3], a[112:115], v[128:131]\n"
    "v_mfma_f32_16x16x32_bf16 v[132:135], a[32:35], a[112:115], v[132:135]\n"
    "ds_read_b128 a[92:95], v252 offset:39936\n"
    "v_mfma_f32_16x16x32_bf16 v[120:123], a[4:7], a[116:119], v[120:123]\n"
    "v_subrev_u32_e32 v236, s61, v232\n"
    "v_mfma_f32_16x16x32_bf16 v[124:127], a[36:39], a[116:119], v[124:127]\n"
    "v_subrev_u32_e32 v237, s61, v233\n"
    "v_mfma_f32_16x16x32_bf16 v[128:131], a[8:11], a[116:119], v[128:131]\n"
    "v_mfma_f32_16x16x32_bf16 v[132:135], a[40:43], a[116:119], v[132:135]\n"
    "s_waitcnt lgkmcnt(8)\n"
    "v_subrev_u32_e32 v238, s61, v234\n"
    "v_mfma_f32_16x16x32_bf16 v[120:123], a[12:15], a[120:123], v[120:123]\n"
    "v_mfma_f32_16x16x32_bf16 v[124:127], a[44:47], a[120:123], v[124:127]\n"
    "v_max3_u32 v236, v236, v237, v238\n"
    "v_mfma_f32_16x16x32_bf16 v[128:131], a[16:19], a[120:123], v[128:131]\n"
    "v_mfma_f32_16x16x32_bf16 v[132:135], a[48:51], a[120:123], v[132:135]\n"
    "v_cmp_le_u32_e32 vcc, 0x5f000000, v236\n"
    "v_mfma_f32_16x16x32_bf16 v[120:123], a[20:23], a[124:127], v[120:123]\n"
    "v_mfma_f32_16x16x32_bf16 v[124:127], a[52:55], a[124:127], v[124:127]\n"
    "buffer_store_dword v148, v149, s[8:11], s88 offen offset:0 sc1\n"
    "v_mfma_f32_16x16x32_bf16 v[128:131], a[24:27], a[124:127], v[128:131]\n"
    "v_mfma_f32_16x16x32_bf16 v[132:135], a[56:59], a[124:127], v[132:135]\n"
    "s_waitcnt lgkmcnt(0)\n"
    "v_mfma_f32_16x16x32_bf16 v[32:35], v[56:59], a[64:67], 0\n"
    "s_cbranch_vccnz .Lat_exit\n"
    "v_mfma_f32_16x16x32_bf16 v[36:39], v[60:63], a[64:67], 0\n"
    "s_add_u32 s48, s58, 8192\n"
    "v_fma_f32 v120, v120, v241, v176\n"
    "v_mfma_f32_16x16x32_bf16 v[32:35], v[72:75], a[68:71], v[32:35]\n"
    "v_fma_f32 v121, v121, v241, v177\n"
    "v_mfma_f32_16x16x32_bf16 v[36:39], v[76:79], a[68:71], v[36:39]\n"
    "v_fma_f32 v122, v122, v241, v178\n"
    "v_mfma_f32_16x16x32_bf16 v[32:35], v[88:91], a[72:75], v[32:35]\n"
    "v_fma_f32 v123, v123, v241, v179\n"
    "v_mfma_f32_16x16x32_bf16 v[36:39], v[92:95], a[72:75], v[36:39]\n"
    "v_fma_f32 v124, v124, v241, v180\n"
    "v_mfma_f32_16x16x32_bf16 v[32:35], v[104:107], a[76:79], v[32:35]\n"
    "v_fma_f32 v125, v125, v241, v181\n"
    "v_mfma_f32_16x16x32_bf16 v[36:39], v[108:111], a[76:79], v[36:39]\n"
    "v_fma_f32 v126, v126, v241, v182\n"
    "v_fma_f32 v127, v127, v241, v183\n"
    "v_mfma_f32_16x16x32_bf16 v[32:35], a[28:31], a[80:83], v[32:35]\n"
    "v_cvt_pk_bf16_f32 v120, v120, v121\n"
    "v_mfma_f32_16x16x32_bf16 v[36:39], a[60:63], a[80:83], v[36:39]\n"
    "v_cvt_pk_bf16_f32 v121, v122, v123\n"
    "v_mfma_f32_16x16x32_bf16 v[32:35], a[4:7], a[84:87], v[32:35]\n"
    "v_cvt_pk_bf16_f32 v122, v124, v125\n"
    "v_mfma_f32_16x16x32_bf16 v[36:39], a[36:39], a[84:87], v[36:39]\n"
    "v_cvt_pk_bf16_f32 v123, v126, v127\n"
    "v_mfma_f32_16x16x32_bf16 v[32:35], a[12:15], a[88:91], v[32:35]\n"
    "buffer_store_dwordx4 v[120:123], v151, s[16:19], s48 offen offset:0 sc0 sc1\n"
    "v_fma_f32 v128, v128, v241, v184\n"
    "v_mfma_f32_16x16x32_bf16 v[36:39], a[44:47], a[88:91], v[36:39]\n"
    "v_fma_f32 v129, v129, v241, v185\n"
    "v_fma_f32 v130, v130, v241, v186\n"
    "v_mfma_f32_16x16x32_bf16 v[32:35], a[20:23], a[92:95], v[32:35]\n"
    "v_fma_f32 v131, v131, v241, v187\n"
    "v_mfma_f32_16x16x32_bf16 v[36:39], a[52:55], a[92:95], v[36:39]\n"
    "v_fma_f32 v132, v132, v241, v188\n"
    "v_mfma_f32_16x16x32_bf16 v[40:43], v[64:67], a[64:67], 0\n"
    "v_fma_f32 v133, v133, v241, v189\n"
    "v_mfma_f32_16x16x32_bf16 v[44:47], v[68:71], a[64:67], 0\n"
    "v_fma_f32 v134, v134, v241, v190\n"
    "v_fma_f32 v135, v135, v241, v191\n"
    "v_mfma_f32_16x16x32_bf16 v[40:43], v[80:83], a[68:71], v[40:43]\n"
    "v_cvt_pk_bf16_f32 v128, v128, v129\n"
    "v_mfma_f32_16x16x32_bf16 v[44:47], v[84:87], a[68:71], v[44:47]\n"
    "v_cvt_pk_bf16_f32 v129, v130, v131\n"
    "v_mfma_f32_16x16x32_bf16 v[40:43], v[96:99], a[72:75], v[40:43]\n"
    "v_cvt_pk_bf16_f32 v130, v132, v133\n"
    "v_cvt_pk_bf16_f32 v131, v134, v135\n"
    "v_mfma_f32_16x16x32_bf16 v[44:47], v[100:103], a[72:75], v[44:47]\n"
    "buffer_store_dwordx4 v[128:131], v151, s[16:19], s48 offen offset:64 sc0 sc1\n"
    "v_mfma_f32_16x16x32_bf16 v[40:43], v[112:115], a[76:79], v[40:43]\n"
    "s_add_u32 s48, s58, 0\n"
    "v_fma_f32 v32, v32, v240, v160\n"
    "v_mfma_f32_16x16x32_bf16 v[44:47], v[116:119], a[76:79], v[44:47]\n"
    "v_fma_f32 v33, v33, v240, v161\n"
    "v_mfma_f32_16x16x32_bf16 v[40:43], a[0:3], a[80:83], v[40:43]\n"
    "v_fma_f32 v34, v34, v240, v162\n"
    "v_mfma_f32_16x16x32_bf16 v[44:47], a[32:35], a[80:83], v[44:47]\n"
    "v_fma_f32 v35, v35, v240, v163\n"
    "v_mfma_f32_16x16x32_bf16 v[40:43], a[8:11], a[84:87], v[40:43]\n"
    "v_fma_f32 v36, v36, v240, v164\n"
    "v_fma_f32 v37, v37, v240, v165\n"
    "v_mfma_f32_16x16x32_bf16 v[44:47], a[40:43], a[84:87], v[44:47]\n"
    "v_fma_f32 v38, v38, v240, v166\n"
    "v_mfma_f32_16x16x32_bf16 v[40:43], a[16:19], a[88:91], v[40:43]\n"
    "v_fma_f32 v39, v39, v240, v167\n"
    "v_mfma_f32_16x16x32_bf16 v[44:47], a[48:51], a[88:91], v[44:47]\n"
    "v_cvt_pk_bf16_f32 v32, v32, v33\n"
    "v_cvt_pk_bf16_f32 v33, v34, v35\n"
    "v_mfma_f32_16x16x32_bf16 v[40:43], a[24:27], a[92:95], v[40:43]\n"
    "v_cvt_pk_bf16_f32 v34, v36, v37\n"
    "v_cvt_pk_bf16_f32 v35, v38, v39\n"
    "v_mfma_f32_16x16x32_bf16 v[44:47], a[56:59], a[92:95], v[44:47]\n"
    "buffer_store_dwordx4 v[32:35], v151, s[16:19], s48 offen offset:0 sc0 sc1\n"
    "s_nop 2\n"
    "v_fma_f32 v40, v40, v240, v168\n"
    "v_fma_f32 v41, v41, v240, v169\n"
    "v_fma_f32 v42, v42, v240, v170\n"
    "v_fma_f32 v43, v43, v240, v171\n"
    "v_fma_f32 v44, v44, v240, v172\n"
    "v_fma_f32 v45, v45, v240, v173\n"
    "v_fma_f32 v46, v46, v240, v174\n"
    "v_fma_f32 v47, v47, v240, v175\n"
    "v_cvt_pk_bf16_f32 v40, v40, v41\n"
    "v_cvt_pk_bf16_f32 v41, v42, v43\n"
    "v_cvt_pk_bf16_f32 v42, v44, v45\n"
    "v_cvt_pk_bf16_f32 v43, v46, v47\n"
    "buffer_store_dwordx4 v[40:43], v151, s[16:19], s48 offen offset:64 sc0 sc1\n"
    "s_endpgm\n"
    ".Lat_two_xpoll:\n"
    "s_mov_b32 s53, 0x400000\n"
    ".Lat_two_xpoll_l:\n"
    "s_sleep 0\n"
    "buffer_load_dword v146, v145, s[8:11], s47 offen offset:128 sc1\n"
    "s_waitcnt vmcnt(0)\n"
    "v_cmp_ne_u32_e32 vcc, s39, v146\n"
    "s_sub_u32 s53, s53, 1\n"
    "s_cbranch_scc1 .Lat_two_xpoll_ret\n"
    "s_nop 2\n"
    "s_cbranch_vccnz .Lat_two_xpoll_l\n"
    "s_branch .Lat_two_xpoll_ret\n"
    ".Lat_two_ypoll:\n"
    "s_mov_b32 s53, 0x400000\n"
    ".Lat_two_ypoll_l:\n"
    "s_sleep 0\n"
    "buffer_load_dword v144, v145, s[8:11], s52 offen offset:0 sc1\n"
    "s_waitcnt vmcnt(0)\n"
    "v_cmp_ne_u32_e32 vcc, s39, v144\n"
    "s_sub_u32 s53, s53, 1\n"
    "s_cbranch_scc1 .Lat_two_ypoll_ret\n"
    "s_nop 2\n"
    "s_cbranch_vccnz .Lat_two_ypoll_l\n"
    "s_branch .Lat_two_ypoll_ret\n"
    ".Lat_wpoll:\n"
    "s_mov_b32 s53, 0x400000\n"
    ".Lat_wpoll_l:\n"
    "s_sleep 1\n"
    "buffer_load_dword v255, v254, s[8:11], s45 offen offset:0 sc1\n"
    "s_waitcnt vmcnt(0)\n"
    "v_cmp_ne_u32_e32 vcc, s39, v255\n"
    "s_sub_u32 s53, s53, 1\n"
    "s_cbranch_scc1 .Lat_wpoll_ret\n"
    "s_nop 2\n"
    "s_cbranch_vccnz .Lat_wpoll_l\n"
    "s_branch .Lat_wpoll_ret\n"
    ".Lat_xpoll:\n"
    "s_mov_b32 s53, 0x400000\n"
    ".Lat_xpoll_l:\n"
    "s_sleep 0\n"
    "buffer_load_dword v146, v145, s[8:11], s47 offen offset:128 sc1\n"
    "s_waitcnt vmcnt(0)\n"
    "v_cmp_ne_u32_e32 vcc, s39, v146\n"
    "s_sub_u32 s53, s53, 1\n"
    "s_cbranch_scc1 .Lat_xpoll_ret\n"
    "s_nop 2\n"
    "s_cbranch_vccnz .Lat_xpoll_l\n"
    "s_branch .Lat_xpoll_ret\n"
    ".Lat_ypoll:\n"
    "s_mov_b32 s53, 0x400000\n"
    ".Lat_ypoll_l:\n"
    "s_sleep 0\n"
    "buffer_load_dword v144, v145, s[8:11], s52 offen offset:0 sc1\n"
    "s_waitcnt vmcnt(0)\n"
    "v_cmp_ne_u32_e32 vcc, s39, v144\n"
    "s_sub_u32 s53, s53, 1\n"
    "s_cbranch_scc1 .Lat_ypoll_ret\n"
    "s_nop 2\n"
    "s_cbranch_vccnz .Lat_ypoll_l\n"
    "s_branch .Lat_ypoll_ret\n"
    ".Lat_apoll:\n"
    "s_mov_b32 s53, 0x400000\n"
    ".Lat_apoll_l:\n"
    "s_sleep 1\n"
    "buffer_load_dword v255, v252, s[8:11], 0 offen offset:0 sc1\n"
    "s_waitcnt vmcnt(0)\n"
    "v_cmp_ne_u32_e32 vcc, s39, v255\n"
    "s_sub_u32 s53, s53, 1\n"
    "s_cbranch_scc1 .Lat_apoll_ret\n"
    "s_nop 2\n"
    "s_cbranch_vccnz .Lat_apoll_l\n"
    "s_branch .Lat_apoll_ret\n"
    ".Lat_h1poll:\n"
    "s_mov_b32 s53, 0x400000\n"
    ".Lat_h1poll_l:\n"
    "s_sleep 1\n"
    "buffer_load_dword v0, v3, s[8:11], s59 offen offset:0 sc1\n"
    "s_waitcnt vmcnt(0)\n"
    "v_cmp_ne_u32_e32 vcc, s39, v0\n"
    "s_sub_u32 s53, s53, 1\n"
    "s_cbranch_scc1 .Lat_h1poll_ret\n"
    "s_nop 2\n"
    "s_cbranch_vccnz .Lat_h1poll_l\n"
    "s_branch .Lat_h1poll_ret\n"
    ".Lat_hpoll:\n"
    "s_mov_b32 s53, 0x400000\n"
    ".Lat_hpoll_l:\n"
    "s_sleep 1\n"
    "buffer_load_dword v0, v253, s[8:11], s59 offen offset:0 sc1\n"
    "buffer_load_dword v1, v2, s[8:11], s59 offen offset:0 sc1\n"
    "s_waitcnt vmcnt(0)\n"
    "v_xor_b32_e32 v1, s39, v1\n"
    "v_xor_b32_e32 v0, s39, v0\n"
    "v_or_b32_e32 v0, v0, v1\n"
    "v_cmp_ne_u32_e32 vcc, 0, v0\n"
    "s_sub_u32 s53, s53, 1\n"
    "s_cbranch_scc1 .Lat_hpoll_ret\n"
    "s_nop 2\n"
    "s_cbranch_vccnz .Lat_hpoll_l\n"
    "s_branch .Lat_hpoll_ret\n"
    ".Lat_qpoll:\n"
    "s_mov_b32 s53, 0x400000\n"
    ".Lat_qpoll_l:\n"
    "s_sleep 0\n"
    "buffer_load_dword v11, v5, s[8:11], s47 offen offset:0 sc1\n"
    "s_waitcnt vmcnt(0)\n"
    "v_cmp_ne_u32_e32 vcc, s39, v11\n"
    "s_sub_u32 s53, s53, 1\n"
    "s_cbranch_scc1 .Lat_qpoll_ret\n"
    "s_nop 2\n"
    "s_cbranch_vccnz .Lat_qpoll_l\n"
    "s_branch .Lat_qpoll_ret\n"
    ".Lat_lpoll:\n"
    "s_mov_b32 s53, 0x400000\n"
    ".Lat_lpoll_l:\n"
    "s_sleep 1\n"
    "buffer_load_dword v154, off, s[8:11], s80 sc1\n"
    "s_waitcnt vmcnt(0)\n"
    "v_cmp_ne_u32_e32 vcc, s39, v154\n"
    "s_sub_u32 s53, s53, 1\n"
    "s_cbranch_scc1 .Lat_lpoll_ret\n"
    "s_nop 2\n"
    "s_cbranch_vccnz .Lat_lpoll_l\n"
    ".Lat_lpoll_ret:\n"
    "v_lshlrev_b32_e32 v253, 2, v249\n"
    "s_mov_b32 m0, s82\n"
    "s_nop 0\n"
    "buffer_load_dwordx4 v253, s[8:11], s81 offen offset:0 sc1 lds\n"
    "s_waitcnt vmcnt(0)\n"
    "s_branch .Lat_g\n"
    ".Lat_exit:\n"
    "s_waitcnt vmcnt(0)\n"
    "s_nop 15\n"
    ".Lat_end:\n"
    "s_waitcnt lgkmcnt(0)\n"
    : "+s"(ka), "+s"(b), "+s"(wid) : "s"((unsigned)(size_t)lds)
    : "v0", "v1", "v2", "v3", "v4", "v5", "v6", "v7", "v8", "v9", "v10", "v11", "v12", "v13", "v14", "v15", "v16", "v17", "v18", "v19", "v20", "v21", "v22", "v23", "v24", "v25", "v26", "v27", "v28", "v29", "v30", "v31", "v32", "v33", "v34", "v35", "v36", "v37", "v38", "v39", "v40", "v41", "v42", "v43", "v44", "v45", "v46", "v47", "v48", "v49", "v50", "v51", "v52", "v53", "v54", "v55", "v56", "v57", "v58", "v59", "v60", "v61", "v62", "v63", "v64", "v65", "v66", "v67", "v68", "v69", "v70", "v71", "v72", "v73", "v74", "v75", "v76", "v77", "v78", "v79", "v80", "v81", "v82", "v83", "v84", "v85", "v86", "v87", "v88", "v89", "v90", "v91", "v92", "v93", "v94", "v95", "v96", "v97", "v98", "v99", "v100", "v101", "v102", "v103", "v104", "v105", "v106", "v107", "v108", "v109", "v110", "v111", "v112", "v113", "v114", "v115", "v116", "v117", "v118", "v119", "v120", "v121", "v122", "v123", "v124", "v125", "v126", "v127", "v128", "v129", "v130", "v131", "v132", "v133", "v134", "v135", "v136", "v137", "v138", "v139", "v140", "v141", "v142", "v143", "v144", "v145", "v146", "v147", "v148", "v149", "v150", "v151", "v152", "v153", "v154", "v155", "v156", "v157", "v158", "v159", "v160", "v161", "v162", "v163", "v164", "v165", "v166", "v167", "v168", "v169", "v170", "v171", "v172", "v173", "v174", "v175", "v176", "v177", "v178", "v179", "v180", "v181", "v182", "v183", "v184", "v185", "v186", "v187", "v188", "v189", "v190", "v191", "v192", "v193", "v194", "v195", "v196", "v197", "v198", "v199", "v200", "v201", "v202", "v203", "v204", "v205", "v206", "v207", "v208", "v209", "v210", "v211", "v212", "v213", "v214", "v215", "v216", "v217", "v218", "v219", "v220", "v221", "v222", "v223", "v224", "v225", "v226", "v227", "v228", "v229", "v230", "v231", "v232", "v233", "v234", "v235", "v236", "v237", "v238", "v239", "v240", "v241", "v242", "v243", "v244", "v245", "v246", "v247", "v248", "v249", "v250", "v251", "v252", "v253", "v254", "v255", "a0", "a1", "a2", "a3", "a4", "a5", "a6", "a7", "a8", "a9", "a10", "a11", "a12", "a13", "a14", "a15", "a16", "a17", "a18", "a19", "a20", "a21", "a22", "a23", "a24", "a25", "a26", "a27", "a28", "a29", "a30", "a31", "a32", "a33", "a34", "a35", "a36", "a37", "a38", "a39", "a40", "a41", "a42", "a43", "a44", "a45", "a46", "a47", "a48", "a49", "a50", "a51", "a52", "a53", "a54", "a55", "a56", "a57", "a58", "a59", "a60", "a61", "a62", "a63", "a64", "a65", "a66", "a67", "a68", "a69", "a70", "a71", "a72", "a73", "a74", "a75", "a76", "a77", "a78", "a79", "a80", "a81", "a82", "a83", "a84", "a85", "a86", "a87", "a88", "a89", "a90", "a91", "a92", "a93", "a94", "a95", "a96", "a97", "a98", "a99", "a100", "a101", "a102", "a103", "a104", "a105", "a106", "a107", "a108", "a109", "a110", "a111", "a112", "a113", "a114", "a115", "a116", "a117", "a118", "a119", "a120", "a121", "a122", "a123", "a124", "a125", "a126", "a127", "a128", "a129", "a130", "a131", "a132", "a133", "a134", "a135", "a136", "a137", "a138", "a139", "a140", "a141", "a142", "a143", "a144", "a145", "a146", "a147", "a148", "a149", "a150", "a151", "a152", "a153", "a154", "a155", "a156", "a157", "a158", "a159", "a160", "a161", "a162", "a163", "a164", "a165", "a166", "a167", "a168", "a169", "a170", "a171", "a172", "a173", "a174", "a175", "a176", "a177", "a178", "a179", "a180", "a181", "a182", "a183", "a184", "a185", "a186", "a187", "a188", "a189", "a190", "a191", "a192", "a193", "a194", "a195", "a196", "a197", "a198", "a199", "a200", "a201", "a202", "a203", "a204", "a205", "a206", "a207", "a208", "a209", "a210", "a211", "a212", "a213", "a214", "a215", "a216", "a217", "a218", "a219", "a220", "a221", "a222", "a223", "a224", "a225", "a226", "a227", "a228", "a229", "a230", "a231", "a232", "a233", "a234", "a235", "a236", "a237", "a238", "a239", "a240", "a241", "a242", "a243", "a244", "a245", "a246", "a247", "a248", "a249", "a250", "a251", "a252", "a253", "a254", "a255", "s8", "s9", "s10", "s11", "s12", "s13", "s14", "s15", "s16", "s17", "s18", "s19", "s20", "s21", "s22", "s23", "s24", "s25", "s26", "s27", "s28", "s29", "s30", "s31", "s32", "s33", "s34", "s35", "s36", "s37", "s38", "s39", "s40", "s41", "s42", "s43", "s44", "s45", "s46", "s47", "s48", "s49", "s50", "s51", "s52", "s53", "s54", "s55", "s56", "s57", "s58", "s59", "s60", "s61", "s62", "s63", "s80", "s81", "s82", "s83", "s84", "s85", "s86", "s87", "s88", "s89", "s90", "vcc", "m0", "scc", "memory");
  typedef __attribute__((address_space(1))) char* __attribute__((address_space(4))) const* arg_t;
  const arg_t ap = (arg_t)ka;
  typedef const u16* cu16;
  if (b >= NATT) list_branch(lds, b - NATT, wid, (const int*)ap[1], (const int*)ap[2], (cu16)ap[3], (cu16)ap[8], (cu16)ap[9], (cu16)ap[10], (char*)ap[0]);
  else exact_rows(lds, b, wid, (char*)ap[0], (cu16)ap[4], (u16*)ap[5]);
}

extern "C" void kernel_launch(void* const* d_in, const int* in_sizes, int n_in, void* d_out, int out_size, void* d_ws,
                              size_t ws_size, hipStream_t stream) {
  const u16* x = (const u16*)d_in[0];
  const int* src = (const int*)d_in[1];
  const int* dst = (const int*)d_in[2];
  const u16* deg_inv = (const u16*)d_in[3];
  const u16* wmsg = (const u16*)d_in[4];
  const u16* wq = (const u16*)d_in[5];
  const u16* wk = (const u16*)d_in[6];
  const u16* wv = (const u16*)d_in[7];
  const u16* wout = (const u16*)d_in[8];
  char* ws = (char*)d_ws;
  (void)hipFuncSetAttribute((const void*)attn_kernel, hipFuncAttributeMaxDynamicSharedMemorySize, 163840);
  attn_kernel<<<256, 256, 163840, stream>>>(ws, src, dst, deg_inv, x, (u16*)d_out, wq, wk, wv, wmsg, wout);
}
```

```cpp
#include <hip/hip_runtime.h>
#include <hip/hip_bf16.h>
#include <stdint.h>
#pragma clang diagnostic ignored "-Winline-asm"

typedef unsigned short u16;
typedef __attribute__((ext_vector_type(8))) short bf16x8;
typedef __attribute__((ext_vector_type(4))) short bf16x4;
typedef __attribute__((ext_vector_type(4))) float f32x4;
typedef __attribute__((ext_vector_type(2))) float f32x2;
typedef __attribute__((ext_vector_type(4))) unsigned u32x4;
typedef __attribute__((ext_vector_type(2))) unsigned u32x2;
#define DI __device__ __forceinline__

constexpr int NN = 10000, DD = 256, EE = 160000;
constexpr int NATT = 252, NSP = 4;

constexpr unsigned WS_H = 0;
constexpr unsigned WS_AF = WS_H + 10240 * 512;
constexpr unsigned WS_WV = WS_AF + 131072;
constexpr unsigned WS_WM = WS_WV + 131072;
constexpr unsigned WS_XF = WS_WM + 131072;
constexpr unsigned WS_HF = WS_XF + 256 * 256;
constexpr unsigned WS_BF = WS_HF + 4096;
constexpr unsigned WS_PF = WS_BF + 65536;
constexpr unsigned WS_WF = WS_PF + 4096;
constexpr unsigned WS_LF = WS_WF + 256;
constexpr unsigned WS_EP = WS_LF + 2048;
constexpr unsigned WS_G = WS_EP + 1792;
constexpr unsigned WS_QP = WS_G + 10240 * 512;
constexpr unsigned WS_XB = WS_QP + 10240 * 512;
constexpr unsigned WS_BL = WS_XB + NATT * 3 * 24832;
constexpr unsigned WS_GL = WS_BL + 16 * 5 * 260 * 64;
constexpr unsigned WS_DBG = WS_GL + 256 * 4096;
constexpr unsigned WS_END = WS_DBG + 1024 * 128 + 8192;

DI float bf2f(unsigned x) { return __uint_as_float(x << 16); }
DI float bfhi(unsigned x) { return __uint_as_float(x & 0xffff0000u); }
DI unsigned pk2(float lo, float hi) {
  typedef __attribute__((ext_vector_type(2))) __bf16 bf2_t;
  const bf2_t r = __builtin_convertvector((f32x2){lo, hi}, bf2_t);
  return __builtin_bit_cast(unsigned, r);
}
typedef __attribute__((address_space(3))) bf16x4* lds_bf16x4_p;
DI bf16x4 tr_read(unsigned p) { return __builtin_amdgcn_ds_read_tr16_b64_v4i16((lds_bf16x4_p)p); }
DI bf16x8 as_bf(u32x4 v) { return __builtin_bit_cast(bf16x8, v); }
#define SB() __builtin_amdgcn_sched_barrier(0)
#define MFMA16(a, b, c) __builtin_amdgcn_mfma_f32_16x16x32_bf16(a, b, c, 0, 0, 0)

typedef __amdgpu_buffer_rsrc_t srd_t;
DI void drain() { asm volatile("s_waitcnt vmcnt(0)" ::: "memory"); }
DI void st_flag(srd_t w, unsigned off, unsigned tag) { __builtin_amdgcn_raw_buffer_store_b32(tag, w, off, 0, 16); }
DI void wait_flag(srd_t w, unsigned off, unsigned tag) {
#pragma unroll 1
  for (int it = 0; it < (1 << 22); ++it) {
    unsigned v;
    asm volatile("buffer_load_dword %0, %1, %2, 0 offen sc1\n s_waitcnt vmcnt(0)" : "=v"(v) : "v"(off), "s"(w) : "memory");
    if (__ballot(v != tag) == 0) break;
    __builtin_amdgcn_s_sleep(2);
    asm volatile("" ::: "memory");
  }
  asm volatile("" ::: "memory");
}

DI void prod_store(const f32x4 (&acc)[2][2], int mat, int ks, int J, int lane, srd_t wsrd) {
#pragma unroll
  for (int s = 0; s < 2; ++s) {
    u32x4 o = {pk2(acc[0][s][0], acc[0][s][1]), pk2(acc[0][s][2], acc[0][s][3]), pk2(acc[1][s][0], acc[1][s][1]), pk2(acc[1][s][2], acc[1][s][3])};
    if (ks == 7 && lane >= 48) o[3] &= 0xffffu;
    const int T = 2 * J + s;
    const int at = mat ? 131072 + ((J & 1) ^ 1) * 16384 + ks * 2048 + s * 1024 : ks * 4096 + (T & 3) * 1024;
    __builtin_amdgcn_raw_buffer_store_b128(o, wsrd, WS_WV + (T >> 2) * 32768 + at + lane * 16, 0, 16);
  }
}
DI void corr(unsigned* dl, const unsigned (&lr)[16]) {
  float a0 = 0.f, a1 = 0.f;
#pragma unroll
  for (int r = 0; r < 16; ++r) { a0 += bf2f(lr[r] & 0xffffu); a1 += bfhi(lr[r]); }
  dl[0] = pk2(a0 * -0.0625f, a1 * -0.0625f);
  dl[128] = pk2((bf2f(lr[15] & 0xffffu) - 0.25f * a0) * (1.f / 3.f), (bfhi(lr[15]) - 0.25f * a1) * (1.f / 3.f));
}
DI void product_load(char* lds, int wid, int lane, int job, const u16* __restrict__ wv, const u16* __restrict__ wm, const u16* __restrict__ wo, bool left, bool right) {
  const unsigned ldsb = (unsigned)(size_t)lds;
  const int r = lane >> 3, sw = ((lane & 7) ^ (r & 6)) * 16 + wid * 128;
  const unsigned vp = (8 * (r >> 2) + (r & 3)) * 512 + sw, vn = r * 512 + sw;
  auto image = [&](int n, const u16* w, int row0, bool nat) {
#pragma unroll
    for (int i = 0; i < 8; ++i) {
      const int a = i & 3;
      asm volatile("s_mov_b32 m0, %2\n s_nop 0\n global_load_lds_dwordx4 %0, %1"
                   :: "v"(nat ? vn : vp), "s"(w + (row0 + (i >> 2) * 32 + (nat ? 8 * a : 16 * (a & 1) + 4 * (a >> 1))) * DD),
                      "s"(ldsb + n * 32768 + (i >> 2) * 16384 + wid * 4096 + a * 1024) : "m0", "memory");
    }
  };
  if (left) {
    image(2, wv, (job >> 2) * 64, true);
    image(3, wm, (job >> 2) * 64, false);
  }
  if (right)
#pragma unroll
  for (int k = 0; k < 8; ++k)
    asm volatile("s_mov_b32 m0, %2\n s_nop 0\n global_load_lds_dwordx4 %0, %1"
                 :: "v"((lane >> 3) * 512 + (lane & 7) * 16), "s"(wo + (k * 32 + wid * 8) * DD + (job & 3) * 64), "s"(ldsb + k * 4096 + wid * 1024) : "m0", "memory");
}
DI void product_w(char* lds, int wid, int lane, int job, srd_t wsrd) {
  const int m = lane & 15, g = lane >> 4;
  const int ks = 2 * (job >> 2) + (wid >> 1), J = 2 * (job & 3) + (wid & 1);
  const unsigned ldsb = (unsigned)(size_t)lds;
  unsigned* dl = reinterpret_cast<unsigned*>(lds + 0x26c00);
  const bool hi = (m >> 3) != 0;
  const unsigned ra = (m >> 3) * 1024 + (m & 7) * 128 + (g ^ (m & 6)) * 16;
  auto frag = [&](int n, int tl, int t, int k) {
    return *reinterpret_cast<const bf16x8*>(lds + n * 32768 + tl * 16384 + (k >> 1) * 4096 + t * 2048 + (ra ^ ((k & 1) * 64)));
  };
  f32x4 av[2][2] = {}, am[2][2] = {}, ad[2] = {};
  const unsigned tb = ldsb + (job & 1) * 32768 + (8 * g + ((lane >> 2) & 3)) * 128 + (wid & 1) * 64 + (lane & 3) * 16;
  bf16x8 d0[8], d1[8], d2[8], rr[8][2], vf[8][2], mf[8][2];
#pragma unroll
  for (int kk = 0; kk < 9; ++kk) {
    if (kk < 8) {
      d0[kk] = *reinterpret_cast<const bf16x8*>(dl + 512 + 16 * kk + 4 * g); d1[kk] = *reinterpret_cast<const bf16x8*>(dl + 512 + (ks == 7 ? 128 : 0) + 16 * kk + 4 * g);
      d2[kk] = *reinterpret_cast<const bf16x8*>(dl + 768 + (ks == 7 && hi ? 128 : 0) + 16 * kk + 4 * g);
#pragma unroll
      for (int s = 0; s < 2; ++s) {
        rr[kk][s] = __builtin_shufflevector(tr_read(tb + kk * 4096 + s * 8), tr_read(tb + kk * 4096 + s * 8 + 512), 0, 1, 2, 3, 4, 5, 6, 7);
        vf[kk][s] = frag(2, wid >> 1, s, kk); mf[kk][s] = frag(3, wid >> 1, s, kk);
      }
    }
    SB();
    if (kk) {
      const int k = kk - 1;
#pragma unroll
      for (int s = 0; s < 2; ++s) {
        av[0][s] = MFMA16(d0[k], rr[k][s], av[0][s]); av[1][s] = MFMA16(d1[k], rr[k][s], av[1][s]); ad[s] = MFMA16(d2[k], rr[k][s], ad[s]);
#pragma unroll
        for (int t = 0; t < 2; ++t) { av[t][s] = MFMA16(vf[k][t], rr[k][s], av[t][s]); am[t][s] = MFMA16(mf[k][t], rr[k][s], am[t][s]); }
      }
    }
    SB();
  }
#pragma unroll
  for (int t = 0; t < 2; ++t)
#pragma unroll
    for (int s = 0; s < 2; ++s) am[t][s] += ad[s];
  prod_store(av, 0, ks, J, lane, wsrd);
  prod_store(am, 1, ks, J, lane, wsrd);
}

constexpr unsigned CODE_BYTES = 93440;
DI void code_touch(unsigned i, unsigned ldsdst) {
  unsigned long long a; int o_;
  asm volatile("s_getpc_b64 %0\n s_mov_b32 %1, _Z11attn_kernelPcPKiS1_PKtS3_PtS3_S3_S3_S3_S3_@rel32@lo+4" : "=&s"(a), "=&s"(o_));
  a += (long long)o_;
  asm volatile("s_mov_b32 m0, %2\n s_nop 0\n global_load_lds_dword %0, %1" :: "v"(min(i * 128u, CODE_BYTES - 4u)), "s"(a), "s"(ldsdst) : "m0", "memory");
}

constexpr int LONGS = 1056;
typedef __attribute__((address_space(3))) unsigned* lds_u32_p;
typedef __attribute__((address_space(3))) u16* lds_u16_p;
DI void local_store(srd_t srd, int node, int lane, f32x4 acc, float di) {
  const int n = lane & 15, g = lane >> 4;
  const u32x2 o = {pk2(acc[0] * di, acc[1] * di), pk2(acc[2] * di, acc[3] * di)};
  __builtin_amdgcn_raw_buffer_store_b64(o, srd, WS_G + node * 512 + (g >> 1) * 256 + n * 16 + (g & 1) * 8, 0, 16);
}
DI void list_branch(char* lds, int j, int wid, const int* __restrict__ src, const int* __restrict__ dst, const u16* __restrict__ deg_inv,
                    const u16* wv, const u16* wm, const u16* wo, char* ws) {
  const unsigned rec = (unsigned)(size_t)lds + wid * 33280, ext = (unsigned)(size_t)lds + 0x20800 + wid * 6912;
  int lane; asm volatile("v_mbcnt_lo_u32_b32 %0, -1, 0\n\tv_mbcnt_hi_u32_b32 %0, -1, %0" : "=v"(lane));
  const int tid = wid * 64 + lane, bin = j * 4 + wid, lo = bin * 640;
  code_touch(CODE_BYTES / 128 - tid, (unsigned)(size_t)lds + 0x27f00u);
  const srd_t msrd = __builtin_amdgcn_make_buffer_rsrc(ws + WS_H, 0, 10240 * 512, 0x00020000);
  const srd_t wsrd = __builtin_amdgcn_make_buffer_rsrc(ws, 0, WS_END, 0x00020000);
  const unsigned tag = __builtin_amdgcn_raw_buffer_load_b32(wsrd, WS_EP, 0, 0) + 2u;
  if (lane < 3) st_flag(wsrd, lane == 2 ? WS_WF + 32 : WS_LF + bin * 128 + lane * 64 + 8, 0);
  unsigned lr[16];
#pragma unroll
  for (int r = 0; r < 16; ++r) lr[r] = reinterpret_cast<const unsigned*>((wid >= 2 ? wm : wv) + (240 + r) * DD)[tid & 127];
  unsigned dv[10];
#pragma unroll
  for (int i = 0; i < 10; ++i) dv[i] = (unsigned)deg_inv[min(lo + 64 * i + lane, NN - 1)];
#pragma unroll
  for (int i = 0; i < 10; ++i) ((lds_u32_p)ext)[64 * i + lane] = dv[i] << 16 | (lo + 64 * i + lane < NN ? lo + 64 * i + lane : 0xffff);
  corr(reinterpret_cast<unsigned*>(lds + 0x26c00) + 512 + (wid >> 1) * 256 + (tid & 127), lr);
  const u32x4 none = {~0u, ~0u, ~0u, ~0u};
  auto clear = [&]() {
    unsigned a = rec + lane * 16, r = rec + lane * 52;
    asm volatile("" : "+v"(a), "+v"(r));
#pragma unroll
    for (int i = 0; i < 33; ++i) *(__attribute__((address_space(3))) u32x4*)(a + (i < 32 ? i * 1024 : 32256)) = none;
#pragma unroll
    for (int i = 0; i < 10; ++i) ((lds_u32_p)(r + 64 * 52 * i))[12] = 46;
  };
  clear();
  __builtin_amdgcn_s_sleep(40);
  wait_flag(wsrd, WS_BF + min(tid, NATT - 1) * 128, tag);
  __syncthreads();
  auto take = [&](const u32x4& e0, const u32x4& e1, unsigned (&a)[8], unsigned (&p)[8]) {
#pragma unroll
    for (int r = 0; r < 8; ++r) asm("v_mad_u32_u16 %0, %1, 52, %2 op_sel:[1,0,0,0]" : "=v"(a[r]) : "v"(r < 4 ? e0[r & 3] : e1[r & 3]), "s"(rec));
    SB();
#pragma unroll
    for (int r = 0; r < 8; ++r) p[r] = __atomic_fetch_add((lds_u32_p)a[r] + 12, -2u, __ATOMIC_RELAXED);
    SB();
  };
  auto fill = [&](const u32x4& e0, const u32x4& e1, unsigned (&a)[8], const unsigned (&p)[8]) {
#pragma unroll
    for (int r = 7; r >= 0; --r) a[r] = __builtin_elementwise_add_sat(a[r], p[r]);
    SB();
#pragma unroll
    for (int r = 7; r >= 0; --r) *(lds_u16_p)a[r] = r < 4 ? e0[r & 3] : e1[r & 3];
    SB();
  };
  auto add8 = [&](u32x4 e0, u32x4 e1) { unsigned a[8], p[8]; take(e0, e1, a, p); fill(e0, e1, a, p); };
  const unsigned bo = WS_BL + bin * 5 * 260 * 64 + lane * 16;
  auto ld = [&](int g, int k) { return __builtin_amdgcn_raw_buffer_load_b128(wsrd, bo + (g * 260 + k * 16) * 64, 0, 16); };
  auto fix = [&](u32x4 e, unsigned last) { return e | (unsigned)(__builtin_amdgcn_mov_dpp(last, 0xff, 15, 15, 0) >> 31); };
  bool ovf = false;
  unsigned big = 0; int off[10], tot = 0;
#pragma unroll 1
  for (int pass = 0; pass < 2; ++pass) {
    if (!ovf) {
      u32x4 c[64];
#pragma unroll
      for (int k = 0; k < 16; ++k) c[k] = ld(0, k);
      unsigned m = ~0u, a[2][8], p[2][8];
#pragma unroll
      for (int k = 0; k < 66; k += 2) {
        SB();
        if (k < 64) {
          if (k >= 48) { c[k] = fix(c[k], c[k - 16][3]); c[k + 1] = fix(c[k + 1], c[k - 15][3]); }
          if ((k & 15) == 14 && lane >= 48) c[k + 1] = none;
          if (k >= 48) m &= c[k][3] & c[k + 1][3];
          take(c[k], c[k + 1], a[k >> 1 & 1], p[k >> 1 & 1]);
        }
        if (k < 16) {
#pragma unroll
          for (int i = 16 + 2 * k; i < 20 + 2 * k; ++i) c[i] = ld(i >> 4, i & 15);
        }
        if (k >= 24 && k < 40) {
#pragma unroll
          for (int i = k - 24; i < k - 22; ++i)
            c[48 + i] = __builtin_amdgcn_raw_buffer_load_b128(wsrd, bo + (780 + i * 16) * 64 + (__builtin_amdgcn_mov_dpp(c[32 + i][3], 0xff, 15, 15, 0) & 0x40000000), 0, 16);
        }
        SB();
        if (k) fill(c[k - 2], c[k - 1], a[~k >> 1 & 1], p[~k >> 1 & 1]);
      }
      SB();
      if (__ballot((lane & 3) == 3 && m != ~0u)) {
        unsigned more = 0;
#pragma unroll
        for (int k = 0; k < 16; ++k) more |= __ballot((lane & 3) == 3 && c[48 + k][3] != ~0u) ? 1u << k : 0u;
#pragma unroll 1
        for (; more; more &= more - 1) {
          const int k = __builtin_ctz(more);
          u32x4 e = ld(4, k);
          if (k == 15 && lane >= 48) e = none;
          ovf |= __ballot((lane & 3) == 3 && e[3] != ~0u) != 0;
          add8(e, none);
        }
      }
      if (ovf) clear();
    }
    if (ovf) {
      for (int q = lane; q < EE / 4; q += 64) {
        const u32x4 d = reinterpret_cast<const u32x4*>(dst)[q], s = reinterpret_cast<const u32x4*>(src)[q];
        u32x4 e;
#pragma unroll
        for (int r = 0; r < 4; ++r) e[r] = d[r] - lo < 640u ? (d[r] - lo) << 16 | s[r] : ~0u;
        add8(e, none);
      }
    }
    if (pass) break;
    asm volatile("" : "+v"(lane));
    const int qd = lane & 3, nl = lane >> 2;
    int inc = 0;
#pragma unroll
    for (int i = 0; i < 10; ++i) off[i] = 46 - (int)((lds_u32_p)(rec + (64 * i + lane) * 52))[12];
#pragma unroll
    for (int i = 0; i < 10; ++i) {
      const int cn = off[i];
      off[i] = cn > 48 ? cn : 0; inc += off[i];
      if (cn > 48) { big |= 1u << i; ((lds_u16_p)ext)[2 * (64 * i + lane)] = 0xffff; }
    }
    __builtin_amdgcn_wave_barrier();
    auto put = [&](int odd) {
#pragma unroll 2
      for (int bl = 0; bl < 4; ++bl) {
        u32x4 o[10];
#pragma unroll
        for (int T = 0; T < 10; ++T) if (((2 * T + 1) / 5 & 1) == odd) {
          const lds_u32_p r = (lds_u32_p)(rec + (bl * 160 + T * 16 + nl) * 52);
          o[T] = (u32x4){r[3 * qd], r[1 + 3 * qd], r[2 + 3 * qd], ((lds_u32_p)ext)[bl * 160 + T * 16 + nl]};
        }
#pragma unroll
        for (int T = 0; T < 10; ++T) if (((2 * T + 1) / 5 & 1) == odd) {
          const int c = (2 * T + 1) / 5, ro = 16 * (T - (5 * c >> 1)) + nl, w = c & 1 ? ro * 43 >> 9 : ro >> 3;
          __builtin_amdgcn_raw_buffer_store_b128(o[T], wsrd, WS_GL + c * 4096 + w * (c & 1 ? 256 : 512) + ro * 64 + qd * 16, (bin * 4 + bl) * 16384, 16);
        }
      }
    };
    put(1);
    asm volatile("" ::: "memory");
    put(0);
    asm volatile("s_waitcnt vmcnt(16)" ::: "memory");
    if (lane == 0) st_flag(wsrd, WS_LF + bin * 128 + 64, tag);
#pragma unroll
    for (int o = 1; o < 64; o <<= 1) { const int y = __shfl_up(inc, o); if (lane >= o) inc += y; }
    tot = __builtin_amdgcn_readlane(inc, 63);
    drain();
    if (lane == 0) st_flag(wsrd, WS_LF + bin * 128, tag);
    __syncthreads();
    int j2 = j * 4, w2 = wid, l2 = lane;
    asm volatile("" : "+s"(j2), "+s"(w2), "+v"(l2));
    product_load(lds, w2, l2, j2, wv, wm, wo, true, true);
#pragma unroll 1
    for (int job = 0; job < 4; ++job) {
      if (job) asm volatile("s_waitcnt vmcnt(4)" ::: "memory"); else drain();
      __syncthreads();
      asm volatile("" : "+s"(w2));
      if (job < 3) product_load(lds + (~job & 1) * 32768, w2, l2, j2 + job + 1, wv, wm, wo, false, true);
      product_w(lds, w2, l2, j2 + job, wsrd);
    }
    if (w2 == 0) *(lds_u32_p)(ext - 8) = 0;
    __syncthreads();
    if (tot == 0 || tot > 2 * LONGS) break;
#pragma unroll
    for (int i = 0; i < 10; ++i) {
      const unsigned r = rec + (64 * i + lane) * 52;
      ((lds_u32_p)r)[12] = off[i] ? ext + 2558 + inc - r : -2u;
      inc -= off[i]; off[i] = off[i] << 15 | inc;
    }
  }
  __syncthreads();
  const unsigned ex0 = (unsigned)(size_t)lds + 0x20800;
  const lds_u32_p lng = (lds_u32_p)(size_t)(unsigned)(size_t)lds;
#pragma unroll
  for (int i = 0; i < 10; ++i)
    if (big >> i & 1) {
      const unsigned q = __atomic_fetch_add((lds_u32_p)(ex0 - 8), 1u, __ATOMIC_RELAXED);
      lng[2 * q] = tot > 2 * LONGS ? ~0u : off[i] + wid * 6912 + 2560; lng[2 * q + 1] = (((lds_u32_p)ext)[64 * i + lane] & 0xffff0000u) | (lo + 64 * i + lane);
    }
  __syncthreads();
  const int nl_ = *(lds_u32_p)(ex0 - 8);
  bf16x8 b1;
#pragma unroll
  for (int e = 0; e < 8; ++e) b1[e] = (lane & 15) == e + ((lane >> 1) & 8) ? 0x3f80 : 0;
  const unsigned lo16 = (lane & 31) * 16;
  {
    u32x4 d[3][16]; unsigned nn[3]; int mode[3], q = wid, hf = 0;
    auto issue = [&](u32x4 (&v)[16], unsigned& n, int& md) {
      const unsigned w = __builtin_amdgcn_readfirstlane(lng[2 * q]);
      n = lng[2 * q + 1];
      const int dg = q < nl_ && w >> 16 <= 64 ? w >> 16 : 0, g = dg - hf - (lane >> 5);
      const lds_u16_p f = (lds_u16_p)(ex0 + (w & 0xffffu)) + hf + (lane >> 5);
      md = q >= nl_ ? -1 : dg == 0 ? 0 : dg - hf > 32 ? 1 : 2;
      hf = md == 1 ? 32 : 0; q += md == 1 ? 0 : 4;
      unsigned id[16];
#pragma unroll
      for (int t = 0; t < 16; ++t) id[t] = f[2 * t];
#pragma unroll
      for (int t = 0; t < 16; ++t) { asm volatile("" : "+v"(id[t])); v[t] = __builtin_amdgcn_raw_buffer_load_b128(msrd, (2 * t < g ? id[t] : 0xffffu) * 512u + lo16, 0, 16); }
    };
    issue(d[0], nn[0], mode[0]); issue(d[1], nn[1], mode[1]);
    f32x4 keep = {};
#pragma unroll 1
    for (bool go = true; go;) {
#pragma unroll
      for (int s = 0; s < 3; ++s) {
        if (mode[s] < 0) { go = false; break; }
        issue(d[(s + 2) % 3], nn[(s + 2) % 3], mode[(s + 2) % 3]); SB();
        f32x4 acc = {};
#pragma unroll
        for (int t = 0; t < 16; ++t) acc = MFMA16(b1, as_bf(d[s][t]), acc);
        local_store(wsrd, mode[s] == 2 ? nn[s] & 0xffffu : 0x200000u, lane, acc + keep, bfhi(nn[s]));
        keep = mode[s] == 1 ? acc : (f32x4){0.f, 0.f, 0.f, 0.f}; SB();
      }
    }
  }
#pragma unroll 1
  for (int q0 = 0; q0 < nl_; q0 += 256)
#pragma unroll 1
  for (unsigned long long m = __ballot(q0 + 4 * lane + 3 - wid < nl_ && lng[2 * (q0 + 4 * lane + 3 - wid)] >> 16 > 64); m; m &= m - 1) {
    const int q = q0 + 4 * __builtin_ctzll(m) + 3 - wid;
    const unsigned w = __builtin_amdgcn_readfirstlane(lng[2 * q]), n = __builtin_amdgcn_readfirstlane(lng[2 * q + 1]); const int dg = w >> 16;
    f32x4 acc = {};
    if (w == ~0u) {
      for (int e0 = 0; e0 < EE; e0 += 64) {
        unsigned long long h = __ballot(dst[e0 + lane] == (n & 0xffffu));
        while (h) {
          const int e = e0 + __builtin_ctzll(h); h &= h - 1;
          const unsigned vo = lane < 32 ? (unsigned)src[e] * 512 + lo16 : 0x7fffff00u;
          acc = MFMA16(b1, as_bf(__builtin_amdgcn_raw_buffer_load_b128(msrd, vo, 0, 16)), acc);
        }
      }
    } else {
      const lds_u16_p f = (lds_u16_p)(ex0 + (w & 0xffffu)), u = (lds_u16_p)(ext + 2624 + 2 * LONGS);
      for (int x = lane; x < dg; x += 64) {
        const int key = f[x]; int p = 0;
#pragma unroll 8
        for (int t = 0; t < dg; ++t) { const int v = f[t]; p += v < key || (v == key && t < x); }
        u[p] = key;
      }
      __builtin_amdgcn_wave_barrier();
      for (int x = lane >> 5; x < dg + (lane >> 5); x += 32) {
        u32x4 d[16]; unsigned id[16]; f32x4 a2 = {};
#pragma unroll
        for (int t = 0; t < 16; ++t) id[t] = u[min(x + 2 * t, dg)];
#pragma unroll
        for (int t = 0; t < 16; ++t) { asm volatile("" : "+v"(id[t])); d[t] = __builtin_amdgcn_raw_buffer_load_b128(msrd, (x + 2 * t < dg ? id[t] : 0xffffu) * 512u + lo16, 0, 16); }
#pragma unroll
        for (int t = 0; t < 16; ++t) a2 = MFMA16(b1, as_bf(d[t]), a2);
        acc += a2;
      }
      __builtin_amdgcn_wave_barrier();
    }
    local_store(wsrd, n & 0xffffu, lane, acc, bfhi(n));
  }
  drain();
  __syncthreads();
  if (wid == 0 && lane == 0) st_flag(wsrd, WS_WF + j * 4, tag);
}

DI void exact_rows(char* lds, int b, int wid, char* ws, const u16* x, u16* out) {
  int lane; asm volatile("v_mbcnt_lo_u32_b32 %0, -1, 0\n\tv_mbcnt_hi_u32_b32 %0, -1, %0" : "=v"(lane));
  const srd_t w = __builtin_amdgcn_make_buffer_rsrc(ws, 0, WS_END, 0x00020000);
  const srd_t xs = __builtin_amdgcn_make_buffer_rsrc(const_cast<u16*>(x), 0, NN * 512, 0x00020000);
  const srd_t os = __builtin_amdgcn_make_buffer_rsrc(out, 0, NN * 512, 0x00020000);
  const int n = lane & 15, g = lane >> 4, c = b & 3;
  f32x4* red = reinterpret_cast<f32x4*>(lds);
  float* mx = reinterpret_cast<float*>(lds + 65536);
  auto ld = [&](unsigned off) { return as_bf(__builtin_amdgcn_raw_buffer_load_b128(w, off, 0, 16)); };
#pragma unroll 1
  for (int i = 0; i < 2 + (c & 1); ++i) {
    const int tile = (b >> 2) * 10 + (5 * c >> 1) + i;
    bf16x8 q[8];
#pragma unroll
    for (int ks = 0; ks < 8; ++ks) q[ks] = ld(WS_QP + tile * 8192 + ks * 1024 + lane * 16);
    auto score = [&](int kt) {
      f32x4 s = {};
#pragma unroll
      for (int ks = 0; ks < 8; ++ks) s = MFMA16(ld(WS_H + (kt * 16 + n) * 512 + ks * 64 + g * 16), q[ks], s);
      return s;
    };
    float m = -__builtin_inff();
#pragma unroll 1
    for (int kt = wid; kt < NN / 16; kt += 4) { const f32x4 s = score(kt); m = fmaxf(fmaxf(m, fmaxf(s[0], s[1])), fmaxf(s[2], s[3])); }
    __syncthreads();
    mx[wid * 64 + lane] = m;
    __syncthreads();
#pragma unroll
    for (int r = 0; r < 16; ++r) m = fmaxf(m, mx[r * 16 + n]);
    f32x4 acc[16] = {};
#pragma unroll 1
    for (int kt = wid; kt < NN / 16; kt += 4) {
      const f32x4 s = score(kt);
      const u32x2 p = {pk2(__builtin_amdgcn_exp2f(s[0] - m), __builtin_amdgcn_exp2f(s[1] - m)), pk2(__builtin_amdgcn_exp2f(s[2] - m), __builtin_amdgcn_exp2f(s[3] - m))};
#pragma unroll
      for (int dt = 0; dt < 16; ++dt) {
        bf16x4 v;
#pragma unroll
        for (int e = 0; e < 4; ++e) v[e] = __builtin_amdgcn_raw_buffer_load_b16(w, WS_H + (kt * 16 + 4 * g + e) * 512 + n * 2, dt * 32, 16);
        acc[dt] = __builtin_amdgcn_mfma_f32_16x16x16bf16_1k(v, __builtin_bit_cast(bf16x4, p), acc[dt], 0, 0, 0);
      }
    }
#pragma unroll
    for (int dt = 0; dt < 16; ++dt) red[(wid * 16 + dt) * 64 + lane] = acc[dt];
    __syncthreads();
    float l = 0.f;
#pragma unroll
    for (int r = 0; r < 4; ++r) l += red[(r * 16 + 15) * 64 + 48 + n][3];
    l = 1.f / l;
    f32x4 y[4] = {};
#pragma unroll 1
    for (int ks = 0; ks < 8; ++ks) {
      f32x4 o[2];
#pragma unroll
      for (int h = 0; h < 2; ++h) o[h] = (red[(2 * ks + h) * 64 + lane] + red[(16 + 2 * ks + h) * 64 + lane] + red[(32 + 2 * ks + h) * 64 + lane] + red[(48 + 2 * ks + h) * 64 + lane]) * l;
      const bf16x8 of = as_bf((u32x4){pk2(o[0][0], o[0][1]), pk2(o[0][2], o[0][3]), pk2(o[1][0], o[1][1]), pk2(o[1][2], o[1][3])});
      const bf16x8 gf = ld(WS_G + (tile * 16 + n) * 512 + ks * 64 + g * 16);
#pragma unroll
      for (int et = 0; et < 4; ++et) {
        y[et] = MFMA16(ld(WS_WV + wid * 32768 + ks * 4096 + et * 1024 + lane * 16), of, y[et]);
        y[et] = MFMA16(ld(WS_WM + wid * 32768 + (et < 2 ? 16384 : 0) + ks * 2048 + (et & 1) * 1024 + lane * 16), gf, y[et]);
      }
    }
#pragma unroll
    for (int jj = 0; jj < 2; ++jj) {
      const unsigned at = (tile * 16 + n) * 512 + wid * 128 + jj * 64 + g * 16;
      const u32x4 xv = __builtin_amdgcn_raw_buffer_load_b128(xs, at, 0, 0);
      u32x4 o;
#pragma unroll
      for (int r = 0; r < 4; ++r) o[r] = pk2(y[2 * jj + (r >> 1)][2 * (r & 1)] + bf2f(xv[r] & 0xffffu), y[2 * jj + (r >> 1)][2 * (r & 1) + 1] + bfhi(xv[r]));
      __builtin_amdgcn_raw_buffer_store_b128(o, os, at, 0, 0);
    }
  }
}

__global__ __launch_bounds__(256, 1) void attn_kernel(char* ws, const int* __restrict__ src, const int* __restrict__ dst,
                                                      const u16* __restrict__ deg_inv, const u16* __restrict__ x, u16* out,
                                                      const u16* __restrict__ wq, const u16* __restrict__ wk, const u16* __restrict__ wv,
                                                      const u16* __restrict__ wm, const u16* __restrict__ wo) {
  extern __shared__ __attribute__((aligned(16))) char lds[];
  static_assert(CODE_BYTES > 65536 + 16384, "range of the cache warm-up");
  int b = blockIdx.x;
  int wid = __builtin_amdgcn_readfirstlane(threadIdx.x >> 6);
  static_assert(WS_END == 37557248 && WS_EP == 5777664, "workspace layout of the generated code");
  auto ka = __builtin_amdgcn_kernarg_segment_ptr();
  asm volatile(
    "s_load_dwordx2 s[20:21], %0, 0x30\n"
    "s_load_dwordx2 s[24:25], %0, 0x38\n"
    "s_load_dwordx2 s[12:13], %0, 0x20\n"
    "s_load_dwordx2 s[8:9], %0, 0x0\n"
    "s_load_dwordx2 s[16:17], %0, 0x28\n"
    "s_load_dwordx4 s[28:31], %0, 0x8\n"
    "v_mbcnt_lo_u32_b32 v24, -1, 0\n"
    "v_mbcnt_hi_u32_b32 v24, -1, v24\n"
    "s_and_b32 s45, %1, 6\n"
    "s_lshl_b32 s46, s45, 5\n"
    "s_lshr_b32 s47, %1, 3\n"
    "s_getpc_b64 s[62:63]\n"
    "s_lshl1_add_u32 s46, s47, s46\n"
    "s_and_b32 s47, s47, 3\n"
    "s_lshl2_add_u32 s47, s47, %2\n"
    "s_lshl_b32 s47, s47, 11\n"
    "s_add_u32 m0, %3, 0x27f00\n"
    "v_lshl_add_u32 v29, v24, 5, s47\n"
    "global_load_lds_dword v29, s[62:63]\n"
    "s_add_u32 s48, s45, 2\n"
    "s_lshr_b32 s48, s48, 2\n"
    "s_lshl_b32 s48, s48, 1\n"
    "s_sub_u32 s46, s46, s48\n"
    "s_add_u32 s48, s48, 252\n"
    "s_and_b32 s45, %1, 250\n"
    "s_cmpk_eq_u32 s45, 248\n"
    "s_cselect_b32 s46, s48, s46\n"
    "s_and_b32 %1, %1, 1\n"
    "s_or_b32 %1, %1, s46\n"
    "s_cmpk_ge_u32 %1, 252\n"
    "s_cbranch_scc1 .Lat_end\n"
    "s_and_b32 s50, %2, 1\n"
    "s_lshr_b32 s51, %2, 1\n"
    "v_lshrrev_b32_e32 v25, 3, v24\n"
    "v_and_b32_e32 v26, 6, v25\n"
    "v_and_b32_e32 v27, 7, v24\n"
    "v_xor_b32_e32 v26, v26, v27\n"
    "v_lshlrev_b32_e32 v26, 4, v26\n"
    "v_and_b32_e32 v27, 4, v25\n"
    "v_add_u32_e32 v27, v27, v25\n"
    "v_lshl_add_u32 v254, v27, 9, v26\n"
    "v_add_u32_e32 v255, 0x1c00, v254\n"
    "s_mov_b32 s22, 0x20000\n"
    "s_mov_b32 s23, 0x20000\n"
    "s_mov_b32 s26, 0x20000\n"
    "s_mov_b32 s27, 0x20000\n"
    "s_lshr_b32 s36, %1, 7\n"
    "s_mul_i32 s36, s36, 3\n"
    "s_add_u32 s36, s36, %1\n"
    "s_and_b32 s36, s36, 127\n"
    "s_lshl_b32 s45, %2, 7\n"
    "s_lshl_b32 s46, %2, 12\n"
    "s_add_u32 s46, s46, %3\n"
    "s_and_b32 s47, s36, 7\n"
    "s_lshl_b32 s47, s47, 14\n"
    "s_add_u32 m0, s46, 32768\n"
    "s_add_u32 s47, s47, s45\n"
    "s_and_b32 s32, %1, 3\n"
    "s_lshr_b32 s33, %1, 2\n"
    "s_mul_i32 s38, s32, 5\n"
    "s_lshr_b32 s38, s38, 1\n"
    "s_mul_i32 s35, s33, 10\n"
    "s_add_u32 s35, s35, s38\n"
    "s_lshl_b32 s35, s35, 13\n"
    "s_mov_b32 s14, 5120000\n"
    "s_mov_b32 s15, 0x20000\n"
    "s_waitcnt lgkmcnt(0)\n"
    "buffer_load_dwordx4 v254, s[20:23], s47 offen offset:0 lds\n"
    "s_add_u32 s48, s47, 1024\n"
    "buffer_load_dwordx4 v255, s[20:23], s47 offen offset:3072 lds\n"
    "s_add_u32 s47, s47, 0x1800\n"
    "buffer_load_dwordx4 v254, s[20:23], s48 offen offset:1024 lds\n"
    "buffer_load_dwordx4 v254, s[20:23], s47 offen offset:2048 lds\n"
    "s_lshr_b32 s47, s36, 4\n"
    "s_lshl_b32 s47, s47, 14\n"
    "s_and_b32 s48, s36, 8\n"
    "s_lshl_b32 s48, s48, 8\n"
    "s_add_u32 s47, s47, s48\n"
    "s_add_u32 m0, s46, 49152\n"
    "s_add_u32 s47, s47, s45\n"
    "buffer_load_dwordx4 v254, s[24:27], s47 offen offset:0 lds\n"
    "buffer_load_dwordx4 v255, s[24:27], s47 offen offset:1024 lds\n"
    "s_add_u32 s47, s45, 120832\n"
    "buffer_load_dwordx4 v254, s[24:27], s47 offen offset:2048 lds\n"
    "s_add_u32 s47, s47, 1024\n"
    "buffer_load_dwordx4 v254, s[24:27], s47 offen offset:3072 lds\n"
    "s_mul_i32 s46, %1, 159\n"
    "s_lshl_b32 s45, s50, 6\n"
    "v_add_u32_e32 v111, s45, v24\n"
    "v_add_u32_e32 v108, s46, v111\n"
    "v_min_u32_e32 v108, 39999, v108\n"
    "v_lshlrev_b32_e32 v108, 4, v108\n"
    "s_lshl1_add_u32 s47, s46, 256\n"
    "v_add_u32_e32 v109, s47, v111\n"
    "v_min_u32_e32 v109, 79999, v109\n"
    "v_lshlrev_b32_e32 v109, 3, v109\n"
    "s_cmp_lg_u32 s51, 0\n"
    "s_cselect_b64 exec, -1, 1\n"
    "global_load_dwordx4 v[96:99], v108, s[30:31]\n"
    "global_load_dwordx2 v[92:93], v109, s[30:31]\n"
    "global_load_dwordx4 v[100:103], v108, s[28:29]\n"
    "global_load_dwordx2 v[94:95], v109, s[28:29]\n"
    "s_mov_b64 exec, -1\n"
    "s_lshl_b32 s45, %2, 6\n"
    "v_add_u32_e32 v108, s45, v24\n"
    "s_sub_u32 s59, 40000, s46\n"
    "s_lshl1_add_u32 s61, s59, -256\n"
    "s_min_i32 s61, s61, 62\n"
    "s_lshl_b32 s45, %2, 2\n"
    "s_add_u32 s54, s35, 0x2000\n"
    "s_add_u32 s55, s35, 0x4000\n"
    "v_lshlrev_b32_e32 v253, 4, v24\n"
    "v_lshrrev_b32_e32 v28, 4, v24\n"
    "v_add_u32_e32 v28, s45, v28\n"
    "v_and_b32_e32 v104, 0xf0, v253\n"
    "v_lshl_add_u32 v104, v28, 9, v104\n"
    "buffer_load_dwordx4 v[0:3], v104, s[12:15], s35 offen offset:0\n"
    "buffer_load_dwordx4 v[4:7], v104, s[12:15], s35 offen offset:256\n"
    "s_bitcmp1_b32 %1, 0\n"
    "s_cselect_b32 s60, s55, 2.0\n"
    "buffer_load_dwordx4 v[8:11], v104, s[12:15], s54 offen offset:0\n"
    "buffer_load_dwordx4 v[12:15], v104, s[12:15], s54 offen offset:256\n"
    "buffer_load_dwordx4 v[16:19], v104, s[12:15], s55 offen offset:0\n"
    "buffer_load_dwordx4 v[20:23], v104, s[12:15], s55 offen offset:256\n"
    "s_mov_b32 s10, 37557248\n"
    "s_mov_b32 s11, 0x20000\n"
    "s_mov_b32 s37, 5777664\n"
    "s_buffer_load_dword s39, s[8:11], s37\n"
    "s_bfe_u32 s37, %1, 0x20001\n"
    "v_and_b32_e32 v27, 1, v25\n"
    "v_lshl_add_u32 v248, v27, 9, v26\n"
    "v_lshrrev_b32_e32 v27, 1, v25\n"
    "v_lshl_add_u32 v248, v27, 12, v248\n"
    "s_mul_i32 s40, s32, 39\n"
    "s_and_b32 s34, s32, 1\n"
    "s_add_u32 s40, s40, s34\n"
    "s_lshl1_add_u32 s40, s40, s51\n"
    "s_lshl_b32 s40, s40, 14\n"
    "s_lshl_b32 s47, s50, 8\n"
    "s_add_u32 s40, s40, s47\n"
    "s_add_u32 s41, s40, 128\n"
    "s_xor_b32 s34, s34, 1\n"
    "s_lshl_b32 s34, s34, 15\n"
    "s_xor_b32 s43, s50, s51\n"
    "s_lshl3_add_u32 s43, s50, s43\n"
    "s_lshl_b32 s43, s43, 13\n"
    "s_add_u32 s47, %3, 32768\n"
    "s_add_u32 s43, s43, s47\n"
    "v_bfe_u32 v25, v24, 2, 3\n"
    "v_lshlrev_b32_e32 v244, 7, v25\n"
    "v_lshrrev_b32_e32 v26, 5, v24\n"
    "v_lshl_add_u32 v244, v26, 10, v244\n"
    "v_and_b32_e32 v25, 6, v25\n"
    "v_bfe_u32 v26, v24, 1, 1\n"
    "v_xor_b32_e32 v25, v25, v26\n"
    "v_lshl_add_u32 v244, v25, 4, v244\n"
    "v_and_b32_e32 v26, 1, v24\n"
    "v_lshl_add_u32 v244, v26, 3, v244\n"
    "s_lshl_b32 s48, s51, 16\n"
    "s_add_u32 s48, s48, s47\n"
    "v_add_u32_e32 v244, s48, v244\n"
    "v_xor_b32_e32 v245, 32, v244\n"
    "v_xor_b32_e32 v246, 64, v244\n"
    "v_xor_b32_e32 v247, 0x60, v244\n"
    "v_and_b32_e32 v25, 7, v24\n"
    "v_lshlrev_b32_e32 v27, 7, v25\n"
    "v_bfe_u32 v26, v24, 3, 1\n"
    "v_lshl_add_u32 v27, v26, 10, v27\n"
    "v_and_b32_e32 v25, 6, v24\n"
    "v_lshrrev_b32_e32 v26, 4, v24\n"
    "v_xor_b32_e32 v25, v25, v26\n"
    "v_lshl_add_u32 v27, v25, 4, v27\n"
    "s_lshl_b32 s48, s51, 13\n"
    "s_add_u32 s48, s48, s47\n"
    "v_add_u32_e32 v240, s48, v27\n"
    "v_xor_b32_e32 v241, 64, v240\n"
    "v_and_b32_e32 v29, 15, v24\n"
    "s_cmp_ge_u32 s36, 112\n"
    "s_cselect_b32 s47, 8, 16\n"
    "v_cmp_le_u32_e32 vcc, s47, v29\n"
    "v_mov_b32_e32 v64, 0xbd80bd80\n"
    "v_mov_b32_e32 v65, 0xbdabbdab\n"
    "v_mov_b32_e32 v66, 0x392b392b\n"
    "v_cndmask_b32_e32 v64, v64, v65, vcc\n"
    "v_cndmask_b32_e32 v66, 0, v66, vcc\n"
    "s_cselect_b32 s47, 56, 64\n"
    "v_cmp_le_u32_e32 vcc, s47, v24\n"
    "v_mov_b32_e32 v65, 0x3e80bdab\n"
    "v_mov_b32_e32 v67, 0x392b\n"
    "v_cndmask_b32_e32 v65, v64, v65, vcc\n"
    "v_cndmask_b32_e32 v67, v66, v67, vcc\n"
    "s_cmp_ge_u32 s36, 120\n"
    "s_cselect_b32 s47, 15, 16\n"
    "v_cmp_ne_u32_e32 vcc, s47, v29\n"
    "v_mov_b32_e32 v68, 0x3db8aa3b\n"
    "s_lshr_b32 s47, s36, 5\n"
    "v_cndmask_b32_e32 v68, 0, v68, vcc\n"
    "s_lshl_b32 s47, s47, 15\n"
    "s_and_b32 s48, s36, 7\n"
    "s_lshl_b32 s48, s48, 11\n"
    "s_add_u32 s47, s47, s48\n"
    "s_and_b32 s48, s36, 8\n"
    "s_lshl_b32 s48, s48, 7\n"
    "s_add_u32 s47, s47, s48\n"
    "s_and_b32 s48, s36, 16\n"
    "s_lshl_b32 s48, s48, 10\n"
    "s_add_u32 s47, s47, s48\n"
    "s_add_u32 s52, s47, 5242880\n"
    "s_lshl_b32 s48, s50, 9\n"
    "s_add_u32 s52, s52, s48\n"
    "s_lshl_b32 s48, s50, 11\n"
    "v_add_u32_e32 v72, s48, v240\n"
    "v_add_u32_e32 v73, s48, v241\n"
    "v_and_b32_e32 v74, 0x1f0, v253\n"
    "v_lshrrev_b32_e32 v29, 5, v24\n"
    "v_lshl_add_u32 v74, v29, 3, v74\n"
    "s_lshl_b32 s53, s36, 1\n"
    "s_add_u32 s53, s53, s50\n"
    "s_lshl_b32 s53, s53, 4\n"
    "s_add_u32 s53, s53, 5771264\n"
    "s_cmp_eq_u32 s51, 0\n"
    "s_cselect_b32 s52, s52, 2.0\n"
    "s_cselect_b32 s53, s53, 2.0\n"
    "s_lshl_b32 s47, %1, 4\n"
    "s_add_u32 s47, s47, 5701632\n"
    "s_lshl_b32 s48, %1, 7\n"
    "s_add_u32 s48, s48, 5705728\n"
    "s_cmp_eq_u32 s50, 0\n"
    "s_cselect_b32 s47, s47, s48\n"
    "s_cmp_eq_u32 s51, 0\n"
    "s_cselect_b32 s47, s53, s47\n"
    "buffer_store_dword v24, off, s[8:11], s47 offset:8 sc1\n"
    "v_lshrrev_b32_e32 v109, 2, v108\n"
    "v_mul_u32_u24_e32 v109, 0x4100, v109\n"
    "v_and_b32_e32 v110, 48, v253\n"
    "v_or_b32_e32 v109, v109, v110\n"
    "v_add_u32_e32 v110, 0x104000, v109\n"
    "v_cmp_gt_u32_e32 vcc, 64, v108\n"
    "s_add_u32 s44, %3, 24576\n"
    "v_lshl_add_u32 v108, v108, 4, s44\n"
    "v_cndmask_b32_e32 v110, 2.0, v110, vcc\n"
    "v_mov_b32_e32 v32, -1\n"
    "v_mov_b32_e32 v33, -1\n"
    "v_mov_b32_e32 v34, -1\n"
    "v_mov_b32_e32 v35, -1\n"
    "ds_write_b128 v108, v[32:35] offset:0\n"
    "ds_write_b128 v108, v[32:35] offset:1280\n"
    "v_and_b32_e32 v29, 6, v28\n"
    "v_xor_b32_e32 v29, v29, v24\n"
    "v_and_b32_e32 v29, 7, v29\n"
    "v_lshlrev_b32_e32 v105, 7, v28\n"
    "v_lshl_add_u32 v105, v29, 4, v105\n"
    "v_bfe_u32 v29, v24, 3, 1\n"
    "v_subrev_u32_e32 v29, s37, v29\n"
    "v_and_b32_e32 v29, 3, v29\n"
    "v_lshl_add_u32 v105, v29, 11, v105\n"
    "s_add_u32 s45, %3, 65536\n"
    "v_add_u32_e32 v105, s45, v105\n"
    "v_xor_b32_e32 v106, 0x1000, v105\n"
    "v_and_b32_e32 v29, 15, v24\n"
    "v_cmp_eq_u32_e32 vcc, 15, v29\n"
    "v_mov_b32_e32 v107, 0xffff\n"
    "s_mov_b32 s58, 0x3f803f80\n"
    "s_mov_b32 s57, 0x43c00000\n"
    "v_cndmask_b32_e32 v107, -1, v107, vcc\n"
    "s_mov_b32 s56, 0xc3c00000\n"
    "s_mov_b32 s62, 0xc000c000\n"
    "s_mov_b32 s63, 0xc000c000\n"
    "v_add_u32_e32 v250, s45, v27\n"
    "v_xor_b32_e32 v251, 64, v250\n"
    "s_lshl_b32 s47, %2, 10\n"
    "s_add_u32 s47, s47, 5771264\n"
    "v_lshl_add_u32 v252, v24, 4, s47\n"
    "s_waitcnt vmcnt(11)\n"
    "s_waitcnt lgkmcnt(0)\n"
    "s_barrier\n"
    "s_add_u32 s39, s39, 2\n"
    "s_cmp_lg_u32 s51, 0\n"
    "s_cbranch_scc1 .Lat_bin\n"
    "ds_read_b128 v[112:115], v72 offset:0\n"
    "ds_read_b128 v[208:211], v240 offset:18432\n"
    "ds_read_b128 v[176:179], v240 offset:16384\n"
    "ds_read_b128 v[116:119], v73 offset:0\n"
    "ds_read_b128 v[212:215], v241 offset:18432\n"
    "ds_read_b128 v[180:183], v241 offset:16384\n"
    "ds_read_b128 v[120:123], v72 offset:4096\n"
    "ds_read_b128 v[216:219], v240 offset:22528\n"
    "ds_read_b128 v[184:187], v240 offset:20480\n"
    "ds_read_b128 v[124:127], v73 offset:4096\n"
    "ds_read_b128 v[220:223], v241 offset:22528\n"
    "ds_read_b128 v[188:191], v241 offset:20480\n"
    "ds_read_b128 v[128:131], v72 offset:8192\n"
    "ds_read_b128 v[224:227], v240 offset:26624\n"
    "ds_read_b128 v[192:195], v240 offset:24576\n"
    "s_waitcnt lgkmcnt(12)\n"
    "v_mfma_f32_16x16x32_bf16 v[56:59], v[208:211], v[112:115], 0\n"
    "ds_read_b128 v[132:135], v73 offset:8192\n"
    "ds_read_b128 v[228:231], v241 offset:26624\n"
    "v_mfma_f32_16x16x32_bf16 v[48:51], v[112:115], v[176:179], 0\n"
    "ds_read_b128 v[196:199], v241 offset:24576\n"
    "s_waitcnt lgkmcnt(12)\n"
    "v_mfma_f32_16x16x32_bf16 v[56:59], v[212:215], v[116:119], v[56:59]\n"
    "ds_read_b128 v[136:139], v72 offset:12288\n"
    "ds_read_b128 v[232:235], v240 offset:30720\n"
    "v_mfma_f32_16x16x32_bf16 v[48:51], v[116:119], v[180:183], v[48:51]\n"
    "ds_read_b128 v[200:203], v240 offset:28672\n"
    "s_waitcnt lgkmcnt(12)\n"
    "v_mfma_f32_16x16x32_bf16 v[56:59], v[216:219], v[120:123], v[56:59]\n"
    "ds_read_b128 v[140:143], v73 offset:12288\n"
    "ds_read_b128 v[236:239], v241 offset:30720\n"
    "v_mfma_f32_16x16x32_bf16 v[48:51], v[120:123], v[184:187], v[48:51]\n"
    "ds_read_b128 v[204:207], v241 offset:28672\n"
    "s_waitcnt lgkmcnt(12)\n"
    "v_mfma_f32_16x16x32_bf16 v[56:59], v[220:223], v[124:127], v[56:59]\n"
    "v_mfma_f32_16x16x32_bf16 v[48:51], v[124:127], v[188:191], v[48:51]\n"
    "s_waitcnt lgkmcnt(9)\n"
    "v_mfma_f32_16x16x32_bf16 v[56:59], v[224:227], v[128:131], v[56:59]\n"
    "v_mfma_f32_16x16x32_bf16 v[48:51], v[128:131], v[192:195], v[48:51]\n"
    "s_waitcnt lgkmcnt(6)\n"
    "v_mfma_f32_16x16x32_bf16 v[56:59], v[228:231], v[132:135], v[56:59]\n"
    "v_mfma_f32_16x16x32_bf16 v[48:51], v[132:135], v[196:199], v[48:51]\n"
    "s_waitcnt lgkmcnt(3)\n"
    "v_mfma_f32_16x16x32_bf16 v[56:59], v[232:235], v[136:139], v[56:59]\n"
    "v_mfma_f32_16x16x32_bf16 v[48:51], v[136:139], v[200:203], v[48:51]\n"
    "s_waitcnt lgkmcnt(0)\n"
    "v_mfma_f32_16x16x32_bf16 v[56:59], v[236:239], v[140:143], v[56:59]\n"
    "v_mfma_f32_16x16x32_bf16 v[48:51], v[140:143], v[204:207], v[48:51]\n"
    "s_nop 3\n"
    "v_cvt_pk_bf16_f32 v56, v56, v57\n"
    "v_cvt_pk_bf16_f32 v57, v58, v59\n"
    "s_nop 1\n"
    "v_mfma_f32_16x16x16_bf16 v[48:51], v[56:57], v[64:65], v[48:51]\n"
    "v_mfma_f32_16x16x16_bf16 v[48:51], v[56:57], v[66:67], v[48:51]\n"
    "s_nop 7\n"
    "v_mul_f32_e32 v48, v48, v68\n"
    "v_mul_f32_e32 v49, v49, v68\n"
    "v_mul_f32_e32 v50, v50, v68\n"
    "v_mul_f32_e32 v51, v51, v68\n"
    "v_cvt_pk_bf16_f32 v48, v48, v49\n"
    "v_cvt_pk_bf16_f32 v49, v50, v51\n"
    "s_branch .Lat_noj2\n"
    ".Lat_apoll:\n"
    "s_mov_b32 s53, 0x400000\n"
    ".Lat_apoll_l:\n"
    "s_sleep 1\n"
    "buffer_load_dword v255, v252, s[8:11], 0 offen offset:0 sc1\n"
    "s_waitcnt vmcnt(0)\n"
    "v_cmp_ne_u32_e32 vcc, s39, v255\n"
    "s_sub_u32 s53, s53, 1\n"
    "s_cbranch_scc1 .Lat_apoll_ret\n"
    "s_nop 2\n"
    "s_cbranch_vccnz .Lat_apoll_l\n"
    "s_branch .Lat_apoll_ret\n"
    ".Lat_h1poll:\n"
    "s_mov_b32 s53, 0x400000\n"
    ".Lat_h1poll_l:\n"
    "s_sleep 1\n"
    "buffer_load_dword v0, v3, s[8:11], s59 offen offset:0 sc1\n"
    "s_waitcnt vmcnt(0)\n"
    "v_cmp_ne_u32_e32 vcc, s39, v0\n"
    "s_sub_u32 s53, s53, 1\n"
    "s_cbranch_scc1 .Lat_h1poll_ret\n"
    "s_nop 2\n"
    "s_cbranch_vccnz .Lat_h1poll_l\n"
    "s_branch .Lat_h1poll_ret\n"
    ".Lat_hpoll:\n"
    "s_mov_b32 s53, 0x400000\n"
    ".Lat_hpoll_l:\n"
    "s_sleep 1\n"
    "buffer_load_dword v0, v253, s[8:11], s59 offen offset:0 sc1\n"
    "buffer_load_dword v1, v2, s[8:11], s59 offen offset:0 sc1\n"
    "s_waitcnt vmcnt(0)\n"
    "v_xor_b32_e32 v1, s39, v1\n"
    "v_xor_b32_e32 v0, s39, v0\n"
    "v_or_b32_e32 v0, v0, v1\n"
    "v_cmp_ne_u32_e32 vcc, 0, v0\n"
    "s_sub_u32 s53, s53, 1\n"
    "s_cbranch_scc1 .Lat_hpoll_ret\n"
    "s_nop 2\n"
    "s_cbranch_vccnz .Lat_hpoll_l\n"
    "s_branch .Lat_hpoll_ret\n"
    ".Lat_qpoll:\n"
    "s_mov_b32 s53, 0x400000\n"
    ".Lat_qpoll_l:\n"
    "s_sleep 0\n"
    "buffer_load_dword v11, v5, s[8:11], s47 offen offset:0 sc1\n"
    "s_waitcnt vmcnt(0)\n"
    "v_cmp_ne_u32_e32 vcc, s39, v11\n"
    "s_sub_u32 s53, s53, 1\n"
    "s_cbranch_scc1 .Lat_qpoll_ret\n"
    "s_nop 2\n"
    "s_cbranch_vccnz .Lat_qpoll_l\n"
    "s_branch .Lat_qpoll_ret\n"
    ".Lat_bin:\n"
    "s_movk_i32 s45, -640\n"
    "s_movk_i32 s48, 80\n"
    "v_mov_b32_e32 v60, 1\n"
    "v_cmp_gt_u32_e32 vcc, s59, v111\n"
    "v_cmp_gt_i32_e64 s[46:47], s61, v111\n"
    "s_waitcnt vmcnt(9)\n"
    "v_mul_u32_u24_e32 v48, 0x6667, v96\n"
    "v_mul_u32_u24_e32 v49, 0x6667, v97\n"
    "v_mul_u32_u24_e32 v50, 0x6667, v98\n"
    "v_mul_u32_u24_e32 v51, 0x6667, v99\n"
    "v_mul_u32_u24_e32 v52, 0x6667, v92\n"
    "v_mul_u32_u24_e32 v53, 0x6667, v93\n"
    "v_lshrrev_b32_e32 v48, 24, v48\n"
    "v_lshrrev_b32_e32 v49, 24, v49\n"
    "v_lshrrev_b32_e32 v50, 24, v50\n"
    "v_lshrrev_b32_e32 v51, 24, v51\n"
    "v_lshrrev_b32_e32 v52, 24, v52\n"
    "v_lshrrev_b32_e32 v53, 24, v53\n"
    "v_min_u32_e32 v48, 15, v48\n"
    "v_min_u32_e32 v49, 15, v49\n"
    "v_min_u32_e32 v50, 15, v50\n"
    "v_min_u32_e32 v51, 15, v51\n"
    "v_min_u32_e32 v52, 15, v52\n"
    "v_min_u32_e32 v53, 15, v53\n"
    "v_lshl_add_u32 v54, v48, 2, s44\n"
    "v_lshl_add_u32 v55, v49, 2, s44\n"
    "v_lshl_add_u32 v56, v50, 2, s44\n"
    "v_lshl_add_u32 v57, v51, 2, s44\n"
    "v_lshl_add_u32 v58, v52, 2, s44\n"
    "v_lshl_add_u32 v59, v53, 2, s44\n"
    "s_mov_b64 exec, vcc\n"
    "ds_add_rtn_u32 v54, v54, v60 offset:5248\n"
    "ds_add_rtn_u32 v55, v55, v60 offset:5248\n"
    "ds_add_rtn_u32 v56, v56, v60 offset:5248\n"
    "ds_add_rtn_u32 v57, v57, v60 offset:5248\n"
    "s_mov_b64 exec, s[46:47]\n"
    "ds_add_rtn_u32 v58, v58, v60 offset:5248\n"
    "ds_add_rtn_u32 v59, v59, v60 offset:5248\n"
    "s_mov_b64 exec, -1\n"
    "s_waitcnt vmcnt(7)\n"
    "v_mad_i32_i24 v96, v48, s45, v96\n"
    "v_mad_i32_i24 v97, v49, s45, v97\n"
    "v_mad_i32_i24 v98, v50, s45, v98\n"
    "v_mad_i32_i24 v99, v51, s45, v99\n"
    "v_mad_i32_i24 v92, v52, s45, v92\n"
    "v_mad_i32_i24 v93, v53, s45, v93\n"
    "v_lshl_or_b32 v96, v96, 16, v100\n"
    "v_lshl_or_b32 v97, v97, 16, v101\n"
    "v_lshl_or_b32 v98, v98, 16, v102\n"
    "v_lshl_or_b32 v99, v99, 16, v103\n"
    "v_lshl_or_b32 v92, v92, 16, v94\n"
    "v_lshl_or_b32 v93, v93, 16, v95\n"
    "s_mov_b64 exec, vcc\n"
    "s_waitcnt lgkmcnt(0)\n"
    "v_min_i32_e32 v54, 78, v54\n"
    "v_min_i32_e32 v55, 78, v55\n"
    "v_min_i32_e32 v56, 78, v56\n"
    "v_min_i32_e32 v57, 78, v57\n"
    "v_mad_u32_u24 v48, v48, s48, v54\n"
    "v_mad_u32_u24 v49, v49, s48, v55\n"
    "v_mad_u32_u24 v50, v50, s48, v56\n"
    "v_mad_u32_u24 v51, v51, s48, v57\n"
    "v_lshl_add_u32 v48, v48, 2, s44\n"
    "v_lshl_add_u32 v49, v49, 2, s44\n"
    "v_lshl_add_u32 v50, v50, 2, s44\n"
    "v_lshl_add_u32 v51, v51, 2, s44\n"
    "ds_write_b32 v48, v96 offset:4\n"
    "ds_write_b32 v49, v97 offset:4\n"
    "ds_write_b32 v50, v98 offset:4\n"
    "ds_write_b32 v51, v99 offset:4\n"
    "s_mov_b64 exec, s[46:47]\n"
    "v_min_i32_e32 v58, 78, v58\n"
    "v_min_i32_e32 v59, 78, v59\n"
    "v_mad_u32_u24 v52, v52, s48, v58\n"
    "v_mad_u32_u24 v53, v53, s48, v59\n"
    "v_lshl_add_u32 v52, v52, 2, s44\n"
    "v_lshl_add_u32 v53, v53, 2, s44\n"
    "ds_write_b32 v52, v92 offset:4\n"
    "ds_write_b32 v53, v93 offset:4\n"
    "s_mov_b64 exec, -1\n"
    ".Lat_noj2:\n"
    "buffer_store_dwordx2 v[48:49], v74, s[8:11], s52 offen offset:0 sc1\n"
    "v_mov_b32_e32 v112, s39\n"
    "v_mov_b32_e32 v113, 0\n"
    "s_waitcnt vmcnt(2)\n"
    "v_dot2_f32_bf16 v24, v0, s58, 0\n"
    "v_dot2_f32_bf16 v32, v8, s58, 0\n"
    "v_dot2_f32_bf16 v40, v16, s58, 0\n"
    "v_dot2_f32_bf16 v25, v4, s58, 0\n"
    "v_dot2_f32_bf16 v33, v12, s58, 0\n"
    "v_dot2_f32_bf16 v41, v20, s58, 0\n"
    "v_dot2_f32_bf16 v26, v0, v0, 0\n"
    "v_dot2_f32_bf16 v34, v8, v8, 0\n"
    "v_dot2_f32_bf16 v42, v16, v16, 0\n"
    "v_dot2_f32_bf16 v27, v4, v4, 0\n"
    "v_dot2_f32_bf16 v35, v12, v12, 0\n"
    "v_dot2_f32_bf16 v43, v20, v20, 0\n"
    "v_dot2_f32_bf16 v24, v1, s58, v24\n"
    "v_dot2_f32_bf16 v32, v9, s58, v32\n"
    "v_dot2_f32_bf16 v40, v17, s58, v40\n"
    "v_dot2_f32_bf16 v25, v5, s58, v25\n"
    "v_dot2_f32_bf16 v33, v13, s58, v33\n"
    "v_dot2_f32_bf16 v41, v21, s58, v41\n"
    "v_dot2_f32_bf16 v26, v1, v1, v26\n"
    "v_dot2_f32_bf16 v34, v9, v9, v34\n"
    "v_dot2_f32_bf16 v42, v17, v17, v42\n"
    "v_dot2_f32_bf16 v27, v5, v5, v27\n"
    "v_dot2_f32_bf16 v35, v13, v13, v35\n"
    "v_dot2_f32_bf16 v43, v21, v21, v43\n"
    "v_dot2_f32_bf16 v24, v2, s58, v24\n"
    "v_dot2_f32_bf16 v32, v10, s58, v32\n"
    "v_dot2_f32_bf16 v40, v18, s58, v40\n"
    "v_dot2_f32_bf16 v25, v6, s58, v25\n"
    "v_dot2_f32_bf16 v33, v14, s58, v33\n"
    "v_dot2_f32_bf16 v41, v22, s58, v41\n"
    "v_dot2_f32_bf16 v26, v2, v2, v26\n"
    "v_dot2_f32_bf16 v34, v10, v10, v34\n"
    "v_dot2_f32_bf16 v42, v18, v18, v42\n"
    "v_dot2_f32_bf16 v27, v6, v6, v27\n"
    "v_dot2_f32_bf16 v35, v14, v14, v35\n"
    "v_dot2_f32_bf16 v43, v22, v22, v43\n"
    "v_dot2_f32_bf16 v24, v3, s58, v24\n"
    "v_dot2_f32_bf16 v32, v11, s58, v32\n"
    "v_dot2_f32_bf16 v40, v19, s58, v40\n"
    "v_dot2_f32_bf16 v25, v7, s58, v25\n"
    "v_dot2_f32_bf16 v33, v15, s58, v33\n"
    "v_dot2_f32_bf16 v41, v23, s58, v41\n"
    "v_dot2_f32_bf16 v26, v3, v3, v26\n"
    "v_dot2_f32_bf16 v34, v11, v11, v34\n"
    "v_dot2_f32_bf16 v42, v19, v19, v42\n"
    "v_dot2_f32_bf16 v27, v7, v7, v27\n"
    "v_dot2_f32_bf16 v35, v15, v15, v35\n"
    "v_dot2_f32_bf16 v43, v23, v23, v43\n"
    "v_and_b32_e32 v30, 0xffff0000, v7\n"
    "v_and_b32_e32 v38, 0xffff0000, v15\n"
    "v_and_b32_e32 v46, 0xffff0000, v23\n"
    "v_add_f32_e32 v24, v24, v25\n"
    "v_add_f32_e32 v32, v32, v33\n"
    "v_add_f32_e32 v40, v40, v41\n"
    "v_add_f32_e32 v26, v26, v27\n"
    "v_add_f32_e32 v34, v34, v35\n"
    "v_add_f32_e32 v42, v42, v43\n"
    "v_add_f32_dpp v25, v25, v25 quad_perm:[1,0,3,2] row_mask:0xf bank_mask:0xf bound_ctrl:1\n"
    "v_lshlrev_b32_e32 v48, 16, v0\n"
    "v_add_f32_dpp v33, v33, v33 quad_perm:[1,0,3,2] row_mask:0xf bank_mask:0xf bound_ctrl:1\n"
    "v_and_b32_e32 v49, 0xffff0000, v0\n"
    "v_add_f32_dpp v41, v41, v41 quad_perm:[1,0,3,2] row_mask:0xf bank_mask:0xf bound_ctrl:1\n"
    "v_lshlrev_b32_e32 v50, 16, v1\n"
    "v_mov_b32_dpp v30, v30 quad_perm:[3,3,3,3] row_mask:0xf bank_mask:0xf bound_ctrl:1\n"
    "v_and_b32_e32 v51, 0xffff0000, v1\n"
    "v_mov_b32_dpp v38, v38 quad_perm:[3,3,3,3] row_mask:0xf bank_mask:0xf bound_ctrl:1\n"
    "v_lshlrev_b32_e32 v52, 16, v2\n"
    "v_mov_b32_dpp v46, v46 quad_perm:[3,3,3,3] row_mask:0xf bank_mask:0xf bound_ctrl:1\n"
    "v_and_b32_e32 v53, 0xffff0000, v2\n"
    "v_add_f32_dpp v24, v24, v24 quad_perm:[1,0,3,2] row_mask:0xf bank_mask:0xf bound_ctrl:1\n"
    "v_lshlrev_b32_e32 v54, 16, v3\n"
    "v_add_f32_dpp v32, v32, v32 quad_perm:[1,0,3,2] row_mask:0xf bank_mask:0xf bound_ctrl:1\n"
    "v_and_b32_e32 v55, 0xffff0000, v3\n"
    "v_add_f32_dpp v40, v40, v40 quad_perm:[1,0,3,2] row_mask:0xf bank_mask:0xf bound_ctrl:1\n"
    "v_lshlrev_b32_e32 v56, 16, v4\n"
    "v_add_f32_dpp v26, v26, v26 quad_perm:[1,0,3,2] row_mask:0xf bank_mask:0xf bound_ctrl:1\n"
    "v_and_b32_e32 v57, 0xffff0000, v4\n"
    "v_add_f32_dpp v34, v34, v34 quad_perm:[1,0,3,2] row_mask:0xf bank_mask:0xf bound_ctrl:1\n"
    "v_lshlrev_b32_e32 v58, 16, v5\n"
    "s_waitcnt vmcnt(0)\n"
    "buffer_store_dword v112, v113, s[8:11], s53 offen offset:0 sc1\n"
    "v_add_f32_dpp v42, v42, v42 quad_perm:[1,0,3,2] row_mask:0xf bank_mask:0xf bound_ctrl:1\n"
    "v_and_b32_e32 v59, 0xffff0000, v5\n"
    "v_add_f32_dpp v24, v24, v24 quad_perm:[2,3,0,1] row_mask:0xf bank_mask:0xf bound_ctrl:1\n"
    "v_lshlrev_b32_e32 v60, 16, v6\n"
    "v_add_f32_dpp v32, v32, v32 quad_perm:[2,3,0,1] row_mask:0xf bank_mask:0xf bound_ctrl:1\n"
    "v_and_b32_e32 v61, 0xffff0000, v6\n"
    "s_waitcnt lgkmcnt(0)\n"
    "s_barrier\n"
    "ds_read_b128 v[96:99], v108 offset:0\n"
    "ds_read_b128 v[100:103], v108 offset:4096\n"
    "s_lshl_b32 s45, %1, 6\n"
    "s_add_u32 s45, s45, 35038208\n"
    "v_add_f32_dpp v40, v40, v40 quad_perm:[2,3,0,1] row_mask:0xf bank_mask:0xf bound_ctrl:1\n"
    "v_lshlrev_b32_e32 v62, 16, v7\n"
    "v_add_f32_dpp v26, v26, v26 quad_perm:[2,3,0,1] row_mask:0xf bank_mask:0xf bound_ctrl:1\n"
    "v_and_b32_e32 v63, 0xffff0000, v7\n"
    "v_add_f32_dpp v34, v34, v34 quad_perm:[2,3,0,1] row_mask:0xf bank_mask:0xf bound_ctrl:1\n"
    "v_lshlrev_b32_e32 v64, 16, v8\n"
    "v_add_f32_dpp v42, v42, v42 quad_perm:[2,3,0,1] row_mask:0xf bank_mask:0xf bound_ctrl:1\n"
    "v_and_b32_e32 v65, 0xffff0000, v8\n"
    "v_add_f32_dpp v24, v24, v24 row_half_mirror row_mask:0xf bank_mask:0xf bound_ctrl:1\n"
    "v_lshlrev_b32_e32 v66, 16, v9\n"
    "v_add_f32_dpp v32, v32, v32 row_half_mirror row_mask:0xf bank_mask:0xf bound_ctrl:1\n"
    "v_and_b32_e32 v67, 0xffff0000, v9\n"
    "v_add_f32_dpp v40, v40, v40 row_half_mirror row_mask:0xf bank_mask:0xf bound_ctrl:1\n"
    "v_lshlrev_b32_e32 v68, 16, v10\n"
    "v_add_f32_dpp v26, v26, v26 row_half_mirror row_mask:0xf bank_mask:0xf bound_ctrl:1\n"
    "v_and_b32_e32 v69, 0xffff0000, v10\n"
    "v_add_f32_dpp v34, v34, v34 row_half_mirror row_mask:0xf bank_mask:0xf bound_ctrl:1\n"
    "v_lshlrev_b32_e32 v70, 16, v11\n"
    "v_add_f32_dpp v42, v42, v42 row_half_mirror row_mask:0xf bank_mask:0xf bound_ctrl:1\n"
    "v_and_b32_e32 v71, 0xffff0000, v11\n"
    "v_add_f32_dpp v24, v24, v24 row_mirror row_mask:0xf bank_mask:0xf bound_ctrl:1\n"
    "v_lshlrev_b32_e32 v72, 16, v12\n"
    "v_add_f32_dpp v32, v32, v32 row_mirror row_mask:0xf bank_mask:0xf bound_ctrl:1\n"
    "v_and_b32_e32 v73, 0xffff0000, v12\n"
    "v_add_f32_dpp v40, v40, v40 row_mirror row_mask:0xf bank_mask:0xf bound_ctrl:1\n"
    "v_lshlrev_b32_e32 v74, 16, v13\n"
    "v_add_f32_dpp v26, v26, v26 row_mirror row_mask:0xf bank_mask:0xf bound_ctrl:1\n"
    "v_and_b32_e32 v75, 0xffff0000, v13\n"
    "v_add_f32_dpp v34, v34, v34 row_mirror row_mask:0xf bank_mask:0xf bound_ctrl:1\n"
    "v_lshlrev_b32_e32 v76, 16, v14\n"
    "s_waitcnt lgkmcnt(0)\n"
    "buffer_store_dwordx4 v[96:99], v109, s[8:11], s45 offen offset:0 sc1\n"
    "buffer_store_dwordx4 v[100:103], v110, s[8:11], s45 offen offset:0 sc1\n"
    "v_add_f32_dpp v42, v42, v42 row_mirror row_mask:0xf bank_mask:0xf bound_ctrl:1\n"
    "v_and_b32_e32 v77, 0xffff0000, v14\n"
    "v_mul_f32_e32 v24, 0x3b800000, v24\n"
    "v_lshlrev_b32_e32 v78, 16, v15\n"
    "v_mul_f32_e32 v32, 0x3b800000, v32\n"
    "v_and_b32_e32 v79, 0xffff0000, v15\n"
    "v_mul_f32_e32 v40, 0x3b800000, v40\n"
    "v_lshlrev_b32_e32 v80, 16, v16\n"
    "v_mul_f32_e32 v25, 0.25, v25\n"
    "v_and_b32_e32 v81, 0xffff0000, v16\n"
    "v_mul_f32_e32 v33, 0.25, v33\n"
    "v_lshlrev_b32_e32 v82, 16, v17\n"
    "v_mul_f32_e32 v41, 0.25, v41\n"
    "v_and_b32_e32 v83, 0xffff0000, v17\n"
    "v_mul_f32_e64 v27, v24, -v24\n"
    "v_lshlrev_b32_e32 v84, 16, v18\n"
    "v_mul_f32_e64 v35, v32, -v32\n"
    "v_and_b32_e32 v85, 0xffff0000, v18\n"
    "v_mul_f32_e64 v43, v40, -v40\n"
    "v_lshlrev_b32_e32 v86, 16, v19\n"
    "v_sub_f32_e32 v25, v25, v30\n"
    "v_and_b32_e32 v87, 0xffff0000, v19\n"
    "v_sub_f32_e32 v33, v33, v38\n"
    "v_lshlrev_b32_e32 v88, 16, v20\n"
    "v_sub_f32_e32 v41, v41, v46\n"
    "v_and_b32_e32 v89, 0xffff0000, v20\n"
    "v_fmac_f32_e32 v27, 0x3b800000, v26\n"
    "v_lshlrev_b32_e32 v90, 16, v21\n"
    "v_fmac_f32_e32 v35, 0x3b800000, v34\n"
    "v_and_b32_e32 v91, 0xffff0000, v21\n"
    "v_fmac_f32_e32 v43, 0x3b800000, v42\n"
    "v_lshlrev_b32_e32 v92, 16, v22\n"
    "v_mul_f32_e32 v25, 0x3eaaaaab, v25\n"
    "v_and_b32_e32 v93, 0xffff0000, v22\n"
    "v_mul_f32_e32 v33, 0x3eaaaaab, v33\n"
    "v_lshlrev_b32_e32 v94, 16, v23\n"
    "v_mul_f32_e32 v41, 0x3eaaaaab, v41\n"
    "v_and_b32_e32 v95, 0xffff0000, v23\n"
    "v_max_f32_e32 v27, 0, v27\n"
    "v_max_f32_e32 v35, 0, v35\n"
    "v_max_f32_e32 v43, 0, v43\n"
    "v_cndmask_b32_e64 v25, v24, v25, s[62:63]\n"
    "v_cndmask_b32_e64 v33, v32, v33, s[62:63]\n"
    "v_cndmask_b32_e64 v41, v40, v41, s[62:63]\n"
    "v_add_f32_e32 v27, 0x3727c5ac, v27\n"
    "v_add_f32_e32 v35, 0x3727c5ac, v35\n"
    "v_add_f32_e32 v43, 0x3727c5ac, v43\n"
    "v_rsq_f32_e32 v28, v27\n"
    "v_rsq_f32_e32 v36, v35\n"
    "v_rsq_f32_e32 v44, v43\n"
    "v_fma_f32 v29, -v24, v28, s57\n"
    "v_fma_f32 v37, -v32, v36, s57\n"
    "v_fma_f32 v45, -v40, v44, s57\n"
    "v_fma_f32 v31, -v25, v28, s57\n"
    "v_fma_f32 v39, -v33, v36, s57\n"
    "v_fma_f32 v47, -v41, v44, s57\n"
    "v_sub_f32_e32 v24, v29, v31\n"
    "v_sub_f32_e32 v32, v37, v39\n"
    "v_sub_f32_e32 v40, v45, v47\n"
    "v_pk_fma_f32 v[48:49], v[48:49], v[28:29], v[28:29] op_sel:[0,0,1] op_sel_hi:[1,0,1]\n"
    "v_pk_fma_f32 v[50:51], v[50:51], v[28:29], v[28:29] op_sel:[0,0,1] op_sel_hi:[1,0,1]\n"
    "v_pk_fma_f32 v[52:53], v[52:53], v[28:29], v[28:29] op_sel:[0,0,1] op_sel_hi:[1,0,1]\n"
    "v_pk_fma_f32 v[54:55], v[54:55], v[28:29], v[28:29] op_sel:[0,0,1] op_sel_hi:[1,0,1]\n"
    "v_pk_fma_f32 v[56:57], v[56:57], v[28:29], v[30:31] op_sel:[0,0,1] op_sel_hi:[1,0,1]\n"
    "v_pk_fma_f32 v[58:59], v[58:59], v[28:29], v[30:31] op_sel:[0,0,1] op_sel_hi:[1,0,1]\n"
    "v_pk_fma_f32 v[60:61], v[60:61], v[28:29], v[30:31] op_sel:[0,0,1] op_sel_hi:[1,0,1]\n"
    "v_pk_fma_f32 v[62:63], v[62:63], v[28:29], v[30:31] op_sel:[0,0,1] op_sel_hi:[1,0,1]\n"
    "v_pk_add_f32 v[48:49], v[48:49], s[56:57] op_sel_hi:[1,0]\n"
    "v_pk_add_f32 v[50:51], v[50:51], s[56:57] op_sel_hi:[1,0]\n"
    "v_pk_add_f32 v[52:53], v[52:53], s[56:57] op_sel_hi:[1,0]\n"
    "v_pk_add_f32 v[54:55], v[54:55], s[56:57] op_sel_hi:[1,0]\n"
    "v_pk_add_f32 v[56:57], v[56:57], s[56:57] op_sel_hi:[1,0]\n"
    "v_pk_add_f32 v[58:59], v[58:59], s[56:57] op_sel_hi:[1,0]\n"
    "v_pk_add_f32 v[60:61], v[60:61], s[56:57] op_sel_hi:[1,0]\n"
    "v_pk_add_f32 v[62:63], v[62:63], s[56:57] op_sel_hi:[1,0]\n"
    "v_cvt_pk_bf16_f32 v0, v48, v49\n"
    "v_cvt_pk_bf16_f32 v1, v50, v51\n"
    "v_cvt_pk_bf16_f32 v2, v52, v53\n"
    "v_cvt_pk_bf16_f32 v3, v54, v55\n"
    "v_cvt_pk_bf16_f32 v4, v56, v57\n"
    "v_cvt_pk_bf16_f32 v5, v58, v59\n"
    "v_cvt_pk_bf16_f32 v6, v60, v61\n"
    "v_cvt_pk_bf16_f32 v7, v62, v63\n"
    "buffer_store_dwordx4 v[0:3], v104, s[8:11], s35 offen offset:0 sc1\n"
    "v_bfi_b32 v7, v107, v7, 1.0\n"
    "ds_write_b128 v105, v[0:3] offset:0\n"
    "buffer_store_dwordx4 v[4:7], v104, s[8:11], s35 offen offset:256 sc1\n"
    "v_pk_fma_f32 v[64:65], v[64:65], v[36:37], v[36:37] op_sel:[0,0,1] op_sel_hi:[1,0,1]\n"
    "v_pk_fma_f32 v[66:67], v[66:67], v[36:37], v[36:37] op_sel:[0,0,1] op_sel_hi:[1,0,1]\n"
    "v_pk_fma_f32 v[68:69], v[68:69], v[36:37], v[36:37] op_sel:[0,0,1] op_sel_hi:[1,0,1]\n"
    "v_pk_fma_f32 v[70:71], v[70:71], v[36:37], v[36:37] op_sel:[0,0,1] op_sel_hi:[1,0,1]\n"
    "v_pk_fma_f32 v[72:73], v[72:73], v[36:37], v[38:39] op_sel:[0,0,1] op_sel_hi:[1,0,1]\n"
    "v_pk_fma_f32 v[74:75], v[74:75], v[36:37], v[38:39] op_sel:[0,0,1] op_sel_hi:[1,0,1]\n"
    "v_pk_fma_f32 v[76:77], v[76:77], v[36:37], v[38:39] op_sel:[0,0,1] op_sel_hi:[1,0,1]\n"
    "v_pk_fma_f32 v[78:79], v[78:79], v[36:37], v[38:39] op_sel:[0,0,1] op_sel_hi:[1,0,1]\n"
    "v_pk_add_f32 v[64:65], v[64:65], s[56:57] op_sel_hi:[1,0]\n"
    "v_pk_add_f32 v[66:67], v[66:67], s[56:57] op_sel_hi:[1,0]\n"
    "v_pk_add_f32 v[68:69], v[68:69], s[56:57] op_sel_hi:[1,0]\n"
    "v_pk_add_f32 v[70:71], v[70:71], s[56:57] op_sel_hi:[1,0]\n"
    "v_pk_add_f32 v[72:73], v[72:73], s[56:57] op_sel_hi:[1,0]\n"
    "v_pk_add_f32 v[74:75], v[74:75], s[56:57] op_sel_hi:[1,0]\n"
    "v_pk_add_f32 v[76:77], v[76:77], s[56:57] op_sel_hi:[1,0]\n"
    "v_pk_add_f32 v[78:79], v[78:79], s[56:57] op_sel_hi:[1,0]\n"
    "v_cvt_pk_bf16_f32 v8, v64, v65\n"
    "v_cvt_pk_bf16_f32 v9, v66, v67\n"
    "v_cvt_pk_bf16_f32 v10, v68, v69\n"
    "v_cvt_pk_bf16_f32 v11, v70, v71\n"
    "v_cvt_pk_bf16_f32 v12, v72, v73\n"
    "v_cvt_pk_bf16_f32 v13, v74, v75\n"
    "v_cvt_pk_bf16_f32 v14, v76, v77\n"
    "v_cvt_pk_bf16_f32 v15, v78, v79\n"
    "buffer_store_dwordx4 v[8:11], v104, s[8:11], s54 offen offset:0 sc1\n"
    "v_bfi_b32 v15, v107, v15, 1.0\n"
    "ds_write_b128 v105, v[8:11] offset:8192\n"
    "buffer_store_dwordx4 v[12:15], v104, s[8:11], s54 offen offset:256 sc1\n"
    "v_pk_fma_f32 v[80:81], v[80:81], v[44:45], v[44:45] op_sel:[0,0,1] op_sel_hi:[1,0,1]\n"
    "v_pk_fma_f32 v[82:83], v[82:83], v[44:45], v[44:45] op_sel:[0,0,1] op_sel_hi:[1,0,1]\n"
    "v_pk_fma_f32 v[84:85], v[84:85], v[44:45], v[44:45] op_sel:[0,0,1] op_sel_hi:[1,0,1]\n"
    "v_pk_fma_f32 v[86:87], v[86:87], v[44:45], v[44:45] op_sel:[0,0,1] op_sel_hi:[1,0,1]\n"
    "v_pk_fma_f32 v[88:89], v[88:89], v[44:45], v[46:47] op_sel:[0,0,1] op_sel_hi:[1,0,1]\n"
    "v_pk_fma_f32 v[90:91], v[90:91], v[44:45], v[46:47] op_sel:[0,0,1] op_sel_hi:[1,0,1]\n"
    "v_pk_fma_f32 v[92:93], v[92:93], v[44:45], v[46:47] op_sel:[0,0,1] op_sel_hi:[1,0,1]\n"
    "v_pk_fma_f32 v[94:95], v[94:95], v[44:45], v[46:47] op_sel:[0,0,1] op_sel_hi:[1,0,1]\n"
    "v_pk_add_f32 v[80:81], v[80:81], s[56:57] op_sel_hi:[1,0]\n"
    "v_pk_add_f32 v[82:83], v[82:83], s[56:57] op_sel_hi:[1,0]\n"
    "v_pk_add_f32 v[84:85], v[84:85], s[56:57] op_sel_hi:[1,0]\n"
    "v_pk_add_f32 v[86:87], v[86:87], s[56:57] op_sel_hi:[1,0]\n"
    "v_pk_add_f32 v[88:89], v[88:89], s[56:57] op_sel_hi:[1,0]\n"
    "v_pk_add_f32 v[90:91], v[90:91], s[56:57] op_sel_hi:[1,0]\n"
    "v_pk_add_f32 v[92:93], v[92:93], s[56:57] op_sel_hi:[1,0]\n"
    "v_pk_add_f32 v[94:95], v[94:95], s[56:57] op_sel_hi:[1,0]\n"
    "v_cvt_pk_bf16_f32 v16, v80, v81\n"
    "v_cvt_pk_bf16_f32 v17, v82, v83\n"
    "v_cvt_pk_bf16_f32 v18, v84, v85\n"
    "v_cvt_pk_bf16_f32 v19, v86, v87\n"
    "v_cvt_pk_bf16_f32 v20, v88, v89\n"
    "v_cvt_pk_bf16_f32 v21, v90, v91\n"
    "v_cvt_pk_bf16_f32 v22, v92, v93\n"
    "v_cvt_pk_bf16_f32 v23, v94, v95\n"
    "buffer_store_dwordx4 v[16:19], v104, s[8:11], s60 offen offset:0 sc1\n"
    "v_bfi_b32 v23, v107, v23, 1.0\n"
    "ds_write_b128 v105, v[16:19] offset:16384\n"
    "buffer_store_dwordx4 v[20:23], v104, s[8:11], s60 offen offset:256 sc1\n"
    "v_pk_add_f32 v[56:57], v[56:57], v[24:25] op_sel_hi:[1,0]\n"
    "v_pk_add_f32 v[58:59], v[58:59], v[24:25] op_sel_hi:[1,0]\n"
    "v_pk_add_f32 v[60:61], v[60:61], v[24:25] op_sel_hi:[1,0]\n"
    "v_pk_add_f32 v[62:63], v[62:63], v[24:25] op_sel_hi:[1,0]\n"
    "v_cvt_pk_bf16_f32 v56, v56, v57\n"
    "v_cvt_pk_bf16_f32 v57, v58, v59\n"
    "v_cvt_pk_bf16_f32 v58, v60, v61\n"
    "v_cvt_pk_bf16_f32 v59, v62, v63\n"
    "ds_write_b128 v106, v[56:59] offset:0\n"
    "v_pk_add_f32 v[72:73], v[72:73], v[32:33] op_sel_hi:[1,0]\n"
    "v_pk_add_f32 v[74:75], v[74:75], v[32:33] op_sel_hi:[1,0]\n"
    "v_pk_add_f32 v[76:77], v[76:77], v[32:33] op_sel_hi:[1,0]\n"
    "v_pk_add_f32 v[78:79], v[78:79], v[32:33] op_sel_hi:[1,0]\n"
    "v_cvt_pk_bf16_f32 v72, v72, v73\n"
    "v_cvt_pk_bf16_f32 v73, v74, v75\n"
    "v_cvt_pk_bf16_f32 v74, v76, v77\n"
    "v_cvt_pk_bf16_f32 v75, v78, v79\n"
    "ds_write_b128 v106, v[72:75] offset:8192\n"
    "v_pk_add_f32 v[88:89], v[88:89], v[40:41] op_sel_hi:[1,0]\n"
    "v_pk_add_f32 v[90:91], v[90:91], v[40:41] op_sel_hi:[1,0]\n"
    "v_pk_add_f32 v[92:93], v[92:93], v[40:41] op_sel_hi:[1,0]\n"
    "v_pk_add_f32 v[94:95], v[94:95], v[40:41] op_sel_hi:[1,0]\n"
    "v_cvt_pk_bf16_f32 v88, v88, v89\n"
    "v_cvt_pk_bf16_f32 v89, v90, v91\n"
    "v_cvt_pk_bf16_f32 v90, v92, v93\n"
    "v_cvt_pk_bf16_f32 v91, v94, v95\n"
    "ds_write_b128 v106, v[88:91] offset:16384\n"
    "s_waitcnt lgkmcnt(0)\n"
    "s_barrier\n"
    "ds_read_b128 a[0:3], v250 offset:0\n"
    "ds_read_b128 a[32:35], v250 offset:8192\n"
    "ds_read_b128 a[64:67], v250 offset:16384\n"
    "ds_read_b128 a[4:7], v251 offset:0\n"
    "ds_read_b128 a[36:39], v251 offset:8192\n"
    "ds_read_b128 a[68:71], v251 offset:16384\n"
    "ds_read_b128 a[8:11], v250 offset:2048\n"
    "ds_read_b128 a[40:43], v250 offset:10240\n"
    "ds_read_b128 a[72:75], v250 offset:18432\n"
    "ds_read_b128 a[12:15], v251 offset:2048\n"
    "ds_read_b128 a[44:47], v251 offset:10240\n"
    "ds_read_b128 a[76:79], v251 offset:18432\n"
    "buffer_load_dword v255, v252, s[8:11], 0 offen offset:0 sc1\n"
    "ds_read_b128 a[16:19], v250 offset:4096\n"
    "ds_read_b128 a[48:51], v250 offset:12288\n"
    "ds_read_b128 a[80:83], v250 offset:20480\n"
    "ds_read_b128 a[20:23], v251 offset:4096\n"
    "ds_read_b128 a[52:55], v251 offset:12288\n"
    "ds_read_b128 a[84:87], v251 offset:20480\n"
    "ds_read_b128 a[24:27], v250 offset:6144\n"
    "ds_read_b128 a[56:59], v250 offset:14336\n"
    "ds_read_b128 a[88:91], v250 offset:22528\n"
    "ds_read_b128 a[28:31], v251 offset:6144\n"
    "ds_read_b128 a[60:63], v251 offset:14336\n"
    "ds_read_b128 a[92:95], v251 offset:22528\n"
    "s_lshl_b32 s52, %2, 15\n"
    "s_add_u32 s52, s52, 5242880\n"
    "s_lshl_b32 s61, s37, 12\n"
    "s_lshl_b32 s46, %1, 4\n"
    "s_lshl_b32 s47, %1, 7\n"
    "s_add_u32 s47, s47, 4096\n"
    "s_cmp_eq_u32 s50, 0\n"
    "s_cselect_b32 s46, s46, s47\n"
    "s_add_u32 s46, s46, 5701632\n"
    "s_cmp_eq_u32 s51, 0\n"
    "s_cselect_b32 s46, s46, 2.0\n"
    "v_mov_b32_e32 v40, s39\n"
    "v_mov_b32_e32 v41, 0\n"
    "s_waitcnt vmcnt(1)\n"
    "s_waitcnt lgkmcnt(0)\n"
    "s_barrier\n"
    "buffer_store_dword v40, v41, s[8:11], s46 offen offset:0 sc1\n"
    "s_waitcnt vmcnt(1)\n"
    "v_cmp_ne_u32_e32 vcc, s39, v255\n"
    "s_nop 4\n"
    "s_cbranch_vccnz .Lat_apoll\n"
    ".Lat_apoll_ret:\n"
    "s_add_u32 s47, s52, s61\n"
    "buffer_load_dwordx4 v[112:115], v253, s[8:11], s47 offen offset:0 sc1\n"
    "buffer_load_dwordx4 v[116:119], v253, s[8:11], s47 offen offset:1024 sc1\n"
    "buffer_load_dwordx4 v[128:131], v253, s[8:11], s47 offen offset:2048 sc1\n"
    "s_add_u32 s61, s61, 0x1000\n"
    "buffer_load_dwordx4 v[132:135], v253, s[8:11], s47 offen offset:3072 sc1\n"
    "s_bitset0_b32 s61, 14\n"
    "s_add_u32 s47, s52, s61\n"
    "buffer_load_dwordx4 v[144:147], v253, s[8:11], s47 offen offset:0 sc1\n"
    "buffer_load_dwordx4 v[148:151], v253, s[8:11], s47 offen offset:1024 sc1\n"
    "buffer_load_dwordx4 v[160:163], v253, s[8:11], s47 offen offset:2048 sc1\n"
    "s_add_u32 s61, s61, 0x1000\n"
    "buffer_load_dwordx4 v[164:167], v253, s[8:11], s47 offen offset:3072 sc1\n"
    "s_bitset0_b32 s61, 14\n"
    "s_add_u32 s47, s52, s61\n"
    "buffer_load_dwordx4 v[176:179], v253, s[8:11], s47 offen offset:0 sc1\n"
    "buffer_load_dwordx4 v[180:183], v253, s[8:11], s47 offen offset:1024 sc1\n"
    "buffer_load_dwordx4 v[192:195], v253, s[8:11], s47 offen offset:2048 sc1\n"
    "s_add_u32 s61, s61, 0x1000\n"
    "s_lshl_b32 s59, s32, 4\n"
    "s_lshr_b32 s48, s32, 1\n"
    "s_sub_u32 s59, s59, s48\n"
    "s_lshl_b32 s59, s59, 6\n"
    "s_sub_u32 s48, 3984, s59\n"
    "v_cmp_le_u32_e32 vcc, 0x40, v253\n"
    "v_mov_b32_e32 v0, s48\n"
    "s_cmp_eq_u32 s32, 2\n"
    "s_cselect_b32 s48, 1024, 0\n"
    "v_mov_b32_e32 v2, s48\n"
    "s_add_u32 s59, s59, 5701632\n"
    "v_cndmask_b32_e32 v3, v253, v0, vcc\n"
    "s_lshl_b32 s45, %2, 11\n"
    "s_add_u32 s48, s45, %3\n"
    "v_add_u32_e32 v9, s48, v253\n"
    "s_add_u32 s45, s45, 11022336\n"
    "v_add_u32_e32 v8, s45, v253\n"
    "v_lshrrev_b32_e32 v13, 7, v253\n"
    "v_and_b32_e32 v12, 0x70, v254\n"
    "v_lshl_add_u32 v12, v13, 9, v12\n"
    "v_add_u32_e32 v13, 0xc00, v12\n"
    "v_add_u32_e32 v14, 0x1800, v12\n"
    "v_add_u32_e32 v15, 0x2400, v12\n"
    "s_lshl_b32 s57, s50, 8\n"
    "s_add_u32 s57, s57, 5111808\n"
    "s_add_u32 s62, s43, s34\n"
    "s_xor_b32 s63, s34, 0xc000\n"
    "s_add_u32 s63, s63, s43\n"
    "s_waitcnt vmcnt(9)\n"
    "v_mfma_f32_16x16x32_bf16 v[24:27], v[112:115], a[0:3], 0\n"
    "buffer_load_dwordx4 v[196:199], v253, s[8:11], s47 offen offset:3072 sc1\n"
    "v_mfma_f32_16x16x32_bf16 v[28:31], v[116:119], a[0:3], 0\n"
    "s_bitset0_b32 s61, 14\n"
    "s_mov_b32 s18, 5120000\n"
    "s_mov_b32 s19, 0x20000\n"
    "v_mfma_f32_16x16x32_bf16 v[48:51], v[112:115], a[32:35], 0\n"
    "s_add_u32 s47, s52, s61\n"
    "v_xor_b32_e32 v242, 0x12000, v240\n"
    "v_xor_b32_e32 v243, 0x12000, v241\n"
    "v_mfma_f32_16x16x32_bf16 v[52:55], v[116:119], a[32:35], 0\n"
    "buffer_load_dwordx4 v[208:211], v253, s[8:11], s47 offen offset:0 sc1\n"
    "v_mfma_f32_16x16x32_bf16 v[64:67], v[112:115], a[64:67], 0\n"
    "s_cmp_eq_u32 %2, 3\n"
    "s_cselect_b32 s88, 0x2ff, -1\n"
    "v_mov_b32_e32 v10, 0xc2980000\n"
    "v_mfma_f32_16x16x32_bf16 v[68:71], v[116:119], a[64:67], 0\n"
    "s_waitcnt vmcnt(9)\n"
    "v_cmp_lt_u32_e32 vcc, s88, v253\n"
    "s_nop 1\n"
    "v_cndmask_b32_e32 v10, 0, v10, vcc\n"
    "v_mfma_f32_16x16x32_bf16 v[24:27], v[128:131], a[4:7], v[24:27]\n"
    "buffer_load_dwordx4 v[212:215], v253, s[8:11], s47 offen offset:1024 sc1\n"
    "v_mfma_f32_16x16x32_bf16 v[28:31], v[132:135], a[4:7], v[28:31]\n"
    "s_add_u32 s52, s52, 0x4000\n"
    "v_lshrrev_b32_e32 v5, 4, v253\n"
    "v_min_u32_e32 v5, 2, v5\n"
    "v_mfma_f32_16x16x32_bf16 v[48:51], v[128:131], a[36:39], v[48:51]\n"
    "s_add_u32 s88, s32, 1\n"
    "v_add_u32_e32 v5, s88, v5\n"
    "v_mov_b32_e32 v80, 0\n"
    "v_mfma_f32_16x16x32_bf16 v[52:55], v[132:135], a[36:39], v[52:55]\n"
    "buffer_load_dwordx4 v[224:227], v253, s[8:11], s47 offen offset:2048 sc1\n"
    "v_mfma_f32_16x16x32_bf16 v[64:67], v[128:131], a[68:71], v[64:67]\n"
    "s_add_u32 s61, s61, 0x1000\n"
    "v_and_b32_e32 v5, 3, v5\n"
    "v_lshlrev_b32_e32 v5, 7, v5\n"
    "v_mfma_f32_16x16x32_bf16 v[68:71], v[132:135], a[68:71], v[68:71]\n"
    "s_waitcnt vmcnt(9)\n"
    "s_lshl_b32 s58, %1, 7\n"
    "s_add_u32 s58, s58, 5738432\n"
    "s_cmp_eq_u32 %2, 2\n"
    "s_cselect_b32 s58, s58, 2.0\n"
    "v_mfma_f32_16x16x32_bf16 v[24:27], v[144:147], a[8:11], v[24:27]\n"
    "buffer_load_dwordx4 v[228:231], v253, s[8:11], s47 offen offset:3072 sc1\n"
    "v_mfma_f32_16x16x32_bf16 v[28:31], v[148:151], a[8:11], v[28:31]\n"
    "s_bitset0_b32 s61, 14\n"
    "s_lshl_b32 s88, s50, 13\n"
    "s_add_u32 s88, s88, %3\n"
    "v_mfma_f32_16x16x32_bf16 v[48:51], v[144:147], a[40:43], v[48:51]\n"
    "s_add_u32 s47, s52, s61\n"
    "v_add_u32_e32 v6, s88, v253\n"
    "v_add_u32_e32 v7, %3, v253\n"
    "v_mfma_f32_16x16x32_bf16 v[52:55], v[148:151], a[40:43], v[52:55]\n"
    "buffer_load_dwordx4 v[120:123], v253, s[8:11], s47 offen offset:0 sc1\n"
    "v_mfma_f32_16x16x32_bf16 v[64:67], v[144:147], a[72:75], v[64:67]\n"
    "s_mul_i32 s88, %2, 1024\n"
    "s_add_u32 s88, s88, %3\n"
    "v_lshrrev_b32_e32 v250, 2, v253\n"
    "v_mfma_f32_16x16x32_bf16 v[68:71], v[148:151], a[72:75], v[68:71]\n"
    "s_waitcnt vmcnt(9)\n"
    "v_add_u32_e32 v249, s88, v250\n"
    "s_mov_b32 s49, s88\n"
    "v_mfma_f32_16x16x32_bf16 v[24:27], v[160:163], a[12:15], v[24:27]\n"
    "buffer_load_dwordx4 v[124:127], v253, s[8:11], s47 offen offset:1024 sc1\n"
    "v_mfma_f32_16x16x32_bf16 v[28:31], v[164:167], a[12:15], v[28:31]\n"
    "s_lshl_b32 s81, s88, 2\n"
    "s_lshl2_add_u32 s90, %1, %2\n"
    "s_lshl_b32 s90, s90, 10\n"
    "v_mfma_f32_16x16x32_bf16 v[48:51], v[160:163], a[44:47], v[48:51]\n"
    "s_sub_u32 s81, s90, s81\n"
    "s_add_u32 s81, s81, 36369408\n"
    "s_xor_b32 s88, %2, 2\n"
    "v_mfma_f32_16x16x32_bf16 v[52:55], v[164:167], a[44:47], v[52:55]\n"
    "buffer_load_dwordx4 v[136:139], v253, s[8:11], s47 offen offset:2048 sc1\n"
    "v_mfma_f32_16x16x32_bf16 v[64:67], v[160:163], a[76:79], v[64:67]\n"
    "s_add_u32 s61, s61, 0x1000\n"
    "s_mul_i32 s88, s88, 1024\n"
    "s_add_u32 s88, s88, %3\n"
    "v_mfma_f32_16x16x32_bf16 v[68:71], v[164:167], a[76:79], v[68:71]\n"
    "s_waitcnt vmcnt(9)\n"
    "v_add_u32_e32 v250, s88, v250\n"
    "v_and_b32_e32 v251, 0x1f0, v253\n"
    "v_mfma_f32_16x16x32_bf16 v[24:27], v[176:179], a[16:19], v[24:27]\n"
    "buffer_load_dwordx4 v[140:143], v253, s[8:11], s47 offen offset:3072 sc1\n"
    "v_mfma_f32_16x16x32_bf16 v[28:31], v[180:183], a[16:19], v[28:31]\n"
    "s_bitset0_b32 s61, 14\n"
    "s_lshl_b32 s82, %2, 10\n"
    "s_add_u32 s82, s82, %3\n"
    "v_mfma_f32_16x16x32_bf16 v[48:51], v[176:179], a[48:51], v[48:51]\n"
    "s_add_u32 s47, s52, s61\n"
    "s_add_u32 s82, s82, 20480\n"
    "v_lshrrev_b32_e32 v252, 9, v253\n"
    "v_mfma_f32_16x16x32_bf16 v[52:55], v[180:183], a[48:51], v[52:55]\n"
    "buffer_load_dwordx4 v[152:155], v253, s[8:11], s47 offen offset:0 sc1\n"
    "v_mfma_f32_16x16x32_bf16 v[64:67], v[176:179], a[80:83], v[64:67]\n"
    "s_lshr_b32 s80, %1, 4\n"
    "s_lshl_b32 s80, s80, 7\n"
    "v_lshl_add_u32 v252, v252, 4, s82\n"
    "v_mfma_f32_16x16x32_bf16 v[68:71], v[180:183], a[80:83], v[68:71]\n"
    "s_waitcnt vmcnt(9)\n"
    "s_add_u32 s80, s80, 5775616\n"
    "s_and_b32 s88, s32, 1\n"
    "v_mfma_f32_16x16x32_bf16 v[24:27], v[192:195], a[20:23], v[24:27]\n"
    "buffer_load_dwordx4 v[156:159], v253, s[8:11], s47 offen offset:1024 sc1\n"
    "v_mfma_f32_16x16x32_bf16 v[28:31], v[196:199], a[20:23], v[28:31]\n"
    "s_lshl_b32 s88, s88, 6\n"
    "s_add_u32 s80, s80, s88\n"
    "s_mov_b64 s[20:21], s[8:9]\n"
    "v_mfma_f32_16x16x32_bf16 v[48:51], v[192:195], a[52:55], v[48:51]\n"
    "s_mov_b32 s22, 5242880\n"
    "s_add_u32 s24, s8, 5779456\n"
    "s_addc_u32 s25, s9, 0\n"
    "v_mfma_f32_16x16x32_bf16 v[52:55], v[196:199], a[52:55], v[52:55]\n"
    "buffer_load_dwordx4 v[168:171], v253, s[8:11], s47 offen offset:2048 sc1\n"
    "v_mfma_f32_16x16x32_bf16 v[64:67], v[192:195], a[84:87], v[64:67]\n"
    "s_add_u32 s61, s61, 0x1000\n"
    "s_mov_b32 s26, 5242880\n"
    "s_movk_i32 s42, 512\n"
    "v_mfma_f32_16x16x32_bf16 v[68:71], v[196:199], a[84:87], v[68:71]\n"
    "s_waitcnt vmcnt(9)\n"
    "s_mov_b32 s37, 0\n"
    "s_add_u32 s38, s38, s50\n"
    "v_mfma_f32_16x16x32_bf16 v[24:27], v[208:211], a[24:27], v[24:27]\n"
    "buffer_load_dwordx4 v[172:175], v253, s[8:11], s47 offen offset:3072 sc1\n"
    "v_mfma_f32_16x16x32_bf16 v[28:31], v[212:215], a[24:27], v[28:31]\n"
    "s_bitset0_b32 s61, 14\n"
    "v_bfe_u32 v16, v253, 4, 2\n"
    "v_bfe_u32 v17, v253, 6, 2\n"
    "v_mov_b32_e32 v18, 0x3f80\n"
    "v_mfma_f32_16x16x32_bf16 v[48:51], v[208:211], a[56:59], v[48:51]\n"
    "s_add_u32 s47, s52, s61\n"
    "v_cmp_eq_u32_e32 vcc, 3, v16\n"
    "s_nop 1\n"
    "v_cndmask_b32_e64 v18, v18, 1.0, vcc\n"
    "v_cndmask_b32_e64 v16, v16, 0, vcc\n"
    "v_mfma_f32_16x16x32_bf16 v[52:55], v[212:215], a[56:59], v[52:55]\n"
    "buffer_load_dwordx4 v[184:187], v253, s[8:11], s47 offen offset:0 sc1\n"
    "v_mfma_f32_16x16x32_bf16 v[64:67], v[208:211], a[88:91], v[64:67]\n"
    "v_or_b32_e32 v16, 12, v16\n"
    "v_mul_u32_u24_e32 v16, 0x1111, v16\n"
    "v_mfma_f32_16x16x32_bf16 v[68:71], v[212:215], a[88:91], v[68:71]\n"
    "s_waitcnt vmcnt(9)\n"
    "v_cmp_eq_u32_e32 vcc, 0, v17\n"
    "s_nop 1\n"
    "v_cndmask_b32_e32 v19, 0, v18, vcc\n"
    "v_mfma_f32_16x16x32_bf16 v[24:27], v[224:227], a[28:31], v[24:27]\n"
    "buffer_load_dwordx4 v[188:191], v253, s[8:11], s47 offen offset:1024 sc1\n"
    "v_mfma_f32_16x16x32_bf16 v[28:31], v[228:231], a[28:31], v[28:31]\n"
    "v_accvgpr_write_b32 a192, v19\n"
    "v_mov_b32_e32 v81, 0\n"
    "v_mfma_f32_16x16x32_bf16 v[48:51], v[224:227], a[60:63], v[48:51]\n"
    "v_cmp_eq_u32_e32 vcc, 1, v17\n"
    "s_nop 1\n"
    "v_cndmask_b32_e32 v19, 0, v18, vcc\n"
    "v_mfma_f32_16x16x32_bf16 v[52:55], v[228:231], a[60:63], v[52:55]\n"
    "buffer_load_dwordx4 v[200:203], v253, s[8:11], s47 offen offset:2048 sc1\n"
    "v_mfma_f32_16x16x32_bf16 v[64:67], v[224:227], a[92:95], v[64:67]\n"
    "s_add_u32 s61, s61, 0x1000\n"
    "v_cvt_pk_bf16_f32 v24, v24, v25\n"
    "v_mov_b32_e32 v82, 0\n"
    "v_mfma_f32_16x16x32_bf16 v[68:71], v[228:231], a[92:95], v[68:71]\n"
    "s_waitcnt vmcnt(9)\n"
    "v_cvt_pk_bf16_f32 v25, v26, v27\n"
    "v_accvgpr_write_b32 a193, v19\n"
    "v_mfma_f32_16x16x32_bf16 v[32:35], v[120:123], a[0:3], 0\n"
    "buffer_load_dwordx4 v[204:207], v253, s[8:11], s47 offen offset:3072 sc1\n"
    "v_mfma_f32_16x16x32_bf16 v[36:39], v[124:127], a[0:3], 0\n"
    "s_bitset0_b32 s61, 14\n"
    "v_cvt_pk_bf16_f32 v26, v28, v29\n"
    "v_cmp_eq_u32_e32 vcc, 2, v17\n"
    "s_nop 1\n"
    "v_cndmask_b32_e32 v19, 0, v18, vcc\n"
    "v_mfma_f32_16x16x32_bf16 v[56:59], v[120:123], a[32:35], 0\n"
    "s_add_u32 s47, s52, s61\n"
    "v_cvt_pk_bf16_f32 v27, v30, v31\n"
    "v_accvgpr_write_b32 a194, v19\n"
    "v_mfma_f32_16x16x32_bf16 v[60:63], v[124:127], a[32:35], 0\n"
    "buffer_load_dwordx4 v[216:219], v253, s[8:11], s47 offen offset:0 sc1\n"
    "v_mfma_f32_16x16x32_bf16 v[72:75], v[120:123], a[64:67], 0\n"
    "buffer_store_dwordx4 v[24:27], v8, s[8:11], s35 offen offset:0 sc1\n"
    "v_cvt_pk_bf16_f32 v48, v48, v49\n"
    "v_cmp_eq_u32_e32 vcc, 3, v17\n"
    "s_nop 1\n"
    "v_cndmask_b32_e32 v19, 0, v18, vcc\n"
    "v_mfma_f32_16x16x32_bf16 v[76:79], v[124:127], a[64:67], 0\n"
    "s_waitcnt vmcnt(10)\n"
    "v_cvt_pk_bf16_f32 v49, v50, v51\n"
    "v_accvgpr_write_b32 a195, v19\n"
    "v_mfma_f32_16x16x32_bf16 v[32:35], v[136:139], a[4:7], v[32:35]\n"
    "buffer_load_dwordx4 v[220:223], v253, s[8:11], s47 offen offset:1024 sc1\n"
    "v_mfma_f32_16x16x32_bf16 v[36:39], v[140:143], a[4:7], v[36:39]\n"
    "s_add_u32 s52, s52, 0x4000\n"
    "v_cvt_pk_bf16_f32 v50, v52, v53\n"
    "v_cvt_pk_bf16_f32 v51, v54, v55\n"
    "v_mfma_f32_16x16x32_bf16 v[56:59], v[136:139], a[36:39], v[56:59]\n"
    "buffer_load_dword v0, v3, s[8:11], s59 offen offset:0 sc1\n"
    "v_mfma_f32_16x16x32_bf16 v[60:63], v[140:143], a[36:39], v[60:63]\n"
    "buffer_load_dwordx4 v[232:235], v253, s[8:11], s47 offen offset:2048 sc1\n"
    "buffer_store_dwordx4 v[48:51], v8, s[8:11], s54 offen offset:0 sc1\n"
    "v_mov_b32_e32 v83, 0\n"
    "v_mfma_f32_16x16x32_bf16 v[72:75], v[136:139], a[68:71], v[72:75]\n"
    "s_add_u32 s61, s61, 0x1000\n"
    "v_cvt_pk_bf16_f32 v64, v64, v65\n"
    "v_mov_b32_e32 v84, 0\n"
    "v_mfma_f32_16x16x32_bf16 v[76:79], v[140:143], a[68:71], v[76:79]\n"
    "s_waitcnt vmcnt(12)\n"
    "v_cvt_pk_bf16_f32 v65, v66, v67\n"
    "v_mov_b32_e32 v85, 0\n"
    "v_mfma_f32_16x16x32_bf16 v[32:35], v[152:155], a[8:11], v[32:35]\n"
    "buffer_load_dwordx4 v[236:239], v253, s[8:11], s47 offen offset:3072 sc1\n"
    "v_mfma_f32_16x16x32_bf16 v[36:39], v[156:159], a[8:11], v[36:39]\n"
    "s_bitset0_b32 s61, 14\n"
    "v_cvt_pk_bf16_f32 v66, v68, v69\n"
    "v_mov_b32_e32 v86, 0\n"
    "v_mfma_f32_16x16x32_bf16 v[56:59], v[152:155], a[40:43], v[56:59]\n"
    "s_add_u32 s28, s38, 2\n"
    "v_cvt_pk_bf16_f32 v67, v70, v71\n"
    "v_mov_b32_e32 v87, 0\n"
    "v_mfma_f32_16x16x32_bf16 v[60:63], v[156:159], a[40:43], v[60:63]\n"
    "buffer_store_dwordx4 v[64:67], v8, s[8:11], s60 offen offset:0 sc1\n"
    "v_mov_b32_e32 v88, 0\n"
    "v_mov_b32_e32 v89, 0\n"
    "v_mfma_f32_16x16x32_bf16 v[72:75], v[152:155], a[72:75], v[72:75]\n"
    "s_sub_u32 s88, s28, 10\n"
    "s_min_u32 s28, s28, s88\n"
    "v_mov_b32_e32 v90, 0\n"
    "v_mfma_f32_16x16x32_bf16 v[76:79], v[156:159], a[72:75], v[76:79]\n"
    "s_waitcnt vmcnt(12)\n"
    "s_lshl_b32 s88, s28, 0\n"
    "v_mov_b32_e32 v91, 0\n"
    "v_mfma_f32_16x16x32_bf16 v[32:35], v[168:171], a[12:15], v[32:35]\n"
    "s_or_b32 s36, s88, s88\n"
    "s_lshl1_add_u32 s88, s28, 1\n"
    "v_mov_b32_e32 v92, 0\n"
    "v_mfma_f32_16x16x32_bf16 v[36:39], v[172:175], a[12:15], v[36:39]\n"
    "s_mul_i32 s88, s88, 52\n"
    "s_lshr_b32 s88, s88, 8\n"
    "v_mov_b32_e32 v93, 0\n"
    "v_mfma_f32_16x16x32_bf16 v[56:59], v[168:171], a[44:47], v[56:59]\n"
    "s_mul_i32 s90, s88, 5\n"
    "ds_write_b128 v9, v[24:27] offset:0\n"
    "v_mfma_f32_16x16x32_bf16 v[60:63], v[172:175], a[44:47], v[60:63]\n"
    "s_lshr_b32 s90, s90, 1\n"
    "s_sub_u32 s28, s28, s90\n"
    "v_mov_b32_e32 v94, 0\n"
    "v_mfma_f32_16x16x32_bf16 v[72:75], v[168:171], a[76:79], v[72:75]\n"
    "s_sub_u32 s90, s32, s88\n"
    "s_sub_u32 s90, s90, 1\n"
    "v_mov_b32_e32 v95, 0\n"
    "v_mfma_f32_16x16x32_bf16 v[76:79], v[172:175], a[76:79], v[76:79]\n"
    "s_waitcnt vmcnt(10)\n"
    "s_and_b32 s90, s90, 3\n"
    "v_mov_b32_e32 v96, 0\n"
    "v_mfma_f32_16x16x32_bf16 v[32:35], v[184:187], a[16:19], v[32:35]\n"
    "s_lshl2_add_u32 s88, s33, s88\n"
    "s_mul_i32 s88, s88, 3\n"
    "v_mov_b32_e32 v97, 0\n"
    "v_mfma_f32_16x16x32_bf16 v[36:39], v[188:191], a[16:19], v[36:39]\n"
    "s_add_u32 s88, s88, s90\n"
    "v_mov_b32_e32 v98, 0\n"
    "v_mov_b32_e32 v99, 0\n"
    "v_mfma_f32_16x16x32_bf16 v[56:59], v[184:187], a[48:51], v[56:59]\n"
    "s_mul_i32 s88, s88, 24832\n"
    "s_add_u32 s88, s88, 16265216\n"
    "v_mov_b32_e32 v100, 0\n"
    "v_mfma_f32_16x16x32_bf16 v[60:63], v[188:191], a[48:51], v[60:63]\n"
    "s_cmp_eq_u32 s90, 3\n"
    "s_cselect_b32 s88, 2.0, s88\n"
    "s_cselect_b32 s89, 2, 3\n"
    "v_mfma_f32_16x16x32_bf16 v[72:75], v[184:187], a[80:83], v[72:75]\n"
    "s_lshl1_add_u32 s83, s28, s51\n"
    "s_lshl_b32 s83, s83, 12\n"
    "v_mov_b32_e32 v101, 0\n"
    "v_mfma_f32_16x16x32_bf16 v[76:79], v[188:191], a[80:83], v[76:79]\n"
    "s_waitcnt vmcnt(8)\n"
    "ds_write_b128 v9, v[48:51] offset:8192\n"
    "v_mfma_f32_16x16x32_bf16 v[32:35], v[200:203], a[20:23], v[32:35]\n"
    "s_add_u32 s83, s83, s88\n"
    "s_lshl2_add_u32 s28, s28, s88\n"
    "v_mov_b32_e32 v102, 0\n"
    "v_mfma_f32_16x16x32_bf16 v[36:39], v[204:207], a[20:23], v[36:39]\n"
    "s_add_u32 s29, s38, 4\n"
    "s_sub_u32 s88, s29, 10\n"
    "v_mov_b32_e32 v103, 0\n"
    "v_mfma_f32_16x16x32_bf16 v[56:59], v[200:203], a[52:55], v[56:59]\n"
    "s_min_u32 s29, s29, s88\n"
    "s_lshl_b32 s88, s29, 4\n"
    "v_mov_b32_e32 v104, 0\n"
    "v_mfma_f32_16x16x32_bf16 v[60:63], v[204:207], a[52:55], v[60:63]\n"
    "s_or_b32 s36, s88, s36\n"
    "s_lshl1_add_u32 s88, s29, 1\n"
    "v_mov_b32_e32 v105, 0\n"
    "v_mfma_f32_16x16x32_bf16 v[72:75], v[200:203], a[84:87], v[72:75]\n"
    "s_waitcnt vmcnt(4)\n"
    "v_cmp_ne_u32_e32 vcc, s39, v0\n"
    "v_mov_b32_e32 v106, 0\n"
    "v_mfma_f32_16x16x32_bf16 v[76:79], v[204:207], a[84:87], v[76:79]\n"
    "s_waitcnt vmcnt(5)\n"
    "s_mul_i32 s88, s88, 52\n"
    "v_mov_b32_e32 v107, 0\n"
    "v_mfma_f32_16x16x32_bf16 v[32:35], v[216:219], a[24:27], v[32:35]\n"
    "s_lshr_b32 s88, s88, 8\n"
    "s_mul_i32 s90, s88, 5\n"
    "v_mfma_f32_16x16x32_bf16 v[36:39], v[220:223], a[24:27], v[36:39]\n"
    "s_lshr_b32 s90, s90, 1\n"
    "ds_write_b128 v9, v[64:67] offset:16384\n"
    "v_mfma_f32_16x16x32_bf16 v[56:59], v[216:219], a[56:59], v[56:59]\n"
    "s_cbranch_vccnz .Lat_h1poll\n"
    ".Lat_h1poll_ret:\n"
    "v_mfma_f32_16x16x32_bf16 v[60:63], v[220:223], a[56:59], v[60:63]\n"
    "s_mov_b32 m0, s63\n"
    "s_sub_u32 s29, s29, s90\n"
    "v_mov_b32_e32 v108, 0\n"
    "v_mfma_f32_16x16x32_bf16 v[72:75], v[216:219], a[88:91], v[72:75]\n"
    "buffer_load_dwordx4 v254, s[8:11], s57 offen offset:0 sc1 lds\n"
    "v_mfma_f32_16x16x32_bf16 v[76:79], v[220:223], a[88:91], v[76:79]\n"
    "s_waitcnt vmcnt(2)\n"
    "s_sub_u32 m0, s63, 1024\n"
    "v_mov_b32_e32 v109, 0\n"
    "v_mfma_f32_16x16x32_bf16 v[32:35], v[232:235], a[28:31], v[32:35]\n"
    "buffer_load_dwordx4 v254, s[8:11], s57 offen offset:2048 sc1 lds\n"
    "v_mfma_f32_16x16x32_bf16 v[36:39], v[236:239], a[28:31], v[36:39]\n"
    "s_add_u32 m0, s63, 3968\n"
    "s_sub_u32 s90, s32, s88\n"
    "v_mov_b32_e32 v110, 0\n"
    "v_mfma_f32_16x16x32_bf16 v[56:59], v[232:235], a[60:63], v[56:59]\n"
    "buffer_load_dwordx4 v254, s[8:11], s57 offen offset:128 sc1 lds\n"
    "v_mfma_f32_16x16x32_bf16 v[60:63], v[236:239], a[60:63], v[60:63]\n"
    "s_add_u32 m0, s63, 2944\n"
    "s_sub_u32 s90, s90, 1\n"
    "s_and_b32 s90, s90, 3\n"
    "v_mfma_f32_16x16x32_bf16 v[72:75], v[232:235], a[92:95], v[72:75]\n"
    "buffer_load_dwordx4 v254, s[8:11], s57 offen offset:2176 sc1 lds\n"
    "v_mfma_f32_16x16x32_bf16 v[76:79], v[236:239], a[92:95], v[76:79]\n"
    "s_lshl2_add_u32 s88, s33, s88\n"
    "s_mul_i32 s88, s88, 3\n"
    "v_mov_b32_e32 v111, 0\n"
    "v_cvt_pk_bf16_f32 v32, v32, v33\n"
    "v_cvt_pk_bf16_f32 v33, v34, v35\n"
    "v_cvt_pk_bf16_f32 v34, v36, v37\n"
    "v_cvt_pk_bf16_f32 v35, v38, v39\n"
    "v_or_b32_e32 v35, v10, v35\n"
    "ds_write_b128 v9, v[32:35] offset:1024\n"
    "buffer_store_dwordx4 v[32:35], v8, s[8:11], s35 offen offset:1024 sc1\n"
    "v_cvt_pk_bf16_f32 v56, v56, v57\n"
    "v_cvt_pk_bf16_f32 v57, v58, v59\n"
    "v_cvt_pk_bf16_f32 v58, v60, v61\n"
    "v_cvt_pk_bf16_f32 v59, v62, v63\n"
    "v_or_b32_e32 v59, v10, v59\n"
    "ds_write_b128 v9, v[56:59] offset:9216\n"
    "buffer_store_dwordx4 v[56:59], v8, s[8:11], s54 offen offset:1024 sc1\n"
    "v_cvt_pk_bf16_f32 v72, v72, v73\n"
    "v_cvt_pk_bf16_f32 v73, v74, v75\n"
    "v_cvt_pk_bf16_f32 v74, v76, v77\n"
    "v_cvt_pk_bf16_f32 v75, v78, v79\n"
    "v_or_b32_e32 v75, v10, v75\n"
    "ds_write_b128 v9, v[72:75] offset:17408\n"
    "buffer_store_dwordx4 v[72:75], v8, s[8:11], s60 offen offset:1024 sc1\n"
    "v_mov_b32_e32 v112, 0\n"
    "v_mov_b32_e32 v113, 0\n"
    "v_mov_b32_e32 v114, 0\n"
    "s_add_u32 m0, s62, 0\n"
    "v_mov_b32_e32 v115, 0\n"
    "v_mov_b32_e32 v116, 0\n"
    "v_mov_b32_e32 v117, 0\n"
    "buffer_load_dwordx4 v12, s[8:11], s40 offen offset:0 sc1 lds\n"
    "v_mov_b32_e32 v118, 0\n"
    "v_mov_b32_e32 v119, 0\n"
    "v_mov_b32_e32 v120, 0\n"
    "buffer_load_dwordx4 v13, s[8:11], s40 offen offset:1024 sc1 lds\n"
    "v_mov_b32_e32 v121, 0\n"
    "v_mov_b32_e32 v122, 0\n"
    "v_mov_b32_e32 v123, 0\n"
    "buffer_load_dwordx4 v14, s[8:11], s40 offen offset:2048 sc1 lds\n"
    "v_mov_b32_e32 v124, 0\n"
    "v_mov_b32_e32 v125, 0\n"
    "v_mov_b32_e32 v126, 0\n"
    "buffer_load_dwordx4 v15, s[8:11], s40 offen offset:3072 sc1 lds\n"
    "v_mov_b32_e32 v127, 0\n"
    "v_mov_b32_e32 v128, 0\n"
    "v_mov_b32_e32 v129, 0\n"
    "s_add_u32 s40, s40, 0x8000\n"
    "v_mov_b32_e32 v130, 0\n"
    "v_mov_b32_e32 v131, 0\n"
    "v_mov_b32_e32 v132, 0\n"
    "s_add_u32 m0, s62, 4096\n"
    "v_mov_b32_e32 v133, 0\n"
    "v_mov_b32_e32 v134, 0\n"
    "v_mov_b32_e32 v135, 0\n"
    "buffer_load_dwordx4 v12, s[8:11], s41 offen offset:0 sc1 lds\n"
    "v_mov_b32_e32 v136, 0\n"
    "v_mov_b32_e32 v137, 0\n"
    "v_mov_b32_e32 v138, 0\n"
    "buffer_load_dwordx4 v13, s[8:11], s41 offen offset:1024 sc1 lds\n"
    "v_mov_b32_e32 v139, 0\n"
    "v_mov_b32_e32 v140, 0\n"
    "v_mov_b32_e32 v141, 0\n"
    "buffer_load_dwordx4 v14, s[8:11], s41 offen offset:2048 sc1 lds\n"
    "v_mov_b32_e32 v142, 0\n"
    "v_mov_b32_e32 v143, 0\n"
    "v_mov_b32_e32 v144, 0\n"
    "buffer_load_dwordx4 v15, s[8:11], s41 offen offset:3072 sc1 lds\n"
    "v_mov_b32_e32 v145, 0\n"
    "v_mov_b32_e32 v146, 0\n"
    "v_mov_b32_e32 v147, 0\n"
    "s_add_u32 s41, s41, 0x8000\n"
    "v_mov_b32_e32 v148, 0\n"
    "v_mov_b32_e32 v149, 0\n"
    "v_mov_b32_e32 v150, 0\n"
    "v_mov_b32_e32 v151, 0\n"
    "s_xor_b32 s45, s34, 0xc000\n"
    "v_add_u32_e32 v204, s45, v240\n"
    "v_add_u32_e32 v205, s45, v241\n"
    "v_add_u32_e32 v206, s45, v242\n"
    "v_add_u32_e32 v207, s45, v243\n"
    "v_add_u32_e32 v208, s45, v244\n"
    "v_add_u32_e32 v209, s45, v245\n"
    "v_add_u32_e32 v210, s45, v246\n"
    "buffer_load_dword v0, v253, s[8:11], s59 offen offset:0 sc1\n"
    "buffer_load_dword v1, v2, s[8:11], s59 offen offset:0 sc1\n"
    "v_add_u32_e32 v211, s45, v247\n"
    "v_add_u32_e32 v200, s34, v240\n"
    "v_add_u32_e32 v201, s34, v241\n"
    "v_add_u32_e32 v202, s34, v242\n"
    "v_add_u32_e32 v203, s34, v243\n"
    "v_mov_b32_e32 v152, v16\n"
    "v_and_b32_e32 v153, 0xf0, v253\n"
    "v_lshrrev_b32_e32 v154, 9, v253\n"
    "v_lshl_add_u32 v153, v154, 8, v153\n"
    "v_bfe_u32 v154, v253, 8, 1\n"
    "v_lshl_add_u32 v153, v154, 3, v153\n"
    "v_mov_b32_e32 v156, 0\n"
    "v_mov_b32_e32 v157, 0\n"
    "v_mov_b32_e32 v158, 0\n"
    "v_mov_b32_e32 v159, 0\n"
    "v_mov_b32_e32 v56, 0\n"
    "v_mov_b32_e32 v57, 0\n"
    "v_mov_b32_e32 v58, 0\n"
    "v_mov_b32_e32 v59, 0\n"
    "v_mov_b32_e32 v60, 0\n"
    "v_mov_b32_e32 v61, 0\n"
    "v_mov_b32_e32 v62, 0\n"
    "v_mov_b32_e32 v63, 0\n"
    "v_mov_b32_e32 v64, 0\n"
    "v_mov_b32_e32 v65, 0\n"
    "v_mov_b32_e32 v66, 0\n"
    "v_mov_b32_e32 v67, 0\n"
    "v_mov_b32_e32 v68, 0\n"
    "v_mov_b32_e32 v69, 0\n"
    "v_mov_b32_e32 v70, 0\n"
    "v_mov_b32_e32 v71, 0\n"
    "v_mov_b32_e32 v72, 0\n"
    "v_mov_b32_e32 v73, 0\n"
    "v_mov_b32_e32 v74, 0\n"
    "v_mov_b32_e32 v75, 0\n"
    "v_mov_b32_e32 v76, 0\n"
    "v_mov_b32_e32 v77, 0\n"
    "v_mov_b32_e32 v78, 0\n"
    "v_mov_b32_e32 v79, 0\n"
    "s_add_u32 s88, s88, s90\n"
    "s_mul_i32 s88, s88, 24832\n"
    "s_add_u32 s88, s88, 16265216\n"
    "s_lshl1_add_u32 s84, s29, s51\n"
    "s_lshl_b32 s84, s84, 12\n"
    "s_add_u32 s84, s84, s88\n"
    "s_lshl2_add_u32 s29, s29, s88\n"
    "s_add_u32 s30, s38, 6\n"
    "s_sub_u32 s88, s30, 10\n"
    "s_min_u32 s30, s30, s88\n"
    "s_lshl_b32 s88, s30, 8\n"
    "s_or_b32 s36, s88, s36\n"
    "s_lshl1_add_u32 s88, s30, 1\n"
    "s_mul_i32 s88, s88, 52\n"
    "s_lshr_b32 s88, s88, 8\n"
    "s_mul_i32 s90, s88, 5\n"
    "s_lshr_b32 s90, s90, 1\n"
    "s_sub_u32 s30, s30, s90\n"
    "s_sub_u32 s90, s32, s88\n"
    "s_sub_u32 s90, s90, 1\n"
    "s_and_b32 s90, s90, 3\n"
    "s_lshl2_add_u32 s88, s33, s88\n"
    "s_mul_i32 s88, s88, 3\n"
    "s_add_u32 s88, s88, s90\n"
    "s_mul_i32 s88, s88, 24832\n"
    "s_add_u32 s88, s88, 16265216\n"
    "s_lshl1_add_u32 s85, s30, s51\n"
    "s_lshl_b32 s85, s85, 12\n"
    "s_add_u32 s85, s85, s88\n"
    "s_lshl2_add_u32 s30, s30, s88\n"
    "s_add_u32 s31, s38, 8\n"
    "s_sub_u32 s88, s31, 10\n"
    "s_min_u32 s31, s31, s88\n"
    "s_lshl_b32 s88, s31, 12\n"
    "s_or_b32 s36, s88, s36\n"
    "s_lshl1_add_u32 s88, s31, 1\n"
    "s_mul_i32 s88, s88, 52\n"
    "s_lshr_b32 s88, s88, 8\n"
    "s_mul_i32 s90, s88, 5\n"
    "s_lshr_b32 s90, s90, 1\n"
    "s_sub_u32 s31, s31, s90\n"
    "s_sub_u32 s90, s32, s88\n"
    "s_sub_u32 s90, s90, 1\n"
    "s_and_b32 s90, s90, 3\n"
    "s_lshl2_add_u32 s88, s33, s88\n"
    "s_mul_i32 s88, s88, 3\n"
    "s_add_u32 s88, s88, s90\n"
    "s_mul_i32 s88, s88, 24832\n"
    "s_add_u32 s88, s88, 16265216\n"
    "s_lshl1_add_u32 s86, s31, s51\n"
    "s_lshl_b32 s86, s86, 12\n"
    "s_add_u32 s86, s86, s88\n"
    "s_lshl2_add_u32 s31, s31, s88\n"
    "s_lshl_b32 s87, %1, 6\n"
    "s_add_u32 s87, s87, %2\n"
    "s_lshl2_add_u32 s87, s87, 5636096\n"
    "s_or_b32 s88, %1, %2\n"
    "s_cselect_b32 s88, 2.0, 5777664\n"
    "s_lshl_b32 s47, s33, 9\n"
    "s_add_u32 s47, s47, 5738496\n"
    "v_lshlrev_b32_e32 v9, 3, v253\n"
    "s_cmp_eq_u32 s50, 0\n"
    "s_cselect_b32 s46, s35, s54\n"
    "s_cselect_b32 s45, s60, 2.0\n"
    "s_cmp_eq_u32 s51, 0\n"
    "s_cselect_b32 s46, s46, s45\n"
    "s_add_u32 s46, s46, 11022336\n"
    "s_waitcnt vmcnt(10)\n"
    "s_waitcnt lgkmcnt(0)\n"
    "s_barrier\n"
    "buffer_store_dword v40, v41, s[8:11], s58 offen offset:64 sc1\n"
    "buffer_load_dword v228, v9, s[8:11], s46 offen offset:0 sc1\n"
    "ds_read_b128 a[0:3], v6 offset:0\n"
    "ds_read_b128 a[4:7], v6 offset:1024\n"
    "ds_read_b128 a[8:11], v6 offset:2048\n"
    "ds_read_b128 a[12:15], v6 offset:3072\n"
    "ds_read_b128 a[16:19], v6 offset:4096\n"
    "ds_read_b128 a[20:23], v6 offset:5120\n"
    "ds_read_b128 a[24:27], v6 offset:6144\n"
    "ds_read_b128 a[28:31], v6 offset:7168\n"
    "s_waitcnt vmcnt(4)\n"
    "s_barrier\n"
    "ds_read_b128 a[160:163], v204 offset:0\n"
    "ds_read_b128 a[164:167], v205 offset:0\n"
    "ds_read_b128 a[168:171], v204 offset:4096\n"
    "ds_read_b128 a[172:175], v205 offset:4096\n"
    "ds_read_b128 a[176:179], v206 offset:0\n"
    "ds_read_b128 a[180:183], v207 offset:0\n"
    "ds_read_b128 a[184:187], v206 offset:4096\n"
    "ds_read_b128 a[188:191], v207 offset:4096\n"
    "s_add_u32 m0, s62, 16384\n"
    "s_nop 0\n"
    "buffer_load_dwordx4 v12, s[8:11], s40 offen offset:0 sc1 lds\n"
    "buffer_load_dwordx4 v13, s[8:11], s40 offen offset:1024 sc1 lds\n"
    "buffer_load_dwordx4 v14, s[8:11], s40 offen offset:2048 sc1 lds\n"
    "buffer_load_dwordx4 v15, s[8:11], s40 offen offset:3072 sc1 lds\n"
    "s_add_u32 s40, s40, 0x8000\n"
    "s_add_u32 m0, s62, 20480\n"
    "s_nop 0\n"
    "buffer_load_dwordx4 v12, s[8:11], s41 offen offset:0 sc1 lds\n"
    "buffer_load_dwordx4 v13, s[8:11], s41 offen offset:1024 sc1 lds\n"
    "buffer_load_dwordx4 v14, s[8:11], s41 offen offset:2048 sc1 lds\n"
    "buffer_load_dwordx4 v15, s[8:11], s41 offen offset:3072 sc1 lds\n"
    "s_add_u32 s41, s41, 0x8000\n"
    "s_waitcnt lgkmcnt(4)\n"
    "v_mfma_f32_16x16x32_bf16 v[220:223], a[160:163], a[0:3], 0\n"
    "ds_read_b128 a[32:35], v7 offset:16384\n"
    "ds_read_b128 a[36:39], v7 offset:17408\n"
    "v_mfma_f32_16x16x32_bf16 v[220:223], a[164:167], a[4:7], v[220:223]\n"
    "ds_read_b128 a[40:43], v7 offset:18432\n"
    "ds_read_b128 a[44:47], v7 offset:19456\n"
    "v_mfma_f32_16x16x32_bf16 v[220:223], a[168:171], a[8:11], v[220:223]\n"
    "ds_read_b128 a[48:51], v7 offset:20480\n"
    "ds_read_b128 a[52:55], v7 offset:21504\n"
    "v_mfma_f32_16x16x32_bf16 v[220:223], a[172:175], a[12:15], v[220:223]\n"
    "ds_read_b128 a[56:59], v7 offset:22528\n"
    "ds_read_b128 a[60:63], v7 offset:23552\n"
    "s_waitcnt lgkmcnt(8)\n"
    "v_mfma_f32_16x16x32_bf16 v[220:223], a[176:179], a[16:19], v[220:223]\n"
    "ds_read_b64_tr_b16 a[232:233], v208 offset:0\n"
    "ds_read_b64_tr_b16 a[234:235], v209 offset:0\n"
    "v_mfma_f32_16x16x32_bf16 v[220:223], a[180:183], a[20:23], v[220:223]\n"
    "ds_read_b64_tr_b16 a[236:237], v210 offset:0\n"
    "ds_read_b64_tr_b16 a[238:239], v211 offset:0\n"
    "v_mfma_f32_16x16x32_bf16 v[220:223], a[184:187], a[24:27], v[220:223]\n"
    "ds_read_b64_tr_b16 a[240:241], v208 offset:4096\n"
    "ds_read_b64_tr_b16 a[242:243], v209 offset:4096\n"
    "v_mfma_f32_16x16x32_bf16 v[220:223], a[188:191], a[28:31], v[220:223]\n"
    "buffer_load_dword v11, v5, s[8:11], s47 offen offset:0 sc1\n"
    "ds_read_b64_tr_b16 a[244:245], v210 offset:4096\n"
    "s_waitcnt lgkmcnt(11)\n"
    "v_mfma_f32_16x16x32_bf16 v[224:227], a[160:163], a[32:35], 0\n"
    "ds_read_b64_tr_b16 a[246:247], v211 offset:4096\n"
    "ds_read_b128 a[200:203], v203 offset:6144\n"
    "v_mfma_f32_16x16x32_bf16 v[224:227], a[164:167], a[36:39], v[224:227]\n"
    "ds_read_b128 a[204:207], v200 offset:2048\n"
    "ds_read_b128 a[208:211], v201 offset:2048\n"
    "v_mfma_f32_16x16x32_bf16 v[224:227], a[168:171], a[40:43], v[224:227]\n"
    "ds_read_b128 a[212:215], v200 offset:6144\n"
    "ds_read_b128 a[216:219], v201 offset:6144\n"
    "v_mfma_f32_16x16x32_bf16 v[224:227], a[172:175], a[44:47], v[224:227]\n"
    "ds_read_b128 a[220:223], v202 offset:2048\n"
    "ds_read_b128 a[224:227], v203 offset:2048\n"
    "s_waitcnt lgkmcnt(15)\n"
    "v_mfma_f32_16x16x32_bf16 v[224:227], a[176:179], a[48:51], v[224:227]\n"
    "ds_read_b128 a[228:231], v202 offset:6144\n"
    "v_mfma_f32_16x16x32_bf16 v[224:227], a[180:183], a[52:55], v[224:227]\n"
    "v_mfma_f32_16x16x32_bf16 v[224:227], a[184:187], a[56:59], v[224:227]\n"
    "v_mfma_f32_16x16x32_bf16 v[224:227], a[188:191], a[60:63], v[224:227]\n"
    "s_andn2_b32 s61, s32, s50\n"
    "s_bfe_i32 s61, s61, 0x10000\n"
    "ds_read_b128 a[160:163], v203 offset:4096\n"
    "ds_read_b128 a[164:167], v200 offset:0\n"
    "ds_read_b128 a[168:171], v201 offset:0\n"
    "ds_read_b128 a[172:175], v200 offset:4096\n"
    "ds_read_b128 a[176:179], v201 offset:4096\n"
    "ds_read_b128 a[180:183], v202 offset:0\n"
    "ds_read_b128 a[184:187], v203 offset:0\n"
    "ds_read_b128 a[188:191], v202 offset:4096\n"
    "s_and_b32 s57, s63, 0xffffdfff\n"
    "s_sub_u32 s57, s57, 16384\n"
    "v_add_u32_e32 v216, s57, v253\n"
    "s_lshl_b32 s45, s51, 12\n"
    "s_add_u32 s57, s57, s45\n"
    "s_mul_i32 s48, s33, 81920\n"
    "s_add_u32 s48, s48, s45\n"
    "s_add_u32 s48, s48, 11022336\n"
    "v_exp_f32_e32 v220, v220\n"
    "v_exp_f32_e32 v221, v221\n"
    "v_exp_f32_e32 v222, v222\n"
    "v_exp_f32_e32 v223, v223\n"
    "v_exp_f32_e32 v224, v224\n"
    "v_exp_f32_e32 v225, v225\n"
    "v_exp_f32_e32 v226, v226\n"
    "v_exp_f32_e32 v227, v227\n"
    "v_cvt_pk_bf16_f32 v220, v220, v221\n"
    "v_cvt_pk_bf16_f32 v221, v222, v223\n"
    "v_cvt_pk_bf16_f32 v222, v224, v225\n"
    "v_cvt_pk_bf16_f32 v223, v226, v227\n"
    "v_and_b32_e32 v222, s61, v222\n"
    "v_and_b32_e32 v223, s61, v223\n"
    "s_waitcnt vmcnt(11)\n"
    "s_waitcnt lgkmcnt(0)\n"
    "s_barrier\n"
    "v_xor_b32_e32 v1, s39, v1\n"
    "v_xor_b32_e32 v0, s39, v0\n"
    "v_or_b32_e32 v0, v0, v1\n"
    "v_cmp_ne_u32_e32 vcc, 0, v0\n"
    "s_nop 3\n"
    "s_cbranch_vccnz .Lat_hpoll\n"
    ".Lat_hpoll_ret:\n"
    "s_waitcnt vmcnt(0)\n"
    "v_cmp_ne_u32_e32 vcc, s39, v11\n"
    "s_nop 3\n"
    "s_cbranch_vccnz .Lat_qpoll\n"
    ".Lat_qpoll_ret:\n"
    "s_cmp_eq_u32 s50, 0\n"
    "s_cbranch_scc1 .Lat_qx\n"
    "s_bfe_u32 s45, s36, 0x40000\n"
    "s_add_u32 m0, s57, 0\n"
    "s_lshl_b32 s45, s45, 13\n"
    "s_add_u32 s45, s45, s48\n"
    "buffer_load_dwordx4 v253, s[8:11], s45 offen offset:0 sc1 lds\n"
    "buffer_load_dwordx4 v253, s[8:11], s45 offen offset:1024 sc1 lds\n"
    "buffer_load_dwordx4 v253, s[8:11], s45 offen offset:2048 sc1 lds\n"
    "buffer_load_dwordx4 v253, s[8:11], s45 offen offset:3072 sc1 lds\n"
    ".Lat_qx:\n"
    "s_bfe_u32 s45, s36, 0x40004\n"
    "s_add_u32 m0, s57, 8192\n"
    "s_lshl_b32 s45, s45, 13\n"
    "s_add_u32 s45, s45, s48\n"
    "buffer_load_dwordx4 v253, s[8:11], s45 offen offset:0 sc1 lds\n"
    "buffer_load_dwordx4 v253, s[8:11], s45 offen offset:1024 sc1 lds\n"
    "buffer_load_dwordx4 v253, s[8:11], s45 offen offset:2048 sc1 lds\n"
    "buffer_load_dwordx4 v253, s[8:11], s45 offen offset:3072 sc1 lds\n"
    "v_mfma_f32_16x16x16_bf16 v[52:55], a[246:247], v[222:223], 0\n"
    "v_mfma_f32_16x16x16_bf16 v[48:51], a[244:245], v[222:223], 0\n"
    "s_bfe_u32 s45, s36, 0x40008\n"
    "s_add_u32 m0, s57, 16384\n"
    "s_lshl_b32 s45, s45, 13\n"
    "s_add_u32 s45, s45, s48\n"
    "v_mfma_f32_16x16x16_bf16 v[44:47], a[242:243], v[222:223], 0\n"
    "buffer_load_dwordx4 v253, s[8:11], s45 offen offset:0 sc1 lds\n"
    "v_mfma_f32_16x16x16_bf16 v[40:43], a[240:241], v[222:223], 0\n"
    "v_mfma_f32_16x16x16_bf16 v[36:39], a[238:239], v[222:223], 0\n"
    "buffer_load_dwordx4 v253, s[8:11], s45 offen offset:1024 sc1 lds\n"
    "v_mfma_f32_16x16x16_bf16 v[32:35], a[236:237], v[222:223], 0\n"
    "v_mfma_f32_16x16x16_bf16 a[252:255], a[234:235], v[222:223], 0\n"
    "buffer_load_dwordx4 v253, s[8:11], s45 offen offset:2048 sc1 lds\n"
    "v_mfma_f32_16x16x16_bf16 a[248:251], a[232:233], v[222:223], 0\n"
    "v_mfma_f32_16x16x16_bf16 v[28:31], a[246:247], v[220:221], 0\n"
    "buffer_load_dwordx4 v253, s[8:11], s45 offen offset:3072 sc1 lds\n"
    "v_mfma_f32_16x16x16_bf16 v[24:27], a[244:245], v[220:221], 0\n"
    "s_bfe_u32 s45, s36, 0x4000c\n"
    "s_add_u32 m0, s57, 24576\n"
    "s_lshl_b32 s45, s45, 13\n"
    "s_add_u32 s45, s45, s48\n"
    "v_mfma_f32_16x16x16_bf16 v[20:23], a[242:243], v[220:221], 0\n"
    "buffer_load_dwordx4 v253, s[8:11], s45 offen offset:0 sc1 lds\n"
    "v_mfma_f32_16x16x16_bf16 v[16:19], a[240:241], v[220:221], 0\n"
    "v_mfma_f32_16x16x16_bf16 v[12:15], a[238:239], v[220:221], 0\n"
    "buffer_load_dwordx4 v253, s[8:11], s45 offen offset:1024 sc1 lds\n"
    "v_mfma_f32_16x16x16_bf16 v[8:11], a[236:237], v[220:221], 0\n"
    "v_mfma_f32_16x16x16_bf16 v[4:7], a[234:235], v[220:221], 0\n"
    "buffer_load_dwordx4 v253, s[8:11], s45 offen offset:2048 sc1 lds\n"
    "v_mfma_f32_16x16x16_bf16 v[0:3], a[232:233], v[220:221], 0\n"
    "v_mfma_f32_16x16x32_bf16 v[160:163], a[160:163], a[28:31], 0\n"
    "buffer_load_dwordx4 v253, s[8:11], s45 offen offset:3072 sc1 lds\n"
    "v_mfma_f32_16x16x32_bf16 v[160:163], a[164:167], a[0:3], v[160:163]\n"
    "s_mov_b32 m0, s49\n"
    "v_mfma_f32_16x16x32_bf16 v[160:163], a[168:171], a[4:7], v[160:163]\n"
    "v_mfma_f32_16x16x32_bf16 v[160:163], a[172:175], a[8:11], v[160:163]\n"
    "v_mfma_f32_16x16x32_bf16 v[160:163], a[176:179], a[12:15], v[160:163]\n"
    "v_mfma_f32_16x16x32_bf16 v[160:163], a[180:183], a[16:19], v[160:163]\n"
    "v_mfma_f32_16x16x32_bf16 v[160:163], a[184:187], a[20:23], v[160:163]\n"
    "v_mfma_f32_16x16x32_bf16 v[160:163], a[188:191], a[24:27], v[160:163]\n"
    "v_mfma_f32_16x16x32_bf16 v[164:167], a[200:203], a[28:31], 0\n"
    "v_mfma_f32_16x16x32_bf16 v[164:167], a[204:207], a[0:3], v[164:167]\n"
    "v_mfma_f32_16x16x32_bf16 v[164:167], a[208:211], a[4:7], v[164:167]\n"
    "v_exp_f32_e32 v160, v160\n"
    "v_mfma_f32_16x16x32_bf16 v[164:167], a[212:215], a[8:11], v[164:167]\n"
    "v_exp_f32_e32 v161, v161\n"
    "v_mfma_f32_16x16x32_bf16 v[164:167], a[216:219], a[12:15], v[164:167]\n"
    "v_exp_f32_e32 v162, v162\n"
    "v_mfma_f32_16x16x32_bf16 v[164:167], a[220:223], a[16:19], v[164:167]\n"
    "v_exp_f32_e32 v163, v163\n"
    "v_mfma_f32_16x16x32_bf16 v[164:167], a[224:227], a[20:23], v[164:167]\n"
    "v_cvt_pk_bf16_f32 v160, v160, v161\n"
    "v_mfma_f32_16x16x32_bf16 v[164:167], a[228:231], a[24:27], v[164:167]\n"
    "s_waitcnt vmcnt(8)\n"
    "s_barrier\n"
    "s_cmp_eq_u32 s50, 0\n"
    "s_cbranch_scc1 .Lat_qy\n"
    "ds_read_b128 a[60:63], v216 offset:7168\n"
    "ds_read_b128 a[32:35], v216 offset:0\n"
    "ds_read_b128 a[36:39], v216 offset:1024\n"
    "ds_read_b128 a[40:43], v216 offset:2048\n"
    "ds_read_b128 a[44:47], v216 offset:3072\n"
    "ds_read_b128 a[48:51], v216 offset:4096\n"
    "ds_read_b128 a[52:55], v216 offset:5120\n"
    "ds_read_b128 a[56:59], v216 offset:6144\n"
    ".Lat_qy:\n"
    "ds_read_b128 a[92:95], v216 offset:15360\n"
    "ds_read_b128 a[64:67], v216 offset:8192\n"
    "s_waitcnt lgkmcnt(8)\n"
    "v_mfma_f32_16x16x32_bf16 v[168:171], a[160:163], a[60:63], 0\n"
    "v_cvt_pk_bf16_f32 v161, v162, v163\n"
    "v_mfma_f32_16x16x32_bf16 v[168:171], a[164:167], a[32:35], v[168:171]\n"
    "s_waitcnt lgkmcnt(6)\n"
    "ds_read_b128 a[68:71], v216 offset:9216\n"
    "v_mfma_f32_16x16x32_bf16 v[168:171], a[168:171], a[36:39], v[168:171]\n"
    "ds_read_b128 a[72:75], v216 offset:10240\n"
    "v_mfma_f32_16x16x32_bf16 v[168:171], a[172:175], a[40:43], v[168:171]\n"
    "s_waitcnt lgkmcnt(6)\n"
    "ds_read_b128 a[76:79], v216 offset:11264\n"
    "ds_write_addtid_b32 v160 offset:0\n"
    "v_mfma_f32_16x16x32_bf16 v[168:171], a[176:179], a[44:47], v[168:171]\n"
    "v_exp_f32_e32 v164, v164\n"
    "v_mfma_f32_16x16x32_bf16 v[168:171], a[180:183], a[48:51], v[168:171]\n"
    "s_waitcnt lgkmcnt(6)\n"
    "ds_read_b128 a[80:83], v216 offset:12288\n"
    "v_mfma_f32_16x16x32_bf16 v[168:171], a[184:187], a[52:55], v[168:171]\n"
    "ds_read_b128 a[84:87], v216 offset:13312\n"
    "v_mfma_f32_16x16x32_bf16 v[168:171], a[188:191], a[56:59], v[168:171]\n"
    "ds_read_b128 a[88:91], v216 offset:14336\n"
    "ds_write_addtid_b32 v161 offset:256\n"
    "v_mfma_f32_16x16x32_bf16 v[172:175], a[200:203], a[60:63], 0\n"
    "v_exp_f32_e32 v165, v165\n"
    "v_mfma_f32_16x16x32_bf16 v[172:175], a[204:207], a[32:35], v[172:175]\n"
    "v_exp_f32_e32 v166, v166\n"
    "v_mfma_f32_16x16x32_bf16 v[172:175], a[208:211], a[36:39], v[172:175]\n"
    "v_exp_f32_e32 v167, v167\n"
    "v_mfma_f32_16x16x32_bf16 v[172:175], a[212:215], a[40:43], v[172:175]\n"
    "v_cvt_pk_bf16_f32 v162, v164, v165\n"
    "v_cvt_pk_bf16_f32 v163, v166, v167\n"
    "v_mfma_f32_16x16x32_bf16 v[172:175], a[216:219], a[44:47], v[172:175]\n"
    "ds_write_addtid_b32 v162 offset:512\n"
    "v_exp_f32_e32 v168, v168\n"
    "v_mfma_f32_16x16x32_bf16 v[172:175], a[220:223], a[48:51], v[172:175]\n"
    "v_exp_f32_e32 v169, v169\n"
    "v_mfma_f32_16x16x32_bf16 v[172:175], a[224:227], a[52:55], v[172:175]\n"
    "v_exp_f32_e32 v170, v170\n"
    "v_mfma_f32_16x16x32_bf16 v[172:175], a[228:231], a[56:59], v[172:175]\n"
    "s_waitcnt lgkmcnt(9)\n"
    "v_exp_f32_e32 v171, v171\n"
    "v_mfma_f32_16x16x32_bf16 v[176:179], a[160:163], a[92:95], 0\n"
    "ds_write_addtid_b32 v163 offset:768\n"
    "v_cvt_pk_bf16_f32 v168, v168, v169\n"
    "v_mfma_f32_16x16x32_bf16 v[176:179], a[164:167], a[64:67], v[176:179]\n"
    "s_waitcnt lgkmcnt(8)\n"
    "ds_write_addtid_b32 v168 offset:4096\n"
    "v_cvt_pk_bf16_f32 v169, v170, v171\n"
    "v_mfma_f32_16x16x32_bf16 v[176:179], a[168:171], a[68:71], v[176:179]\n"
    "v_exp_f32_e32 v172, v172\n"
    "v_mfma_f32_16x16x32_bf16 v[176:179], a[172:175], a[72:75], v[176:179]\n"
    "s_waitcnt lgkmcnt(6)\n"
    "v_exp_f32_e32 v173, v173\n"
    "v_mfma_f32_16x16x32_bf16 v[176:179], a[176:179], a[76:79], v[176:179]\n"
    "v_exp_f32_e32 v174, v174\n"
    "v_mfma_f32_16x16x32_bf16 v[176:179], a[180:183], a[80:83], v[176:179]\n"
    "s_waitcnt vmcnt(0)\n"
    "s_barrier\n"
    "s_waitcnt lgkmcnt(4)\n"
    "v_mfma_f32_16x16x32_bf16 v[176:179], a[184:187], a[84:87], v[176:179]\n"
    "ds_write_addtid_b32 v169 offset:4352\n"
    "v_exp_f32_e32 v175, v175\n"
    "v_mfma_f32_16x16x32_bf16 v[176:179], a[188:191], a[88:91], v[176:179]\n"
    "ds_read_b128 a[124:127], v216 offset:23552\n"
    "v_cvt_pk_bf16_f32 v170, v172, v173\n"
    "v_mfma_f32_16x16x32_bf16 v[180:183], a[200:203], a[92:95], 0\n"
    "ds_read_b128 a[96:99], v216 offset:16384\n"
    "v_cvt_pk_bf16_f32 v171, v174, v175\n"
    "v_mfma_f32_16x16x32_bf16 v[180:183], a[204:207], a[64:67], v[180:183]\n"
    "ds_read_b128 a[100:103], v216 offset:17408\n"
    "v_exp_f32_e32 v176, v176\n"
    "v_mfma_f32_16x16x32_bf16 v[180:183], a[208:211], a[68:71], v[180:183]\n"
    "ds_read_b128 a[104:107], v216 offset:18432\n"
    "ds_write_addtid_b32 v170 offset:4608\n"
    "v_mfma_f32_16x16x32_bf16 v[180:183], a[212:215], a[72:75], v[180:183]\n"
    "ds_read_b128 a[108:111], v216 offset:19456\n"
    "v_exp_f32_e32 v177, v177\n"
    "v_mfma_f32_16x16x32_bf16 v[180:183], a[216:219], a[76:79], v[180:183]\n"
    "ds_read_b128 a[112:115], v216 offset:20480\n"
    "v_exp_f32_e32 v178, v178\n"
    "v_mfma_f32_16x16x32_bf16 v[180:183], a[220:223], a[80:83], v[180:183]\n"
    "ds_read_b128 a[116:119], v216 offset:21504\n"
    "ds_write_addtid_b32 v171 offset:4864\n"
    "v_mfma_f32_16x16x32_bf16 v[180:183], a[224:227], a[84:87], v[180:183]\n"
    "ds_read_b128 a[120:123], v216 offset:22528\n"
    "v_mfma_f32_16x16x32_bf16 v[180:183], a[228:231], a[88:91], v[180:183]\n"
    "s_waitcnt lgkmcnt(8)\n"
    "s_sub_u32 m0, s63, 16384\n"
    "v_exp_f32_e32 v179, v179\n"
    "v_mfma_f32_16x16x32_bf16 v[184:187], a[160:163], a[124:127], 0\n"
    "ds_read_b128 a[156:159], v216 offset:31744\n"
    "buffer_load_dwordx4 v248, s[8:11], s40 offen offset:0 sc1 lds\n"
    "v_cvt_pk_bf16_f32 v176, v176, v177\n"
    "v_mfma_f32_16x16x32_bf16 v[184:187], a[164:167], a[96:99], v[184:187]\n"
    "s_waitcnt lgkmcnt(7)\n"
    "ds_read_b128 a[128:131], v216 offset:24576\n"
    "v_cvt_pk_bf16_f32 v177, v178, v179\n"
    "v_mfma_f32_16x16x32_bf16 v[184:187], a[168:171], a[100:103], v[184:187]\n"
    "ds_read_b128 a[132:135], v216 offset:25600\n"
    "v_mfma_f32_16x16x32_bf16 v[184:187], a[172:175], a[104:107], v[184:187]\n"
    "s_waitcnt lgkmcnt(6)\n"
    "ds_read_b128 a[136:139], v216 offset:26624\n"
    "v_mfma_f32_16x16x32_bf16 v[184:187], a[176:179], a[108:111], v[184:187]\n"
    "ds_read_b128 a[140:143], v216 offset:27648\n"
    "buffer_load_dwordx4 v248, s[8:11], s40 offen offset:1024 sc1 lds\n"
    "v_mfma_f32_16x16x32_bf16 v[184:187], a[180:183], a[112:115], v[184:187]\n"
    "s_waitcnt lgkmcnt(5)\n"
    "ds_read_b128 a[144:147], v216 offset:28672\n"
    "v_mfma_f32_16x16x32_bf16 v[184:187], a[184:187], a[116:119], v[184:187]\n"
    "ds_read_b128 a[148:151], v216 offset:29696\n"
    "v_mfma_f32_16x16x32_bf16 v[184:187], a[188:191], a[120:123], v[184:187]\n"
    "ds_read_b128 a[152:155], v216 offset:30720\n"
    "v_mfma_f32_16x16x32_bf16 v[188:191], a[200:203], a[124:127], 0\n"
    "buffer_load_dwordx4 v248, s[8:11], s40 offen offset:2048 sc1 lds\n"
    "v_exp_f32_e32 v180, v180\n"
    "v_mfma_f32_16x16x32_bf16 v[188:191], a[204:207], a[96:99], v[188:191]\n"
    "ds_write_b32 v249, v176 offset:8192\n"
    "v_exp_f32_e32 v181, v181\n"
    "v_mfma_f32_16x16x32_bf16 v[188:191], a[208:211], a[100:103], v[188:191]\n"
    "ds_write_b32 v249, v177 offset:8448\n"
    "v_exp_f32_e32 v182, v182\n"
    "v_mfma_f32_16x16x32_bf16 v[188:191], a[212:215], a[104:107], v[188:191]\n"
    "v_exp_f32_e32 v183, v183\n"
    "v_mfma_f32_16x16x32_bf16 v[188:191], a[216:219], a[108:111], v[188:191]\n"
    "buffer_load_dwordx4 v248, s[8:11], s40 offen offset:3072 sc1 lds\n"
    "v_cvt_pk_bf16_f32 v178, v180, v181\n"
    "v_cvt_pk_bf16_f32 v179, v182, v183\n"
    "v_mfma_f32_16x16x32_bf16 v[188:191], a[220:223], a[112:115], v[188:191]\n"
    "s_add_u32 s40, s40, 0x8000\n"
    "ds_write_b32 v249, v178 offset:8704\n"
    "v_exp_f32_e32 v184, v184\n"
    "v_mfma_f32_16x16x32_bf16 v[188:191], a[224:227], a[116:119], v[188:191]\n"
    "s_sub_u32 m0, s63, 12288\n"
    "ds_write_b32 v249, v179 offset:8960\n"
    "v_exp_f32_e32 v185, v185\n"
    "v_mfma_f32_16x16x32_bf16 v[188:191], a[228:231], a[120:123], v[188:191]\n"
    "s_waitcnt lgkmcnt(10)\n"
    "v_exp_f32_e32 v186, v186\n"
    "v_mfma_f32_16x16x32_bf16 v[192:195], a[160:163], a[156:159], 0\n"
    "buffer_load_dwordx4 v248, s[8:11], s41 offen offset:0 sc1 lds\n"
    "v_exp_f32_e32 v187, v187\n"
    "v_mfma_f32_16x16x32_bf16 v[192:195], a[164:167], a[128:131], v[192:195]\n"
    "s_waitcnt lgkmcnt(8)\n"
    "ds_read_b128 a[160:163], v203 offset:20480\n"
    "v_cvt_pk_bf16_f32 v184, v184, v185\n"
    "v_mfma_f32_16x16x32_bf16 v[192:195], a[168:171], a[132:135], v[192:195]\n"
    "ds_read_b128 a[164:167], v200 offset:16384\n"
    "v_cvt_pk_bf16_f32 v185, v186, v187\n"
    "v_mfma_f32_16x16x32_bf16 v[192:195], a[172:175], a[136:139], v[192:195]\n"
    "s_waitcnt lgkmcnt(8)\n"
    "ds_read_b128 a[168:171], v201 offset:16384\n"
    "v_exp_f32_e32 v188, v188\n"
    "v_mfma_f32_16x16x32_bf16 v[192:195], a[176:179], a[140:143], v[192:195]\n"
    "ds_read_b128 a[172:175], v200 offset:20480\n"
    "buffer_load_dwordx4 v248, s[8:11], s41 offen offset:1024 sc1 lds\n"
    "v_exp_f32_e32 v189, v189\n"
    "v_mfma_f32_16x16x32_bf16 v[192:195], a[180:183], a[144:147], v[192:195]\n"
    "s_waitcnt lgkmcnt(8)\n"
    "ds_read_b128 a[176:179], v201 offset:20480\n"
    "v_exp_f32_e32 v190, v190\n"
    "v_mfma_f32_16x16x32_bf16 v[192:195], a[184:187], a[148:151], v[192:195]\n"
    "ds_read_b128 a[180:183], v202 offset:16384\n"
    "v_mfma_f32_16x16x32_bf16 v[192:195], a[188:191], a[152:155], v[192:195]\n"
    "ds_read_b128 a[184:187], v203 offset:16384\n"
    "v_mfma_f32_16x16x32_bf16 v[196:199], a[200:203], a[156:159], 0\n"
    "ds_read_b128 a[188:191], v202 offset:20480\n"
    "buffer_load_dwordx4 v248, s[8:11], s41 offen offset:2048 sc1 lds\n"
    "v_mfma_f32_16x16x32_bf16 v[196:199], a[204:207], a[128:131], v[196:199]\n"
    "ds_write_b32 v249, v184 offset:12288\n"
    "v_exp_f32_e32 v191, v191\n"
    "v_mfma_f32_16x16x32_bf16 v[196:199], a[208:211], a[132:135], v[196:199]\n"
    "ds_write_b32 v249, v185 offset:12544\n"
    "v_cvt_pk_bf16_f32 v186, v188, v189\n"
    "v_cvt_pk_bf16_f32 v187, v190, v191\n"
    "v_mfma_f32_16x16x32_bf16 v[196:199], a[212:215], a[136:139], v[196:199]\n"
    "ds_write_b32 v249, v186 offset:12800\n"
    "v_exp_f32_e32 v192, v192\n"
    "v_mfma_f32_16x16x32_bf16 v[196:199], a[216:219], a[140:143], v[196:199]\n"
    "buffer_load_dwordx4 v248, s[8:11], s41 offen offset:3072 sc1 lds\n"
    "v_exp_f32_e32 v193, v193\n"
    "v_mfma_f32_16x16x32_bf16 v[196:199], a[220:223], a[144:147], v[196:199]\n"
    "s_add_u32 s41, s41, 0x8000\n"
    "ds_write_b32 v249, v187 offset:13056\n"
    "v_exp_f32_e32 v194, v194\n"
    "v_mfma_f32_16x16x32_bf16 v[196:199], a[224:227], a[148:151], v[196:199]\n"
    "v_exp_f32_e32 v195, v195\n"
    "v_mfma_f32_16x16x32_bf16 v[196:199], a[228:231], a[152:155], v[196:199]\n"
    "v_cvt_pk_bf16_f32 v192, v192, v193\n"
    "ds_write_b32 v249, v192 offset:16384\n"
    "v_cvt_pk_bf16_f32 v193, v194, v195\n"
    "ds_write_b32 v249, v193 offset:16640\n"
    "s_cmp_lg_u32 s34, 0\n"
    "s_cselect_b32 s44, 2, 1\n"
    "s_waitcnt lgkmcnt(4)\n"
    "s_nop 0\n"
    "v_exp_f32_e32 v196, v196\n"
    "v_exp_f32_e32 v197, v197\n"
    "v_exp_f32_e32 v198, v198\n"
    "v_exp_f32_e32 v199, v199\n"
    "v_cvt_pk_bf16_f32 v194, v196, v197\n"
    "v_cvt_pk_bf16_f32 v195, v198, v199\n"
    "ds_write_b32 v249, v194 offset:16896\n"
    "ds_write_b32 v249, v195 offset:17152\n"
    "s_cbranch_scc1 .Lat_p2e\n"
    ".p2align 6\n"
    ".Lat_p:\n"
    "v_mfma_f32_16x16x32_bf16 v[200:203], a[160:163], a[28:31], 0\n"
    "v_mfma_f32_16x16x32_bf16 v[200:203], a[164:167], a[0:3], v[200:203]\n"
    "v_mfma_f32_16x16x32_bf16 v[208:211], a[164:167], a[32:35], 0\n"
    "v_mfma_f32_16x16x32_bf16 v[208:211], a[160:163], a[60:63], v[208:211]\n"
    "v_mfma_f32_16x16x32_bf16 v[216:219], a[160:163], a[92:95], 0\n"
    "v_mfma_f32_16x16x32_bf16 v[216:219], a[164:167], a[64:67], v[216:219]\n"
    "s_waitcnt lgkmcnt(0)\n"
    "v_mfma_f32_16x16x32_bf16 v[224:227], a[164:167], a[96:99], 0\n"
    "s_barrier\n"
    "v_mfma_f32_16x16x32_bf16 v[224:227], a[160:163], a[124:127], v[224:227]\n"
    "s_add_u32 m0, s43, 49152\n"
    "ds_read2st64_b32 v[164:165], v250 offset0:0 offset1:1\n"
    "v_mfma_f32_16x16x32_bf16 v[232:235], a[160:163], a[156:159], 0\n"
    "ds_read_b128 a[160:163], v243 offset:22528\n"
    "v_mfma_f32_16x16x32_bf16 v[232:235], a[164:167], a[128:131], v[232:235]\n"
    "ds_read_b128 a[164:167], v240 offset:18432\n"
    "v_mfma_f32_16x16x32_bf16 v[232:235], a[168:171], a[132:135], v[232:235]\n"
    "buffer_load_dwordx4 v248, s[8:11], s40 offen offset:0 sc1 lds\n"
    "v_mfma_f32_16x16x32_bf16 v[232:235], a[172:175], a[136:139], v[232:235]\n"
    "v_mfma_f32_16x16x32_bf16 v[224:227], a[172:175], a[104:107], v[224:227]\n"
    "v_mfma_f32_16x16x32_bf16 v[224:227], a[168:171], a[100:103], v[224:227]\n"
    "ds_read2st64_b32 v[166:167], v250 offset0:2 offset1:3\n"
    "v_mfma_f32_16x16x32_bf16 v[216:219], a[168:171], a[68:71], v[216:219]\n"
    "v_mfma_f32_16x16x32_bf16 v[216:219], a[172:175], a[72:75], v[216:219]\n"
    "buffer_load_dwordx4 v248, s[8:11], s40 offen offset:1024 sc1 lds\n"
    "v_mfma_f32_16x16x32_bf16 v[208:211], a[172:175], a[40:43], v[208:211]\n"
    "v_mfma_f32_16x16x32_bf16 v[208:211], a[168:171], a[36:39], v[208:211]\n"
    "ds_read2st64_b32 v[172:173], v250 offset0:16 offset1:17\n"
    "v_mfma_f32_16x16x32_bf16 v[200:203], a[168:171], a[4:7], v[200:203]\n"
    "ds_read_b128 a[168:171], v241 offset:18432\n"
    "v_mfma_f32_16x16x32_bf16 v[200:203], a[172:175], a[8:11], v[200:203]\n"
    "ds_read_b128 a[172:175], v240 offset:22528\n"
    "v_mfma_f32_16x16x32_bf16 v[200:203], a[176:179], a[12:15], v[200:203]\n"
    "buffer_load_dwordx4 v248, s[8:11], s40 offen offset:2048 sc1 lds\n"
    "v_mfma_f32_16x16x32_bf16 v[200:203], a[180:183], a[16:19], v[200:203]\n"
    "ds_read2st64_b32 v[174:175], v250 offset0:18 offset1:19\n"
    "v_mfma_f32_16x16x32_bf16 v[208:211], a[180:183], a[48:51], v[208:211]\n"
    "v_mfma_f32_16x16x32_bf16 v[208:211], a[176:179], a[44:47], v[208:211]\n"
    "v_mfma_f32_16x16x32_bf16 v[216:219], a[176:179], a[76:79], v[216:219]\n"
    "ds_read2st64_b32 v[180:181], v250 offset0:32 offset1:33\n"
    "v_mfma_f32_16x16x32_bf16 v[216:219], a[180:183], a[80:83], v[216:219]\n"
    "buffer_load_dwordx4 v248, s[8:11], s40 offen offset:3072 sc1 lds\n"
    "v_mfma_f32_16x16x32_bf16 v[224:227], a[180:183], a[112:115], v[224:227]\n"
    "s_add_u32 m0, s43, 53248\n"
    "v_mfma_f32_16x16x32_bf16 v[224:227], a[176:179], a[108:111], v[224:227]\n"
    "s_add_u32 s40, s40, 0x8000\n"
    "ds_read2st64_b32 v[182:183], v250 offset0:34 offset1:35\n"
    "v_mfma_f32_16x16x32_bf16 v[232:235], a[176:179], a[140:143], v[232:235]\n"
    "ds_read_b128 a[176:179], v241 offset:22528\n"
    "v_mfma_f32_16x16x32_bf16 v[232:235], a[180:183], a[144:147], v[232:235]\n"
    "ds_read_b128 a[180:183], v242 offset:18432\n"
    "v_mfma_f32_16x16x32_bf16 v[232:235], a[184:187], a[148:151], v[232:235]\n"
    "buffer_load_dwordx4 v248, s[8:11], s41 offen offset:0 sc1 lds\n"
    "v_mfma_f32_16x16x32_bf16 v[232:235], a[188:191], a[152:155], v[232:235]\n"
    "v_mfma_f32_16x16x32_bf16 v[224:227], a[188:191], a[120:123], v[224:227]\n"
    "ds_read2st64_b32 v[188:189], v250 offset0:48 offset1:49\n"
    "v_mfma_f32_16x16x32_bf16 v[224:227], a[184:187], a[116:119], v[224:227]\n"
    "v_mfma_f32_16x16x32_bf16 v[216:219], a[184:187], a[84:87], v[216:219]\n"
    "v_exp_f32_e32 v232, v232\n"
    "v_mfma_f32_16x16x32_bf16 v[216:219], a[188:191], a[88:91], v[216:219]\n"
    "buffer_load_dwordx4 v248, s[8:11], s41 offen offset:1024 sc1 lds\n"
    "v_mfma_f32_16x16x32_bf16 v[208:211], a[188:191], a[56:59], v[208:211]\n"
    "ds_read2st64_b32 v[190:191], v250 offset0:50 offset1:51\n"
    "v_mfma_f32_16x16x32_bf16 v[208:211], a[184:187], a[52:55], v[208:211]\n"
    "v_exp_f32_e32 v233, v233\n"
    "v_mfma_f32_16x16x32_bf16 v[200:203], a[184:187], a[20:23], v[200:203]\n"
    "ds_read_b128 a[184:187], v243 offset:18432\n"
    "v_mfma_f32_16x16x32_bf16 v[200:203], a[188:191], a[24:27], v[200:203]\n"
    "s_waitcnt lgkmcnt(8)\n"
    "ds_read_b128 a[188:191], v242 offset:22528\n"
    "v_mfma_f32_16x16x32_bf16 v[204:207], a[160:163], a[28:31], 0\n"
    "buffer_load_dwordx4 v248, s[8:11], s41 offen offset:2048 sc1 lds\n"
    "v_mfma_f32_16x16x32_bf16 v[204:207], a[164:167], a[0:3], v[204:207]\n"
    "ds_read2st64_b32 v[196:197], v250 offset0:64 offset1:65\n"
    "v_mfma_f32_16x16x32_bf16 v[212:215], a[164:167], a[32:35], 0\n"
    "v_exp_f32_e32 v234, v234\n"
    "v_mfma_f32_16x16x32_bf16 v[212:215], a[160:163], a[60:63], v[212:215]\n"
    "ds_read2st64_b32 v[198:199], v250 offset0:66 offset1:67\n"
    "v_mfma_f32_16x16x32_bf16 v[220:223], a[160:163], a[92:95], 0\n"
    "v_exp_f32_e32 v235, v235\n"
    "v_mfma_f32_16x16x32_bf16 v[220:223], a[164:167], a[64:67], v[220:223]\n"
    "buffer_load_dwordx4 v248, s[8:11], s41 offen offset:3072 sc1 lds\n"
    "v_mfma_f32_16x16x32_bf16 v[228:231], a[164:167], a[96:99], 0\n"
    "s_add_u32 s41, s41, 0x8000\n"
    "v_cvt_pk_bf16_f32 v232, v232, v233\n"
    "v_mfma_f32_16x16x32_bf16 v[228:231], a[160:163], a[124:127], v[228:231]\n"
    "v_exp_f32_e32 v224, v224\n"
    "v_mfma_f32_16x16x32_bf16 v[236:239], a[160:163], a[156:159], 0\n"
    "ds_read_b64_tr_b16 a[160:161], v244 offset:0\n"
    "v_mfma_f32_16x16x32_bf16 v[236:239], a[164:167], a[128:131], v[236:239]\n"
    "ds_read_b64_tr_b16 a[162:163], v244 offset:2048\n"
    "v_mfma_f32_16x16x32_bf16 v[236:239], a[168:171], a[132:135], v[236:239]\n"
    "ds_read_b64_tr_b16 a[164:165], v244 offset:8192\n"
    "v_mfma_f32_16x16x32_bf16 v[236:239], a[172:175], a[136:139], v[236:239]\n"
    "ds_read_b64_tr_b16 a[166:167], v244 offset:10240\n"
    "v_mfma_f32_16x16x32_bf16 v[228:231], a[172:175], a[104:107], v[228:231]\n"
    "v_exp_f32_e32 v225, v225\n"
    "v_mfma_f32_16x16x32_bf16 v[228:231], a[168:171], a[100:103], v[228:231]\n"
    "v_cvt_pk_bf16_f32 v233, v234, v235\n"
    "v_mfma_f32_16x16x32_bf16 v[220:223], a[168:171], a[68:71], v[220:223]\n"
    "v_exp_f32_e32 v226, v226\n"
    "v_mfma_f32_16x16x32_bf16 v[220:223], a[172:175], a[72:75], v[220:223]\n"
    "v_exp_f32_e32 v227, v227\n"
    "v_mfma_f32_16x16x32_bf16 v[212:215], a[172:175], a[40:43], v[212:215]\n"
    "v_cvt_pk_bf16_f32 v224, v224, v225\n"
    "v_mfma_f32_16x16x32_bf16 v[212:215], a[168:171], a[36:39], v[212:215]\n"
    "v_exp_f32_e32 v216, v216\n"
    "v_mfma_f32_16x16x32_bf16 v[204:207], a[168:171], a[4:7], v[204:207]\n"
    "ds_read_b64_tr_b16 a[168:169], v245 offset:0\n"
    "v_mfma_f32_16x16x32_bf16 v[204:207], a[172:175], a[8:11], v[204:207]\n"
    "s_waitcnt lgkmcnt(7)\n"
    "ds_read_b64_tr_b16 a[170:171], v245 offset:2048\n"
    "v_mfma_f32_16x16x32_bf16 v[204:207], a[176:179], a[12:15], v[204:207]\n"
    "ds_read_b64_tr_b16 a[172:173], v245 offset:8192\n"
    "v_mfma_f32_16x16x32_bf16 v[204:207], a[180:183], a[16:19], v[204:207]\n"
    "ds_read_b64_tr_b16 a[174:175], v245 offset:10240\n"
    "v_mfma_f32_16x16x32_bf16 v[212:215], a[180:183], a[48:51], v[212:215]\n"
    "v_exp_f32_e32 v217, v217\n"
    "v_mfma_f32_16x16x32_bf16 v[212:215], a[176:179], a[44:47], v[212:215]\n"
    "v_cvt_pk_bf16_f32 v225, v226, v227\n"
    "v_mfma_f32_16x16x32_bf16 v[220:223], a[176:179], a[76:79], v[220:223]\n"
    "v_exp_f32_e32 v218, v218\n"
    "v_mfma_f32_16x16x32_bf16 v[220:223], a[180:183], a[80:83], v[220:223]\n"
    "v_exp_f32_e32 v219, v219\n"
    "v_mfma_f32_16x16x32_bf16 v[228:231], a[180:183], a[112:115], v[228:231]\n"
    "v_cvt_pk_bf16_f32 v216, v216, v217\n"
    "v_mfma_f32_16x16x32_bf16 v[228:231], a[176:179], a[108:111], v[228:231]\n"
    "v_exp_f32_e32 v208, v208\n"
    "v_mfma_f32_16x16x32_bf16 v[236:239], a[176:179], a[140:143], v[236:239]\n"
    "ds_read_b64_tr_b16 a[176:177], v246 offset:0\n"
    "v_mfma_f32_16x16x32_bf16 v[236:239], a[180:183], a[144:147], v[236:239]\n"
    "ds_read_b64_tr_b16 a[178:179], v246 offset:2048\n"
    "v_mfma_f32_16x16x32_bf16 v[236:239], a[184:187], a[148:151], v[236:239]\n"
    "ds_read_b64_tr_b16 a[180:181], v246 offset:8192\n"
    "v_mfma_f32_16x16x32_bf16 v[236:239], a[188:191], a[152:155], v[236:239]\n"
    "ds_read_b64_tr_b16 a[182:183], v246 offset:10240\n"
    "v_mfma_f32_16x16x32_bf16 v[228:231], a[188:191], a[120:123], v[228:231]\n"
    "v_exp_f32_e32 v209, v209\n"
    "v_mfma_f32_16x16x32_bf16 v[228:231], a[184:187], a[116:119], v[228:231]\n"
    "v_cvt_pk_bf16_f32 v217, v218, v219\n"
    "v_mfma_f32_16x16x32_bf16 v[220:223], a[184:187], a[84:87], v[220:223]\n"
    "v_exp_f32_e32 v210, v210\n"
    "v_mfma_f32_16x16x32_bf16 v[220:223], a[188:191], a[88:91], v[220:223]\n"
    "v_exp_f32_e32 v211, v211\n"
    "v_mfma_f32_16x16x32_bf16 v[212:215], a[188:191], a[56:59], v[212:215]\n"
    "v_cvt_pk_bf16_f32 v208, v208, v209\n"
    "v_mfma_f32_16x16x32_bf16 v[212:215], a[184:187], a[52:55], v[212:215]\n"
    "v_exp_f32_e32 v200, v200\n"
    "v_mfma_f32_16x16x32_bf16 v[204:207], a[184:187], a[20:23], v[204:207]\n"
    "ds_read_b64_tr_b16 a[184:185], v247 offset:0\n"
    "v_mfma_f32_16x16x32_bf16 v[204:207], a[188:191], a[24:27], v[204:207]\n"
    "s_waitcnt lgkmcnt(5)\n"
    "ds_read_b64_tr_b16 a[186:187], v247 offset:2048\n"
    "v_mfma_f32_16x16x32_bf16 v[0:3], a[160:163], v[160:163], v[0:3]\n"
    "ds_read_b64_tr_b16 a[188:189], v247 offset:8192\n"
    "v_mfma_f32_16x16x32_bf16 v[0:3], a[164:167], v[164:167], v[0:3]\n"
    "ds_read_b64_tr_b16 a[190:191], v247 offset:10240\n"
    "v_mfma_f32_16x16x32_bf16 a[248:251], a[164:167], v[172:175], a[248:251]\n"
    "v_exp_f32_e32 v201, v201\n"
    "v_mfma_f32_16x16x32_bf16 a[248:251], a[160:163], v[168:171], a[248:251]\n"
    "v_cvt_pk_bf16_f32 v209, v210, v211\n"
    "v_mfma_f32_16x16x32_bf16 v[56:59], a[160:163], v[176:179], v[56:59]\n"
    "v_exp_f32_e32 v202, v202\n"
    "v_mfma_f32_16x16x32_bf16 v[56:59], a[164:167], v[180:183], v[56:59]\n"
    "v_exp_f32_e32 v203, v203\n"
    "v_mfma_f32_16x16x32_bf16 v[60:63], a[164:167], v[188:191], v[60:63]\n"
    "v_cvt_pk_bf16_f32 v200, v200, v201\n"
    "v_mfma_f32_16x16x32_bf16 v[60:63], a[160:163], v[184:187], v[60:63]\n"
    "v_cvt_pk_bf16_f32 v201, v202, v203\n"
    "v_mfma_f32_16x16x32_bf16 v[64:67], a[160:163], v[192:195], v[64:67]\n"
    "ds_read_b64_tr_b16 a[160:161], v244 offset:4096\n"
    "v_mfma_f32_16x16x32_bf16 v[64:67], a[164:167], v[196:199], v[64:67]\n"
    "ds_read_b64_tr_b16 a[162:163], v244 offset:6144\n"
    "v_mfma_f32_16x16x32_bf16 v[76:79], a[168:171], v[192:195], v[76:79]\n"
    "ds_read_b64_tr_b16 a[164:165], v244 offset:12288\n"
    "v_mfma_f32_16x16x32_bf16 v[76:79], a[172:175], v[196:199], v[76:79]\n"
    "ds_read_b64_tr_b16 a[166:167], v244 offset:14336\n"
    "v_mfma_f32_16x16x32_bf16 v[72:75], a[172:175], v[188:191], v[72:75]\n"
    "v_exp_f32_e32 v236, v236\n"
    "v_mfma_f32_16x16x32_bf16 v[72:75], a[168:171], v[184:187], v[72:75]\n"
    "v_exp_f32_e32 v237, v237\n"
    "v_mfma_f32_16x16x32_bf16 v[68:71], a[168:171], v[176:179], v[68:71]\n"
    "v_exp_f32_e32 v238, v238\n"
    "v_mfma_f32_16x16x32_bf16 v[68:71], a[172:175], v[180:183], v[68:71]\n"
    "v_exp_f32_e32 v239, v239\n"
    "v_mfma_f32_16x16x32_bf16 a[252:255], a[172:175], v[172:175], a[252:255]\n"
    "v_cvt_pk_bf16_f32 v234, v236, v237\n"
    "v_mfma_f32_16x16x32_bf16 a[252:255], a[168:171], v[168:171], a[252:255]\n"
    "v_exp_f32_e32 v228, v228\n"
    "v_mfma_f32_16x16x32_bf16 v[4:7], a[168:171], v[160:163], v[4:7]\n"
    "ds_read_b64_tr_b16 a[168:169], v245 offset:4096\n"
    "v_mfma_f32_16x16x32_bf16 v[4:7], a[172:175], v[164:167], v[4:7]\n"
    "s_waitcnt lgkmcnt(5)\n"
    "ds_read_b64_tr_b16 a[170:171], v245 offset:6144\n"
    "v_mfma_f32_16x16x32_bf16 v[8:11], a[176:179], v[160:163], v[8:11]\n"
    "ds_read_b64_tr_b16 a[172:173], v245 offset:12288\n"
    "v_mfma_f32_16x16x32_bf16 v[8:11], a[180:183], v[164:167], v[8:11]\n"
    "ds_read_b64_tr_b16 a[174:175], v245 offset:14336\n"
    "v_mfma_f32_16x16x32_bf16 v[32:35], a[180:183], v[172:175], v[32:35]\n"
    "v_exp_f32_e32 v229, v229\n"
    "v_mfma_f32_16x16x32_bf16 v[32:35], a[176:179], v[168:171], v[32:35]\n"
    "v_cvt_pk_bf16_f32 v235, v238, v239\n"
    "v_mfma_f32_16x16x32_bf16 v[80:83], a[176:179], v[176:179], v[80:83]\n"
    "v_exp_f32_e32 v230, v230\n"
    "v_mfma_f32_16x16x32_bf16 v[80:83], a[180:183], v[180:183], v[80:83]\n"
    "v_exp_f32_e32 v231, v231\n"
    "v_mfma_f32_16x16x32_bf16 v[84:87], a[180:183], v[188:191], v[84:87]\n"
    "v_cvt_pk_bf16_f32 v226, v228, v229\n"
    "v_mfma_f32_16x16x32_bf16 v[84:87], a[176:179], v[184:187], v[84:87]\n"
    "v_exp_f32_e32 v220, v220\n"
    "v_mfma_f32_16x16x32_bf16 v[88:91], a[176:179], v[192:195], v[88:91]\n"
    "ds_read_b64_tr_b16 a[176:177], v246 offset:4096\n"
    "v_mfma_f32_16x16x32_bf16 v[88:91], a[180:183], v[196:199], v[88:91]\n"
    "ds_read_b64_tr_b16 a[178:179], v246 offset:6144\n"
    "v_mfma_f32_16x16x32_bf16 v[100:103], a[184:187], v[192:195], v[100:103]\n"
    "ds_read_b64_tr_b16 a[180:181], v246 offset:12288\n"
    "v_mfma_f32_16x16x32_bf16 v[100:103], a[188:191], v[196:199], v[100:103]\n"
    "ds_read_b64_tr_b16 a[182:183], v246 offset:14336\n"
    "v_mfma_f32_16x16x32_bf16 v[96:99], a[188:191], v[188:191], v[96:99]\n"
    "v_exp_f32_e32 v221, v221\n"
    "v_mfma_f32_16x16x32_bf16 v[96:99], a[184:187], v[184:187], v[96:99]\n"
    "v_cvt_pk_bf16_f32 v227, v230, v231\n"
    "v_mfma_f32_16x16x32_bf16 v[92:95], a[184:187], v[176:179], v[92:95]\n"
    "v_exp_f32_e32 v222, v222\n"
    "v_mfma_f32_16x16x32_bf16 v[92:95], a[188:191], v[180:183], v[92:95]\n"
    "v_exp_f32_e32 v223, v223\n"
    "v_mfma_f32_16x16x32_bf16 v[36:39], a[188:191], v[172:175], v[36:39]\n"
    "v_cvt_pk_bf16_f32 v218, v220, v221\n"
    "v_mfma_f32_16x16x32_bf16 v[36:39], a[184:187], v[168:171], v[36:39]\n"
    "v_exp_f32_e32 v212, v212\n"
    "v_mfma_f32_16x16x32_bf16 v[12:15], a[184:187], v[160:163], v[12:15]\n"
    "s_mov_b32 m0, s49\n"
    "ds_read_b64_tr_b16 a[184:185], v247 offset:4096\n"
    "v_mfma_f32_16x16x32_bf16 v[12:15], a[188:191], v[164:167], v[12:15]\n"
    "s_waitcnt lgkmcnt(5)\n"
    "ds_read_b64_tr_b16 a[186:187], v247 offset:6144\n"
    "v_mfma_f32_16x16x32_bf16 v[16:19], a[160:163], v[160:163], v[16:19]\n"
    "ds_read_b64_tr_b16 a[188:189], v247 offset:12288\n"
    "v_mfma_f32_16x16x32_bf16 v[16:19], a[164:167], v[164:167], v[16:19]\n"
    "ds_read_b64_tr_b16 a[190:191], v247 offset:14336\n"
    "v_mfma_f32_16x16x32_bf16 v[40:43], a[164:167], v[172:175], v[40:43]\n"
    "v_exp_f32_e32 v213, v213\n"
    "v_mfma_f32_16x16x32_bf16 v[40:43], a[160:163], v[168:171], v[40:43]\n"
    "v_cvt_pk_bf16_f32 v219, v222, v223\n"
    "v_mfma_f32_16x16x32_bf16 v[104:107], a[160:163], v[176:179], v[104:107]\n"
    "v_exp_f32_e32 v214, v214\n"
    "v_mfma_f32_16x16x32_bf16 v[104:107], a[164:167], v[180:183], v[104:107]\n"
    "s_waitcnt vmcnt(8)\n"
    "v_mfma_f32_16x16x32_bf16 v[108:111], a[164:167], v[188:191], v[108:111]\n"
    "s_barrier\n"
    "v_mfma_f32_16x16x32_bf16 v[108:111], a[160:163], v[184:187], v[108:111]\n"
    "v_exp_f32_e32 v215, v215\n"
    "v_mfma_f32_16x16x32_bf16 v[112:115], a[160:163], v[192:195], v[112:115]\n"
    "ds_read_b128 a[160:163], v243 offset:36864\n"
    "v_mfma_f32_16x16x32_bf16 v[112:115], a[164:167], v[196:199], v[112:115]\n"
    "ds_read_b128 a[164:167], v240 offset:32768\n"
    "v_mfma_f32_16x16x32_bf16 v[124:127], a[168:171], v[192:195], v[124:127]\n"
    "ds_write_addtid_b32 v232 offset:16384\n"
    "v_cvt_pk_bf16_f32 v210, v212, v213\n"
    "v_mfma_f32_16x16x32_bf16 v[124:127], a[172:175], v[196:199], v[124:127]\n"
    "v_exp_f32_e32 v204, v204\n"
    "v_mfma_f32_16x16x32_bf16 v[120:123], a[172:175], v[188:191], v[120:123]\n"
    "v_exp_f32_e32 v205, v205\n"
    "v_mfma_f32_16x16x32_bf16 v[120:123], a[168:171], v[184:187], v[120:123]\n"
    "ds_write_addtid_b32 v233 offset:16640\n"
    "v_cvt_pk_bf16_f32 v211, v214, v215\n"
    "v_mfma_f32_16x16x32_bf16 v[116:119], a[168:171], v[176:179], v[116:119]\n"
    "v_exp_f32_e32 v206, v206\n"
    "v_mfma_f32_16x16x32_bf16 v[116:119], a[172:175], v[180:183], v[116:119]\n"
    "v_exp_f32_e32 v207, v207\n"
    "v_mfma_f32_16x16x32_bf16 v[44:47], a[172:175], v[172:175], v[44:47]\n"
    "ds_write_addtid_b32 v224 offset:12288\n"
    "v_cvt_pk_bf16_f32 v202, v204, v205\n"
    "v_mfma_f32_16x16x32_bf16 v[44:47], a[168:171], v[168:171], v[44:47]\n"
    "ds_write_addtid_b32 v225 offset:12544\n"
    "v_cvt_pk_bf16_f32 v203, v206, v207\n"
    "v_mfma_f32_16x16x32_bf16 v[20:23], a[168:171], v[160:163], v[20:23]\n"
    "ds_read_b128 a[168:171], v241 offset:32768\n"
    "v_mfma_f32_16x16x32_bf16 v[20:23], a[172:175], v[164:167], v[20:23]\n"
    "s_waitcnt lgkmcnt(7)\n"
    "ds_read_b128 a[172:175], v240 offset:36864\n"
    "v_mfma_f32_16x16x32_bf16 v[24:27], a[176:179], v[160:163], v[24:27]\n"
    "ds_write_addtid_b32 v216 offset:8192\n"
    "v_mfma_f32_16x16x32_bf16 v[24:27], a[180:183], v[164:167], v[24:27]\n"
    "ds_write_addtid_b32 v217 offset:8448\n"
    "v_mfma_f32_16x16x32_bf16 v[48:51], a[180:183], v[172:175], v[48:51]\n"
    "ds_write_addtid_b32 v208 offset:4096\n"
    "v_mfma_f32_16x16x32_bf16 v[48:51], a[176:179], v[168:171], v[48:51]\n"
    "ds_write_addtid_b32 v209 offset:4352\n"
    "v_mfma_f32_16x16x32_bf16 v[128:131], a[176:179], v[176:179], v[128:131]\n"
    "ds_write_addtid_b32 v200 offset:0\n"
    "v_mfma_f32_16x16x32_bf16 v[128:131], a[180:183], v[180:183], v[128:131]\n"
    "ds_write_addtid_b32 v201 offset:256\n"
    "v_mfma_f32_16x16x32_bf16 v[132:135], a[180:183], v[188:191], v[132:135]\n"
    "ds_write_addtid_b32 v234 offset:16896\n"
    "v_mfma_f32_16x16x32_bf16 v[132:135], a[176:179], v[184:187], v[132:135]\n"
    "ds_write_addtid_b32 v235 offset:17152\n"
    "v_mfma_f32_16x16x32_bf16 v[136:139], a[176:179], v[192:195], v[136:139]\n"
    "ds_read_b128 a[176:179], v241 offset:36864\n"
    "v_mfma_f32_16x16x32_bf16 v[136:139], a[180:183], v[196:199], v[136:139]\n"
    "ds_read_b128 a[180:183], v242 offset:32768\n"
    "v_mfma_f32_16x16x32_bf16 v[148:151], a[184:187], v[192:195], v[148:151]\n"
    "ds_write_addtid_b32 v226 offset:12800\n"
    "v_mfma_f32_16x16x32_bf16 v[148:151], a[188:191], v[196:199], v[148:151]\n"
    "ds_write_addtid_b32 v227 offset:13056\n"
    "v_mfma_f32_16x16x32_bf16 v[144:147], a[188:191], v[188:191], v[144:147]\n"
    "ds_write_addtid_b32 v218 offset:8704\n"
    "v_mfma_f32_16x16x32_bf16 v[144:147], a[184:187], v[184:187], v[144:147]\n"
    "ds_write_addtid_b32 v219 offset:8960\n"
    "v_mfma_f32_16x16x32_bf16 v[140:143], a[184:187], v[176:179], v[140:143]\n"
    "ds_write_addtid_b32 v210 offset:4608\n"
    "v_mfma_f32_16x16x32_bf16 v[140:143], a[188:191], v[180:183], v[140:143]\n"
    "ds_write_addtid_b32 v211 offset:4864\n"
    "v_mfma_f32_16x16x32_bf16 v[52:55], a[188:191], v[172:175], v[52:55]\n"
    "ds_write_addtid_b32 v202 offset:512\n"
    "v_mfma_f32_16x16x32_bf16 v[52:55], a[184:187], v[168:171], v[52:55]\n"
    "ds_write_addtid_b32 v203 offset:768\n"
    "v_mfma_f32_16x16x32_bf16 v[28:31], a[184:187], v[160:163], v[28:31]\n"
    "ds_read_b128 a[184:187], v243 offset:32768\n"
    "v_mfma_f32_16x16x32_bf16 v[28:31], a[188:191], v[164:167], v[28:31]\n"
    "s_waitcnt lgkmcnt(15)\n"
    "ds_read_b128 a[188:191], v242 offset:36864\n"
    "v_mfma_f32_16x16x32_bf16 v[160:163], a[160:163], a[28:31], 0\n"
    "v_mfma_f32_16x16x32_bf16 v[160:163], a[164:167], a[0:3], v[160:163]\n"
    "v_mfma_f32_16x16x32_bf16 v[168:171], a[164:167], a[32:35], 0\n"
    "v_mfma_f32_16x16x32_bf16 v[168:171], a[160:163], a[60:63], v[168:171]\n"
    "v_mfma_f32_16x16x32_bf16 v[176:179], a[160:163], a[92:95], 0\n"
    "v_mfma_f32_16x16x32_bf16 v[176:179], a[164:167], a[64:67], v[176:179]\n"
    "s_waitcnt lgkmcnt(0)\n"
    "v_mfma_f32_16x16x32_bf16 v[184:187], a[164:167], a[96:99], 0\n"
    "s_barrier\n"
    "v_mfma_f32_16x16x32_bf16 v[184:187], a[160:163], a[124:127], v[184:187]\n"
    "s_add_u32 m0, s43, 0\n"
    "ds_read2st64_b32 v[204:205], v250 offset0:0 offset1:1\n"
    "v_mfma_f32_16x16x32_bf16 v[192:195], a[160:163], a[156:159], 0\n"
    "ds_read_b128 a[160:163], v243 offset:38912\n"
    "v_mfma_f32_16x16x32_bf16 v[192:195], a[164:167], a[128:131], v[192:195]\n"
    "ds_read_b128 a[164:167], v240 offset:34816\n"
    "v_mfma_f32_16x16x32_bf16 v[192:195], a[168:171], a[132:135], v[192:195]\n"
    "buffer_load_dwordx4 v248, s[8:11], s40 offen offset:0 sc1 lds\n"
    "v_mfma_f32_16x16x32_bf16 v[192:195], a[172:175], a[136:139], v[192:195]\n"
    "v_mfma_f32_16x16x32_bf16 v[184:187], a[172:175], a[104:107], v[184:187]\n"
    "v_mfma_f32_16x16x32_bf16 v[184:187], a[168:171], a[100:103], v[184:187]\n"
    "ds_read2st64_b32 v[206:207], v250 offset0:2 offset1:3\n"
    "v_mfma_f32_16x16x32_bf16 v[176:179], a[168:171], a[68:71], v[176:179]\n"
    "v_mfma_f32_16x16x32_bf16 v[176:179], a[172:175], a[72:75], v[176:179]\n"
    "buffer_load_dwordx4 v248, s[8:11], s40 offen offset:1024 sc1 lds\n"
    "v_mfma_f32_16x16x32_bf16 v[168:171], a[172:175], a[40:43], v[168:171]\n"
    "v_mfma_f32_16x16x32_bf16 v[168:171], a[168:171], a[36:39], v[168:171]\n"
    "ds_read2st64_b32 v[212:213], v250 offset0:16 offset1:17\n"
    "v_mfma_f32_16x16x32_bf16 v[160:163], a[168:171], a[4:7], v[160:163]\n"
    "ds_read_b128 a[168:171], v241 offset:34816\n"
    "v_mfma_f32_16x16x32_bf16 v[160:163], a[172:175], a[8:11], v[160:163]\n"
    "ds_read_b128 a[172:175], v240 offset:38912\n"
    "v_mfma_f32_16x16x32_bf16 v[160:163], a[176:179], a[12:15], v[160:163]\n"
    "buffer_load_dwordx4 v248, s[8:11], s40 offen offset:2048 sc1 lds\n"
    "v_mfma_f32_16x16x32_bf16 v[160:163], a[180:183], a[16:19], v[160:163]\n"
    "ds_read2st64_b32 v[214:215], v250 offset0:18 offset1:19\n"
    "v_mfma_f32_16x16x32_bf16 v[168:171], a[180:183], a[48:51], v[168:171]\n"
    "v_mfma_f32_16x16x32_bf16 v[168:171], a[176:179], a[44:47], v[168:171]\n"
    "v_mfma_f32_16x16x32_bf16 v[176:179], a[176:179], a[76:79], v[176:179]\n"
    "ds_read2st64_b32 v[220:221], v250 offset0:32 offset1:33\n"
    "v_mfma_f32_16x16x32_bf16 v[176:179], a[180:183], a[80:83], v[176:179]\n"
    "buffer_load_dwordx4 v248, s[8:11], s40 offen offset:3072 sc1 lds\n"
    "v_mfma_f32_16x16x32_bf16 v[184:187], a[180:183], a[112:115], v[184:187]\n"
    "s_add_u32 m0, s43, 4096\n"
    "v_mfma_f32_16x16x32_bf16 v[184:187], a[176:179], a[108:111], v[184:187]\n"
    "s_add_u32 s40, s40, 0x8000\n"
    "ds_read2st64_b32 v[222:223], v250 offset0:34 offset1:35\n"
    "v_mfma_f32_16x16x32_bf16 v[192:195], a[176:179], a[140:143], v[192:195]\n"
    "ds_read_b128 a[176:179], v241 offset:38912\n"
    "v_mfma_f32_16x16x32_bf16 v[192:195], a[180:183], a[144:147], v[192:195]\n"
    "ds_read_b128 a[180:183], v242 offset:34816\n"
    "v_mfma_f32_16x16x32_bf16 v[192:195], a[184:187], a[148:151], v[192:195]\n"
    "buffer_load_dwordx4 v248, s[8:11], s41 offen offset:0 sc1 lds\n"
    "v_mfma_f32_16x16x32_bf16 v[192:195], a[188:191], a[152:155], v[192:195]\n"
    "v_mfma_f32_16x16x32_bf16 v[184:187], a[188:191], a[120:123], v[184:187]\n"
    "ds_read2st64_b32 v[228:229], v250 offset0:48 offset1:49\n"
    "v_mfma_f32_16x16x32_bf16 v[184:187], a[184:187], a[116:119], v[184:187]\n"
    "v_mfma_f32_16x16x32_bf16 v[176:179], a[184:187], a[84:87], v[176:179]\n"
    "v_exp_f32_e32 v192, v192\n"
    "v_mfma_f32_16x16x32_bf16 v[176:179], a[188:191], a[88:91], v[176:179]\n"
    "buffer_load_dwordx4 v248, s[8:11], s41 offen offset:1024 sc1 lds\n"
    "v_mfma_f32_16x16x32_bf16 v[168:171], a[188:191], a[56:59], v[168:171]\n"
    "ds_read2st64_b32 v[230:231], v250 offset0:50 offset1:51\n"
    "v_mfma_f32_16x16x32_bf16 v[168:171], a[184:187], a[52:55], v[168:171]\n"
    "v_exp_f32_e32 v193, v193\n"
    "v_mfma_f32_16x16x32_bf16 v[160:163], a[184:187], a[20:23], v[160:163]\n"
    "ds_read_b128 a[184:187], v243 offset:34816\n"
    "v_mfma_f32_16x16x32_bf16 v[160:163], a[188:191], a[24:27], v[160:163]\n"
    "s_waitcnt lgkmcnt(8)\n"
    "ds_read_b128 a[188:191], v242 offset:38912\n"
    "v_mfma_f32_16x16x32_bf16 v[164:167], a[160:163], a[28:31], 0\n"
    "buffer_load_dwordx4 v248, s[8:11], s41 offen offset:2048 sc1 lds\n"
    "v_mfma_f32_16x16x32_bf16 v[164:167], a[164:167], a[0:3], v[164:167]\n"
    "ds_read2st64_b32 v[236:237], v250 offset0:64 offset1:65\n"
    "v_mfma_f32_16x16x32_bf16 v[172:175], a[164:167], a[32:35], 0\n"
    "v_exp_f32_e32 v194, v194\n"
    "v_mfma_f32_16x16x32_bf16 v[172:175], a[160:163], a[60:63], v[172:175]\n"
    "ds_read2st64_b32 v[238:239], v250 offset0:66 offset1:67\n"
    "v_mfma_f32_16x16x32_bf16 v[180:183], a[160:163], a[92:95], 0\n"
    "v_exp_f32_e32 v195, v195\n"
    "v_mfma_f32_16x16x32_bf16 v[180:183], a[164:167], a[64:67], v[180:183]\n"
    "buffer_load_dwordx4 v248, s[8:11], s41 offen offset:3072 sc1 lds\n"
    "v_mfma_f32_16x16x32_bf16 v[188:191], a[164:167], a[96:99], 0\n"
    "s_add_u32 s41, s41, 0x8000\n"
    "v_cvt_pk_bf16_f32 v192, v192, v193\n"
    "v_mfma_f32_16x16x32_bf16 v[188:191], a[160:163], a[124:127], v[188:191]\n"
    "v_exp_f32_e32 v184, v184\n"
    "v_mfma_f32_16x16x32_bf16 v[196:199], a[160:163], a[156:159], 0\n"
    "ds_read_b64_tr_b16 a[160:161], v244 offset:16384\n"
    "v_mfma_f32_16x16x32_bf16 v[196:199], a[164:167], a[128:131], v[196:199]\n"
    "ds_read_b64_tr_b16 a[162:163], v244 offset:18432\n"
    "v_mfma_f32_16x16x32_bf16 v[196:199], a[168:171], a[132:135], v[196:199]\n"
    "ds_read_b64_tr_b16 a[164:165], v244 offset:24576\n"
    "v_mfma_f32_16x16x32_bf16 v[196:199], a[172:175], a[136:139], v[196:199]\n"
    "ds_read_b64_tr_b16 a[166:167], v244 offset:26624\n"
    "v_mfma_f32_16x16x32_bf16 v[188:191], a[172:175], a[104:107], v[188:191]\n"
    "v_exp_f32_e32 v185, v185\n"
    "v_mfma_f32_16x16x32_bf16 v[188:191], a[168:171], a[100:103], v[188:191]\n"
    "v_cvt_pk_bf16_f32 v193, v194, v195\n"
    "v_mfma_f32_16x16x32_bf16 v[180:183], a[168:171], a[68:71], v[180:183]\n"
    "v_exp_f32_e32 v186, v186\n"
    "v_mfma_f32_16x16x32_bf16 v[180:183], a[172:175], a[72:75], v[180:183]\n"
    "v_exp_f32_e32 v187, v187\n"
    "v_mfma_f32_16x16x32_bf16 v[172:175], a[172:175], a[40:43], v[172:175]\n"
    "v_cvt_pk_bf16_f32 v184, v184, v185\n"
    "v_mfma_f32_16x16x32_bf16 v[172:175], a[168:171], a[36:39], v[172:175]\n"
    "v_exp_f32_e32 v176, v176\n"
    "v_mfma_f32_16x16x32_bf16 v[164:167], a[168:171], a[4:7], v[164:167]\n"
    "ds_read_b64_tr_b16 a[168:169], v245 offset:16384\n"
    "v_mfma_f32_16x16x32_bf16 v[164:167], a[172:175], a[8:11], v[164:167]\n"
    "s_waitcnt lgkmcnt(7)\n"
    "ds_read_b64_tr_b16 a[170:171], v245 offset:18432\n"
    "v_mfma_f32_16x16x32_bf16 v[164:167], a[176:179], a[12:15], v[164:167]\n"
    "ds_read_b64_tr_b16 a[172:173], v245 offset:24576\n"
    "v_mfma_f32_16x16x32_bf16 v[164:167], a[180:183], a[16:19], v[164:167]\n"
    "ds_read_b64_tr_b16 a[174:175], v245 offset:26624\n"
    "v_mfma_f32_16x16x32_bf16 v[172:175], a[180:183], a[48:51], v[172:175]\n"
    "v_exp_f32_e32 v177, v177\n"
    "v_mfma_f32_16x16x32_bf16 v[172:175], a[176:179], a[44:47], v[172:175]\n"
    "v_cvt_pk_bf16_f32 v185, v186, v187\n"
    "v_mfma_f32_16x16x32_bf16 v[180:183], a[176:179], a[76:79], v[180:183]\n"
    "v_exp_f32_e32 v178, v178\n"
    "v_mfma_f32_16x16x32_bf16 v[180:183], a[180:183], a[80:83], v[180:183]\n"
    "v_exp_f32_e32 v179, v179\n"
    "v_mfma_f32_16x16x32_bf16 v[188:191], a[180:183], a[112:115], v[188:191]\n"
    "v_cvt_pk_bf16_f32 v176, v176, v177\n"
    "v_mfma_f32_16x16x32_bf16 v[188:191], a[176:179], a[108:111], v[188:191]\n"
    "v_exp_f32_e32 v168, v168\n"
    "v_mfma_f32_16x16x32_bf16 v[196:199], a[176:179], a[140:143], v[196:199]\n"
    "ds_read_b64_tr_b16 a[176:177], v246 offset:16384\n"
    "v_mfma_f32_16x16x32_bf16 v[196:199], a[180:183], a[144:147], v[196:199]\n"
    "ds_read_b64_tr_b16 a[178:179], v246 offset:18432\n"
    "v_mfma_f32_16x16x32_bf16 v[196:199], a[184:187], a[148:151], v[196:199]\n"
    "ds_read_b64_tr_b16 a[180:181], v246 offset:24576\n"
    "v_mfma_f32_16x16x32_bf16 v[196:199], a[188:191], a[152:155], v[196:199]\n"
    "ds_read_b64_tr_b16 a[182:183], v246 offset:26624\n"
    "v_mfma_f32_16x16x32_bf16 v[188:191], a[188:191], a[120:123], v[188:191]\n"
    "v_exp_f32_e32 v169, v169\n"
    "v_mfma_f32_16x16x32_bf16 v[188:191], a[184:187], a[116:119], v[188:191]\n"
    "v_cvt_pk_bf16_f32 v177, v178, v179\n"
    "v_mfma_f32_16x16x32_bf16 v[180:183], a[184:187], a[84:87], v[180:183]\n"
    "v_exp_f32_e32 v170, v170\n"
    "v_mfma_f32_16x16x32_bf16 v[180:183], a[188:191], a[88:91], v[180:183]\n"
    "v_exp_f32_e32 v171, v171\n"
    "v_mfma_f32_16x16x32_bf16 v[172:175], a[188:191], a[56:59], v[172:175]\n"
    "v_cvt_pk_bf16_f32 v168, v168, v169\n"
    "v_mfma_f32_16x16x32_bf16 v[172:175], a[184:187], a[52:55], v[172:175]\n"
    "v_exp_f32_e32 v160, v160\n"
    "v_mfma_f32_16x16x32_bf16 v[164:167], a[184:187], a[20:23], v[164:167]\n"
    "ds_read_b64_tr_b16 a[184:185], v247 offset:16384\n"
    "v_mfma_f32_16x16x32_bf16 v[164:167], a[188:191], a[24:27], v[164:167]\n"
    "s_waitcnt lgkmcnt(5)\n"
    "ds_read_b64_tr_b16 a[186:187], v247 offset:18432\n"
    "v_mfma_f32_16x16x32_bf16 v[0:3], a[160:163], v[200:203], v[0:3]\n"
    "ds_read_b64_tr_b16 a[188:189], v247 offset:24576\n"
    "v_mfma_f32_16x16x32_bf16 v[0:3], a[164:167], v[204:207], v[0:3]\n"
    "ds_read_b64_tr_b16 a[190:191], v247 offset:26624\n"
    "v_mfma_f32_16x16x32_bf16 a[248:251], a[164:167], v[212:215], a[248:251]\n"
    "v_exp_f32_e32 v161, v161\n"
    "v_mfma_f32_16x16x32_bf16 a[248:251], a[160:163], v[208:211], a[248:251]\n"
    "v_cvt_pk_bf16_f32 v169, v170, v171\n"
    "v_mfma_f32_16x16x32_bf16 v[56:59], a[160:163], v[216:219], v[56:59]\n"
    "v_exp_f32_e32 v162, v162\n"
    "v_mfma_f32_16x16x32_bf16 v[56:59], a[164:167], v[220:223], v[56:59]\n"
    "v_exp_f32_e32 v163, v163\n"
    "v_mfma_f32_16x16x32_bf16 v[60:63], a[164:167], v[228:231], v[60:63]\n"
    "v_cvt_pk_bf16_f32 v160, v160, v161\n"
    "v_mfma_f32_16x16x32_bf16 v[60:63], a[160:163], v[224:227], v[60:63]\n"
    "v_cvt_pk_bf16_f32 v161, v162, v163\n"
    "v_mfma_f32_16x16x32_bf16 v[64:67], a[160:163], v[232:235], v[64:67]\n"
    "ds_read_b64_tr_b16 a[160:161], v244 offset:20480\n"
    "v_mfma_f32_16x16x32_bf16 v[64:67], a[164:167], v[236:239], v[64:67]\n"
    "ds_read_b64_tr_b16 a[162:163], v244 offset:22528\n"
    "v_mfma_f32_16x16x32_bf16 v[76:79], a[168:171], v[232:235], v[76:79]\n"
    "ds_read_b64_tr_b16 a[164:165], v244 offset:28672\n"
    "v_mfma_f32_16x16x32_bf16 v[76:79], a[172:175], v[236:239], v[76:79]\n"
    "ds_read_b64_tr_b16 a[166:167], v244 offset:30720\n"
    "v_mfma_f32_16x16x32_bf16 v[72:75], a[172:175], v[228:231], v[72:75]\n"
    "v_exp_f32_e32 v196, v196\n"
    "v_mfma_f32_16x16x32_bf16 v[72:75], a[168:171], v[224:227], v[72:75]\n"
    "v_exp_f32_e32 v197, v197\n"
    "v_mfma_f32_16x16x32_bf16 v[68:71], a[168:171], v[216:219], v[68:71]\n"
    "v_exp_f32_e32 v198, v198\n"
    "v_mfma_f32_16x16x32_bf16 v[68:71], a[172:175], v[220:223], v[68:71]\n"
    "v_exp_f32_e32 v199, v199\n"
    "v_mfma_f32_16x16x32_bf16 a[252:255], a[172:175], v[212:215], a[252:255]\n"
    "v_cvt_pk_bf16_f32 v194, v196, v197\n"
    "v_mfma_f32_16x16x32_bf16 a[252:255], a[168:171], v[208:211], a[252:255]\n"
    "v_exp_f32_e32 v188, v188\n"
    "v_mfma_f32_16x16x32_bf16 v[4:7], a[168:171], v[200:203], v[4:7]\n"
    "ds_read_b64_tr_b16 a[168:169], v245 offset:20480\n"
    "v_mfma_f32_16x16x32_bf16 v[4:7], a[172:175], v[204:207], v[4:7]\n"
    "s_waitcnt lgkmcnt(5)\n"
    "ds_read_b64_tr_b16 a[170:171], v245 offset:22528\n"
    "v_mfma_f32_16x16x32_bf16 v[8:11], a[176:179], v[200:203], v[8:11]\n"
    "ds_read_b64_tr_b16 a[172:173], v245 offset:28672\n"
    "v_mfma_f32_16x16x32_bf16 v[8:11], a[180:183], v[204:207], v[8:11]\n"
    "ds_read_b64_tr_b16 a[174:175], v245 offset:30720\n"
    "v_mfma_f32_16x16x32_bf16 v[32:35], a[180:183], v[212:215], v[32:35]\n"
    "v_exp_f32_e32 v189, v189\n"
    "v_mfma_f32_16x16x32_bf16 v[32:35], a[176:179], v[208:211], v[32:35]\n"
    "v_cvt_pk_bf16_f32 v195, v198, v199\n"
    "v_mfma_f32_16x16x32_bf16 v[80:83], a[176:179], v[216:219], v[80:83]\n"
    "v_exp_f32_e32 v190, v190\n"
    "v_mfma_f32_16x16x32_bf16 v[80:83], a[180:183], v[220:223], v[80:83]\n"
    "v_exp_f32_e32 v191, v191\n"
    "v_mfma_f32_16x16x32_bf16 v[84:87], a[180:183], v[228:231], v[84:87]\n"
    "v_cvt_pk_bf16_f32 v186, v188, v189\n"
    "v_mfma_f32_16x16x32_bf16 v[84:87], a[176:179], v[224:227], v[84:87]\n"
    "v_exp_f32_e32 v180, v180\n"
    "v_mfma_f32_16x16x32_bf16 v[88:91], a[176:179], v[232:235], v[88:91]\n"
    "ds_read_b64_tr_b16 a[176:177], v246 offset:20480\n"
    "v_mfma_f32_16x16x32_bf16 v[88:91], a[180:183], v[236:239], v[88:91]\n"
    "ds_read_b64_tr_b16 a[178:179], v246 offset:22528\n"
    "v_mfma_f32_16x16x32_bf16 v[100:103], a[184:187], v[232:235], v[100:103]\n"
    "ds_read_b64_tr_b16 a[180:181], v246 offset:28672\n"
    "v_mfma_f32_16x16x32_bf16 v[100:103], a[188:191], v[236:239], v[100:103]\n"
    "ds_read_b64_tr_b16 a[182:183], v246 offset:30720\n"
    "v_mfma_f32_16x16x32_bf16 v[96:99], a[188:191], v[228:231], v[96:99]\n"
    "v_exp_f32_e32 v181, v181\n"
    "v_mfma_f32_16x16x32_bf16 v[96:99], a[184:187], v[224:227], v[96:99]\n"
    "v_cvt_pk_bf16_f32 v187, v190, v191\n"
    "v_mfma_f32_16x16x32_bf16 v[92:95], a[184:187], v[216:219], v[92:95]\n"
    "v_exp_f32_e32 v182, v182\n"
    "v_mfma_f32_16x16x32_bf16 v[92:95], a[188:191], v[220:223], v[92:95]\n"
    "v_exp_f32_e32 v183, v183\n"
    "v_mfma_f32_16x16x32_bf16 v[36:39], a[188:191], v[212:215], v[36:39]\n"
    "v_cvt_pk_bf16_f32 v178, v180, v181\n"
    "v_mfma_f32_16x16x32_bf16 v[36:39], a[184:187], v[208:211], v[36:39]\n"
    "v_exp_f32_e32 v172, v172\n"
    "v_mfma_f32_16x16x32_bf16 v[12:15], a[184:187], v[200:203], v[12:15]\n"
    "s_mov_b32 m0, s49\n"
    "ds_read_b64_tr_b16 a[184:185], v247 offset:20480\n"
    "v_mfma_f32_16x16x32_bf16 v[12:15], a[188:191], v[204:207], v[12:15]\n"
    "s_waitcnt lgkmcnt(5)\n"
    "ds_read_b64_tr_b16 a[186:187], v247 offset:22528\n"
    "v_mfma_f32_16x16x32_bf16 v[16:19], a[160:163], v[200:203], v[16:19]\n"
    "ds_read_b64_tr_b16 a[188:189], v247 offset:28672\n"
    "v_mfma_f32_16x16x32_bf16 v[16:19], a[164:167], v[204:207], v[16:19]\n"
    "ds_read_b64_tr_b16 a[190:191], v247 offset:30720\n"
    "v_mfma_f32_16x16x32_bf16 v[40:43], a[164:167], v[212:215], v[40:43]\n"
    "v_exp_f32_e32 v173, v173\n"
    "v_mfma_f32_16x16x32_bf16 v[40:43], a[160:163], v[208:211], v[40:43]\n"
    "v_cvt_pk_bf16_f32 v179, v182, v183\n"
    "v_mfma_f32_16x16x32_bf16 v[104:107], a[160:163], v[216:219], v[104:107]\n"
    "v_exp_f32_e32 v174, v174\n"
    "v_mfma_f32_16x16x32_bf16 v[104:107], a[164:167], v[220:223], v[104:107]\n"
    "s_waitcnt vmcnt(8)\n"
    "v_mfma_f32_16x16x32_bf16 v[108:111], a[164:167], v[228:231], v[108:111]\n"
    "s_barrier\n"
    "v_mfma_f32_16x16x32_bf16 v[108:111], a[160:163], v[224:227], v[108:111]\n"
    "v_exp_f32_e32 v175, v175\n"
    "v_mfma_f32_16x16x32_bf16 v[112:115], a[160:163], v[232:235], v[112:115]\n"
    "ds_read_b128 a[160:163], v243 offset:53248\n"
    "v_mfma_f32_16x16x32_bf16 v[112:115], a[164:167], v[236:239], v[112:115]\n"
    "ds_read_b128 a[164:167], v240 offset:49152\n"
    "v_mfma_f32_16x16x32_bf16 v[124:127], a[168:171], v[232:235], v[124:127]\n"
    "ds_write_addtid_b32 v192 offset:16384\n"
    "v_cvt_pk_bf16_f32 v170, v172, v173\n"
    "v_mfma_f32_16x16x32_bf16 v[124:127], a[172:175], v[236:239], v[124:127]\n"
    "v_exp_f32_e32 v164, v164\n"
    "v_mfma_f32_16x16x32_bf16 v[120:123], a[172:175], v[228:231], v[120:123]\n"
    "v_exp_f32_e32 v165, v165\n"
    "v_mfma_f32_16x16x32_bf16 v[120:123], a[168:171], v[224:227], v[120:123]\n"
    "ds_write_addtid_b32 v193 offset:16640\n"
    "v_cvt_pk_bf16_f32 v171, v174, v175\n"
    "v_mfma_f32_16x16x32_bf16 v[116:119], a[168:171], v[216:219], v[116:119]\n"
    "v_exp_f32_e32 v166, v166\n"
    "v_mfma_f32_16x16x32_bf16 v[116:119], a[172:175], v[220:223], v[116:119]\n"
    "v_exp_f32_e32 v167, v167\n"
    "v_mfma_f32_16x16x32_bf16 v[44:47], a[172:175], v[212:215], v[44:47]\n"
    "ds_write_addtid_b32 v184 offset:12288\n"
    "v_cvt_pk_bf16_f32 v162, v164, v165\n"
    "v_mfma_f32_16x16x32_bf16 v[44:47], a[168:171], v[208:211], v[44:47]\n"
    "ds_write_addtid_b32 v185 offset:12544\n"
    "v_cvt_pk_bf16_f32 v163, v166, v167\n"
    "v_mfma_f32_16x16x32_bf16 v[20:23], a[168:171], v[200:203], v[20:23]\n"
    "ds_read_b128 a[168:171], v241 offset:49152\n"
    "v_mfma_f32_16x16x32_bf16 v[20:23], a[172:175], v[204:207], v[20:23]\n"
    "s_waitcnt lgkmcnt(7)\n"
    "ds_read_b128 a[172:175], v240 offset:53248\n"
    "v_mfma_f32_16x16x32_bf16 v[24:27], a[176:179], v[200:203], v[24:27]\n"
    "ds_write_addtid_b32 v176 offset:8192\n"
    "v_mfma_f32_16x16x32_bf16 v[24:27], a[180:183], v[204:207], v[24:27]\n"
    "ds_write_addtid_b32 v177 offset:8448\n"
    "v_mfma_f32_16x16x32_bf16 v[48:51], a[180:183], v[212:215], v[48:51]\n"
    "ds_write_addtid_b32 v168 offset:4096\n"
    "v_mfma_f32_16x16x32_bf16 v[48:51], a[176:179], v[208:211], v[48:51]\n"
    "ds_write_addtid_b32 v169 offset:4352\n"
    "v_mfma_f32_16x16x32_bf16 v[128:131], a[176:179], v[216:219], v[128:131]\n"
    "ds_write_addtid_b32 v160 offset:0\n"
    "v_mfma_f32_16x16x32_bf16 v[128:131], a[180:183], v[220:223], v[128:131]\n"
    "ds_write_addtid_b32 v161 offset:256\n"
    "v_mfma_f32_16x16x32_bf16 v[132:135], a[180:183], v[228:231], v[132:135]\n"
    "ds_write_addtid_b32 v194 offset:16896\n"
    "v_mfma_f32_16x16x32_bf16 v[132:135], a[176:179], v[224:227], v[132:135]\n"
    "ds_write_addtid_b32 v195 offset:17152\n"
    "v_mfma_f32_16x16x32_bf16 v[136:139], a[176:179], v[232:235], v[136:139]\n"
    "ds_read_b128 a[176:179], v241 offset:53248\n"
    "v_mfma_f32_16x16x32_bf16 v[136:139], a[180:183], v[236:239], v[136:139]\n"
    "ds_read_b128 a[180:183], v242 offset:49152\n"
    "v_mfma_f32_16x16x32_bf16 v[148:151], a[184:187], v[232:235], v[148:151]\n"
    "ds_write_addtid_b32 v186 offset:12800\n"
    "v_mfma_f32_16x16x32_bf16 v[148:151], a[188:191], v[236:239], v[148:151]\n"
    "ds_write_addtid_b32 v187 offset:13056\n"
    "v_mfma_f32_16x16x32_bf16 v[144:147], a[188:191], v[228:231], v[144:147]\n"
    "ds_write_addtid_b32 v178 offset:8704\n"
    "v_mfma_f32_16x16x32_bf16 v[144:147], a[184:187], v[224:227], v[144:147]\n"
    "ds_write_addtid_b32 v179 offset:8960\n"
    "v_mfma_f32_16x16x32_bf16 v[140:143], a[184:187], v[216:219], v[140:143]\n"
    "ds_write_addtid_b32 v170 offset:4608\n"
    "v_mfma_f32_16x16x32_bf16 v[140:143], a[188:191], v[220:223], v[140:143]\n"
    "ds_write_addtid_b32 v171 offset:4864\n"
    "v_mfma_f32_16x16x32_bf16 v[52:55], a[188:191], v[212:215], v[52:55]\n"
    "ds_write_addtid_b32 v162 offset:512\n"
    "v_mfma_f32_16x16x32_bf16 v[52:55], a[184:187], v[208:211], v[52:55]\n"
    "ds_write_addtid_b32 v163 offset:768\n"
    "v_mfma_f32_16x16x32_bf16 v[28:31], a[184:187], v[200:203], v[28:31]\n"
    "ds_read_b128 a[184:187], v243 offset:49152\n"
    "v_mfma_f32_16x16x32_bf16 v[28:31], a[188:191], v[204:207], v[28:31]\n"
    "s_waitcnt lgkmcnt(15)\n"
    "ds_read_b128 a[188:191], v242 offset:53248\n"
    ".Lat_p2e:\n"
    "v_mfma_f32_16x16x32_bf16 v[200:203], a[160:163], a[28:31], 0\n"
    "v_mfma_f32_16x16x32_bf16 v[200:203], a[164:167], a[0:3], v[200:203]\n"
    "v_mfma_f32_16x16x32_bf16 v[208:211], a[164:167], a[32:35], 0\n"
    "v_mfma_f32_16x16x32_bf16 v[208:211], a[160:163], a[60:63], v[208:211]\n"
    "v_mfma_f32_16x16x32_bf16 v[216:219], a[160:163], a[92:95], 0\n"
    "v_mfma_f32_16x16x32_bf16 v[216:219], a[164:167], a[64:67], v[216:219]\n"
    "s_waitcnt lgkmcnt(0)\n"
    "v_mfma_f32_16x16x32_bf16 v[224:227], a[164:167], a[96:99], 0\n"
    "s_barrier\n"
    "v_mfma_f32_16x16x32_bf16 v[224:227], a[160:163], a[124:127], v[224:227]\n"
    "s_add_u32 m0, s43, 16384\n"
    "ds_read2st64_b32 v[164:165], v250 offset0:0 offset1:1\n"
    "v_mfma_f32_16x16x32_bf16 v[232:235], a[160:163], a[156:159], 0\n"
    "ds_read_b128 a[160:163], v243 offset:55296\n"
    "v_mfma_f32_16x16x32_bf16 v[232:235], a[164:167], a[128:131], v[232:235]\n"
    "ds_read_b128 a[164:167], v240 offset:51200\n"
    "v_mfma_f32_16x16x32_bf16 v[232:235], a[168:171], a[132:135], v[232:235]\n"
    "buffer_load_dwordx4 v248, s[8:11], s40 offen offset:0 sc1 lds\n"
    "v_mfma_f32_16x16x32_bf16 v[232:235], a[172:175], a[136:139], v[232:235]\n"
    "v_mfma_f32_16x16x32_bf16 v[224:227], a[172:175], a[104:107], v[224:227]\n"
    "v_mfma_f32_16x16x32_bf16 v[224:227], a[168:171], a[100:103], v[224:227]\n"
    "ds_read2st64_b32 v[166:167], v250 offset0:2 offset1:3\n"
    "v_mfma_f32_16x16x32_bf16 v[216:219], a[168:171], a[68:71], v[216:219]\n"
    "v_mfma_f32_16x16x32_bf16 v[216:219], a[172:175], a[72:75], v[216:219]\n"
    "buffer_load_dwordx4 v248, s[8:11], s40 offen offset:1024 sc1 lds\n"
    "v_mfma_f32_16x16x32_bf16 v[208:211], a[172:175], a[40:43], v[208:211]\n"
    "v_mfma_f32_16x16x32_bf16 v[208:211], a[168:171], a[36:39], v[208:211]\n"
    "ds_read2st64_b32 v[172:173], v250 offset0:16 offset1:17\n"
    "v_mfma_f32_16x16x32_bf16 v[200:203], a[168:171], a[4:7], v[200:203]\n"
    "ds_read_b128 a[168:171], v241 offset:51200\n"
    "v_mfma_f32_16x16x32_bf16 v[200:203], a[172:175], a[8:11], v[200:203]\n"
    "ds_read_b128 a[172:175], v240 offset:55296\n"
    "v_mfma_f32_16x16x32_bf16 v[200:203], a[176:179], a[12:15], v[200:203]\n"
    "buffer_load_dwordx4 v248, s[8:11], s40 offen offset:2048 sc1 lds\n"
    "v_mfma_f32_16x16x32_bf16 v[200:203], a[180:183], a[16:19], v[200:203]\n"
    "ds_read2st64_b32 v[174:175], v250 offset0:18 offset1:19\n"
    "v_mfma_f32_16x16x32_bf16 v[208:211], a[180:183], a[48:51], v[208:211]\n"
    "v_mfma_f32_16x16x32_bf16 v[208:211], a[176:179], a[44:47], v[208:211]\n"
    "v_mfma_f32_16x16x32_bf16 v[216:219], a[176:179], a[76:79], v[216:219]\n"
    "ds_read2st64_b32 v[180:181], v250 offset0:32 offset1:33\n"
    "v_mfma_f32_16x16x32_bf16 v[216:219], a[180:183], a[80:83], v[216:219]\n"
    "buffer_load_dwordx4 v248, s[8:11], s40 offen offset:3072 sc1 lds\n"
    "v_mfma_f32_16x16x32_bf16 v[224:227], a[180:183], a[112:115], v[224:227]\n"
    "s_add_u32 m0, s43, 20480\n"
    "v_mfma_f32_16x16x32_bf16 v[224:227], a[176:179], a[108:111], v[224:227]\n"
    "s_add_u32 s40, s40, 0x8000\n"
    "ds_read2st64_b32 v[182:183], v250 offset0:34 offset1:35\n"
    "v_mfma_f32_16x16x32_bf16 v[232:235], a[176:179], a[140:143], v[232:235]\n"
    "ds_read_b128 a[176:179], v241 offset:55296\n"
    "v_mfma_f32_16x16x32_bf16 v[232:235], a[180:183], a[144:147], v[232:235]\n"
    "ds_read_b128 a[180:183], v242 offset:51200\n"
    "v_mfma_f32_16x16x32_bf16 v[232:235], a[184:187], a[148:151], v[232:235]\n"
    "buffer_load_dwordx4 v248, s[8:11], s41 offen offset:0 sc1 lds\n"
    "v_mfma_f32_16x16x32_bf16 v[232:235], a[188:191], a[152:155], v[232:235]\n"
    "v_mfma_f32_16x16x32_bf16 v[224:227], a[188:191], a[120:123], v[224:227]\n"
    "ds_read2st64_b32 v[188:189], v250 offset0:48 offset1:49\n"
    "v_mfma_f32_16x16x32_bf16 v[224:227], a[184:187], a[116:119], v[224:227]\n"
    "v_mfma_f32_16x16x32_bf16 v[216:219], a[184:187], a[84:87], v[216:219]\n"
    "v_exp_f32_e32 v232, v232\n"
    "v_mfma_f32_16x16x32_bf16 v[216:219], a[188:191], a[88:91], v[216:219]\n"
    "buffer_load_dwordx4 v248, s[8:11], s41 offen offset:1024 sc1 lds\n"
    "v_mfma_f32_16x16x32_bf16 v[208:211], a[188:191], a[56:59], v[208:211]\n"
    "ds_read2st64_b32 v[190:191], v250 offset0:50 offset1:51\n"
    "v_mfma_f32_16x16x32_bf16 v[208:211], a[184:187], a[52:55], v[208:211]\n"
    "v_exp_f32_e32 v233, v233\n"
    "v_mfma_f32_16x16x32_bf16 v[200:203], a[184:187], a[20:23], v[200:203]\n"
    "ds_read_b128 a[184:187], v243 offset:51200\n"
    "v_mfma_f32_16x16x32_bf16 v[200:203], a[188:191], a[24:27], v[200:203]\n"
    "s_waitcnt lgkmcnt(8)\n"
    "ds_read_b128 a[188:191], v242 offset:55296\n"
    "v_mfma_f32_16x16x32_bf16 v[204:207], a[160:163], a[28:31], 0\n"
    "buffer_load_dwordx4 v248, s[8:11], s41 offen offset:2048 sc1 lds\n"
    "v_mfma_f32_16x16x32_bf16 v[204:207], a[164:167], a[0:3], v[204:207]\n"
    "ds_read2st64_b32 v[196:197], v250 offset0:64 offset1:65\n"
    "v_mfma_f32_16x16x32_bf16 v[212:215], a[164:167], a[32:35], 0\n"
    "v_exp_f32_e32 v234, v234\n"
    "v_mfma_f32_16x16x32_bf16 v[212:215], a[160:163], a[60:63], v[212:215]\n"
    "ds_read2st64_b32 v[198:199], v250 offset0:66 offset1:67\n"
    "v_mfma_f32_16x16x32_bf16 v[220:223], a[160:163], a[92:95], 0\n"
    "v_exp_f32_e32 v235, v235\n"
    "v_mfma_f32_16x16x32_bf16 v[220:223], a[164:167], a[64:67], v[220:223]\n"
    "buffer_load_dwordx4 v248, s[8:11], s41 offen offset:3072 sc1 lds\n"
    "v_cvt_pk_bf16_f32 v232, v232, v233\n"
    "v_mfma_f32_16x16x32_bf16 v[228:231], a[164:167], a[96:99], 0\n"
    "s_add_u32 s41, s41, 0x8000\n"
    "v_exp_f32_e32 v224, v224\n"
    "v_mfma_f32_16x16x32_bf16 v[228:231], a[160:163], a[124:127], v[228:231]\n"
    "v_exp_f32_e32 v225, v225\n"
    "v_mfma_f32_16x16x32_bf16 v[236:239], a[160:163], a[156:159], 0\n"
    "ds_read_b64_tr_b16 a[160:161], v244 offset:32768\n"
    "v_cvt_pk_bf16_f32 v233, v234, v235\n"
    "v_mfma_f32_16x16x32_bf16 v[236:239], a[164:167], a[128:131], v[236:239]\n"
    "ds_read_b64_tr_b16 a[162:163], v244 offset:34816\n"
    "v_mfma_f32_16x16x32_bf16 v[236:239], a[168:171], a[132:135], v[236:239]\n"
    "ds_read_b64_tr_b16 a[164:165], v244 offset:40960\n"
    "v_mfma_f32_16x16x32_bf16 v[236:239], a[172:175], a[136:139], v[236:239]\n"
    "ds_read_b64_tr_b16 a[166:167], v244 offset:43008\n"
    "v_mfma_f32_16x16x32_bf16 v[228:231], a[172:175], a[104:107], v[228:231]\n"
    "v_exp_f32_e32 v226, v226\n"
    "v_mfma_f32_16x16x32_bf16 v[228:231], a[168:171], a[100:103], v[228:231]\n"
    "v_exp_f32_e32 v227, v227\n"
    "v_mfma_f32_16x16x32_bf16 v[220:223], a[168:171], a[68:71], v[220:223]\n"
    "v_cvt_pk_bf16_f32 v224, v224, v225\n"
    "v_mfma_f32_16x16x32_bf16 v[220:223], a[172:175], a[72:75], v[220:223]\n"
    "v_exp_f32_e32 v216, v216\n"
    "v_mfma_f32_16x16x32_bf16 v[212:215], a[172:175], a[40:43], v[212:215]\n"
    "v_exp_f32_e32 v217, v217\n"
    "v_mfma_f32_16x16x32_bf16 v[212:215], a[168:171], a[36:39], v[212:215]\n"
    "v_cvt_pk_bf16_f32 v225, v226, v227\n"
    "v_mfma_f32_16x16x32_bf16 v[204:207], a[168:171], a[4:7], v[204:207]\n"
    "ds_read_b64_tr_b16 a[168:169], v245 offset:32768\n"
    "v_mfma_f32_16x16x32_bf16 v[204:207], a[172:175], a[8:11], v[204:207]\n"
    "s_waitcnt lgkmcnt(7)\n"
    "ds_read_b64_tr_b16 a[170:171], v245 offset:34816\n"
    "v_mfma_f32_16x16x32_bf16 v[204:207], a[176:179], a[12:15], v[204:207]\n"
    "ds_read_b64_tr_b16 a[172:173], v245 offset:40960\n"
    "v_mfma_f32_16x16x32_bf16 v[204:207], a[180:183], a[16:19], v[204:207]\n"
    "ds_read_b64_tr_b16 a[174:175], v245 offset:43008\n"
    "v_mfma_f32_16x16x32_bf16 v[212:215], a[180:183], a[48:51], v[212:215]\n"
    "v_exp_f32_e32 v218, v218\n"
    "v_mfma_f32_16x16x32_bf16 v[212:215], a[176:179], a[44:47], v[212:215]\n"
    "v_exp_f32_e32 v219, v219\n"
    "v_mfma_f32_16x16x32_bf16 v[220:223], a[176:179], a[76:79], v[220:223]\n"
    "v_cvt_pk_bf16_f32 v216, v216, v217\n"
    "v_mfma_f32_16x16x32_bf16 v[220:223], a[180:183], a[80:83], v[220:223]\n"
    "v_exp_f32_e32 v208, v208\n"
    "v_mfma_f32_16x16x32_bf16 v[228:231], a[180:183], a[112:115], v[228:231]\n"
    "v_exp_f32_e32 v209, v209\n"
    "v_mfma_f32_16x16x32_bf16 v[228:231], a[176:179], a[108:111], v[228:231]\n"
    "v_cvt_pk_bf16_f32 v217, v218, v219\n"
    "v_mfma_f32_16x16x32_bf16 v[236:239], a[176:179], a[140:143], v[236:239]\n"
    "ds_read_b64_tr_b16 a[176:177], v246 offset:32768\n"
    "v_mfma_f32_16x16x32_bf16 v[236:239], a[180:183], a[144:147], v[236:239]\n"
    "ds_read_b64_tr_b16 a[178:179], v246 offset:34816\n"
    "v_mfma_f32_16x16x32_bf16 v[236:239], a[184:187], a[148:151], v[236:239]\n"
    "ds_read_b64_tr_b16 a[180:181], v246 offset:40960\n"
    "v_mfma_f32_16x16x32_bf16 v[236:239], a[188:191], a[152:155], v[236:239]\n"
    "ds_read_b64_tr_b16 a[182:183], v246 offset:43008\n"
    "v_mfma_f32_16x16x32_bf16 v[228:231], a[188:191], a[120:123], v[228:231]\n"
    "v_exp_f32_e32 v210, v210\n"
    "v_mfma_f32_16x16x32_bf16 v[228:231], a[184:187], a[116:119], v[228:231]\n"
    "v_exp_f32_e32 v211, v211\n"
    "v_mfma_f32_16x16x32_bf16 v[220:223], a[184:187], a[84:87], v[220:223]\n"
    "v_cvt_pk_bf16_f32 v208, v208, v209\n"
    "v_mfma_f32_16x16x32_bf16 v[220:223], a[188:191], a[88:91], v[220:223]\n"
    "v_exp_f32_e32 v200, v200\n"
    "v_mfma_f32_16x16x32_bf16 v[212:215], a[188:191], a[56:59], v[212:215]\n"
    "v_exp_f32_e32 v201, v201\n"
    "v_mfma_f32_16x16x32_bf16 v[212:215], a[184:187], a[52:55], v[212:215]\n"
    "v_cvt_pk_bf16_f32 v209, v210, v211\n"
    "v_mfma_f32_16x16x32_bf16 v[204:207], a[184:187], a[20:23], v[204:207]\n"
    "ds_read_b64_tr_b16 a[184:185], v247 offset:32768\n"
    "v_mfma_f32_16x16x32_bf16 v[204:207], a[188:191], a[24:27], v[204:207]\n"
    "s_waitcnt lgkmcnt(5)\n"
    "ds_read_b64_tr_b16 a[186:187], v247 offset:34816\n"
    "v_mfma_f32_16x16x32_bf16 v[0:3], a[160:163], v[160:163], v[0:3]\n"
    "ds_read_b64_tr_b16 a[188:189], v247 offset:40960\n"
    "v_mfma_f32_16x16x32_bf16 v[0:3], a[164:167], v[164:167], v[0:3]\n"
    "ds_read_b64_tr_b16 a[190:191], v247 offset:43008\n"
    "v_mfma_f32_16x16x32_bf16 a[248:251], a[164:167], v[172:175], a[248:251]\n"
    "v_exp_f32_e32 v202, v202\n"
    "v_mfma_f32_16x16x32_bf16 a[248:251], a[160:163], v[168:171], a[248:251]\n"
    "v_exp_f32_e32 v203, v203\n"
    "v_mfma_f32_16x16x32_bf16 v[56:59], a[160:163], v[176:179], v[56:59]\n"
    "v_cvt_pk_bf16_f32 v200, v200, v201\n"
    "v_mfma_f32_16x16x32_bf16 v[56:59], a[164:167], v[180:183], v[56:59]\n"
    "v_cvt_pk_bf16_f32 v201, v202, v203\n"
    "v_mfma_f32_16x16x32_bf16 v[60:63], a[164:167], v[188:191], v[60:63]\n"
    "v_exp_f32_e32 v236, v236\n"
    "v_mfma_f32_16x16x32_bf16 v[60:63], a[160:163], v[184:187], v[60:63]\n"
    "v_exp_f32_e32 v237, v237\n"
    "v_mfma_f32_16x16x32_bf16 v[64:67], a[160:163], v[192:195], v[64:67]\n"
    "ds_read_b64_tr_b16 a[160:161], v244 offset:36864\n"
    "v_mfma_f32_16x16x32_bf16 v[64:67], a[164:167], v[196:199], v[64:67]\n"
    "ds_read_b64_tr_b16 a[162:163], v244 offset:38912\n"
    "v_mfma_f32_16x16x32_bf16 v[76:79], a[168:171], v[192:195], v[76:79]\n"
    "ds_read_b64_tr_b16 a[164:165], v244 offset:45056\n"
    "v_mfma_f32_16x16x32_bf16 v[76:79], a[172:175], v[196:199], v[76:79]\n"
    "ds_read_b64_tr_b16 a[166:167], v244 offset:47104\n"
    "v_mfma_f32_16x16x32_bf16 v[72:75], a[172:175], v[188:191], v[72:75]\n"
    "v_exp_f32_e32 v238, v238\n"
    "v_mfma_f32_16x16x32_bf16 v[72:75], a[168:171], v[184:187], v[72:75]\n"
    "v_exp_f32_e32 v239, v239\n"
    "v_mfma_f32_16x16x32_bf16 v[68:71], a[168:171], v[176:179], v[68:71]\n"
    "v_cvt_pk_bf16_f32 v234, v236, v237\n"
    "v_mfma_f32_16x16x32_bf16 v[68:71], a[172:175], v[180:183], v[68:71]\n"
    "v_exp_f32_e32 v228, v228\n"
    "v_mfma_f32_16x16x32_bf16 a[252:255], a[172:175], v[172:175], a[252:255]\n"
    "v_exp_f32_e32 v229, v229\n"
    "v_mfma_f32_16x16x32_bf16 a[252:255], a[168:171], v[168:171], a[252:255]\n"
    "v_cvt_pk_bf16_f32 v235, v238, v239\n"
    "v_mfma_f32_16x16x32_bf16 v[4:7], a[168:171], v[160:163], v[4:7]\n"
    "ds_read_b64_tr_b16 a[168:169], v245 offset:36864\n"
    "v_mfma_f32_16x16x32_bf16 v[4:7], a[172:175], v[164:167], v[4:7]\n"
    "s_waitcnt lgkmcnt(5)\n"
    "ds_read_b64_tr_b16 a[170:171], v245 offset:38912\n"
    "v_mfma_f32_16x16x32_bf16 v[8:11], a[176:179], v[160:163], v[8:11]\n"
    "ds_read_b64_tr_b16 a[172:173], v245 offset:45056\n"
    "v_mfma_f32_16x16x32_bf16 v[8:11], a[180:183], v[164:167], v[8:11]\n"
    "ds_read_b64_tr_b16 a[174:175], v245 offset:47104\n"
    "v_mfma_f32_16x16x32_bf16 v[32:35], a[180:183], v[172:175], v[32:35]\n"
    "s_or_b32 s45, s44, s37\n"
    "s_cselect_b32 s46, 2.0, s81\n"
    "s_cselect_b32 s45, 2.0, s80\n"
    "v_mfma_f32_16x16x32_bf16 v[32:35], a[176:179], v[168:171], v[32:35]\n"
    "v_exp_f32_e32 v230, v230\n"
    "v_mfma_f32_16x16x32_bf16 v[80:83], a[176:179], v[176:179], v[80:83]\n"
    "v_exp_f32_e32 v231, v231\n"
    "v_mfma_f32_16x16x32_bf16 v[80:83], a[180:183], v[180:183], v[80:83]\n"
    "v_cvt_pk_bf16_f32 v226, v228, v229\n"
    "v_mfma_f32_16x16x32_bf16 v[84:87], a[180:183], v[188:191], v[84:87]\n"
    "v_exp_f32_e32 v220, v220\n"
    "v_mfma_f32_16x16x32_bf16 v[84:87], a[176:179], v[184:187], v[84:87]\n"
    "v_exp_f32_e32 v221, v221\n"
    "v_mfma_f32_16x16x32_bf16 v[88:91], a[176:179], v[192:195], v[88:91]\n"
    "ds_read_b64_tr_b16 a[176:177], v246 offset:36864\n"
    "v_mfma_f32_16x16x32_bf16 v[88:91], a[180:183], v[196:199], v[88:91]\n"
    "ds_read_b64_tr_b16 a[178:179], v246 offset:38912\n"
    "v_mfma_f32_16x16x32_bf16 v[100:103], a[184:187], v[192:195], v[100:103]\n"
    "ds_read_b64_tr_b16 a[180:181], v246 offset:45056\n"
    "v_mfma_f32_16x16x32_bf16 v[100:103], a[188:191], v[196:199], v[100:103]\n"
    "ds_read_b64_tr_b16 a[182:183], v246 offset:47104\n"
    "v_mfma_f32_16x16x32_bf16 v[96:99], a[188:191], v[188:191], v[96:99]\n"
    "v_cvt_pk_bf16_f32 v227, v230, v231\n"
    "v_mfma_f32_16x16x32_bf16 v[96:99], a[184:187], v[184:187], v[96:99]\n"
    "v_exp_f32_e32 v222, v222\n"
    "v_mfma_f32_16x16x32_bf16 v[92:95], a[184:187], v[176:179], v[92:95]\n"
    "v_exp_f32_e32 v223, v223\n"
    "v_mfma_f32_16x16x32_bf16 v[92:95], a[188:191], v[180:183], v[92:95]\n"
    "v_cvt_pk_bf16_f32 v218, v220, v221\n"
    "v_mfma_f32_16x16x32_bf16 v[36:39], a[188:191], v[172:175], v[36:39]\n"
    "v_exp_f32_e32 v212, v212\n"
    "v_mfma_f32_16x16x32_bf16 v[36:39], a[184:187], v[168:171], v[36:39]\n"
    "v_exp_f32_e32 v213, v213\n"
    "v_mfma_f32_16x16x32_bf16 v[12:15], a[184:187], v[160:163], v[12:15]\n"
    "s_mov_b32 m0, s49\n"
    "ds_read_b64_tr_b16 a[184:185], v247 offset:36864\n"
    "v_mfma_f32_16x16x32_bf16 v[12:15], a[188:191], v[164:167], v[12:15]\n"
    "s_waitcnt lgkmcnt(5)\n"
    "ds_read_b64_tr_b16 a[186:187], v247 offset:38912\n"
    "v_mfma_f32_16x16x32_bf16 v[16:19], a[160:163], v[160:163], v[16:19]\n"
    "ds_read_b64_tr_b16 a[188:189], v247 offset:45056\n"
    "v_mfma_f32_16x16x32_bf16 v[16:19], a[164:167], v[164:167], v[16:19]\n"
    "ds_read_b64_tr_b16 a[190:191], v247 offset:47104\n"
    "v_mfma_f32_16x16x32_bf16 v[40:43], a[164:167], v[172:175], v[40:43]\n"
    "buffer_load_dword v154, off, s[8:11], s45 sc1\n"
    "v_mfma_f32_16x16x32_bf16 v[40:43], a[160:163], v[168:171], v[40:43]\n"
    "v_cvt_pk_bf16_f32 v219, v222, v223\n"
    "v_mfma_f32_16x16x32_bf16 v[104:107], a[160:163], v[176:179], v[104:107]\n"
    "v_exp_f32_e32 v214, v214\n"
    "v_mfma_f32_16x16x32_bf16 v[104:107], a[164:167], v[180:183], v[104:107]\n"
    "s_waitcnt vmcnt(9)\n"
    "v_mfma_f32_16x16x32_bf16 v[108:111], a[164:167], v[188:191], v[108:111]\n"
    "s_barrier\n"
    "v_mfma_f32_16x16x32_bf16 v[108:111], a[160:163], v[184:187], v[108:111]\n"
    "v_exp_f32_e32 v215, v215\n"
    "v_mfma_f32_16x16x32_bf16 v[112:115], a[160:163], v[192:195], v[112:115]\n"
    "ds_read_b128 a[160:163], v243 offset:4096\n"
    "v_mfma_f32_16x16x32_bf16 v[112:115], a[164:167], v[196:199], v[112:115]\n"
    "ds_read_b128 a[164:167], v240 offset:0\n"
    "v_mfma_f32_16x16x32_bf16 v[124:127], a[168:171], v[192:195], v[124:127]\n"
    "ds_write_addtid_b32 v232 offset:16384\n"
    "v_cvt_pk_bf16_f32 v210, v212, v213\n"
    "v_mfma_f32_16x16x32_bf16 v[124:127], a[172:175], v[196:199], v[124:127]\n"
    "v_exp_f32_e32 v204, v204\n"
    "v_mfma_f32_16x16x32_bf16 v[120:123], a[172:175], v[188:191], v[120:123]\n"
    "v_exp_f32_e32 v205, v205\n"
    "v_mfma_f32_16x16x32_bf16 v[120:123], a[168:171], v[184:187], v[120:123]\n"
    "ds_write_addtid_b32 v233 offset:16640\n"
    "v_cvt_pk_bf16_f32 v211, v214, v215\n"
    "v_mfma_f32_16x16x32_bf16 v[116:119], a[168:171], v[176:179], v[116:119]\n"
    "v_exp_f32_e32 v206, v206\n"
    "v_mfma_f32_16x16x32_bf16 v[116:119], a[172:175], v[180:183], v[116:119]\n"
    "v_exp_f32_e32 v207, v207\n"
    "v_mfma_f32_16x16x32_bf16 v[44:47], a[172:175], v[172:175], v[44:47]\n"
    "ds_write_addtid_b32 v224 offset:12288\n"
    "v_cvt_pk_bf16_f32 v202, v204, v205\n"
    "v_mfma_f32_16x16x32_bf16 v[44:47], a[168:171], v[168:171], v[44:47]\n"
    "ds_write_addtid_b32 v225 offset:12544\n"
    "v_cvt_pk_bf16_f32 v203, v206, v207\n"
    "v_mfma_f32_16x16x32_bf16 v[20:23], a[168:171], v[160:163], v[20:23]\n"
    "ds_read_b128 a[168:171], v241 offset:0\n"
    "v_mfma_f32_16x16x32_bf16 v[20:23], a[172:175], v[164:167], v[20:23]\n"
    "s_waitcnt lgkmcnt(7)\n"
    "ds_read_b128 a[172:175], v240 offset:4096\n"
    "v_mfma_f32_16x16x32_bf16 v[24:27], a[176:179], v[160:163], v[24:27]\n"
    "ds_write_addtid_b32 v216 offset:8192\n"
    "v_mfma_f32_16x16x32_bf16 v[24:27], a[180:183], v[164:167], v[24:27]\n"
    "ds_write_addtid_b32 v217 offset:8448\n"
    "v_mfma_f32_16x16x32_bf16 v[48:51], a[180:183], v[172:175], v[48:51]\n"
    "ds_write_addtid_b32 v208 offset:4096\n"
    "v_mfma_f32_16x16x32_bf16 v[48:51], a[176:179], v[168:171], v[48:51]\n"
    "ds_write_addtid_b32 v209 offset:4352\n"
    "v_mfma_f32_16x16x32_bf16 v[128:131], a[176:179], v[176:179], v[128:131]\n"
    "ds_write_addtid_b32 v200 offset:0\n"
    "v_mfma_f32_16x16x32_bf16 v[128:131], a[180:183], v[180:183], v[128:131]\n"
    "ds_write_addtid_b32 v201 offset:256\n"
    "v_mfma_f32_16x16x32_bf16 v[132:135], a[180:183], v[188:191], v[132:135]\n"
    "ds_write_addtid_b32 v234 offset:16896\n"
    "v_mfma_f32_16x16x32_bf16 v[132:135], a[176:179], v[184:187], v[132:135]\n"
    "ds_write_addtid_b32 v235 offset:17152\n"
    "v_mfma_f32_16x16x32_bf16 v[136:139], a[176:179], v[192:195], v[136:139]\n"
    "ds_read_b128 a[176:179], v241 offset:4096\n"
    "v_mfma_f32_16x16x32_bf16 v[136:139], a[180:183], v[196:199], v[136:139]\n"
    "ds_read_b128 a[180:183], v242 offset:0\n"
    "v_mfma_f32_16x16x32_bf16 v[148:151], a[184:187], v[192:195], v[148:151]\n"
    "ds_write_addtid_b32 v226 offset:12800\n"
    "v_mfma_f32_16x16x32_bf16 v[148:151], a[188:191], v[196:199], v[148:151]\n"
    "ds_write_addtid_b32 v227 offset:13056\n"
    "v_mfma_f32_16x16x32_bf16 v[144:147], a[188:191], v[188:191], v[144:147]\n"
    "ds_write_addtid_b32 v218 offset:8704\n"
    "v_mfma_f32_16x16x32_bf16 v[144:147], a[184:187], v[184:187], v[144:147]\n"
    "ds_write_addtid_b32 v219 offset:8960\n"
    "v_mfma_f32_16x16x32_bf16 v[140:143], a[184:187], v[176:179], v[140:143]\n"
    "ds_write_addtid_b32 v210 offset:4608\n"
    "v_mfma_f32_16x16x32_bf16 v[140:143], a[188:191], v[180:183], v[140:143]\n"
    "ds_write_addtid_b32 v211 offset:4864\n"
    "v_mfma_f32_16x16x32_bf16 v[52:55], a[188:191], v[172:175], v[52:55]\n"
    "ds_write_addtid_b32 v202 offset:512\n"
    "v_mfma_f32_16x16x32_bf16 v[52:55], a[184:187], v[168:171], v[52:55]\n"
    "ds_write_addtid_b32 v203 offset:768\n"
    "v_mfma_f32_16x16x32_bf16 v[28:31], a[184:187], v[160:163], v[28:31]\n"
    "ds_read_b128 a[184:187], v243 offset:0\n"
    "v_mfma_f32_16x16x32_bf16 v[28:31], a[188:191], v[164:167], v[28:31]\n"
    "s_waitcnt lgkmcnt(15)\n"
    "ds_read_b128 a[188:191], v242 offset:4096\n"
    "v_mfma_f32_16x16x32_bf16 v[160:163], a[160:163], a[28:31], 0\n"
    "v_lshlrev_b32_e32 v253, 2, v249\n"
    "v_mfma_f32_16x16x32_bf16 v[160:163], a[164:167], a[0:3], v[160:163]\n"
    "v_mfma_f32_16x16x32_bf16 v[168:171], a[164:167], a[32:35], 0\n"
    "v_mfma_f32_16x16x32_bf16 v[168:171], a[160:163], a[60:63], v[168:171]\n"
    "v_mfma_f32_16x16x32_bf16 v[176:179], a[160:163], a[92:95], 0\n"
    "v_mfma_f32_16x16x32_bf16 v[176:179], a[164:167], a[64:67], v[176:179]\n"
    "s_waitcnt lgkmcnt(0)\n"
    "v_mfma_f32_16x16x32_bf16 v[184:187], a[164:167], a[96:99], 0\n"
    "s_barrier\n"
    "v_mfma_f32_16x16x32_bf16 v[184:187], a[160:163], a[124:127], v[184:187]\n"
    "s_add_u32 m0, s43, 32768\n"
    "ds_read2st64_b32 v[204:205], v250 offset0:0 offset1:1\n"
    "v_mfma_f32_16x16x32_bf16 v[192:195], a[160:163], a[156:159], 0\n"
    "ds_read_b128 a[160:163], v243 offset:6144\n"
    "v_mfma_f32_16x16x32_bf16 v[192:195], a[164:167], a[128:131], v[192:195]\n"
    "ds_read_b128 a[164:167], v240 offset:2048\n"
    "v_mfma_f32_16x16x32_bf16 v[192:195], a[168:171], a[132:135], v[192:195]\n"
    "buffer_load_dwordx4 v248, s[8:11], s40 offen offset:0 sc1 lds\n"
    "v_mfma_f32_16x16x32_bf16 v[192:195], a[172:175], a[136:139], v[192:195]\n"
    "v_mfma_f32_16x16x32_bf16 v[184:187], a[172:175], a[104:107], v[184:187]\n"
    "v_mfma_f32_16x16x32_bf16 v[184:187], a[168:171], a[100:103], v[184:187]\n"
    "ds_read2st64_b32 v[206:207], v250 offset0:2 offset1:3\n"
    "v_mfma_f32_16x16x32_bf16 v[176:179], a[168:171], a[68:71], v[176:179]\n"
    "v_mfma_f32_16x16x32_bf16 v[176:179], a[172:175], a[72:75], v[176:179]\n"
    "buffer_load_dwordx4 v248, s[8:11], s40 offen offset:1024 sc1 lds\n"
    "v_mfma_f32_16x16x32_bf16 v[168:171], a[172:175], a[40:43], v[168:171]\n"
    "v_mfma_f32_16x16x32_bf16 v[168:171], a[168:171], a[36:39], v[168:171]\n"
    "ds_read2st64_b32 v[212:213], v250 offset0:16 offset1:17\n"
    "v_mfma_f32_16x16x32_bf16 v[160:163], a[168:171], a[4:7], v[160:163]\n"
    "ds_read_b128 a[168:171], v241 offset:2048\n"
    "v_mfma_f32_16x16x32_bf16 v[160:163], a[172:175], a[8:11], v[160:163]\n"
    "ds_read_b128 a[172:175], v240 offset:6144\n"
    "v_mfma_f32_16x16x32_bf16 v[160:163], a[176:179], a[12:15], v[160:163]\n"
    "buffer_load_dwordx4 v248, s[8:11], s40 offen offset:2048 sc1 lds\n"
    "v_mfma_f32_16x16x32_bf16 v[160:163], a[180:183], a[16:19], v[160:163]\n"
    "ds_read2st64_b32 v[214:215], v250 offset0:18 offset1:19\n"
    "v_mfma_f32_16x16x32_bf16 v[168:171], a[180:183], a[48:51], v[168:171]\n"
    "v_mfma_f32_16x16x32_bf16 v[168:171], a[176:179], a[44:47], v[168:171]\n"
    "v_mfma_f32_16x16x32_bf16 v[176:179], a[176:179], a[76:79], v[176:179]\n"
    "ds_read2st64_b32 v[220:221], v250 offset0:32 offset1:33\n"
    "v_mfma_f32_16x16x32_bf16 v[176:179], a[180:183], a[80:83], v[176:179]\n"
    "buffer_load_dwordx4 v248, s[8:11], s40 offen offset:3072 sc1 lds\n"
    "v_mfma_f32_16x16x32_bf16 v[184:187], a[180:183], a[112:115], v[184:187]\n"
    "s_add_u32 m0, s43, 36864\n"
    "v_mfma_f32_16x16x32_bf16 v[184:187], a[176:179], a[108:111], v[184:187]\n"
    "s_add_u32 s40, s40, 0x8000\n"
    "ds_read2st64_b32 v[222:223], v250 offset0:34 offset1:35\n"
    "v_mfma_f32_16x16x32_bf16 v[192:195], a[176:179], a[140:143], v[192:195]\n"
    "ds_read_b128 a[176:179], v241 offset:6144\n"
    "v_mfma_f32_16x16x32_bf16 v[192:195], a[180:183], a[144:147], v[192:195]\n"
    "ds_read_b128 a[180:183], v242 offset:2048\n"
    "v_mfma_f32_16x16x32_bf16 v[192:195], a[184:187], a[148:151], v[192:195]\n"
    "buffer_load_dwordx4 v248, s[8:11], s41 offen offset:0 sc1 lds\n"
    "v_mfma_f32_16x16x32_bf16 v[192:195], a[188:191], a[152:155], v[192:195]\n"
    "v_mfma_f32_16x16x32_bf16 v[184:187], a[188:191], a[120:123], v[184:187]\n"
    "ds_read2st64_b32 v[228:229], v250 offset0:48 offset1:49\n"
    "v_mfma_f32_16x16x32_bf16 v[184:187], a[184:187], a[116:119], v[184:187]\n"
    "v_mfma_f32_16x16x32_bf16 v[176:179], a[184:187], a[84:87], v[176:179]\n"
    "v_exp_f32_e32 v192, v192\n"
    "v_mfma_f32_16x16x32_bf16 v[176:179], a[188:191], a[88:91], v[176:179]\n"
    "buffer_load_dwordx4 v248, s[8:11], s41 offen offset:1024 sc1 lds\n"
    "v_mfma_f32_16x16x32_bf16 v[168:171], a[188:191], a[56:59], v[168:171]\n"
    "ds_read2st64_b32 v[230:231], v250 offset0:50 offset1:51\n"
    "v_mfma_f32_16x16x32_bf16 v[168:171], a[184:187], a[52:55], v[168:171]\n"
    "v_exp_f32_e32 v193, v193\n"
    "v_mfma_f32_16x16x32_bf16 v[160:163], a[184:187], a[20:23], v[160:163]\n"
    "ds_read_b128 a[184:187], v243 offset:2048\n"
    "v_mfma_f32_16x16x32_bf16 v[160:163], a[188:191], a[24:27], v[160:163]\n"
    "s_waitcnt lgkmcnt(8)\n"
    "ds_read_b128 a[188:191], v242 offset:6144\n"
    "v_mfma_f32_16x16x32_bf16 v[164:167], a[160:163], a[28:31], 0\n"
    "buffer_load_dwordx4 v248, s[8:11], s41 offen offset:2048 sc1 lds\n"
    "v_mfma_f32_16x16x32_bf16 v[164:167], a[164:167], a[0:3], v[164:167]\n"
    "ds_read2st64_b32 v[236:237], v250 offset0:64 offset1:65\n"
    "v_mfma_f32_16x16x32_bf16 v[172:175], a[164:167], a[32:35], 0\n"
    "v_exp_f32_e32 v194, v194\n"
    "v_mfma_f32_16x16x32_bf16 v[172:175], a[160:163], a[60:63], v[172:175]\n"
    "ds_read2st64_b32 v[238:239], v250 offset0:66 offset1:67\n"
    "v_mfma_f32_16x16x32_bf16 v[180:183], a[160:163], a[92:95], 0\n"
    "v_exp_f32_e32 v195, v195\n"
    "v_mfma_f32_16x16x32_bf16 v[180:183], a[164:167], a[64:67], v[180:183]\n"
    "buffer_load_dwordx4 v248, s[8:11], s41 offen offset:3072 sc1 lds\n"
    "v_cvt_pk_bf16_f32 v192, v192, v193\n"
    "v_mfma_f32_16x16x32_bf16 v[188:191], a[164:167], a[96:99], 0\n"
    "s_add_u32 s41, s41, 0x8000\n"
    "v_exp_f32_e32 v184, v184\n"
    "v_mfma_f32_16x16x32_bf16 v[188:191], a[160:163], a[124:127], v[188:191]\n"
    "v_exp_f32_e32 v185, v185\n"
    "v_mfma_f32_16x16x32_bf16 v[196:199], a[160:163], a[156:159], 0\n"
    "ds_read_b64_tr_b16 a[160:161], v244 offset:49152\n"
    "v_mfma_f32_16x16x32_bf16 v[196:199], a[164:167], a[128:131], v[196:199]\n"
    "ds_read_b64_tr_b16 a[162:163], v244 offset:51200\n"
    "v_mfma_f32_16x16x32_bf16 v[196:199], a[168:171], a[132:135], v[196:199]\n"
    "ds_read_b64_tr_b16 a[164:165], v244 offset:57344\n"
    "v_mfma_f32_16x16x32_bf16 v[196:199], a[172:175], a[136:139], v[196:199]\n"
    "ds_read_b64_tr_b16 a[166:167], v244 offset:59392\n"
    "v_mfma_f32_16x16x32_bf16 v[188:191], a[172:175], a[104:107], v[188:191]\n"
    "v_cvt_pk_bf16_f32 v193, v194, v195\n"
    "v_mfma_f32_16x16x32_bf16 v[188:191], a[168:171], a[100:103], v[188:191]\n"
    "v_exp_f32_e32 v186, v186\n"
    "v_mfma_f32_16x16x32_bf16 v[180:183], a[168:171], a[68:71], v[180:183]\n"
    "v_exp_f32_e32 v187, v187\n"
    "v_mfma_f32_16x16x32_bf16 v[180:183], a[172:175], a[72:75], v[180:183]\n"
    "v_cvt_pk_bf16_f32 v184, v184, v185\n"
    "v_mfma_f32_16x16x32_bf16 v[172:175], a[172:175], a[40:43], v[172:175]\n"
    "v_exp_f32_e32 v176, v176\n"
    "v_mfma_f32_16x16x32_bf16 v[172:175], a[168:171], a[36:39], v[172:175]\n"
    "v_exp_f32_e32 v177, v177\n"
    "v_mfma_f32_16x16x32_bf16 v[164:167], a[168:171], a[4:7], v[164:167]\n"
    "ds_read_b64_tr_b16 a[168:169], v245 offset:49152\n"
    "v_mfma_f32_16x16x32_bf16 v[164:167], a[172:175], a[8:11], v[164:167]\n"
    "s_waitcnt lgkmcnt(7)\n"
    "ds_read_b64_tr_b16 a[170:171], v245 offset:51200\n"
    "v_mfma_f32_16x16x32_bf16 v[164:167], a[176:179], a[12:15], v[164:167]\n"
    "s_mov_b32 m0, s82\n"
    "ds_read_b64_tr_b16 a[172:173], v245 offset:57344\n"
    "v_mfma_f32_16x16x32_bf16 v[164:167], a[180:183], a[16:19], v[164:167]\n"
    "s_waitcnt vmcnt(8)\n"
    "ds_read_b64_tr_b16 a[174:175], v245 offset:59392\n"
    "v_mfma_f32_16x16x32_bf16 v[172:175], a[180:183], a[48:51], v[172:175]\n"
    "buffer_load_dwordx4 v253, s[8:11], s46 offen offset:0 sc1 lds\n"
    "v_mfma_f32_16x16x32_bf16 v[172:175], a[176:179], a[44:47], v[172:175]\n"
    "v_cvt_pk_bf16_f32 v185, v186, v187\n"
    "v_mfma_f32_16x16x32_bf16 v[180:183], a[176:179], a[76:79], v[180:183]\n"
    "v_exp_f32_e32 v178, v178\n"
    "v_mfma_f32_16x16x32_bf16 v[180:183], a[180:183], a[80:83], v[180:183]\n"
    "v_exp_f32_e32 v179, v179\n"
    "v_mfma_f32_16x16x32_bf16 v[188:191], a[180:183], a[112:115], v[188:191]\n"
    "v_cvt_pk_bf16_f32 v176, v176, v177\n"
    "v_mfma_f32_16x16x32_bf16 v[188:191], a[176:179], a[108:111], v[188:191]\n"
    "v_exp_f32_e32 v168, v168\n"
    "v_mfma_f32_16x16x32_bf16 v[196:199], a[176:179], a[140:143], v[196:199]\n"
    "ds_read_b64_tr_b16 a[176:177], v246 offset:49152\n"
    "v_mfma_f32_16x16x32_bf16 v[196:199], a[180:183], a[144:147], v[196:199]\n"
    "ds_read_b64_tr_b16 a[178:179], v246 offset:51200\n"
    "v_mfma_f32_16x16x32_bf16 v[196:199], a[184:187], a[148:151], v[196:199]\n"
    "ds_read_b64_tr_b16 a[180:181], v246 offset:57344\n"
    "v_mfma_f32_16x16x32_bf16 v[196:199], a[188:191], a[152:155], v[196:199]\n"
    "ds_read_b64_tr_b16 a[182:183], v246 offset:59392\n"
    "v_mfma_f32_16x16x32_bf16 v[188:191], a[188:191], a[120:123], v[188:191]\n"
    "v_exp_f32_e32 v169, v169\n"
    "v_mfma_f32_16x16x32_bf16 v[188:191], a[184:187], a[116:119], v[188:191]\n"
    "v_cvt_pk_bf16_f32 v177, v178, v179\n"
    "v_mfma_f32_16x16x32_bf16 v[180:183], a[184:187], a[84:87], v[180:183]\n"
    "v_exp_f32_e32 v170, v170\n"
    "v_mfma_f32_16x16x32_bf16 v[180:183], a[188:191], a[88:91], v[180:183]\n"
    "v_exp_f32_e32 v171, v171\n"
    "v_mfma_f32_16x16x32_bf16 v[172:175], a[188:191], a[56:59], v[172:175]\n"
    "v_cvt_pk_bf16_f32 v168, v168, v169\n"
    "v_mfma_f32_16x16x32_bf16 v[172:175], a[184:187], a[52:55], v[172:175]\n"
    "v_exp_f32_e32 v160, v160\n"
    "v_mfma_f32_16x16x32_bf16 v[164:167], a[184:187], a[20:23], v[164:167]\n"
    "ds_read_b64_tr_b16 a[184:185], v247 offset:49152\n"
    "v_mfma_f32_16x16x32_bf16 v[164:167], a[188:191], a[24:27], v[164:167]\n"
    "s_waitcnt lgkmcnt(5)\n"
    "ds_read_b64_tr_b16 a[186:187], v247 offset:51200\n"
    "v_mfma_f32_16x16x32_bf16 v[0:3], a[160:163], v[200:203], v[0:3]\n"
    "ds_read_b64_tr_b16 a[188:189], v247 offset:57344\n"
    "v_mfma_f32_16x16x32_bf16 v[0:3], a[164:167], v[204:207], v[0:3]\n"
    "ds_read_b64_tr_b16 a[190:191], v247 offset:59392\n"
    "v_mfma_f32_16x16x32_bf16 a[248:251], a[164:167], v[212:215], a[248:251]\n"
    "v_exp_f32_e32 v161, v161\n"
    "v_mfma_f32_16x16x32_bf16 a[248:251], a[160:163], v[208:211], a[248:251]\n"
    "v_cvt_pk_bf16_f32 v169, v170, v171\n"
    "v_mfma_f32_16x16x32_bf16 v[56:59], a[160:163], v[216:219], v[56:59]\n"
    "v_exp_f32_e32 v162, v162\n"
    "v_mfma_f32_16x16x32_bf16 v[56:59], a[164:167], v[220:223], v[56:59]\n"
    "v_exp_f32_e32 v163, v163\n"
    "v_mfma_f32_16x16x32_bf16 v[60:63], a[164:167], v[228:231], v[60:63]\n"
    "v_cvt_pk_bf16_f32 v160, v160, v161\n"
    "v_mfma_f32_16x16x32_bf16 v[60:63], a[160:163], v[224:227], v[60:63]\n"
    "v_cvt_pk_bf16_f32 v161, v162, v163\n"
    "v_mfma_f32_16x16x32_bf16 v[64:67], a[160:163], v[232:235], v[64:67]\n"
    "ds_read_b64_tr_b16 a[160:161], v244 offset:53248\n"
    "v_mfma_f32_16x16x32_bf16 v[64:67], a[164:167], v[236:239], v[64:67]\n"
    "ds_read_b64_tr_b16 a[162:163], v244 offset:55296\n"
    "v_mfma_f32_16x16x32_bf16 v[76:79], a[168:171], v[232:235], v[76:79]\n"
    "ds_read_b64_tr_b16 a[164:165], v244 offset:61440\n"
    "v_mfma_f32_16x16x32_bf16 v[76:79], a[172:175], v[236:239], v[76:79]\n"
    "ds_read_b64_tr_b16 a[166:167], v244 offset:63488\n"
    "v_mfma_f32_16x16x32_bf16 v[72:75], a[172:175], v[228:231], v[72:75]\n"
    "v_exp_f32_e32 v196, v196\n"
    "v_mfma_f32_16x16x32_bf16 v[72:75], a[168:171], v[224:227], v[72:75]\n"
    "v_exp_f32_e32 v197, v197\n"
    "v_mfma_f32_16x16x32_bf16 v[68:71], a[168:171], v[216:219], v[68:71]\n"
    "v_exp_f32_e32 v198, v198\n"
    "v_mfma_f32_16x16x32_bf16 v[68:71], a[172:175], v[220:223], v[68:71]\n"
    "v_exp_f32_e32 v199, v199\n"
    "v_mfma_f32_16x16x32_bf16 a[252:255], a[172:175], v[212:215], a[252:255]\n"
    "v_cvt_pk_bf16_f32 v194, v196, v197\n"
    "v_mfma_f32_16x16x32_bf16 a[252:255], a[168:171], v[208:211], a[252:255]\n"
    "v_exp_f32_e32 v188, v188\n"
    "v_mfma_f32_16x16x32_bf16 v[4:7], a[168:171], v[200:203], v[4:7]\n"
    "ds_read_b64_tr_b16 a[168:169], v245 offset:53248\n"
    "v_mfma_f32_16x16x32_bf16 v[4:7], a[172:175], v[204:207], v[4:7]\n"
    "s_waitcnt lgkmcnt(5)\n"
    "ds_read_b64_tr_b16 a[170:171], v245 offset:55296\n"
    "v_mfma_f32_16x16x32_bf16 v[8:11], a[176:179], v[200:203], v[8:11]\n"
    "ds_read_b64_tr_b16 a[172:173], v245 offset:61440\n"
    "v_mfma_f32_16x16x32_bf16 v[8:11], a[180:183], v[204:207], v[8:11]\n"
    "ds_read_b64_tr_b16 a[174:175], v245 offset:63488\n"
    "v_mfma_f32_16x16x32_bf16 v[32:35], a[180:183], v[212:215], v[32:35]\n"
    "v_exp_f32_e32 v189, v189\n"
    "v_mfma_f32_16x16x32_bf16 v[32:35], a[176:179], v[208:211], v[32:35]\n"
    "v_cvt_pk_bf16_f32 v195, v198, v199\n"
    "v_mfma_f32_16x16x32_bf16 v[80:83], a[176:179], v[216:219], v[80:83]\n"
    "v_exp_f32_e32 v190, v190\n"
    "v_mfma_f32_16x16x32_bf16 v[80:83], a[180:183], v[220:223], v[80:83]\n"
    "v_exp_f32_e32 v191, v191\n"
    "v_mfma_f32_16x16x32_bf16 v[84:87], a[180:183], v[228:231], v[84:87]\n"
    "v_cvt_pk_bf16_f32 v186, v188, v189\n"
    "v_mfma_f32_16x16x32_bf16 v[84:87], a[176:179], v[224:227], v[84:87]\n"
    "v_exp_f32_e32 v180, v180\n"
    "v_mfma_f32_16x16x32_bf16 v[88:91], a[176:179], v[232:235], v[88:91]\n"
    "ds_read_b64_tr_b16 a[176:177], v246 offset:53248\n"
    "v_mfma_f32_16x16x32_bf16 v[88:91], a[180:183], v[236:239], v[88:91]\n"
    "ds_read_b64_tr_b16 a[178:179], v246 offset:55296\n"
    "v_mfma_f32_16x16x32_bf16 v[100:103], a[184:187], v[232:235], v[100:103]\n"
    "ds_read_b64_tr_b16 a[180:181], v246 offset:61440\n"
    "v_mfma_f32_16x16x32_bf16 v[100:103], a[188:191], v[236:239], v[100:103]\n"
    "ds_read_b64_tr_b16 a[182:183], v246 offset:63488\n"
    "v_mfma_f32_16x16x32_bf16 v[96:99], a[188:191], v[228:231], v[96:99]\n"
    "v_exp_f32_e32 v181, v181\n"
    "v_mfma_f32_16x16x32_bf16 v[96:99], a[184:187], v[224:227], v[96:99]\n"
    "v_cvt_pk_bf16_f32 v187, v190, v191\n"
    "v_mfma_f32_16x16x32_bf16 v[92:95], a[184:187], v[216:219], v[92:95]\n"
    "v_exp_f32_e32 v182, v182\n"
    "v_mfma_f32_16x16x32_bf16 v[92:95], a[188:191], v[220:223], v[92:95]\n"
    "v_exp_f32_e32 v183, v183\n"
    "v_mfma_f32_16x16x32_bf16 v[36:39], a[188:191], v[212:215], v[36:39]\n"
    "v_cvt_pk_bf16_f32 v178, v180, v181\n"
    "v_mfma_f32_16x16x32_bf16 v[36:39], a[184:187], v[208:211], v[36:39]\n"
    "v_exp_f32_e32 v172, v172\n"
    "v_mfma_f32_16x16x32_bf16 v[12:15], a[184:187], v[200:203], v[12:15]\n"
    "s_mov_b32 m0, s49\n"
    "ds_read_b64_tr_b16 a[184:185], v247 offset:53248\n"
    "v_mfma_f32_16x16x32_bf16 v[12:15], a[188:191], v[204:207], v[12:15]\n"
    "s_waitcnt lgkmcnt(5)\n"
    "ds_read_b64_tr_b16 a[186:187], v247 offset:55296\n"
    "v_mfma_f32_16x16x32_bf16 v[16:19], a[160:163], v[200:203], v[16:19]\n"
    "ds_read_b64_tr_b16 a[188:189], v247 offset:61440\n"
    "v_mfma_f32_16x16x32_bf16 v[16:19], a[164:167], v[204:207], v[16:19]\n"
    "ds_read_b64_tr_b16 a[190:191], v247 offset:63488\n"
    "v_mfma_f32_16x16x32_bf16 v[40:43], a[164:167], v[212:215], v[40:43]\n"
    "v_exp_f32_e32 v173, v173\n"
    "v_mfma_f32_16x16x32_bf16 v[40:43], a[160:163], v[208:211], v[40:43]\n"
    "v_cvt_pk_bf16_f32 v179, v182, v183\n"
    "v_mfma_f32_16x16x32_bf16 v[104:107], a[160:163], v[216:219], v[104:107]\n"
    "v_exp_f32_e32 v174, v174\n"
    "v_mfma_f32_16x16x32_bf16 v[104:107], a[164:167], v[220:223], v[104:107]\n"
    "s_waitcnt vmcnt(9)\n"
    "v_mfma_f32_16x16x32_bf16 v[108:111], a[164:167], v[228:231], v[108:111]\n"
    "s_barrier\n"
    "v_mfma_f32_16x16x32_bf16 v[108:111], a[160:163], v[224:227], v[108:111]\n"
    "v_exp_f32_e32 v175, v175\n"
    "v_mfma_f32_16x16x32_bf16 v[112:115], a[160:163], v[232:235], v[112:115]\n"
    "ds_read_b128 a[160:163], v243 offset:20480\n"
    "v_mfma_f32_16x16x32_bf16 v[112:115], a[164:167], v[236:239], v[112:115]\n"
    "ds_read_b128 a[164:167], v240 offset:16384\n"
    "v_mfma_f32_16x16x32_bf16 v[124:127], a[168:171], v[232:235], v[124:127]\n"
    "ds_write_addtid_b32 v192 offset:16384\n"
    "v_cvt_pk_bf16_f32 v170, v172, v173\n"
    "v_mfma_f32_16x16x32_bf16 v[124:127], a[172:175], v[236:239], v[124:127]\n"
    "v_exp_f32_e32 v164, v164\n"
    "v_mfma_f32_16x16x32_bf16 v[120:123], a[172:175], v[228:231], v[120:123]\n"
    "v_exp_f32_e32 v165, v165\n"
    "v_mfma_f32_16x16x32_bf16 v[120:123], a[168:171], v[224:227], v[120:123]\n"
    "ds_write_addtid_b32 v193 offset:16640\n"
    "v_cvt_pk_bf16_f32 v171, v174, v175\n"
    "v_mfma_f32_16x16x32_bf16 v[116:119], a[168:171], v[216:219], v[116:119]\n"
    "v_exp_f32_e32 v166, v166\n"
    "v_mfma_f32_16x16x32_bf16 v[116:119], a[172:175], v[220:223], v[116:119]\n"
    "v_exp_f32_e32 v167, v167\n"
    "v_mfma_f32_16x16x32_bf16 v[44:47], a[172:175], v[212:215], v[44:47]\n"
    "ds_write_addtid_b32 v184 offset:12288\n"
    "v_cvt_pk_bf16_f32 v162, v164, v165\n"
    "v_mfma_f32_16x16x32_bf16 v[44:47], a[168:171], v[208:211], v[44:47]\n"
    "ds_write_addtid_b32 v185 offset:12544\n"
    "v_cvt_pk_bf16_f32 v163, v166, v167\n"
    "v_mfma_f32_16x16x32_bf16 v[20:23], a[168:171], v[200:203], v[20:23]\n"
    "ds_read_b128 a[168:171], v241 offset:16384\n"
    "v_mfma_f32_16x16x32_bf16 v[20:23], a[172:175], v[204:207], v[20:23]\n"
    "s_waitcnt lgkmcnt(7)\n"
    "ds_read_b128 a[172:175], v240 offset:20480\n"
    "v_mfma_f32_16x16x32_bf16 v[24:27], a[176:179], v[200:203], v[24:27]\n"
    "ds_write_addtid_b32 v176 offset:8192\n"
    "v_mfma_f32_16x16x32_bf16 v[24:27], a[180:183], v[204:207], v[24:27]\n"
    "ds_write_addtid_b32 v177 offset:8448\n"
    "v_mfma_f32_16x16x32_bf16 v[48:51], a[180:183], v[212:215], v[48:51]\n"
    "ds_write_addtid_b32 v168 offset:4096\n"
    "v_mfma_f32_16x16x32_bf16 v[48:51], a[176:179], v[208:211], v[48:51]\n"
    "ds_write_addtid_b32 v169 offset:4352\n"
    "v_mfma_f32_16x16x32_bf16 v[128:131], a[176:179], v[216:219], v[128:131]\n"
    "ds_write_addtid_b32 v160 offset:0\n"
    "v_mfma_f32_16x16x32_bf16 v[128:131], a[180:183], v[220:223], v[128:131]\n"
    "ds_write_addtid_b32 v161 offset:256\n"
    "v_mfma_f32_16x16x32_bf16 v[132:135], a[180:183], v[228:231], v[132:135]\n"
    "ds_write_addtid_b32 v194 offset:16896\n"
    "v_mfma_f32_16x16x32_bf16 v[132:135], a[176:179], v[224:227], v[132:135]\n"
    "ds_write_addtid_b32 v195 offset:17152\n"
    "v_mfma_f32_16x16x32_bf16 v[136:139], a[176:179], v[232:235], v[136:139]\n"
    "ds_read_b128 a[176:179], v241 offset:20480\n"
    "v_mfma_f32_16x16x32_bf16 v[136:139], a[180:183], v[236:239], v[136:139]\n"
    "ds_read_b128 a[180:183], v242 offset:16384\n"
    "v_mfma_f32_16x16x32_bf16 v[148:151], a[184:187], v[232:235], v[148:151]\n"
    "ds_write_addtid_b32 v186 offset:12800\n"
    "v_mfma_f32_16x16x32_bf16 v[148:151], a[188:191], v[236:239], v[148:151]\n"
    "ds_write_addtid_b32 v187 offset:13056\n"
    "v_mfma_f32_16x16x32_bf16 v[144:147], a[188:191], v[228:231], v[144:147]\n"
    "ds_write_addtid_b32 v178 offset:8704\n"
    "v_mfma_f32_16x16x32_bf16 v[144:147], a[184:187], v[224:227], v[144:147]\n"
    "ds_write_addtid_b32 v179 offset:8960\n"
    "v_mfma_f32_16x16x32_bf16 v[140:143], a[184:187], v[216:219], v[140:143]\n"
    "s_sub_u32 s44, s44, 1\n"
    "ds_write_addtid_b32 v170 offset:4608\n"
    "v_mfma_f32_16x16x32_bf16 v[140:143], a[188:191], v[220:223], v[140:143]\n"
    "ds_write_addtid_b32 v171 offset:4864\n"
    "v_mfma_f32_16x16x32_bf16 v[52:55], a[188:191], v[212:215], v[52:55]\n"
    "ds_write_addtid_b32 v162 offset:512\n"
    "v_mfma_f32_16x16x32_bf16 v[52:55], a[184:187], v[208:211], v[52:55]\n"
    "ds_write_addtid_b32 v163 offset:768\n"
    "v_mfma_f32_16x16x32_bf16 v[28:31], a[184:187], v[200:203], v[28:31]\n"
    "ds_read_b128 a[184:187], v243 offset:16384\n"
    "v_mfma_f32_16x16x32_bf16 v[28:31], a[188:191], v[204:207], v[28:31]\n"
    "s_waitcnt lgkmcnt(15)\n"
    "ds_read_b128 a[188:191], v242 offset:20480\n"
    "s_cbranch_scc0 .Lat_p\n"
    "s_cmp_lg_u32 s37, 0\n"
    "s_cbranch_scc1 .Lat_tail\n"
    "s_waitcnt vmcnt(0)\n"
    "v_cmp_ne_u32_e32 vcc, s39, v154\n"
    "s_mov_b32 s37, 1\n"
    "s_bitcmp1_b32 %1, 0\n"
    "s_cselect_b32 s44, 5, 3\n"
    "s_nop 1\n"
    "s_cbranch_vccnz .Lat_lpoll\n"
    ".p2align 4\n"
    ".Lat_g:\n"
    "v_mfma_f32_16x16x32_bf16 v[200:203], a[160:163], a[28:31], 0\n"
    "v_mfma_f32_16x16x32_bf16 v[200:203], a[164:167], a[0:3], v[200:203]\n"
    "v_mfma_f32_16x16x32_bf16 v[208:211], a[164:167], a[32:35], 0\n"
    "v_mfma_f32_16x16x32_bf16 v[208:211], a[160:163], a[60:63], v[208:211]\n"
    "v_mfma_f32_16x16x32_bf16 v[216:219], a[160:163], a[92:95], 0\n"
    "v_mfma_f32_16x16x32_bf16 v[216:219], a[164:167], a[64:67], v[216:219]\n"
    "s_waitcnt lgkmcnt(0)\n"
    "v_mfma_f32_16x16x32_bf16 v[224:227], a[164:167], a[96:99], 0\n"
    "s_barrier\n"
    "v_mfma_f32_16x16x32_bf16 v[224:227], a[160:163], a[124:127], v[224:227]\n"
    "s_add_u32 m0, s43, 49152\n"
    "ds_read2st64_b32 v[164:165], v250 offset0:0 offset1:1\n"
    "v_mfma_f32_16x16x32_bf16 v[232:235], a[160:163], a[156:159], 0\n"
    "ds_read_b128 a[160:163], v243 offset:22528\n"
    "v_mfma_f32_16x16x32_bf16 v[232:235], a[164:167], a[128:131], v[232:235]\n"
    "ds_read_b128 a[164:167], v240 offset:18432\n"
    "v_mfma_f32_16x16x32_bf16 v[232:235], a[168:171], a[132:135], v[232:235]\n"
    "buffer_load_dwordx4 v248, s[8:11], s40 offen offset:0 sc1 lds\n"
    "v_mfma_f32_16x16x32_bf16 v[232:235], a[172:175], a[136:139], v[232:235]\n"
    "v_mfma_f32_16x16x32_bf16 v[224:227], a[172:175], a[104:107], v[224:227]\n"
    "v_mfma_f32_16x16x32_bf16 v[224:227], a[168:171], a[100:103], v[224:227]\n"
    "ds_read2st64_b32 v[166:167], v250 offset0:2 offset1:3\n"
    "v_mfma_f32_16x16x32_bf16 v[216:219], a[168:171], a[68:71], v[216:219]\n"
    "buffer_load_dwordx4 v248, s[8:11], s40 offen offset:1024 sc1 lds\n"
    "v_mfma_f32_16x16x32_bf16 v[216:219], a[172:175], a[72:75], v[216:219]\n"
    "v_mfma_f32_16x16x32_bf16 v[208:211], a[172:175], a[40:43], v[208:211]\n"
    "v_mfma_f32_16x16x32_bf16 v[208:211], a[168:171], a[36:39], v[208:211]\n"
    "ds_read2st64_b32 v[172:173], v250 offset0:16 offset1:17\n"
    "v_mfma_f32_16x16x32_bf16 v[200:203], a[168:171], a[4:7], v[200:203]\n"
    "ds_read_b128 a[168:171], v241 offset:18432\n"
    "buffer_load_dwordx4 v248, s[8:11], s40 offen offset:2048 sc1 lds\n"
    "v_mfma_f32_16x16x32_bf16 v[200:203], a[172:175], a[8:11], v[200:203]\n"
    "ds_read_b128 a[172:175], v240 offset:22528\n"
    "v_mfma_f32_16x16x32_bf16 v[200:203], a[176:179], a[12:15], v[200:203]\n"
    "ds_read2st64_b32 v[174:175], v250 offset0:18 offset1:19\n"
    "v_mfma_f32_16x16x32_bf16 v[200:203], a[180:183], a[16:19], v[200:203]\n"
    "v_mfma_f32_16x16x32_bf16 v[208:211], a[180:183], a[48:51], v[208:211]\n"
    "buffer_load_dwordx4 v248, s[8:11], s40 offen offset:3072 sc1 lds\n"
    "v_mfma_f32_16x16x32_bf16 v[208:211], a[176:179], a[44:47], v[208:211]\n"
    "s_add_u32 m0, s43, 53248\n"
    "v_mfma_f32_16x16x32_bf16 v[216:219], a[176:179], a[76:79], v[216:219]\n"
    "s_add_u32 s40, s40, 0x8000\n"
    "ds_read2st64_b32 v[180:181], v250 offset0:32 offset1:33\n"
    "v_mfma_f32_16x16x32_bf16 v[216:219], a[180:183], a[80:83], v[216:219]\n"
    "v_mfma_f32_16x16x32_bf16 v[224:227], a[180:183], a[112:115], v[224:227]\n"
    "buffer_load_dwordx4 v248, s[8:11], s41 offen offset:0 sc1 lds\n"
    "v_mfma_f32_16x16x32_bf16 v[224:227], a[176:179], a[108:111], v[224:227]\n"
    "ds_read2st64_b32 v[182:183], v250 offset0:34 offset1:35\n"
    "v_mfma_f32_16x16x32_bf16 v[232:235], a[176:179], a[140:143], v[232:235]\n"
    "ds_read_b128 a[176:179], v241 offset:22528\n"
    "v_mfma_f32_16x16x32_bf16 v[232:235], a[180:183], a[144:147], v[232:235]\n"
    "ds_read_b128 a[180:183], v242 offset:18432\n"
    "v_mfma_f32_16x16x32_bf16 v[232:235], a[184:187], a[148:151], v[232:235]\n"
    "buffer_load_dwordx4 v248, s[8:11], s41 offen offset:1024 sc1 lds\n"
    "v_mfma_f32_16x16x32_bf16 v[232:235], a[188:191], a[152:155], v[232:235]\n"
    "v_mfma_f32_16x16x32_bf16 v[224:227], a[188:191], a[120:123], v[224:227]\n"
    "ds_read2st64_b32 v[188:189], v250 offset0:48 offset1:49\n"
    "v_mfma_f32_16x16x32_bf16 v[224:227], a[184:187], a[116:119], v[224:227]\n"
    "v_mfma_f32_16x16x32_bf16 v[216:219], a[184:187], a[84:87], v[216:219]\n"
    "buffer_load_dwordx4 v248, s[8:11], s41 offen offset:2048 sc1 lds\n"
    "v_mfma_f32_16x16x32_bf16 v[216:219], a[188:191], a[88:91], v[216:219]\n"
    "ds_read2st64_b32 v[190:191], v250 offset0:50 offset1:51\n"
    "v_mfma_f32_16x16x32_bf16 v[208:211], a[188:191], a[56:59], v[208:211]\n"
    "ds_read_b64 v[254:255], v252 offset:0\n"
    "v_mfma_f32_16x16x32_bf16 v[208:211], a[184:187], a[52:55], v[208:211]\n"
    "ds_read2st64_b32 v[196:197], v250 offset0:64 offset1:65\n"
    "v_mfma_f32_16x16x32_bf16 v[200:203], a[184:187], a[20:23], v[200:203]\n"
    "ds_read_b128 a[184:187], v243 offset:18432\n"
    "buffer_load_dwordx4 v248, s[8:11], s41 offen offset:3072 sc1 lds\n"
    "v_mfma_f32_16x16x32_bf16 v[200:203], a[188:191], a[24:27], v[200:203]\n"
    "s_waitcnt lgkmcnt(15)\n"
    "s_add_u32 s41, s41, 0x8000\n"
    "ds_read_b128 a[188:191], v242 offset:22528\n"
    "v_mfma_f32_16x16x32_bf16 v[204:207], a[160:163], a[28:31], 0\n"
    "s_waitcnt lgkmcnt(3)\n"
    "v_mad_u32_u16 v253, v254, s42, v251 op_sel:[0,0,0,0]\n"
    "v_mfma_f32_16x16x32_bf16 v[204:207], a[164:167], a[0:3], v[204:207]\n"
    "buffer_load_dwordx4 a[200:203], v253, s[20:23], 0 offen offset:0 sc1\n"
    "v_mfma_f32_16x16x32_bf16 v[212:215], a[164:167], a[32:35], 0\n"
    "v_exp_f32_e32 v232, v232\n"
    "v_mfma_f32_16x16x32_bf16 v[212:215], a[160:163], a[60:63], v[212:215]\n"
    "ds_read2st64_b32 v[198:199], v250 offset0:66 offset1:67\n"
    "v_mfma_f32_16x16x32_bf16 v[220:223], a[160:163], a[92:95], 0\n"
    "v_mad_u32_u16 v253, v254, s42, v251 op_sel:[1,0,0,0]\n"
    "v_mfma_f32_16x16x32_bf16 v[220:223], a[164:167], a[64:67], v[220:223]\n"
    "buffer_load_dwordx4 a[204:207], v253, s[20:23], 0 offen offset:0 sc1\n"
    "v_mfma_f32_16x16x32_bf16 v[228:231], a[164:167], a[96:99], 0\n"
    "v_exp_f32_e32 v233, v233\n"
    "v_mfma_f32_16x16x32_bf16 v[228:231], a[160:163], a[124:127], v[228:231]\n"
    "v_exp_f32_e32 v234, v234\n"
    "v_mfma_f32_16x16x32_bf16 v[236:239], a[160:163], a[156:159], 0\n"
    "ds_read_b64_tr_b16 a[160:161], v244 offset:0\n"
    "v_mad_u32_u16 v253, v255, s42, v251 op_sel:[0,0,0,0]\n"
    "v_mfma_f32_16x16x32_bf16 v[236:239], a[164:167], a[128:131], v[236:239]\n"
    "ds_read_b64_tr_b16 a[162:163], v244 offset:2048\n"
    "buffer_load_dwordx4 a[208:211], v253, s[20:23], 0 offen offset:0 sc1\n"
    "v_mfma_f32_16x16x32_bf16 v[236:239], a[168:171], a[132:135], v[236:239]\n"
    "ds_read_b64_tr_b16 a[164:165], v244 offset:8192\n"
    "v_mfma_f32_16x16x32_bf16 v[236:239], a[172:175], a[136:139], v[236:239]\n"
    "ds_read_b64_tr_b16 a[166:167], v244 offset:10240\n"
    "v_mfma_f32_16x16x32_bf16 v[228:231], a[172:175], a[104:107], v[228:231]\n"
    "v_mad_u32_u16 v253, v255, s42, v251 op_sel:[1,0,0,0]\n"
    "v_mfma_f32_16x16x32_bf16 v[228:231], a[168:171], a[100:103], v[228:231]\n"
    "buffer_load_dwordx4 a[212:215], v253, s[20:23], 0 offen offset:0 sc1\n"
    "ds_read_b64 v[254:255], v252 offset:8\n"
    "v_mfma_f32_16x16x32_bf16 v[220:223], a[168:171], a[68:71], v[220:223]\n"
    "v_exp_f32_e32 v235, v235\n"
    "v_mfma_f32_16x16x32_bf16 v[220:223], a[172:175], a[72:75], v[220:223]\n"
    "v_cvt_pk_bf16_f32 v232, v232, v233\n"
    "v_mfma_f32_16x16x32_bf16 v[212:215], a[172:175], a[40:43], v[212:215]\n"
    "v_exp_f32_e32 v224, v224\n"
    "v_mfma_f32_16x16x32_bf16 v[212:215], a[168:171], a[36:39], v[212:215]\n"
    "v_exp_f32_e32 v225, v225\n"
    "v_mfma_f32_16x16x32_bf16 v[204:207], a[168:171], a[4:7], v[204:207]\n"
    "ds_read_b64_tr_b16 a[168:169], v245 offset:0\n"
    "v_cvt_pk_bf16_f32 v233, v234, v235\n"
    "v_mfma_f32_16x16x32_bf16 v[204:207], a[172:175], a[8:11], v[204:207]\n"
    "ds_read_b64_tr_b16 a[170:171], v245 offset:2048\n"
    "v_mfma_f32_16x16x32_bf16 v[204:207], a[176:179], a[12:15], v[204:207]\n"
    "s_waitcnt lgkmcnt(2)\n"
    "ds_read_b64_tr_b16 a[172:173], v245 offset:8192\n"
    "v_mad_u32_u16 v253, v254, s42, v251 op_sel:[0,0,0,0]\n"
    "v_mfma_f32_16x16x32_bf16 v[204:207], a[180:183], a[16:19], v[204:207]\n"
    "ds_read_b64_tr_b16 a[174:175], v245 offset:10240\n"
    "buffer_load_dwordx4 a[216:219], v253, s[20:23], 0 offen offset:0 sc1\n"
    "v_mfma_f32_16x16x32_bf16 v[212:215], a[180:183], a[48:51], v[212:215]\n"
    "v_exp_f32_e32 v226, v226\n"
    "v_mfma_f32_16x16x32_bf16 v[212:215], a[176:179], a[44:47], v[212:215]\n"
    "v_exp_f32_e32 v227, v227\n"
    "v_mfma_f32_16x16x32_bf16 v[220:223], a[176:179], a[76:79], v[220:223]\n"
    "v_mad_u32_u16 v253, v254, s42, v251 op_sel:[1,0,0,0]\n"
    "v_cvt_pk_bf16_f32 v224, v224, v225\n"
    "v_mfma_f32_16x16x32_bf16 v[220:223], a[180:183], a[80:83], v[220:223]\n"
    "buffer_load_dwordx4 a[220:223], v253, s[20:23], 0 offen offset:0 sc1\n"
    "v_mfma_f32_16x16x32_bf16 v[228:231], a[180:183], a[112:115], v[228:231]\n"
    "v_exp_f32_e32 v216, v216\n"
    "v_mfma_f32_16x16x32_bf16 v[228:231], a[176:179], a[108:111], v[228:231]\n"
    "v_exp_f32_e32 v217, v217\n"
    "v_mfma_f32_16x16x32_bf16 v[236:239], a[176:179], a[140:143], v[236:239]\n"
    "ds_read_b64_tr_b16 a[176:177], v246 offset:0\n"
    "v_cvt_pk_bf16_f32 v225, v226, v227\n"
    "v_mfma_f32_16x16x32_bf16 v[236:239], a[180:183], a[144:147], v[236:239]\n"
    "ds_read_b64_tr_b16 a[178:179], v246 offset:2048\n"
    "v_mfma_f32_16x16x32_bf16 v[236:239], a[184:187], a[148:151], v[236:239]\n"
    "ds_read_b64_tr_b16 a[180:181], v246 offset:8192\n"
    "v_mfma_f32_16x16x32_bf16 v[236:239], a[188:191], a[152:155], v[236:239]\n"
    "ds_read_b64_tr_b16 a[182:183], v246 offset:10240\n"
    "v_mfma_f32_16x16x32_bf16 v[228:231], a[188:191], a[120:123], v[228:231]\n"
    "ds_read_b64 v[254:255], v252 offset:32\n"
    "v_mfma_f32_16x16x32_bf16 v[228:231], a[184:187], a[116:119], v[228:231]\n"
    "v_exp_f32_e32 v218, v218\n"
    "v_mfma_f32_16x16x32_bf16 v[220:223], a[184:187], a[84:87], v[220:223]\n"
    "v_exp_f32_e32 v219, v219\n"
    "v_mfma_f32_16x16x32_bf16 v[220:223], a[188:191], a[88:91], v[220:223]\n"
    "v_cvt_pk_bf16_f32 v216, v216, v217\n"
    "v_mfma_f32_16x16x32_bf16 v[212:215], a[188:191], a[56:59], v[212:215]\n"
    "s_waitcnt lgkmcnt(0)\n"
    "v_mad_u32_u16 v253, v254, s42, v251 op_sel:[0,0,0,0]\n"
    "v_mfma_f32_16x16x32_bf16 v[212:215], a[184:187], a[52:55], v[212:215]\n"
    "buffer_load_dwordx4 a[224:227], v253, s[20:23], 0 offen offset:0 sc1\n"
    "v_mfma_f32_16x16x32_bf16 v[204:207], a[184:187], a[20:23], v[204:207]\n"
    "ds_read_b64_tr_b16 a[184:185], v247 offset:0\n"
    "v_mfma_f32_16x16x32_bf16 v[204:207], a[188:191], a[24:27], v[204:207]\n"
    "ds_read_b64_tr_b16 a[186:187], v247 offset:2048\n"
    "v_mfma_f32_16x16x32_bf16 v[0:3], a[160:163], v[160:163], v[0:3]\n"
    "ds_read_b64_tr_b16 a[188:189], v247 offset:8192\n"
    "v_mad_u32_u16 v253, v254, s42, v251 op_sel:[1,0,0,0]\n"
    "v_mfma_f32_16x16x32_bf16 v[0:3], a[164:167], v[164:167], v[0:3]\n"
    "ds_read_b64_tr_b16 a[190:191], v247 offset:10240\n"
    "buffer_load_dwordx4 a[228:231], v253, s[20:23], 0 offen offset:0 sc1\n"
    "v_mfma_f32_16x16x32_bf16 a[248:251], a[164:167], v[172:175], a[248:251]\n"
    "v_exp_f32_e32 v208, v208\n"
    "v_mfma_f32_16x16x32_bf16 a[248:251], a[160:163], v[168:171], a[248:251]\n"
    "v_exp_f32_e32 v209, v209\n"
    "v_mfma_f32_16x16x32_bf16 v[56:59], a[160:163], v[176:179], v[56:59]\n"
    "v_mad_u32_u16 v253, v255, s42, v251 op_sel:[0,0,0,0]\n"
    "v_cvt_pk_bf16_f32 v217, v218, v219\n"
    "v_mfma_f32_16x16x32_bf16 v[56:59], a[164:167], v[180:183], v[56:59]\n"
    "buffer_load_dwordx4 a[232:235], v253, s[20:23], 0 offen offset:0 sc1\n"
    "v_mfma_f32_16x16x32_bf16 v[60:63], a[164:167], v[188:191], v[60:63]\n"
    "v_exp_f32_e32 v210, v210\n"
    "v_mfma_f32_16x16x32_bf16 v[60:63], a[160:163], v[184:187], v[60:63]\n"
    "v_exp_f32_e32 v211, v211\n"
    "v_mfma_f32_16x16x32_bf16 v[64:67], a[160:163], v[192:195], v[64:67]\n"
    "ds_read_b64_tr_b16 a[160:161], v244 offset:4096\n"
    "v_mad_u32_u16 v253, v255, s42, v251 op_sel:[1,0,0,0]\n"
    "v_mfma_f32_16x16x32_bf16 v[64:67], a[164:167], v[196:199], v[64:67]\n"
    "ds_read_b64_tr_b16 a[162:163], v244 offset:6144\n"
    "buffer_load_dwordx4 a[236:239], v253, s[20:23], 0 offen offset:0 sc1\n"
    "v_mfma_f32_16x16x32_bf16 v[76:79], a[168:171], v[192:195], v[76:79]\n"
    "ds_read_b64_tr_b16 a[164:165], v244 offset:12288\n"
    "v_cvt_pk_bf16_f32 v208, v208, v209\n"
    "v_mfma_f32_16x16x32_bf16 v[76:79], a[172:175], v[196:199], v[76:79]\n"
    "ds_read_b64_tr_b16 a[166:167], v244 offset:14336\n"
    "v_mfma_f32_16x16x32_bf16 v[72:75], a[172:175], v[188:191], v[72:75]\n"
    "ds_read_b64 v[254:255], v252 offset:40\n"
    "v_mfma_f32_16x16x32_bf16 v[72:75], a[168:171], v[184:187], v[72:75]\n"
    "v_exp_f32_e32 v200, v200\n"
    "v_mfma_f32_16x16x32_bf16 v[68:71], a[168:171], v[176:179], v[68:71]\n"
    "v_exp_f32_e32 v201, v201\n"
    "v_mfma_f32_16x16x32_bf16 v[68:71], a[172:175], v[180:183], v[68:71]\n"
    "v_cvt_pk_bf16_f32 v209, v210, v211\n"
    "v_mfma_f32_16x16x32_bf16 a[252:255], a[172:175], v[172:175], a[252:255]\n"
    "v_exp_f32_e32 v202, v202\n"
    "v_mfma_f32_16x16x32_bf16 a[252:255], a[168:171], v[168:171], a[252:255]\n"
    "v_exp_f32_e32 v203, v203\n"
    "v_mfma_f32_16x16x32_bf16 v[4:7], a[168:171], v[160:163], v[4:7]\n"
    "s_waitcnt lgkmcnt(0)\n"
    "ds_read_b64_tr_b16 a[168:169], v245 offset:4096\n"
    "v_mad_u32_u16 v253, v254, s42, v251 op_sel:[0,0,0,0]\n"
    "v_mfma_f32_16x16x32_bf16 v[4:7], a[172:175], v[164:167], v[4:7]\n"
    "ds_read_b64_tr_b16 a[170:171], v245 offset:6144\n"
    "v_cvt_pk_bf16_f32 v200, v200, v201\n"
    "v_mfma_f32_16x16x32_bf16 v[8:11], a[176:179], v[160:163], v[8:11]\n"
    "ds_read_b64_tr_b16 a[172:173], v245 offset:12288\n"
    "buffer_load_dwordx4 a[240:243], v253, s[20:23], 0 offen offset:0 sc1\n"
    "v_mfma_f32_16x16x32_bf16 v[8:11], a[180:183], v[164:167], v[8:11]\n"
    "ds_read_b64_tr_b16 a[174:175], v245 offset:14336\n"
    "v_cvt_pk_bf16_f32 v201, v202, v203\n"
    "v_mfma_f32_16x16x32_bf16 v[32:35], a[180:183], v[172:175], v[32:35]\n"
    "v_exp_f32_e32 v236, v236\n"
    "v_mfma_f32_16x16x32_bf16 v[32:35], a[176:179], v[168:171], v[32:35]\n"
    "v_mad_u32_u16 v253, v254, s42, v251 op_sel:[1,0,0,0]\n"
    "v_mfma_f32_16x16x32_bf16 v[80:83], a[176:179], v[176:179], v[80:83]\n"
    "buffer_load_dwordx4 a[244:247], v253, s[20:23], 0 offen offset:0 sc1\n"
    "v_mfma_f32_16x16x32_bf16 v[80:83], a[180:183], v[180:183], v[80:83]\n"
    "v_exp_f32_e32 v237, v237\n"
    "v_mfma_f32_16x16x32_bf16 v[84:87], a[180:183], v[188:191], v[84:87]\n"
    "v_exp_f32_e32 v238, v238\n"
    "v_mfma_f32_16x16x32_bf16 v[84:87], a[176:179], v[184:187], v[84:87]\n"
    "v_exp_f32_e32 v239, v239\n"
    "v_mfma_f32_16x16x32_bf16 v[88:91], a[176:179], v[192:195], v[88:91]\n"
    "ds_read_b64_tr_b16 a[176:177], v246 offset:4096\n"
    "v_cvt_pk_bf16_f32 v234, v236, v237\n"
    "v_mfma_f32_16x16x32_bf16 v[88:91], a[180:183], v[196:199], v[88:91]\n"
    "ds_read_b64_tr_b16 a[178:179], v246 offset:6144\n"
    "v_exp_f32_e32 v228, v228\n"
    "v_mfma_f32_16x16x32_bf16 v[100:103], a[184:187], v[192:195], v[100:103]\n"
    "ds_read_b64_tr_b16 a[180:181], v246 offset:12288\n"
    "v_exp_f32_e32 v229, v229\n"
    "v_mfma_f32_16x16x32_bf16 v[100:103], a[188:191], v[196:199], v[100:103]\n"
    "ds_read_b64_tr_b16 a[182:183], v246 offset:14336\n"
    "v_cvt_pk_bf16_f32 v235, v238, v239\n"
    "v_mfma_f32_16x16x32_bf16 v[96:99], a[188:191], v[188:191], v[96:99]\n"
    "v_exp_f32_e32 v230, v230\n"
    "v_mfma_f32_16x16x32_bf16 v[96:99], a[184:187], v[184:187], v[96:99]\n"
    "v_exp_f32_e32 v231, v231\n"
    "v_cvt_pk_bf16_f32 v226, v228, v229\n"
    "v_mfma_f32_16x16x32_bf16 v[92:95], a[184:187], v[176:179], v[92:95]\n"
    "v_exp_f32_e32 v220, v220\n"
    "v_mfma_f32_16x16x32_bf16 v[92:95], a[188:191], v[180:183], v[92:95]\n"
    "v_exp_f32_e32 v221, v221\n"
    "v_mfma_f32_16x16x32_bf16 v[36:39], a[188:191], v[172:175], v[36:39]\n"
    "v_cvt_pk_bf16_f32 v227, v230, v231\n"
    "v_mfma_f32_16x16x32_bf16 v[36:39], a[184:187], v[168:171], v[36:39]\n"
    "v_exp_f32_e32 v222, v222\n"
    "v_mfma_f32_16x16x32_bf16 v[12:15], a[184:187], v[160:163], v[12:15]\n"
    "s_mov_b32 m0, s49\n"
    "ds_read_b64_tr_b16 a[184:185], v247 offset:4096\n"
    "v_mfma_f32_16x16x32_bf16 v[12:15], a[188:191], v[164:167], v[12:15]\n"
    "s_waitcnt lgkmcnt(5)\n"
    "ds_read_b64_tr_b16 a[186:187], v247 offset:6144\n"
    "v_mfma_f32_16x16x32_bf16 v[16:19], a[160:163], v[160:163], v[16:19]\n"
    "ds_read_b64_tr_b16 a[188:189], v247 offset:12288\n"
    "v_mfma_f32_16x16x32_bf16 v[16:19], a[164:167], v[164:167], v[16:19]\n"
    "ds_read_b64_tr_b16 a[190:191], v247 offset:14336\n"
    "v_mfma_f32_16x16x32_bf16 v[40:43], a[164:167], v[172:175], v[40:43]\n"
    "v_exp_f32_e32 v223, v223\n"
    "v_mfma_f32_16x16x32_bf16 v[40:43], a[160:163], v[168:171], v[40:43]\n"
    "v_cvt_pk_bf16_f32 v218, v220, v221\n"
    "v_mfma_f32_16x16x32_bf16 v[104:107], a[160:163], v[176:179], v[104:107]\n"
    "v_exp_f32_e32 v212, v212\n"
    "v_mfma_f32_16x16x32_bf16 v[104:107], a[164:167], v[180:183], v[104:107]\n"
    "s_waitcnt vmcnt(20)\n"
    "v_mfma_f32_16x16x32_bf16 v[108:111], a[164:167], v[188:191], v[108:111]\n"
    "s_barrier\n"
    "v_mfma_f32_16x16x32_bf16 v[108:111], a[160:163], v[184:187], v[108:111]\n"
    "v_exp_f32_e32 v213, v213\n"
    "v_mfma_f32_16x16x32_bf16 v[112:115], a[160:163], v[192:195], v[112:115]\n"
    "ds_read_b128 a[160:163], v243 offset:36864\n"
    "v_cvt_pk_bf16_f32 v219, v222, v223\n"
    "v_mfma_f32_16x16x32_bf16 v[112:115], a[164:167], v[196:199], v[112:115]\n"
    "ds_read_b128 a[164:167], v240 offset:32768\n"
    "ds_write_addtid_b32 v232 offset:16384\n"
    "v_mfma_f32_16x16x32_bf16 v[124:127], a[168:171], v[192:195], v[124:127]\n"
    "v_exp_f32_e32 v214, v214\n"
    "v_mfma_f32_16x16x32_bf16 v[124:127], a[172:175], v[196:199], v[124:127]\n"
    "v_exp_f32_e32 v215, v215\n"
    "v_mfma_f32_16x16x32_bf16 v[120:123], a[172:175], v[188:191], v[120:123]\n"
    "ds_write_addtid_b32 v233 offset:16640\n"
    "v_cvt_pk_bf16_f32 v210, v212, v213\n"
    "v_mfma_f32_16x16x32_bf16 v[120:123], a[168:171], v[184:187], v[120:123]\n"
    "v_exp_f32_e32 v204, v204\n"
    "v_mfma_f32_16x16x32_bf16 v[116:119], a[168:171], v[176:179], v[116:119]\n"
    "v_exp_f32_e32 v205, v205\n"
    "v_mfma_f32_16x16x32_bf16 v[116:119], a[172:175], v[180:183], v[116:119]\n"
    "ds_write_addtid_b32 v224 offset:12288\n"
    "v_cvt_pk_bf16_f32 v211, v214, v215\n"
    "v_mfma_f32_16x16x32_bf16 v[44:47], a[172:175], v[172:175], v[44:47]\n"
    "v_exp_f32_e32 v206, v206\n"
    "v_mfma_f32_16x16x32_bf16 v[44:47], a[168:171], v[168:171], v[44:47]\n"
    "v_exp_f32_e32 v207, v207\n"
    "v_mfma_f32_16x16x32_bf16 v[20:23], a[168:171], v[160:163], v[20:23]\n"
    "ds_read_b128 a[168:171], v241 offset:32768\n"
    "v_cvt_pk_bf16_f32 v202, v204, v205\n"
    "v_mfma_f32_16x16x32_bf16 v[20:23], a[172:175], v[164:167], v[20:23]\n"
    "s_waitcnt lgkmcnt(6)\n"
    "ds_read_b128 a[172:175], v240 offset:36864\n"
    "v_cvt_pk_bf16_f32 v203, v206, v207\n"
    "v_mfma_f32_16x16x32_bf16 v[24:27], a[176:179], v[160:163], v[24:27]\n"
    "ds_write_addtid_b32 v225 offset:12544\n"
    "ds_write_addtid_b32 v216 offset:8192\n"
    "v_mfma_f32_16x16x32_bf16 v[24:27], a[180:183], v[164:167], v[24:27]\n"
    "ds_write_addtid_b32 v217 offset:8448\n"
    "v_mfma_f32_16x16x32_bf16 v[48:51], a[180:183], v[172:175], v[48:51]\n"
    "ds_write_addtid_b32 v208 offset:4096\n"
    "v_mfma_f32_16x16x32_bf16 v[48:51], a[176:179], v[168:171], v[48:51]\n"
    "ds_write_addtid_b32 v209 offset:4352\n"
    "v_mfma_f32_16x16x32_bf16 v[128:131], a[176:179], v[176:179], v[128:131]\n"
    "ds_write_addtid_b32 v200 offset:0\n"
    "v_mfma_f32_16x16x32_bf16 v[128:131], a[180:183], v[180:183], v[128:131]\n"
    "ds_write_addtid_b32 v201 offset:256\n"
    "v_mfma_f32_16x16x32_bf16 v[132:135], a[180:183], v[188:191], v[132:135]\n"
    "ds_write_addtid_b32 v234 offset:16896\n"
    "v_mfma_f32_16x16x32_bf16 v[132:135], a[176:179], v[184:187], v[132:135]\n"
    "ds_write_addtid_b32 v235 offset:17152\n"
    "v_mfma_f32_16x16x32_bf16 v[136:139], a[176:179], v[192:195], v[136:139]\n"
    "ds_read_b128 a[176:179], v241 offset:36864\n"
    "v_mfma_f32_16x16x32_bf16 v[136:139], a[180:183], v[196:199], v[136:139]\n"
    "ds_read_b128 a[180:183], v242 offset:32768\n"
    "v_mfma_f32_16x16x32_bf16 v[148:151], a[184:187], v[192:195], v[148:151]\n"
    "ds_write_addtid_b32 v226 offset:12800\n"
    "v_mfma_f32_16x16x32_bf16 v[148:151], a[188:191], v[196:199], v[148:151]\n"
    "ds_write_addtid_b32 v227 offset:13056\n"
    "v_mfma_f32_16x16x32_bf16 v[144:147], a[188:191], v[188:191], v[144:147]\n"
    "ds_write_addtid_b32 v218 offset:8704\n"
    "v_mfma_f32_16x16x32_bf16 v[144:147], a[184:187], v[184:187], v[144:147]\n"
    "ds_write_addtid_b32 v219 offset:8960\n"
    "v_mfma_f32_16x16x32_bf16 v[140:143], a[184:187], v[176:179], v[140:143]\n"
    "ds_write_addtid_b32 v210 offset:4608\n"
    "v_mfma_f32_16x16x32_bf16 v[140:143], a[188:191], v[180:183], v[140:143]\n"
    "ds_write_addtid_b32 v211 offset:4864\n"
    "v_mfma_f32_16x16x32_bf16 v[52:55], a[188:191], v[172:175], v[52:55]\n"
    "ds_write_addtid_b32 v202 offset:512\n"
    "v_mfma_f32_16x16x32_bf16 v[52:55], a[184:187], v[168:171], v[52:55]\n"
    "ds_write_addtid_b32 v203 offset:768\n"
    "v_mfma_f32_16x16x32_bf16 v[28:31], a[184:187], v[160:163], v[28:31]\n"
    "ds_read_b128 a[184:187], v243 offset:32768\n"
    "v_mfma_f32_16x16x32_bf16 v[28:31], a[188:191], v[164:167], v[28:31]\n"
    "s_waitcnt lgkmcnt(15)\n"
    "ds_read_b128 a[188:191], v242 offset:36864\n"
    "v_mfma_f32_16x16x32_bf16 v[160:163], a[160:163], a[28:31], 0\n"
    "v_mfma_f32_16x16x32_bf16 v[160:163], a[164:167], a[0:3], v[160:163]\n"
    "v_mfma_f32_16x16x32_bf16 v[168:171], a[164:167], a[32:35], 0\n"
    "v_mfma_f32_16x16x32_bf16 v[168:171], a[160:163], a[60:63], v[168:171]\n"
    "v_mfma_f32_16x16x32_bf16 v[176:179], a[160:163], a[92:95], 0\n"
    "v_mfma_f32_16x16x32_bf16 v[176:179], a[164:167], a[64:67], v[176:179]\n"
    "s_waitcnt lgkmcnt(0)\n"
    "v_mfma_f32_16x16x32_bf16 v[184:187], a[164:167], a[96:99], 0\n"
    "s_barrier\n"
    "v_mfma_f32_16x16x32_bf16 v[184:187], a[160:163], a[124:127], v[184:187]\n"
    "s_add_u32 m0, s43, 0\n"
    "v_mfma_f32_16x16x32_bf16 v[192:195], a[160:163], a[156:159], 0\n"
    "ds_read_b128 a[160:163], v243 offset:38912\n"
    "v_mfma_f32_16x16x32_bf16 v[192:195], a[164:167], a[128:131], v[192:195]\n"
    "ds_read_b128 a[164:167], v240 offset:34816\n"
    "v_mfma_f32_16x16x32_bf16 v[192:195], a[168:171], a[132:135], v[192:195]\n"
    "buffer_load_dwordx4 v248, s[8:11], s40 offen offset:0 sc1 lds\n"
    "v_mfma_f32_16x16x32_bf16 v[192:195], a[172:175], a[136:139], v[192:195]\n"
    "ds_read2st64_b32 v[204:205], v250 offset0:0 offset1:1\n"
    "v_mfma_f32_16x16x32_bf16 v[184:187], a[172:175], a[104:107], v[184:187]\n"
    "s_waitcnt vmcnt(11)\n"
    "v_smfmac_f32_16x16x64_bf16 v[156:159], a[192:195], a[200:207], v152\n"
    "v_mfma_f32_16x16x32_bf16 v[184:187], a[168:171], a[100:103], v[184:187]\n"
    "ds_read2st64_b32 v[206:207], v250 offset0:2 offset1:3\n"
    "v_mfma_f32_16x16x32_bf16 v[176:179], a[168:171], a[68:71], v[176:179]\n"
    "buffer_load_dwordx4 v248, s[8:11], s40 offen offset:1024 sc1 lds\n"
    "v_mfma_f32_16x16x32_bf16 v[176:179], a[172:175], a[72:75], v[176:179]\n"
    "v_mfma_f32_16x16x32_bf16 v[168:171], a[172:175], a[40:43], v[168:171]\n"
    "s_waitcnt vmcnt(10)\n"
    "v_smfmac_f32_16x16x64_bf16 v[156:159], a[192:195], a[208:215], v152\n"
    "ds_read2st64_b32 v[212:213], v250 offset0:16 offset1:17\n"
    "v_mfma_f32_16x16x32_bf16 v[168:171], a[168:171], a[36:39], v[168:171]\n"
    "v_mfma_f32_16x16x32_bf16 v[160:163], a[168:171], a[4:7], v[160:163]\n"
    "ds_read_b128 a[168:171], v241 offset:34816\n"
    "buffer_load_dwordx4 v248, s[8:11], s40 offen offset:2048 sc1 lds\n"
    "v_mfma_f32_16x16x32_bf16 v[160:163], a[172:175], a[8:11], v[160:163]\n"
    "ds_read_b128 a[172:175], v240 offset:38912\n"
    "v_mfma_f32_16x16x32_bf16 v[160:163], a[176:179], a[12:15], v[160:163]\n"
    "s_waitcnt vmcnt(9)\n"
    "ds_read2st64_b32 v[214:215], v250 offset0:18 offset1:19\n"
    "v_smfmac_f32_16x16x64_bf16 v[156:159], a[192:195], a[216:223], v152\n"
    "v_mfma_f32_16x16x32_bf16 v[160:163], a[180:183], a[16:19], v[160:163]\n"
    "v_mfma_f32_16x16x32_bf16 v[168:171], a[180:183], a[48:51], v[168:171]\n"
    "buffer_load_dwordx4 v248, s[8:11], s40 offen offset:3072 sc1 lds\n"
    "v_mfma_f32_16x16x32_bf16 v[168:171], a[176:179], a[44:47], v[168:171]\n"
    "s_add_u32 m0, s43, 4096\n"
    "v_mfma_f32_16x16x32_bf16 v[176:179], a[176:179], a[76:79], v[176:179]\n"
    "s_add_u32 s40, s40, 0x8000\n"
    "ds_read2st64_b32 v[220:221], v250 offset0:32 offset1:33\n"
    "v_mfma_f32_16x16x32_bf16 v[176:179], a[180:183], a[80:83], v[176:179]\n"
    "v_mfma_f32_16x16x32_bf16 v[184:187], a[180:183], a[112:115], v[184:187]\n"
    "v_mfma_f32_16x16x32_bf16 v[184:187], a[176:179], a[108:111], v[184:187]\n"
    "buffer_load_dwordx4 v248, s[8:11], s41 offen offset:0 sc1 lds\n"
    "v_mfma_f32_16x16x32_bf16 v[192:195], a[176:179], a[140:143], v[192:195]\n"
    "ds_read_b128 a[176:179], v241 offset:38912\n"
    "v_mfma_f32_16x16x32_bf16 v[192:195], a[180:183], a[144:147], v[192:195]\n"
    "ds_read_b128 a[180:183], v242 offset:34816\n"
    "v_mfma_f32_16x16x32_bf16 v[192:195], a[184:187], a[148:151], v[192:195]\n"
    "ds_read2st64_b32 v[222:223], v250 offset0:34 offset1:35\n"
    "v_mfma_f32_16x16x32_bf16 v[192:195], a[188:191], a[152:155], v[192:195]\n"
    "v_mfma_f32_16x16x32_bf16 v[184:187], a[188:191], a[120:123], v[184:187]\n"
    "buffer_load_dwordx4 v248, s[8:11], s41 offen offset:1024 sc1 lds\n"
    "v_mfma_f32_16x16x32_bf16 v[184:187], a[184:187], a[116:119], v[184:187]\n"
    "ds_read2st64_b32 v[228:229], v250 offset0:48 offset1:49\n"
    "v_mfma_f32_16x16x32_bf16 v[176:179], a[184:187], a[84:87], v[176:179]\n"
    "s_waitcnt vmcnt(10)\n"
    "v_exp_f32_e32 v192, v192\n"
    "v_smfmac_f32_16x16x64_bf16 v[156:159], a[192:195], a[224:231], v152\n"
    "v_exp_f32_e32 v193, v193\n"
    "v_mfma_f32_16x16x32_bf16 v[176:179], a[188:191], a[88:91], v[176:179]\n"
    "v_exp_f32_e32 v194, v194\n"
    "v_mfma_f32_16x16x32_bf16 v[168:171], a[188:191], a[56:59], v[168:171]\n"
    "buffer_load_dwordx4 v248, s[8:11], s41 offen offset:2048 sc1 lds\n"
    "v_mfma_f32_16x16x32_bf16 v[168:171], a[184:187], a[52:55], v[168:171]\n"
    "ds_read2st64_b32 v[230:231], v250 offset0:50 offset1:51\n"
    "v_mfma_f32_16x16x32_bf16 v[160:163], a[184:187], a[20:23], v[160:163]\n"
    "s_waitcnt vmcnt(9)\n"
    "ds_read_b128 a[184:187], v243 offset:34816\n"
    "v_smfmac_f32_16x16x64_bf16 v[156:159], a[192:195], a[232:239], v152\n"
    "ds_read2st64_b32 v[236:237], v250 offset0:64 offset1:65\n"
    "v_mfma_f32_16x16x32_bf16 v[160:163], a[188:191], a[24:27], v[160:163]\n"
    "s_waitcnt lgkmcnt(9)\n"
    "ds_read_b128 a[188:191], v242 offset:38912\n"
    "v_mfma_f32_16x16x32_bf16 v[164:167], a[160:163], a[28:31], 0\n"
    "buffer_load_dwordx4 v248, s[8:11], s41 offen offset:3072 sc1 lds\n"
    "v_mfma_f32_16x16x32_bf16 v[164:167], a[164:167], a[0:3], v[164:167]\n"
    "s_add_u32 s41, s41, 0x8000\n"
    "v_exp_f32_e32 v195, v195\n"
    "v_mfma_f32_16x16x32_bf16 v[172:175], a[164:167], a[32:35], 0\n"
    "s_waitcnt vmcnt(8)\n"
    "v_cvt_pk_bf16_f32 v192, v192, v193\n"
    "v_smfmac_f32_16x16x64_bf16 v[156:159], a[192:195], a[240:247], v152\n"
    "v_exp_f32_e32 v184, v184\n"
    "v_mfma_f32_16x16x32_bf16 v[172:175], a[160:163], a[60:63], v[172:175]\n"
    "ds_read2st64_b32 v[238:239], v250 offset0:66 offset1:67\n"
    "v_mfma_f32_16x16x32_bf16 v[180:183], a[160:163], a[92:95], 0\n"
    "v_exp_f32_e32 v185, v185\n"
    "v_mfma_f32_16x16x32_bf16 v[180:183], a[164:167], a[64:67], v[180:183]\n"
    "v_cvt_pk_bf16_f32 v193, v194, v195\n"
    "v_mfma_f32_16x16x32_bf16 v[188:191], a[164:167], a[96:99], 0\n"
    "v_exp_f32_e32 v186, v186\n"
    "v_mfma_f32_16x16x32_bf16 v[188:191], a[160:163], a[124:127], v[188:191]\n"
    "v_exp_f32_e32 v187, v187\n"
    "v_mfma_f32_16x16x32_bf16 v[196:199], a[160:163], a[156:159], 0\n"
    "ds_read_b64_tr_b16 a[160:161], v244 offset:16384\n"
    "v_and_b32_e32 v253, 0xffff0000, v255\n"
    "v_mfma_f32_16x16x32_bf16 v[196:199], a[164:167], a[128:131], v[196:199]\n"
    "ds_read_b64_tr_b16 a[162:163], v244 offset:18432\n"
    "v_mfma_f32_16x16x32_bf16 v[196:199], a[168:171], a[132:135], v[196:199]\n"
    "ds_read_b64_tr_b16 a[164:165], v244 offset:24576\n"
    "v_mfma_f32_16x16x32_bf16 v[196:199], a[172:175], a[136:139], v[196:199]\n"
    "ds_read_b64_tr_b16 a[166:167], v244 offset:26624\n"
    "v_mfma_f32_16x16x32_bf16 v[188:191], a[172:175], a[104:107], v[188:191]\n"
    "v_cvt_pk_bf16_f32 v184, v184, v185\n"
    "v_mfma_f32_16x16x32_bf16 v[188:191], a[168:171], a[100:103], v[188:191]\n"
    "v_exp_f32_e32 v176, v176\n"
    "v_mfma_f32_16x16x32_bf16 v[180:183], a[168:171], a[68:71], v[180:183]\n"
    "v_exp_f32_e32 v177, v177\n"
    "v_mfma_f32_16x16x32_bf16 v[180:183], a[172:175], a[72:75], v[180:183]\n"
    "v_cvt_pk_bf16_f32 v185, v186, v187\n"
    "v_mul_f32_e32 v156, v253, v156\n"
    "v_mfma_f32_16x16x32_bf16 v[172:175], a[172:175], a[40:43], v[172:175]\n"
    "v_exp_f32_e32 v178, v178\n"
    "v_mfma_f32_16x16x32_bf16 v[172:175], a[168:171], a[36:39], v[172:175]\n"
    "v_exp_f32_e32 v179, v179\n"
    "v_mfma_f32_16x16x32_bf16 v[164:167], a[168:171], a[4:7], v[164:167]\n"
    "ds_read_b64_tr_b16 a[168:169], v245 offset:16384\n"
    "v_mfma_f32_16x16x32_bf16 v[164:167], a[172:175], a[8:11], v[164:167]\n"
    "s_waitcnt lgkmcnt(6)\n"
    "ds_read_b64_tr_b16 a[170:171], v245 offset:18432\n"
    "v_mfma_f32_16x16x32_bf16 v[164:167], a[176:179], a[12:15], v[164:167]\n"
    "ds_read_b64_tr_b16 a[172:173], v245 offset:24576\n"
    "v_mfma_f32_16x16x32_bf16 v[164:167], a[180:183], a[16:19], v[164:167]\n"
    "ds_read_b64_tr_b16 a[174:175], v245 offset:26624\n"
    "v_mfma_f32_16x16x32_bf16 v[172:175], a[180:183], a[48:51], v[172:175]\n"
    "v_cvt_pk_bf16_f32 v176, v176, v177\n"
    "v_mfma_f32_16x16x32_bf16 v[172:175], a[176:179], a[44:47], v[172:175]\n"
    "v_exp_f32_e32 v168, v168\n"
    "v_mfma_f32_16x16x32_bf16 v[180:183], a[176:179], a[76:79], v[180:183]\n"
    "v_mul_f32_e32 v157, v253, v157\n"
    "v_mfma_f32_16x16x32_bf16 v[180:183], a[180:183], a[80:83], v[180:183]\n"
    "v_exp_f32_e32 v169, v169\n"
    "v_mfma_f32_16x16x32_bf16 v[188:191], a[180:183], a[112:115], v[188:191]\n"
    "v_cvt_pk_bf16_f32 v177, v178, v179\n"
    "v_mfma_f32_16x16x32_bf16 v[188:191], a[176:179], a[108:111], v[188:191]\n"
    "v_exp_f32_e32 v170, v170\n"
    "v_mfma_f32_16x16x32_bf16 v[196:199], a[176:179], a[140:143], v[196:199]\n"
    "ds_read_b64_tr_b16 a[176:177], v246 offset:16384\n"
    "v_mul_f32_e32 v158, v253, v158\n"
    "v_mfma_f32_16x16x32_bf16 v[196:199], a[180:183], a[144:147], v[196:199]\n"
    "ds_read_b64_tr_b16 a[178:179], v246 offset:18432\n"
    "v_mfma_f32_16x16x32_bf16 v[196:199], a[184:187], a[148:151], v[196:199]\n"
    "ds_read_b64_tr_b16 a[180:181], v246 offset:24576\n"
    "v_mfma_f32_16x16x32_bf16 v[196:199], a[188:191], a[152:155], v[196:199]\n"
    "ds_read_b64_tr_b16 a[182:183], v246 offset:26624\n"
    "v_mfma_f32_16x16x32_bf16 v[188:191], a[188:191], a[120:123], v[188:191]\n"
    "v_exp_f32_e32 v171, v171\n"
    "v_mfma_f32_16x16x32_bf16 v[188:191], a[184:187], a[116:119], v[188:191]\n"
    "v_cvt_pk_bf16_f32 v168, v168, v169\n"
    "v_mul_f32_e32 v159, v253, v159\n"
    "v_mfma_f32_16x16x32_bf16 v[180:183], a[184:187], a[84:87], v[180:183]\n"
    "v_exp_f32_e32 v160, v160\n"
    "v_mfma_f32_16x16x32_bf16 v[180:183], a[188:191], a[88:91], v[180:183]\n"
    "v_exp_f32_e32 v161, v161\n"
    "v_mfma_f32_16x16x32_bf16 v[172:175], a[188:191], a[56:59], v[172:175]\n"
    "v_cvt_pk_bf16_f32 v169, v170, v171\n"
    "v_mfma_f32_16x16x32_bf16 v[172:175], a[184:187], a[52:55], v[172:175]\n"
    "v_exp_f32_e32 v162, v162\n"
    "v_mfma_f32_16x16x32_bf16 v[164:167], a[184:187], a[20:23], v[164:167]\n"
    "ds_read_b64_tr_b16 a[184:185], v247 offset:16384\n"
    "v_mfma_f32_16x16x32_bf16 v[164:167], a[188:191], a[24:27], v[164:167]\n"
    "s_waitcnt lgkmcnt(5)\n"
    "ds_read_b64_tr_b16 a[186:187], v247 offset:18432\n"
    "v_mfma_f32_16x16x32_bf16 v[0:3], a[160:163], v[200:203], v[0:3]\n"
    "ds_read_b64_tr_b16 a[188:189], v247 offset:24576\n"
    "v_mad_u32_u16 v253, v255, s42, v153 op_sel:[0,0,0,0]\n"
    "v_mfma_f32_16x16x32_bf16 v[0:3], a[164:167], v[204:207], v[0:3]\n"
    "ds_read_b64_tr_b16 a[190:191], v247 offset:26624\n"
    "v_mfma_f32_16x16x32_bf16 a[248:251], a[164:167], v[212:215], a[248:251]\n"
    "v_exp_f32_e32 v163, v163\n"
    "v_mfma_f32_16x16x32_bf16 a[248:251], a[160:163], v[208:211], a[248:251]\n"
    "v_cvt_pk_bf16_f32 v160, v160, v161\n"
    "v_mfma_f32_16x16x32_bf16 v[56:59], a[160:163], v[216:219], v[56:59]\n"
    "v_cvt_pk_bf16_f32 v154, v156, v157\n"
    "v_mfma_f32_16x16x32_bf16 v[56:59], a[164:167], v[220:223], v[56:59]\n"
    "v_cvt_pk_bf16_f32 v161, v162, v163\n"
    "v_mfma_f32_16x16x32_bf16 v[60:63], a[164:167], v[228:231], v[60:63]\n"
    "v_exp_f32_e32 v196, v196\n"
    "v_mfma_f32_16x16x32_bf16 v[60:63], a[160:163], v[224:227], v[60:63]\n"
    "v_exp_f32_e32 v197, v197\n"
    "v_mfma_f32_16x16x32_bf16 v[64:67], a[160:163], v[232:235], v[64:67]\n"
    "ds_read_b64_tr_b16 a[160:161], v244 offset:20480\n"
    "v_mfma_f32_16x16x32_bf16 v[64:67], a[164:167], v[236:239], v[64:67]\n"
    "ds_read_b64_tr_b16 a[162:163], v244 offset:22528\n"
    "v_mfma_f32_16x16x32_bf16 v[76:79], a[168:171], v[232:235], v[76:79]\n"
    "ds_read_b64_tr_b16 a[164:165], v244 offset:28672\n"
    "v_mfma_f32_16x16x32_bf16 v[76:79], a[172:175], v[236:239], v[76:79]\n"
    "ds_read_b64_tr_b16 a[166:167], v244 offset:30720\n"
    "v_mfma_f32_16x16x32_bf16 v[72:75], a[172:175], v[228:231], v[72:75]\n"
    "v_exp_f32_e32 v198, v198\n"
    "v_mfma_f32_16x16x32_bf16 v[72:75], a[168:171], v[224:227], v[72:75]\n"
    "v_exp_f32_e32 v199, v199\n"
    "v_mfma_f32_16x16x32_bf16 v[68:71], a[168:171], v[216:219], v[68:71]\n"
    "v_cvt_pk_bf16_f32 v194, v196, v197\n"
    "v_cvt_pk_bf16_f32 v155, v158, v159\n"
    "v_mfma_f32_16x16x32_bf16 v[68:71], a[172:175], v[220:223], v[68:71]\n"
    "v_exp_f32_e32 v188, v188\n"
    "v_mfma_f32_16x16x32_bf16 a[252:255], a[172:175], v[212:215], a[252:255]\n"
    "v_exp_f32_e32 v189, v189\n"
    "v_mfma_f32_16x16x32_bf16 a[252:255], a[168:171], v[208:211], a[252:255]\n"
    "v_cvt_pk_bf16_f32 v195, v198, v199\n"
    "v_mfma_f32_16x16x32_bf16 v[4:7], a[168:171], v[200:203], v[4:7]\n"
    "ds_read_b64_tr_b16 a[168:169], v245 offset:20480\n"
    "v_mfma_f32_16x16x32_bf16 v[4:7], a[172:175], v[204:207], v[4:7]\n"
    "s_waitcnt lgkmcnt(5)\n"
    "ds_read_b64_tr_b16 a[170:171], v245 offset:22528\n"
    "v_mfma_f32_16x16x32_bf16 v[8:11], a[176:179], v[200:203], v[8:11]\n"
    "ds_read_b64_tr_b16 a[172:173], v245 offset:28672\n"
    "v_mov_b32_e32 v156, 0\n"
    "v_mfma_f32_16x16x32_bf16 v[8:11], a[180:183], v[204:207], v[8:11]\n"
    "ds_read_b64_tr_b16 a[174:175], v245 offset:30720\n"
    "v_mfma_f32_16x16x32_bf16 v[32:35], a[180:183], v[212:215], v[32:35]\n"
    "v_exp_f32_e32 v190, v190\n"
    "v_mfma_f32_16x16x32_bf16 v[32:35], a[176:179], v[208:211], v[32:35]\n"
    "v_exp_f32_e32 v191, v191\n"
    "v_mfma_f32_16x16x32_bf16 v[80:83], a[176:179], v[216:219], v[80:83]\n"
    "v_mov_b32_e32 v157, 0\n"
    "v_mfma_f32_16x16x32_bf16 v[80:83], a[180:183], v[220:223], v[80:83]\n"
    "v_cvt_pk_bf16_f32 v186, v188, v189\n"
    "v_mfma_f32_16x16x32_bf16 v[84:87], a[180:183], v[228:231], v[84:87]\n"
    "v_exp_f32_e32 v180, v180\n"
    "v_mfma_f32_16x16x32_bf16 v[84:87], a[176:179], v[224:227], v[84:87]\n"
    "v_exp_f32_e32 v181, v181\n"
    "v_mfma_f32_16x16x32_bf16 v[88:91], a[176:179], v[232:235], v[88:91]\n"
    "ds_read_b64_tr_b16 a[176:177], v246 offset:20480\n"
    "v_mfma_f32_16x16x32_bf16 v[88:91], a[180:183], v[236:239], v[88:91]\n"
    "ds_read_b64_tr_b16 a[178:179], v246 offset:22528\n"
    "v_mfma_f32_16x16x32_bf16 v[100:103], a[184:187], v[232:235], v[100:103]\n"
    "ds_read_b64_tr_b16 a[180:181], v246 offset:28672\n"
    "v_mfma_f32_16x16x32_bf16 v[100:103], a[188:191], v[236:239], v[100:103]\n"
    "ds_read_b64_tr_b16 a[182:183], v246 offset:30720\n"
    "v_mfma_f32_16x16x32_bf16 v[96:99], a[188:191], v[228:231], v[96:99]\n"
    "v_cvt_pk_bf16_f32 v187, v190, v191\n"
    "v_mov_b32_e32 v158, 0\n"
    "v_mfma_f32_16x16x32_bf16 v[96:99], a[184:187], v[224:227], v[96:99]\n"
    "v_exp_f32_e32 v182, v182\n"
    "v_mfma_f32_16x16x32_bf16 v[92:95], a[184:187], v[216:219], v[92:95]\n"
    "v_exp_f32_e32 v183, v183\n"
    "v_mfma_f32_16x16x32_bf16 v[92:95], a[188:191], v[220:223], v[92:95]\n"
    "v_cvt_pk_bf16_f32 v178, v180, v181\n"
    "v_mfma_f32_16x16x32_bf16 v[36:39], a[188:191], v[212:215], v[36:39]\n"
    "v_exp_f32_e32 v172, v172\n"
    "v_mfma_f32_16x16x32_bf16 v[36:39], a[184:187], v[208:211], v[36:39]\n"
    "v_exp_f32_e32 v173, v173\n"
    "v_mfma_f32_16x16x32_bf16 v[12:15], a[184:187], v[200:203], v[12:15]\n"
    "s_mov_b32 m0, s49\n"
    "ds_read_b64_tr_b16 a[184:185], v247 offset:20480\n"
    "v_mfma_f32_16x16x32_bf16 v[12:15], a[188:191], v[204:207], v[12:15]\n"
    "s_waitcnt lgkmcnt(5)\n"
    "ds_read_b64_tr_b16 a[186:187], v247 offset:22528\n"
    "v_mov_b32_e32 v159, 0\n"
    "v_mfma_f32_16x16x32_bf16 v[16:19], a[160:163], v[200:203], v[16:19]\n"
    "ds_read_b64_tr_b16 a[188:189], v247 offset:28672\n"
    "v_mfma_f32_16x16x32_bf16 v[16:19], a[164:167], v[204:207], v[16:19]\n"
    "ds_read_b64_tr_b16 a[190:191], v247 offset:30720\n"
    "v_mfma_f32_16x16x32_bf16 v[40:43], a[164:167], v[212:215], v[40:43]\n"
    "buffer_store_dwordx2 v[154:155], v253, s[24:27], 0 offen offset:0 sc1\n"
    "v_cvt_pk_bf16_f32 v179, v182, v183\n"
    "v_mfma_f32_16x16x32_bf16 v[40:43], a[160:163], v[208:211], v[40:43]\n"
    "v_exp_f32_e32 v174, v174\n"
    "v_mfma_f32_16x16x32_bf16 v[104:107], a[160:163], v[216:219], v[104:107]\n"
    "v_exp_f32_e32 v175, v175\n"
    "v_mfma_f32_16x16x32_bf16 v[104:107], a[164:167], v[220:223], v[104:107]\n"
    "s_waitcnt vmcnt(9)\n"
    "v_mfma_f32_16x16x32_bf16 v[108:111], a[164:167], v[228:231], v[108:111]\n"
    "s_barrier\n"
    "v_mfma_f32_16x16x32_bf16 v[108:111], a[160:163], v[224:227], v[108:111]\n"
    "ds_write_addtid_b32 v192 offset:16384\n"
    "v_cvt_pk_bf16_f32 v170, v172, v173\n"
    "v_mfma_f32_16x16x32_bf16 v[112:115], a[160:163], v[232:235], v[112:115]\n"
    "ds_read_b128 a[160:163], v243 offset:53248\n"
    "v_mfma_f32_16x16x32_bf16 v[112:115], a[164:167], v[236:239], v[112:115]\n"
    "ds_read_b128 a[164:167], v240 offset:49152\n"
    "v_mfma_f32_16x16x32_bf16 v[124:127], a[168:171], v[232:235], v[124:127]\n"
    "v_exp_f32_e32 v164, v164\n"
    "v_mfma_f32_16x16x32_bf16 v[124:127], a[172:175], v[236:239], v[124:127]\n"
    "v_exp_f32_e32 v165, v165\n"
    "v_mfma_f32_16x16x32_bf16 v[120:123], a[172:175], v[228:231], v[120:123]\n"
    "ds_write_addtid_b32 v193 offset:16640\n"
    "v_cvt_pk_bf16_f32 v171, v174, v175\n"
    "v_mfma_f32_16x16x32_bf16 v[120:123], a[168:171], v[224:227], v[120:123]\n"
    "ds_write_addtid_b32 v184 offset:12288\n"
    "v_mfma_f32_16x16x32_bf16 v[116:119], a[168:171], v[216:219], v[116:119]\n"
    "v_exp_f32_e32 v166, v166\n"
    "v_mfma_f32_16x16x32_bf16 v[116:119], a[172:175], v[220:223], v[116:119]\n"
    "v_exp_f32_e32 v167, v167\n"
    "v_mfma_f32_16x16x32_bf16 v[44:47], a[172:175], v[212:215], v[44:47]\n"
    "ds_write_addtid_b32 v185 offset:12544\n"
    "v_cvt_pk_bf16_f32 v162, v164, v165\n"
    "v_mfma_f32_16x16x32_bf16 v[44:47], a[168:171], v[208:211], v[44:47]\n"
    "v_cvt_pk_bf16_f32 v163, v166, v167\n"
    "v_mfma_f32_16x16x32_bf16 v[20:23], a[168:171], v[200:203], v[20:23]\n"
    "ds_read_b128 a[168:171], v241 offset:49152\n"
    "v_mfma_f32_16x16x32_bf16 v[20:23], a[172:175], v[204:207], v[20:23]\n"
    "s_waitcnt lgkmcnt(7)\n"
    "ds_read_b128 a[172:175], v240 offset:53248\n"
    "v_mfma_f32_16x16x32_bf16 v[24:27], a[176:179], v[200:203], v[24:27]\n"
    "ds_write_addtid_b32 v176 offset:8192\n"
    "v_mfma_f32_16x16x32_bf16 v[24:27], a[180:183], v[204:207], v[24:27]\n"
    "ds_write_addtid_b32 v177 offset:8448\n"
    "v_mfma_f32_16x16x32_bf16 v[48:51], a[180:183], v[212:215], v[48:51]\n"
    "ds_write_addtid_b32 v168 offset:4096\n"
    "v_mfma_f32_16x16x32_bf16 v[48:51], a[176:179], v[208:211], v[48:51]\n"
    "ds_write_addtid_b32 v169 offset:4352\n"
    "v_mfma_f32_16x16x32_bf16 v[128:131], a[176:179], v[216:219], v[128:131]\n"
    "ds_write_addtid_b32 v160 offset:0\n"
    "v_mfma_f32_16x16x32_bf16 v[128:131], a[180:183], v[220:223], v[128:131]\n"
    "ds_write_addtid_b32 v161 offset:256\n"
    "v_mfma_f32_16x16x32_bf16 v[132:135], a[180:183], v[228:231], v[132:135]\n"
    "ds_write_addtid_b32 v194 offset:16896\n"
    "v_mfma_f32_16x16x32_bf16 v[132:135], a[176:179], v[224:227], v[132:135]\n"
    "ds_write_addtid_b32 v195 offset:17152\n"
    "v_mfma_f32_16x16x32_bf16 v[136:139], a[176:179], v[232:235], v[136:139]\n"
    "ds_read_b128 a[176:179], v241 offset:53248\n"
    "v_mfma_f32_16x16x32_bf16 v[136:139], a[180:183], v[236:239], v[136:139]\n"
    "ds_read_b128 a[180:183], v242 offset:49152\n"
    "v_mfma_f32_16x16x32_bf16 v[148:151], a[184:187], v[232:235], v[148:151]\n"
    "ds_write_addtid_b32 v186 offset:12800\n"
    "v_mfma_f32_16x16x32_bf16 v[148:151], a[188:191], v[236:239], v[148:151]\n"
    "ds_write_addtid_b32 v187 offset:13056\n"
    "v_mfma_f32_16x16x32_bf16 v[144:147], a[188:191], v[228:231], v[144:147]\n"
    "ds_write_addtid_b32 v178 offset:8704\n"
    "v_mfma_f32_16x16x32_bf16 v[144:147], a[184:187], v[224:227], v[144:147]\n"
    "ds_write_addtid_b32 v179 offset:8960\n"
    "v_mfma_f32_16x16x32_bf16 v[140:143], a[184:187], v[216:219], v[140:143]\n"
    "ds_write_addtid_b32 v170 offset:4608\n"
    "v_mfma_f32_16x16x32_bf16 v[140:143], a[188:191], v[220:223], v[140:143]\n"
    "ds_write_addtid_b32 v171 offset:4864\n"
    "v_mfma_f32_16x16x32_bf16 v[52:55], a[188:191], v[212:215], v[52:55]\n"
    "ds_write_addtid_b32 v162 offset:512\n"
    "v_mfma_f32_16x16x32_bf16 v[52:55], a[184:187], v[208:211], v[52:55]\n"
    "ds_write_addtid_b32 v163 offset:768\n"
    "v_mfma_f32_16x16x32_bf16 v[28:31], a[184:187], v[200:203], v[28:31]\n"
    "ds_read_b128 a[184:187], v243 offset:49152\n"
    "v_mfma_f32_16x16x32_bf16 v[28:31], a[188:191], v[204:207], v[28:31]\n"
    "s_waitcnt lgkmcnt(15)\n"
    "ds_read_b128 a[188:191], v242 offset:53248\n"
    "v_mfma_f32_16x16x32_bf16 v[200:203], a[160:163], a[28:31], 0\n"
    "v_mfma_f32_16x16x32_bf16 v[200:203], a[164:167], a[0:3], v[200:203]\n"
    "v_mfma_f32_16x16x32_bf16 v[208:211], a[164:167], a[32:35], 0\n"
    "v_mfma_f32_16x16x32_bf16 v[208:211], a[160:163], a[60:63], v[208:211]\n"
    "v_mfma_f32_16x16x32_bf16 v[216:219], a[160:163], a[92:95], 0\n"
    "v_mfma_f32_16x16x32_bf16 v[216:219], a[164:167], a[64:67], v[216:219]\n"
    "s_waitcnt lgkmcnt(0)\n"
    "v_mfma_f32_16x16x32_bf16 v[224:227], a[164:167], a[96:99], 0\n"
    "s_barrier\n"
    "v_mfma_f32_16x16x32_bf16 v[224:227], a[160:163], a[124:127], v[224:227]\n"
    "s_add_u32 m0, s43, 16384\n"
    "ds_read2st64_b32 v[164:165], v250 offset0:0 offset1:1\n"
    "v_mfma_f32_16x16x32_bf16 v[232:235], a[160:163], a[156:159], 0\n"
    "ds_read_b128 a[160:163], v243 offset:55296\n"
    "v_mfma_f32_16x16x32_bf16 v[232:235], a[164:167], a[128:131], v[232:235]\n"
    "ds_read_b128 a[164:167], v240 offset:51200\n"
    "v_mfma_f32_16x16x32_bf16 v[232:235], a[168:171], a[132:135], v[232:235]\n"
    "buffer_load_dwordx4 v248, s[8:11], s40 offen offset:0 sc1 lds\n"
    "v_mfma_f32_16x16x32_bf16 v[232:235], a[172:175], a[136:139], v[232:235]\n"
    "v_mfma_f32_16x16x32_bf16 v[224:227], a[172:175], a[104:107], v[224:227]\n"
    "v_mfma_f32_16x16x32_bf16 v[224:227], a[168:171], a[100:103], v[224:227]\n"
    "ds_read2st64_b32 v[166:167], v250 offset0:2 offset1:3\n"
    "v_mfma_f32_16x16x32_bf16 v[216:219], a[168:171], a[68:71], v[216:219]\n"
    "buffer_load_dwordx4 v248, s[8:11], s40 offen offset:1024 sc1 lds\n"
    "v_mfma_f32_16x16x32_bf16 v[216:219], a[172:175], a[72:75], v[216:219]\n"
    "v_mfma_f32_16x16x32_bf16 v[208:211], a[172:175], a[40:43], v[208:211]\n"
    "v_mfma_f32_16x16x32_bf16 v[208:211], a[168:171], a[36:39], v[208:211]\n"
    "ds_read2st64_b32 v[172:173], v250 offset0:16 offset1:17\n"
    "v_mfma_f32_16x16x32_bf16 v[200:203], a[168:171], a[4:7], v[200:203]\n"
    "ds_read_b128 a[168:171], v241 offset:51200\n"
    "buffer_load_dwordx4 v248, s[8:11], s40 offen offset:2048 sc1 lds\n"
    "v_mfma_f32_16x16x32_bf16 v[200:203], a[172:175], a[8:11], v[200:203]\n"
    "ds_read_b128 a[172:175], v240 offset:55296\n"
    "v_mfma_f32_16x16x32_bf16 v[200:203], a[176:179], a[12:15], v[200:203]\n"
    "ds_read2st64_b32 v[174:175], v250 offset0:18 offset1:19\n"
    "v_mfma_f32_16x16x32_bf16 v[200:203], a[180:183], a[16:19], v[200:203]\n"
    "v_mfma_f32_16x16x32_bf16 v[208:211], a[180:183], a[48:51], v[208:211]\n"
    "buffer_load_dwordx4 v248, s[8:11], s40 offen offset:3072 sc1 lds\n"
    "v_mfma_f32_16x16x32_bf16 v[208:211], a[176:179], a[44:47], v[208:211]\n"
    "s_add_u32 m0, s43, 20480\n"
    "v_mfma_f32_16x16x32_bf16 v[216:219], a[176:179], a[76:79], v[216:219]\n"
    "s_add_u32 s40, s40, 0x8000\n"
    "ds_read2st64_b32 v[180:181], v250 offset0:32 offset1:33\n"
    "v_mfma_f32_16x16x32_bf16 v[216:219], a[180:183], a[80:83], v[216:219]\n"
    "v_mfma_f32_16x16x32_bf16 v[224:227], a[180:183], a[112:115], v[224:227]\n"
    "buffer_load_dwordx4 v248, s[8:11], s41 offen offset:0 sc1 lds\n"
    "v_mfma_f32_16x16x32_bf16 v[224:227], a[176:179], a[108:111], v[224:227]\n"
    "ds_read2st64_b32 v[182:183], v250 offset0:34 offset1:35\n"
    "v_mfma_f32_16x16x32_bf16 v[232:235], a[176:179], a[140:143], v[232:235]\n"
    "ds_read_b128 a[176:179], v241 offset:55296\n"
    "v_mfma_f32_16x16x32_bf16 v[232:235], a[180:183], a[144:147], v[232:235]\n"
    "ds_read_b128 a[180:183], v242 offset:51200\n"
    "v_mfma_f32_16x16x32_bf16 v[232:235], a[184:187], a[148:151], v[232:235]\n"
    "buffer_load_dwordx4 v248, s[8:11], s41 offen offset:1024 sc1 lds\n"
    "v_mfma_f32_16x16x32_bf16 v[232:235], a[188:191], a[152:155], v[232:235]\n"
    "v_mfma_f32_16x16x32_bf16 v[224:227], a[188:191], a[120:123], v[224:227]\n"
    "ds_read2st64_b32 v[188:189], v250 offset0:48 offset1:49\n"
    "v_mfma_f32_16x16x32_bf16 v[224:227], a[184:187], a[116:119], v[224:227]\n"
    "v_mfma_f32_16x16x32_bf16 v[216:219], a[184:187], a[84:87], v[216:219]\n"
    "buffer_load_dwordx4 v248, s[8:11], s41 offen offset:2048 sc1 lds\n"
    "v_mfma_f32_16x16x32_bf16 v[216:219], a[188:191], a[88:91], v[216:219]\n"
    "ds_read2st64_b32 v[190:191], v250 offset0:50 offset1:51\n"
    "v_mfma_f32_16x16x32_bf16 v[208:211], a[188:191], a[56:59], v[208:211]\n"
    "ds_read_b64 v[254:255], v252 offset:64\n"
    "v_mfma_f32_16x16x32_bf16 v[208:211], a[184:187], a[52:55], v[208:211]\n"
    "ds_read2st64_b32 v[196:197], v250 offset0:64 offset1:65\n"
    "v_mfma_f32_16x16x32_bf16 v[200:203], a[184:187], a[20:23], v[200:203]\n"
    "ds_read_b128 a[184:187], v243 offset:51200\n"
    "buffer_load_dwordx4 v248, s[8:11], s41 offen offset:3072 sc1 lds\n"
    "v_mfma_f32_16x16x32_bf16 v[200:203], a[188:191], a[24:27], v[200:203]\n"
    "s_waitcnt lgkmcnt(15)\n"
    "s_add_u32 s41, s41, 0x8000\n"
    "ds_read_b128 a[188:191], v242 offset:55296\n"
    "v_mfma_f32_16x16x32_bf16 v[204:207], a[160:163], a[28:31], 0\n"
    "s_waitcnt lgkmcnt(3)\n"
    "v_mad_u32_u16 v253, v254, s42, v251 op_sel:[0,0,0,0]\n"
    "v_mfma_f32_16x16x32_bf16 v[204:207], a[164:167], a[0:3], v[204:207]\n"
    "buffer_load_dwordx4 a[200:203], v253, s[20:23], 0 offen offset:0 sc1\n"
    "v_mfma_f32_16x16x32_bf16 v[212:215], a[164:167], a[32:35], 0\n"
    "v_exp_f32_e32 v232, v232\n"
    "v_mfma_f32_16x16x32_bf16 v[212:215], a[160:163], a[60:63], v[212:215]\n"
    "ds_read2st64_b32 v[198:199], v250 offset0:66 offset1:67\n"
    "v_mfma_f32_16x16x32_bf16 v[220:223], a[160:163], a[92:95], 0\n"
    "v_mad_u32_u16 v253, v254, s42, v251 op_sel:[1,0,0,0]\n"
    "v_mfma_f32_16x16x32_bf16 v[220:223], a[164:167], a[64:67], v[220:223]\n"
    "buffer_load_dwordx4 a[204:207], v253, s[20:23], 0 offen offset:0 sc1\n"
    "v_mfma_f32_16x16x32_bf16 v[228:231], a[164:167], a[96:99], 0\n"
    "v_exp_f32_e32 v233, v233\n"
    "v_mfma_f32_16x16x32_bf16 v[228:231], a[160:163], a[124:127], v[228:231]\n"
    "v_exp_f32_e32 v234, v234\n"
    "v_mfma_f32_16x16x32_bf16 v[236:239], a[160:163], a[156:159], 0\n"
    "ds_read_b64_tr_b16 a[160:161], v244 offset:32768\n"
    "v_mad_u32_u16 v253, v255, s42, v251 op_sel:[0,0,0,0]\n"
    "v_mfma_f32_16x16x32_bf16 v[236:239], a[164:167], a[128:131], v[236:239]\n"
    "ds_read_b64_tr_b16 a[162:163], v244 offset:34816\n"
    "buffer_load_dwordx4 a[208:211], v253, s[20:23], 0 offen offset:0 sc1\n"
    "v_mfma_f32_16x16x32_bf16 v[236:239], a[168:171], a[132:135], v[236:239]\n"
    "ds_read_b64_tr_b16 a[164:165], v244 offset:40960\n"
    "v_mfma_f32_16x16x32_bf16 v[236:239], a[172:175], a[136:139], v[236:239]\n"
    "ds_read_b64_tr_b16 a[166:167], v244 offset:43008\n"
    "v_mfma_f32_16x16x32_bf16 v[228:231], a[172:175], a[104:107], v[228:231]\n"
    "v_mad_u32_u16 v253, v255, s42, v251 op_sel:[1,0,0,0]\n"
    "v_mfma_f32_16x16x32_bf16 v[228:231], a[168:171], a[100:103], v[228:231]\n"
    "buffer_load_dwordx4 a[212:215], v253, s[20:23], 0 offen offset:0 sc1\n"
    "ds_read_b64 v[254:255], v252 offset:72\n"
    "v_mfma_f32_16x16x32_bf16 v[220:223], a[168:171], a[68:71], v[220:223]\n"
    "v_exp_f32_e32 v235, v235\n"
    "v_mfma_f32_16x16x32_bf16 v[220:223], a[172:175], a[72:75], v[220:223]\n"
    "v_cvt_pk_bf16_f32 v232, v232, v233\n"
    "v_mfma_f32_16x16x32_bf16 v[212:215], a[172:175], a[40:43], v[212:215]\n"
    "v_exp_f32_e32 v224, v224\n"
    "v_mfma_f32_16x16x32_bf16 v[212:215], a[168:171], a[36:39], v[212:215]\n"
    "v_exp_f32_e32 v225, v225\n"
    "v_mfma_f32_16x16x32_bf16 v[204:207], a[168:171], a[4:7], v[204:207]\n"
    "ds_read_b64_tr_b16 a[168:169], v245 offset:32768\n"
    "v_cvt_pk_bf16_f32 v233, v234, v235\n"
    "v_mfma_f32_16x16x32_bf16 v[204:207], a[172:175], a[8:11], v[204:207]\n"
    "ds_read_b64_tr_b16 a[170:171], v245 offset:34816\n"
    "v_mfma_f32_16x16x32_bf16 v[204:207], a[176:179], a[12:15], v[204:207]\n"
    "s_waitcnt lgkmcnt(2)\n"
    "ds_read_b64_tr_b16 a[172:173], v245 offset:40960\n"
    "v_mad_u32_u16 v253, v254, s42, v251 op_sel:[0,0,0,0]\n"
    "v_mfma_f32_16x16x32_bf16 v[204:207], a[180:183], a[16:19], v[204:207]\n"
    "ds_read_b64_tr_b16 a[174:175], v245 offset:43008\n"
    "buffer_load_dwordx4 a[216:219], v253, s[20:23], 0 offen offset:0 sc1\n"
    "v_mfma_f32_16x16x32_bf16 v[212:215], a[180:183], a[48:51], v[212:215]\n"
    "v_exp_f32_e32 v226, v226\n"
    "v_mfma_f32_16x16x32_bf16 v[212:215], a[176:179], a[44:47], v[212:215]\n"
    "v_exp_f32_e32 v227, v227\n"
    "v_mfma_f32_16x16x32_bf16 v[220:223], a[176:179], a[76:79], v[220:223]\n"
    "v_mad_u32_u16 v253, v254, s42, v251 op_sel:[1,0,0,0]\n"
    "v_cvt_pk_bf16_f32 v224, v224, v225\n"
    "v_mfma_f32_16x16x32_bf16 v[220:223], a[180:183], a[80:83], v[220:223]\n"
    "buffer_load_dwordx4 a[220:223], v253, s[20:23], 0 offen offset:0 sc1\n"
    "v_mfma_f32_16x16x32_bf16 v[228:231], a[180:183], a[112:115], v[228:231]\n"
    "v_exp_f32_e32 v216, v216\n"
    "v_mfma_f32_16x16x32_bf16 v[228:231], a[176:179], a[108:111], v[228:231]\n"
    "v_exp_f32_e32 v217, v217\n"
    "v_mfma_f32_16x16x32_bf16 v[236:239], a[176:179], a[140:143], v[236:239]\n"
    "ds_read_b64_tr_b16 a[176:177], v246 offset:32768\n"
    "v_cvt_pk_bf16_f32 v225, v226, v227\n"
    "v_mfma_f32_16x16x32_bf16 v[236:239], a[180:183], a[144:147], v[236:239]\n"
    "ds_read_b64_tr_b16 a[178:179], v246 offset:34816\n"
    "v_mfma_f32_16x16x32_bf16 v[236:239], a[184:187], a[148:151], v[236:239]\n"
    "ds_read_b64_tr_b16 a[180:181], v246 offset:40960\n"
    "v_mfma_f32_16x16x32_bf16 v[236:239], a[188:191], a[152:155], v[236:239]\n"
    "ds_read_b64_tr_b16 a[182:183], v246 offset:43008\n"
    "v_mfma_f32_16x16x32_bf16 v[228:231], a[188:191], a[120:123], v[228:231]\n"
    "ds_read_b64 v[254:255], v252 offset:96\n"
    "v_mfma_f32_16x16x32_bf16 v[228:231], a[184:187], a[116:119], v[228:231]\n"
    "v_exp_f32_e32 v218, v218\n"
    "v_mfma_f32_16x16x32_bf16 v[220:223], a[184:187], a[84:87], v[220:223]\n"
    "v_exp_f32_e32 v219, v219\n"
    "v_mfma_f32_16x16x32_bf16 v[220:223], a[188:191], a[88:91], v[220:223]\n"
    "v_cvt_pk_bf16_f32 v216, v216, v217\n"
    "v_mfma_f32_16x16x32_bf16 v[212:215], a[188:191], a[56:59], v[212:215]\n"
    "s_waitcnt lgkmcnt(0)\n"
    "v_mad_u32_u16 v253, v254, s42, v251 op_sel:[0,0,0,0]\n"
    "v_mfma_f32_16x16x32_bf16 v[212:215], a[184:187], a[52:55], v[212:215]\n"
    "buffer_load_dwordx4 a[224:227], v253, s[20:23], 0 offen offset:0 sc1\n"
    "v_mfma_f32_16x16x32_bf16 v[204:207], a[184:187], a[20:23], v[204:207]\n"
    "ds_read_b64_tr_b16 a[184:185], v247 offset:32768\n"
    "v_mfma_f32_16x16x32_bf16 v[204:207], a[188:191], a[24:27], v[204:207]\n"
    "ds_read_b64_tr_b16 a[186:187], v247 offset:34816\n"
    "v_mfma_f32_16x16x32_bf16 v[0:3], a[160:163], v[160:163], v[0:3]\n"
    "ds_read_b64_tr_b16 a[188:189], v247 offset:40960\n"
    "v_mad_u32_u16 v253, v254, s42, v251 op_sel:[1,0,0,0]\n"
    "v_mfma_f32_16x16x32_bf16 v[0:3], a[164:167], v[164:167], v[0:3]\n"
    "ds_read_b64_tr_b16 a[190:191], v247 offset:43008\n"
    "buffer_load_dwordx4 a[228:231], v253, s[20:23], 0 offen offset:0 sc1\n"
    "v_mfma_f32_16x16x32_bf16 a[248:251], a[164:167], v[172:175], a[248:251]\n"
    "v_exp_f32_e32 v208, v208\n"
    "v_mfma_f32_16x16x32_bf16 a[248:251], a[160:163], v[168:171], a[248:251]\n"
    "v_exp_f32_e32 v209, v209\n"
    "v_mfma_f32_16x16x32_bf16 v[56:59], a[160:163], v[176:179], v[56:59]\n"
    "v_mad_u32_u16 v253, v255, s42, v251 op_sel:[0,0,0,0]\n"
    "v_cvt_pk_bf16_f32 v217, v218, v219\n"
    "v_mfma_f32_16x16x32_bf16 v[56:59], a[164:167], v[180:183], v[56:59]\n"
    "buffer_load_dwordx4 a[232:235], v253, s[20:23], 0 offen offset:0 sc1\n"
    "v_mfma_f32_16x16x32_bf16 v[60:63], a[164:167], v[188:191], v[60:63]\n"
    "v_exp_f32_e32 v210, v210\n"
    "v_mfma_f32_16x16x32_bf16 v[60:63], a[160:163], v[184:187], v[60:63]\n"
    "v_exp_f32_e32 v211, v211\n"
    "v_mfma_f32_16x16x32_bf16 v[64:67], a[160:163], v[192:195], v[64:67]\n"
    "ds_read_b64_tr_b16 a[160:161], v244 offset:36864\n"
    "v_mad_u32_u16 v253, v255, s42, v251 op_sel:[1,0,0,0]\n"
    "v_mfma_f32_16x16x32_bf16 v[64:67], a[164:167], v[196:199], v[64:67]\n"
    "ds_read_b64_tr_b16 a[162:163], v244 offset:38912\n"
    "buffer_load_dwordx4 a[236:239], v253, s[20:23], 0 offen offset:0 sc1\n"
    "v_mfma_f32_16x16x32_bf16 v[76:79], a[168:171], v[192:195], v[76:79]\n"
    "ds_read_b64_tr_b16 a[164:165], v244 offset:45056\n"
    "v_cvt_pk_bf16_f32 v208, v208, v209\n"
    "v_mfma_f32_16x16x32_bf16 v[76:79], a[172:175], v[196:199], v[76:79]\n"
    "ds_read_b64_tr_b16 a[166:167], v244 offset:47104\n"
    "v_mfma_f32_16x16x32_bf16 v[72:75], a[172:175], v[188:191], v[72:75]\n"
    "ds_read_b64 v[254:255], v252 offset:104\n"
    "v_mfma_f32_16x16x32_bf16 v[72:75], a[168:171], v[184:187], v[72:75]\n"
    "v_exp_f32_e32 v200, v200\n"
    "v_mfma_f32_16x16x32_bf16 v[68:71], a[168:171], v[176:179], v[68:71]\n"
    "v_exp_f32_e32 v201, v201\n"
    "v_mfma_f32_16x16x32_bf16 v[68:71], a[172:175], v[180:183], v[68:71]\n"
    "v_cvt_pk_bf16_f32 v209, v210, v211\n"
    "v_mfma_f32_16x16x32_bf16 a[252:255], a[172:175], v[172:175], a[252:255]\n"
    "v_exp_f32_e32 v202, v202\n"
    "v_mfma_f32_16x16x32_bf16 a[252:255], a[168:171], v[168:171], a[252:255]\n"
    "v_exp_f32_e32 v203, v203\n"
    "v_mfma_f32_16x16x32_bf16 v[4:7], a[168:171], v[160:163], v[4:7]\n"
    "s_waitcnt lgkmcnt(0)\n"
    "ds_read_b64_tr_b16 a[168:169], v245 offset:36864\n"
    "v_mad_u32_u16 v253, v254, s42, v251 op_sel:[0,0,0,0]\n"
    "v_mfma_f32_16x16x32_bf16 v[4:7], a[172:175], v[164:167], v[4:7]\n"
    "ds_read_b64_tr_b16 a[170:171], v245 offset:38912\n"
    "v_cvt_pk_bf16_f32 v200, v200, v201\n"
    "v_mfma_f32_16x16x32_bf16 v[8:11], a[176:179], v[160:163], v[8:11]\n"
    "ds_read_b64_tr_b16 a[172:173], v245 offset:45056\n"
    "buffer_load_dwordx4 a[240:243], v253, s[20:23], 0 offen offset:0 sc1\n"
    "v_mfma_f32_16x16x32_bf16 v[8:11], a[180:183], v[164:167], v[8:11]\n"
    "ds_read_b64_tr_b16 a[174:175], v245 offset:47104\n"
    "v_cvt_pk_bf16_f32 v201, v202, v203\n"
    "v_mfma_f32_16x16x32_bf16 v[32:35], a[180:183], v[172:175], v[32:35]\n"
    "v_exp_f32_e32 v236, v236\n"
    "v_mfma_f32_16x16x32_bf16 v[32:35], a[176:179], v[168:171], v[32:35]\n"
    "v_mad_u32_u16 v253, v254, s42, v251 op_sel:[1,0,0,0]\n"
    "v_mfma_f32_16x16x32_bf16 v[80:83], a[176:179], v[176:179], v[80:83]\n"
    "buffer_load_dwordx4 a[244:247], v253, s[20:23], 0 offen offset:0 sc1\n"
    "v_mfma_f32_16x16x32_bf16 v[80:83], a[180:183], v[180:183], v[80:83]\n"
    "v_exp_f32_e32 v237, v237\n"
    "v_mfma_f32_16x16x32_bf16 v[84:87], a[180:183], v[188:191], v[84:87]\n"
    "v_exp_f32_e32 v238, v238\n"
    "v_mfma_f32_16x16x32_bf16 v[84:87], a[176:179], v[184:187], v[84:87]\n"
    "v_exp_f32_e32 v239, v239\n"
    "v_mfma_f32_16x16x32_bf16 v[88:91], a[176:179], v[192:195], v[88:91]\n"
    "ds_read_b64_tr_b16 a[176:177], v246 offset:36864\n"
    "v_cvt_pk_bf16_f32 v234, v236, v237\n"
    "v_mfma_f32_16x16x32_bf16 v[88:91], a[180:183], v[196:199], v[88:91]\n"
    "ds_read_b64_tr_b16 a[178:179], v246 offset:38912\n"
    "v_exp_f32_e32 v228, v228\n"
    "v_mfma_f32_16x16x32_bf16 v[100:103], a[184:187], v[192:195], v[100:103]\n"
    "ds_read_b64_tr_b16 a[180:181], v246 offset:45056\n"
    "v_exp_f32_e32 v229, v229\n"
    "v_mfma_f32_16x16x32_bf16 v[100:103], a[188:191], v[196:199], v[100:103]\n"
    "ds_read_b64_tr_b16 a[182:183], v246 offset:47104\n"
    "v_cvt_pk_bf16_f32 v235, v238, v239\n"
    "v_mfma_f32_16x16x32_bf16 v[96:99], a[188:191], v[188:191], v[96:99]\n"
    "v_exp_f32_e32 v230, v230\n"
    "v_mfma_f32_16x16x32_bf16 v[96:99], a[184:187], v[184:187], v[96:99]\n"
    "v_exp_f32_e32 v231, v231\n"
    "v_cvt_pk_bf16_f32 v226, v228, v229\n"
    "v_mfma_f32_16x16x32_bf16 v[92:95], a[184:187], v[176:179], v[92:95]\n"
    "v_exp_f32_e32 v220, v220\n"
    "v_mfma_f32_16x16x32_bf16 v[92:95], a[188:191], v[180:183], v[92:95]\n"
    "v_exp_f32_e32 v221, v221\n"
    "v_mfma_f32_16x16x32_bf16 v[36:39], a[188:191], v[172:175], v[36:39]\n"
    "v_cvt_pk_bf16_f32 v227, v230, v231\n"
    "v_mfma_f32_16x16x32_bf16 v[36:39], a[184:187], v[168:171], v[36:39]\n"
    "v_exp_f32_e32 v222, v222\n"
    "v_mfma_f32_16x16x32_bf16 v[12:15], a[184:187], v[160:163], v[12:15]\n"
    "s_mov_b32 m0, s49\n"
    "ds_read_b64_tr_b16 a[184:185], v247 offset:36864\n"
    "v_mfma_f32_16x16x32_bf16 v[12:15], a[188:191], v[164:167], v[12:15]\n"
    "s_waitcnt lgkmcnt(5)\n"
    "ds_read_b64_tr_b16 a[186:187], v247 offset:38912\n"
    "v_mfma_f32_16x16x32_bf16 v[16:19], a[160:163], v[160:163], v[16:19]\n"
    "ds_read_b64_tr_b16 a[188:189], v247 offset:45056\n"
    "v_mfma_f32_16x16x32_bf16 v[16:19], a[164:167], v[164:167], v[16:19]\n"
    "ds_read_b64_tr_b16 a[190:191], v247 offset:47104\n"
    "v_mfma_f32_16x16x32_bf16 v[40:43], a[164:167], v[172:175], v[40:43]\n"
    "v_exp_f32_e32 v223, v223\n"
    "v_mfma_f32_16x16x32_bf16 v[40:43], a[160:163], v[168:171], v[40:43]\n"
    "v_cvt_pk_bf16_f32 v218, v220, v221\n"
    "v_mfma_f32_16x16x32_bf16 v[104:107], a[160:163], v[176:179], v[104:107]\n"
    "v_exp_f32_e32 v212, v212\n"
    "v_mfma_f32_16x16x32_bf16 v[104:107], a[164:167], v[180:183], v[104:107]\n"
    "s_waitcnt vmcnt(20)\n"
    "v_mfma_f32_16x16x32_bf16 v[108:111], a[164:167], v[188:191], v[108:111]\n"
    "s_barrier\n"
    "v_mfma_f32_16x16x32_bf16 v[108:111], a[160:163], v[184:187], v[108:111]\n"
    "v_exp_f32_e32 v213, v213\n"
    "v_mfma_f32_16x16x32_bf16 v[112:115], a[160:163], v[192:195], v[112:115]\n"
    "ds_read_b128 a[160:163], v243 offset:4096\n"
    "v_cvt_pk_bf16_f32 v219, v222, v223\n"
    "v_mfma_f32_16x16x32_bf16 v[112:115], a[164:167], v[196:199], v[112:115]\n"
    "ds_read_b128 a[164:167], v240 offset:0\n"
    "ds_write_addtid_b32 v232 offset:16384\n"
    "v_mfma_f32_16x16x32_bf16 v[124:127], a[168:171], v[192:195], v[124:127]\n"
    "v_exp_f32_e32 v214, v214\n"
    "v_mfma_f32_16x16x32_bf16 v[124:127], a[172:175], v[196:199], v[124:127]\n"
    "v_exp_f32_e32 v215, v215\n"
    "v_mfma_f32_16x16x32_bf16 v[120:123], a[172:175], v[188:191], v[120:123]\n"
    "ds_write_addtid_b32 v233 offset:16640\n"
    "v_cvt_pk_bf16_f32 v210, v212, v213\n"
    "v_mfma_f32_16x16x32_bf16 v[120:123], a[168:171], v[184:187], v[120:123]\n"
    "v_exp_f32_e32 v204, v204\n"
    "v_mfma_f32_16x16x32_bf16 v[116:119], a[168:171], v[176:179], v[116:119]\n"
    "v_exp_f32_e32 v205, v205\n"
    "v_mfma_f32_16x16x32_bf16 v[116:119], a[172:175], v[180:183], v[116:119]\n"
    "ds_write_addtid_b32 v224 offset:12288\n"
    "v_cvt_pk_bf16_f32 v211, v214, v215\n"
    "v_mfma_f32_16x16x32_bf16 v[44:47], a[172:175], v[172:175], v[44:47]\n"
    "v_exp_f32_e32 v206, v206\n"
    "v_mfma_f32_16x16x32_bf16 v[44:47], a[168:171], v[168:171], v[44:47]\n"
    "v_exp_f32_e32 v207, v207\n"
    "v_mfma_f32_16x16x32_bf16 v[20:23], a[168:171], v[160:163], v[20:23]\n"
    "ds_read_b128 a[168:171], v241 offset:0\n"
    "v_cvt_pk_bf16_f32 v202, v204, v205\n"
    "v_mfma_f32_16x16x32_bf16 v[20:23], a[172:175], v[164:167], v[20:23]\n"
    "s_waitcnt lgkmcnt(6)\n"
    "ds_read_b128 a[172:175], v240 offset:4096\n"
    "v_cvt_pk_bf16_f32 v203, v206, v207\n"
    "v_mfma_f32_16x16x32_bf16 v[24:27], a[176:179], v[160:163], v[24:27]\n"
    "ds_write_addtid_b32 v225 offset:12544\n"
    "ds_write_addtid_b32 v216 offset:8192\n"
    "v_mfma_f32_16x16x32_bf16 v[24:27], a[180:183], v[164:167], v[24:27]\n"
    "ds_write_addtid_b32 v217 offset:8448\n"
    "v_mfma_f32_16x16x32_bf16 v[48:51], a[180:183], v[172:175], v[48:51]\n"
    "ds_write_addtid_b32 v208 offset:4096\n"
    "v_mfma_f32_16x16x32_bf16 v[48:51], a[176:179], v[168:171], v[48:51]\n"
    "ds_write_addtid_b32 v209 offset:4352\n"
    "v_mfma_f32_16x16x32_bf16 v[128:131], a[176:179], v[176:179], v[128:131]\n"
    "ds_write_addtid_b32 v200 offset:0\n"
    "v_mfma_f32_16x16x32_bf16 v[128:131], a[180:183], v[180:183], v[128:131]\n"
    "ds_write_addtid_b32 v201 offset:256\n"
    "v_mfma_f32_16x16x32_bf16 v[132:135], a[180:183], v[188:191], v[132:135]\n"
    "ds_write_addtid_b32 v234 offset:16896\n"
    "v_mfma_f32_16x16x32_bf16 v[132:135], a[176:179], v[184:187], v[132:135]\n"
    "ds_write_addtid_b32 v235 offset:17152\n"
    "v_mfma_f32_16x16x32_bf16 v[136:139], a[176:179], v[192:195], v[136:139]\n"
    "ds_read_b128 a[176:179], v241 offset:4096\n"
    "v_mfma_f32_16x16x32_bf16 v[136:139], a[180:183], v[196:199], v[136:139]\n"
    "ds_read_b128 a[180:183], v242 offset:0\n"
    "v_mfma_f32_16x16x32_bf16 v[148:151], a[184:187], v[192:195], v[148:151]\n"
    "ds_write_addtid_b32 v226 offset:12800\n"
    "v_mfma_f32_16x16x32_bf16 v[148:151], a[188:191], v[196:199], v[148:151]\n"
    "ds_write_addtid_b32 v227 offset:13056\n"
    "v_mfma_f32_16x16x32_bf16 v[144:147], a[188:191], v[188:191], v[144:147]\n"
    "ds_write_addtid_b32 v218 offset:8704\n"
    "v_mfma_f32_16x16x32_bf16 v[144:147], a[184:187], v[184:187], v[144:147]\n"
    "ds_write_addtid_b32 v219 offset:8960\n"
    "v_mfma_f32_16x16x32_bf16 v[140:143], a[184:187], v[176:179], v[140:143]\n"
    "ds_write_addtid_b32 v210 offset:4608\n"
    "v_mfma_f32_16x16x32_bf16 v[140:143], a[188:191], v[180:183], v[140:143]\n"
    "ds_write_addtid_b32 v211 offset:4864\n"
    "v_mfma_f32_16x16x32_bf16 v[52:55], a[188:191], v[172:175], v[52:55]\n"
    "ds_write_addtid_b32 v202 offset:512\n"
    "v_mfma_f32_16x16x32_bf16 v[52:55], a[184:187], v[168:171], v[52:55]\n"
    "ds_write_addtid_b32 v203 offset:768\n"
    "v_mfma_f32_16x16x32_bf16 v[28:31], a[184:187], v[160:163], v[28:31]\n"
    "ds_read_b128 a[184:187], v243 offset:0\n"
    "v_mfma_f32_16x16x32_bf16 v[28:31], a[188:191], v[164:167], v[28:31]\n"
    "s_waitcnt lgkmcnt(15)\n"
    "ds_read_b128 a[188:191], v242 offset:4096\n"
    "v_mfma_f32_16x16x32_bf16 v[160:163], a[160:163], a[28:31], 0\n"
    "v_mfma_f32_16x16x32_bf16 v[160:163], a[164:167], a[0:3], v[160:163]\n"
    "v_mfma_f32_16x16x32_bf16 v[168:171], a[164:167], a[32:35], 0\n"
    "v_mfma_f32_16x16x32_bf16 v[168:171], a[160:163], a[60:63], v[168:171]\n"
    "v_mfma_f32_16x16x32_bf16 v[176:179], a[160:163], a[92:95], 0\n"
    "v_mfma_f32_16x16x32_bf16 v[176:179], a[164:167], a[64:67], v[176:179]\n"
    "s_waitcnt lgkmcnt(0)\n"
    "v_mfma_f32_16x16x32_bf16 v[184:187], a[164:167], a[96:99], 0\n"
    "s_barrier\n"
    "v_mfma_f32_16x16x32_bf16 v[184:187], a[160:163], a[124:127], v[184:187]\n"
    "s_add_u32 m0, s43, 32768\n"
    "v_mfma_f32_16x16x32_bf16 v[192:195], a[160:163], a[156:159], 0\n"
    "ds_read_b128 a[160:163], v243 offset:6144\n"
    "v_mfma_f32_16x16x32_bf16 v[192:195], a[164:167], a[128:131], v[192:195]\n"
    "ds_read_b128 a[164:167], v240 offset:2048\n"
    "v_mfma_f32_16x16x32_bf16 v[192:195], a[168:171], a[132:135], v[192:195]\n"
    "buffer_load_dwordx4 v248, s[8:11], s40 offen offset:0 sc1 lds\n"
    "v_mfma_f32_16x16x32_bf16 v[192:195], a[172:175], a[136:139], v[192:195]\n"
    "ds_read2st64_b32 v[204:205], v250 offset0:0 offset1:1\n"
    "v_mfma_f32_16x16x32_bf16 v[184:187], a[172:175], a[104:107], v[184:187]\n"
    "s_waitcnt vmcnt(11)\n"
    "v_smfmac_f32_16x16x64_bf16 v[156:159], a[192:195], a[200:207], v152\n"
    "v_mfma_f32_16x16x32_bf16 v[184:187], a[168:171], a[100:103], v[184:187]\n"
    "ds_read2st64_b32 v[206:207], v250 offset0:2 offset1:3\n"
    "v_mfma_f32_16x16x32_bf16 v[176:179], a[168:171], a[68:71], v[176:179]\n"
    "buffer_load_dwordx4 v248, s[8:11], s40 offen offset:1024 sc1 lds\n"
    "v_mfma_f32_16x16x32_bf16 v[176:179], a[172:175], a[72:75], v[176:179]\n"
    "v_mfma_f32_16x16x32_bf16 v[168:171], a[172:175], a[40:43], v[168:171]\n"
    "s_waitcnt vmcnt(10)\n"
    "v_smfmac_f32_16x16x64_bf16 v[156:159], a[192:195], a[208:215], v152\n"
    "ds_read2st64_b32 v[212:213], v250 offset0:16 offset1:17\n"
    "v_mfma_f32_16x16x32_bf16 v[168:171], a[168:171], a[36:39], v[168:171]\n"
    "v_mfma_f32_16x16x32_bf16 v[160:163], a[168:171], a[4:7], v[160:163]\n"
    "ds_read_b128 a[168:171], v241 offset:2048\n"
    "buffer_load_dwordx4 v248, s[8:11], s40 offen offset:2048 sc1 lds\n"
    "v_mfma_f32_16x16x32_bf16 v[160:163], a[172:175], a[8:11], v[160:163]\n"
    "ds_read_b128 a[172:175], v240 offset:6144\n"
    "v_mfma_f32_16x16x32_bf16 v[160:163], a[176:179], a[12:15], v[160:163]\n"
    "s_waitcnt vmcnt(9)\n"
    "ds_read2st64_b32 v[214:215], v250 offset0:18 offset1:19\n"
    "v_smfmac_f32_16x16x64_bf16 v[156:159], a[192:195], a[216:223], v152\n"
    "v_mfma_f32_16x16x32_bf16 v[160:163], a[180:183], a[16:19], v[160:163]\n"
    "v_mfma_f32_16x16x32_bf16 v[168:171], a[180:183], a[48:51], v[168:171]\n"
    "buffer_load_dwordx4 v248, s[8:11], s40 offen offset:3072 sc1 lds\n"
    "v_mfma_f32_16x16x32_bf16 v[168:171], a[176:179], a[44:47], v[168:171]\n"
    "s_add_u32 m0, s43, 36864\n"
    "v_mfma_f32_16x16x32_bf16 v[176:179], a[176:179], a[76:79], v[176:179]\n"
    "s_add_u32 s40, s40, 0x8000\n"
    "ds_read2st64_b32 v[220:221], v250 offset0:32 offset1:33\n"
    "v_mfma_f32_16x16x32_bf16 v[176:179], a[180:183], a[80:83], v[176:179]\n"
    "v_mfma_f32_16x16x32_bf16 v[184:187], a[180:183], a[112:115], v[184:187]\n"
    "v_mfma_f32_16x16x32_bf16 v[184:187], a[176:179], a[108:111], v[184:187]\n"
    "buffer_load_dwordx4 v248, s[8:11], s41 offen offset:0 sc1 lds\n"
    "v_mfma_f32_16x16x32_bf16 v[192:195], a[176:179], a[140:143], v[192:195]\n"
    "ds_read_b128 a[176:179], v241 offset:6144\n"
    "v_mfma_f32_16x16x32_bf16 v[192:195], a[180:183], a[144:147], v[192:195]\n"
    "ds_read_b128 a[180:183], v242 offset:2048\n"
    "v_mfma_f32_16x16x32_bf16 v[192:195], a[184:187], a[148:151], v[192:195]\n"
    "ds_read2st64_b32 v[222:223], v250 offset0:34 offset1:35\n"
    "v_mfma_f32_16x16x32_bf16 v[192:195], a[188:191], a[152:155], v[192:195]\n"
    "v_mfma_f32_16x16x32_bf16 v[184:187], a[188:191], a[120:123], v[184:187]\n"
    "buffer_load_dwordx4 v248, s[8:11], s41 offen offset:1024 sc1 lds\n"
    "v_mfma_f32_16x16x32_bf16 v[184:187], a[184:187], a[116:119], v[184:187]\n"
    "ds_read2st64_b32 v[228:229], v250 offset0:48 offset1:49\n"
    "v_mfma_f32_16x16x32_bf16 v[176:179], a[184:187], a[84:87], v[176:179]\n"
    "s_waitcnt vmcnt(10)\n"
    "v_exp_f32_e32 v192, v192\n"
    "v_smfmac_f32_16x16x64_bf16 v[156:159], a[192:195], a[224:231], v152\n"
    "v_exp_f32_e32 v193, v193\n"
    "v_mfma_f32_16x16x32_bf16 v[176:179], a[188:191], a[88:91], v[176:179]\n"
    "v_exp_f32_e32 v194, v194\n"
    "v_mfma_f32_16x16x32_bf16 v[168:171], a[188:191], a[56:59], v[168:171]\n"
    "buffer_load_dwordx4 v248, s[8:11], s41 offen offset:2048 sc1 lds\n"
    "v_mfma_f32_16x16x32_bf16 v[168:171], a[184:187], a[52:55], v[168:171]\n"
    "ds_read2st64_b32 v[230:231], v250 offset0:50 offset1:51\n"
    "v_mfma_f32_16x16x32_bf16 v[160:163], a[184:187], a[20:23], v[160:163]\n"
    "s_waitcnt vmcnt(9)\n"
    "ds_read_b128 a[184:187], v243 offset:2048\n"
    "v_smfmac_f32_16x16x64_bf16 v[156:159], a[192:195], a[232:239], v152\n"
    "ds_read2st64_b32 v[236:237], v250 offset0:64 offset1:65\n"
    "v_mfma_f32_16x16x32_bf16 v[160:163], a[188:191], a[24:27], v[160:163]\n"
    "s_waitcnt lgkmcnt(9)\n"
    "ds_read_b128 a[188:191], v242 offset:6144\n"
    "v_mfma_f32_16x16x32_bf16 v[164:167], a[160:163], a[28:31], 0\n"
    "buffer_load_dwordx4 v248, s[8:11], s41 offen offset:3072 sc1 lds\n"
    "v_mfma_f32_16x16x32_bf16 v[164:167], a[164:167], a[0:3], v[164:167]\n"
    "s_add_u32 s41, s41, 0x8000\n"
    "v_exp_f32_e32 v195, v195\n"
    "v_mfma_f32_16x16x32_bf16 v[172:175], a[164:167], a[32:35], 0\n"
    "s_waitcnt vmcnt(8)\n"
    "v_cvt_pk_bf16_f32 v192, v192, v193\n"
    "v_smfmac_f32_16x16x64_bf16 v[156:159], a[192:195], a[240:247], v152\n"
    "v_exp_f32_e32 v184, v184\n"
    "v_mfma_f32_16x16x32_bf16 v[172:175], a[160:163], a[60:63], v[172:175]\n"
    "ds_read2st64_b32 v[238:239], v250 offset0:66 offset1:67\n"
    "v_mfma_f32_16x16x32_bf16 v[180:183], a[160:163], a[92:95], 0\n"
    "v_exp_f32_e32 v185, v185\n"
    "v_mfma_f32_16x16x32_bf16 v[180:183], a[164:167], a[64:67], v[180:183]\n"
    "v_cvt_pk_bf16_f32 v193, v194, v195\n"
    "v_mfma_f32_16x16x32_bf16 v[188:191], a[164:167], a[96:99], 0\n"
    "v_exp_f32_e32 v186, v186\n"
    "v_mfma_f32_16x16x32_bf16 v[188:191], a[160:163], a[124:127], v[188:191]\n"
    "v_exp_f32_e32 v187, v187\n"
    "v_mfma_f32_16x16x32_bf16 v[196:199], a[160:163], a[156:159], 0\n"
    "ds_read_b64_tr_b16 a[160:161], v244 offset:49152\n"
    "v_and_b32_e32 v253, 0xffff0000, v255\n"
    "v_mfma_f32_16x16x32_bf16 v[196:199], a[164:167], a[128:131], v[196:199]\n"
    "ds_read_b64_tr_b16 a[162:163], v244 offset:51200\n"
    "v_mfma_f32_16x16x32_bf16 v[196:199], a[168:171], a[132:135], v[196:199]\n"
    "ds_read_b64_tr_b16 a[164:165], v244 offset:57344\n"
    "v_mfma_f32_16x16x32_bf16 v[196:199], a[172:175], a[136:139], v[196:199]\n"
    "ds_read_b64_tr_b16 a[166:167], v244 offset:59392\n"
    "v_mfma_f32_16x16x32_bf16 v[188:191], a[172:175], a[104:107], v[188:191]\n"
    "v_cvt_pk_bf16_f32 v184, v184, v185\n"
    "v_mfma_f32_16x16x32_bf16 v[188:191], a[168:171], a[100:103], v[188:191]\n"
    "v_exp_f32_e32 v176, v176\n"
    "v_mfma_f32_16x16x32_bf16 v[180:183], a[168:171], a[68:71], v[180:183]\n"
    "v_exp_f32_e32 v177, v177\n"
    "v_mfma_f32_16x16x32_bf16 v[180:183], a[172:175], a[72:75], v[180:183]\n"
    "v_cvt_pk_bf16_f32 v185, v186, v187\n"
    "v_mul_f32_e32 v156, v253, v156\n"
    "v_mfma_f32_16x16x32_bf16 v[172:175], a[172:175], a[40:43], v[172:175]\n"
    "v_exp_f32_e32 v178, v178\n"
    "v_mfma_f32_16x16x32_bf16 v[172:175], a[168:171], a[36:39], v[172:175]\n"
    "v_exp_f32_e32 v179, v179\n"
    "v_mfma_f32_16x16x32_bf16 v[164:167], a[168:171], a[4:7], v[164:167]\n"
    "ds_read_b64_tr_b16 a[168:169], v245 offset:49152\n"
    "v_mfma_f32_16x16x32_bf16 v[164:167], a[172:175], a[8:11], v[164:167]\n"
    "s_waitcnt lgkmcnt(6)\n"
    "ds_read_b64_tr_b16 a[170:171], v245 offset:51200\n"
    "v_mfma_f32_16x16x32_bf16 v[164:167], a[176:179], a[12:15], v[164:167]\n"
    "ds_read_b64_tr_b16 a[172:173], v245 offset:57344\n"
    "v_mul_f32_e32 v157, v253, v157\n"
    "v_mfma_f32_16x16x32_bf16 v[164:167], a[180:183], a[16:19], v[164:167]\n"
    "ds_read_b64_tr_b16 a[174:175], v245 offset:59392\n"
    "v_mfma_f32_16x16x32_bf16 v[172:175], a[180:183], a[48:51], v[172:175]\n"
    "v_cvt_pk_bf16_f32 v176, v176, v177\n"
    "v_mfma_f32_16x16x32_bf16 v[172:175], a[176:179], a[44:47], v[172:175]\n"
    "v_exp_f32_e32 v168, v168\n"
    "v_mfma_f32_16x16x32_bf16 v[180:183], a[176:179], a[76:79], v[180:183]\n"
    "v_mul_f32_e32 v158, v253, v158\n"
    "v_mfma_f32_16x16x32_bf16 v[180:183], a[180:183], a[80:83], v[180:183]\n"
    "v_exp_f32_e32 v169, v169\n"
    "v_mfma_f32_16x16x32_bf16 v[188:191], a[180:183], a[112:115], v[188:191]\n"
    "v_cvt_pk_bf16_f32 v177, v178, v179\n"
    "v_mfma_f32_16x16x32_bf16 v[188:191], a[176:179], a[108:111], v[188:191]\n"
    "v_exp_f32_e32 v170, v170\n"
    "v_mfma_f32_16x16x32_bf16 v[196:199], a[176:179], a[140:143], v[196:199]\n"
    "ds_read_b64_tr_b16 a[176:177], v246 offset:49152\n"
    "v_mfma_f32_16x16x32_bf16 v[196:199], a[180:183], a[144:147], v[196:199]\n"
    "ds_read_b64_tr_b16 a[178:179], v246 offset:51200\n"
    "v_mfma_f32_16x16x32_bf16 v[196:199], a[184:187], a[148:151], v[196:199]\n"
    "ds_read_b64_tr_b16 a[180:181], v246 offset:57344\n"
    "v_mfma_f32_16x16x32_bf16 v[196:199], a[188:191], a[152:155], v[196:199]\n"
    "ds_read_b64_tr_b16 a[182:183], v246 offset:59392\n"
    "v_mul_f32_e32 v159, v253, v159\n"
    "v_mfma_f32_16x16x32_bf16 v[188:191], a[188:191], a[120:123], v[188:191]\n"
    "v_exp_f32_e32 v171, v171\n"
    "v_mfma_f32_16x16x32_bf16 v[188:191], a[184:187], a[116:119], v[188:191]\n"
    "v_cvt_pk_bf16_f32 v168, v168, v169\n"
    "v_mfma_f32_16x16x32_bf16 v[180:183], a[184:187], a[84:87], v[180:183]\n"
    "v_exp_f32_e32 v160, v160\n"
    "v_mfma_f32_16x16x32_bf16 v[180:183], a[188:191], a[88:91], v[180:183]\n"
    "v_exp_f32_e32 v161, v161\n"
    "v_mfma_f32_16x16x32_bf16 v[172:175], a[188:191], a[56:59], v[172:175]\n"
    "v_cvt_pk_bf16_f32 v169, v170, v171\n"
    "v_mfma_f32_16x16x32_bf16 v[172:175], a[184:187], a[52:55], v[172:175]\n"
    "v_exp_f32_e32 v162, v162\n"
    "v_mfma_f32_16x16x32_bf16 v[164:167], a[184:187], a[20:23], v[164:167]\n"
    "ds_read_b64_tr_b16 a[184:185], v247 offset:49152\n"
    "v_mad_u32_u16 v253, v255, s42, v153 op_sel:[0,0,0,0]\n"
    "v_mfma_f32_16x16x32_bf16 v[164:167], a[188:191], a[24:27], v[164:167]\n"
    "s_waitcnt lgkmcnt(5)\n"
    "ds_read_b64_tr_b16 a[186:187], v247 offset:51200\n"
    "v_mfma_f32_16x16x32_bf16 v[0:3], a[160:163], v[200:203], v[0:3]\n"
    "ds_read_b64_tr_b16 a[188:189], v247 offset:57344\n"
    "v_mfma_f32_16x16x32_bf16 v[0:3], a[164:167], v[204:207], v[0:3]\n"
    "ds_read_b64_tr_b16 a[190:191], v247 offset:59392\n"
    "v_mfma_f32_16x16x32_bf16 a[248:251], a[164:167], v[212:215], a[248:251]\n"
    "v_exp_f32_e32 v163, v163\n"
    "v_mfma_f32_16x16x32_bf16 a[248:251], a[160:163], v[208:211], a[248:251]\n"
    "v_cvt_pk_bf16_f32 v160, v160, v161\n"
    "v_mfma_f32_16x16x32_bf16 v[56:59], a[160:163], v[216:219], v[56:59]\n"
    "v_cvt_pk_bf16_f32 v154, v156, v157\n"
    "v_mfma_f32_16x16x32_bf16 v[56:59], a[164:167], v[220:223], v[56:59]\n"
    "v_cvt_pk_bf16_f32 v161, v162, v163\n"
    "v_mfma_f32_16x16x32_bf16 v[60:63], a[164:167], v[228:231], v[60:63]\n"
    "v_exp_f32_e32 v196, v196\n"
    "v_mfma_f32_16x16x32_bf16 v[60:63], a[160:163], v[224:227], v[60:63]\n"
    "v_exp_f32_e32 v197, v197\n"
    "v_mfma_f32_16x16x32_bf16 v[64:67], a[160:163], v[232:235], v[64:67]\n"
    "ds_read_b64_tr_b16 a[160:161], v244 offset:53248\n"
    "v_mfma_f32_16x16x32_bf16 v[64:67], a[164:167], v[236:239], v[64:67]\n"
    "ds_read_b64_tr_b16 a[162:163], v244 offset:55296\n"
    "v_cvt_pk_bf16_f32 v155, v158, v159\n"
    "v_mfma_f32_16x16x32_bf16 v[76:79], a[168:171], v[232:235], v[76:79]\n"
    "ds_read_b64_tr_b16 a[164:165], v244 offset:61440\n"
    "v_mfma_f32_16x16x32_bf16 v[76:79], a[172:175], v[236:239], v[76:79]\n"
    "ds_read_b64_tr_b16 a[166:167], v244 offset:63488\n"
    "v_mfma_f32_16x16x32_bf16 v[72:75], a[172:175], v[228:231], v[72:75]\n"
    "v_exp_f32_e32 v198, v198\n"
    "v_mfma_f32_16x16x32_bf16 v[72:75], a[168:171], v[224:227], v[72:75]\n"
    "v_exp_f32_e32 v199, v199\n"
    "v_mfma_f32_16x16x32_bf16 v[68:71], a[168:171], v[216:219], v[68:71]\n"
    "v_cvt_pk_bf16_f32 v194, v196, v197\n"
    "v_mov_b32_e32 v156, 0\n"
    "v_mfma_f32_16x16x32_bf16 v[68:71], a[172:175], v[220:223], v[68:71]\n"
    "v_exp_f32_e32 v188, v188\n"
    "v_mfma_f32_16x16x32_bf16 a[252:255], a[172:175], v[212:215], a[252:255]\n"
    "v_exp_f32_e32 v189, v189\n"
    "v_mfma_f32_16x16x32_bf16 a[252:255], a[168:171], v[208:211], a[252:255]\n"
    "v_cvt_pk_bf16_f32 v195, v198, v199\n"
    "v_mfma_f32_16x16x32_bf16 v[4:7], a[168:171], v[200:203], v[4:7]\n"
    "ds_read_b64_tr_b16 a[168:169], v245 offset:53248\n"
    "v_mfma_f32_16x16x32_bf16 v[4:7], a[172:175], v[204:207], v[4:7]\n"
    "s_waitcnt lgkmcnt(5)\n"
    "ds_read_b64_tr_b16 a[170:171], v245 offset:55296\n"
    "v_mfma_f32_16x16x32_bf16 v[8:11], a[176:179], v[200:203], v[8:11]\n"
    "ds_read_b64_tr_b16 a[172:173], v245 offset:61440\n"
    "v_mfma_f32_16x16x32_bf16 v[8:11], a[180:183], v[204:207], v[8:11]\n"
    "ds_read_b64_tr_b16 a[174:175], v245 offset:63488\n"
    "v_mov_b32_e32 v157, 0\n"
    "v_mfma_f32_16x16x32_bf16 v[32:35], a[180:183], v[212:215], v[32:35]\n"
    "v_exp_f32_e32 v190, v190\n"
    "v_mfma_f32_16x16x32_bf16 v[32:35], a[176:179], v[208:211], v[32:35]\n"
    "v_exp_f32_e32 v191, v191\n"
    "v_mfma_f32_16x16x32_bf16 v[80:83], a[176:179], v[216:219], v[80:83]\n"
    "v_mov_b32_e32 v158, 0\n"
    "v_mfma_f32_16x16x32_bf16 v[80:83], a[180:183], v[220:223], v[80:83]\n"
    "v_cvt_pk_bf16_f32 v186, v188, v189\n"
    "v_mfma_f32_16x16x32_bf16 v[84:87], a[180:183], v[228:231], v[84:87]\n"
    "v_exp_f32_e32 v180, v180\n"
    "v_mfma_f32_16x16x32_bf16 v[84:87], a[176:179], v[224:227], v[84:87]\n"
    "v_exp_f32_e32 v181, v181\n"
    "v_mfma_f32_16x16x32_bf16 v[88:91], a[176:179], v[232:235], v[88:91]\n"
    "ds_read_b64_tr_b16 a[176:177], v246 offset:53248\n"
    "v_mfma_f32_16x16x32_bf16 v[88:91], a[180:183], v[236:239], v[88:91]\n"
    "ds_read_b64_tr_b16 a[178:179], v246 offset:55296\n"
    "v_mfma_f32_16x16x32_bf16 v[100:103], a[184:187], v[232:235], v[100:103]\n"
    "ds_read_b64_tr_b16 a[180:181], v246 offset:61440\n"
    "v_mfma_f32_16x16x32_bf16 v[100:103], a[188:191], v[236:239], v[100:103]\n"
    "ds_read_b64_tr_b16 a[182:183], v246 offset:63488\n"
    "v_mfma_f32_16x16x32_bf16 v[96:99], a[188:191], v[228:231], v[96:99]\n"
    "v_cvt_pk_bf16_f32 v187, v190, v191\n"
    "v_mov_b32_e32 v159, 0\n"
    "v_mfma_f32_16x16x32_bf16 v[96:99], a[184:187], v[224:227], v[96:99]\n"
    "v_exp_f32_e32 v182, v182\n"
    "v_mfma_f32_16x16x32_bf16 v[92:95], a[184:187], v[216:219], v[92:95]\n"
    "v_exp_f32_e32 v183, v183\n"
    "v_mfma_f32_16x16x32_bf16 v[92:95], a[188:191], v[220:223], v[92:95]\n"
    "v_cvt_pk_bf16_f32 v178, v180, v181\n"
    "v_mfma_f32_16x16x32_bf16 v[36:39], a[188:191], v[212:215], v[36:39]\n"
    "v_exp_f32_e32 v172, v172\n"
    "v_mfma_f32_16x16x32_bf16 v[36:39], a[184:187], v[208:211], v[36:39]\n"
    "v_exp_f32_e32 v173, v173\n"
    "v_mfma_f32_16x16x32_bf16 v[12:15], a[184:187], v[200:203], v[12:15]\n"
    "s_mov_b32 m0, s49\n"
    "ds_read_b64_tr_b16 a[184:185], v247 offset:53248\n"
    "v_mfma_f32_16x16x32_bf16 v[12:15], a[188:191], v[204:207], v[12:15]\n"
    "s_waitcnt lgkmcnt(5)\n"
    "ds_read_b64_tr_b16 a[186:187], v247 offset:55296\n"
    "v_add_u32_e32 v252, 0x80, v252\n"
    "v_mfma_f32_16x16x32_bf16 v[16:19], a[160:163], v[200:203], v[16:19]\n"
    "ds_read_b64_tr_b16 a[188:189], v247 offset:61440\n"
    "v_mfma_f32_16x16x32_bf16 v[16:19], a[164:167], v[204:207], v[16:19]\n"
    "ds_read_b64_tr_b16 a[190:191], v247 offset:63488\n"
    "v_mfma_f32_16x16x32_bf16 v[40:43], a[164:167], v[212:215], v[40:43]\n"
    "buffer_store_dwordx2 v[154:155], v253, s[24:27], 0 offen offset:0 sc1\n"
    "v_cvt_pk_bf16_f32 v179, v182, v183\n"
    "v_mfma_f32_16x16x32_bf16 v[40:43], a[160:163], v[208:211], v[40:43]\n"
    "v_exp_f32_e32 v174, v174\n"
    "v_mfma_f32_16x16x32_bf16 v[104:107], a[160:163], v[216:219], v[104:107]\n"
    "v_exp_f32_e32 v175, v175\n"
    "v_mfma_f32_16x16x32_bf16 v[104:107], a[164:167], v[220:223], v[104:107]\n"
    "s_waitcnt vmcnt(9)\n"
    "v_mfma_f32_16x16x32_bf16 v[108:111], a[164:167], v[228:231], v[108:111]\n"
    "s_barrier\n"
    "v_mfma_f32_16x16x32_bf16 v[108:111], a[160:163], v[224:227], v[108:111]\n"
    "ds_write_addtid_b32 v192 offset:16384\n"
    "v_cvt_pk_bf16_f32 v170, v172, v173\n"
    "v_mfma_f32_16x16x32_bf16 v[112:115], a[160:163], v[232:235], v[112:115]\n"
    "ds_read_b128 a[160:163], v243 offset:20480\n"
    "v_mfma_f32_16x16x32_bf16 v[112:115], a[164:167], v[236:239], v[112:115]\n"
    "ds_read_b128 a[164:167], v240 offset:16384\n"
    "v_mfma_f32_16x16x32_bf16 v[124:127], a[168:171], v[232:235], v[124:127]\n"
    "v_exp_f32_e32 v164, v164\n"
    "v_mfma_f32_16x16x32_bf16 v[124:127], a[172:175], v[236:239], v[124:127]\n"
    "v_exp_f32_e32 v165, v165\n"
    "v_mfma_f32_16x16x32_bf16 v[120:123], a[172:175], v[228:231], v[120:123]\n"
    "ds_write_addtid_b32 v193 offset:16640\n"
    "v_cvt_pk_bf16_f32 v171, v174, v175\n"
    "v_mfma_f32_16x16x32_bf16 v[120:123], a[168:171], v[224:227], v[120:123]\n"
    "ds_write_addtid_b32 v184 offset:12288\n"
    "v_mfma_f32_16x16x32_bf16 v[116:119], a[168:171], v[216:219], v[116:119]\n"
    "v_exp_f32_e32 v166, v166\n"
    "v_mfma_f32_16x16x32_bf16 v[116:119], a[172:175], v[220:223], v[116:119]\n"
    "v_exp_f32_e32 v167, v167\n"
    "v_mfma_f32_16x16x32_bf16 v[44:47], a[172:175], v[212:215], v[44:47]\n"
    "ds_write_addtid_b32 v185 offset:12544\n"
    "v_cvt_pk_bf16_f32 v162, v164, v165\n"
    "v_mfma_f32_16x16x32_bf16 v[44:47], a[168:171], v[208:211], v[44:47]\n"
    "v_cvt_pk_bf16_f32 v163, v166, v167\n"
    "v_mfma_f32_16x16x32_bf16 v[20:23], a[168:171], v[200:203], v[20:23]\n"
    "ds_read_b128 a[168:171], v241 offset:16384\n"
    "v_mfma_f32_16x16x32_bf16 v[20:23], a[172:175], v[204:207], v[20:23]\n"
    "s_waitcnt lgkmcnt(7)\n"
    "ds_read_b128 a[172:175], v240 offset:20480\n"
    "v_mfma_f32_16x16x32_bf16 v[24:27], a[176:179], v[200:203], v[24:27]\n"
    "ds_write_addtid_b32 v176 offset:8192\n"
    "v_mfma_f32_16x16x32_bf16 v[24:27], a[180:183], v[204:207], v[24:27]\n"
    "ds_write_addtid_b32 v177 offset:8448\n"
    "v_mfma_f32_16x16x32_bf16 v[48:51], a[180:183], v[212:215], v[48:51]\n"
    "ds_write_addtid_b32 v168 offset:4096\n"
    "v_mfma_f32_16x16x32_bf16 v[48:51], a[176:179], v[208:211], v[48:51]\n"
    "ds_write_addtid_b32 v169 offset:4352\n"
    "v_mfma_f32_16x16x32_bf16 v[128:131], a[176:179], v[216:219], v[128:131]\n"
    "ds_write_addtid_b32 v160 offset:0\n"
    "v_mfma_f32_16x16x32_bf16 v[128:131], a[180:183], v[220:223], v[128:131]\n"
    "ds_write_addtid_b32 v161 offset:256\n"
    "v_mfma_f32_16x16x32_bf16 v[132:135], a[180:183], v[228:231], v[132:135]\n"
    "ds_write_addtid_b32 v194 offset:16896\n"
    "v_mfma_f32_16x16x32_bf16 v[132:135], a[176:179], v[224:227], v[132:135]\n"
    "ds_write_addtid_b32 v195 offset:17152\n"
    "v_mfma_f32_16x16x32_bf16 v[136:139], a[176:179], v[232:235], v[136:139]\n"
    "ds_read_b128 a[176:179], v241 offset:20480\n"
    "v_mfma_f32_16x16x32_bf16 v[136:139], a[180:183], v[236:239], v[136:139]\n"
    "ds_read_b128 a[180:183], v242 offset:16384\n"
    "v_mfma_f32_16x16x32_bf16 v[148:151], a[184:187], v[232:235], v[148:151]\n"
    "ds_write_addtid_b32 v186 offset:12800\n"
    "v_mfma_f32_16x16x32_bf16 v[148:151], a[188:191], v[236:239], v[148:151]\n"
    "ds_write_addtid_b32 v187 offset:13056\n"
    "v_mfma_f32_16x16x32_bf16 v[144:147], a[188:191], v[228:231], v[144:147]\n"
    "ds_write_addtid_b32 v178 offset:8704\n"
    "v_mfma_f32_16x16x32_bf16 v[144:147], a[184:187], v[224:227], v[144:147]\n"
    "ds_write_addtid_b32 v179 offset:8960\n"
    "v_mfma_f32_16x16x32_bf16 v[140:143], a[184:187], v[216:219], v[140:143]\n"
    "s_sub_u32 s44, s44, 1\n"
    "ds_write_addtid_b32 v170 offset:4608\n"
    "v_mfma_f32_16x16x32_bf16 v[140:143], a[188:191], v[220:223], v[140:143]\n"
    "ds_write_addtid_b32 v171 offset:4864\n"
    "v_mfma_f32_16x16x32_bf16 v[52:55], a[188:191], v[212:215], v[52:55]\n"
    "ds_write_addtid_b32 v162 offset:512\n"
    "v_mfma_f32_16x16x32_bf16 v[52:55], a[184:187], v[208:211], v[52:55]\n"
    "ds_write_addtid_b32 v163 offset:768\n"
    "v_mfma_f32_16x16x32_bf16 v[28:31], a[184:187], v[200:203], v[28:31]\n"
    "ds_read_b128 a[184:187], v243 offset:16384\n"
    "v_mfma_f32_16x16x32_bf16 v[28:31], a[188:191], v[204:207], v[28:31]\n"
    "s_waitcnt lgkmcnt(15)\n"
    "ds_read_b128 a[188:191], v242 offset:20480\n"
    "s_cbranch_scc0 .Lat_g\n"
    "s_mov_b32 s44, 1\n"
    "s_bitcmp0_b32 %1, 0\n"
    "s_cbranch_scc1 .Lat_p\n"
    ".Lat_tail:\n"
    "v_mfma_f32_16x16x32_bf16 v[216:219], a[160:163], a[92:95], 0\n"
    "s_mov_b32 s45, 5775360\n"
    "v_mbcnt_lo_u32_b32 v253, -1, 0\n"
    "v_mfma_f32_16x16x32_bf16 v[216:219], a[164:167], a[64:67], v[216:219]\n"
    "s_add_u32 m0, s43, 49152\n"
    "v_mbcnt_hi_u32_b32 v253, -1, v253\n"
    "v_mfma_f32_16x16x32_bf16 v[224:227], a[164:167], a[96:99], 0\n"
    "buffer_load_dwordx4 v248, s[8:11], s40 offen offset:0 sc1 lds\n"
    "v_mfma_f32_16x16x32_bf16 v[224:227], a[160:163], a[124:127], v[224:227]\n"
    "s_lshl_b32 s56, %2, 15\n"
    "v_and_b32_e32 v254, 3, v253\n"
    "v_mfma_f32_16x16x32_bf16 v[232:235], a[160:163], a[156:159], 0\n"
    "ds_read_b128 a[160:163], v243 offset:22528\n"
    "v_mfma_f32_16x16x32_bf16 v[232:235], a[164:167], a[128:131], v[232:235]\n"
    "s_waitcnt lgkmcnt(1)\n"
    "ds_read_b128 a[164:167], v240 offset:18432\n"
    "v_mfma_f32_16x16x32_bf16 v[232:235], a[168:171], a[132:135], v[232:235]\n"
    "s_barrier\n"
    "v_mfma_f32_16x16x32_bf16 v[232:235], a[172:175], a[136:139], v[232:235]\n"
    "buffer_load_dwordx4 v248, s[8:11], s40 offen offset:1024 sc1 lds\n"
    "v_mfma_f32_16x16x32_bf16 v[224:227], a[172:175], a[104:107], v[224:227]\n"
    "s_add_u32 s57, s56, 5505024\n"
    "v_lshlrev_b32_e32 v254, 2, v254\n"
    "v_mfma_f32_16x16x32_bf16 v[224:227], a[168:171], a[100:103], v[224:227]\n"
    "s_add_u32 s56, s56, 5373952\n"
    "ds_read2st64_b32 v[164:165], v250 offset0:0 offset1:1\n"
    "v_and_b32_e32 v158, 15, v253\n"
    "v_mfma_f32_16x16x32_bf16 v[216:219], a[168:171], a[68:71], v[216:219]\n"
    "ds_read_b128 a[168:171], v241 offset:18432\n"
    "v_mfma_f32_16x16x32_bf16 v[216:219], a[172:175], a[72:75], v[216:219]\n"
    "ds_read_b128 a[172:175], v240 offset:22528\n"
    "v_mfma_f32_16x16x32_bf16 v[216:219], a[176:179], a[76:79], v[216:219]\n"
    "buffer_load_dwordx4 v248, s[8:11], s40 offen offset:2048 sc1 lds\n"
    "ds_read2st64_b32 v[166:167], v250 offset0:2 offset1:3\n"
    "v_mfma_f32_16x16x32_bf16 v[216:219], a[180:183], a[80:83], v[216:219]\n"
    "s_lshl_b32 s47, %2, 7\n"
    "v_lshlrev_b32_e32 v158, 9, v158\n"
    "v_mfma_f32_16x16x32_bf16 v[224:227], a[180:183], a[112:115], v[224:227]\n"
    "s_add_u32 s47, s47, s35\n"
    "v_and_or_b32 v158, v253, 48, v158\n"
    "v_mfma_f32_16x16x32_bf16 v[224:227], a[176:179], a[108:111], v[224:227]\n"
    "ds_read2st64_b32 v[172:173], v250 offset0:16 offset1:17\n"
    "v_bfe_u32 v252, v253, 4, 2\n"
    "v_mfma_f32_16x16x32_bf16 v[232:235], a[176:179], a[140:143], v[232:235]\n"
    "ds_read_b128 a[176:179], v241 offset:22528\n"
    "buffer_load_dwordx4 v248, s[8:11], s40 offen offset:3072 sc1 lds\n"
    "v_mfma_f32_16x16x32_bf16 v[232:235], a[180:183], a[144:147], v[232:235]\n"
    "s_add_u32 s40, s40, 32768\n"
    "ds_read_b128 a[180:183], v242 offset:18432\n"
    "v_mfma_f32_16x16x32_bf16 v[232:235], a[184:187], a[148:151], v[232:235]\n"
    "s_add_u32 m0, s43, 53248\n"
    "v_lshlrev_b32_e32 v252, 1, v252\n"
    "v_mfma_f32_16x16x32_bf16 v[232:235], a[188:191], a[152:155], v[232:235]\n"
    "ds_read2st64_b32 v[174:175], v250 offset0:18 offset1:19\n"
    "v_xor_b32_e32 v252, v252, v253\n"
    "v_mfma_f32_16x16x32_bf16 v[224:227], a[188:191], a[120:123], v[224:227]\n"
    "buffer_load_dwordx4 v248, s[8:11], s41 offen offset:0 sc1 lds\n"
    "v_mfma_f32_16x16x32_bf16 v[224:227], a[184:187], a[116:119], v[224:227]\n"
    "v_and_b32_e32 v252, 7, v252\n"
    "v_mfma_f32_16x16x32_bf16 v[216:219], a[184:187], a[84:87], v[216:219]\n"
    "ds_read_b128 a[184:187], v243 offset:18432\n"
    "v_mfma_f32_16x16x32_bf16 v[216:219], a[188:191], a[88:91], v[216:219]\n"
    "s_waitcnt lgkmcnt(9)\n"
    "ds_read_b128 a[188:191], v242 offset:22528\n"
    "v_mfma_f32_16x16x32_bf16 v[220:223], a[160:163], a[92:95], 0\n"
    "buffer_load_dwordx4 v248, s[8:11], s41 offen offset:1024 sc1 lds\n"
    "ds_read2st64_b32 v[180:181], v250 offset0:32 offset1:33\n"
    "v_mfma_f32_16x16x32_bf16 v[220:223], a[164:167], a[64:67], v[220:223]\n"
    "v_lshlrev_b32_e32 v252, 4, v252\n"
    "v_mfma_f32_16x16x32_bf16 v[228:231], a[164:167], a[96:99], 0\n"
    "ds_read2st64_b32 v[182:183], v250 offset0:34 offset1:35\n"
    "v_lshrrev_b32_e32 v255, 3, v253\n"
    "v_mfma_f32_16x16x32_bf16 v[228:231], a[160:163], a[124:127], v[228:231]\n"
    "ds_read2st64_b32 v[188:189], v250 offset0:48 offset1:49\n"
    "v_lshl_add_u32 v252, v255, 9, v252\n"
    "v_mfma_f32_16x16x32_bf16 v[236:239], a[160:163], a[156:159], 0\n"
    "ds_read_b64_tr_b16 a[160:161], v244 offset:0\n"
    "buffer_load_dwordx4 v248, s[8:11], s41 offen offset:2048 sc1 lds\n"
    "v_mfma_f32_16x16x32_bf16 v[236:239], a[164:167], a[128:131], v[236:239]\n"
    "s_waitcnt lgkmcnt(11)\n"
    "ds_read_b64_tr_b16 a[162:163], v244 offset:2048\n"
    "v_mfma_f32_16x16x32_bf16 v[236:239], a[168:171], a[132:135], v[236:239]\n"
    "ds_read_b64_tr_b16 a[164:165], v244 offset:8192\n"
    "v_mfma_f32_16x16x32_bf16 v[236:239], a[172:175], a[136:139], v[236:239]\n"
    "ds_read_b64_tr_b16 a[166:167], v244 offset:10240\n"
    "v_mfma_f32_16x16x32_bf16 v[228:231], a[172:175], a[104:107], v[228:231]\n"
    "buffer_load_dwordx4 v248, s[8:11], s41 offen offset:3072 sc1 lds\n"
    "ds_read2st64_b32 v[190:191], v250 offset0:50 offset1:51\n"
    "v_mfma_f32_16x16x32_bf16 v[228:231], a[168:171], a[100:103], v[228:231]\n"
    "s_add_u32 s41, s41, 32768\n"
    "s_add_u32 s61, s47, 0\n"
    "v_lshlrev_b32_e32 v253, 4, v253\n"
    "v_mfma_f32_16x16x32_bf16 v[220:223], a[168:171], a[68:71], v[220:223]\n"
    "s_mov_b32 m0, s49\n"
    "ds_read_b64_tr_b16 a[168:169], v245 offset:0\n"
    "v_mfma_f32_16x16x32_bf16 v[220:223], a[172:175], a[72:75], v[220:223]\n"
    "s_waitcnt lgkmcnt(12)\n"
    "ds_read_b64_tr_b16 a[170:171], v245 offset:2048\n"
    "v_mfma_f32_16x16x32_bf16 v[220:223], a[176:179], a[76:79], v[220:223]\n"
    "ds_read_b64_tr_b16 a[172:173], v245 offset:8192\n"
    "buffer_load_dwordx4 a[224:227], v158, s[12:15], s61 offen offset:0\n"
    "v_mfma_f32_16x16x32_bf16 v[220:223], a[180:183], a[80:83], v[220:223]\n"
    "ds_read_b64_tr_b16 a[174:175], v245 offset:10240\n"
    "v_mfma_f32_16x16x32_bf16 v[228:231], a[180:183], a[112:115], v[228:231]\n"
    "ds_read2st64_b32 v[196:197], v250 offset0:64 offset1:65\n"
    "v_mfma_f32_16x16x32_bf16 v[228:231], a[176:179], a[108:111], v[228:231]\n"
    "ds_read2st64_b32 v[198:199], v250 offset0:66 offset1:67\n"
    "v_mfma_f32_16x16x32_bf16 v[236:239], a[176:179], a[140:143], v[236:239]\n"
    "ds_read_b64_tr_b16 a[176:177], v246 offset:0\n"
    "v_mfma_f32_16x16x32_bf16 v[236:239], a[180:183], a[144:147], v[236:239]\n"
    "s_waitcnt lgkmcnt(15)\n"
    "ds_read_b64_tr_b16 a[178:179], v246 offset:2048\n"
    "v_mfma_f32_16x16x32_bf16 v[236:239], a[184:187], a[148:151], v[236:239]\n"
    "ds_read_b64_tr_b16 a[180:181], v246 offset:8192\n"
    "v_mfma_f32_16x16x32_bf16 v[236:239], a[188:191], a[152:155], v[236:239]\n"
    "ds_read_b64_tr_b16 a[182:183], v246 offset:10240\n"
    "v_mfma_f32_16x16x32_bf16 v[228:231], a[188:191], a[120:123], v[228:231]\n"
    "v_exp_f32_e32 v232, v232\n"
    "v_mfma_f32_16x16x32_bf16 v[228:231], a[184:187], a[116:119], v[228:231]\n"
    "v_exp_f32_e32 v233, v233\n"
    "v_mfma_f32_16x16x32_bf16 v[220:223], a[184:187], a[84:87], v[220:223]\n"
    "ds_read_b64_tr_b16 a[184:185], v247 offset:0\n"
    "v_mfma_f32_16x16x32_bf16 v[220:223], a[188:191], a[88:91], v[220:223]\n"
    "s_waitcnt lgkmcnt(5)\n"
    "ds_read_b64_tr_b16 a[186:187], v247 offset:2048\n"
    "v_mfma_f32_16x16x32_bf16 v[0:3], a[160:163], v[160:163], v[0:3]\n"
    "ds_read_b64_tr_b16 a[188:189], v247 offset:8192\n"
    "buffer_load_dwordx4 a[228:231], v158, s[12:15], s61 offen offset:64\n"
    "v_mfma_f32_16x16x32_bf16 v[0:3], a[164:167], v[164:167], v[0:3]\n"
    "s_add_u32 s61, s47, 8192\n"
    "ds_read_b64_tr_b16 a[190:191], v247 offset:10240\n"
    "v_mfma_f32_16x16x32_bf16 a[248:251], a[164:167], v[172:175], a[248:251]\n"
    "v_exp_f32_e32 v234, v234\n"
    "v_mfma_f32_16x16x32_bf16 a[248:251], a[160:163], v[168:171], a[248:251]\n"
    "v_exp_f32_e32 v235, v235\n"
    "v_mfma_f32_16x16x32_bf16 v[56:59], a[160:163], v[176:179], v[56:59]\n"
    "v_cvt_pk_bf16_f32 v232, v232, v233\n"
    "v_mfma_f32_16x16x32_bf16 v[56:59], a[164:167], v[180:183], v[56:59]\n"
    "v_exp_f32_e32 v224, v224\n"
    "v_mfma_f32_16x16x32_bf16 v[60:63], a[164:167], v[188:191], v[60:63]\n"
    "v_exp_f32_e32 v225, v225\n"
    "v_mfma_f32_16x16x32_bf16 v[60:63], a[160:163], v[184:187], v[60:63]\n"
    "v_cvt_pk_bf16_f32 v233, v234, v235\n"
    "v_mfma_f32_16x16x32_bf16 v[64:67], a[160:163], v[192:195], v[64:67]\n"
    "ds_read_b64_tr_b16 a[160:161], v244 offset:4096\n"
    "v_mfma_f32_16x16x32_bf16 v[64:67], a[164:167], v[196:199], v[64:67]\n"
    "ds_read_b64_tr_b16 a[162:163], v244 offset:6144\n"
    "v_mfma_f32_16x16x32_bf16 v[76:79], a[168:171], v[192:195], v[76:79]\n"
    "ds_read_b64_tr_b16 a[164:165], v244 offset:12288\n"
    "v_mfma_f32_16x16x32_bf16 v[76:79], a[172:175], v[196:199], v[76:79]\n"
    "ds_read_b64_tr_b16 a[166:167], v244 offset:14336\n"
    "v_mfma_f32_16x16x32_bf16 v[72:75], a[172:175], v[188:191], v[72:75]\n"
    "buffer_load_dwordx4 a[232:235], v158, s[12:15], s61 offen offset:0\n"
    "v_mfma_f32_16x16x32_bf16 v[72:75], a[168:171], v[184:187], v[72:75]\n"
    "s_waitcnt vmcnt(11)\n"
    "v_exp_f32_e32 v226, v226\n"
    "v_mfma_f32_16x16x32_bf16 v[68:71], a[168:171], v[176:179], v[68:71]\n"
    "s_barrier\n"
    "v_mfma_f32_16x16x32_bf16 v[68:71], a[172:175], v[180:183], v[68:71]\n"
    "v_exp_f32_e32 v227, v227\n"
    "v_mfma_f32_16x16x32_bf16 a[252:255], a[172:175], v[172:175], a[252:255]\n"
    "v_cvt_pk_bf16_f32 v224, v224, v225\n"
    "v_mfma_f32_16x16x32_bf16 a[252:255], a[168:171], v[168:171], a[252:255]\n"
    "v_exp_f32_e32 v216, v216\n"
    "v_mfma_f32_16x16x32_bf16 v[4:7], a[168:171], v[160:163], v[4:7]\n"
    "ds_read_b64_tr_b16 a[168:169], v245 offset:4096\n"
    "v_mfma_f32_16x16x32_bf16 v[4:7], a[172:175], v[164:167], v[4:7]\n"
    "s_waitcnt lgkmcnt(9)\n"
    "ds_read_b64_tr_b16 a[170:171], v245 offset:6144\n"
    "v_mfma_f32_16x16x32_bf16 v[8:11], a[176:179], v[160:163], v[8:11]\n"
    "ds_read_b64_tr_b16 a[172:173], v245 offset:12288\n"
    "v_mfma_f32_16x16x32_bf16 v[8:11], a[180:183], v[164:167], v[8:11]\n"
    "ds_read_b64_tr_b16 a[174:175], v245 offset:14336\n"
    "v_mfma_f32_16x16x32_bf16 v[32:35], a[180:183], v[172:175], v[32:35]\n"
    "v_exp_f32_e32 v217, v217\n"
    "v_mfma_f32_16x16x32_bf16 v[32:35], a[176:179], v[168:171], v[32:35]\n"
    "v_cvt_pk_bf16_f32 v225, v226, v227\n"
    "v_mfma_f32_16x16x32_bf16 v[80:83], a[176:179], v[176:179], v[80:83]\n"
    "buffer_load_dwordx4 a[236:239], v158, s[12:15], s61 offen offset:64\n"
    "v_mfma_f32_16x16x32_bf16 v[80:83], a[180:183], v[180:183], v[80:83]\n"
    "s_add_u32 s61, s47, 16384\n"
    "v_exp_f32_e32 v218, v218\n"
    "v_mfma_f32_16x16x32_bf16 v[84:87], a[180:183], v[188:191], v[84:87]\n"
    "v_exp_f32_e32 v219, v219\n"
    "v_mfma_f32_16x16x32_bf16 v[84:87], a[176:179], v[184:187], v[84:87]\n"
    "v_cvt_pk_bf16_f32 v216, v216, v217\n"
    "v_mfma_f32_16x16x32_bf16 v[88:91], a[176:179], v[192:195], v[88:91]\n"
    "ds_read_b64_tr_b16 a[176:177], v246 offset:4096\n"
    "v_mfma_f32_16x16x32_bf16 v[88:91], a[180:183], v[196:199], v[88:91]\n"
    "s_waitcnt lgkmcnt(9)\n"
    "ds_read_b64_tr_b16 a[178:179], v246 offset:6144\n"
    "v_mfma_f32_16x16x32_bf16 v[100:103], a[184:187], v[192:195], v[100:103]\n"
    "ds_read_b64_tr_b16 a[180:181], v246 offset:12288\n"
    "v_mfma_f32_16x16x32_bf16 v[100:103], a[188:191], v[196:199], v[100:103]\n"
    "ds_read_b64_tr_b16 a[182:183], v246 offset:14336\n"
    "v_mfma_f32_16x16x32_bf16 v[96:99], a[188:191], v[188:191], v[96:99]\n"
    "v_cvt_pk_bf16_f32 v217, v218, v219\n"
    "v_mfma_f32_16x16x32_bf16 v[96:99], a[184:187], v[184:187], v[96:99]\n"
    "v_exp_f32_e32 v236, v236\n"
    "v_mfma_f32_16x16x32_bf16 v[92:95], a[184:187], v[176:179], v[92:95]\n"
    "v_exp_f32_e32 v237, v237\n"
    "v_mfma_f32_16x16x32_bf16 v[92:95], a[188:191], v[180:183], v[92:95]\n"
    "v_exp_f32_e32 v238, v238\n"
    "v_mfma_f32_16x16x32_bf16 v[36:39], a[188:191], v[172:175], v[36:39]\n"
    "buffer_load_dwordx4 a[240:243], v158, s[12:15], s61 offen offset:0\n"
    "v_mfma_f32_16x16x32_bf16 v[36:39], a[184:187], v[168:171], v[36:39]\n"
    "v_exp_f32_e32 v239, v239\n"
    "v_mfma_f32_16x16x32_bf16 v[12:15], a[184:187], v[160:163], v[12:15]\n"
    "ds_read_b64_tr_b16 a[184:185], v247 offset:4096\n"
    "v_mfma_f32_16x16x32_bf16 v[12:15], a[188:191], v[164:167], v[12:15]\n"
    "s_waitcnt lgkmcnt(9)\n"
    "ds_read_b64_tr_b16 a[186:187], v247 offset:6144\n"
    "v_mfma_f32_16x16x32_bf16 v[16:19], a[160:163], v[160:163], v[16:19]\n"
    "ds_read_b64_tr_b16 a[188:189], v247 offset:12288\n"
    "v_mfma_f32_16x16x32_bf16 v[16:19], a[164:167], v[164:167], v[16:19]\n"
    "ds_read_b64_tr_b16 a[190:191], v247 offset:14336\n"
    "v_mfma_f32_16x16x32_bf16 v[40:43], a[164:167], v[172:175], v[40:43]\n"
    "v_cvt_pk_bf16_f32 v234, v236, v237\n"
    "v_mfma_f32_16x16x32_bf16 v[40:43], a[160:163], v[168:171], v[40:43]\n"
    "v_exp_f32_e32 v228, v228\n"
    "v_mfma_f32_16x16x32_bf16 v[104:107], a[160:163], v[176:179], v[104:107]\n"
    "ds_write_addtid_b32 v232 offset:16384\n"
    "v_mfma_f32_16x16x32_bf16 v[104:107], a[164:167], v[180:183], v[104:107]\n"
    "ds_write_addtid_b32 v233 offset:16640\n"
    "v_mfma_f32_16x16x32_bf16 v[108:111], a[164:167], v[188:191], v[108:111]\n"
    "v_exp_f32_e32 v229, v229\n"
    "v_mfma_f32_16x16x32_bf16 v[108:111], a[160:163], v[184:187], v[108:111]\n"
    "ds_write_addtid_b32 v224 offset:12288\n"
    "v_mfma_f32_16x16x32_bf16 v[112:115], a[160:163], v[192:195], v[112:115]\n"
    "ds_read_b128 a[160:163], v243 offset:36864\n"
    "buffer_load_dwordx4 a[244:247], v158, s[12:15], s61 offen offset:64\n"
    "v_mfma_f32_16x16x32_bf16 v[112:115], a[164:167], v[196:199], v[112:115]\n"
    "s_waitcnt lgkmcnt(12)\n"
    "ds_read_b128 a[164:167], v240 offset:32768\n"
    "v_mfma_f32_16x16x32_bf16 v[124:127], a[168:171], v[192:195], v[124:127]\n"
    "v_cvt_pk_bf16_f32 v235, v238, v239\n"
    "v_mfma_f32_16x16x32_bf16 v[124:127], a[172:175], v[196:199], v[124:127]\n"
    "ds_write_addtid_b32 v225 offset:12544\n"
    "v_mfma_f32_16x16x32_bf16 v[120:123], a[172:175], v[188:191], v[120:123]\n"
    "v_exp_f32_e32 v230, v230\n"
    "v_mfma_f32_16x16x32_bf16 v[120:123], a[168:171], v[184:187], v[120:123]\n"
    "ds_write_addtid_b32 v216 offset:8192\n"
    "v_mfma_f32_16x16x32_bf16 v[116:119], a[168:171], v[176:179], v[116:119]\n"
    "ds_write_addtid_b32 v217 offset:8448\n"
    "v_mfma_f32_16x16x32_bf16 v[116:119], a[172:175], v[180:183], v[116:119]\n"
    "v_exp_f32_e32 v231, v231\n"
    "v_mfma_f32_16x16x32_bf16 v[44:47], a[172:175], v[172:175], v[44:47]\n"
    "ds_write_addtid_b32 v234 offset:16896\n"
    "v_mfma_f32_16x16x32_bf16 v[44:47], a[168:171], v[168:171], v[44:47]\n"
    "v_cvt_pk_bf16_f32 v226, v228, v229\n"
    "v_mfma_f32_16x16x32_bf16 v[20:23], a[168:171], v[160:163], v[20:23]\n"
    "ds_read_b128 a[168:171], v241 offset:32768\n"
    "v_mfma_f32_16x16x32_bf16 v[20:23], a[172:175], v[164:167], v[20:23]\n"
    "s_waitcnt lgkmcnt(14)\n"
    "ds_read_b128 a[172:175], v240 offset:36864\n"
    "v_mfma_f32_16x16x32_bf16 v[24:27], a[176:179], v[160:163], v[24:27]\n"
    "v_mfma_f32_16x16x32_bf16 v[24:27], a[180:183], v[164:167], v[24:27]\n"
    "v_exp_f32_e32 v220, v220\n"
    "v_mfma_f32_16x16x32_bf16 v[48:51], a[180:183], v[172:175], v[48:51]\n"
    "v_mfma_f32_16x16x32_bf16 v[48:51], a[176:179], v[168:171], v[48:51]\n"
    "ds_write_addtid_b32 v235 offset:17152\n"
    "v_mfma_f32_16x16x32_bf16 v[128:131], a[176:179], v[176:179], v[128:131]\n"
    "v_exp_f32_e32 v221, v221\n"
    "v_mfma_f32_16x16x32_bf16 v[128:131], a[180:183], v[180:183], v[128:131]\n"
    "v_mfma_f32_16x16x32_bf16 v[132:135], a[180:183], v[188:191], v[132:135]\n"
    "ds_write_addtid_b32 v226 offset:12800\n"
    "v_mfma_f32_16x16x32_bf16 v[132:135], a[176:179], v[184:187], v[132:135]\n"
    "v_cvt_pk_bf16_f32 v227, v230, v231\n"
    "v_mfma_f32_16x16x32_bf16 v[136:139], a[176:179], v[192:195], v[136:139]\n"
    "ds_read_b128 a[176:179], v241 offset:36864\n"
    "v_mfma_f32_16x16x32_bf16 v[136:139], a[180:183], v[196:199], v[136:139]\n"
    "s_waitcnt lgkmcnt(14)\n"
    "ds_read_b128 a[180:183], v242 offset:32768\n"
    "v_mfma_f32_16x16x32_bf16 v[148:151], a[184:187], v[192:195], v[148:151]\n"
    "v_exp_f32_e32 v222, v222\n"
    "v_mfma_f32_16x16x32_bf16 v[148:151], a[188:191], v[196:199], v[148:151]\n"
    "v_mfma_f32_16x16x32_bf16 v[144:147], a[188:191], v[188:191], v[144:147]\n"
    "ds_write_addtid_b32 v227 offset:13056\n"
    "v_mfma_f32_16x16x32_bf16 v[144:147], a[184:187], v[184:187], v[144:147]\n"
    "v_exp_f32_e32 v223, v223\n"
    "v_mfma_f32_16x16x32_bf16 v[140:143], a[184:187], v[176:179], v[140:143]\n"
    "v_mfma_f32_16x16x32_bf16 v[140:143], a[188:191], v[180:183], v[140:143]\n"
    "v_mfma_f32_16x16x32_bf16 v[52:55], a[188:191], v[172:175], v[52:55]\n"
    "v_cvt_pk_bf16_f32 v218, v220, v221\n"
    "v_mfma_f32_16x16x32_bf16 v[52:55], a[184:187], v[168:171], v[52:55]\n"
    "v_cvt_pk_bf16_f32 v219, v222, v223\n"
    "v_mfma_f32_16x16x32_bf16 v[28:31], a[184:187], v[160:163], v[28:31]\n"
    "ds_read_b128 a[184:187], v243 offset:32768\n"
    "v_mfma_f32_16x16x32_bf16 v[28:31], a[188:191], v[164:167], v[28:31]\n"
    "s_waitcnt lgkmcnt(12)\n"
    "ds_read_b128 a[188:191], v242 offset:36864\n"
    "v_mfma_f32_16x16x32_bf16 v[176:179], a[160:163], a[92:95], 0\n"
    "ds_write_addtid_b32 v218 offset:8704\n"
    "v_mfma_f32_16x16x32_bf16 v[176:179], a[164:167], a[64:67], v[176:179]\n"
    "v_mfma_f32_16x16x32_bf16 v[184:187], a[164:167], a[96:99], 0\n"
    "v_mfma_f32_16x16x32_bf16 v[184:187], a[160:163], a[124:127], v[184:187]\n"
    "ds_write_addtid_b32 v219 offset:8960\n"
    "v_mfma_f32_16x16x32_bf16 v[192:195], a[160:163], a[156:159], 0\n"
    "ds_read_b128 a[160:163], v243 offset:38912\n"
    "v_mfma_f32_16x16x32_bf16 v[192:195], a[164:167], a[128:131], v[192:195]\n"
    "s_waitcnt lgkmcnt(10)\n"
    "ds_read_b128 a[164:167], v240 offset:34816\n"
    "v_mfma_f32_16x16x32_bf16 v[192:195], a[168:171], a[132:135], v[192:195]\n"
    "v_mfma_f32_16x16x32_bf16 v[192:195], a[172:175], a[136:139], v[192:195]\n"
    "s_waitcnt lgkmcnt(2)\n"
    "v_mfma_f32_16x16x32_bf16 v[184:187], a[172:175], a[104:107], v[184:187]\n"
    "s_barrier\n"
    "v_mfma_f32_16x16x32_bf16 v[184:187], a[168:171], a[100:103], v[184:187]\n"
    "s_add_u32 m0, s43, 0\n"
    "v_mfma_f32_16x16x32_bf16 v[176:179], a[168:171], a[68:71], v[176:179]\n"
    "ds_read_b128 a[168:171], v241 offset:34816\n"
    "buffer_load_dwordx4 v248, s[8:11], s40 offen offset:0 sc1 lds\n"
    "v_mfma_f32_16x16x32_bf16 v[176:179], a[172:175], a[72:75], v[176:179]\n"
    "ds_read_b128 a[172:175], v240 offset:38912\n"
    "v_mfma_f32_16x16x32_bf16 v[176:179], a[176:179], a[76:79], v[176:179]\n"
    "ds_read2st64_b32 v[220:221], v250 offset0:32 offset1:33\n"
    "v_mfma_f32_16x16x32_bf16 v[176:179], a[180:183], a[80:83], v[176:179]\n"
    "v_mfma_f32_16x16x32_bf16 v[184:187], a[180:183], a[112:115], v[184:187]\n"
    "buffer_load_dwordx4 v248, s[8:11], s40 offen offset:1024 sc1 lds\n"
    "v_mfma_f32_16x16x32_bf16 v[184:187], a[176:179], a[108:111], v[184:187]\n"
    "v_mfma_f32_16x16x32_bf16 v[192:195], a[176:179], a[140:143], v[192:195]\n"
    "ds_read_b128 a[176:179], v241 offset:38912\n"
    "v_mfma_f32_16x16x32_bf16 v[192:195], a[180:183], a[144:147], v[192:195]\n"
    "ds_read_b128 a[180:183], v242 offset:34816\n"
    "v_mfma_f32_16x16x32_bf16 v[192:195], a[184:187], a[148:151], v[192:195]\n"
    "buffer_load_dwordx4 v248, s[8:11], s40 offen offset:2048 sc1 lds\n"
    "v_mfma_f32_16x16x32_bf16 v[192:195], a[188:191], a[152:155], v[192:195]\n"
    "ds_read2st64_b32 v[222:223], v250 offset0:34 offset1:35\n"
    "v_mfma_f32_16x16x32_bf16 v[184:187], a[188:191], a[120:123], v[184:187]\n"
    "v_mfma_f32_16x16x32_bf16 v[184:187], a[184:187], a[116:119], v[184:187]\n"
    "v_exp_f32_e32 v192, v192\n"
    "v_mfma_f32_16x16x32_bf16 v[176:179], a[184:187], a[84:87], v[176:179]\n"
    "ds_read_b128 a[184:187], v243 offset:34816\n"
    "buffer_load_dwordx4 v248, s[8:11], s40 offen offset:3072 sc1 lds\n"
    "v_mfma_f32_16x16x32_bf16 v[176:179], a[188:191], a[88:91], v[176:179]\n"
    "s_waitcnt lgkmcnt(7)\n"
    "s_add_u32 s40, s40, 32768\n"
    "ds_read_b128 a[188:191], v242 offset:38912\n"
    "v_mfma_f32_16x16x32_bf16 v[180:183], a[160:163], a[92:95], 0\n"
    "s_add_u32 m0, s43, 4096\n"
    "ds_read2st64_b32 v[228:229], v250 offset0:48 offset1:49\n"
    "v_mfma_f32_16x16x32_bf16 v[180:183], a[164:167], a[64:67], v[180:183]\n"
    "v_exp_f32_e32 v193, v193\n"
    "v_mfma_f32_16x16x32_bf16 v[188:191], a[164:167], a[96:99], 0\n"
    "buffer_load_dwordx4 v248, s[8:11], s41 offen offset:0 sc1 lds\n"
    "v_exp_f32_e32 v194, v194\n"
    "v_mfma_f32_16x16x32_bf16 v[188:191], a[160:163], a[124:127], v[188:191]\n"
    "ds_read2st64_b32 v[230:231], v250 offset0:50 offset1:51\n"
    "v_mfma_f32_16x16x32_bf16 v[196:199], a[160:163], a[156:159], 0\n"
    "ds_read_b64_tr_b16 a[160:161], v244 offset:16384\n"
    "v_mfma_f32_16x16x32_bf16 v[196:199], a[164:167], a[128:131], v[196:199]\n"
    "s_waitcnt lgkmcnt(9)\n"
    "ds_read_b64_tr_b16 a[162:163], v244 offset:18432\n"
    "v_mfma_f32_16x16x32_bf16 v[196:199], a[168:171], a[132:135], v[196:199]\n"
    "ds_read_b64_tr_b16 a[164:165], v244 offset:24576\n"
    "buffer_load_dwordx4 v248, s[8:11], s41 offen offset:1024 sc1 lds\n"
    "v_mfma_f32_16x16x32_bf16 v[196:199], a[172:175], a[136:139], v[196:199]\n"
    "ds_read_b64_tr_b16 a[166:167], v244 offset:26624\n"
    "v_mfma_f32_16x16x32_bf16 v[188:191], a[172:175], a[104:107], v[188:191]\n"
    "v_exp_f32_e32 v195, v195\n"
    "v_mfma_f32_16x16x32_bf16 v[188:191], a[168:171], a[100:103], v[188:191]\n"
    "ds_read2st64_b32 v[236:237], v250 offset0:64 offset1:65\n"
    "v_cvt_pk_bf16_f32 v192, v192, v193\n"
    "v_mfma_f32_16x16x32_bf16 v[180:183], a[168:171], a[68:71], v[180:183]\n"
    "ds_read_b64_tr_b16 a[168:169], v245 offset:16384\n"
    "buffer_load_dwordx4 v248, s[8:11], s41 offen offset:2048 sc1 lds\n"
    "v_mfma_f32_16x16x32_bf16 v[180:183], a[172:175], a[72:75], v[180:183]\n"
    "s_waitcnt lgkmcnt(11)\n"
    "ds_read_b64_tr_b16 a[170:171], v245 offset:18432\n"
    "v_mfma_f32_16x16x32_bf16 v[180:183], a[176:179], a[76:79], v[180:183]\n"
    "ds_read_b64_tr_b16 a[172:173], v245 offset:24576\n"
    "v_mfma_f32_16x16x32_bf16 v[180:183], a[180:183], a[80:83], v[180:183]\n"
    "ds_read_b64_tr_b16 a[174:175], v245 offset:26624\n"
    "v_mfma_f32_16x16x32_bf16 v[188:191], a[180:183], a[112:115], v[188:191]\n"
    "buffer_load_dwordx4 v248, s[8:11], s41 offen offset:3072 sc1 lds\n"
    "v_mfma_f32_16x16x32_bf16 v[188:191], a[176:179], a[108:111], v[188:191]\n"
    "s_add_u32 s41, s41, 32768\n"
    "ds_read2st64_b32 v[238:239], v250 offset0:66 offset1:67\n"
    "v_mfma_f32_16x16x32_bf16 v[196:199], a[176:179], a[140:143], v[196:199]\n"
    "s_mov_b32 m0, s49\n"
    "ds_read_b64_tr_b16 a[176:177], v246 offset:16384\n"
    "v_mfma_f32_16x16x32_bf16 v[196:199], a[180:183], a[144:147], v[196:199]\n"
    "s_waitcnt lgkmcnt(13)\n"
    "ds_read_b64_tr_b16 a[178:179], v246 offset:18432\n"
    "v_mfma_f32_16x16x32_bf16 v[196:199], a[184:187], a[148:151], v[196:199]\n"
    "ds_read_b64_tr_b16 a[180:181], v246 offset:24576\n"
    "v_mfma_f32_16x16x32_bf16 v[196:199], a[188:191], a[152:155], v[196:199]\n"
    "ds_read_b64_tr_b16 a[182:183], v246 offset:26624\n"
    "v_mfma_f32_16x16x32_bf16 v[188:191], a[188:191], a[120:123], v[188:191]\n"
    "v_exp_f32_e32 v184, v184\n"
    "v_mfma_f32_16x16x32_bf16 v[188:191], a[184:187], a[116:119], v[188:191]\n"
    "v_exp_f32_e32 v185, v185\n"
    "v_mfma_f32_16x16x32_bf16 v[180:183], a[184:187], a[84:87], v[180:183]\n"
    "ds_read_b64_tr_b16 a[184:185], v247 offset:16384\n"
    "v_cvt_pk_bf16_f32 v193, v194, v195\n"
    "v_mfma_f32_16x16x32_bf16 v[180:183], a[188:191], a[88:91], v[180:183]\n"
    "s_waitcnt lgkmcnt(5)\n"
    "ds_read_b64_tr_b16 a[186:187], v247 offset:18432\n"
    "v_mfma_f32_16x16x32_bf16 v[56:59], a[160:163], v[216:219], v[56:59]\n"
    "ds_read_b64_tr_b16 a[188:189], v247 offset:24576\n"
    "v_mfma_f32_16x16x32_bf16 v[56:59], a[164:167], v[220:223], v[56:59]\n"
    "ds_read_b64_tr_b16 a[190:191], v247 offset:26624\n"
    "v_mfma_f32_16x16x32_bf16 v[60:63], a[164:167], v[228:231], v[60:63]\n"
    "v_exp_f32_e32 v186, v186\n"
    "v_mfma_f32_16x16x32_bf16 v[60:63], a[160:163], v[224:227], v[60:63]\n"
    "v_exp_f32_e32 v187, v187\n"
    "v_mfma_f32_16x16x32_bf16 v[64:67], a[160:163], v[232:235], v[64:67]\n"
    "ds_read_b64_tr_b16 a[160:161], v244 offset:20480\n"
    "v_cvt_pk_bf16_f32 v184, v184, v185\n"
    "v_mfma_f32_16x16x32_bf16 v[64:67], a[164:167], v[236:239], v[64:67]\n"
    "ds_read_b64_tr_b16 a[162:163], v244 offset:22528\n"
    "v_mfma_f32_16x16x32_bf16 v[76:79], a[168:171], v[232:235], v[76:79]\n"
    "ds_read_b64_tr_b16 a[164:165], v244 offset:28672\n"
    "v_mfma_f32_16x16x32_bf16 v[76:79], a[172:175], v[236:239], v[76:79]\n"
    "ds_read_b64_tr_b16 a[166:167], v244 offset:30720\n"
    "v_mfma_f32_16x16x32_bf16 v[72:75], a[172:175], v[228:231], v[72:75]\n"
    "v_exp_f32_e32 v176, v176\n"
    "v_mfma_f32_16x16x32_bf16 v[72:75], a[168:171], v[224:227], v[72:75]\n"
    "v_exp_f32_e32 v177, v177\n"
    "v_mfma_f32_16x16x32_bf16 v[68:71], a[168:171], v[216:219], v[68:71]\n"
    "ds_read_b64_tr_b16 a[168:169], v245 offset:20480\n"
    "v_cvt_pk_bf16_f32 v185, v186, v187\n"
    "v_mfma_f32_16x16x32_bf16 v[68:71], a[172:175], v[220:223], v[68:71]\n"
    "s_waitcnt lgkmcnt(9)\n"
    "ds_read_b64_tr_b16 a[170:171], v245 offset:22528\n"
    "v_mfma_f32_16x16x32_bf16 v[80:83], a[176:179], v[216:219], v[80:83]\n"
    "ds_read_b64_tr_b16 a[172:173], v245 offset:28672\n"
    "v_mfma_f32_16x16x32_bf16 v[80:83], a[180:183], v[220:223], v[80:83]\n"
    "ds_read_b64_tr_b16 a[174:175], v245 offset:30720\n"
    "v_mfma_f32_16x16x32_bf16 v[84:87], a[180:183], v[228:231], v[84:87]\n"
    "v_exp_f32_e32 v178, v178\n"
    "v_mfma_f32_16x16x32_bf16 v[84:87], a[176:179], v[224:227], v[84:87]\n"
    "s_waitcnt vmcnt(14)\n"
    "v_exp_f32_e32 v179, v179\n"
    "v_mfma_f32_16x16x32_bf16 v[88:91], a[176:179], v[232:235], v[88:91]\n"
    "s_barrier\n"
    "v_mfma_f32_16x16x32_bf16 v[88:91], a[180:183], v[236:239], v[88:91]\n"
    "s_waitcnt lgkmcnt(8)\n"
    "ds_read_b64_tr_b16 a[176:177], v246 offset:20480\n"
    "v_cvt_pk_bf16_f32 v176, v176, v177\n"
    "v_mfma_f32_16x16x32_bf16 v[100:103], a[184:187], v[232:235], v[100:103]\n"
    "ds_read_b64_tr_b16 a[178:179], v246 offset:22528\n"
    "v_cvt_pk_bf16_f32 v177, v178, v179\n"
    "v_mfma_f32_16x16x32_bf16 v[100:103], a[188:191], v[236:239], v[100:103]\n"
    "ds_read_b64_tr_b16 a[180:181], v246 offset:28672\n"
    "v_mfma_f32_16x16x32_bf16 v[96:99], a[188:191], v[228:231], v[96:99]\n"
    "ds_read_b64_tr_b16 a[182:183], v246 offset:30720\n"
    "v_mfma_f32_16x16x32_bf16 v[96:99], a[184:187], v[224:227], v[96:99]\n"
    "v_exp_f32_e32 v196, v196\n"
    "v_mfma_f32_16x16x32_bf16 v[92:95], a[184:187], v[216:219], v[92:95]\n"
    "ds_read_b64_tr_b16 a[184:185], v247 offset:20480\n"
    "ds_write_addtid_b32 v192 offset:16384\n"
    "v_mfma_f32_16x16x32_bf16 v[92:95], a[188:191], v[220:223], v[92:95]\n"
    "s_waitcnt lgkmcnt(10)\n"
    "ds_read_b64_tr_b16 a[186:187], v247 offset:22528\n"
    "v_mfma_f32_16x16x32_bf16 v[104:107], a[160:163], v[216:219], v[104:107]\n"
    "ds_read_b64_tr_b16 a[188:189], v247 offset:28672\n"
    "ds_write_addtid_b32 v193 offset:16640\n"
    "v_mfma_f32_16x16x32_bf16 v[104:107], a[164:167], v[220:223], v[104:107]\n"
    "ds_read_b64_tr_b16 a[190:191], v247 offset:30720\n"
    "v_mfma_f32_16x16x32_bf16 v[108:111], a[164:167], v[228:231], v[108:111]\n"
    "v_exp_f32_e32 v197, v197\n"
    "v_mfma_f32_16x16x32_bf16 v[108:111], a[160:163], v[224:227], v[108:111]\n"
    "v_exp_f32_e32 v198, v198\n"
    "v_mfma_f32_16x16x32_bf16 v[112:115], a[160:163], v[232:235], v[112:115]\n"
    "ds_read_b128 a[160:163], v243 offset:53248\n"
    "ds_write_addtid_b32 v184 offset:12288\n"
    "v_mfma_f32_16x16x32_bf16 v[112:115], a[164:167], v[236:239], v[112:115]\n"
    "s_waitcnt lgkmcnt(12)\n"
    "ds_read_b128 a[164:167], v240 offset:49152\n"
    "ds_write_addtid_b32 v185 offset:12544\n"
    "v_mfma_f32_16x16x32_bf16 v[124:127], a[168:171], v[232:235], v[124:127]\n"
    "v_exp_f32_e32 v199, v199\n"
    "v_mfma_f32_16x16x32_bf16 v[124:127], a[172:175], v[236:239], v[124:127]\n"
    "ds_write_addtid_b32 v176 offset:8192\n"
    "v_cvt_pk_bf16_f32 v194, v196, v197\n"
    "v_mfma_f32_16x16x32_bf16 v[120:123], a[172:175], v[228:231], v[120:123]\n"
    "v_exp_f32_e32 v188, v188\n"
    "v_mfma_f32_16x16x32_bf16 v[120:123], a[168:171], v[224:227], v[120:123]\n"
    "v_exp_f32_e32 v189, v189\n"
    "v_mfma_f32_16x16x32_bf16 v[116:119], a[168:171], v[216:219], v[116:119]\n"
    "ds_read_b128 a[168:171], v241 offset:49152\n"
    "v_cvt_pk_bf16_f32 v195, v198, v199\n"
    "v_mfma_f32_16x16x32_bf16 v[116:119], a[172:175], v[220:223], v[116:119]\n"
    "s_waitcnt lgkmcnt(12)\n"
    "ds_read_b128 a[172:175], v240 offset:53248\n"
    "ds_write_addtid_b32 v177 offset:8448\n"
    "v_mfma_f32_16x16x32_bf16 v[128:131], a[176:179], v[216:219], v[128:131]\n"
    "v_exp_f32_e32 v190, v190\n"
    "v_mfma_f32_16x16x32_bf16 v[128:131], a[180:183], v[220:223], v[128:131]\n"
    "v_exp_f32_e32 v191, v191\n"
    "v_mfma_f32_16x16x32_bf16 v[132:135], a[180:183], v[228:231], v[132:135]\n"
    "ds_write_addtid_b32 v194 offset:16896\n"
    "v_cvt_pk_bf16_f32 v186, v188, v189\n"
    "v_mfma_f32_16x16x32_bf16 v[132:135], a[176:179], v[224:227], v[132:135]\n"
    "v_exp_f32_e32 v180, v180\n"
    "v_mfma_f32_16x16x32_bf16 v[136:139], a[176:179], v[232:235], v[136:139]\n"
    "ds_read_b128 a[176:179], v241 offset:53248\n"
    "v_mfma_f32_16x16x32_bf16 v[136:139], a[180:183], v[236:239], v[136:139]\n"
    "s_waitcnt lgkmcnt(10)\n"
    "ds_read_b128 a[180:183], v242 offset:49152\n"
    "v_mfma_f32_16x16x32_bf16 v[148:151], a[184:187], v[232:235], v[148:151]\n"
    "v_exp_f32_e32 v181, v181\n"
    "v_mfma_f32_16x16x32_bf16 v[148:151], a[188:191], v[236:239], v[148:151]\n"
    "ds_write_addtid_b32 v195 offset:17152\n"
    "v_cvt_pk_bf16_f32 v187, v190, v191\n"
    "v_mfma_f32_16x16x32_bf16 v[144:147], a[188:191], v[228:231], v[144:147]\n"
    "v_exp_f32_e32 v182, v182\n"
    "v_mfma_f32_16x16x32_bf16 v[144:147], a[184:187], v[224:227], v[144:147]\n"
    "v_exp_f32_e32 v183, v183\n"
    "v_mfma_f32_16x16x32_bf16 v[140:143], a[184:187], v[216:219], v[140:143]\n"
    "ds_read_b128 a[184:187], v243 offset:49152\n"
    "v_cvt_pk_bf16_f32 v178, v180, v181\n"
    "v_mfma_f32_16x16x32_bf16 v[140:143], a[188:191], v[220:223], v[140:143]\n"
    "s_waitcnt lgkmcnt(10)\n"
    "ds_read_b128 a[188:191], v242 offset:53248\n"
    "v_cvt_pk_bf16_f32 v179, v182, v183\n"
    "v_mfma_f32_16x16x32_bf16 v[216:219], a[160:163], a[92:95], 0\n"
    "ds_write_addtid_b32 v186 offset:12800\n"
    "v_mfma_f32_16x16x32_bf16 v[216:219], a[164:167], a[64:67], v[216:219]\n"
    "ds_write_addtid_b32 v187 offset:13056\n"
    "v_mfma_f32_16x16x32_bf16 v[224:227], a[164:167], a[96:99], 0\n"
    "ds_write_addtid_b32 v178 offset:8704\n"
    "v_mfma_f32_16x16x32_bf16 v[224:227], a[160:163], a[124:127], v[224:227]\n"
    "ds_write_addtid_b32 v179 offset:8960\n"
    "v_mfma_f32_16x16x32_bf16 v[232:235], a[160:163], a[156:159], 0\n"
    "ds_read_b128 a[160:163], v243 offset:55296\n"
    "v_mfma_f32_16x16x32_bf16 v[232:235], a[164:167], a[128:131], v[232:235]\n"
    "s_waitcnt lgkmcnt(12)\n"
    "ds_read_b128 a[164:167], v240 offset:51200\n"
    "v_mfma_f32_16x16x32_bf16 v[232:235], a[168:171], a[132:135], v[232:235]\n"
    "v_mfma_f32_16x16x32_bf16 v[232:235], a[172:175], a[136:139], v[232:235]\n"
    "s_waitcnt lgkmcnt(2)\n"
    "v_mfma_f32_16x16x32_bf16 v[224:227], a[172:175], a[104:107], v[224:227]\n"
    "s_barrier\n"
    "v_mfma_f32_16x16x32_bf16 v[224:227], a[168:171], a[100:103], v[224:227]\n"
    "s_add_u32 m0, s43, 16384\n"
    "v_mfma_f32_16x16x32_bf16 v[216:219], a[168:171], a[68:71], v[216:219]\n"
    "ds_read_b128 a[168:171], v241 offset:51200\n"
    "buffer_load_dwordx4 v248, s[8:11], s40 offen offset:0 sc1 lds\n"
    "v_mfma_f32_16x16x32_bf16 v[216:219], a[172:175], a[72:75], v[216:219]\n"
    "ds_read_b128 a[172:175], v240 offset:55296\n"
    "v_mfma_f32_16x16x32_bf16 v[216:219], a[176:179], a[76:79], v[216:219]\n"
    "ds_read2st64_b32 v[180:181], v250 offset0:32 offset1:33\n"
    "v_mfma_f32_16x16x32_bf16 v[216:219], a[180:183], a[80:83], v[216:219]\n"
    "v_mfma_f32_16x16x32_bf16 v[224:227], a[180:183], a[112:115], v[224:227]\n"
    "buffer_load_dwordx4 v248, s[8:11], s40 offen offset:1024 sc1 lds\n"
    "v_mfma_f32_16x16x32_bf16 v[224:227], a[176:179], a[108:111], v[224:227]\n"
    "v_mfma_f32_16x16x32_bf16 v[232:235], a[176:179], a[140:143], v[232:235]\n"
    "ds_read_b128 a[176:179], v241 offset:55296\n"
    "v_mfma_f32_16x16x32_bf16 v[232:235], a[180:183], a[144:147], v[232:235]\n"
    "ds_read_b128 a[180:183], v242 offset:51200\n"
    "v_mfma_f32_16x16x32_bf16 v[232:235], a[184:187], a[148:151], v[232:235]\n"
    "buffer_load_dwordx4 v248, s[8:11], s40 offen offset:2048 sc1 lds\n"
    "v_mfma_f32_16x16x32_bf16 v[232:235], a[188:191], a[152:155], v[232:235]\n"
    "ds_read2st64_b32 v[182:183], v250 offset0:34 offset1:35\n"
    "v_mfma_f32_16x16x32_bf16 v[224:227], a[188:191], a[120:123], v[224:227]\n"
    "v_mfma_f32_16x16x32_bf16 v[224:227], a[184:187], a[116:119], v[224:227]\n"
    "v_exp_f32_e32 v232, v232\n"
    "v_mfma_f32_16x16x32_bf16 v[216:219], a[184:187], a[84:87], v[216:219]\n"
    "ds_read_b128 a[184:187], v243 offset:51200\n"
    "buffer_load_dwordx4 v248, s[8:11], s40 offen offset:3072 sc1 lds\n"
    "v_mfma_f32_16x16x32_bf16 v[216:219], a[188:191], a[88:91], v[216:219]\n"
    "s_waitcnt lgkmcnt(7)\n"
    "s_add_u32 s40, s40, -131072\n"
    "ds_read_b128 a[188:191], v242 offset:55296\n"
    "v_mfma_f32_16x16x32_bf16 v[220:223], a[160:163], a[92:95], 0\n"
    "s_add_u32 m0, s43, 20480\n"
    "ds_read2st64_b32 v[188:189], v250 offset0:48 offset1:49\n"
    "v_mfma_f32_16x16x32_bf16 v[220:223], a[164:167], a[64:67], v[220:223]\n"
    "v_exp_f32_e32 v233, v233\n"
    "v_mfma_f32_16x16x32_bf16 v[228:231], a[164:167], a[96:99], 0\n"
    "buffer_load_dwordx4 v248, s[8:11], s41 offen offset:0 sc1 lds\n"
    "v_exp_f32_e32 v234, v234\n"
    "v_mfma_f32_16x16x32_bf16 v[228:231], a[160:163], a[124:127], v[228:231]\n"
    "ds_read2st64_b32 v[190:191], v250 offset0:50 offset1:51\n"
    "v_mfma_f32_16x16x32_bf16 v[236:239], a[160:163], a[156:159], 0\n"
    "ds_read_b64_tr_b16 a[160:161], v244 offset:32768\n"
    "v_mfma_f32_16x16x32_bf16 v[236:239], a[164:167], a[128:131], v[236:239]\n"
    "s_waitcnt lgkmcnt(9)\n"
    "ds_read_b64_tr_b16 a[162:163], v244 offset:34816\n"
    "v_mfma_f32_16x16x32_bf16 v[236:239], a[168:171], a[132:135], v[236:239]\n"
    "ds_read_b64_tr_b16 a[164:165], v244 offset:40960\n"
    "buffer_load_dwordx4 v248, s[8:11], s41 offen offset:1024 sc1 lds\n"
    "v_mfma_f32_16x16x32_bf16 v[236:239], a[172:175], a[136:139], v[236:239]\n"
    "ds_read_b64_tr_b16 a[166:167], v244 offset:43008\n"
    "v_mfma_f32_16x16x32_bf16 v[228:231], a[172:175], a[104:107], v[228:231]\n"
    "v_exp_f32_e32 v235, v235\n"
    "v_mfma_f32_16x16x32_bf16 v[228:231], a[168:171], a[100:103], v[228:231]\n"
    "ds_read2st64_b32 v[196:197], v250 offset0:64 offset1:65\n"
    "v_cvt_pk_bf16_f32 v232, v232, v233\n"
    "v_mfma_f32_16x16x32_bf16 v[220:223], a[168:171], a[68:71], v[220:223]\n"
    "ds_read_b64_tr_b16 a[168:169], v245 offset:32768\n"
    "buffer_load_dwordx4 v248, s[8:11], s41 offen offset:2048 sc1 lds\n"
    "v_mfma_f32_16x16x32_bf16 v[220:223], a[172:175], a[72:75], v[220:223]\n"
    "s_waitcnt lgkmcnt(11)\n"
    "ds_read_b64_tr_b16 a[170:171], v245 offset:34816\n"
    "v_mfma_f32_16x16x32_bf16 v[220:223], a[176:179], a[76:79], v[220:223]\n"
    "ds_read_b64_tr_b16 a[172:173], v245 offset:40960\n"
    "v_mfma_f32_16x16x32_bf16 v[220:223], a[180:183], a[80:83], v[220:223]\n"
    "ds_read_b64_tr_b16 a[174:175], v245 offset:43008\n"
    "v_mfma_f32_16x16x32_bf16 v[228:231], a[180:183], a[112:115], v[228:231]\n"
    "buffer_load_dwordx4 v248, s[8:11], s41 offen offset:3072 sc1 lds\n"
    "v_mfma_f32_16x16x32_bf16 v[228:231], a[176:179], a[108:111], v[228:231]\n"
    "s_add_u32 s41, s41, -131072\n"
    "ds_read2st64_b32 v[198:199], v250 offset0:66 offset1:67\n"
    "v_mfma_f32_16x16x32_bf16 v[236:239], a[176:179], a[140:143], v[236:239]\n"
    "s_mov_b32 m0, s49\n"
    "ds_read_b64_tr_b16 a[176:177], v246 offset:32768\n"
    "v_mfma_f32_16x16x32_bf16 v[236:239], a[180:183], a[144:147], v[236:239]\n"
    "s_waitcnt lgkmcnt(13)\n"
    "ds_read_b64_tr_b16 a[178:179], v246 offset:34816\n"
    "v_mfma_f32_16x16x32_bf16 v[236:239], a[184:187], a[148:151], v[236:239]\n"
    "ds_read_b64_tr_b16 a[180:181], v246 offset:40960\n"
    "buffer_load_dword v255, v254, s[8:11], s45 offen offset:0 sc1\n"
    "v_mfma_f32_16x16x32_bf16 v[236:239], a[188:191], a[152:155], v[236:239]\n"
    "ds_read_b64_tr_b16 a[182:183], v246 offset:43008\n"
    "v_mfma_f32_16x16x32_bf16 v[228:231], a[188:191], a[120:123], v[228:231]\n"
    "v_exp_f32_e32 v224, v224\n"
    "v_mfma_f32_16x16x32_bf16 v[228:231], a[184:187], a[116:119], v[228:231]\n"
    "v_exp_f32_e32 v225, v225\n"
    "v_mfma_f32_16x16x32_bf16 v[220:223], a[184:187], a[84:87], v[220:223]\n"
    "ds_read_b64_tr_b16 a[184:185], v247 offset:32768\n"
    "v_cvt_pk_bf16_f32 v233, v234, v235\n"
    "v_mfma_f32_16x16x32_bf16 v[220:223], a[188:191], a[88:91], v[220:223]\n"
    "s_waitcnt lgkmcnt(5)\n"
    "ds_read_b64_tr_b16 a[186:187], v247 offset:34816\n"
    "v_mfma_f32_16x16x32_bf16 v[56:59], a[160:163], v[176:179], v[56:59]\n"
    "ds_read_b64_tr_b16 a[188:189], v247 offset:40960\n"
    "v_mfma_f32_16x16x32_bf16 v[56:59], a[164:167], v[180:183], v[56:59]\n"
    "ds_read_b64_tr_b16 a[190:191], v247 offset:43008\n"
    "v_mfma_f32_16x16x32_bf16 v[60:63], a[164:167], v[188:191], v[60:63]\n"
    "v_exp_f32_e32 v226, v226\n"
    "v_mfma_f32_16x16x32_bf16 v[60:63], a[160:163], v[184:187], v[60:63]\n"
    "v_exp_f32_e32 v227, v227\n"
    "v_mfma_f32_16x16x32_bf16 v[64:67], a[160:163], v[192:195], v[64:67]\n"
    "ds_read_b64_tr_b16 a[160:161], v244 offset:36864\n"
    "v_cvt_pk_bf16_f32 v224, v224, v225\n"
    "v_mfma_f32_16x16x32_bf16 v[64:67], a[164:167], v[196:199], v[64:67]\n"
    "ds_read_b64_tr_b16 a[162:163], v244 offset:38912\n"
    "v_mfma_f32_16x16x32_bf16 v[76:79], a[168:171], v[192:195], v[76:79]\n"
    "ds_read_b64_tr_b16 a[164:165], v244 offset:45056\n"
    "v_mfma_f32_16x16x32_bf16 v[76:79], a[172:175], v[196:199], v[76:79]\n"
    "ds_read_b64_tr_b16 a[166:167], v244 offset:47104\n"
    "v_mfma_f32_16x16x32_bf16 v[72:75], a[172:175], v[188:191], v[72:75]\n"
    "v_exp_f32_e32 v216, v216\n"
    "v_mfma_f32_16x16x32_bf16 v[72:75], a[168:171], v[184:187], v[72:75]\n"
    "v_exp_f32_e32 v217, v217\n"
    "v_mfma_f32_16x16x32_bf16 v[68:71], a[168:171], v[176:179], v[68:71]\n"
    "ds_read_b64_tr_b16 a[168:169], v245 offset:36864\n"
    "v_cvt_pk_bf16_f32 v225, v226, v227\n"
    "v_mfma_f32_16x16x32_bf16 v[68:71], a[172:175], v[180:183], v[68:71]\n"
    "s_waitcnt lgkmcnt(9)\n"
    "ds_read_b64_tr_b16 a[170:171], v245 offset:38912\n"
    "v_mfma_f32_16x16x32_bf16 v[80:83], a[176:179], v[176:179], v[80:83]\n"
    "ds_read_b64_tr_b16 a[172:173], v245 offset:45056\n"
    "v_mfma_f32_16x16x32_bf16 v[80:83], a[180:183], v[180:183], v[80:83]\n"
    "ds_read_b64_tr_b16 a[174:175], v245 offset:47104\n"
    "v_mfma_f32_16x16x32_bf16 v[84:87], a[180:183], v[188:191], v[84:87]\n"
    "v_exp_f32_e32 v218, v218\n"
    "v_mfma_f32_16x16x32_bf16 v[84:87], a[176:179], v[184:187], v[84:87]\n"
    "s_waitcnt vmcnt(9)\n"
    "v_exp_f32_e32 v219, v219\n"
    "v_mfma_f32_16x16x32_bf16 v[88:91], a[176:179], v[192:195], v[88:91]\n"
    "s_barrier\n"
    "v_mfma_f32_16x16x32_bf16 v[88:91], a[180:183], v[196:199], v[88:91]\n"
    "s_waitcnt lgkmcnt(8)\n"
    "ds_read_b64_tr_b16 a[176:177], v246 offset:36864\n"
    "v_cvt_pk_bf16_f32 v216, v216, v217\n"
    "v_mfma_f32_16x16x32_bf16 v[100:103], a[184:187], v[192:195], v[100:103]\n"
    "ds_read_b64_tr_b16 a[178:179], v246 offset:38912\n"
    "v_cvt_pk_bf16_f32 v217, v218, v219\n"
    "v_mfma_f32_16x16x32_bf16 v[100:103], a[188:191], v[196:199], v[100:103]\n"
    "ds_read_b64_tr_b16 a[180:181], v246 offset:45056\n"
    "v_mfma_f32_16x16x32_bf16 v[96:99], a[188:191], v[188:191], v[96:99]\n"
    "ds_read_b64_tr_b16 a[182:183], v246 offset:47104\n"
    "v_mfma_f32_16x16x32_bf16 v[96:99], a[184:187], v[184:187], v[96:99]\n"
    "v_exp_f32_e32 v236, v236\n"
    "v_mfma_f32_16x16x32_bf16 v[92:95], a[184:187], v[176:179], v[92:95]\n"
    "ds_read_b64_tr_b16 a[184:185], v247 offset:36864\n"
    "ds_write_addtid_b32 v232 offset:16384\n"
    "v_mfma_f32_16x16x32_bf16 v[92:95], a[188:191], v[180:183], v[92:95]\n"
    "s_waitcnt lgkmcnt(10)\n"
    "ds_read_b64_tr_b16 a[186:187], v247 offset:38912\n"
    "v_mfma_f32_16x16x32_bf16 v[104:107], a[160:163], v[176:179], v[104:107]\n"
    "ds_read_b64_tr_b16 a[188:189], v247 offset:45056\n"
    "ds_write_addtid_b32 v233 offset:16640\n"
    "v_mfma_f32_16x16x32_bf16 v[104:107], a[164:167], v[180:183], v[104:107]\n"
    "ds_read_b64_tr_b16 a[190:191], v247 offset:47104\n"
    "v_mfma_f32_16x16x32_bf16 v[108:111], a[164:167], v[188:191], v[108:111]\n"
    "v_exp_f32_e32 v237, v237\n"
    "v_mfma_f32_16x16x32_bf16 v[108:111], a[160:163], v[184:187], v[108:111]\n"
    "v_exp_f32_e32 v238, v238\n"
    "v_mfma_f32_16x16x32_bf16 v[112:115], a[160:163], v[192:195], v[112:115]\n"
    "ds_read_b128 a[160:163], v243 offset:4096\n"
    "ds_write_addtid_b32 v224 offset:12288\n"
    "v_mfma_f32_16x16x32_bf16 v[112:115], a[164:167], v[196:199], v[112:115]\n"
    "s_waitcnt lgkmcnt(12)\n"
    "ds_read_b128 a[164:167], v240 offset:0\n"
    "ds_write_addtid_b32 v225 offset:12544\n"
    "v_mfma_f32_16x16x32_bf16 v[124:127], a[168:171], v[192:195], v[124:127]\n"
    "v_exp_f32_e32 v239, v239\n"
    "v_mfma_f32_16x16x32_bf16 v[124:127], a[172:175], v[196:199], v[124:127]\n"
    "ds_write_addtid_b32 v216 offset:8192\n"
    "v_cvt_pk_bf16_f32 v234, v236, v237\n"
    "v_mfma_f32_16x16x32_bf16 v[120:123], a[172:175], v[188:191], v[120:123]\n"
    "v_exp_f32_e32 v228, v228\n"
    "v_mfma_f32_16x16x32_bf16 v[120:123], a[168:171], v[184:187], v[120:123]\n"
    "v_exp_f32_e32 v229, v229\n"
    "v_mfma_f32_16x16x32_bf16 v[116:119], a[168:171], v[176:179], v[116:119]\n"
    "ds_read_b128 a[168:171], v241 offset:0\n"
    "v_cvt_pk_bf16_f32 v235, v238, v239\n"
    "v_mfma_f32_16x16x32_bf16 v[116:119], a[172:175], v[180:183], v[116:119]\n"
    "s_waitcnt lgkmcnt(12)\n"
    "ds_read_b128 a[172:175], v240 offset:4096\n"
    "ds_write_addtid_b32 v217 offset:8448\n"
    "v_mfma_f32_16x16x32_bf16 v[128:131], a[176:179], v[176:179], v[128:131]\n"
    "v_exp_f32_e32 v230, v230\n"
    "v_mfma_f32_16x16x32_bf16 v[128:131], a[180:183], v[180:183], v[128:131]\n"
    "v_exp_f32_e32 v231, v231\n"
    "v_mfma_f32_16x16x32_bf16 v[132:135], a[180:183], v[188:191], v[132:135]\n"
    "ds_write_addtid_b32 v234 offset:16896\n"
    "v_cvt_pk_bf16_f32 v226, v228, v229\n"
    "v_mfma_f32_16x16x32_bf16 v[132:135], a[176:179], v[184:187], v[132:135]\n"
    "v_exp_f32_e32 v220, v220\n"
    "v_mfma_f32_16x16x32_bf16 v[136:139], a[176:179], v[192:195], v[136:139]\n"
    "ds_read_b128 a[176:179], v241 offset:4096\n"
    "v_mfma_f32_16x16x32_bf16 v[136:139], a[180:183], v[196:199], v[136:139]\n"
    "s_waitcnt lgkmcnt(10)\n"
    "ds_read_b128 a[180:183], v242 offset:0\n"
    "ds_write_addtid_b32 v235 offset:17152\n"
    "v_mfma_f32_16x16x32_bf16 v[148:151], a[184:187], v[192:195], v[148:151]\n"
    "v_exp_f32_e32 v221, v221\n"
    "v_mfma_f32_16x16x32_bf16 v[148:151], a[188:191], v[196:199], v[148:151]\n"
    "ds_write_addtid_b32 v226 offset:12800\n"
    "v_cvt_pk_bf16_f32 v227, v230, v231\n"
    "v_mfma_f32_16x16x32_bf16 v[144:147], a[188:191], v[188:191], v[144:147]\n"
    "v_exp_f32_e32 v222, v222\n"
    "v_mfma_f32_16x16x32_bf16 v[144:147], a[184:187], v[184:187], v[144:147]\n"
    "v_exp_f32_e32 v223, v223\n"
    "v_mfma_f32_16x16x32_bf16 v[140:143], a[184:187], v[176:179], v[140:143]\n"
    "ds_read_b128 a[184:187], v243 offset:0\n"
    "v_cvt_pk_bf16_f32 v218, v220, v221\n"
    "v_mfma_f32_16x16x32_bf16 v[140:143], a[188:191], v[180:183], v[140:143]\n"
    "s_waitcnt lgkmcnt(11)\n"
    "ds_read_b128 a[188:191], v242 offset:4096\n"
    "v_cvt_pk_bf16_f32 v219, v222, v223\n"
    "v_mfma_f32_16x16x32_bf16 v[176:179], a[160:163], a[92:95], 0\n"
    "s_waitcnt vmcnt(0)\n"
    "ds_write_addtid_b32 v227 offset:13056\n"
    "v_cmp_ne_u32_e32 vcc, s39, v255\n"
    "v_mfma_f32_16x16x32_bf16 v[176:179], a[164:167], a[64:67], v[176:179]\n"
    "ds_write_addtid_b32 v218 offset:8704\n"
    "v_mfma_f32_16x16x32_bf16 v[184:187], a[164:167], a[96:99], 0\n"
    "s_cbranch_vccnz .Lat_wpoll\n"
    ".Lat_wpoll_ret:\n"
    "v_mfma_f32_16x16x32_bf16 v[184:187], a[160:163], a[124:127], v[184:187]\n"
    "ds_write_addtid_b32 v219 offset:8960\n"
    "v_mfma_f32_16x16x32_bf16 v[192:195], a[160:163], a[156:159], 0\n"
    "ds_read_b128 a[160:163], v243 offset:6144\n"
    "v_mfma_f32_16x16x32_bf16 v[192:195], a[164:167], a[128:131], v[192:195]\n"
    "s_waitcnt lgkmcnt(12)\n"
    "ds_read_b128 a[164:167], v240 offset:2048\n"
    "v_mfma_f32_16x16x32_bf16 v[192:195], a[168:171], a[132:135], v[192:195]\n"
    "v_mfma_f32_16x16x32_bf16 v[192:195], a[172:175], a[136:139], v[192:195]\n"
    "s_waitcnt lgkmcnt(2)\n"
    "v_mfma_f32_16x16x32_bf16 v[184:187], a[172:175], a[104:107], v[184:187]\n"
    "s_barrier\n"
    "v_mfma_f32_16x16x32_bf16 v[184:187], a[168:171], a[100:103], v[184:187]\n"
    "s_add_u32 s61, s47, 5791744\n"
    "v_mfma_f32_16x16x32_bf16 v[176:179], a[168:171], a[68:71], v[176:179]\n"
    "s_add_u32 m0, s49, 20480\n"
    "ds_read_b128 a[168:171], v241 offset:2048\n"
    "v_mfma_f32_16x16x32_bf16 v[176:179], a[172:175], a[72:75], v[176:179]\n"
    "ds_read_b128 a[172:175], v240 offset:6144\n"
    "v_mfma_f32_16x16x32_bf16 v[176:179], a[176:179], a[76:79], v[176:179]\n"
    "buffer_load_dwordx4 v252, s[8:11], s61 offen offset:0 sc1 lds\n"
    "v_mfma_f32_16x16x32_bf16 v[176:179], a[180:183], a[80:83], v[176:179]\n"
    "s_add_u32 s61, s47, 5795840\n"
    "ds_read2st64_b32 v[220:221], v250 offset0:32 offset1:33\n"
    "v_mfma_f32_16x16x32_bf16 v[184:187], a[180:183], a[112:115], v[184:187]\n"
    "s_add_u32 m0, s49, 24576\n"
    "v_mfma_f32_16x16x32_bf16 v[184:187], a[176:179], a[108:111], v[184:187]\n"
    "v_mfma_f32_16x16x32_bf16 v[192:195], a[176:179], a[140:143], v[192:195]\n"
    "ds_read_b128 a[176:179], v241 offset:6144\n"
    "buffer_load_dwordx4 v252, s[8:11], s61 offen offset:0 sc1 lds\n"
    "v_mfma_f32_16x16x32_bf16 v[192:195], a[180:183], a[144:147], v[192:195]\n"
    "s_add_u32 s61, s47, 5799936\n"
    "ds_read_b128 a[180:183], v242 offset:2048\n"
    "v_mfma_f32_16x16x32_bf16 v[192:195], a[184:187], a[148:151], v[192:195]\n"
    "s_add_u32 m0, s49, 28672\n"
    "ds_read2st64_b32 v[222:223], v250 offset0:34 offset1:35\n"
    "v_mfma_f32_16x16x32_bf16 v[192:195], a[188:191], a[152:155], v[192:195]\n"
    "v_mfma_f32_16x16x32_bf16 v[184:187], a[188:191], a[120:123], v[184:187]\n"
    "buffer_load_dwordx4 v252, s[8:11], s61 offen offset:0 sc1 lds\n"
    "v_mfma_f32_16x16x32_bf16 v[184:187], a[184:187], a[116:119], v[184:187]\n"
    "s_mov_b32 m0, s49\n"
    "ds_read2st64_b32 v[228:229], v250 offset0:48 offset1:49\n"
    "v_mfma_f32_16x16x32_bf16 v[176:179], a[184:187], a[84:87], v[176:179]\n"
    "s_add_u32 s48, s57, 30720\n"
    "ds_read_b128 a[184:187], v243 offset:2048\n"
    "v_exp_f32_e32 v192, v192\n"
    "v_mfma_f32_16x16x32_bf16 v[176:179], a[188:191], a[88:91], v[176:179]\n"
    "s_waitcnt lgkmcnt(8)\n"
    "ds_read_b128 a[188:191], v242 offset:6144\n"
    "v_mfma_f32_16x16x32_bf16 v[180:183], a[160:163], a[92:95], 0\n"
    "buffer_load_dwordx4 a[192:195], v253, s[8:11], s48 offen offset:0 sc1\n"
    "v_mfma_f32_16x16x32_bf16 v[180:183], a[164:167], a[64:67], v[180:183]\n"
    "v_exp_f32_e32 v193, v193\n"
    "v_mfma_f32_16x16x32_bf16 v[188:191], a[164:167], a[96:99], 0\n"
    "v_exp_f32_e32 v194, v194\n"
    "v_mfma_f32_16x16x32_bf16 v[188:191], a[160:163], a[124:127], v[188:191]\n"
    "ds_read2st64_b32 v[230:231], v250 offset0:50 offset1:51\n"
    "v_exp_f32_e32 v195, v195\n"
    "v_mfma_f32_16x16x32_bf16 v[196:199], a[160:163], a[156:159], 0\n"
    "ds_read_b64_tr_b16 a[160:161], v244 offset:49152\n"
    "buffer_load_dwordx4 a[196:199], v253, s[8:11], s48 offen offset:1024 sc1\n"
    "v_mfma_f32_16x16x32_bf16 v[196:199], a[164:167], a[128:131], v[196:199]\n"
    "s_waitcnt lgkmcnt(9)\n"
    "s_add_u32 s48, s57, 14336\n"
    "ds_read_b64_tr_b16 a[162:163], v244 offset:51200\n"
    "v_mfma_f32_16x16x32_bf16 v[196:199], a[168:171], a[132:135], v[196:199]\n"
    "ds_read_b64_tr_b16 a[164:165], v244 offset:57344\n"
    "v_cvt_pk_bf16_f32 v192, v192, v193\n"
    "v_mfma_f32_16x16x32_bf16 v[196:199], a[172:175], a[136:139], v[196:199]\n"
    "ds_read_b64_tr_b16 a[166:167], v244 offset:59392\n"
    "v_mfma_f32_16x16x32_bf16 v[188:191], a[172:175], a[104:107], v[188:191]\n"
    "buffer_load_dwordx4 a[200:203], v253, s[8:11], s48 offen offset:0 sc1\n"
    "v_mfma_f32_16x16x32_bf16 v[188:191], a[168:171], a[100:103], v[188:191]\n"
    "ds_read2st64_b32 v[236:237], v250 offset0:64 offset1:65\n"
    "v_mfma_f32_16x16x32_bf16 v[180:183], a[168:171], a[68:71], v[180:183]\n"
    "ds_read_b64_tr_b16 a[168:169], v245 offset:49152\n"
    "v_mfma_f32_16x16x32_bf16 v[180:183], a[172:175], a[72:75], v[180:183]\n"
    "s_waitcnt lgkmcnt(11)\n"
    "ds_read_b64_tr_b16 a[170:171], v245 offset:51200\n"
    "v_mfma_f32_16x16x32_bf16 v[180:183], a[176:179], a[76:79], v[180:183]\n"
    "ds_read_b64_tr_b16 a[172:173], v245 offset:57344\n"
    "buffer_load_dwordx4 a[204:207], v253, s[8:11], s48 offen offset:1024 sc1\n"
    "v_mfma_f32_16x16x32_bf16 v[180:183], a[180:183], a[80:83], v[180:183]\n"
    "s_add_u32 s48, s57, 16384\n"
    "ds_read_b64_tr_b16 a[174:175], v245 offset:59392\n"
    "v_mfma_f32_16x16x32_bf16 v[188:191], a[180:183], a[112:115], v[188:191]\n"
    "ds_read2st64_b32 v[238:239], v250 offset0:66 offset1:67\n"
    "v_mfma_f32_16x16x32_bf16 v[188:191], a[176:179], a[108:111], v[188:191]\n"
    "v_exp_f32_e32 v184, v184\n"
    "v_mfma_f32_16x16x32_bf16 v[196:199], a[176:179], a[140:143], v[196:199]\n"
    "ds_read_b64_tr_b16 a[176:177], v246 offset:49152\n"
    "buffer_load_dwordx4 a[208:211], v253, s[8:11], s48 offen offset:0 sc1\n"
    "v_mfma_f32_16x16x32_bf16 v[196:199], a[180:183], a[144:147], v[196:199]\n"
    "s_waitcnt lgkmcnt(12)\n"
    "ds_read_b64_tr_b16 a[178:179], v246 offset:51200\n"
    "v_mfma_f32_16x16x32_bf16 v[196:199], a[184:187], a[148:151], v[196:199]\n"
    "ds_read_b64_tr_b16 a[180:181], v246 offset:57344\n"
    "v_mfma_f32_16x16x32_bf16 v[196:199], a[188:191], a[152:155], v[196:199]\n"
    "ds_read_b64_tr_b16 a[182:183], v246 offset:59392\n"
    "v_mfma_f32_16x16x32_bf16 v[188:191], a[188:191], a[120:123], v[188:191]\n"
    "buffer_load_dwordx4 a[212:215], v253, s[8:11], s48 offen offset:1024 sc1\n"
    "v_mfma_f32_16x16x32_bf16 v[188:191], a[184:187], a[116:119], v[188:191]\n"
    "s_add_u32 s48, s57, 0\n"
    "v_exp_f32_e32 v185, v185\n"
    "v_mfma_f32_16x16x32_bf16 v[180:183], a[184:187], a[84:87], v[180:183]\n"
    "ds_read_b64_tr_b16 a[184:185], v247 offset:49152\n"
    "v_cvt_pk_bf16_f32 v193, v194, v195\n"
    "v_mfma_f32_16x16x32_bf16 v[180:183], a[188:191], a[88:91], v[180:183]\n"
    "s_waitcnt lgkmcnt(5)\n"
    "ds_read_b64_tr_b16 a[186:187], v247 offset:51200\n"
    "v_mfma_f32_16x16x32_bf16 v[56:59], a[160:163], v[216:219], v[56:59]\n"
    "ds_read_b64_tr_b16 a[188:189], v247 offset:57344\n"
    "buffer_load_dwordx4 a[216:219], v253, s[8:11], s48 offen offset:0 sc1\n"
    "v_mfma_f32_16x16x32_bf16 v[56:59], a[164:167], v[220:223], v[56:59]\n"
    "ds_read_b64_tr_b16 a[190:191], v247 offset:59392\n"
    "v_mfma_f32_16x16x32_bf16 v[60:63], a[164:167], v[228:231], v[60:63]\n"
    "v_exp_f32_e32 v186, v186\n"
    "v_mfma_f32_16x16x32_bf16 v[60:63], a[160:163], v[224:227], v[60:63]\n"
    "v_exp_f32_e32 v187, v187\n"
    "v_mfma_f32_16x16x32_bf16 v[64:67], a[160:163], v[232:235], v[64:67]\n"
    "ds_read_b64_tr_b16 a[160:161], v244 offset:53248\n"
    "buffer_load_dwordx4 a[220:223], v253, s[8:11], s48 offen offset:1024 sc1\n"
    "v_mfma_f32_16x16x32_bf16 v[64:67], a[164:167], v[236:239], v[64:67]\n"
    "ds_read_b64_tr_b16 a[162:163], v244 offset:55296\n"
    "v_cvt_pk_bf16_f32 v184, v184, v185\n"
    "v_mfma_f32_16x16x32_bf16 v[76:79], a[168:171], v[232:235], v[76:79]\n"
    "ds_read_b64_tr_b16 a[164:165], v244 offset:61440\n"
    "v_exp_f32_e32 v176, v176\n"
    "v_mfma_f32_16x16x32_bf16 v[76:79], a[172:175], v[236:239], v[76:79]\n"
    "s_add_u32 s61, s28, 24576\n"
    "ds_read_b64_tr_b16 a[166:167], v244 offset:63488\n"
    "v_mfma_f32_16x16x32_bf16 v[72:75], a[172:175], v[228:231], v[72:75]\n"
    "v_and_b32_e32 v254, 0x80, v253\n"
    "v_mfma_f32_16x16x32_bf16 v[72:75], a[168:171], v[224:227], v[72:75]\n"
    "v_exp_f32_e32 v177, v177\n"
    "v_mfma_f32_16x16x32_bf16 v[68:71], a[168:171], v[216:219], v[68:71]\n"
    "ds_read_b64_tr_b16 a[168:169], v245 offset:53248\n"
    "buffer_load_dword v249, v254, s[8:11], s61 offen offset:0 sc1\n"
    "v_mfma_f32_16x16x32_bf16 v[68:71], a[172:175], v[220:223], v[68:71]\n"
    "s_waitcnt lgkmcnt(9)\n"
    "s_add_u32 s61, s29, 24576\n"
    "ds_read_b64_tr_b16 a[170:171], v245 offset:55296\n"
    "v_mfma_f32_16x16x32_bf16 v[80:83], a[176:179], v[216:219], v[80:83]\n"
    "ds_read_b64_tr_b16 a[172:173], v245 offset:61440\n"
    "v_cvt_pk_bf16_f32 v185, v186, v187\n"
    "v_mfma_f32_16x16x32_bf16 v[80:83], a[180:183], v[220:223], v[80:83]\n"
    "ds_read_b64_tr_b16 a[174:175], v245 offset:63488\n"
    "buffer_load_dword v249, v254, s[8:11], s61 offen offset:0 sc1\n"
    "v_mfma_f32_16x16x32_bf16 v[84:87], a[180:183], v[228:231], v[84:87]\n"
    "s_add_u32 s61, s30, 24576\n"
    "v_exp_f32_e32 v178, v178\n"
    "v_mfma_f32_16x16x32_bf16 v[84:87], a[176:179], v[224:227], v[84:87]\n"
    "s_waitcnt vmcnt(14)\n"
    "v_exp_f32_e32 v179, v179\n"
    "v_mfma_f32_16x16x32_bf16 v[88:91], a[176:179], v[232:235], v[88:91]\n"
    "s_barrier\n"
    "v_mfma_f32_16x16x32_bf16 v[88:91], a[180:183], v[236:239], v[88:91]\n"
    "s_waitcnt lgkmcnt(8)\n"
    "ds_read_b64_tr_b16 a[176:177], v246 offset:53248\n"
    "buffer_load_dword v249, v254, s[8:11], s61 offen offset:0 sc1\n"
    "v_mfma_f32_16x16x32_bf16 v[100:103], a[184:187], v[232:235], v[100:103]\n"
    "s_add_u32 s61, s31, 24576\n"
    "ds_read_b64_tr_b16 a[178:179], v246 offset:55296\n"
    "v_cvt_pk_bf16_f32 v176, v176, v177\n"
    "v_mfma_f32_16x16x32_bf16 v[100:103], a[188:191], v[236:239], v[100:103]\n"
    "ds_read_b64_tr_b16 a[180:181], v246 offset:61440\n"
    "v_cvt_pk_bf16_f32 v177, v178, v179\n"
    "v_mfma_f32_16x16x32_bf16 v[96:99], a[188:191], v[228:231], v[96:99]\n"
    "ds_read_b64_tr_b16 a[182:183], v246 offset:63488\n"
    "buffer_load_dword v249, v254, s[8:11], s61 offen offset:0 sc1\n"
    "v_mfma_f32_16x16x32_bf16 v[96:99], a[184:187], v[224:227], v[96:99]\n"
    "v_exp_f32_e32 v196, v196\n"
    "v_mfma_f32_16x16x32_bf16 v[92:95], a[184:187], v[216:219], v[92:95]\n"
    "ds_read_b64_tr_b16 a[184:185], v247 offset:53248\n"
    "v_exp_f32_e32 v197, v197\n"
    "v_mfma_f32_16x16x32_bf16 v[92:95], a[188:191], v[220:223], v[92:95]\n"
    "s_waitcnt lgkmcnt(9)\n"
    "ds_read_b64_tr_b16 a[186:187], v247 offset:55296\n"
    "ds_write_addtid_b32 v192 offset:16384\n"
    "v_mfma_f32_16x16x32_bf16 v[104:107], a[160:163], v[216:219], v[104:107]\n"
    "ds_read_b64_tr_b16 a[188:189], v247 offset:61440\n"
    "buffer_load_dword v249, v254, s[8:11], s87 offen offset:0 sc1\n"
    "v_mfma_f32_16x16x32_bf16 v[104:107], a[164:167], v[220:223], v[104:107]\n"
    "ds_read_b64_tr_b16 a[190:191], v247 offset:63488\n"
    "ds_write_addtid_b32 v193 offset:16640\n"
    "v_mfma_f32_16x16x32_bf16 v[108:111], a[164:167], v[228:231], v[108:111]\n"
    "v_exp_f32_e32 v198, v198\n"
    "v_mfma_f32_16x16x32_bf16 v[108:111], a[160:163], v[224:227], v[108:111]\n"
    "s_getpc_b64 s[62:63]\n"
    "v_exp_f32_e32 v199, v199\n"
    "v_mfma_f32_16x16x32_bf16 v[112:115], a[160:163], v[232:235], v[112:115]\n"
    "s_bfe_u32 s61, %1, 0x20001\n"
    "ds_read_b128 a[160:163], v243 offset:20480\n"
    "v_cvt_pk_bf16_f32 v194, v196, v197\n"
    "v_mfma_f32_16x16x32_bf16 v[112:115], a[164:167], v[236:239], v[112:115]\n"
    "s_waitcnt lgkmcnt(11)\n"
    "ds_read_b128 a[164:167], v240 offset:16384\n"
    "ds_write_addtid_b32 v184 offset:12288\n"
    "v_mfma_f32_16x16x32_bf16 v[124:127], a[168:171], v[232:235], v[124:127]\n"
    "s_lshl2_add_u32 s61, s61, %2\n"
    "v_exp_f32_e32 v188, v188\n"
    "v_mfma_f32_16x16x32_bf16 v[124:127], a[172:175], v[236:239], v[124:127]\n"
    "s_lshl_b32 s61, s61, 11\n"
    "v_exp_f32_e32 v189, v189\n"
    "v_mfma_f32_16x16x32_bf16 v[120:123], a[172:175], v[228:231], v[120:123]\n"
    "v_lshl_add_u32 v158, v253, 1, s61\n"
    "v_cvt_pk_bf16_f32 v195, v198, v199\n"
    "v_mfma_f32_16x16x32_bf16 v[120:123], a[168:171], v[224:227], v[120:123]\n"
    "v_exp_f32_e32 v190, v190\n"
    "v_mfma_f32_16x16x32_bf16 v[116:119], a[168:171], v[216:219], v[116:119]\n"
    "ds_read_b128 a[168:171], v241 offset:16384\n"
    "global_load_dword v249, v158, s[62:63]\n"
    "v_mfma_f32_16x16x32_bf16 v[116:119], a[172:175], v[220:223], v[116:119]\n"
    "s_waitcnt lgkmcnt(10)\n"
    "ds_read_b128 a[172:175], v240 offset:20480\n"
    "ds_write_addtid_b32 v185 offset:12544\n"
    "v_mfma_f32_16x16x32_bf16 v[128:131], a[176:179], v[216:219], v[128:131]\n"
    "v_exp_f32_e32 v191, v191\n"
    "v_mfma_f32_16x16x32_bf16 v[128:131], a[180:183], v[220:223], v[128:131]\n"
    "ds_write_addtid_b32 v176 offset:8192\n"
    "v_cvt_pk_bf16_f32 v186, v188, v189\n"
    "v_mfma_f32_16x16x32_bf16 v[132:135], a[180:183], v[228:231], v[132:135]\n"
    "s_bfe_u32 s61, %1, 0x40001\n"
    "v_exp_f32_e32 v180, v180\n"
    "v_mfma_f32_16x16x32_bf16 v[132:135], a[176:179], v[224:227], v[132:135]\n"
    "s_lshl2_add_u32 s61, s61, %2\n"
    "v_exp_f32_e32 v181, v181\n"
    "v_mfma_f32_16x16x32_bf16 v[136:139], a[176:179], v[232:235], v[136:139]\n"
    "s_lshl_b32 s61, s61, 11\n"
    "ds_read_b128 a[176:179], v241 offset:20480\n"
    "v_cvt_pk_bf16_f32 v187, v190, v191\n"
    "v_mfma_f32_16x16x32_bf16 v[136:139], a[180:183], v[236:239], v[136:139]\n"
    "s_waitcnt lgkmcnt(9)\n"
    "ds_read_b128 a[180:183], v242 offset:16384\n"
    "ds_write_addtid_b32 v177 offset:8448\n"
    "v_mfma_f32_16x16x32_bf16 v[148:151], a[184:187], v[232:235], v[148:151]\n"
    "s_add_u32 s61, s61, 5373952\n"
    "ds_write_addtid_b32 v194 offset:16896\n"
    "v_lshlrev_b32_e32 v158, 1, v253\n"
    "v_mfma_f32_16x16x32_bf16 v[148:151], a[188:191], v[236:239], v[148:151]\n"
    "v_exp_f32_e32 v182, v182\n"
    "v_mfma_f32_16x16x32_bf16 v[144:147], a[188:191], v[228:231], v[144:147]\n"
    "buffer_load_dword v249, v158, s[8:11], s61 offen offset:0 sc1\n"
    "ds_write_addtid_b32 v195 offset:17152\n"
    "v_mfma_f32_16x16x32_bf16 v[144:147], a[184:187], v[224:227], v[144:147]\n"
    "v_exp_f32_e32 v183, v183\n"
    "v_mfma_f32_16x16x32_bf16 v[140:143], a[184:187], v[216:219], v[140:143]\n"
    "ds_read_b128 a[184:187], v243 offset:16384\n"
    "v_cvt_pk_bf16_f32 v178, v180, v181\n"
    "v_mfma_f32_16x16x32_bf16 v[140:143], a[188:191], v[220:223], v[140:143]\n"
    "s_waitcnt lgkmcnt(11)\n"
    "ds_read_b128 a[188:191], v242 offset:20480\n"
    "v_cvt_pk_bf16_f32 v179, v182, v183\n"
    "v_mfma_f32_16x16x32_bf16 v[216:219], a[160:163], a[92:95], 0\n"
    "ds_write_addtid_b32 v186 offset:12800\n"
    "v_mfma_f32_16x16x32_bf16 v[216:219], a[164:167], a[64:67], v[216:219]\n"
    "ds_write_addtid_b32 v187 offset:13056\n"
    "v_mfma_f32_16x16x32_bf16 v[224:227], a[164:167], a[96:99], 0\n"
    "ds_write_addtid_b32 v178 offset:8704\n"
    "v_mfma_f32_16x16x32_bf16 v[224:227], a[160:163], a[124:127], v[224:227]\n"
    "ds_write_addtid_b32 v179 offset:8960\n"
    "v_mfma_f32_16x16x32_bf16 v[232:235], a[160:163], a[156:159], 0\n"
    "ds_read_b128 a[160:163], v243 offset:22528\n"
    "v_mfma_f32_16x16x32_bf16 v[232:235], a[164:167], a[128:131], v[232:235]\n"
    "s_waitcnt lgkmcnt(14)\n"
    "ds_read_b128 a[164:167], v240 offset:18432\n"
    "v_mfma_f32_16x16x32_bf16 v[232:235], a[168:171], a[132:135], v[232:235]\n"
    "v_mfma_f32_16x16x32_bf16 v[232:235], a[172:175], a[136:139], v[232:235]\n"
    "s_waitcnt lgkmcnt(2)\n"
    "v_mfma_f32_16x16x32_bf16 v[224:227], a[172:175], a[104:107], v[224:227]\n"
    "s_barrier\n"
    "v_mfma_f32_16x16x32_bf16 v[224:227], a[168:171], a[100:103], v[224:227]\n"
    "v_mfma_f32_16x16x32_bf16 v[216:219], a[168:171], a[68:71], v[216:219]\n"
    "ds_read_b128 a[168:171], v241 offset:18432\n"
    "v_mfma_f32_16x16x32_bf16 v[216:219], a[172:175], a[72:75], v[216:219]\n"
    "ds_read_b128 a[172:175], v240 offset:22528\n"
    "v_mfma_f32_16x16x32_bf16 v[216:219], a[176:179], a[76:79], v[216:219]\n"
    "ds_read2st64_b32 v[180:181], v250 offset0:32 offset1:33\n"
    "v_mfma_f32_16x16x32_bf16 v[216:219], a[180:183], a[80:83], v[216:219]\n"
    "v_mfma_f32_16x16x32_bf16 v[224:227], a[180:183], a[112:115], v[224:227]\n"
    "v_mfma_f32_16x16x32_bf16 v[224:227], a[176:179], a[108:111], v[224:227]\n"
    "v_mfma_f32_16x16x32_bf16 v[232:235], a[176:179], a[140:143], v[232:235]\n"
    "ds_read_b128 a[176:179], v241 offset:22528\n"
    "v_mfma_f32_16x16x32_bf16 v[232:235], a[180:183], a[144:147], v[232:235]\n"
    "ds_read_b128 a[180:183], v242 offset:18432\n"
    "v_mfma_f32_16x16x32_bf16 v[232:235], a[184:187], a[148:151], v[232:235]\n"
    "ds_read2st64_b32 v[182:183], v250 offset0:34 offset1:35\n"
    "v_mfma_f32_16x16x32_bf16 v[232:235], a[188:191], a[152:155], v[232:235]\n"
    "v_mfma_f32_16x16x32_bf16 v[224:227], a[188:191], a[120:123], v[224:227]\n"
    "v_mfma_f32_16x16x32_bf16 v[224:227], a[184:187], a[116:119], v[224:227]\n"
    "ds_read2st64_b32 v[188:189], v250 offset0:48 offset1:49\n"
    "v_mfma_f32_16x16x32_bf16 v[216:219], a[184:187], a[84:87], v[216:219]\n"
    "ds_read_b128 a[184:187], v243 offset:18432\n"
    "v_mfma_f32_16x16x32_bf16 v[216:219], a[188:191], a[88:91], v[216:219]\n"
    "s_waitcnt lgkmcnt(8)\n"
    "ds_read_b128 a[188:191], v242 offset:22528\n"
    "v_mfma_f32_16x16x32_bf16 v[220:223], a[160:163], a[92:95], 0\n"
    "v_exp_f32_e32 v232, v232\n"
    "v_mfma_f32_16x16x32_bf16 v[220:223], a[164:167], a[64:67], v[220:223]\n"
    "v_exp_f32_e32 v233, v233\n"
    "v_mfma_f32_16x16x32_bf16 v[228:231], a[164:167], a[96:99], 0\n"
    "s_add_u32 s48, s57, 18432\n"
    "v_exp_f32_e32 v234, v234\n"
    "v_mfma_f32_16x16x32_bf16 v[228:231], a[160:163], a[124:127], v[228:231]\n"
    "ds_read2st64_b32 v[190:191], v250 offset0:50 offset1:51\n"
    "v_mfma_f32_16x16x32_bf16 v[236:239], a[160:163], a[156:159], 0\n"
    "ds_read_b64_tr_b16 a[160:161], v244 offset:0\n"
    "v_exp_f32_e32 v235, v235\n"
    "v_mfma_f32_16x16x32_bf16 v[236:239], a[164:167], a[128:131], v[236:239]\n"
    "s_waitcnt lgkmcnt(9)\n"
    "ds_read_b64_tr_b16 a[162:163], v244 offset:2048\n"
    "v_mfma_f32_16x16x32_bf16 v[236:239], a[168:171], a[132:135], v[236:239]\n"
    "ds_read_b64_tr_b16 a[164:165], v244 offset:8192\n"
    "buffer_load_dwordx4 a[92:95], v253, s[8:11], s48 offen offset:0 sc1\n"
    "v_mfma_f32_16x16x32_bf16 v[236:239], a[172:175], a[136:139], v[236:239]\n"
    "ds_read_b64_tr_b16 a[166:167], v244 offset:10240\n"
    "v_cvt_pk_bf16_f32 v232, v232, v233\n"
    "v_mfma_f32_16x16x32_bf16 v[228:231], a[172:175], a[104:107], v[228:231]\n"
    "ds_read2st64_b32 v[196:197], v250 offset0:64 offset1:65\n"
    "v_mfma_f32_16x16x32_bf16 v[228:231], a[168:171], a[100:103], v[228:231]\n"
    "v_exp_f32_e32 v224, v224\n"
    "v_mfma_f32_16x16x32_bf16 v[220:223], a[168:171], a[68:71], v[220:223]\n"
    "ds_read_b64_tr_b16 a[168:169], v245 offset:0\n"
    "buffer_load_dwordx4 a[124:127], v253, s[8:11], s48 offen offset:1024 sc1\n"
    "v_mfma_f32_16x16x32_bf16 v[220:223], a[172:175], a[72:75], v[220:223]\n"
    "s_waitcnt lgkmcnt(11)\n"
    "s_add_u32 s48, s57, 2048\n"
    "ds_read_b64_tr_b16 a[170:171], v245 offset:2048\n"
    "v_mfma_f32_16x16x32_bf16 v[220:223], a[176:179], a[76:79], v[220:223]\n"
    "ds_read_b64_tr_b16 a[172:173], v245 offset:8192\n"
    "v_exp_f32_e32 v225, v225\n"
    "v_mfma_f32_16x16x32_bf16 v[220:223], a[180:183], a[80:83], v[220:223]\n"
    "ds_read_b64_tr_b16 a[174:175], v245 offset:10240\n"
    "v_cvt_pk_bf16_f32 v233, v234, v235\n"
    "v_mfma_f32_16x16x32_bf16 v[228:231], a[180:183], a[112:115], v[228:231]\n"
    "buffer_load_dwordx4 a[156:159], v253, s[8:11], s48 offen offset:0 sc1\n"
    "v_mfma_f32_16x16x32_bf16 v[228:231], a[176:179], a[108:111], v[228:231]\n"
    "ds_read2st64_b32 v[198:199], v250 offset0:66 offset1:67\n"
    "v_exp_f32_e32 v226, v226\n"
    "v_mfma_f32_16x16x32_bf16 v[236:239], a[176:179], a[140:143], v[236:239]\n"
    "ds_read_b64_tr_b16 a[176:177], v246 offset:0\n"
    "v_mfma_f32_16x16x32_bf16 v[236:239], a[180:183], a[144:147], v[236:239]\n"
    "s_waitcnt lgkmcnt(12)\n"
    "ds_read_b64_tr_b16 a[178:179], v246 offset:2048\n"
    "v_exp_f32_e32 v227, v227\n"
    "v_mfma_f32_16x16x32_bf16 v[236:239], a[184:187], a[148:151], v[236:239]\n"
    "ds_read_b64_tr_b16 a[180:181], v246 offset:8192\n"
    "buffer_load_dwordx4 a[64:67], v253, s[8:11], s48 offen offset:1024 sc1\n"
    "v_mfma_f32_16x16x32_bf16 v[236:239], a[188:191], a[152:155], v[236:239]\n"
    "s_add_u32 s48, s57, 20480\n"
    "ds_read_b64_tr_b16 a[182:183], v246 offset:10240\n"
    "v_cvt_pk_bf16_f32 v224, v224, v225\n"
    "v_mfma_f32_16x16x32_bf16 v[228:231], a[188:191], a[120:123], v[228:231]\n"
    "v_exp_f32_e32 v216, v216\n"
    "v_mfma_f32_16x16x32_bf16 v[228:231], a[184:187], a[116:119], v[228:231]\n"
    "v_exp_f32_e32 v217, v217\n"
    "v_mfma_f32_16x16x32_bf16 v[220:223], a[184:187], a[84:87], v[220:223]\n"
    "ds_read_b64_tr_b16 a[184:185], v247 offset:0\n"
    "buffer_load_dwordx4 a[96:99], v253, s[8:11], s48 offen offset:0 sc1\n"
    "v_mfma_f32_16x16x32_bf16 v[220:223], a[188:191], a[88:91], v[220:223]\n"
    "s_waitcnt lgkmcnt(5)\n"
    "ds_read_b64_tr_b16 a[186:187], v247 offset:2048\n"
    "v_mfma_f32_16x16x32_bf16 v[56:59], a[160:163], v[176:179], v[56:59]\n"
    "ds_read_b64_tr_b16 a[188:189], v247 offset:8192\n"
    "v_mfma_f32_16x16x32_bf16 v[56:59], a[164:167], v[180:183], v[56:59]\n"
    "ds_read_b64_tr_b16 a[190:191], v247 offset:10240\n"
    "v_mfma_f32_16x16x32_bf16 v[60:63], a[164:167], v[188:191], v[60:63]\n"
    "buffer_load_dwordx4 a[128:131], v253, s[8:11], s48 offen offset:1024 sc1\n"
    "v_cvt_pk_bf16_f32 v225, v226, v227\n"
    "v_mfma_f32_16x16x32_bf16 v[60:63], a[160:163], v[184:187], v[60:63]\n"
    "s_add_u32 s48, s57, 4096\n"
    "v_exp_f32_e32 v218, v218\n"
    "v_mfma_f32_16x16x32_bf16 v[64:67], a[160:163], v[192:195], v[64:67]\n"
    "ds_read_b64_tr_b16 a[160:161], v244 offset:4096\n"
    "v_mfma_f32_16x16x32_bf16 v[64:67], a[164:167], v[196:199], v[64:67]\n"
    "ds_read_b64_tr_b16 a[162:163], v244 offset:6144\n"
    "v_mfma_f32_16x16x32_bf16 v[76:79], a[168:171], v[192:195], v[76:79]\n"
    "ds_read_b64_tr_b16 a[164:165], v244 offset:12288\n"
    "buffer_load_dwordx4 a[68:71], v253, s[8:11], s48 offen offset:0 sc1\n"
    "v_mfma_f32_16x16x32_bf16 v[76:79], a[172:175], v[196:199], v[76:79]\n"
    "ds_read_b64_tr_b16 a[166:167], v244 offset:14336\n"
    "v_mfma_f32_16x16x32_bf16 v[72:75], a[172:175], v[188:191], v[72:75]\n"
    "s_barrier\n"
    "v_mfma_f32_16x16x32_bf16 v[72:75], a[168:171], v[184:187], v[72:75]\n"
    "v_exp_f32_e32 v219, v219\n"
    "v_mfma_f32_16x16x32_bf16 v[68:71], a[168:171], v[176:179], v[68:71]\n"
    "ds_read_b64_tr_b16 a[168:169], v245 offset:4096\n"
    "buffer_load_dwordx4 a[100:103], v253, s[8:11], s48 offen offset:1024 sc1\n"
    "v_mfma_f32_16x16x32_bf16 v[68:71], a[172:175], v[180:183], v[68:71]\n"
    "s_waitcnt lgkmcnt(9)\n"
    "s_add_u32 s48, s57, 22528\n"
    "ds_read_b64_tr_b16 a[170:171], v245 offset:6144\n"
    "v_mfma_f32_16x16x32_bf16 v[80:83], a[176:179], v[176:179], v[80:83]\n"
    "ds_read_b64_tr_b16 a[172:173], v245 offset:12288\n"
    "v_cvt_pk_bf16_f32 v216, v216, v217\n"
    "v_mfma_f32_16x16x32_bf16 v[80:83], a[180:183], v[180:183], v[80:83]\n"
    "ds_read_b64_tr_b16 a[174:175], v245 offset:14336\n"
    "v_cvt_pk_bf16_f32 v217, v218, v219\n"
    "v_mfma_f32_16x16x32_bf16 v[84:87], a[180:183], v[188:191], v[84:87]\n"
    "buffer_load_dwordx4 a[132:135], v253, s[8:11], s48 offen offset:0 sc1\n"
    "ds_write_addtid_b32 v232 offset:16384\n"
    "v_mfma_f32_16x16x32_bf16 v[84:87], a[176:179], v[184:187], v[84:87]\n"
    "v_exp_f32_e32 v236, v236\n"
    "v_mfma_f32_16x16x32_bf16 v[88:91], a[176:179], v[192:195], v[88:91]\n"
    "ds_read_b64_tr_b16 a[176:177], v246 offset:4096\n"
    "ds_write_addtid_b32 v233 offset:16640\n"
    "v_mfma_f32_16x16x32_bf16 v[88:91], a[180:183], v[196:199], v[88:91]\n"
    "s_waitcnt lgkmcnt(11)\n"
    "ds_read_b64_tr_b16 a[178:179], v246 offset:6144\n"
    "v_mfma_f32_16x16x32_bf16 v[100:103], a[184:187], v[192:195], v[100:103]\n"
    "ds_read_b64_tr_b16 a[180:181], v246 offset:12288\n"
    "buffer_load_dwordx4 a[72:75], v253, s[8:11], s48 offen offset:1024 sc1\n"
    "v_mfma_f32_16x16x32_bf16 v[100:103], a[188:191], v[196:199], v[100:103]\n"
    "s_add_u32 s48, s57, 6144\n"
    "ds_read_b64_tr_b16 a[182:183], v246 offset:14336\n"
    "ds_write_addtid_b32 v224 offset:12288\n"
    "v_mfma_f32_16x16x32_bf16 v[96:99], a[188:191], v[188:191], v[96:99]\n"
    "v_exp_f32_e32 v237, v237\n"
    "v_mfma_f32_16x16x32_bf16 v[96:99], a[184:187], v[184:187], v[96:99]\n"
    "v_exp_f32_e32 v238, v238\n"
    "v_mfma_f32_16x16x32_bf16 v[92:95], a[184:187], v[176:179], v[92:95]\n"
    "ds_read_b64_tr_b16 a[184:185], v247 offset:4096\n"
    "buffer_load_dwordx4 a[104:107], v253, s[8:11], s48 offen offset:0 sc1\n"
    "v_mfma_f32_16x16x32_bf16 v[92:95], a[188:191], v[180:183], v[92:95]\n"
    "s_waitcnt lgkmcnt(12)\n"
    "ds_read_b64_tr_b16 a[186:187], v247 offset:6144\n"
    "ds_write_addtid_b32 v225 offset:12544\n"
    "v_mfma_f32_16x16x32_bf16 v[104:107], a[160:163], v[176:179], v[104:107]\n"
    "ds_read_b64_tr_b16 a[188:189], v247 offset:12288\n"
    "v_mfma_f32_16x16x32_bf16 v[104:107], a[164:167], v[180:183], v[104:107]\n"
    "ds_read_b64_tr_b16 a[190:191], v247 offset:14336\n"
    "v_mfma_f32_16x16x32_bf16 v[108:111], a[164:167], v[188:191], v[108:111]\n"
    "buffer_load_dwordx4 a[136:139], v253, s[8:11], s48 offen offset:1024 sc1\n"
    "ds_write_addtid_b32 v216 offset:8192\n"
    "v_mfma_f32_16x16x32_bf16 v[108:111], a[160:163], v[184:187], v[108:111]\n"
    "s_add_u32 s48, s57, 24576\n"
    "v_exp_f32_e32 v239, v239\n"
    "v_mfma_f32_16x16x32_bf16 v[112:115], a[160:163], v[192:195], v[112:115]\n"
    "ds_read_b128 a[160:163], v243 offset:36864\n"
    "v_cvt_pk_bf16_f32 v234, v236, v237\n"
    "v_mfma_f32_16x16x32_bf16 v[112:115], a[164:167], v[196:199], v[112:115]\n"
    "s_waitcnt lgkmcnt(14)\n"
    "ds_read_b128 a[164:167], v240 offset:32768\n"
    "v_mfma_f32_16x16x32_bf16 v[124:127], a[168:171], v[192:195], v[124:127]\n"
    "buffer_load_dwordx4 a[76:79], v253, s[8:11], s48 offen offset:0 sc1\n"
    "ds_write_addtid_b32 v217 offset:8448\n"
    "v_mfma_f32_16x16x32_bf16 v[124:127], a[172:175], v[196:199], v[124:127]\n"
    "v_exp_f32_e32 v228, v228\n"
    "v_mfma_f32_16x16x32_bf16 v[120:123], a[172:175], v[188:191], v[120:123]\n"
    "v_exp_f32_e32 v229, v229\n"
    "v_mfma_f32_16x16x32_bf16 v[120:123], a[168:171], v[184:187], v[120:123]\n"
    "ds_write_addtid_b32 v234 offset:16896\n"
    "v_cvt_pk_bf16_f32 v235, v238, v239\n"
    "v_mfma_f32_16x16x32_bf16 v[116:119], a[168:171], v[176:179], v[116:119]\n"
    "ds_read_b128 a[168:171], v241 offset:32768\n"
    "buffer_load_dwordx4 a[108:111], v253, s[8:11], s48 offen offset:1024 sc1\n"
    "v_mfma_f32_16x16x32_bf16 v[116:119], a[172:175], v[180:183], v[116:119]\n"
    "s_waitcnt lgkmcnt(12)\n"
    "s_add_u32 s48, s57, 8192\n"
    "ds_read_b128 a[172:175], v240 offset:36864\n"
    "v_mfma_f32_16x16x32_bf16 v[128:131], a[176:179], v[176:179], v[128:131]\n"
    "v_exp_f32_e32 v230, v230\n"
    "v_mfma_f32_16x16x32_bf16 v[128:131], a[180:183], v[180:183], v[128:131]\n"
    "v_exp_f32_e32 v231, v231\n"
    "v_mfma_f32_16x16x32_bf16 v[132:135], a[180:183], v[188:191], v[132:135]\n"
    "buffer_load_dwordx4 a[140:143], v253, s[8:11], s48 offen offset:0 sc1\n"
    "v_cvt_pk_bf16_f32 v226, v228, v229\n"
    "v_mfma_f32_16x16x32_bf16 v[132:135], a[176:179], v[184:187], v[132:135]\n"
    "v_exp_f32_e32 v220, v220\n"
    "v_mfma_f32_16x16x32_bf16 v[136:139], a[176:179], v[192:195], v[136:139]\n"
    "ds_read_b128 a[176:179], v241 offset:36864\n"
    "v_mfma_f32_16x16x32_bf16 v[136:139], a[180:183], v[196:199], v[136:139]\n"
    "s_waitcnt lgkmcnt(8)\n"
    "ds_read_b128 a[180:183], v242 offset:32768\n"
    "v_mfma_f32_16x16x32_bf16 v[148:151], a[184:187], v[192:195], v[148:151]\n"
    "buffer_load_dwordx4 a[80:83], v253, s[8:11], s48 offen offset:1024 sc1\n"
    "ds_write_addtid_b32 v235 offset:17152\n"
    "v_mfma_f32_16x16x32_bf16 v[148:151], a[188:191], v[196:199], v[148:151]\n"
    "s_add_u32 s48, s57, 26624\n"
    "v_exp_f32_e32 v221, v221\n"
    "v_mfma_f32_16x16x32_bf16 v[144:147], a[188:191], v[188:191], v[144:147]\n"
    "ds_write_addtid_b32 v226 offset:12800\n"
    "v_cvt_pk_bf16_f32 v227, v230, v231\n"
    "v_mfma_f32_16x16x32_bf16 v[144:147], a[184:187], v[184:187], v[144:147]\n"
    "v_exp_f32_e32 v222, v222\n"
    "v_mfma_f32_16x16x32_bf16 v[140:143], a[184:187], v[176:179], v[140:143]\n"
    "ds_read_b128 a[184:187], v243 offset:32768\n"
    "buffer_load_dwordx4 a[112:115], v253, s[8:11], s48 offen offset:0 sc1\n"
    "v_mfma_f32_16x16x32_bf16 v[140:143], a[188:191], v[180:183], v[140:143]\n"
    "s_waitcnt lgkmcnt(5)\n"
    "ds_read_b128 a[188:191], v242 offset:36864\n"
    "v_mfma_f32_16x16x32_bf16 v[160:163], a[160:163], a[28:31], 0\n"
    "v_exp_f32_e32 v223, v223\n"
    "v_mfma_f32_16x16x32_bf16 v[160:163], a[164:167], a[0:3], v[160:163]\n"
    "v_cvt_pk_bf16_f32 v218, v220, v221\n"
    "v_cvt_pk_bf16_f32 v219, v222, v223\n"
    "v_mfma_f32_16x16x32_bf16 v[168:171], a[164:167], a[32:35], 0\n"
    "buffer_load_dwordx4 a[144:147], v253, s[8:11], s48 offen offset:1024 sc1\n"
    "ds_write_addtid_b32 v227 offset:13056\n"
    "v_mfma_f32_16x16x32_bf16 v[168:171], a[160:163], a[60:63], v[168:171]\n"
    "s_add_u32 s48, s57, 10240\n"
    "ds_read_b128 a[160:163], v243 offset:38912\n"
    "v_mfma_f32_16x16x32_bf16 v[168:171], a[168:171], a[36:39], v[168:171]\n"
    "ds_read_b128 a[164:167], v240 offset:34816\n"
    "v_mfma_f32_16x16x32_bf16 v[168:171], a[172:175], a[40:43], v[168:171]\n"
    "ds_write_addtid_b32 v218 offset:8704\n"
    "v_mfma_f32_16x16x32_bf16 v[160:163], a[172:175], a[8:11], v[160:163]\n"
    "buffer_load_dwordx4 a[84:87], v253, s[8:11], s48 offen offset:0 sc1\n"
    "ds_write_addtid_b32 v219 offset:8960\n"
    "v_mfma_f32_16x16x32_bf16 v[160:163], a[168:171], a[4:7], v[160:163]\n"
    "s_waitcnt lgkmcnt(5)\n"
    "ds_read_b128 a[168:171], v241 offset:34816\n"
    "v_mfma_f32_16x16x32_bf16 v[160:163], a[176:179], a[12:15], v[160:163]\n"
    "ds_read_b128 a[172:175], v240 offset:38912\n"
    "v_mfma_f32_16x16x32_bf16 v[160:163], a[180:183], a[16:19], v[160:163]\n"
    "v_mfma_f32_16x16x32_bf16 v[168:171], a[180:183], a[48:51], v[168:171]\n"
    "buffer_load_dwordx4 a[116:119], v253, s[8:11], s48 offen offset:1024 sc1\n"
    "v_mfma_f32_16x16x32_bf16 v[168:171], a[176:179], a[44:47], v[168:171]\n"
    "s_waitcnt lgkmcnt(2)\n"
    "s_add_u32 s48, s57, 28672\n"
    "ds_read_b128 a[176:179], v241 offset:38912\n"
    "v_mfma_f32_16x16x32_bf16 v[168:171], a[184:187], a[52:55], v[168:171]\n"
    "s_barrier\n"
    "v_mfma_f32_16x16x32_bf16 v[168:171], a[188:191], a[56:59], v[168:171]\n"
    "ds_read_b128 a[180:183], v242 offset:34816\n"
    "v_mfma_f32_16x16x32_bf16 v[160:163], a[188:191], a[24:27], v[160:163]\n"
    "buffer_load_dwordx4 a[148:151], v253, s[8:11], s48 offen offset:0 sc1\n"
    "ds_read2st64_b32 v[220:221], v250 offset0:32 offset1:33\n"
    "v_mfma_f32_16x16x32_bf16 v[160:163], a[184:187], a[20:23], v[160:163]\n"
    "s_waitcnt lgkmcnt(3)\n"
    "ds_read_b128 a[184:187], v243 offset:34816\n"
    "v_mfma_f32_16x16x32_bf16 v[164:167], a[160:163], a[28:31], 0\n"
    "ds_read_b128 a[188:191], v242 offset:38912\n"
    "v_mfma_f32_16x16x32_bf16 v[164:167], a[164:167], a[0:3], v[164:167]\n"
    "ds_read2st64_b32 v[222:223], v250 offset0:34 offset1:35\n"
    "v_mfma_f32_16x16x32_bf16 v[172:175], a[164:167], a[32:35], 0\n"
    "buffer_load_dwordx4 a[88:91], v253, s[8:11], s48 offen offset:1024 sc1\n"
    "ds_read2st64_b32 v[228:229], v250 offset0:48 offset1:49\n"
    "v_mfma_f32_16x16x32_bf16 v[172:175], a[160:163], a[60:63], v[172:175]\n"
    "s_add_u32 s48, s57, 12288\n"
    "ds_read_b64_tr_b16 a[160:161], v244 offset:16384\n"
    "ds_read2st64_b32 v[230:231], v250 offset0:50 offset1:51\n"
    "v_mfma_f32_16x16x32_bf16 v[172:175], a[168:171], a[36:39], v[172:175]\n"
    "ds_read_b64_tr_b16 a[162:163], v244 offset:18432\n"
    "v_mfma_f32_16x16x32_bf16 v[172:175], a[172:175], a[40:43], v[172:175]\n"
    "ds_read_b64_tr_b16 a[164:165], v244 offset:24576\n"
    "ds_read2st64_b32 v[236:237], v250 offset0:64 offset1:65\n"
    "v_mfma_f32_16x16x32_bf16 v[164:167], a[172:175], a[8:11], v[164:167]\n"
    "ds_read_b64_tr_b16 a[166:167], v244 offset:26624\n"
    "buffer_load_dwordx4 a[120:123], v253, s[8:11], s48 offen offset:0 sc1\n"
    "v_mfma_f32_16x16x32_bf16 v[164:167], a[168:171], a[4:7], v[164:167]\n"
    "s_waitcnt lgkmcnt(8)\n"
    "ds_read_b64_tr_b16 a[168:169], v245 offset:16384\n"
    "ds_read2st64_b32 v[238:239], v250 offset0:66 offset1:67\n"
    "v_mfma_f32_16x16x32_bf16 v[164:167], a[176:179], a[12:15], v[164:167]\n"
    "ds_read_b64_tr_b16 a[170:171], v245 offset:18432\n"
    "v_mfma_f32_16x16x32_bf16 v[164:167], a[180:183], a[16:19], v[164:167]\n"
    "ds_read_b64_tr_b16 a[172:173], v245 offset:24576\n"
    "v_mfma_f32_16x16x32_bf16 v[172:175], a[180:183], a[48:51], v[172:175]\n"
    "ds_read_b64_tr_b16 a[174:175], v245 offset:26624\n"
    "buffer_load_dwordx4 a[152:155], v253, s[8:11], s48 offen offset:1024 sc1\n"
    "v_mfma_f32_16x16x32_bf16 v[172:175], a[176:179], a[44:47], v[172:175]\n"
    "s_waitcnt lgkmcnt(3)\n"
    "ds_read_b64_tr_b16 a[176:177], v246 offset:16384\n"
    "v_mfma_f32_16x16x32_bf16 v[172:175], a[184:187], a[52:55], v[172:175]\n"
    "s_barrier\n"
    "v_mfma_f32_16x16x32_bf16 v[172:175], a[188:191], a[56:59], v[172:175]\n"
    "s_add_u32 s61, s47, 5779456\n"
    "ds_read_b64_tr_b16 a[178:179], v246 offset:18432\n"
    "v_mfma_f32_16x16x32_bf16 v[164:167], a[188:191], a[24:27], v[164:167]\n"
    "s_add_u32 m0, s49, 8192\n"
    "ds_read_b64_tr_b16 a[180:181], v246 offset:24576\n"
    "v_mfma_f32_16x16x32_bf16 v[164:167], a[184:187], a[20:23], v[164:167]\n"
    "ds_read_b64_tr_b16 a[182:183], v246 offset:26624\n"
    "buffer_load_dwordx4 v252, s[8:11], s61 offen offset:0 sc1 lds\n"
    "v_mfma_f32_16x16x32_bf16 v[56:59], a[160:163], v[216:219], v[56:59]\n"
    "s_add_u32 s61, s47, 5783552\n"
    "ds_read_b64_tr_b16 a[184:185], v247 offset:16384\n"
    "v_mfma_f32_16x16x32_bf16 v[56:59], a[164:167], v[220:223], v[56:59]\n"
    "s_add_u32 m0, s49, 12288\n"
    "ds_read_b64_tr_b16 a[186:187], v247 offset:18432\n"
    "v_mfma_f32_16x16x32_bf16 v[60:63], a[164:167], v[228:231], v[60:63]\n"
    "ds_read_b64_tr_b16 a[188:189], v247 offset:24576\n"
    "buffer_load_dwordx4 v252, s[8:11], s61 offen offset:0 sc1 lds\n"
    "v_mfma_f32_16x16x32_bf16 v[60:63], a[160:163], v[224:227], v[60:63]\n"
    "s_add_u32 s61, s47, 5787648\n"
    "ds_read_b64_tr_b16 a[190:191], v247 offset:26624\n"
    "v_mfma_f32_16x16x32_bf16 v[64:67], a[160:163], v[232:235], v[64:67]\n"
    "s_add_u32 m0, s49, 16384\n"
    "ds_read_b64_tr_b16 a[160:161], v244 offset:20480\n"
    "v_lshrrev_b32_e32 v158, 4, v253\n"
    "v_mfma_f32_16x16x32_bf16 v[64:67], a[164:167], v[236:239], v[64:67]\n"
    "s_waitcnt lgkmcnt(9)\n"
    "ds_read_b64_tr_b16 a[162:163], v244 offset:22528\n"
    "buffer_load_dwordx4 v252, s[8:11], s61 offen offset:0 sc1 lds\n"
    "v_mfma_f32_16x16x32_bf16 v[76:79], a[168:171], v[232:235], v[76:79]\n"
    "s_mov_b32 m0, s49\n"
    "ds_read_b64_tr_b16 a[164:165], v244 offset:28672\n"
    "v_lshrrev_b32_e32 v159, 4, v158\n"
    "v_mfma_f32_16x16x32_bf16 v[76:79], a[172:175], v[236:239], v[76:79]\n"
    "ds_read_b64_tr_b16 a[166:167], v244 offset:30720\n"
    "v_cvt_pk_bf16_f32 v64, v64, v65\n"
    "v_mfma_f32_16x16x32_bf16 v[72:75], a[172:175], v[228:231], v[72:75]\n"
    "v_exp_f32_e32 v168, v168\n"
    "v_mfma_f32_16x16x32_bf16 v[72:75], a[168:171], v[224:227], v[72:75]\n"
    "v_exp_f32_e32 v169, v169\n"
    "v_mfma_f32_16x16x32_bf16 v[68:71], a[168:171], v[216:219], v[68:71]\n"
    "ds_read_b64_tr_b16 a[168:169], v245 offset:20480\n"
    "v_cvt_pk_bf16_f32 v65, v66, v67\n"
    "v_mfma_f32_16x16x32_bf16 v[68:71], a[172:175], v[220:223], v[68:71]\n"
    "s_waitcnt lgkmcnt(9)\n"
    "ds_read_b64_tr_b16 a[170:171], v245 offset:22528\n"
    "v_cvt_pk_bf16_f32 v66, v76, v77\n"
    "v_mfma_f32_16x16x32_bf16 v[80:83], a[176:179], v[216:219], v[80:83]\n"
    "ds_read_b64_tr_b16 a[172:173], v245 offset:28672\n"
    "v_cvt_pk_bf16_f32 v67, v78, v79\n"
    "v_mfma_f32_16x16x32_bf16 v[80:83], a[180:183], v[220:223], v[80:83]\n"
    "ds_read_b64_tr_b16 a[174:175], v245 offset:30720\n"
    "v_cvt_pk_bf16_f32 v60, v60, v61\n"
    "v_mfma_f32_16x16x32_bf16 v[84:87], a[180:183], v[228:231], v[84:87]\n"
    "v_exp_f32_e32 v170, v170\n"
    "v_mfma_f32_16x16x32_bf16 v[84:87], a[176:179], v[224:227], v[84:87]\n"
    "v_exp_f32_e32 v171, v171\n"
    "v_mfma_f32_16x16x32_bf16 v[88:91], a[176:179], v[232:235], v[88:91]\n"
    "ds_read_b64_tr_b16 a[176:177], v246 offset:20480\n"
    "buffer_store_dwordx4 v[64:67], v253, s[8:11], s86 offen offset:0 sc1\n"
    "v_cvt_pk_bf16_f32 v168, v168, v169\n"
    "v_cvt_pk_bf16_f32 v61, v62, v63\n"
    "v_bfe_u32 v255, v158, 2, 2\n"
    "v_mfma_f32_16x16x32_bf16 v[88:91], a[180:183], v[236:239], v[88:91]\n"
    "s_waitcnt lgkmcnt(9)\n"
    "ds_read_b64_tr_b16 a[178:179], v246 offset:22528\n"
    "v_cvt_pk_bf16_f32 v62, v72, v73\n"
    "v_mfma_f32_16x16x32_bf16 v[100:103], a[184:187], v[232:235], v[100:103]\n"
    "ds_read_b64_tr_b16 a[180:181], v246 offset:28672\n"
    "v_cvt_pk_bf16_f32 v63, v74, v75\n"
    "v_mfma_f32_16x16x32_bf16 v[100:103], a[188:191], v[236:239], v[100:103]\n"
    "ds_read_b64_tr_b16 a[182:183], v246 offset:30720\n"
    "v_cvt_pk_bf16_f32 v56, v56, v57\n"
    "v_mfma_f32_16x16x32_bf16 v[96:99], a[188:191], v[228:231], v[96:99]\n"
    "v_exp_f32_e32 v160, v160\n"
    "v_mfma_f32_16x16x32_bf16 v[96:99], a[184:187], v[224:227], v[96:99]\n"
    "v_exp_f32_e32 v161, v161\n"
    "v_and_b32_e32 v158, 3, v158\n"
    "v_mfma_f32_16x16x32_bf16 v[92:95], a[184:187], v[216:219], v[92:95]\n"
    "ds_read_b64_tr_b16 a[184:185], v247 offset:20480\n"
    "v_cvt_pk_bf16_f32 v169, v170, v171\n"
    "v_mfma_f32_16x16x32_bf16 v[92:95], a[188:191], v[220:223], v[92:95]\n"
    "s_waitcnt lgkmcnt(9)\n"
    "ds_read_b64_tr_b16 a[186:187], v247 offset:22528\n"
    "v_cvt_pk_bf16_f32 v57, v58, v59\n"
    "v_mfma_f32_16x16x32_bf16 v[104:107], a[160:163], v[216:219], v[104:107]\n"
    "ds_read_b64_tr_b16 a[188:189], v247 offset:28672\n"
    "v_cvt_pk_bf16_f32 v58, v68, v69\n"
    "v_mfma_f32_16x16x32_bf16 v[104:107], a[164:167], v[220:223], v[104:107]\n"
    "ds_read_b64_tr_b16 a[190:191], v247 offset:30720\n"
    "v_cvt_pk_bf16_f32 v59, v70, v71\n"
    "v_xor_b32_e32 v159, v159, v255\n"
    "v_mfma_f32_16x16x32_bf16 v[108:111], a[164:167], v[228:231], v[108:111]\n"
    "buffer_store_dwordx4 v[60:63], v253, s[8:11], s85 offen offset:0 sc1\n"
    "v_exp_f32_e32 v162, v162\n"
    "v_mfma_f32_16x16x32_bf16 v[108:111], a[160:163], v[224:227], v[108:111]\n"
    "v_exp_f32_e32 v163, v163\n"
    "v_mfma_f32_16x16x32_bf16 v[112:115], a[160:163], v[232:235], v[112:115]\n"
    "ds_read_b128 a[160:163], v243 offset:53248\n"
    "v_cvt_pk_bf16_f32 v160, v160, v161\n"
    "v_mfma_f32_16x16x32_bf16 v[112:115], a[164:167], v[236:239], v[112:115]\n"
    "s_waitcnt lgkmcnt(9)\n"
    "ds_read_b128 a[164:167], v240 offset:49152\n"
    "v_cvt_pk_bf16_f32 v161, v162, v163\n"
    "v_mfma_f32_16x16x32_bf16 v[124:127], a[168:171], v[232:235], v[124:127]\n"
    "v_exp_f32_e32 v172, v172\n"
    "v_lshl_or_b32 v158, v159, 2, v158\n"
    "v_mfma_f32_16x16x32_bf16 v[124:127], a[172:175], v[236:239], v[124:127]\n"
    "v_exp_f32_e32 v173, v173\n"
    "v_mfma_f32_16x16x32_bf16 v[120:123], a[172:175], v[228:231], v[120:123]\n"
    "v_exp_f32_e32 v174, v174\n"
    "v_mfma_f32_16x16x32_bf16 v[120:123], a[168:171], v[224:227], v[120:123]\n"
    "v_exp_f32_e32 v175, v175\n"
    "v_mfma_f32_16x16x32_bf16 v[116:119], a[168:171], v[216:219], v[116:119]\n"
    "ds_read_b128 a[168:171], v241 offset:49152\n"
    "ds_write_addtid_b32 v168 offset:4096\n"
    "v_mfma_f32_16x16x32_bf16 v[116:119], a[172:175], v[220:223], v[116:119]\n"
    "s_waitcnt lgkmcnt(8)\n"
    "ds_read_b128 a[172:175], v240 offset:53248\n"
    "v_cvt_pk_bf16_f32 v88, v88, v89\n"
    "v_mov_b32_e32 v159, 0x3f80\n"
    "v_mfma_f32_16x16x32_bf16 v[128:131], a[176:179], v[216:219], v[128:131]\n"
    "ds_write_addtid_b32 v169 offset:4352\n"
    "v_cvt_pk_bf16_f32 v170, v172, v173\n"
    "v_mfma_f32_16x16x32_bf16 v[128:131], a[180:183], v[220:223], v[128:131]\n"
    "buffer_store_dwordx4 v[56:59], v253, s[8:11], s84 offen offset:0 sc1\n"
    "v_exp_f32_e32 v164, v164\n"
    "v_mfma_f32_16x16x32_bf16 v[132:135], a[180:183], v[228:231], v[132:135]\n"
    "v_exp_f32_e32 v165, v165\n"
    "v_mfma_f32_16x16x32_bf16 v[132:135], a[176:179], v[224:227], v[132:135]\n"
    "ds_write_addtid_b32 v160 offset:0\n"
    "v_cvt_pk_bf16_f32 v171, v174, v175\n"
    "v_mfma_f32_16x16x32_bf16 v[136:139], a[176:179], v[232:235], v[136:139]\n"
    "ds_read_b128 a[176:179], v241 offset:53248\n"
    "v_cvt_pk_bf16_f32 v89, v90, v91\n"
    "v_mfma_f32_16x16x32_bf16 v[136:139], a[180:183], v[236:239], v[136:139]\n"
    "s_waitcnt lgkmcnt(8)\n"
    "ds_read_b128 a[180:183], v242 offset:49152\n"
    "v_cvt_pk_bf16_f32 v90, v100, v101\n"
    "v_mfma_f32_16x16x32_bf16 v[148:151], a[184:187], v[232:235], v[148:151]\n"
    "v_exp_f32_e32 v166, v166\n"
    "v_mfma_f32_16x16x32_bf16 v[148:151], a[188:191], v[236:239], v[148:151]\n"
    "v_exp_f32_e32 v167, v167\n"
    "v_mfma_f32_16x16x32_bf16 v[144:147], a[188:191], v[228:231], v[144:147]\n"
    "ds_write_addtid_b32 v161 offset:256\n"
    "v_cvt_pk_bf16_f32 v162, v164, v165\n"
    "v_mfma_f32_16x16x32_bf16 v[144:147], a[184:187], v[224:227], v[144:147]\n"
    "ds_write_addtid_b32 v170 offset:4608\n"
    "v_cvt_pk_bf16_f32 v163, v166, v167\n"
    "v_cvt_pk_bf16_f32 v91, v102, v103\n"
    "v_mfma_f32_16x16x32_bf16 v[140:143], a[184:187], v[216:219], v[140:143]\n"
    "ds_read_b128 a[184:187], v243 offset:49152\n"
    "buffer_store_dwordx4 v[88:91], v253, s[8:11], s86 offen offset:1024 sc1\n"
    "v_cvt_pk_bf16_f32 v84, v84, v85\n"
    "v_mov_b32_e32 v152, 0\n"
    "v_mfma_f32_16x16x32_bf16 v[140:143], a[188:191], v[220:223], v[140:143]\n"
    "s_waitcnt lgkmcnt(7)\n"
    "ds_read_b128 a[188:191], v242 offset:53248\n"
    "v_mov_b32_e32 v153, 0\n"
    "v_mfma_f32_16x16x32_bf16 v[200:203], a[160:163], a[28:31], 0\n"
    "ds_write_addtid_b32 v171 offset:4864\n"
    "v_mov_b32_e32 v156, 0\n"
    "v_mfma_f32_16x16x32_bf16 v[200:203], a[164:167], a[0:3], v[200:203]\n"
    "ds_write_addtid_b32 v162 offset:512\n"
    "v_mov_b32_e32 v157, 0\n"
    "v_mfma_f32_16x16x32_bf16 v[208:211], a[164:167], a[32:35], 0\n"
    "ds_write_addtid_b32 v163 offset:768\n"
    "v_cmp_eq_u32_e32 vcc, 0, v158\n"
    "v_mfma_f32_16x16x32_bf16 v[208:211], a[160:163], a[60:63], v[208:211]\n"
    "ds_read_b128 a[160:163], v243 offset:55296\n"
    "v_cndmask_b32_e32 v154, 0, v159, vcc\n"
    "v_mfma_f32_16x16x32_bf16 v[208:211], a[168:171], a[36:39], v[208:211]\n"
    "ds_read_b128 a[164:167], v240 offset:51200\n"
    "v_cmp_eq_u32_e32 vcc, 1, v158\n"
    "v_mfma_f32_16x16x32_bf16 v[208:211], a[172:175], a[40:43], v[208:211]\n"
    "s_waitcnt lgkmcnt(2)\n"
    "v_cvt_pk_bf16_f32 v85, v86, v87\n"
    "v_cndmask_b32_e64 v154, v154, 1.0, vcc\n"
    "v_mfma_f32_16x16x32_bf16 v[200:203], a[172:175], a[8:11], v[200:203]\n"
    "s_barrier\n"
    "v_mfma_f32_16x16x32_bf16 v[200:203], a[168:171], a[4:7], v[200:203]\n"
    "ds_read_b128 a[168:171], v241 offset:51200\n"
    "v_cmp_eq_u32_e32 vcc, 2, v158\n"
    "v_mfma_f32_16x16x32_bf16 v[200:203], a[176:179], a[12:15], v[200:203]\n"
    "ds_read_b128 a[172:175], v240 offset:55296\n"
    "v_cndmask_b32_e32 v155, 0, v159, vcc\n"
    "v_mfma_f32_16x16x32_bf16 v[200:203], a[180:183], a[16:19], v[200:203]\n"
    "ds_read2st64_b32 v[164:165], v250 offset0:0 offset1:1\n"
    "v_cmp_eq_u32_e32 vcc, 3, v158\n"
    "s_nop 1\n"
    "v_cndmask_b32_e64 v155, v155, 1.0, vcc\n"
    "v_mfma_f32_16x16x32_bf16 v[208:211], a[180:183], a[48:51], v[208:211]\n"
    "v_cvt_pk_bf16_f32 v86, v96, v97\n"
    "v_and_b32_e32 v251, 0xf0, v253\n"
    "v_mov_b32_e32 v254, 2.0\n"
    "v_mfma_f32_16x16x32_bf16 v[208:211], a[176:179], a[44:47], v[208:211]\n"
    "ds_read_b128 a[176:179], v241 offset:55296\n"
    "v_cmp_le_u32_e32 vcc, 0x100, v253\n"
    "v_mov_b32_e32 v255, s29\n"
    "v_mfma_f32_16x16x32_bf16 v[208:211], a[184:187], a[52:55], v[208:211]\n"
    "ds_read_b128 a[180:183], v242 offset:51200\n"
    "v_cvt_pk_bf16_f32 v87, v98, v99\n"
    "v_mfma_f32_16x16x32_bf16 v[208:211], a[188:191], a[56:59], v[208:211]\n"
    "v_cvt_pk_bf16_f32 v80, v80, v81\n"
    "v_cvt_pk_bf16_f32 v81, v82, v83\n"
    "v_mfma_f32_16x16x32_bf16 v[200:203], a[188:191], a[24:27], v[200:203]\n"
    "ds_read2st64_b32 v[166:167], v250 offset0:2 offset1:3\n"
    "v_cvt_pk_bf16_f32 v82, v92, v93\n"
    "v_mfma_f32_16x16x32_bf16 v[200:203], a[184:187], a[20:23], v[200:203]\n"
    "s_waitcnt lgkmcnt(4)\n"
    "ds_read_b128 a[184:187], v243 offset:51200\n"
    "v_cvt_pk_bf16_f32 v83, v94, v95\n"
    "v_mfma_f32_16x16x32_bf16 v[204:207], a[160:163], a[28:31], 0\n"
    "ds_read_b128 a[188:191], v242 offset:55296\n"
    "v_cvt_pk_bf16_f32 v112, v112, v113\n"
    "v_mfma_f32_16x16x32_bf16 v[204:207], a[164:167], a[0:3], v[204:207]\n"
    "v_exp_f32_e32 v208, v208\n"
    "v_mfma_f32_16x16x32_bf16 v[212:215], a[164:167], a[32:35], 0\n"
    "ds_read2st64_b32 v[172:173], v250 offset0:16 offset1:17\n"
    "v_cvt_pk_bf16_f32 v113, v114, v115\n"
    "v_mfma_f32_16x16x32_bf16 v[212:215], a[160:163], a[60:63], v[212:215]\n"
    "ds_read_b64_tr_b16 a[160:161], v244 offset:32768\n"
    "ds_read_b64_tr_b16 a[162:163], v244 offset:34816\n"
    "v_mfma_f32_16x16x32_bf16 v[212:215], a[168:171], a[36:39], v[212:215]\n"
    "v_exp_f32_e32 v209, v209\n"
    "v_mfma_f32_16x16x32_bf16 v[212:215], a[172:175], a[40:43], v[212:215]\n"
    "ds_read_b64_tr_b16 a[164:165], v244 offset:40960\n"
    "ds_read_b64_tr_b16 a[166:167], v244 offset:43008\n"
    "v_mfma_f32_16x16x32_bf16 v[204:207], a[172:175], a[8:11], v[204:207]\n"
    "v_exp_f32_e32 v210, v210\n"
    "v_mfma_f32_16x16x32_bf16 v[204:207], a[168:171], a[4:7], v[204:207]\n"
    "s_waitcnt lgkmcnt(5)\n"
    "ds_read_b64_tr_b16 a[168:169], v245 offset:32768\n"
    "ds_read_b64_tr_b16 a[170:171], v245 offset:34816\n"
    "v_mfma_f32_16x16x32_bf16 v[204:207], a[176:179], a[12:15], v[204:207]\n"
    "ds_read2st64_b32 v[174:175], v250 offset0:18 offset1:19\n"
    "v_cvt_pk_bf16_f32 v114, v124, v125\n"
    "v_mfma_f32_16x16x32_bf16 v[204:207], a[180:183], a[16:19], v[204:207]\n"
    "ds_read_b64_tr_b16 a[172:173], v245 offset:40960\n"
    "ds_read_b64_tr_b16 a[174:175], v245 offset:43008\n"
    "v_mfma_f32_16x16x32_bf16 v[212:215], a[180:183], a[48:51], v[212:215]\n"
    "v_exp_f32_e32 v211, v211\n"
    "v_mfma_f32_16x16x32_bf16 v[212:215], a[176:179], a[44:47], v[212:215]\n"
    "ds_read_b64_tr_b16 a[176:177], v246 offset:32768\n"
    "ds_read_b64_tr_b16 a[178:179], v246 offset:34816\n"
    "v_mfma_f32_16x16x32_bf16 v[212:215], a[184:187], a[52:55], v[212:215]\n"
    "v_cvt_pk_bf16_f32 v208, v208, v209\n"
    "v_cvt_pk_bf16_f32 v115, v126, v127\n"
    "v_mfma_f32_16x16x32_bf16 v[212:215], a[188:191], a[56:59], v[212:215]\n"
    "ds_read_b64_tr_b16 a[180:181], v246 offset:40960\n"
    "ds_read_b64_tr_b16 a[182:183], v246 offset:43008\n"
    "v_mfma_f32_16x16x32_bf16 v[204:207], a[188:191], a[24:27], v[204:207]\n"
    "v_exp_f32_e32 v200, v200\n"
    "v_mfma_f32_16x16x32_bf16 v[204:207], a[184:187], a[20:23], v[204:207]\n"
    "s_waitcnt lgkmcnt(4)\n"
    "ds_read_b64_tr_b16 a[184:185], v247 offset:32768\n"
    "ds_read_b64_tr_b16 a[186:187], v247 offset:34816\n"
    "v_mfma_f32_16x16x32_bf16 v[0:3], a[160:163], v[160:163], v[0:3]\n"
    "v_exp_f32_e32 v201, v201\n"
    "v_mfma_f32_16x16x32_bf16 v[0:3], a[164:167], v[164:167], v[0:3]\n"
    "ds_read_b64_tr_b16 a[188:189], v247 offset:40960\n"
    "ds_read_b64_tr_b16 a[190:191], v247 offset:43008\n"
    "v_mfma_f32_16x16x32_bf16 a[248:251], a[164:167], v[172:175], a[248:251]\n"
    "v_cvt_pk_bf16_f32 v209, v210, v211\n"
    "v_cvt_pk_bf16_f32 v108, v108, v109\n"
    "v_mfma_f32_16x16x32_bf16 a[248:251], a[160:163], v[168:171], a[248:251]\n"
    "ds_read_b64_tr_b16 a[160:161], v244 offset:36864\n"
    "ds_read_b64_tr_b16 a[162:163], v244 offset:38912\n"
    "v_mfma_f32_16x16x32_bf16 a[252:255], a[168:171], v[168:171], a[252:255]\n"
    "v_exp_f32_e32 v202, v202\n"
    "v_mfma_f32_16x16x32_bf16 a[252:255], a[172:175], v[172:175], a[252:255]\n"
    "s_barrier\n"
    "v_mfma_f32_16x16x32_bf16 v[4:7], a[172:175], v[164:167], v[4:7]\n"
    "ds_read_b64_tr_b16 a[164:165], v244 offset:45056\n"
    "ds_read_b64_tr_b16 a[166:167], v244 offset:47104\n"
    "v_mfma_f32_16x16x32_bf16 v[4:7], a[168:171], v[160:163], v[4:7]\n"
    "s_waitcnt lgkmcnt(4)\n"
    "v_exp_f32_e32 v203, v203\n"
    "v_mfma_f32_16x16x32_bf16 v[8:11], a[176:179], v[160:163], v[8:11]\n"
    "ds_read_b64_tr_b16 a[168:169], v245 offset:36864\n"
    "ds_read_b64_tr_b16 a[170:171], v245 offset:38912\n"
    "v_mfma_f32_16x16x32_bf16 v[8:11], a[180:183], v[164:167], v[8:11]\n"
    "buffer_store_dwordx4 v[84:87], v253, s[8:11], s85 offen offset:1024 sc1\n"
    "v_cvt_pk_bf16_f32 v200, v200, v201\n"
    "v_cvt_pk_bf16_f32 v201, v202, v203\n"
    "v_mfma_f32_16x16x32_bf16 v[32:35], a[180:183], v[172:175], v[32:35]\n"
    "ds_read_b64_tr_b16 a[172:173], v245 offset:45056\n"
    "ds_read_b64_tr_b16 a[174:175], v245 offset:47104\n"
    "v_mfma_f32_16x16x32_bf16 v[32:35], a[176:179], v[168:171], v[32:35]\n"
    "v_exp_f32_e32 v212, v212\n"
    "v_mfma_f32_16x16x32_bf16 v[36:39], a[184:187], v[168:171], v[36:39]\n"
    "ds_read_b64_tr_b16 a[176:177], v246 offset:36864\n"
    "ds_read_b64_tr_b16 a[178:179], v246 offset:38912\n"
    "v_mfma_f32_16x16x32_bf16 v[36:39], a[188:191], v[172:175], v[36:39]\n"
    "buffer_store_dwordx4 v[80:83], v253, s[8:11], s84 offen offset:1024 sc1\n"
    "v_exp_f32_e32 v213, v213\n"
    "v_mfma_f32_16x16x32_bf16 v[12:15], a[188:191], v[164:167], v[12:15]\n"
    "ds_read_b64_tr_b16 a[180:181], v246 offset:45056\n"
    "ds_read_b64_tr_b16 a[182:183], v246 offset:47104\n"
    "v_mfma_f32_16x16x32_bf16 v[12:15], a[184:187], v[160:163], v[12:15]\n"
    "s_waitcnt lgkmcnt(4)\n"
    "v_exp_f32_e32 v214, v214\n"
    "v_mfma_f32_16x16x32_bf16 v[16:19], a[160:163], v[160:163], v[16:19]\n"
    "ds_read_b64_tr_b16 a[184:185], v247 offset:36864\n"
    "ds_read_b64_tr_b16 a[186:187], v247 offset:38912\n"
    "v_mfma_f32_16x16x32_bf16 v[16:19], a[164:167], v[164:167], v[16:19]\n"
    "v_exp_f32_e32 v215, v215\n"
    "v_mfma_f32_16x16x32_bf16 v[40:43], a[164:167], v[172:175], v[40:43]\n"
    "ds_read_b64_tr_b16 a[188:189], v247 offset:45056\n"
    "ds_read_b64_tr_b16 a[190:191], v247 offset:47104\n"
    "v_mfma_f32_16x16x32_bf16 v[40:43], a[160:163], v[168:171], v[40:43]\n"
    "ds_write_addtid_b32 v208 offset:4096\n"
    "buffer_store_dwordx4 v[112:115], v253, s[8:11], s86 offen offset:2048 sc1\n"
    "v_cvt_pk_bf16_f32 v210, v212, v213\n"
    "v_cvt_pk_bf16_f32 v109, v110, v111\n"
    "v_mfma_f32_16x16x32_bf16 v[44:47], a[168:171], v[168:171], v[44:47]\n"
    "ds_read_b128 a[160:163], v243 offset:4096\n"
    "ds_write_addtid_b32 v209 offset:4352\n"
    "v_mfma_f32_16x16x32_bf16 v[44:47], a[172:175], v[172:175], v[44:47]\n"
    "ds_read_b128 a[164:167], v240 offset:0\n"
    "ds_write_addtid_b32 v200 offset:0\n"
    "v_mfma_f32_16x16x32_bf16 v[20:23], a[172:175], v[164:167], v[20:23]\n"
    "v_exp_f32_e32 v204, v204\n"
    "v_mfma_f32_16x16x32_bf16 v[20:23], a[168:171], v[160:163], v[20:23]\n"
    "s_waitcnt lgkmcnt(5)\n"
    "ds_read_b128 a[168:171], v241 offset:0\n"
    "v_cvt_pk_bf16_f32 v110, v120, v121\n"
    "v_mfma_f32_16x16x32_bf16 v[24:27], a[176:179], v[160:163], v[24:27]\n"
    "ds_read_b128 a[172:175], v240 offset:4096\n"
    "v_cvt_pk_bf16_f32 v111, v122, v123\n"
    "v_mfma_f32_16x16x32_bf16 v[24:27], a[180:183], v[164:167], v[24:27]\n"
    "buffer_store_dwordx4 v[108:111], v253, s[8:11], s85 offen offset:2048 sc1\n"
    "v_exp_f32_e32 v205, v205\n"
    "v_mfma_f32_16x16x32_bf16 v[48:51], a[180:183], v[172:175], v[48:51]\n"
    "ds_write_addtid_b32 v201 offset:256\n"
    "v_cvt_pk_bf16_f32 v211, v214, v215\n"
    "v_mfma_f32_16x16x32_bf16 v[48:51], a[176:179], v[168:171], v[48:51]\n"
    "ds_read_b128 a[176:179], v241 offset:4096\n"
    "v_cndmask_b32_e32 v254, v254, v255, vcc\n"
    "v_mfma_f32_16x16x32_bf16 v[52:55], a[184:187], v[168:171], v[52:55]\n"
    "ds_read_b128 a[180:183], v242 offset:0\n"
    "v_cmp_le_u32_e32 vcc, 0x200, v253\n"
    "v_mov_b32_e32 v255, s30\n"
    "v_mfma_f32_16x16x32_bf16 v[52:55], a[188:191], v[172:175], v[52:55]\n"
    "v_exp_f32_e32 v206, v206\n"
    "v_mfma_f32_16x16x32_bf16 v[28:31], a[188:191], v[164:167], v[28:31]\n"
    "v_exp_f32_e32 v207, v207\n"
    "v_mfma_f32_16x16x32_bf16 v[28:31], a[184:187], v[160:163], v[28:31]\n"
    "s_waitcnt lgkmcnt(3)\n"
    "ds_read_b128 a[184:187], v243 offset:0\n"
    "v_cvt_pk_bf16_f32 v202, v204, v205\n"
    "v_cndmask_b32_e32 v254, v254, v255, vcc\n"
    "v_mfma_f32_16x16x32_bf16 v[160:163], a[160:163], a[28:31], 0\n"
    "ds_read_b128 a[188:191], v242 offset:4096\n"
    "v_cmp_le_u32_e32 vcc, 0x300, v253\n"
    "v_mov_b32_e32 v255, s31\n"
    "v_mfma_f32_16x16x32_bf16 v[160:163], a[164:167], a[0:3], v[160:163]\n"
    "ds_write_addtid_b32 v210 offset:4608\n"
    "v_cvt_pk_bf16_f32 v203, v206, v207\n"
    "v_cndmask_b32_e32 v254, v254, v255, vcc\n"
    "v_mfma_f32_16x16x32_bf16 v[168:171], a[164:167], a[32:35], 0\n"
    "s_cmp_eq_u32 s51, 0\n"
    "s_cselect_b32 s60, 2.0, 24576\n"
    "ds_write_addtid_b32 v211 offset:4864\n"
    "v_add_u32_e32 v251, v251, v254\n"
    "v_mfma_f32_16x16x32_bf16 v[168:171], a[160:163], a[60:63], v[168:171]\n"
    "s_lshl_b32 s45, s51, 16\n"
    "s_add_u32 s45, s45, %3\n"
    "s_add_u32 s45, s45, 73728\n"
    "ds_read_b128 a[160:163], v243 offset:6144\n"
    "v_mfma_f32_16x16x32_bf16 v[168:171], a[168:171], a[36:39], v[168:171]\n"
    "s_lshl_b32 s46, s89, 12\n"
    "s_add_u32 s46, s46, s45\n"
    "ds_read_b128 a[164:167], v240 offset:2048\n"
    "v_add_u32_e32 v159, s46, v253\n"
    "v_mfma_f32_16x16x32_bf16 v[168:171], a[172:175], a[40:43], v[168:171]\n"
    "ds_write_addtid_b32 v202 offset:512\n"
    "v_cvt_pk_bf16_f32 v104, v104, v105\n"
    "v_mfma_f32_16x16x32_bf16 v[160:163], a[172:175], a[8:11], v[160:163]\n"
    "ds_write_addtid_b32 v203 offset:768\n"
    "v_cvt_pk_bf16_f32 v105, v106, v107\n"
    "v_mfma_f32_16x16x32_bf16 v[160:163], a[168:171], a[4:7], v[160:163]\n"
    "s_waitcnt lgkmcnt(6)\n"
    "ds_read_b128 a[168:171], v241 offset:2048\n"
    "v_cvt_pk_bf16_f32 v106, v116, v117\n"
    "v_mfma_f32_16x16x32_bf16 v[160:163], a[176:179], a[12:15], v[160:163]\n"
    "ds_read_b128 a[172:175], v240 offset:6144\n"
    "v_cvt_pk_bf16_f32 v107, v118, v119\n"
    "v_mfma_f32_16x16x32_bf16 v[160:163], a[180:183], a[16:19], v[160:163]\n"
    "s_waitcnt lgkmcnt(2)\n"
    "v_cvt_pk_bf16_f32 v136, v136, v137\n"
    "v_cvt_pk_bf16_f32 v137, v138, v139\n"
    "v_mfma_f32_16x16x32_bf16 v[168:171], a[180:183], a[48:51], v[168:171]\n"
    "s_barrier\n"
    "v_mfma_f32_16x16x32_bf16 v[168:171], a[176:179], a[44:47], v[168:171]\n"
    "s_add_u32 m0, s43, 32768\n"
    "ds_read_b128 a[176:179], v241 offset:6144\n"
    "v_cvt_pk_bf16_f32 v138, v148, v149\n"
    "v_mfma_f32_16x16x32_bf16 v[168:171], a[184:187], a[52:55], v[168:171]\n"
    "ds_read_b128 a[180:183], v242 offset:2048\n"
    "buffer_load_dwordx4 v248, s[8:11], s40 offen offset:0 sc1 lds\n"
    "v_mfma_f32_16x16x32_bf16 v[168:171], a[188:191], a[56:59], v[168:171]\n"
    "ds_read2st64_b32 v[204:205], v250 offset0:0 offset1:1\n"
    "v_cvt_pk_bf16_f32 v139, v150, v151\n"
    "v_mfma_f32_16x16x32_bf16 v[160:163], a[188:191], a[24:27], v[160:163]\n"
    "v_cvt_pk_bf16_f32 v132, v132, v133\n"
    "v_cvt_pk_bf16_f32 v133, v134, v135\n"
    "v_mfma_f32_16x16x32_bf16 v[160:163], a[184:187], a[20:23], v[160:163]\n"
    "s_waitcnt lgkmcnt(3)\n"
    "ds_read_b128 a[184:187], v243 offset:2048\n"
    "buffer_load_dwordx4 v248, s[8:11], s40 offen offset:1024 sc1 lds\n"
    "v_mfma_f32_16x16x32_bf16 v[164:167], a[160:163], a[28:31], 0\n"
    "ds_read_b128 a[188:191], v242 offset:6144\n"
    "v_exp_f32_e32 v168, v168\n"
    "v_mfma_f32_16x16x32_bf16 v[164:167], a[164:167], a[0:3], v[164:167]\n"
    "ds_read2st64_b32 v[206:207], v250 offset0:2 offset1:3\n"
    "v_cvt_pk_bf16_f32 v134, v144, v145\n"
    "v_mfma_f32_16x16x32_bf16 v[172:175], a[164:167], a[32:35], 0\n"
    "buffer_load_dwordx4 v248, s[8:11], s40 offen offset:2048 sc1 lds\n"
    "v_exp_f32_e32 v169, v169\n"
    "v_mfma_f32_16x16x32_bf16 v[172:175], a[160:163], a[60:63], v[172:175]\n"
    "ds_read_b64_tr_b16 a[160:161], v244 offset:49152\n"
    "ds_read_b64_tr_b16 a[162:163], v244 offset:51200\n"
    "v_mfma_f32_16x16x32_bf16 v[172:175], a[168:171], a[36:39], v[172:175]\n"
    "ds_read2st64_b32 v[212:213], v250 offset0:16 offset1:17\n"
    "v_cvt_pk_bf16_f32 v135, v146, v147\n"
    "v_mfma_f32_16x16x32_bf16 v[172:175], a[172:175], a[40:43], v[172:175]\n"
    "ds_read_b64_tr_b16 a[164:165], v244 offset:57344\n"
    "ds_read_b64_tr_b16 a[166:167], v244 offset:59392\n"
    "buffer_load_dwordx4 v248, s[8:11], s40 offen offset:3072 sc1 lds\n"
    "v_mfma_f32_16x16x32_bf16 v[164:167], a[172:175], a[8:11], v[164:167]\n"
    "s_add_u32 s40, s40, 32768\n"
    "ds_read2st64_b32 v[214:215], v250 offset0:18 offset1:19\n"
    "v_cvt_pk_bf16_f32 v128, v128, v129\n"
    "v_mfma_f32_16x16x32_bf16 v[164:167], a[168:171], a[4:7], v[164:167]\n"
    "s_waitcnt lgkmcnt(7)\n"
    "s_add_u32 m0, s43, 36864\n"
    "ds_read_b64_tr_b16 a[168:169], v245 offset:49152\n"
    "ds_read_b64_tr_b16 a[170:171], v245 offset:51200\n"
    "v_mfma_f32_16x16x32_bf16 v[164:167], a[176:179], a[12:15], v[164:167]\n"
    "buffer_load_dwordx4 v248, s[8:11], s41 offen offset:0 sc1 lds\n"
    "v_cvt_pk_bf16_f32 v129, v130, v131\n"
    "v_mfma_f32_16x16x32_bf16 v[164:167], a[180:183], a[16:19], v[164:167]\n"
    "ds_read_b64_tr_b16 a[172:173], v245 offset:57344\n"
    "ds_read_b64_tr_b16 a[174:175], v245 offset:59392\n"
    "v_mfma_f32_16x16x32_bf16 v[172:175], a[180:183], a[48:51], v[172:175]\n"
    "v_exp_f32_e32 v170, v170\n"
    "v_mfma_f32_16x16x32_bf16 v[172:175], a[176:179], a[44:47], v[172:175]\n"
    "ds_read_b64_tr_b16 a[176:177], v246 offset:49152\n"
    "ds_read_b64_tr_b16 a[178:179], v246 offset:51200\n"
    "buffer_load_dwordx4 v248, s[8:11], s41 offen offset:1024 sc1 lds\n"
    "v_mfma_f32_16x16x32_bf16 v[172:175], a[184:187], a[52:55], v[172:175]\n"
    "v_exp_f32_e32 v171, v171\n"
    "v_mfma_f32_16x16x32_bf16 v[172:175], a[188:191], a[56:59], v[172:175]\n"
    "ds_read_b64_tr_b16 a[180:181], v246 offset:57344\n"
    "ds_read_b64_tr_b16 a[182:183], v246 offset:59392\n"
    "v_mfma_f32_16x16x32_bf16 v[164:167], a[188:191], a[24:27], v[164:167]\n"
    "buffer_load_dwordx4 v248, s[8:11], s41 offen offset:2048 sc1 lds\n"
    "v_cvt_pk_bf16_f32 v168, v168, v169\n"
    "v_mfma_f32_16x16x32_bf16 v[164:167], a[184:187], a[20:23], v[164:167]\n"
    "s_waitcnt lgkmcnt(4)\n"
    "ds_read_b64_tr_b16 a[184:185], v247 offset:49152\n"
    "ds_read_b64_tr_b16 a[186:187], v247 offset:51200\n"
    "v_mfma_f32_16x16x32_bf16 v[0:3], a[160:163], v[200:203], v[0:3]\n"
    "v_exp_f32_e32 v160, v160\n"
    "v_mfma_f32_16x16x32_bf16 v[0:3], a[164:167], v[204:207], v[0:3]\n"
    "ds_read_b64_tr_b16 a[188:189], v247 offset:57344\n"
    "ds_read_b64_tr_b16 a[190:191], v247 offset:59392\n"
    "buffer_load_dwordx4 v248, s[8:11], s41 offen offset:3072 sc1 lds\n"
    "v_mfma_f32_16x16x32_bf16 a[248:251], a[164:167], v[212:215], a[248:251]\n"
    "s_add_u32 s41, s41, 32768\n"
    "v_exp_f32_e32 v161, v161\n"
    "v_mfma_f32_16x16x32_bf16 a[248:251], a[160:163], v[208:211], a[248:251]\n"
    "s_mov_b32 m0, s49\n"
    "ds_read_b64_tr_b16 a[160:161], v244 offset:53248\n"
    "ds_read_b64_tr_b16 a[162:163], v244 offset:55296\n"
    "v_mfma_f32_16x16x32_bf16 a[252:255], a[168:171], v[208:211], a[252:255]\n"
    "v_cvt_pk_bf16_f32 v169, v170, v171\n"
    "v_cvt_pk_bf16_f32 v130, v140, v141\n"
    "v_mfma_f32_16x16x32_bf16 a[252:255], a[172:175], v[212:215], a[252:255]\n"
    "s_barrier\n"
    "v_mfma_f32_16x16x32_bf16 v[4:7], a[172:175], v[204:207], v[4:7]\n"
    "ds_read_b64_tr_b16 a[164:165], v244 offset:61440\n"
    "ds_read_b64_tr_b16 a[166:167], v244 offset:63488\n"
    "v_mfma_f32_16x16x32_bf16 v[4:7], a[168:171], v[200:203], v[4:7]\n"
    "s_waitcnt lgkmcnt(4)\n"
    "v_exp_f32_e32 v162, v162\n"
    "v_mfma_f32_16x16x32_bf16 v[8:11], a[176:179], v[200:203], v[8:11]\n"
    "s_add_u32 s45, s45, 16384\n"
    "ds_read_b64_tr_b16 a[168:169], v245 offset:53248\n"
    "ds_read_b64_tr_b16 a[170:171], v245 offset:55296\n"
    "v_lshrrev_b32_e32 v255, 2, v253\n"
    "v_mfma_f32_16x16x32_bf16 v[8:11], a[180:183], v[204:207], v[8:11]\n"
    "buffer_store_dwordx4 v[104:107], v253, s[8:11], s84 offen offset:2048 sc1\n"
    "v_exp_f32_e32 v163, v163\n"
    "v_mfma_f32_16x16x32_bf16 v[32:35], a[180:183], v[212:215], v[32:35]\n"
    "ds_read_b64_tr_b16 a[172:173], v245 offset:61440\n"
    "ds_read_b64_tr_b16 a[174:175], v245 offset:63488\n"
    "v_mfma_f32_16x16x32_bf16 v[32:35], a[176:179], v[208:211], v[32:35]\n"
    "v_cvt_pk_bf16_f32 v160, v160, v161\n"
    "v_cvt_pk_bf16_f32 v161, v162, v163\n"
    "v_mfma_f32_16x16x32_bf16 v[36:39], a[184:187], v[208:211], v[36:39]\n"
    "ds_read_b64_tr_b16 a[176:177], v246 offset:53248\n"
    "ds_read_b64_tr_b16 a[178:179], v246 offset:55296\n"
    "v_mfma_f32_16x16x32_bf16 v[36:39], a[188:191], v[212:215], v[36:39]\n"
    "buffer_store_dwordx4 v[136:139], v253, s[8:11], s86 offen offset:3072 sc1\n"
    "v_exp_f32_e32 v172, v172\n"
    "v_add_u32_e32 v255, s45, v255\n"
    "v_mfma_f32_16x16x32_bf16 v[12:15], a[188:191], v[204:207], v[12:15]\n"
    "ds_read_b64_tr_b16 a[180:181], v246 offset:61440\n"
    "ds_read_b64_tr_b16 a[182:183], v246 offset:63488\n"
    "v_mfma_f32_16x16x32_bf16 v[12:15], a[184:187], v[200:203], v[12:15]\n"
    "s_waitcnt lgkmcnt(4)\n"
    "v_exp_f32_e32 v173, v173\n"
    "v_mfma_f32_16x16x32_bf16 v[16:19], a[160:163], v[200:203], v[16:19]\n"
    "ds_read_b64_tr_b16 a[184:185], v247 offset:53248\n"
    "ds_read_b64_tr_b16 a[186:187], v247 offset:55296\n"
    "v_mfma_f32_16x16x32_bf16 v[16:19], a[164:167], v[204:207], v[16:19]\n"
    "v_exp_f32_e32 v174, v174\n"
    "v_mfma_f32_16x16x32_bf16 v[40:43], a[164:167], v[212:215], v[40:43]\n"
    "s_lshl_b32 s45, s89, 8\n"
    "ds_read_b64_tr_b16 a[188:189], v247 offset:61440\n"
    "ds_read_b64_tr_b16 a[190:191], v247 offset:63488\n"
    "v_add_u32_e32 v254, s45, v255\n"
    "v_mfma_f32_16x16x32_bf16 v[40:43], a[160:163], v[208:211], v[40:43]\n"
    "buffer_store_dwordx4 v[132:135], v253, s[8:11], s85 offen offset:3072 sc1\n"
    "v_exp_f32_e32 v175, v175\n"
    "v_mfma_f32_16x16x32_bf16 v[44:47], a[168:171], v[208:211], v[44:47]\n"
    "ds_read_b128 a[160:163], v243 offset:20480\n"
    "v_cvt_pk_bf16_f32 v170, v172, v173\n"
    "v_mfma_f32_16x16x32_bf16 v[44:47], a[172:175], v[212:215], v[44:47]\n"
    "ds_read_b128 a[164:167], v240 offset:16384\n"
    "ds_write_addtid_b32 v168 offset:4096\n"
    "v_mfma_f32_16x16x32_bf16 v[20:23], a[172:175], v[204:207], v[20:23]\n"
    "s_lshl_b32 s45, s50, 8\n"
    "v_exp_f32_e32 v164, v164\n"
    "v_add_u32_e32 v255, s45, v255\n"
    "v_mfma_f32_16x16x32_bf16 v[20:23], a[168:171], v[200:203], v[20:23]\n"
    "s_waitcnt lgkmcnt(3)\n"
    "ds_read_b128 a[168:171], v241 offset:16384\n"
    "ds_write_addtid_b32 v169 offset:4352\n"
    "v_mfma_f32_16x16x32_bf16 v[24:27], a[176:179], v[200:203], v[24:27]\n"
    "ds_read_b128 a[172:175], v240 offset:20480\n"
    "v_cvt_pk_bf16_f32 v131, v142, v143\n"
    "v_mfma_f32_16x16x32_bf16 v[24:27], a[180:183], v[204:207], v[24:27]\n"
    "buffer_store_dwordx4 v[128:131], v253, s[8:11], s84 offen offset:3072 sc1\n"
    "v_exp_f32_e32 v165, v165\n"
    "v_mfma_f32_16x16x32_bf16 v[48:51], a[180:183], v[212:215], v[48:51]\n"
    "ds_write_addtid_b32 v160 offset:0\n"
    "v_cvt_pk_bf16_f32 v171, v174, v175\n"
    "v_permlane16_swap_b32_e32 v147, v151\n"
    "v_mfma_f32_16x16x32_bf16 v[48:51], a[176:179], v[208:211], v[48:51]\n"
    "ds_read_b128 a[176:179], v241 offset:20480\n"
    "v_mov_b32_e32 v148, s39\n"
    "v_mfma_f32_16x16x32_bf16 v[52:55], a[184:187], v[208:211], v[52:55]\n"
    "ds_read_b128 a[180:183], v242 offset:16384\n"
    "ds_write_addtid_b32 v161 offset:256\n"
    "v_mfma_f32_16x16x32_bf16 v[52:55], a[188:191], v[212:215], v[52:55]\n"
    "v_exp_f32_e32 v166, v166\n"
    "v_mov_b32_e32 v149, 0\n"
    "v_mfma_f32_16x16x32_bf16 v[28:31], a[188:191], v[204:207], v[28:31]\n"
    "v_exp_f32_e32 v167, v167\n"
    "v_permlane32_swap_b32_e32 v143, v151\n"
    "v_mfma_f32_16x16x32_bf16 v[28:31], a[184:187], v[200:203], v[28:31]\n"
    "s_waitcnt lgkmcnt(4)\n"
    "ds_read_b128 a[184:187], v243 offset:16384\n"
    "v_cvt_pk_bf16_f32 v162, v164, v165\n"
    "v_mfma_f32_16x16x32_bf16 v[200:203], a[160:163], a[28:31], 0\n"
    "ds_read_b128 a[188:191], v242 offset:20480\n"
    "v_mfma_f32_16x16x32_bf16 v[200:203], a[164:167], a[0:3], v[200:203]\n"
    "ds_write_addtid_b32 v170 offset:4608\n"
    "buffer_store_dword v151, v251, s[8:11], s60 offen offset:0 sc1\n"
    "v_cvt_pk_bf16_f32 v163, v166, v167\n"
    "v_mfma_f32_16x16x32_bf16 v[208:211], a[164:167], a[32:35], 0\n"
    "ds_write_addtid_b32 v171 offset:4864\n"
    "v_mfma_f32_16x16x32_bf16 v[208:211], a[160:163], a[60:63], v[208:211]\n"
    "ds_read_b128 a[160:163], v243 offset:22528\n"
    "v_lshrrev_b32_e32 v147, 4, v253\n"
    "v_mfma_f32_16x16x32_bf16 v[208:211], a[168:171], a[36:39], v[208:211]\n"
    "ds_read_b128 a[164:167], v240 offset:18432\n"
    "v_mfma_f32_16x16x32_bf16 v[208:211], a[172:175], a[40:43], v[208:211]\n"
    "ds_write_addtid_b32 v162 offset:512\n"
    "v_mfma_f32_16x16x32_bf16 v[200:203], a[172:175], a[8:11], v[200:203]\n"
    "ds_write_addtid_b32 v163 offset:768\n"
    "v_bfe_u32 v145, v147, 2, 2\n"
    "v_mfma_f32_16x16x32_bf16 v[200:203], a[168:171], a[4:7], v[200:203]\n"
    "s_waitcnt lgkmcnt(6)\n"
    "ds_read_b128 a[168:171], v241 offset:18432\n"
    "v_mfma_f32_16x16x32_bf16 v[200:203], a[176:179], a[12:15], v[200:203]\n"
    "ds_read_b128 a[172:175], v240 offset:22528\n"
    "v_mfma_f32_16x16x32_bf16 v[200:203], a[180:183], a[16:19], v[200:203]\n"
    "s_waitcnt lgkmcnt(2)\n"
    "v_min_u32_e32 v145, 2, v145\n"
    "v_mfma_f32_16x16x32_bf16 v[208:211], a[180:183], a[48:51], v[208:211]\n"
    "s_barrier\n"
    "v_mfma_f32_16x16x32_bf16 v[208:211], a[176:179], a[44:47], v[208:211]\n"
    "s_add_u32 s46, s32, 1\n"
    "ds_read_b128 a[176:179], v241 offset:22528\n"
    "v_mfma_f32_16x16x32_bf16 v[208:211], a[184:187], a[52:55], v[208:211]\n"
    "ds_read_b128 a[180:183], v242 offset:18432\n"
    "v_mfma_f32_16x16x32_bf16 v[208:211], a[188:191], a[56:59], v[208:211]\n"
    "ds_read2st64_b32 v[164:165], v250 offset0:0 offset1:1\n"
    "v_mfma_f32_16x16x32_bf16 v[200:203], a[188:191], a[24:27], v[200:203]\n"
    "v_add_u32_e32 v145, s46, v145\n"
    "v_mfma_f32_16x16x32_bf16 v[200:203], a[184:187], a[20:23], v[200:203]\n"
    "s_waitcnt lgkmcnt(3)\n"
    "ds_read_b128 a[184:187], v243 offset:18432\n"
    "v_and_b32_e32 v145, 3, v145\n"
    "v_mfma_f32_16x16x32_bf16 v[204:207], a[160:163], a[28:31], 0\n"
    "ds_read_b128 a[188:191], v242 offset:22528\n"
    "v_lshlrev_b32_e32 v145, 6, v145\n"
    "v_mfma_f32_16x16x32_bf16 v[204:207], a[164:167], a[0:3], v[204:207]\n"
    "ds_read2st64_b32 v[166:167], v250 offset0:2 offset1:3\n"
    "v_and_or_b32 v145, v147, 3, v145\n"
    "v_mfma_f32_16x16x32_bf16 v[212:215], a[164:167], a[32:35], 0\n"
    "v_exp_f32_e32 v208, v208\n"
    "v_mfma_f32_16x16x32_bf16 v[212:215], a[160:163], a[60:63], v[212:215]\n"
    "ds_read_b64_tr_b16 a[160:161], v244 offset:0\n"
    "ds_read_b64_tr_b16 a[162:163], v244 offset:2048\n"
    "v_mfma_f32_16x16x32_bf16 v[212:215], a[168:171], a[36:39], v[212:215]\n"
    "ds_read2st64_b32 v[172:173], v250 offset0:16 offset1:17\n"
    "v_lshlrev_b32_e32 v145, 2, v145\n"
    "v_mfma_f32_16x16x32_bf16 v[212:215], a[172:175], a[40:43], v[212:215]\n"
    "ds_read_b64_tr_b16 a[164:165], v244 offset:8192\n"
    "ds_read_b64_tr_b16 a[166:167], v244 offset:10240\n"
    "v_mfma_f32_16x16x32_bf16 v[204:207], a[172:175], a[8:11], v[204:207]\n"
    "s_lshl_b32 s47, s33, 10\n"
    "v_exp_f32_e32 v209, v209\n"
    "v_mfma_f32_16x16x32_bf16 v[204:207], a[168:171], a[4:7], v[204:207]\n"
    "s_waitcnt lgkmcnt(6)\n"
    "ds_read_b64_tr_b16 a[168:169], v245 offset:0\n"
    "ds_read_b64_tr_b16 a[170:171], v245 offset:2048\n"
    "v_mfma_f32_16x16x32_bf16 v[204:207], a[176:179], a[12:15], v[204:207]\n"
    "s_add_u32 s47, s47, 5636096\n"
    "ds_read2st64_b32 v[174:175], v250 offset0:18 offset1:19\n"
    "v_cmp_gt_u32_e32 vcc, 0x300, v253\n"
    "v_mfma_f32_16x16x32_bf16 v[204:207], a[180:183], a[16:19], v[204:207]\n"
    "s_add_u32 s48, s56, 0\n"
    "ds_read_b64_tr_b16 a[172:173], v245 offset:8192\n"
    "ds_read_b64_tr_b16 a[174:175], v245 offset:10240\n"
    "v_mfma_f32_16x16x32_bf16 v[212:215], a[180:183], a[48:51], v[212:215]\n"
    "v_exp_f32_e32 v210, v210\n"
    "v_mfma_f32_16x16x32_bf16 v[212:215], a[176:179], a[44:47], v[212:215]\n"
    "ds_read_b64_tr_b16 a[176:177], v246 offset:0\n"
    "ds_read_b64_tr_b16 a[178:179], v246 offset:2048\n"
    "v_mfma_f32_16x16x32_bf16 v[212:215], a[184:187], a[52:55], v[212:215]\n"
    "buffer_load_dwordx4 v[56:59], v253, s[8:11], s48 offen offset:0 sc1\n"
    "v_exp_f32_e32 v211, v211\n"
    "v_mfma_f32_16x16x32_bf16 v[212:215], a[188:191], a[56:59], v[212:215]\n"
    "ds_read_b64_tr_b16 a[180:181], v246 offset:8192\n"
    "ds_read_b64_tr_b16 a[182:183], v246 offset:10240\n"
    "v_mfma_f32_16x16x32_bf16 v[204:207], a[188:191], a[24:27], v[204:207]\n"
    "s_add_u32 s60, s60, s28\n"
    "v_cvt_pk_bf16_f32 v208, v208, v209\n"
    "v_and_b32_e32 v251, 0xf0, v253\n"
    "v_mfma_f32_16x16x32_bf16 v[204:207], a[184:187], a[20:23], v[204:207]\n"
    "s_waitcnt lgkmcnt(4)\n"
    "ds_read_b64_tr_b16 a[184:185], v247 offset:0\n"
    "ds_read_b64_tr_b16 a[186:187], v247 offset:2048\n"
    "v_mfma_f32_16x16x32_bf16 v[0:3], a[160:163], v[160:163], v[0:3]\n"
    "buffer_load_dwordx4 v[60:63], v253, s[8:11], s48 offen offset:1024 sc1\n"
    "v_exp_f32_e32 v200, v200\n"
    "v_mfma_f32_16x16x32_bf16 v[0:3], a[164:167], v[164:167], v[0:3]\n"
    "ds_read_b64_tr_b16 a[188:189], v247 offset:8192\n"
    "ds_read_b64_tr_b16 a[190:191], v247 offset:10240\n"
    "v_mfma_f32_16x16x32_bf16 a[248:251], a[164:167], v[172:175], a[248:251]\n"
    "v_exp_f32_e32 v201, v201\n"
    "v_mfma_f32_16x16x32_bf16 a[248:251], a[160:163], v[168:171], a[248:251]\n"
    "ds_read_b64_tr_b16 a[160:161], v244 offset:4096\n"
    "ds_read_b64_tr_b16 a[162:163], v244 offset:6144\n"
    "v_mfma_f32_16x16x32_bf16 a[252:255], a[168:171], v[168:171], a[252:255]\n"
    "buffer_load_dwordx4 v[64:67], v253, s[8:11], s48 offen offset:2048 sc1\n"
    "v_cvt_pk_bf16_f32 v209, v210, v211\n"
    "v_cndmask_b32_e64 v251, v251, 2.0, vcc\n"
    "v_mfma_f32_16x16x32_bf16 a[252:255], a[172:175], v[172:175], a[252:255]\n"
    "s_waitcnt vmcnt(8)\n"
    "s_barrier\n"
    "v_mfma_f32_16x16x32_bf16 v[4:7], a[172:175], v[164:167], v[4:7]\n"
    "ds_read_b64_tr_b16 a[164:165], v244 offset:12288\n"
    "ds_read_b64_tr_b16 a[166:167], v244 offset:14336\n"
    "v_mfma_f32_16x16x32_bf16 v[4:7], a[168:171], v[160:163], v[4:7]\n"
    "s_waitcnt lgkmcnt(4)\n"
    "v_exp_f32_e32 v202, v202\n"
    "v_mfma_f32_16x16x32_bf16 v[8:11], a[176:179], v[160:163], v[8:11]\n"
    "ds_read_b64_tr_b16 a[168:169], v245 offset:4096\n"
    "ds_read_b64_tr_b16 a[170:171], v245 offset:6144\n"
    "buffer_load_dwordx4 v[68:71], v253, s[8:11], s48 offen offset:3072 sc1\n"
    "v_mfma_f32_16x16x32_bf16 v[8:11], a[180:183], v[164:167], v[8:11]\n"
    "s_add_u32 s48, s56, 4096\n"
    "v_exp_f32_e32 v203, v203\n"
    "v_mfma_f32_16x16x32_bf16 v[32:35], a[180:183], v[172:175], v[32:35]\n"
    "ds_read_b64_tr_b16 a[172:173], v245 offset:12288\n"
    "ds_read_b64_tr_b16 a[174:175], v245 offset:14336\n"
    "v_mfma_f32_16x16x32_bf16 v[32:35], a[176:179], v[168:171], v[32:35]\n"
    "v_cvt_pk_bf16_f32 v200, v200, v201\n"
    "v_cvt_pk_bf16_f32 v201, v202, v203\n"
    "v_mfma_f32_16x16x32_bf16 v[36:39], a[184:187], v[168:171], v[36:39]\n"
    "ds_read_b64_tr_b16 a[176:177], v246 offset:4096\n"
    "ds_read_b64_tr_b16 a[178:179], v246 offset:6144\n"
    "buffer_load_dwordx4 v[72:75], v253, s[8:11], s48 offen offset:0 sc1\n"
    "v_mfma_f32_16x16x32_bf16 v[36:39], a[188:191], v[172:175], v[36:39]\n"
    "v_exp_f32_e32 v212, v212\n"
    "v_mfma_f32_16x16x32_bf16 v[12:15], a[188:191], v[164:167], v[12:15]\n"
    "ds_read_b64_tr_b16 a[180:181], v246 offset:12288\n"
    "ds_read_b64_tr_b16 a[182:183], v246 offset:14336\n"
    "v_mfma_f32_16x16x32_bf16 v[12:15], a[184:187], v[160:163], v[12:15]\n"
    "s_waitcnt lgkmcnt(4)\n"
    "v_exp_f32_e32 v213, v213\n"
    "v_mfma_f32_16x16x32_bf16 v[16:19], a[160:163], v[160:163], v[16:19]\n"
    "ds_read_b64_tr_b16 a[184:185], v247 offset:4096\n"
    "ds_read_b64_tr_b16 a[186:187], v247 offset:6144\n"
    "buffer_load_dwordx4 v[76:79], v253, s[8:11], s48 offen offset:1024 sc1\n"
    "v_mfma_f32_16x16x32_bf16 v[16:19], a[164:167], v[164:167], v[16:19]\n"
    "v_exp_f32_e32 v214, v214\n"
    "v_mfma_f32_16x16x32_bf16 v[40:43], a[164:167], v[172:175], v[40:43]\n"
    "ds_read_b64_tr_b16 a[188:189], v247 offset:12288\n"
    "ds_read_b64_tr_b16 a[190:191], v247 offset:14336\n"
    "v_mfma_f32_16x16x32_bf16 v[40:43], a[160:163], v[168:171], v[40:43]\n"
    "v_exp_f32_e32 v215, v215\n"
    "v_mfma_f32_16x16x32_bf16 v[44:47], a[168:171], v[168:171], v[44:47]\n"
    "ds_read_b128 a[160:163], v243 offset:36864\n"
    "buffer_load_dwordx4 v[80:83], v253, s[8:11], s48 offen offset:2048 sc1\n"
    "v_cvt_pk_bf16_f32 v210, v212, v213\n"
    "v_mfma_f32_16x16x32_bf16 v[44:47], a[172:175], v[172:175], v[44:47]\n"
    "ds_read_b128 a[164:167], v240 offset:32768\n"
    "ds_write_addtid_b32 v208 offset:4096\n"
    "v_mfma_f32_16x16x32_bf16 v[20:23], a[172:175], v[164:167], v[20:23]\n"
    "v_exp_f32_e32 v204, v204\n"
    "v_mfma_f32_16x16x32_bf16 v[20:23], a[168:171], v[160:163], v[20:23]\n"
    "s_waitcnt lgkmcnt(3)\n"
    "ds_read_b128 a[168:171], v241 offset:32768\n"
    "ds_write_addtid_b32 v209 offset:4352\n"
    "v_mfma_f32_16x16x32_bf16 v[24:27], a[176:179], v[160:163], v[24:27]\n"
    "ds_read_b128 a[172:175], v240 offset:36864\n"
    "ds_write_addtid_b32 v200 offset:0\n"
    "buffer_load_dwordx4 v[84:87], v253, s[8:11], s48 offen offset:3072 sc1\n"
    "v_mfma_f32_16x16x32_bf16 v[24:27], a[180:183], v[164:167], v[24:27]\n"
    "s_add_u32 s48, s56, 8192\n"
    "v_exp_f32_e32 v205, v205\n"
    "v_mfma_f32_16x16x32_bf16 v[48:51], a[180:183], v[172:175], v[48:51]\n"
    "s_waitcnt vmcnt(8)\n"
    "ds_write_addtid_b32 v201 offset:256\n"
    "buffer_store_dword v148, v149, s[8:11], s87 offen offset:128 sc1\n"
    "v_cvt_pk_bf16_f32 v211, v214, v215\n"
    "v_mfma_f32_16x16x32_bf16 v[48:51], a[176:179], v[168:171], v[48:51]\n"
    "ds_read_b128 a[176:179], v241 offset:36864\n"
    "v_mfma_f32_16x16x32_bf16 v[52:55], a[184:187], v[168:171], v[52:55]\n"
    "ds_read_b128 a[180:183], v242 offset:32768\n"
    "buffer_load_dwordx4 v[88:91], v253, s[8:11], s48 offen offset:0 sc1\n"
    "v_mfma_f32_16x16x32_bf16 v[52:55], a[188:191], v[172:175], v[52:55]\n"
    "v_exp_f32_e32 v206, v206\n"
    "v_mfma_f32_16x16x32_bf16 v[28:31], a[188:191], v[164:167], v[28:31]\n"
    "v_exp_f32_e32 v207, v207\n"
    "v_mfma_f32_16x16x32_bf16 v[28:31], a[184:187], v[160:163], v[28:31]\n"
    "s_waitcnt lgkmcnt(4)\n"
    "ds_read_b128 a[184:187], v243 offset:32768\n"
    "v_cvt_pk_bf16_f32 v202, v204, v205\n"
    "v_mfma_f32_16x16x32_bf16 v[160:163], a[160:163], a[28:31], 0\n"
    "ds_read_b128 a[188:191], v242 offset:36864\n"
    "buffer_load_dwordx4 v[92:95], v253, s[8:11], s48 offen offset:1024 sc1\n"
    "v_mfma_f32_16x16x32_bf16 v[160:163], a[164:167], a[0:3], v[160:163]\n"
    "ds_write_addtid_b32 v210 offset:4608\n"
    "v_cvt_pk_bf16_f32 v203, v206, v207\n"
    "v_mfma_f32_16x16x32_bf16 v[168:171], a[164:167], a[32:35], 0\n"
    "ds_write_addtid_b32 v211 offset:4864\n"
    "v_mfma_f32_16x16x32_bf16 v[168:171], a[160:163], a[60:63], v[168:171]\n"
    "ds_read_b128 a[160:163], v243 offset:38912\n"
    "v_mfma_f32_16x16x32_bf16 v[168:171], a[168:171], a[36:39], v[168:171]\n"
    "ds_read_b128 a[164:167], v240 offset:34816\n"
    "buffer_load_dwordx4 v[96:99], v253, s[8:11], s48 offen offset:2048 sc1\n"
    "v_mfma_f32_16x16x32_bf16 v[168:171], a[172:175], a[40:43], v[168:171]\n"
    "ds_write_addtid_b32 v202 offset:512\n"
    "v_mfma_f32_16x16x32_bf16 v[160:163], a[172:175], a[8:11], v[160:163]\n"
    "ds_write_addtid_b32 v203 offset:768\n"
    "v_mfma_f32_16x16x32_bf16 v[160:163], a[168:171], a[4:7], v[160:163]\n"
    "s_waitcnt lgkmcnt(6)\n"
    "ds_read_b128 a[168:171], v241 offset:34816\n"
    "v_mfma_f32_16x16x32_bf16 v[160:163], a[176:179], a[12:15], v[160:163]\n"
    "ds_read_b128 a[172:175], v240 offset:38912\n"
    "buffer_load_dwordx4 v[100:103], v253, s[8:11], s48 offen offset:3072 sc1\n"
    "v_mfma_f32_16x16x32_bf16 v[160:163], a[180:183], a[16:19], v[160:163]\n"
    "s_waitcnt lgkmcnt(2)\n"
    "s_add_u32 s48, s56, 12288\n"
    "v_mfma_f32_16x16x32_bf16 v[168:171], a[180:183], a[48:51], v[168:171]\n"
    "s_barrier\n"
    "v_mfma_f32_16x16x32_bf16 v[168:171], a[176:179], a[44:47], v[168:171]\n"
    "ds_read_b128 a[176:179], v241 offset:38912\n"
    "v_mfma_f32_16x16x32_bf16 v[168:171], a[184:187], a[52:55], v[168:171]\n"
    "ds_read_b128 a[180:183], v242 offset:34816\n"
    "buffer_load_dwordx4 v[104:107], v253, s[8:11], s48 offen offset:0 sc1\n"
    "v_mfma_f32_16x16x32_bf16 v[168:171], a[188:191], a[56:59], v[168:171]\n"
    "ds_read2st64_b32 v[204:205], v250 offset0:0 offset1:1\n"
    "v_mfma_f32_16x16x32_bf16 v[160:163], a[188:191], a[24:27], v[160:163]\n"
    "v_mfma_f32_16x16x32_bf16 v[160:163], a[184:187], a[20:23], v[160:163]\n"
    "s_waitcnt lgkmcnt(3)\n"
    "ds_read_b128 a[184:187], v243 offset:34816\n"
    "v_exp_f32_e32 v168, v168\n"
    "v_mfma_f32_16x16x32_bf16 v[164:167], a[160:163], a[28:31], 0\n"
    "ds_read_b128 a[188:191], v242 offset:38912\n"
    "buffer_load_dwordx4 v[108:111], v253, s[8:11], s48 offen offset:1024 sc1\n"
    "v_exp_f32_e32 v169, v169\n"
    "v_mfma_f32_16x16x32_bf16 v[164:167], a[164:167], a[0:3], v[164:167]\n"
    "ds_read2st64_b32 v[206:207], v250 offset0:2 offset1:3\n"
    "v_exp_f32_e32 v170, v170\n"
    "v_mfma_f32_16x16x32_bf16 v[172:175], a[164:167], a[32:35], 0\n"
    "v_exp_f32_e32 v171, v171\n"
    "v_mfma_f32_16x16x32_bf16 v[172:175], a[160:163], a[60:63], v[172:175]\n"
    "ds_read_b64_tr_b16 a[160:161], v244 offset:16384\n"
    "ds_read_b64_tr_b16 a[162:163], v244 offset:18432\n"
    "v_mfma_f32_16x16x32_bf16 v[172:175], a[168:171], a[36:39], v[172:175]\n"
    "ds_read2st64_b32 v[212:213], v250 offset0:16 offset1:17\n"
    "buffer_load_dwordx4 v[112:115], v253, s[8:11], s48 offen offset:2048 sc1\n"
    "v_cvt_pk_bf16_f32 v168, v168, v169\n"
    "v_mfma_f32_16x16x32_bf16 v[172:175], a[172:175], a[40:43], v[172:175]\n"
    "ds_read_b64_tr_b16 a[164:165], v244 offset:24576\n"
    "ds_read_b64_tr_b16 a[166:167], v244 offset:26624\n"
    "v_mfma_f32_16x16x32_bf16 v[164:167], a[172:175], a[8:11], v[164:167]\n"
    "v_exp_f32_e32 v160, v160\n"
    "v_mfma_f32_16x16x32_bf16 v[164:167], a[168:171], a[4:7], v[164:167]\n"
    "s_waitcnt lgkmcnt(6)\n"
    "ds_read_b64_tr_b16 a[168:169], v245 offset:16384\n"
    "ds_read_b64_tr_b16 a[170:171], v245 offset:18432\n"
    "v_mfma_f32_16x16x32_bf16 v[164:167], a[176:179], a[12:15], v[164:167]\n"
    "ds_read2st64_b32 v[214:215], v250 offset0:18 offset1:19\n"
    "buffer_load_dwordx4 v[116:119], v253, s[8:11], s48 offen offset:3072 sc1\n"
    "v_mfma_f32_16x16x32_bf16 v[164:167], a[180:183], a[16:19], v[164:167]\n"
    "s_add_u32 s48, s56, 16384\n"
    "ds_read_b64_tr_b16 a[172:173], v245 offset:24576\n"
    "ds_read_b64_tr_b16 a[174:175], v245 offset:26624\n"
    "v_mfma_f32_16x16x32_bf16 v[172:175], a[180:183], a[48:51], v[172:175]\n"
    "v_exp_f32_e32 v161, v161\n"
    "v_mfma_f32_16x16x32_bf16 v[172:175], a[176:179], a[44:47], v[172:175]\n"
    "ds_read_b64_tr_b16 a[176:177], v246 offset:16384\n"
    "ds_read_b64_tr_b16 a[178:179], v246 offset:18432\n"
    "v_cvt_pk_bf16_f32 v169, v170, v171\n"
    "v_mfma_f32_16x16x32_bf16 v[172:175], a[184:187], a[52:55], v[172:175]\n"
    "buffer_load_dwordx4 a[28:31], v253, s[8:11], s48 offen offset:0 sc1\n"
    "v_exp_f32_e32 v162, v162\n"
    "v_mfma_f32_16x16x32_bf16 v[172:175], a[188:191], a[56:59], v[172:175]\n"
    "ds_read_b64_tr_b16 a[180:181], v246 offset:24576\n"
    "ds_read_b64_tr_b16 a[182:183], v246 offset:26624\n"
    "v_mfma_f32_16x16x32_bf16 v[164:167], a[188:191], a[24:27], v[164:167]\n"
    "v_exp_f32_e32 v163, v163\n"
    "v_mfma_f32_16x16x32_bf16 v[164:167], a[184:187], a[20:23], v[164:167]\n"
    "s_waitcnt lgkmcnt(4)\n"
    "ds_read_b64_tr_b16 a[184:185], v247 offset:16384\n"
    "ds_read_b64_tr_b16 a[186:187], v247 offset:18432\n"
    "v_mfma_f32_16x16x32_bf16 v[0:3], a[160:163], v[200:203], v[0:3]\n"
    "buffer_load_dwordx4 a[60:63], v253, s[8:11], s48 offen offset:1024 sc1\n"
    "v_cvt_pk_bf16_f32 v160, v160, v161\n"
    "v_cvt_pk_bf16_f32 v161, v162, v163\n"
    "v_mfma_f32_16x16x32_bf16 v[0:3], a[164:167], v[204:207], v[0:3]\n"
    "ds_read_b64_tr_b16 a[188:189], v247 offset:24576\n"
    "ds_read_b64_tr_b16 a[190:191], v247 offset:26624\n"
    "v_mfma_f32_16x16x32_bf16 a[248:251], a[164:167], v[212:215], a[248:251]\n"
    "v_exp_f32_e32 v172, v172\n"
    "v_mfma_f32_16x16x32_bf16 a[248:251], a[160:163], v[208:211], a[248:251]\n"
    "ds_read_b64_tr_b16 a[160:161], v244 offset:20480\n"
    "ds_read_b64_tr_b16 a[162:163], v244 offset:22528\n"
    "v_mfma_f32_16x16x32_bf16 a[252:255], a[168:171], v[208:211], a[252:255]\n"
    "buffer_load_dwordx4 a[0:3], v253, s[8:11], s48 offen offset:2048 sc1\n"
    "v_exp_f32_e32 v173, v173\n"
    "v_mfma_f32_16x16x32_bf16 a[252:255], a[172:175], v[212:215], a[252:255]\n"
    "s_waitcnt vmcnt(41)\n"
    "s_barrier\n"
    "v_mfma_f32_16x16x32_bf16 v[4:7], a[172:175], v[204:207], v[4:7]\n"
    "ds_read_b64_tr_b16 a[164:165], v244 offset:28672\n"
    "ds_read_b64_tr_b16 a[166:167], v244 offset:30720\n"
    "v_mfma_f32_16x16x32_bf16 v[4:7], a[168:171], v[200:203], v[4:7]\n"
    "s_waitcnt lgkmcnt(4)\n"
    "ds_write_addtid_b32 v168 offset:4096\n"
    "v_exp_f32_e32 v174, v174\n"
    "v_mfma_f32_16x16x32_bf16 v[8:11], a[176:179], v[200:203], v[8:11]\n"
    "ds_read_b64_tr_b16 a[168:169], v245 offset:20480\n"
    "ds_read_b64_tr_b16 a[170:171], v245 offset:22528\n"
    "buffer_load_dwordx4 a[32:35], v253, s[8:11], s48 offen offset:3072 sc1\n"
    "v_mfma_f32_16x16x32_bf16 v[8:11], a[180:183], v[204:207], v[8:11]\n"
    "s_add_u32 s48, s56, 20480\n"
    "ds_write_addtid_b32 v169 offset:4352\n"
    "v_exp_f32_e32 v175, v175\n"
    "v_mfma_f32_16x16x32_bf16 v[32:35], a[180:183], v[212:215], v[32:35]\n"
    "ds_read_b64_tr_b16 a[172:173], v245 offset:28672\n"
    "ds_read_b64_tr_b16 a[174:175], v245 offset:30720\n"
    "v_cvt_pk_bf16_f32 v170, v172, v173\n"
    "v_mfma_f32_16x16x32_bf16 v[32:35], a[176:179], v[208:211], v[32:35]\n"
    "v_exp_f32_e32 v164, v164\n"
    "v_mfma_f32_16x16x32_bf16 v[36:39], a[184:187], v[208:211], v[36:39]\n"
    "ds_read_b64_tr_b16 a[176:177], v246 offset:20480\n"
    "ds_read_b64_tr_b16 a[178:179], v246 offset:22528\n"
    "buffer_load_dwordx4 a[4:7], v253, s[8:11], s48 offen offset:0 sc1\n"
    "v_mfma_f32_16x16x32_bf16 v[36:39], a[188:191], v[212:215], v[36:39]\n"
    "v_exp_f32_e32 v165, v165\n"
    "v_mfma_f32_16x16x32_bf16 v[12:15], a[188:191], v[204:207], v[12:15]\n"
    "ds_read_b64_tr_b16 a[180:181], v246 offset:28672\n"
    "ds_read_b64_tr_b16 a[182:183], v246 offset:30720\n"
    "v_mfma_f32_16x16x32_bf16 v[12:15], a[184:187], v[200:203], v[12:15]\n"
    "s_waitcnt lgkmcnt(4)\n"
    "ds_write_addtid_b32 v160 offset:0\n"
    "v_cvt_pk_bf16_f32 v171, v174, v175\n"
    "v_mfma_f32_16x16x32_bf16 v[16:19], a[160:163], v[200:203], v[16:19]\n"
    "ds_read_b64_tr_b16 a[184:185], v247 offset:20480\n"
    "ds_read_b64_tr_b16 a[186:187], v247 offset:22528\n"
    "buffer_load_dwordx4 a[36:39], v253, s[8:11], s48 offen offset:1024 sc1\n"
    "v_mfma_f32_16x16x32_bf16 v[16:19], a[164:167], v[204:207], v[16:19]\n"
    "v_exp_f32_e32 v166, v166\n"
    "v_mfma_f32_16x16x32_bf16 v[40:43], a[164:167], v[212:215], v[40:43]\n"
    "ds_read_b64_tr_b16 a[188:189], v247 offset:28672\n"
    "ds_read_b64_tr_b16 a[190:191], v247 offset:30720\n"
    "v_mfma_f32_16x16x32_bf16 v[40:43], a[160:163], v[208:211], v[40:43]\n"
    "v_exp_f32_e32 v167, v167\n"
    "v_mfma_f32_16x16x32_bf16 v[44:47], a[168:171], v[208:211], v[44:47]\n"
    "ds_read_b64_tr_b16 a[160:161], v244 offset:32768\n"
    "buffer_load_dwordx4 a[8:11], v253, s[8:11], s48 offen offset:2048 sc1\n"
    "v_cvt_pk_bf16_f32 v162, v164, v165\n"
    "v_mfma_f32_16x16x32_bf16 v[44:47], a[172:175], v[212:215], v[44:47]\n"
    "ds_read_b64_tr_b16 a[162:163], v244 offset:34816\n"
    "v_cvt_pk_bf16_f32 v163, v166, v167\n"
    "v_mfma_f32_16x16x32_bf16 v[20:23], a[172:175], v[204:207], v[20:23]\n"
    "ds_read_b64_tr_b16 a[164:165], v244 offset:40960\n"
    "ds_write_addtid_b32 v161 offset:256\n"
    "v_mfma_f32_16x16x32_bf16 v[20:23], a[168:171], v[200:203], v[20:23]\n"
    "s_waitcnt lgkmcnt(4)\n"
    "ds_read_b64_tr_b16 a[166:167], v244 offset:43008\n"
    "ds_write_addtid_b32 v170 offset:4608\n"
    "v_mfma_f32_16x16x32_bf16 v[24:27], a[176:179], v[200:203], v[24:27]\n"
    "ds_read_b64_tr_b16 a[168:169], v245 offset:32768\n"
    "ds_write_addtid_b32 v171 offset:4864\n"
    "buffer_load_dwordx4 a[40:43], v253, s[8:11], s48 offen offset:3072 sc1\n"
    "v_mfma_f32_16x16x32_bf16 v[24:27], a[180:183], v[204:207], v[24:27]\n"
    "s_add_u32 s48, s56, 24576\n"
    "ds_read_b64_tr_b16 a[170:171], v245 offset:34816\n"
    "ds_write_addtid_b32 v162 offset:512\n"
    "v_mfma_f32_16x16x32_bf16 v[48:51], a[180:183], v[212:215], v[48:51]\n"
    "ds_read_b64_tr_b16 a[172:173], v245 offset:40960\n"
    "ds_write_addtid_b32 v163 offset:768\n"
    "v_mfma_f32_16x16x32_bf16 v[48:51], a[176:179], v[208:211], v[48:51]\n"
    "ds_read_b64_tr_b16 a[174:175], v245 offset:43008\n"
    "v_and_b32_e32 v242, 0x400, v240\n"
    "v_mfma_f32_16x16x32_bf16 v[52:55], a[184:187], v[208:211], v[52:55]\n"
    "ds_read_b64_tr_b16 a[176:177], v246 offset:32768\n"
    "buffer_load_dwordx4 a[12:15], v253, s[8:11], s48 offen offset:0 sc1\n"
    "v_and_b32_e32 v240, 0x3ff, v240\n"
    "v_mfma_f32_16x16x32_bf16 v[52:55], a[188:191], v[212:215], v[52:55]\n"
    "s_waitcnt lgkmcnt(2)\n"
    "s_barrier\n"
    "v_mfma_f32_16x16x32_bf16 v[28:31], a[188:191], v[204:207], v[28:31]\n"
    "ds_read_b64_tr_b16 a[178:179], v246 offset:34816\n"
    "v_lshl_or_b32 v240, v242, 2, v240\n"
    "v_mfma_f32_16x16x32_bf16 v[28:31], a[184:187], v[200:203], v[28:31]\n"
    "s_waitcnt lgkmcnt(0)\n"
    "ds_read_b64_tr_b16 a[180:181], v246 offset:40960\n"
    "ds_read2st64_b32 v[164:165], v250 offset0:0 offset1:1\n"
    "v_mfma_f32_16x16x32_bf16 v[0:3], a[160:163], v[160:163], v[0:3]\n"
    "ds_read_b64_tr_b16 a[182:183], v246 offset:43008\n"
    "ds_read2st64_b32 v[166:167], v250 offset0:2 offset1:3\n"
    "buffer_load_dwordx4 a[44:47], v253, s[8:11], s48 offen offset:1024 sc1\n"
    "v_mfma_f32_16x16x32_bf16 a[248:251], a[160:163], v[168:171], a[248:251]\n"
    "ds_read_b64_tr_b16 a[184:185], v247 offset:32768\n"
    "ds_read2st64_b32 v[172:173], v250 offset0:16 offset1:17\n"
    "v_mfma_f32_16x16x32_bf16 a[252:255], a[168:171], v[168:171], a[252:255]\n"
    "ds_read_b64_tr_b16 a[186:187], v247 offset:34816\n"
    "ds_read2st64_b32 v[174:175], v250 offset0:18 offset1:19\n"
    "v_mfma_f32_16x16x32_bf16 v[4:7], a[168:171], v[160:163], v[4:7]\n"
    "ds_read_b64_tr_b16 a[188:189], v247 offset:40960\n"
    "v_mfma_f32_16x16x32_bf16 v[8:11], a[176:179], v[160:163], v[8:11]\n"
    "ds_read_b64_tr_b16 a[190:191], v247 offset:43008\n"
    "buffer_load_dwordx4 a[16:19], v253, s[8:11], s48 offen offset:2048 sc1\n"
    "v_mfma_f32_16x16x32_bf16 v[32:35], a[176:179], v[168:171], v[32:35]\n"
    "s_waitcnt lgkmcnt(2)\n"
    "ds_read_b64_tr_b16 a[160:161], v244 offset:36864\n"
    "v_xor_b32_e32 v241, 64, v240\n"
    "v_mfma_f32_16x16x32_bf16 v[0:3], a[164:167], v[164:167], v[0:3]\n"
    "ds_read_b64_tr_b16 a[162:163], v244 offset:38912\n"
    "v_mfma_f32_16x16x32_bf16 a[248:251], a[164:167], v[172:175], a[248:251]\n"
    "s_waitcnt lgkmcnt(2)\n"
    "ds_read_b64_tr_b16 a[164:165], v244 offset:45056\n"
    "v_mfma_f32_16x16x32_bf16 a[252:255], a[172:175], v[172:175], a[252:255]\n"
    "ds_read_b64_tr_b16 a[166:167], v244 offset:47104\n"
    "buffer_load_dwordx4 a[48:51], v253, s[8:11], s48 offen offset:3072 sc1\n"
    "v_mfma_f32_16x16x32_bf16 v[4:7], a[172:175], v[164:167], v[4:7]\n"
    "s_add_u32 s48, s56, 28672\n"
    "ds_read_b64_tr_b16 a[168:169], v245 offset:36864\n"
    "v_mfma_f32_16x16x32_bf16 v[8:11], a[180:183], v[164:167], v[8:11]\n"
    "ds_read_b64_tr_b16 a[170:171], v245 offset:38912\n"
    "v_mfma_f32_16x16x32_bf16 v[32:35], a[180:183], v[172:175], v[32:35]\n"
    "ds_read_b64_tr_b16 a[172:173], v245 offset:45056\n"
    "v_accvgpr_read_b32 v216, a248\n"
    "v_mfma_f32_16x16x32_bf16 v[36:39], a[184:187], v[168:171], v[36:39]\n"
    "ds_read_b64_tr_b16 a[174:175], v245 offset:47104\n"
    "buffer_load_dwordx4 a[20:23], v253, s[8:11], s48 offen offset:0 sc1\n"
    "v_mfma_f32_16x16x32_bf16 v[36:39], a[188:191], v[172:175], v[36:39]\n"
    "s_lshl_b32 s45, s51, 16\n"
    "ds_read_b64_tr_b16 a[176:177], v246 offset:36864\n"
    "v_accvgpr_read_b32 v217, a249\n"
    "v_mfma_f32_16x16x32_bf16 v[12:15], a[188:191], v[164:167], v[12:15]\n"
    "ds_read_b64_tr_b16 a[178:179], v246 offset:38912\n"
    "buffer_load_dword v146, v145, s[8:11], s47 offen offset:128 sc1\n"
    "v_accvgpr_read_b32 v218, a250\n"
    "v_mfma_f32_16x16x32_bf16 v[12:15], a[184:187], v[160:163], v[12:15]\n"
    "s_waitcnt lgkmcnt(2)\n"
    "ds_read_b64_tr_b16 a[180:181], v246 offset:45056\n"
    "v_accvgpr_read_b32 v219, a251\n"
    "v_mfma_f32_16x16x32_bf16 v[16:19], a[160:163], v[160:163], v[16:19]\n"
    "ds_read_b64_tr_b16 a[182:183], v246 offset:47104\n"
    "buffer_load_dwordx4 a[52:55], v253, s[8:11], s48 offen offset:1024 sc1\n"
    "v_mfma_f32_16x16x32_bf16 v[16:19], a[164:167], v[164:167], v[16:19]\n"
    "s_lshl_b32 s46, s50, 11\n"
    "ds_read_b64_tr_b16 a[184:185], v247 offset:36864\n"
    "v_accvgpr_read_b32 v220, a252\n"
    "v_mfma_f32_16x16x32_bf16 v[40:43], a[164:167], v[172:175], v[40:43]\n"
    "s_add_u32 s46, s46, s45\n"
    "ds_read_b64_tr_b16 a[186:187], v247 offset:38912\n"
    "v_accvgpr_read_b32 v221, a253\n"
    "v_mfma_f32_16x16x32_bf16 v[40:43], a[160:163], v[168:171], v[40:43]\n"
    "s_add_u32 s46, s46, %3\n"
    "ds_read_b64_tr_b16 a[188:189], v247 offset:45056\n"
    "v_accvgpr_read_b32 v222, a254\n"
    "v_mfma_f32_16x16x32_bf16 v[44:47], a[168:171], v[168:171], v[44:47]\n"
    "ds_read_b64_tr_b16 a[190:191], v247 offset:47104\n"
    "buffer_load_dwordx4 a[24:27], v253, s[8:11], s48 offen offset:2048 sc1\n"
    "v_mfma_f32_16x16x32_bf16 v[44:47], a[172:175], v[172:175], v[44:47]\n"
    "s_bitcmp0_b32 %1, 0\n"
    "s_cbranch_scc1 .Lat_two\n"
    "s_add_u32 s46, s46, 81920\n"
    "ds_read_b128 a[160:163], v241 offset:11264\n"
    "v_accvgpr_read_b32 v223, a255\n"
    "v_mfma_f32_16x16x32_bf16 v[20:23], a[172:175], v[164:167], v[20:23]\n"
    "ds_read_b128 a[164:167], v241 offset:19456\n"
    "v_cvt_pk_bf16_f32 v216, v216, v217\n"
    "v_mfma_f32_16x16x32_bf16 v[20:23], a[168:171], v[160:163], v[20:23]\n"
    "s_waitcnt lgkmcnt(2)\n"
    "ds_read_b128 a[168:171], v241 offset:27648\n"
    "v_cvt_pk_bf16_f32 v217, v218, v219\n"
    "v_mfma_f32_16x16x32_bf16 v[24:27], a[176:179], v[160:163], v[24:27]\n"
    "ds_read_b128 a[172:175], v240 offset:8192\n"
    "buffer_load_dwordx4 a[56:59], v253, s[8:11], s48 offen offset:3072 sc1\n"
    "v_mfma_f32_16x16x32_bf16 v[24:27], a[180:183], v[164:167], v[24:27]\n"
    "v_cvt_pk_bf16_f32 v218, v220, v221\n"
    "v_cvt_pk_bf16_f32 v219, v222, v223\n"
    "v_mfma_f32_16x16x32_bf16 v[48:51], a[180:183], v[172:175], v[48:51]\n"
    "buffer_store_dwordx4 v[216:219], v253, s[8:11], s83 offen offset:0 sc1\n"
    "v_cvt_pk_bf16_f32 v32, v32, v33\n"
    "v_cvt_pk_bf16_f32 v33, v34, v35\n"
    "v_mfma_f32_16x16x32_bf16 v[48:51], a[176:179], v[168:171], v[48:51]\n"
    "ds_read_b128 a[176:179], v240 offset:16384\n"
    "v_cvt_pk_bf16_f32 v34, v36, v37\n"
    "v_mfma_f32_16x16x32_bf16 v[52:55], a[184:187], v[168:171], v[52:55]\n"
    "ds_read_b128 a[180:183], v240 offset:24576\n"
    "v_cvt_pk_bf16_f32 v35, v38, v39\n"
    "v_mfma_f32_16x16x32_bf16 v[52:55], a[188:191], v[172:175], v[52:55]\n"
    "buffer_store_dwordx4 v[32:35], v253, s[8:11], s83 offen offset:1024 sc1\n"
    "v_cvt_pk_bf16_f32 v40, v40, v41\n"
    "v_cvt_pk_bf16_f32 v41, v42, v43\n"
    "v_mfma_f32_16x16x32_bf16 v[28:31], a[188:191], v[164:167], v[28:31]\n"
    "v_cvt_pk_bf16_f32 v42, v44, v45\n"
    "v_cvt_pk_bf16_f32 v43, v46, v47\n"
    "v_mfma_f32_16x16x32_bf16 v[28:31], a[184:187], v[160:163], v[28:31]\n"
    "ds_read_b128 a[184:187], v241 offset:8192\n"
    "buffer_store_dwordx4 v[40:43], v253, s[8:11], s83 offen offset:2048 sc1\n"
    "v_cvt_pk_bf16_f32 v48, v48, v49\n"
    "v_add_u32_e32 v158, s46, v253\n"
    "v_mfma_f32_16x16x32_bf16 v[160:163], v[154:157], a[224:227], 0\n"
    "ds_read_b128 a[188:191], v241 offset:16384\n"
    "v_cvt_pk_bf16_f32 v49, v50, v51\n"
    "v_mfma_f32_16x16x32_bf16 v[164:167], v[152:155], a[224:227], 0\n"
    "v_cvt_pk_bf16_f32 v50, v52, v53\n"
    "v_cvt_pk_bf16_f32 v51, v54, v55\n"
    "v_mfma_f32_16x16x32_bf16 v[168:171], v[154:157], a[228:231], 0\n"
    "s_add_u32 s45, %3, 155840\n"
    "buffer_store_dwordx4 v[48:51], v253, s[8:11], s83 offen offset:3072 sc1\n"
    "v_and_b32_e32 v150, 15, v147\n"
    "v_lshlrev_b32_e32 v151, 9, v150\n"
    "v_mfma_f32_16x16x32_bf16 v[172:175], v[152:155], a[228:231], 0\n"
    "buffer_store_dword v55, v251, s[8:11], s60 offen offset:0 sc1\n"
    "v_lshl_add_u32 v150, v150, 2, s45\n"
    "v_and_or_b32 v151, v147, 48, v151\n"
    "v_mfma_f32_16x16x32_bf16 v[176:179], v[154:157], a[232:235], 0\n"
    "s_lshl_b32 s46, %2, 7\n"
    "s_add_u32 s58, s35, s46\n"
    "s_mul_i32 s45, %1, 74496\n"
    "v_mfma_f32_16x16x32_bf16 v[180:183], v[152:155], a[232:235], 0\n"
    "s_add_u32 s45, s45, 16265216\n"
    "ds_write_b128 v159, v[216:219] offset:0\n"
    "v_mfma_f32_16x16x32_bf16 v[184:187], v[154:157], a[236:239], 0\n"
    "s_lshl_b32 s54, %2, 11\n"
    "v_and_b32_e32 v147, 0xf0, v253\n"
    "v_add_u32_e32 v252, %3, v253\n"
    "v_mfma_f32_16x16x32_bf16 v[188:191], v[152:155], a[236:239], 0\n"
    "s_add_u32 s54, s54, s45\n"
    "ds_write_b128 v159, v[32:35] offset:1024\n"
    "v_mfma_f32_16x16x32_bf16 v[192:195], v[154:157], a[240:243], 0\n"
    "s_add_u32 s54, s54, 16384\n"
    "ds_write_b128 v159, v[40:43] offset:2048\n"
    "v_mfma_f32_16x16x32_bf16 v[196:199], v[152:155], a[240:243], 0\n"
    "s_lshl1_add_u32 s59, s50, s51\n"
    "s_lshl_b32 s59, s59, 12\n"
    "v_mfma_f32_16x16x32_bf16 v[200:203], v[154:157], a[244:247], 0\n"
    "s_add_u32 s46, s59, s45\n"
    "ds_write_b128 v159, v[48:51] offset:3072\n"
    "v_mfma_f32_16x16x32_bf16 v[204:207], v[152:155], a[244:247], 0\n"
    "s_waitcnt lgkmcnt(10)\n"
    "s_bitcmp1_b32 %1, 0\n"
    "s_cselect_b32 s54, s54, 2.0\n"
    "s_cselect_b32 s55, s58, 2.0\n"
    "v_mfma_f32_16x16x32_bf16 v[160:163], a[192:195], a[160:163], v[160:163]\n"
    "ds_write_b32 v254, v55 offset:0\n"
    "v_mfma_f32_16x16x32_bf16 v[164:167], a[196:199], a[160:163], v[164:167]\n"
    "s_add_u32 s61, s54, 0\n"
    "ds_write_b32 v255, v31 offset:0\n"
    "v_mfma_f32_16x16x32_bf16 v[168:171], a[200:203], a[160:163], v[168:171]\n"
    "s_waitcnt vmcnt(8)\n"
    "v_cmp_ne_u32_e32 vcc, s39, v146\n"
    "v_mfma_f32_16x16x32_bf16 v[172:175], a[204:207], a[160:163], v[172:175]\n"
    "ds_read_b128 a[160:163], v241 offset:24576\n"
    "v_mfma_f32_16x16x32_bf16 v[176:179], a[192:195], a[164:167], v[176:179]\n"
    "s_cbranch_vccnz .Lat_xpoll\n"
    ".Lat_xpoll_ret:\n"
    "v_mfma_f32_16x16x32_bf16 v[180:183], a[196:199], a[164:167], v[180:183]\n"
    "v_mfma_f32_16x16x32_bf16 v[184:187], a[200:203], a[164:167], v[184:187]\n"
    "buffer_load_dwordx4 a[224:227], v253, s[8:11], s61 offen offset:0 sc1\n"
    "v_mfma_f32_16x16x32_bf16 v[188:191], a[204:207], a[164:167], v[188:191]\n"
    "s_waitcnt lgkmcnt(11)\n"
    "ds_read_b128 a[164:167], v240 offset:9216\n"
    "v_mfma_f32_16x16x32_bf16 v[192:195], a[192:195], a[168:171], v[192:195]\n"
    "v_mfma_f32_16x16x32_bf16 v[196:199], a[196:199], a[168:171], v[196:199]\n"
    "v_mfma_f32_16x16x32_bf16 v[200:203], a[200:203], a[168:171], v[200:203]\n"
    "v_mfma_f32_16x16x32_bf16 v[204:207], a[204:207], a[168:171], v[204:207]\n"
    "ds_read_b128 a[168:171], v240 offset:17408\n"
    "buffer_load_dwordx4 a[228:231], v253, s[8:11], s61 offen offset:1024 sc1\n"
    "v_mfma_f32_16x16x32_bf16 v[160:163], a[208:211], a[172:175], v[160:163]\n"
    "s_waitcnt lgkmcnt(3)\n"
    "s_barrier\n"
    "v_mfma_f32_16x16x32_bf16 v[164:167], a[212:215], a[172:175], v[164:167]\n"
    "s_add_u32 s61, s54, 24832\n"
    "v_mfma_f32_16x16x32_bf16 v[168:171], a[216:219], a[172:175], v[168:171]\n"
    "v_mfma_f32_16x16x32_bf16 v[172:175], a[220:223], a[172:175], v[172:175]\n"
    "ds_read_b128 a[172:175], v240 offset:25600\n"
    "v_mfma_f32_16x16x32_bf16 v[176:179], a[208:211], a[176:179], v[176:179]\n"
    "buffer_load_dwordx4 a[232:235], v253, s[8:11], s61 offen offset:0 sc1\n"
    "v_mfma_f32_16x16x32_bf16 v[180:183], a[212:215], a[176:179], v[180:183]\n"
    "ds_read_b32 v232, v150 offset:0\n"
    "v_mfma_f32_16x16x32_bf16 v[184:187], a[216:219], a[176:179], v[184:187]\n"
    "v_mfma_f32_16x16x32_bf16 v[188:191], a[220:223], a[176:179], v[188:191]\n"
    "ds_read_b128 a[176:179], v241 offset:9216\n"
    "v_mfma_f32_16x16x32_bf16 v[192:195], a[208:211], a[180:183], v[192:195]\n"
    "v_mfma_f32_16x16x32_bf16 v[196:199], a[212:215], a[180:183], v[196:199]\n"
    "buffer_load_dwordx4 a[236:239], v253, s[8:11], s61 offen offset:1024 sc1\n"
    "v_mfma_f32_16x16x32_bf16 v[200:203], a[216:219], a[180:183], v[200:203]\n"
    "s_add_u32 s61, s54, 49664\n"
    "ds_read_b32 v233, v150 offset:256\n"
    "v_mfma_f32_16x16x32_bf16 v[204:207], a[220:223], a[180:183], v[204:207]\n"
    "ds_read_b128 a[180:183], v241 offset:17408\n"
    "v_mfma_f32_16x16x32_bf16 v[160:163], a[92:95], a[184:187], v[160:163]\n"
    "v_mfma_f32_16x16x32_bf16 v[164:167], a[124:127], a[184:187], v[164:167]\n"
    "v_mfma_f32_16x16x32_bf16 v[168:171], a[156:159], a[184:187], v[168:171]\n"
    "buffer_load_dwordx4 a[240:243], v253, s[8:11], s61 offen offset:0 sc1\n"
    "v_mfma_f32_16x16x32_bf16 v[172:175], a[64:67], a[184:187], v[172:175]\n"
    "ds_read_b128 a[184:187], v241 offset:25600\n"
    "v_mfma_f32_16x16x32_bf16 v[176:179], a[92:95], a[188:191], v[176:179]\n"
    "ds_read_b32 v234, v150 offset:512\n"
    "v_mfma_f32_16x16x32_bf16 v[180:183], a[124:127], a[188:191], v[180:183]\n"
    "v_mfma_f32_16x16x32_bf16 v[184:187], a[156:159], a[188:191], v[184:187]\n"
    "v_mfma_f32_16x16x32_bf16 v[188:191], a[64:67], a[188:191], v[188:191]\n"
    "s_waitcnt lgkmcnt(8)\n"
    "ds_read_b128 a[188:191], v240 offset:10240\n"
    "v_mfma_f32_16x16x32_bf16 v[192:195], a[92:95], a[160:163], v[192:195]\n"
    "buffer_load_dwordx4 a[244:247], v253, s[8:11], s61 offen offset:1024 sc1\n"
    "v_mfma_f32_16x16x32_bf16 v[196:199], a[124:127], a[160:163], v[196:199]\n"
    "s_add_u32 s61, s46, 0\n"
    "ds_read_b128 a[248:251], v158 offset:0\n"
    "v_mfma_f32_16x16x32_bf16 v[200:203], a[156:159], a[160:163], v[200:203]\n"
    "v_mfma_f32_16x16x32_bf16 v[204:207], a[64:67], a[160:163], v[204:207]\n"
    "ds_read_b128 a[160:163], v240 offset:18432\n"
    "v_mfma_f32_16x16x32_bf16 v[160:163], a[96:99], a[164:167], v[160:163]\n"
    "s_waitcnt vmcnt(6)\n"
    "buffer_store_dword v148, v149, s[8:11], s87 offen offset:0 sc1\n"
    "v_mfma_f32_16x16x32_bf16 v[164:167], a[128:131], a[164:167], v[164:167]\n"
    "buffer_load_dwordx4 a[192:195], v253, s[8:11], s61 offen offset:0 sc1\n"
    "v_mfma_f32_16x16x32_bf16 v[168:171], a[68:71], a[164:167], v[168:171]\n"
    "ds_read_b128 a[252:255], v158 offset:1024\n"
    "v_mfma_f32_16x16x32_bf16 v[172:175], a[100:103], a[164:167], v[172:175]\n"
    "s_waitcnt lgkmcnt(10)\n"
    "ds_read_b128 a[164:167], v240 offset:26624\n"
    "v_mfma_f32_16x16x32_bf16 v[176:179], a[96:99], a[168:171], v[176:179]\n"
    "v_mfma_f32_16x16x32_bf16 v[180:183], a[128:131], a[168:171], v[180:183]\n"
    "v_mfma_f32_16x16x32_bf16 v[184:187], a[68:71], a[168:171], v[184:187]\n"
    "buffer_load_dwordx4 a[196:199], v253, s[8:11], s61 offen offset:1024 sc1\n"
    "v_mfma_f32_16x16x32_bf16 v[188:191], a[100:103], a[168:171], v[188:191]\n"
    "s_lshl_b32 s52, %2, 11\n"
    "ds_read_b128 a[168:171], v241 offset:10240\n"
    "v_mfma_f32_16x16x32_bf16 v[192:195], a[96:99], a[172:175], v[192:195]\n"
    "v_mfma_f32_16x16x32_bf16 v[196:199], a[128:131], a[172:175], v[196:199]\n"
    "v_mfma_f32_16x16x32_bf16 v[200:203], a[68:71], a[172:175], v[200:203]\n"
    "v_mfma_f32_16x16x32_bf16 v[204:207], a[100:103], a[172:175], v[204:207]\n"
    "s_waitcnt lgkmcnt(8)\n"
    "ds_read_b128 a[172:175], v241 offset:18432\n"
    "v_mfma_f32_16x16x32_bf16 v[160:163], a[132:135], a[176:179], v[160:163]\n"
    "buffer_load_dwordx4 a[200:203], v253, s[8:11], s61 offen offset:2048 sc1\n"
    "v_mfma_f32_16x16x32_bf16 v[164:167], a[72:75], a[176:179], v[164:167]\n"
    "v_add_u32_e32 v251, s52, v252\n"
    "v_mfma_f32_16x16x32_bf16 v[168:171], a[104:107], a[176:179], v[168:171]\n"
    "v_mfma_f32_16x16x32_bf16 v[172:175], a[136:139], a[176:179], v[172:175]\n"
    "ds_read_b128 a[176:179], v241 offset:26624\n"
    "v_mfma_f32_16x16x32_bf16 v[176:179], a[132:135], a[180:183], v[176:179]\n"
    "v_mfma_f32_16x16x32_bf16 v[180:183], a[72:75], a[180:183], v[180:183]\n"
    "buffer_load_dwordx4 a[204:207], v253, s[8:11], s61 offen offset:3072 sc1\n"
    "v_mfma_f32_16x16x32_bf16 v[184:187], a[104:107], a[180:183], v[184:187]\n"
    "s_add_u32 s61, s46, 24832\n"
    "v_add_u32_e32 v158, s59, v252\n"
    "v_mfma_f32_16x16x32_bf16 v[188:191], a[136:139], a[180:183], v[188:191]\n"
    "s_waitcnt lgkmcnt(7)\n"
    "ds_read_b128 a[180:183], v240 offset:11264\n"
    "v_mfma_f32_16x16x32_bf16 v[192:195], a[132:135], a[184:187], v[192:195]\n"
    "v_mfma_f32_16x16x32_bf16 v[196:199], a[72:75], a[184:187], v[196:199]\n"
    "v_mfma_f32_16x16x32_bf16 v[200:203], a[104:107], a[184:187], v[200:203]\n"
    "buffer_load_dwordx4 a[208:211], v253, s[8:11], s61 offen offset:0 sc1\n"
    "v_mfma_f32_16x16x32_bf16 v[204:207], a[136:139], a[184:187], v[204:207]\n"
    "ds_read_b128 a[184:187], v240 offset:19456\n"
    "v_mfma_f32_16x16x32_bf16 v[160:163], a[76:79], a[188:191], v[160:163]\n"
    "v_and_b32_e32 v145, 12, v145\n"
    "v_mfma_f32_16x16x32_bf16 v[164:167], a[108:111], a[188:191], v[164:167]\n"
    "v_mfma_f32_16x16x32_bf16 v[168:171], a[140:143], a[188:191], v[168:171]\n"
    "v_mfma_f32_16x16x32_bf16 v[172:175], a[80:83], a[188:191], v[172:175]\n"
    "s_waitcnt lgkmcnt(5)\n"
    "ds_read_b128 a[188:191], v240 offset:27648\n"
    "v_mfma_f32_16x16x32_bf16 v[176:179], a[76:79], a[160:163], v[176:179]\n"
    "buffer_load_dwordx4 a[212:215], v253, s[8:11], s61 offen offset:1024 sc1\n"
    "v_mfma_f32_16x16x32_bf16 v[180:183], a[108:111], a[160:163], v[180:183]\n"
    "s_add_u32 s52, s32, 3\n"
    "v_mfma_f32_16x16x32_bf16 v[184:187], a[140:143], a[160:163], v[184:187]\n"
    "v_mfma_f32_16x16x32_bf16 v[188:191], a[80:83], a[160:163], v[188:191]\n"
    "v_mfma_f32_16x16x32_bf16 v[192:195], a[76:79], a[164:167], v[192:195]\n"
    "v_mfma_f32_16x16x32_bf16 v[196:199], a[108:111], a[164:167], v[196:199]\n"
    "buffer_load_dwordx4 a[216:219], v253, s[8:11], s61 offen offset:2048 sc1\n"
    "v_mfma_f32_16x16x32_bf16 v[200:203], a[140:143], a[164:167], v[200:203]\n"
    "s_and_b32 s52, s52, 3\n"
    "v_mfma_f32_16x16x32_bf16 v[204:207], a[80:83], a[164:167], v[204:207]\n"
    "s_waitcnt lgkmcnt(4)\n"
    "v_mfma_f32_16x16x32_bf16 v[160:163], a[112:115], a[168:171], v[160:163]\n"
    "v_mfma_f32_16x16x32_bf16 v[164:167], a[144:147], a[168:171], v[164:167]\n"
    "v_mfma_f32_16x16x32_bf16 v[168:171], a[84:87], a[168:171], v[168:171]\n"
    "buffer_load_dwordx4 a[220:223], v253, s[8:11], s61 offen offset:3072 sc1\n"
    "v_mfma_f32_16x16x32_bf16 v[172:175], a[116:119], a[168:171], v[172:175]\n"
    "s_add_u32 s61, s45, 24576\n"
    "v_mfma_f32_16x16x32_bf16 v[176:179], a[112:115], a[172:175], v[176:179]\n"
    "s_lshl_b32 s52, s52, 8\n"
    "v_mfma_f32_16x16x32_bf16 v[180:183], a[144:147], a[172:175], v[180:183]\n"
    "v_mfma_f32_16x16x32_bf16 v[184:187], a[84:87], a[172:175], v[184:187]\n"
    "v_mfma_f32_16x16x32_bf16 v[188:191], a[116:119], a[172:175], v[188:191]\n"
    "s_waitcnt lgkmcnt(2)\n"
    "buffer_load_dwordx3 v[236:238], v147, s[8:11], s61 offen offset:0 sc1\n"
    "v_mfma_f32_16x16x32_bf16 v[192:195], a[112:115], a[176:179], v[192:195]\n"
    "s_add_u32 s61, s45, 49408\n"
    "v_mfma_f32_16x16x32_bf16 v[196:199], a[144:147], a[176:179], v[196:199]\n"
    "s_add_u32 s52, s52, s47\n"
    "v_mfma_f32_16x16x32_bf16 v[200:203], a[84:87], a[176:179], v[200:203]\n"
    "v_mfma_f32_16x16x32_bf16 v[204:207], a[116:119], a[176:179], v[204:207]\n"
    "v_mfma_f32_16x16x32_bf16 v[160:163], a[148:151], a[180:183], v[160:163]\n"
    "buffer_load_dwordx3 v[248:250], v147, s[8:11], s61 offen offset:0 sc1\n"
    "v_mfma_f32_16x16x32_bf16 v[164:167], a[88:91], a[180:183], v[164:167]\n"
    "v_mfma_f32_16x16x32_bf16 v[168:171], a[120:123], a[180:183], v[168:171]\n"
    "v_mfma_f32_16x16x32_bf16 v[172:175], a[152:155], a[180:183], v[172:175]\n"
    "s_waitcnt lgkmcnt(0)\n"
    "v_mfma_f32_16x16x32_bf16 v[176:179], a[148:151], a[184:187], v[176:179]\n"
    "v_mfma_f32_16x16x32_bf16 v[180:183], a[88:91], a[184:187], v[180:183]\n"
    "v_mfma_f32_16x16x32_bf16 v[184:187], a[120:123], a[184:187], v[184:187]\n"
    "v_mfma_f32_16x16x32_bf16 v[188:191], a[152:155], a[184:187], v[188:191]\n"
    "v_mfma_f32_16x16x32_bf16 v[192:195], a[148:151], a[188:191], v[192:195]\n"
    "v_mfma_f32_16x16x32_bf16 v[196:199], a[88:91], a[188:191], v[196:199]\n"
    "v_mfma_f32_16x16x32_bf16 v[200:203], a[120:123], a[188:191], v[200:203]\n"
    "v_mfma_f32_16x16x32_bf16 v[204:207], a[152:155], a[188:191], v[204:207]\n"
    "v_mfma_f32_16x16x32_bf16 v[128:131], v[154:157], a[248:251], 0\n"
    "v_mfma_f32_16x16x32_bf16 v[132:135], v[152:155], a[248:251], 0\n"
    "v_mfma_f32_16x16x32_bf16 v[136:139], v[154:157], a[252:255], 0\n"
    "v_mfma_f32_16x16x32_bf16 v[140:143], v[152:155], a[252:255], 0\n"
    "s_waitcnt vmcnt(15)\n"
    "v_mfma_f32_16x16x32_bf16 v[128:131], v[154:157], a[224:227], v[128:131]\n"
    "v_mfma_f32_16x16x32_bf16 v[132:135], v[152:155], a[224:227], v[132:135]\n"
    "v_mfma_f32_16x16x32_bf16 v[136:139], v[154:157], a[228:231], v[136:139]\n"
    "buffer_load_dword v144, v145, s[8:11], s52 offen offset:0 sc1\n"
    "v_mfma_f32_16x16x32_bf16 v[140:143], v[152:155], a[228:231], v[140:143]\n"
    "s_waitcnt vmcnt(14)\n"
    "v_mfma_f32_16x16x32_bf16 v[128:131], v[154:157], a[232:235], v[128:131]\n"
    "v_mfma_f32_16x16x32_bf16 v[132:135], v[152:155], a[232:235], v[132:135]\n"
    "v_mfma_f32_16x16x32_bf16 v[136:139], v[154:157], a[236:239], v[136:139]\n"
    "v_mfma_f32_16x16x32_bf16 v[140:143], v[152:155], a[236:239], v[140:143]\n"
    "s_waitcnt vmcnt(12)\n"
    "v_mfma_f32_16x16x32_bf16 v[128:131], v[154:157], a[240:243], v[128:131]\n"
    "v_mfma_f32_16x16x32_bf16 v[132:135], v[152:155], a[240:243], v[132:135]\n"
    "v_mfma_f32_16x16x32_bf16 v[136:139], v[154:157], a[244:247], v[136:139]\n"
    "v_mfma_f32_16x16x32_bf16 v[140:143], v[152:155], a[244:247], v[140:143]\n"
    "s_waitcnt vmcnt(9)\n"
    "v_mfma_f32_16x16x32_bf16 v[0:3], v[154:157], a[192:195], v[0:3]\n"
    "v_cvt_pk_bf16_f32 v128, v128, v129\n"
    "v_mfma_f32_16x16x32_bf16 v[4:7], v[152:155], a[192:195], v[4:7]\n"
    "v_cvt_pk_bf16_f32 v129, v130, v131\n"
    "v_mfma_f32_16x16x32_bf16 v[8:11], v[154:157], a[196:199], v[8:11]\n"
    "v_cvt_pk_bf16_f32 v130, v132, v133\n"
    "v_mfma_f32_16x16x32_bf16 v[12:15], v[152:155], a[196:199], v[12:15]\n"
    "s_waitcnt vmcnt(7)\n"
    "v_cvt_pk_bf16_f32 v131, v134, v135\n"
    "v_mfma_f32_16x16x32_bf16 v[16:19], v[154:157], a[200:203], v[16:19]\n"
    "ds_write_b128 v251, v[128:131] offset:49152\n"
    "v_mfma_f32_16x16x32_bf16 v[20:23], v[152:155], a[200:203], v[20:23]\n"
    "v_cvt_pk_bf16_f32 v136, v136, v137\n"
    "v_mfma_f32_16x16x32_bf16 v[24:27], v[154:157], a[204:207], v[24:27]\n"
    "v_cvt_pk_bf16_f32 v137, v138, v139\n"
    "v_mfma_f32_16x16x32_bf16 v[28:31], v[152:155], a[204:207], v[28:31]\n"
    "s_waitcnt vmcnt(5)\n"
    "v_cvt_pk_bf16_f32 v138, v140, v141\n"
    "v_cvt_pk_bf16_f32 v139, v142, v143\n"
    "v_mfma_f32_16x16x32_bf16 v[0:3], v[154:157], a[208:211], v[0:3]\n"
    "ds_write_b128 v251, v[136:139] offset:50176\n"
    "v_mfma_f32_16x16x32_bf16 v[4:7], v[152:155], a[208:211], v[4:7]\n"
    "v_mfma_f32_16x16x32_bf16 v[8:11], v[154:157], a[212:215], v[8:11]\n"
    "s_waitcnt lgkmcnt(0)\n"
    "s_barrier\n"
    "ds_read_b128 a[128:131], v252 offset:49152\n"
    "ds_read_b128 a[132:135], v252 offset:50176\n"
    "ds_read_b128 a[136:139], v252 offset:51200\n"
    "v_mfma_f32_16x16x32_bf16 v[12:15], v[152:155], a[212:215], v[12:15]\n"
    "s_waitcnt vmcnt(3)\n"
    "v_mfma_f32_16x16x32_bf16 v[16:19], v[154:157], a[216:219], v[16:19]\n"
    "ds_read_b128 a[140:143], v252 offset:52224\n"
    "v_mfma_f32_16x16x32_bf16 v[20:23], v[152:155], a[216:219], v[20:23]\n"
    "v_mfma_f32_16x16x32_bf16 v[24:27], v[154:157], a[220:223], v[24:27]\n"
    "ds_read_b128 a[144:147], v252 offset:53248\n"
    "v_mfma_f32_16x16x32_bf16 v[28:31], v[152:155], a[220:223], v[28:31]\n"
    "s_waitcnt lgkmcnt(3)\n"
    "v_mfma_f32_16x16x32_bf16 v[216:219], v[56:59], a[128:131], 0\n"
    "ds_read_b128 a[148:151], v252 offset:54272\n"
    "v_mfma_f32_16x16x32_bf16 v[220:223], v[60:63], a[128:131], 0\n"
    "ds_read_b128 a[152:155], v252 offset:55296\n"
    "v_mfma_f32_16x16x32_bf16 v[224:227], v[64:67], a[128:131], 0\n"
    "s_waitcnt vmcnt(0)\n"
    "v_cmp_ne_u32_e32 vcc, s39, v144\n"
    "s_nop 3\n"
    "s_cbranch_vccnz .Lat_ypoll\n"
    ".Lat_ypoll_ret:\n"
    "s_add_u32 s61, s46, 49664\n"
    "buffer_load_dwordx4 a[160:163], v253, s[8:11], s61 offen offset:0 sc1\n"
    "buffer_load_dwordx4 a[164:167], v253, s[8:11], s61 offen offset:1024 sc1\n"
    "buffer_load_dwordx4 a[168:171], v253, s[8:11], s61 offen offset:2048 sc1\n"
    "buffer_load_dwordx4 a[172:175], v253, s[8:11], s61 offen offset:3072 sc1\n"
    "s_add_u32 s61, s45, 74240\n"
    "buffer_load_dwordx3 v[244:246], v147, s[8:11], s61 offen offset:0 sc1\n"
    "v_mfma_f32_16x16x32_bf16 v[228:231], v[68:71], a[128:131], 0\n"
    "ds_read_b128 a[156:159], v252 offset:56320\n"
    "v_mfma_f32_16x16x32_bf16 v[216:219], v[72:75], a[132:135], v[216:219]\n"
    "v_mfma_f32_16x16x32_bf16 v[220:223], v[76:79], a[132:135], v[220:223]\n"
    "s_waitcnt vmcnt(6)\n"
    "v_mfma_f32_16x16x32_bf16 v[224:227], v[80:83], a[132:135], v[224:227]\n"
    "v_mfma_f32_16x16x32_bf16 v[228:231], v[84:87], a[132:135], v[228:231]\n"
    "s_waitcnt lgkmcnt(4)\n"
    "v_mfma_f32_16x16x32_bf16 v[216:219], v[88:91], a[136:139], v[216:219]\n"
    "v_mfma_f32_16x16x32_bf16 v[220:223], v[92:95], a[136:139], v[220:223]\n"
    "v_add_f32_e32 v232, v232, v236\n"
    "v_mfma_f32_16x16x32_bf16 v[224:227], v[96:99], a[136:139], v[224:227]\n"
    "v_mfma_f32_16x16x32_bf16 v[228:231], v[100:103], a[136:139], v[228:231]\n"
    "v_mfma_f32_16x16x32_bf16 v[216:219], v[104:107], a[140:143], v[216:219]\n"
    "v_mfma_f32_16x16x32_bf16 v[220:223], v[108:111], a[140:143], v[220:223]\n"
    "v_add_f32_e32 v233, v233, v237\n"
    "v_mfma_f32_16x16x32_bf16 v[224:227], v[112:115], a[140:143], v[224:227]\n"
    "v_mfma_f32_16x16x32_bf16 v[228:231], v[116:119], a[140:143], v[228:231]\n"
    "s_waitcnt lgkmcnt(2)\n"
    "v_mfma_f32_16x16x32_bf16 v[216:219], a[28:31], a[144:147], v[216:219]\n"
    "v_mfma_f32_16x16x32_bf16 v[220:223], a[60:63], a[144:147], v[220:223]\n"
    "v_add_f32_e32 v234, v234, v238\n"
    "v_mfma_f32_16x16x32_bf16 v[224:227], a[0:3], a[144:147], v[224:227]\n"
    "v_mfma_f32_16x16x32_bf16 v[228:231], a[32:35], a[144:147], v[228:231]\n"
    "v_mfma_f32_16x16x32_bf16 v[216:219], a[4:7], a[148:151], v[216:219]\n"
    "v_mfma_f32_16x16x32_bf16 v[220:223], a[36:39], a[148:151], v[220:223]\n"
    "v_add_f32_e32 v232, v232, v248\n"
    "v_mfma_f32_16x16x32_bf16 v[224:227], a[8:11], a[148:151], v[224:227]\n"
    "v_mfma_f32_16x16x32_bf16 v[228:231], a[40:43], a[148:151], v[228:231]\n"
    "s_waitcnt lgkmcnt(0)\n"
    "v_mfma_f32_16x16x32_bf16 v[216:219], a[12:15], a[152:155], v[216:219]\n"
    "v_mfma_f32_16x16x32_bf16 v[220:223], a[44:47], a[152:155], v[220:223]\n"
    "v_add_f32_e32 v233, v233, v249\n"
    "v_mfma_f32_16x16x32_bf16 v[224:227], a[16:19], a[152:155], v[224:227]\n"
    "v_mfma_f32_16x16x32_bf16 v[228:231], a[48:51], a[152:155], v[228:231]\n"
    "v_mfma_f32_16x16x32_bf16 v[216:219], a[20:23], a[156:159], v[216:219]\n"
    "v_mfma_f32_16x16x32_bf16 v[220:223], a[52:55], a[156:159], v[220:223]\n"
    "v_add_f32_e32 v234, v234, v250\n"
    "v_mfma_f32_16x16x32_bf16 v[224:227], a[24:27], a[156:159], v[224:227]\n"
    "v_mfma_f32_16x16x32_bf16 v[228:231], a[56:59], a[156:159], v[228:231]\n"
    "s_waitcnt vmcnt(4)\n"
    "v_mfma_f32_16x16x32_bf16 v[0:3], v[154:157], a[160:163], v[0:3]\n"
    "v_mfma_f32_16x16x32_bf16 v[4:7], v[152:155], a[160:163], v[4:7]\n"
    "s_waitcnt vmcnt(3)\n"
    "v_mfma_f32_16x16x32_bf16 v[8:11], v[154:157], a[164:167], v[8:11]\n"
    "v_mfma_f32_16x16x32_bf16 v[12:15], v[152:155], a[164:167], v[12:15]\n"
    "s_waitcnt vmcnt(2)\n"
    "v_cvt_pk_bf16_f32 v0, v0, v1\n"
    "v_cvt_pk_bf16_f32 v1, v2, v3\n"
    "v_mfma_f32_16x16x32_bf16 v[16:19], v[154:157], a[168:171], v[16:19]\n"
    "v_cvt_pk_bf16_f32 v2, v4, v5\n"
    "v_cvt_pk_bf16_f32 v3, v6, v7\n"
    "v_mfma_f32_16x16x32_bf16 v[20:23], v[152:155], a[168:171], v[20:23]\n"
    "s_waitcnt vmcnt(1)\n"
    "ds_write_b128 v158, v[0:3] offset:32768\n"
    "v_mfma_f32_16x16x32_bf16 v[24:27], v[154:157], a[172:175], v[24:27]\n"
    "v_cvt_pk_bf16_f32 v8, v8, v9\n"
    "v_cvt_pk_bf16_f32 v9, v10, v11\n"
    "v_cvt_pk_bf16_f32 v10, v12, v13\n"
    "v_cvt_pk_bf16_f32 v11, v14, v15\n"
    "v_mfma_f32_16x16x32_bf16 v[28:31], v[152:155], a[172:175], v[28:31]\n"
    "ds_write_b128 v158, v[8:11] offset:33792\n"
    "v_cvt_pk_bf16_f32 v16, v16, v17\n"
    "v_cvt_pk_bf16_f32 v17, v18, v19\n"
    "v_cvt_pk_bf16_f32 v18, v20, v21\n"
    "v_cvt_pk_bf16_f32 v19, v22, v23\n"
    "ds_write_b128 v158, v[16:19] offset:34816\n"
    "v_cvt_pk_bf16_f32 v24, v24, v25\n"
    "v_cvt_pk_bf16_f32 v25, v26, v27\n"
    "v_cvt_pk_bf16_f32 v26, v28, v29\n"
    "v_cvt_pk_bf16_f32 v27, v30, v31\n"
    "ds_write_b128 v158, v[24:27] offset:35840\n"
    "s_waitcnt lgkmcnt(0)\n"
    "s_barrier\n"
    "ds_read_b128 a[96:99], v252 offset:40960\n"
    "ds_read_b128 a[100:103], v252 offset:41984\n"
    "ds_read_b128 a[104:107], v252 offset:43008\n"
    "ds_read_b128 a[108:111], v252 offset:44032\n"
    "ds_read_b128 a[112:115], v252 offset:45056\n"
    "ds_read_b128 a[116:119], v252 offset:46080\n"
    "ds_read_b128 a[120:123], v252 offset:47104\n"
    "ds_read_b128 a[124:127], v252 offset:48128\n"
    "s_waitcnt lgkmcnt(6)\n"
    "v_mfma_f32_16x16x32_bf16 v[120:123], v[56:59], a[96:99], 0\n"
    "s_waitcnt vmcnt(0)\n"
    "v_add_f32_e32 v234, v234, v246\n"
    "v_mfma_f32_16x16x32_bf16 v[124:127], v[60:63], a[96:99], 0\n"
    "ds_read_b128 a[64:67], v252 offset:32768\n"
    "v_mfma_f32_16x16x32_bf16 v[128:131], v[64:67], a[96:99], 0\n"
    "v_add_f32_e32 v233, v233, v245\n"
    "v_add_f32_e32 v232, v232, v244\n"
    "v_mfma_f32_16x16x32_bf16 v[132:135], v[68:71], a[96:99], 0\n"
    "v_rcp_f32_e32 v242, v234\n"
    "v_mfma_f32_16x16x32_bf16 v[120:123], v[72:75], a[100:103], v[120:123]\n"
    "s_add_u32 s48, s55, 16384\n"
    "ds_read_b128 a[68:71], v252 offset:33792\n"
    "v_mfma_f32_16x16x32_bf16 v[124:127], v[76:79], a[100:103], v[124:127]\n"
    "v_fma_f32 v216, v216, v242, v192\n"
    "v_fma_f32 v217, v217, v242, v193\n"
    "v_mfma_f32_16x16x32_bf16 v[128:131], v[80:83], a[100:103], v[128:131]\n"
    "ds_read_b128 a[72:75], v252 offset:34816\n"
    "v_fma_f32 v218, v218, v242, v194\n"
    "v_mfma_f32_16x16x32_bf16 v[132:135], v[84:87], a[100:103], v[132:135]\n"
    "s_waitcnt lgkmcnt(7)\n"
    "v_fma_f32 v219, v219, v242, v195\n"
    "v_fma_f32 v220, v220, v242, v196\n"
    "v_mfma_f32_16x16x32_bf16 v[120:123], v[88:91], a[104:107], v[120:123]\n"
    "v_fma_f32 v221, v221, v242, v197\n"
    "v_mfma_f32_16x16x32_bf16 v[124:127], v[92:95], a[104:107], v[124:127]\n"
    "ds_read_b128 a[76:79], v252 offset:35840\n"
    "v_fma_f32 v222, v222, v242, v198\n"
    "v_mfma_f32_16x16x32_bf16 v[128:131], v[96:99], a[104:107], v[128:131]\n"
    "v_fma_f32 v223, v223, v242, v199\n"
    "v_cvt_pk_bf16_f32 v216, v216, v217\n"
    "v_mfma_f32_16x16x32_bf16 v[132:135], v[100:103], a[104:107], v[132:135]\n"
    "ds_read_b128 a[80:83], v252 offset:36864\n"
    "v_cvt_pk_bf16_f32 v217, v218, v219\n"
    "v_mfma_f32_16x16x32_bf16 v[120:123], v[104:107], a[108:111], v[120:123]\n"
    "v_cvt_pk_bf16_f32 v218, v220, v221\n"
    "v_cvt_pk_bf16_f32 v219, v222, v223\n"
    "v_mfma_f32_16x16x32_bf16 v[124:127], v[108:111], a[108:111], v[124:127]\n"
    "buffer_store_dwordx4 v[216:219], v151, s[16:19], s48 offen offset:0 sc0 sc1\n"
    "v_fma_f32 v224, v224, v242, v200\n"
    "v_mfma_f32_16x16x32_bf16 v[128:131], v[112:115], a[108:111], v[128:131]\n"
    "ds_read_b128 a[84:87], v252 offset:37888\n"
    "v_fma_f32 v225, v225, v242, v201\n"
    "v_mfma_f32_16x16x32_bf16 v[132:135], v[116:119], a[108:111], v[132:135]\n"
    "s_waitcnt lgkmcnt(8)\n"
    "v_fma_f32 v226, v226, v242, v202\n"
    "v_fma_f32 v227, v227, v242, v203\n"
    "v_mfma_f32_16x16x32_bf16 v[120:123], a[28:31], a[112:115], v[120:123]\n"
    "ds_read_b128 a[88:91], v252 offset:38912\n"
    "v_fma_f32 v228, v228, v242, v204\n"
    "v_mfma_f32_16x16x32_bf16 v[124:127], a[60:63], a[112:115], v[124:127]\n"
    "v_fma_f32 v229, v229, v242, v205\n"
    "v_fma_f32 v230, v230, v242, v206\n"
    "v_mfma_f32_16x16x32_bf16 v[128:131], a[0:3], a[112:115], v[128:131]\n"
    "v_fma_f32 v231, v231, v242, v207\n"
    "v_cvt_pk_bf16_f32 v224, v224, v225\n"
    "v_mfma_f32_16x16x32_bf16 v[132:135], a[32:35], a[112:115], v[132:135]\n"
    "ds_read_b128 a[92:95], v252 offset:39936\n"
    "v_cvt_pk_bf16_f32 v225, v226, v227\n"
    "v_mfma_f32_16x16x32_bf16 v[120:123], a[4:7], a[116:119], v[120:123]\n"
    "v_cvt_pk_bf16_f32 v226, v228, v229\n"
    "v_cvt_pk_bf16_f32 v227, v230, v231\n"
    "v_mfma_f32_16x16x32_bf16 v[124:127], a[36:39], a[116:119], v[124:127]\n"
    "buffer_store_dwordx4 v[224:227], v151, s[16:19], s48 offen offset:64 sc0 sc1\n"
    "v_mfma_f32_16x16x32_bf16 v[128:131], a[8:11], a[116:119], v[128:131]\n"
    "s_bitcmp1_b32 %1, 0\n"
    "s_cselect_b64 vcc, -1, 0\n"
    "v_mfma_f32_16x16x32_bf16 v[132:135], a[40:43], a[116:119], v[132:135]\n"
    "s_waitcnt lgkmcnt(8)\n"
    "v_cndmask_b32_e32 v234, v233, v234, vcc\n"
    "v_mfma_f32_16x16x32_bf16 v[120:123], a[12:15], a[120:123], v[120:123]\n"
    "s_mov_b32 s61, 0x12800000\n"
    "v_rcp_f32_e32 v241, v233\n"
    "v_mfma_f32_16x16x32_bf16 v[124:127], a[44:47], a[120:123], v[124:127]\n"
    "v_rcp_f32_e32 v240, v232\n"
    "v_mfma_f32_16x16x32_bf16 v[128:131], a[16:19], a[120:123], v[128:131]\n"
    "v_subrev_u32_e32 v236, s61, v232\n"
    "v_mfma_f32_16x16x32_bf16 v[132:135], a[48:51], a[120:123], v[132:135]\n"
    "v_subrev_u32_e32 v237, s61, v233\n"
    "v_mfma_f32_16x16x32_bf16 v[120:123], a[20:23], a[124:127], v[120:123]\n"
    "v_subrev_u32_e32 v238, s61, v234\n"
    "v_max3_u32 v236, v236, v237, v238\n"
    "v_mfma_f32_16x16x32_bf16 v[124:127], a[52:55], a[124:127], v[124:127]\n"
    "buffer_store_dword v148, v149, s[8:11], s88 offen offset:0 sc1\n"
    "v_cmp_le_u32_e32 vcc, 0x5f000000, v236\n"
    "v_mfma_f32_16x16x32_bf16 v[128:131], a[24:27], a[124:127], v[128:131]\n"
    "v_mfma_f32_16x16x32_bf16 v[132:135], a[56:59], a[124:127], v[132:135]\n"
    "s_waitcnt lgkmcnt(0)\n"
    "v_mfma_f32_16x16x32_bf16 v[32:35], v[56:59], a[64:67], 0\n"
    "s_cbranch_vccnz .Lat_exit\n"
    "v_mfma_f32_16x16x32_bf16 v[36:39], v[60:63], a[64:67], 0\n"
    "s_add_u32 s48, s58, 8192\n"
    "v_fma_f32 v120, v120, v241, v176\n"
    "v_mfma_f32_16x16x32_bf16 v[32:35], v[72:75], a[68:71], v[32:35]\n"
    "v_fma_f32 v121, v121, v241, v177\n"
    "v_mfma_f32_16x16x32_bf16 v[36:39], v[76:79], a[68:71], v[36:39]\n"
    "v_fma_f32 v122, v122, v241, v178\n"
    "v_mfma_f32_16x16x32_bf16 v[32:35], v[88:91], a[72:75], v[32:35]\n"
    "v_fma_f32 v123, v123, v241, v179\n"
    "v_mfma_f32_16x16x32_bf16 v[36:39], v[92:95], a[72:75], v[36:39]\n"
    "v_fma_f32 v124, v124, v241, v180\n"
    "v_mfma_f32_16x16x32_bf16 v[32:35], v[104:107], a[76:79], v[32:35]\n"
    "v_fma_f32 v125, v125, v241, v181\n"
    "v_mfma_f32_16x16x32_bf16 v[36:39], v[108:111], a[76:79], v[36:39]\n"
    "v_fma_f32 v126, v126, v241, v182\n"
    "v_fma_f32 v127, v127, v241, v183\n"
    "v_mfma_f32_16x16x32_bf16 v[32:35], a[28:31], a[80:83], v[32:35]\n"
    "v_cvt_pk_bf16_f32 v120, v120, v121\n"
    "v_mfma_f32_16x16x32_bf16 v[36:39], a[60:63], a[80:83], v[36:39]\n"
    "v_cvt_pk_bf16_f32 v121, v122, v123\n"
    "v_mfma_f32_16x16x32_bf16 v[32:35], a[4:7], a[84:87], v[32:35]\n"
    "v_cvt_pk_bf16_f32 v122, v124, v125\n"
    "v_mfma_f32_16x16x32_bf16 v[36:39], a[36:39], a[84:87], v[36:39]\n"
    "v_cvt_pk_bf16_f32 v123, v126, v127\n"
    "v_mfma_f32_16x16x32_bf16 v[32:35], a[12:15], a[88:91], v[32:35]\n"
    "buffer_store_dwordx4 v[120:123], v151, s[16:19], s48 offen offset:0 sc0 sc1\n"
    "v_fma_f32 v128, v128, v241, v184\n"
    "v_mfma_f32_16x16x32_bf16 v[36:39], a[44:47], a[88:91], v[36:39]\n"
    "v_fma_f32 v129, v129, v241, v185\n"
    "v_fma_f32 v130, v130, v241, v186\n"
    "v_mfma_f32_16x16x32_bf16 v[32:35], a[20:23], a[92:95], v[32:35]\n"
    "v_fma_f32 v131, v131, v241, v187\n"
    "v_mfma_f32_16x16x32_bf16 v[36:39], a[52:55], a[92:95], v[36:39]\n"
    "v_fma_f32 v132, v132, v241, v188\n"
    "v_mfma_f32_16x16x32_bf16 v[40:43], v[64:67], a[64:67], 0\n"
    "v_fma_f32 v133, v133, v241, v189\n"
    "v_mfma_f32_16x16x32_bf16 v[44:47], v[68:71], a[64:67], 0\n"
    "v_fma_f32 v134, v134, v241, v190\n"
    "v_fma_f32 v135, v135, v241, v191\n"
    "v_mfma_f32_16x16x32_bf16 v[40:43], v[80:83], a[68:71], v[40:43]\n"
    "v_cvt_pk_bf16_f32 v128, v128, v129\n"
    "v_mfma_f32_16x16x32_bf16 v[44:47], v[84:87], a[68:71], v[44:47]\n"
    "v_cvt_pk_bf16_f32 v129, v130, v131\n"
    "v_mfma_f32_16x16x32_bf16 v[40:43], v[96:99], a[72:75], v[40:43]\n"
    "v_cvt_pk_bf16_f32 v130, v132, v133\n"
    "v_cvt_pk_bf16_f32 v131, v134, v135\n"
    "v_mfma_f32_16x16x32_bf16 v[44:47], v[100:103], a[72:75], v[44:47]\n"
    "buffer_store_dwordx4 v[128:131], v151, s[16:19], s48 offen offset:64 sc0 sc1\n"
    "v_mfma_f32_16x16x32_bf16 v[40:43], v[112:115], a[76:79], v[40:43]\n"
    "s_add_u32 s48, s58, 0\n"
    "v_fma_f32 v32, v32, v240, v160\n"
    "v_mfma_f32_16x16x32_bf16 v[44:47], v[116:119], a[76:79], v[44:47]\n"
    "v_fma_f32 v33, v33, v240, v161\n"
    "v_mfma_f32_16x16x32_bf16 v[40:43], a[0:3], a[80:83], v[40:43]\n"
    "v_fma_f32 v34, v34, v240, v162\n"
    "v_mfma_f32_16x16x32_bf16 v[44:47], a[32:35], a[80:83], v[44:47]\n"
    "v_fma_f32 v35, v35, v240, v163\n"
    "v_mfma_f32_16x16x32_bf16 v[40:43], a[8:11], a[84:87], v[40:43]\n"
    "v_fma_f32 v36, v36, v240, v164\n"
    "v_fma_f32 v37, v37, v240, v165\n"
    "v_mfma_f32_16x16x32_bf16 v[44:47], a[40:43], a[84:87], v[44:47]\n"
    "v_fma_f32 v38, v38, v240, v166\n"
    "v_mfma_f32_16x16x32_bf16 v[40:43], a[16:19], a[88:91], v[40:43]\n"
    "v_fma_f32 v39, v39, v240, v167\n"
    "v_mfma_f32_16x16x32_bf16 v[44:47], a[48:51], a[88:91], v[44:47]\n"
    "v_cvt_pk_bf16_f32 v32, v32, v33\n"
    "v_cvt_pk_bf16_f32 v33, v34, v35\n"
    "v_mfma_f32_16x16x32_bf16 v[40:43], a[24:27], a[92:95], v[40:43]\n"
    "v_cvt_pk_bf16_f32 v34, v36, v37\n"
    "v_cvt_pk_bf16_f32 v35, v38, v39\n"
    "v_mfma_f32_16x16x32_bf16 v[44:47], a[56:59], a[92:95], v[44:47]\n"
    "buffer_store_dwordx4 v[32:35], v151, s[16:19], s48 offen offset:0 sc0 sc1\n"
    "s_nop 2\n"
    "v_fma_f32 v40, v40, v240, v168\n"
    "v_fma_f32 v41, v41, v240, v169\n"
    "v_fma_f32 v42, v42, v240, v170\n"
    "v_fma_f32 v43, v43, v240, v171\n"
    "v_fma_f32 v44, v44, v240, v172\n"
    "v_fma_f32 v45, v45, v240, v173\n"
    "v_fma_f32 v46, v46, v240, v174\n"
    "v_fma_f32 v47, v47, v240, v175\n"
    "v_cvt_pk_bf16_f32 v40, v40, v41\n"
    "v_cvt_pk_bf16_f32 v41, v42, v43\n"
    "v_cvt_pk_bf16_f32 v42, v44, v45\n"
    "v_cvt_pk_bf16_f32 v43, v46, v47\n"
    "buffer_store_dwordx4 v[40:43], v151, s[16:19], s48 offen offset:64 sc0 sc1\n"
    "s_endpgm\n"
    ".Lat_two:\n"
    "s_add_u32 s46, s46, 81920\n"
    "ds_read_b128 a[160:163], v241 offset:11264\n"
    "v_accvgpr_read_b32 v223, a255\n"
    "v_add_u32_e32 v158, s46, v253\n"
    "v_mfma_f32_16x16x32_bf16 v[20:23], a[172:175], v[164:167], v[20:23]\n"
    "s_add_u32 s45, %3, 155840\n"
    "ds_read_b128 a[164:167], v241 offset:19456\n"
    "v_cvt_pk_bf16_f32 v216, v216, v217\n"
    "v_and_b32_e32 v150, 15, v147\n"
    "v_mfma_f32_16x16x32_bf16 v[20:23], a[168:171], v[160:163], v[20:23]\n"
    "s_waitcnt lgkmcnt(2)\n"
    "ds_read_b128 a[168:171], v240 offset:8192\n"
    "v_cvt_pk_bf16_f32 v217, v218, v219\n"
    "v_lshlrev_b32_e32 v151, 9, v150\n"
    "v_mfma_f32_16x16x32_bf16 v[24:27], a[176:179], v[160:163], v[24:27]\n"
    "ds_read_b128 a[172:175], v240 offset:16384\n"
    "buffer_load_dwordx4 a[56:59], v253, s[8:11], s48 offen offset:3072 sc1\n"
    "v_lshl_add_u32 v150, v150, 2, s45\n"
    "v_mfma_f32_16x16x32_bf16 v[24:27], a[180:183], v[164:167], v[24:27]\n"
    "s_lshl_b32 s46, %2, 7\n"
    "v_cvt_pk_bf16_f32 v218, v220, v221\n"
    "v_cvt_pk_bf16_f32 v219, v222, v223\n"
    "v_and_or_b32 v151, v147, 48, v151\n"
    "v_mfma_f32_16x16x32_bf16 v[48:51], a[180:183], v[172:175], v[48:51]\n"
    "buffer_store_dwordx4 v[216:219], v253, s[8:11], s83 offen offset:0 sc1\n"
    "v_cvt_pk_bf16_f32 v32, v32, v33\n"
    "v_cvt_pk_bf16_f32 v33, v34, v35\n"
    "v_mfma_f32_16x16x32_bf16 v[48:51], a[176:179], v[168:171], v[48:51]\n"
    "s_add_u32 s58, s35, s46\n"
    "ds_read_b128 a[176:179], v241 offset:8192\n"
    "v_cvt_pk_bf16_f32 v34, v36, v37\n"
    "v_mfma_f32_16x16x32_bf16 v[52:55], a[184:187], v[168:171], v[52:55]\n"
    "s_mul_i32 s45, %1, 74496\n"
    "ds_read_b128 a[180:183], v241 offset:16384\n"
    "v_cvt_pk_bf16_f32 v35, v38, v39\n"
    "v_mfma_f32_16x16x32_bf16 v[52:55], a[188:191], v[172:175], v[52:55]\n"
    "buffer_store_dwordx4 v[32:35], v253, s[8:11], s83 offen offset:1024 sc1\n"
    "v_cvt_pk_bf16_f32 v40, v40, v41\n"
    "v_cvt_pk_bf16_f32 v41, v42, v43\n"
    "v_mfma_f32_16x16x32_bf16 v[28:31], a[188:191], v[164:167], v[28:31]\n"
    "s_add_u32 s45, s45, 16265216\n"
    "v_cvt_pk_bf16_f32 v42, v44, v45\n"
    "v_cvt_pk_bf16_f32 v43, v46, v47\n"
    "v_mfma_f32_16x16x32_bf16 v[28:31], a[184:187], v[160:163], v[28:31]\n"
    "ds_read_b128 a[184:187], v240 offset:9216\n"
    "buffer_store_dwordx4 v[40:43], v253, s[8:11], s83 offen offset:2048 sc1\n"
    "v_cvt_pk_bf16_f32 v48, v48, v49\n"
    "v_mfma_f32_16x16x32_bf16 v[160:163], v[154:157], a[224:227], 0\n"
    "s_lshl_b32 s54, %2, 11\n"
    "ds_read_b128 a[188:191], v240 offset:17408\n"
    "v_cvt_pk_bf16_f32 v49, v50, v51\n"
    "v_mfma_f32_16x16x32_bf16 v[164:167], v[152:155], a[224:227], 0\n"
    "v_cvt_pk_bf16_f32 v50, v52, v53\n"
    "v_cvt_pk_bf16_f32 v51, v54, v55\n"
    "v_mfma_f32_16x16x32_bf16 v[168:171], v[154:157], a[228:231], 0\n"
    "buffer_store_dwordx4 v[48:51], v253, s[8:11], s83 offen offset:3072 sc1\n"
    "v_and_b32_e32 v147, 0xf0, v253\n"
    "v_mfma_f32_16x16x32_bf16 v[172:175], v[152:155], a[228:231], 0\n"
    "buffer_store_dword v55, v251, s[8:11], s60 offen offset:0 sc1\n"
    "v_add_u32_e32 v252, %3, v253\n"
    "v_mfma_f32_16x16x32_bf16 v[176:179], v[154:157], a[232:235], 0\n"
    "s_add_u32 s54, s54, s45\n"
    "s_add_u32 s54, s54, 16384\n"
    "s_lshl1_add_u32 s59, s50, s51\n"
    "v_mfma_f32_16x16x32_bf16 v[180:183], v[152:155], a[232:235], 0\n"
    "s_lshl_b32 s59, s59, 12\n"
    "s_add_u32 s46, s59, s45\n"
    "v_mfma_f32_16x16x32_bf16 v[184:187], v[154:157], a[236:239], 0\n"
    "s_bitcmp1_b32 %1, 0\n"
    "s_cselect_b32 s54, s54, 2.0\n"
    "s_cselect_b32 s55, s58, 2.0\n"
    "v_mfma_f32_16x16x32_bf16 v[188:191], v[152:155], a[236:239], 0\n"
    "s_waitcnt lgkmcnt(6)\n"
    "ds_write_b32 v255, v31 offset:0\n"
    "v_mfma_f32_16x16x32_bf16 v[160:163], a[192:195], a[160:163], v[160:163]\n"
    "s_waitcnt vmcnt(8)\n"
    "v_cmp_ne_u32_e32 vcc, s39, v146\n"
    "v_mfma_f32_16x16x32_bf16 v[164:167], a[196:199], a[160:163], v[164:167]\n"
    "s_add_u32 s61, s46, 0\n"
    "v_mfma_f32_16x16x32_bf16 v[168:171], a[200:203], a[160:163], v[168:171]\n"
    "s_cbranch_vccnz .Lat_two_xpoll\n"
    ".Lat_two_xpoll_ret:\n"
    "v_mfma_f32_16x16x32_bf16 v[172:175], a[204:207], a[160:163], v[172:175]\n"
    "ds_read_b128 a[160:163], v241 offset:9216\n"
    "v_mfma_f32_16x16x32_bf16 v[176:179], a[192:195], a[164:167], v[176:179]\n"
    "buffer_load_dwordx4 a[224:227], v253, s[8:11], s61 offen offset:0 sc1\n"
    "v_mfma_f32_16x16x32_bf16 v[180:183], a[196:199], a[164:167], v[180:183]\n"
    "v_mfma_f32_16x16x32_bf16 v[184:187], a[200:203], a[164:167], v[184:187]\n"
    "v_mfma_f32_16x16x32_bf16 v[188:191], a[204:207], a[164:167], v[188:191]\n"
    "s_waitcnt lgkmcnt(6)\n"
    "ds_read_b128 a[164:167], v241 offset:17408\n"
    "v_mfma_f32_16x16x32_bf16 v[160:163], a[208:211], a[168:171], v[160:163]\n"
    "buffer_load_dwordx4 a[228:231], v253, s[8:11], s61 offen offset:1024 sc1\n"
    "v_mfma_f32_16x16x32_bf16 v[164:167], a[212:215], a[168:171], v[164:167]\n"
    "v_mfma_f32_16x16x32_bf16 v[168:171], a[216:219], a[168:171], v[168:171]\n"
    "v_mfma_f32_16x16x32_bf16 v[172:175], a[220:223], a[168:171], v[172:175]\n"
    "ds_read_b128 a[168:171], v240 offset:10240\n"
    "buffer_load_dwordx4 a[232:235], v253, s[8:11], s61 offen offset:2048 sc1\n"
    "v_mfma_f32_16x16x32_bf16 v[176:179], a[208:211], a[172:175], v[176:179]\n"
    "s_waitcnt lgkmcnt(3)\n"
    "s_barrier\n"
    "v_mfma_f32_16x16x32_bf16 v[180:183], a[212:215], a[172:175], v[180:183]\n"
    "ds_read_b32 v232, v150 offset:0\n"
    "v_mfma_f32_16x16x32_bf16 v[184:187], a[216:219], a[172:175], v[184:187]\n"
    "buffer_load_dwordx4 a[236:239], v253, s[8:11], s61 offen offset:3072 sc1\n"
    "v_mfma_f32_16x16x32_bf16 v[188:191], a[220:223], a[172:175], v[188:191]\n"
    "s_add_u32 s61, s46, 24832\n"
    "ds_read_b128 a[172:175], v240 offset:18432\n"
    "v_mfma_f32_16x16x32_bf16 v[160:163], a[92:95], a[176:179], v[160:163]\n"
    "ds_read_b32 v233, v150 offset:256\n"
    "v_mfma_f32_16x16x32_bf16 v[164:167], a[124:127], a[176:179], v[164:167]\n"
    "buffer_load_dwordx4 a[240:243], v253, s[8:11], s61 offen offset:0 sc1\n"
    "v_mfma_f32_16x16x32_bf16 v[168:171], a[156:159], a[176:179], v[168:171]\n"
    "ds_read_b32 v234, v150 offset:512\n"
    "v_mfma_f32_16x16x32_bf16 v[172:175], a[64:67], a[176:179], v[172:175]\n"
    "ds_read_b128 a[176:179], v241 offset:10240\n"
    "v_mfma_f32_16x16x32_bf16 v[176:179], a[92:95], a[180:183], v[176:179]\n"
    "buffer_load_dwordx4 a[244:247], v253, s[8:11], s61 offen offset:1024 sc1\n"
    "v_mfma_f32_16x16x32_bf16 v[180:183], a[124:127], a[180:183], v[180:183]\n"
    "s_lshl_b32 s52, %2, 11\n"
    "v_mfma_f32_16x16x32_bf16 v[184:187], a[156:159], a[180:183], v[184:187]\n"
    "v_mfma_f32_16x16x32_bf16 v[188:191], a[64:67], a[180:183], v[188:191]\n"
    "ds_read_b128 a[180:183], v241 offset:18432\n"
    "v_mfma_f32_16x16x32_bf16 v[160:163], a[96:99], a[184:187], v[160:163]\n"
    "buffer_load_dwordx4 a[248:251], v253, s[8:11], s61 offen offset:2048 sc1\n"
    "v_mfma_f32_16x16x32_bf16 v[164:167], a[128:131], a[184:187], v[164:167]\n"
    "v_add_u32_e32 v251, s52, v252\n"
    "v_mfma_f32_16x16x32_bf16 v[168:171], a[68:71], a[184:187], v[168:171]\n"
    "v_add_u32_e32 v158, s59, v252\n"
    "v_mfma_f32_16x16x32_bf16 v[172:175], a[100:103], a[184:187], v[172:175]\n"
    "ds_read_b128 a[184:187], v240 offset:11264\n"
    "buffer_load_dwordx4 a[252:255], v253, s[8:11], s61 offen offset:3072 sc1\n"
    "v_mfma_f32_16x16x32_bf16 v[176:179], a[96:99], a[188:191], v[176:179]\n"
    "s_add_u32 s61, s45, 24576\n"
    "v_and_b32_e32 v145, 12, v145\n"
    "v_mfma_f32_16x16x32_bf16 v[180:183], a[128:131], a[188:191], v[180:183]\n"
    "v_mfma_f32_16x16x32_bf16 v[184:187], a[68:71], a[188:191], v[184:187]\n"
    "buffer_load_dwordx3 v[236:238], v147, s[8:11], s61 offen offset:0 sc1\n"
    "v_mfma_f32_16x16x32_bf16 v[188:191], a[100:103], a[188:191], v[188:191]\n"
    "s_waitcnt lgkmcnt(8)\n"
    "s_add_u32 s61, s45, 49408\n"
    "ds_read_b128 a[188:191], v240 offset:19456\n"
    "v_mfma_f32_16x16x32_bf16 v[160:163], a[132:135], a[160:163], v[160:163]\n"
    "s_add_u32 s52, s32, 3\n"
    "v_mfma_f32_16x16x32_bf16 v[164:167], a[72:75], a[160:163], v[164:167]\n"
    "buffer_load_dwordx3 v[248:250], v147, s[8:11], s61 offen offset:0 sc1\n"
    "v_mfma_f32_16x16x32_bf16 v[168:171], a[104:107], a[160:163], v[168:171]\n"
    "s_and_b32 s52, s52, 3\n"
    "v_mfma_f32_16x16x32_bf16 v[172:175], a[136:139], a[160:163], v[172:175]\n"
    "s_lshl_b32 s52, s52, 8\n"
    "v_mfma_f32_16x16x32_bf16 v[176:179], a[132:135], a[164:167], v[176:179]\n"
    "s_waitcnt vmcnt(10)\n"
    "buffer_store_dword v148, v149, s[8:11], s87 offen offset:0 sc1\n"
    "v_mfma_f32_16x16x32_bf16 v[180:183], a[72:75], a[164:167], v[180:183]\n"
    "v_mfma_f32_16x16x32_bf16 v[184:187], a[104:107], a[164:167], v[184:187]\n"
    "s_add_u32 s52, s52, s47\n"
    "v_mfma_f32_16x16x32_bf16 v[188:191], a[136:139], a[164:167], v[188:191]\n"
    "s_waitcnt lgkmcnt(6)\n"
    "v_mfma_f32_16x16x32_bf16 v[160:163], a[76:79], a[168:171], v[160:163]\n"
    "buffer_load_dword v144, v145, s[8:11], s52 offen offset:0 sc1\n"
    "v_mfma_f32_16x16x32_bf16 v[164:167], a[108:111], a[168:171], v[164:167]\n"
    "v_mfma_f32_16x16x32_bf16 v[168:171], a[140:143], a[168:171], v[168:171]\n"
    "v_mfma_f32_16x16x32_bf16 v[172:175], a[80:83], a[168:171], v[172:175]\n"
    "v_mfma_f32_16x16x32_bf16 v[176:179], a[76:79], a[172:175], v[176:179]\n"
    "v_mfma_f32_16x16x32_bf16 v[180:183], a[108:111], a[172:175], v[180:183]\n"
    "v_mfma_f32_16x16x32_bf16 v[184:187], a[140:143], a[172:175], v[184:187]\n"
    "v_mfma_f32_16x16x32_bf16 v[188:191], a[80:83], a[172:175], v[188:191]\n"
    "s_waitcnt lgkmcnt(2)\n"
    "v_mfma_f32_16x16x32_bf16 v[160:163], a[112:115], a[176:179], v[160:163]\n"
    "v_mfma_f32_16x16x32_bf16 v[164:167], a[144:147], a[176:179], v[164:167]\n"
    "v_mfma_f32_16x16x32_bf16 v[168:171], a[84:87], a[176:179], v[168:171]\n"
    "v_mfma_f32_16x16x32_bf16 v[172:175], a[116:119], a[176:179], v[172:175]\n"
    "v_mfma_f32_16x16x32_bf16 v[176:179], a[112:115], a[180:183], v[176:179]\n"
    "v_mfma_f32_16x16x32_bf16 v[180:183], a[144:147], a[180:183], v[180:183]\n"
    "v_mfma_f32_16x16x32_bf16 v[184:187], a[84:87], a[180:183], v[184:187]\n"
    "v_mfma_f32_16x16x32_bf16 v[188:191], a[116:119], a[180:183], v[188:191]\n"
    "s_waitcnt lgkmcnt(0)\n"
    "v_mfma_f32_16x16x32_bf16 v[160:163], a[148:151], a[184:187], v[160:163]\n"
    "v_mfma_f32_16x16x32_bf16 v[164:167], a[88:91], a[184:187], v[164:167]\n"
    "v_mfma_f32_16x16x32_bf16 v[168:171], a[120:123], a[184:187], v[168:171]\n"
    "v_mfma_f32_16x16x32_bf16 v[172:175], a[152:155], a[184:187], v[172:175]\n"
    "v_mfma_f32_16x16x32_bf16 v[176:179], a[148:151], a[188:191], v[176:179]\n"
    "v_mfma_f32_16x16x32_bf16 v[180:183], a[88:91], a[188:191], v[180:183]\n"
    "v_mfma_f32_16x16x32_bf16 v[184:187], a[120:123], a[188:191], v[184:187]\n"
    "v_mfma_f32_16x16x32_bf16 v[188:191], a[152:155], a[188:191], v[188:191]\n"
    "s_waitcnt vmcnt(10)\n"
    "v_mfma_f32_16x16x32_bf16 v[0:3], v[154:157], a[224:227], v[0:3]\n"
    "s_waitcnt vmcnt(0)\n"
    "v_cmp_ne_u32_e32 vcc, s39, v144\n"
    "s_nop 3\n"
    "s_cbranch_vccnz .Lat_two_ypoll\n"
    ".Lat_two_ypoll_ret:\n"
    "s_add_u32 s61, s46, 49664\n"
    "buffer_load_dwordx4 a[192:195], v253, s[8:11], s61 offen offset:0 sc1\n"
    "buffer_load_dwordx4 a[196:199], v253, s[8:11], s61 offen offset:1024 sc1\n"
    "buffer_load_dwordx4 a[200:203], v253, s[8:11], s61 offen offset:2048 sc1\n"
    "buffer_load_dwordx4 a[204:207], v253, s[8:11], s61 offen offset:3072 sc1\n"
    "s_add_u32 s61, s45, 74240\n"
    "buffer_load_dwordx3 v[244:246], v147, s[8:11], s61 offen offset:0 sc1\n"
    "v_mfma_f32_16x16x32_bf16 v[4:7], v[152:155], a[224:227], v[4:7]\n"
    "v_mfma_f32_16x16x32_bf16 v[8:11], v[154:157], a[228:231], v[8:11]\n"
    "s_waitcnt vmcnt(7)\n"
    "v_mfma_f32_16x16x32_bf16 v[12:15], v[152:155], a[228:231], v[12:15]\n"
    "s_waitcnt vmcnt(13)\n"
    "v_mfma_f32_16x16x32_bf16 v[16:19], v[154:157], a[232:235], v[16:19]\n"
    "v_add_f32_e32 v232, v232, v236\n"
    "v_mfma_f32_16x16x32_bf16 v[20:23], v[152:155], a[232:235], v[20:23]\n"
    "v_mfma_f32_16x16x32_bf16 v[24:27], v[154:157], a[236:239], v[24:27]\n"
    "v_add_f32_e32 v233, v233, v237\n"
    "v_mfma_f32_16x16x32_bf16 v[28:31], v[152:155], a[236:239], v[28:31]\n"
    "s_waitcnt vmcnt(11)\n"
    "v_mfma_f32_16x16x32_bf16 v[0:3], v[154:157], a[240:243], v[0:3]\n"
    "v_add_f32_e32 v234, v234, v238\n"
    "v_mfma_f32_16x16x32_bf16 v[4:7], v[152:155], a[240:243], v[4:7]\n"
    "v_mfma_f32_16x16x32_bf16 v[8:11], v[154:157], a[244:247], v[8:11]\n"
    "v_add_f32_e32 v232, v232, v248\n"
    "v_mfma_f32_16x16x32_bf16 v[12:15], v[152:155], a[244:247], v[12:15]\n"
    "s_waitcnt vmcnt(9)\n"
    "v_mfma_f32_16x16x32_bf16 v[16:19], v[154:157], a[248:251], v[16:19]\n"
    "v_add_f32_e32 v233, v233, v249\n"
    "v_mfma_f32_16x16x32_bf16 v[20:23], v[152:155], a[248:251], v[20:23]\n"
    "v_mfma_f32_16x16x32_bf16 v[24:27], v[154:157], a[252:255], v[24:27]\n"
    "v_add_f32_e32 v234, v234, v250\n"
    "v_mfma_f32_16x16x32_bf16 v[28:31], v[152:155], a[252:255], v[28:31]\n"
    "s_waitcnt vmcnt(4)\n"
    "v_mfma_f32_16x16x32_bf16 v[0:3], v[154:157], a[192:195], v[0:3]\n"
    "v_mfma_f32_16x16x32_bf16 v[4:7], v[152:155], a[192:195], v[4:7]\n"
    "s_waitcnt vmcnt(3)\n"
    "v_mfma_f32_16x16x32_bf16 v[8:11], v[154:157], a[196:199], v[8:11]\n"
    "v_mfma_f32_16x16x32_bf16 v[12:15], v[152:155], a[196:199], v[12:15]\n"
    "s_waitcnt vmcnt(2)\n"
    "v_cvt_pk_bf16_f32 v0, v0, v1\n"
    "v_cvt_pk_bf16_f32 v1, v2, v3\n"
    "v_mfma_f32_16x16x32_bf16 v[16:19], v[154:157], a[200:203], v[16:19]\n"
    "v_cvt_pk_bf16_f32 v2, v4, v5\n"
    "v_cvt_pk_bf16_f32 v3, v6, v7\n"
    "v_mfma_f32_16x16x32_bf16 v[20:23], v[152:155], a[200:203], v[20:23]\n"
    "s_waitcnt vmcnt(1)\n"
    "ds_write_b128 v158, v[0:3] offset:32768\n"
    "v_mfma_f32_16x16x32_bf16 v[24:27], v[154:157], a[204:207], v[24:27]\n"
    "v_cvt_pk_bf16_f32 v8, v8, v9\n"
    "v_cvt_pk_bf16_f32 v9, v10, v11\n"
    "v_cvt_pk_bf16_f32 v10, v12, v13\n"
    "v_cvt_pk_bf16_f32 v11, v14, v15\n"
    "v_mfma_f32_16x16x32_bf16 v[28:31], v[152:155], a[204:207], v[28:31]\n"
    "ds_write_b128 v158, v[8:11] offset:33792\n"
    "v_cvt_pk_bf16_f32 v16, v16, v17\n"
    "v_cvt_pk_bf16_f32 v17, v18, v19\n"
    "v_cvt_pk_bf16_f32 v18, v20, v21\n"
    "v_cvt_pk_bf16_f32 v19, v22, v23\n"
    "ds_write_b128 v158, v[16:19] offset:34816\n"
    "v_cvt_pk_bf16_f32 v24, v24, v25\n"
    "v_cvt_pk_bf16_f32 v25, v26, v27\n"
    "v_cvt_pk_bf16_f32 v26, v28, v29\n"
    "v_cvt_pk_bf16_f32 v27, v30, v31\n"
    "ds_write_b128 v158, v[24:27] offset:35840\n"
    "s_waitcnt lgkmcnt(0)\n"
    "s_barrier\n"
    "ds_read_b128 a[96:99], v252 offset:40960\n"
    "ds_read_b128 a[100:103], v252 offset:41984\n"
    "ds_read_b128 a[104:107], v252 offset:43008\n"
    "ds_read_b128 a[108:111], v252 offset:44032\n"
    "ds_read_b128 a[112:115], v252 offset:45056\n"
    "ds_read_b128 a[116:119], v252 offset:46080\n"
    "ds_read_b128 a[120:123], v252 offset:47104\n"
    "ds_read_b128 a[124:127], v252 offset:48128\n"
    "s_waitcnt lgkmcnt(6)\n"
    "v_mfma_f32_16x16x32_bf16 v[120:123], v[56:59], a[96:99], 0\n"
    "v_mfma_f32_16x16x32_bf16 v[124:127], v[60:63], a[96:99], 0\n"
    "s_waitcnt vmcnt(0)\n"
    "ds_read_b128 a[64:67], v252 offset:32768\n"
    "v_mfma_f32_16x16x32_bf16 v[128:131], v[64:67], a[96:99], 0\n"
    "v_mfma_f32_16x16x32_bf16 v[132:135], v[68:71], a[96:99], 0\n"
    "v_add_f32_e32 v234, v234, v246\n"
    "v_mfma_f32_16x16x32_bf16 v[120:123], v[72:75], a[100:103], v[120:123]\n"
    "ds_read_b128 a[68:71], v252 offset:33792\n"
    "v_mfma_f32_16x16x32_bf16 v[124:127], v[76:79], a[100:103], v[124:127]\n"
    "v_add_f32_e32 v233, v233, v245\n"
    "v_mfma_f32_16x16x32_bf16 v[128:131], v[80:83], a[100:103], v[128:131]\n"
    "ds_read_b128 a[72:75], v252 offset:34816\n"
    "v_mfma_f32_16x16x32_bf16 v[132:135], v[84:87], a[100:103], v[132:135]\n"
    "s_waitcnt lgkmcnt(7)\n"
    "v_add_f32_e32 v232, v232, v244\n"
    "v_mfma_f32_16x16x32_bf16 v[120:123], v[88:91], a[104:107], v[120:123]\n"
    "v_mfma_f32_16x16x32_bf16 v[124:127], v[92:95], a[104:107], v[124:127]\n"
    "ds_read_b128 a[76:79], v252 offset:35840\n"
    "v_mfma_f32_16x16x32_bf16 v[128:131], v[96:99], a[104:107], v[128:131]\n"
    "s_bitcmp1_b32 %1, 0\n"
    "s_cselect_b64 vcc, -1, 0\n"
    "v_mfma_f32_16x16x32_bf16 v[132:135], v[100:103], a[104:107], v[132:135]\n"
    "ds_read_b128 a[80:83], v252 offset:36864\n"
    "v_mfma_f32_16x16x32_bf16 v[120:123], v[104:107], a[108:111], v[120:123]\n"
    "v_cndmask_b32_e32 v234, v233, v234, vcc\n"
    "v_mfma_f32_16x16x32_bf16 v[124:127], v[108:111], a[108:111], v[124:127]\n"
    "s_mov_b32 s61, 0x12800000\n"
    "v_mfma_f32_16x16x32_bf16 v[128:131], v[112:115], a[108:111], v[128:131]\n"
    "ds_read_b128 a[84:87], v252 offset:37888\n"
    "v_mfma_f32_16x16x32_bf16 v[132:135], v[116:119], a[108:111], v[132:135]\n"
    "s_waitcnt lgkmcnt(8)\n"
    "v_rcp_f32_e32 v241, v233\n"
    "v_mfma_f32_16x16x32_bf16 v[120:123], a[28:31], a[112:115], v[120:123]\n"
    "ds_read_b128 a[88:91], v252 offset:38912\n"
    "v_mfma_f32_16x16x32_bf16 v[124:127], a[60:63], a[112:115], v[124:127]\n"
    "v_rcp_f32_e32 v240, v232\n"
    "v_mfma_f32_16x16x32_bf16 v[128:131], a[0:3], a[112:115], v[128:131]\n"
    "v_mfma_f32_16x16x32_bf16 v[132:135], a[32:35], a[112:115], v[132:135]\n"
    "ds_read_b128 a[92:95], v252 offset:39936\n"
    "v_mfma_f32_16x16x32_bf16 v[120:123], a[4:7], a[116:119], v[120:123]\n"
    "v_subrev_u32_e32 v236, s61, v232\n"
    "v_mfma_f32_16x16x32_bf16 v[124:127], a[36:39], a[116:119], v[124:127]\n"
    "v_subrev_u32_e32 v237, s61, v233\n"
    "v_mfma_f32_16x16x32_bf16 v[128:131], a[8:11], a[116:119], v[128:131]\n"
    "v_mfma_f32_16x16x32_bf16 v[132:135], a[40:43], a[116:119], v[132:135]\n"
    "s_waitcnt lgkmcnt(8)\n"
    "v_subrev_u32_e32 v238, s61, v234\n"
    "v_mfma_f32_16x16x32_bf16 v[120:123], a[12:15], a[120:123], v[120:123]\n"
    "v_mfma_f32_16x16x32_bf16 v[124:127], a[44:47], a[120:123], v[124:127]\n"
    "v_max3_u32 v236, v236, v237, v238\n"
    "v_mfma_f32_16x16x32_bf16 v[128:131], a[16:19], a[120:123], v[128:131]\n"
    "v_mfma_f32_16x16x32_bf16 v[132:135], a[48:51], a[120:123], v[132:135]\n"
    "v_cmp_le_u32_e32 vcc, 0x5f000000, v236\n"
    "v_mfma_f32_16x16x32_bf16 v[120:123], a[20:23], a[124:127], v[120:123]\n"
    "v_mfma_f32_16x16x32_bf16 v[124:127], a[52:55], a[124:127], v[124:127]\n"
    "buffer_store_dword v148, v149, s[8:11], s88 offen offset:0 sc1\n"
    "v_mfma_f32_16x16x32_bf16 v[128:131], a[24:27], a[124:127], v[128:131]\n"
    "v_mfma_f32_16x16x32_bf16 v[132:135], a[56:59], a[124:127], v[132:135]\n"
    "s_waitcnt lgkmcnt(0)\n"
    "v_mfma_f32_16x16x32_bf16 v[32:35], v[56:59], a[64:67], 0\n"
    "s_cbranch_vccnz .Lat_exit\n"
    "v_mfma_f32_16x16x32_bf16 v[36:39], v[60:63], a[64:67], 0\n"
    "s_add_u32 s48, s58, 8192\n"
    "v_fma_f32 v120, v120, v241, v176\n"
    "v_mfma_f32_16x16x32_bf16 v[32:35], v[72:75], a[68:71], v[32:35]\n"
    "v_fma_f32 v121, v121, v241, v177\n"
    "v_mfma_f32_16x16x32_bf16 v[36:39], v[76:79], a[68:71], v[36:39]\n"
    "v_fma_f32 v122, v122, v241, v178\n"
    "v_mfma_f32_16x16x32_bf16 v[32:35], v[88:91], a[72:75], v[32:35]\n"
    "v_fma_f32 v123, v123, v241, v179\n"
    "v_mfma_f32_16x16x32_bf16 v[36:39], v[92:95], a[72:75], v[36:39]\n"
    "v_fma_f32 v124, v124, v241, v180\n"
    "v_mfma_f32_16x16x32_bf16 v[32:35], v[104:107], a[76:79], v[32:35]\n"
    "v_fma_f32 v125, v125, v241, v181\n"
    "v_mfma_f32_16x16x32_bf16 v[36:39], v[108:111], a[76:79], v[36:39]\n"
    "v_fma_f32 v126, v126, v241, v182\n"
    "v_fma_f32 v127, v127, v241, v183\n"
    "v_mfma_f32_16x16x32_bf16 v[32:35], a[28:31], a[80:83], v[32:35]\n"
    "v_cvt_pk_bf16_f32 v120, v120, v121\n"
    "v_mfma_f32_16x16x32_bf16 v[36:39], a[60:63], a[80:83], v[36:39]\n"
    "v_cvt_pk_bf16_f32 v121, v122, v123\n"
    "v_mfma_f32_16x16x32_bf16 v[32:35], a[4:7], a[84:87], v[32:35]\n"
    "v_cvt_pk_bf16_f32 v122, v124, v125\n"
    "v_mfma_f32_16x16x32_bf16 v[36:39], a[36:39], a[84:87], v[36:39]\n"
    "v_cvt_pk_bf16_f32 v123, v126, v127\n"
    "v_mfma_f32_16x16x32_bf16 v[32:35], a[12:15], a[88:91], v[32:35]\n"
    "buffer_store_dwordx4 v[120:123], v151, s[16:19], s48 offen offset:0 sc0 sc1\n"
    "v_fma_f32 v128, v128, v241, v184\n"
    "v_mfma_f32_16x16x32_bf16 v[36:39], a[44:47], a[88:91], v[36:39]\n"
    "v_fma_f32 v129, v129, v241, v185\n"
    "v_fma_f32 v130, v130, v241, v186\n"
    "v_mfma_f32_16x16x32_bf16 v[32:35], a[20:23], a[92:95], v[32:35]\n"
    "v_fma_f32 v131, v131, v241, v187\n"
    "v_mfma_f32_16x16x32_bf16 v[36:39], a[52:55], a[92:95], v[36:39]\n"
    "v_fma_f32 v132, v132, v241, v188\n"
    "v_mfma_f32_16x16x32_bf16 v[40:43], v[64:67], a[64:67], 0\n"
    "v_fma_f32 v133, v133, v241, v189\n"
    "v_mfma_f32_16x16x32_bf16 v[44:47], v[68:71], a[64:67], 0\n"
    "v_fma_f32 v134, v134, v241, v190\n"
    "v_fma_f32 v135, v135, v241, v191\n"
    "v_mfma_f32_16x16x32_bf16 v[40:43], v[80:83], a[68:71], v[40:43]\n"
    "v_cvt_pk_bf16_f32 v128, v128, v129\n"
    "v_mfma_f32_16x16x32_bf16 v[44:47], v[84:87], a[68:71], v[44:47]\n"
    "v_cvt_pk_bf16_f32 v129, v130, v131\n"
    "v_mfma_f32_16x16x32_bf16 v[40:43], v[96:99], a[72:75], v[40:43]\n"
    "v_cvt_pk_bf16_f32 v130, v132, v133\n"
    "v_cvt_pk_bf16_f32 v131, v134, v135\n"
    "v_mfma_f32_16x16x32_bf16 v[44:47], v[100:103], a[72:75], v[44:47]\n"
    "buffer_store_dwordx4 v[128:131], v151, s[16:19], s48 offen offset:64 sc0 sc1\n"
    "v_mfma_f32_16x16x32_bf16 v[40:43], v[112:115], a[76:79], v[40:43]\n"
    "s_add_u32 s48, s58, 0\n"
    "v_fma_f32 v32, v32, v240, v160\n"
    "v_mfma_f32_16x16x32_bf16 v[44:47], v[116:119], a[76:79], v[44:47]\n"
    "v_fma_f32 v33, v33, v240, v161\n"
    "v_mfma_f32_16x16x32_bf16 v[40:43], a[0:3], a[80:83], v[40:43]\n"
    "v_fma_f32 v34, v34, v240, v162\n"
    "v_mfma_f32_16x16x32_bf16 v[44:47], a[32:35], a[80:83], v[44:47]\n"
    "v_fma_f32 v35, v35, v240, v163\n"
    "v_mfma_f32_16x16x32_bf16 v[40:43], a[8:11], a[84:87], v[40:43]\n"
    "v_fma_f32 v36, v36, v240, v164\n"
    "v_fma_f32 v37, v37, v240, v165\n"
    "v_mfma_f32_16x16x32_bf16 v[44:47], a[40:43], a[84:87], v[44:47]\n"
    "v_fma_f32 v38, v38, v240, v166\n"
    "v_mfma_f32_16x16x32_bf16 v[40:43], a[16:19], a[88:91], v[40:43]\n"
    "v_fma_f32 v39, v39, v240, v167\n"
    "v_mfma_f32_16x16x32_bf16 v[44:47], a[48:51], a[88:91], v[44:47]\n"
    "v_cvt_pk_bf16_f32 v32, v32, v33\n"
    "v_cvt_pk_bf16_f32 v33, v34, v35\n"
    "v_mfma_f32_16x16x32_bf16 v[40:43], a[24:27], a[92:95], v[40:43]\n"
    "v_cvt_pk_bf16_f32 v34, v36, v37\n"
    "v_cvt_pk_bf16_f32 v35, v38, v39\n"
    "v_mfma_f32_16x16x32_bf16 v[44:47], a[56:59], a[92:95], v[44:47]\n"
    "buffer_store_dwordx4 v[32:35], v151, s[16:19], s48 offen offset:0 sc0 sc1\n"
    "s_nop 2\n"
    "v_fma_f32 v40, v40, v240, v168\n"
    "v_fma_f32 v41, v41, v240, v169\n"
    "v_fma_f32 v42, v42, v240, v170\n"
    "v_fma_f32 v43, v43, v240, v171\n"
    "v_fma_f32 v44, v44, v240, v172\n"
    "v_fma_f32 v45, v45, v240, v173\n"
    "v_fma_f32 v46, v46, v240, v174\n"
    "v_fma_f32 v47, v47, v240, v175\n"
    "v_cvt_pk_bf16_f32 v40, v40, v41\n"
    "v_cvt_pk_bf16_f32 v41, v42, v43\n"
    "v_cvt_pk_bf16_f32 v42, v44, v45\n"
    "v_cvt_pk_bf16_f32 v43, v46, v47\n"
    "buffer_store_dwordx4 v[40:43], v151, s[16:19], s48 offen offset:64 sc0 sc1\n"
    "s_endpgm\n"
    ".Lat_two_xpoll:\n"
    "s_mov_b32 s53, 0x400000\n"
    ".Lat_two_xpoll_l:\n"
    "s_sleep 0\n"
    "buffer_load_dword v146, v145, s[8:11], s47 offen offset:128 sc1\n"
    "s_waitcnt vmcnt(0)\n"
    "v_cmp_ne_u32_e32 vcc, s39, v146\n"
    "s_sub_u32 s53, s53, 1\n"
    "s_cbranch_scc1 .Lat_two_xpoll_ret\n"
    "s_nop 2\n"
    "s_cbranch_vccnz .Lat_two_xpoll_l\n"
    "s_branch .Lat_two_xpoll_ret\n"
    ".Lat_two_ypoll:\n"
    "s_mov_b32 s53, 0x400000\n"
    ".Lat_two_ypoll_l:\n"
    "s_sleep 0\n"
    "buffer_load_dword v144, v145, s[8:11], s52 offen offset:0 sc1\n"
    "s_waitcnt vmcnt(0)\n"
    "v_cmp_ne_u32_e32 vcc, s39, v144\n"
    "s_sub_u32 s53, s53, 1\n"
    "s_cbranch_scc1 .Lat_two_ypoll_ret\n"
    "s_nop 2\n"
    "s_cbranch_vccnz .Lat_two_ypoll_l\n"
    "s_branch .Lat_two_ypoll_ret\n"
    ".Lat_wpoll:\n"
    "s_mov_b32 s53, 0x400000\n"
    ".Lat_wpoll_l:\n"
    "s_sleep 1\n"
    "buffer_load_dword v255, v254, s[8:11], s45 offen offset:0 sc1\n"
    "s_waitcnt vmcnt(0)\n"
    "v_cmp_ne_u32_e32 vcc, s39, v255\n"
    "s_sub_u32 s53, s53, 1\n"
    "s_cbranch_scc1 .Lat_wpoll_ret\n"
    "s_nop 2\n"
    "s_cbranch_vccnz .Lat_wpoll_l\n"
    "s_branch .Lat_wpoll_ret\n"
    ".Lat_xpoll:\n"
    "s_mov_b32 s53, 0x400000\n"
    ".Lat_xpoll_l:\n"
    "s_sleep 0\n"
    "buffer_load_dword v146, v145, s[8:11], s47 offen offset:128 sc1\n"
    "s_waitcnt vmcnt(0)\n"
    "v_cmp_ne_u32_e32 vcc, s39, v146\n"
    "s_sub_u32 s53, s53, 1\n"
    "s_cbranch_scc1 .Lat_xpoll_ret\n"
    "s_nop 2\n"
    "s_cbranch_vccnz .Lat_xpoll_l\n"
    "s_branch .Lat_xpoll_ret\n"
    ".Lat_ypoll:\n"
    "s_mov_b32 s53, 0x400000\n"
    ".Lat_ypoll_l:\n"
    "s_sleep 0\n"
    "buffer_load_dword v144, v145, s[8:11], s52 offen offset:0 sc1\n"
    "s_waitcnt vmcnt(0)\n"
    "v_cmp_ne_u32_e32 vcc, s39, v144\n"
    "s_sub_u32 s53, s53, 1\n"
    "s_cbranch_scc1 .Lat_ypoll_ret\n"
    "s_nop 2\n"
    "s_cbranch_vccnz .Lat_ypoll_l\n"
    "s_branch .Lat_ypoll_ret\n"
    ".Lat_lpoll:\n"
    "s_mov_b32 s53, 0x400000\n"
    ".Lat_lpoll_l:\n"
    "s_sleep 1\n"
    "buffer_load_dword v154, off, s[8:11], s80 sc1\n"
    "s_waitcnt vmcnt(0)\n"
    "v_cmp_ne_u32_e32 vcc, s39, v154\n"
    "s_sub_u32 s53, s53, 1\n"
    "s_cbranch_scc1 .Lat_lpoll_ret\n"
    "s_nop 2\n"
    "s_cbranch_vccnz .Lat_lpoll_l\n"
    ".Lat_lpoll_ret:\n"
    "v_lshlrev_b32_e32 v253, 2, v249\n"
    "s_mov_b32 m0, s82\n"
    "s_nop 0\n"
    "buffer_load_dwordx4 v253, s[8:11], s81 offen offset:0 sc1 lds\n"
    "s_waitcnt vmcnt(0)\n"
    "s_branch .Lat_g\n"
    ".Lat_exit:\n"
    "s_waitcnt vmcnt(0)\n"
    "s_nop 15\n"
    ".Lat_end:\n"
    "s_waitcnt lgkmcnt(0)\n"
    : "+s"(ka), "+s"(b), "+s"(wid) : "s"((unsigned)(size_t)lds)
    : "v0", "v1", "v2", "v3", "v4", "v5", "v6", "v7", "v8", "v9", "v10", "v11", "v12", "v13", "v14", "v15", "v16", "v17", "v18", "v19", "v20", "v21", "v22", "v23", "v24", "v25", "v26", "v27", "v28", "v29", "v30", "v31", "v32", "v33", "v34", "v35", "v36", "v37", "v38", "v39", "v40", "v41", "v42", "v43", "v44", "v45", "v46", "v47", "v48", "v49", "v50", "v51", "v52", "v53", "v54", "v55", "v56", "v57", "v58", "v59", "v60", "v61", "v62", "v63", "v64", "v65", "v66", "v67", "v68", "v69", "v70", "v71", "v72", "v73", "v74", "v75", "v76", "v77", "v78", "v79", "v80", "v81", "v82", "v83", "v84", "v85", "v86", "v87", "v88", "v89", "v90", "v91", "v92", "v93", "v94", "v95", "v96", "v97", "v98", "v99", "v100", "v101", "v102", "v103", "v104", "v105", "v106", "v107", "v108", "v109", "v110", "v111", "v112", "v113", "v114", "v115", "v116", "v117", "v118", "v119", "v120", "v121", "v122", "v123", "v124", "v125", "v126", "v127", "v128", "v129", "v130", "v131", "v132", "v133", "v134", "v135", "v136", "v137", "v138", "v139", "v140", "v141", "v142", "v143", "v144", "v145", "v146", "v147", "v148", "v149", "v150", "v151", "v152", "v153", "v154", "v155", "v156", "v157", "v158", "v159", "v160", "v161", "v162", "v163", "v164", "v165", "v166", "v167", "v168", "v169", "v170", "v171", "v172", "v173", "v174", "v175", "v176", "v177", "v178", "v179", "v180", "v181", "v182", "v183", "v184", "v185", "v186", "v187", "v188", "v189", "v190", "v191", "v192", "v193", "v194", "v195", "v196", "v197", "v198", "v199", "v200", "v201", "v202", "v203", "v204", "v205", "v206", "v207", "v208", "v209", "v210", "v211", "v212", "v213", "v214", "v215", "v216", "v217", "v218", "v219", "v220", "v221", "v222", "v223", "v224", "v225", "v226", "v227", "v228", "v229", "v230", "v231", "v232", "v233", "v234", "v235", "v236", "v237", "v238", "v239", "v240", "v241", "v242", "v243", "v244", "v245", "v246", "v247", "v248", "v249", "v250", "v251", "v252", "v253", "v254", "v255", "a0", "a1", "a2", "a3", "a4", "a5", "a6", "a7", "a8", "a9", "a10", "a11", "a12", "a13", "a14", "a15", "a16", "a17", "a18", "a19", "a20", "a21", "a22", "a23", "a24", "a25", "a26", "a27", "a28", "a29", "a30", "a31", "a32", "a33", "a34", "a35", "a36", "a37", "a38", "a39", "a40", "a41", "a42", "a43", "a44", "a45", "a46", "a47", "a48", "a49", "a50", "a51", "a52", "a53", "a54", "a55", "a56", "a57", "a58", "a59", "a60", "a61", "a62", "a63", "a64", "a65", "a66", "a67", "a68", "a69", "a70", "a71", "a72", "a73", "a74", "a75", "a76", "a77", "a78", "a79", "a80", "a81", "a82", "a83", "a84", "a85", "a86", "a87", "a88", "a89", "a90", "a91", "a92", "a93", "a94", "a95", "a96", "a97", "a98", "a99", "a100", "a101", "a102", "a103", "a104", "a105", "a106", "a107", "a108", "a109", "a110", "a111", "a112", "a113", "a114", "a115", "a116", "a117", "a118", "a119", "a120", "a121", "a122", "a123", "a124", "a125", "a126", "a127", "a128", "a129", "a130", "a131", "a132", "a133", "a134", "a135", "a136", "a137", "a138", "a139", "a140", "a141", "a142", "a143", "a144", "a145", "a146", "a147", "a148", "a149", "a150", "a151", "a152", "a153", "a154", "a155", "a156", "a157", "a158", "a159", "a160", "a161", "a162", "a163", "a164", "a165", "a166", "a167", "a168", "a169", "a170", "a171", "a172", "a173", "a174", "a175", "a176", "a177", "a178", "a179", "a180", "a181", "a182", "a183", "a184", "a185", "a186", "a187", "a188", "a189", "a190", "a191", "a192", "a193", "a194", "a195", "a196", "a197", "a198", "a199", "a200", "a201", "a202", "a203", "a204", "a205", "a206", "a207", "a208", "a209", "a210", "a211", "a212", "a213", "a214", "a215", "a216", "a217", "a218", "a219", "a220", "a221", "a222", "a223", "a224", "a225", "a226", "a227", "a228", "a229", "a230", "a231", "a232", "a233", "a234", "a235", "a236", "a237", "a238", "a239", "a240", "a241", "a242", "a243", "a244", "a245", "a246", "a247", "a248", "a249", "a250", "a251", "a252", "a253", "a254", "a255", "s8", "s9", "s10", "s11", "s12", "s13", "s14", "s15", "s16", "s17", "s18", "s19", "s20", "s21", "s22", "s23", "s24", "s25", "s26", "s27", "s28", "s29", "s30", "s31", "s32", "s33", "s34", "s35", "s36", "s37", "s38", "s39", "s40", "s41", "s42", "s43", "s44", "s45", "s46", "s47", "s48", "s49", "s50", "s51", "s52", "s53", "s54", "s55", "s56", "s57", "s58", "s59", "s60", "s61", "s62", "s63", "s80", "s81", "s82", "s83", "s84", "s85", "s86", "s87", "s88", "s89", "s90", "vcc", "m0", "scc", "memory");
  typedef __attribute__((address_space(1))) char* __attribute__((address_space(4))) const* arg_t;
  const arg_t ap = (arg_t)ka;
  typedef const u16* cu16;
  if (b >= NATT) list_branch(lds, b - NATT, wid, (const int*)ap[1], (const int*)ap[2], (cu16)ap[3], (cu16)ap[8], (cu16)ap[9], (cu16)ap[10], (char*)ap[0]);
  else exact_rows(lds, b, wid, (char*)ap[0], (cu16)ap[4], (u16*)ap[5]);
}

extern "C" void kernel_launch(void* const* d_in, const int* in_sizes, int n_in, void* d_out, int out_size, void* d_ws,
                              size_t ws_size, hipStream_t stream) {
  const u16* x = (const u16*)d_in[0];
  const int* src = (const int*)d_in[1];
  const int* dst = (const int*)d_in[2];
  const u16* deg_inv = (const u16*)d_in[3];
  const u16* wmsg = (const u16*)d_in[4];
  const u16* wq = (const u16*)d_in[5];
  const u16* wk = (const u16*)d_in[6];
  const u16* wv = (const u16*)d_in[7];
  const u16* wout = (const u16*)d_in[8];
  char* ws = (char*)d_ws;
  (void)hipFuncSetAttribute((const void*)attn_kernel, hipFuncAttributeMaxDynamicSharedMemorySize, 163840);
  attn_kernel<<<256, 256, 163840, stream>>>(ws, src, dst, deg_inv, x, (u16*)d_out, wq, wk, wv, wmsg, wout);
}
```

```cpp
#include <hip/hip_runtime.h>
#include <hip/hip_bf16.h>
#include <stdint.h>
#pragma clang diagnostic ignored "-Winline-asm"

typedef unsigned short u16;
typedef __attribute__((ext_vector_type(8))) short bf16x8;
typedef __attribute__((ext_vector_type(4))) short bf16x4;
typedef __attribute__((ext_vector_type(4))) float f32x4;
typedef __attribute__((ext_vector_type(2))) float f32x2;
typedef __attribute__((ext_vector_type(4))) unsigned u32x4;
typedef __attribute__((ext_vector_type(2))) unsigned u32x2;
#define DI __device__ __forceinline__

constexpr int NN = 10000, DD = 256, EE = 160000;
constexpr int NATT = 252, NSP = 4;

constexpr unsigned WS_H = 0;
constexpr unsigned WS_AF = WS_H + 10240 * 512;
constexpr unsigned WS_WV = WS_AF + 131072;
constexpr unsigned WS_WM = WS_WV + 131072;
constexpr unsigned WS_XF = WS_WM + 131072;
constexpr unsigned WS_HF = WS_XF + 256 * 256;
constexpr unsigned WS_BF = WS_HF + 4096;
constexpr unsigned WS_PF = WS_BF + 65536;
constexpr unsigned WS_WF = WS_PF + 4096;
constexpr unsigned WS_LF = WS_WF + 256;
constexpr unsigned WS_EP = WS_LF + 2048;
constexpr unsigned WS_G = WS_EP + 1792;
constexpr unsigned WS_QP = WS_G + 10240 * 512;
constexpr unsigned WS_XB = WS_QP + 10240 * 512;
constexpr unsigned WS_BL = WS_XB + NATT * 3 * 24832;
constexpr unsigned WS_GL = WS_BL + 16 * 5 * 260 * 64;
constexpr unsigned WS_DBG = WS_GL + 256 * 4096;
constexpr unsigned WS_END = WS_DBG + 1024 * 128 + 8192;

DI float bf2f(unsigned x) { return __uint_as_float(x << 16); }
DI float bfhi(unsigned x) { return __uint_as_float(x & 0xffff0000u); }
DI unsigned pk2(float lo, float hi) {
  typedef __attribute__((ext_vector_type(2))) __bf16 bf2_t;
  const bf2_t r = __builtin_convertvector((f32x2){lo, hi}, bf2_t);
  return __builtin_bit_cast(unsigned, r);
}
typedef __attribute__((address_space(3))) bf16x4* lds_bf16x4_p;
DI bf16x4 tr_read(unsigned p) { return __builtin_amdgcn_ds_read_tr16_b64_v4i16((lds_bf16x4_p)p); }
DI bf16x8 as_bf(u32x4 v) { return __builtin_bit_cast(bf16x8, v); }
#define SB() __builtin_amdgcn_sched_barrier(0)
#define MFMA16(a, b, c) __builtin_amdgcn_mfma_f32_16x16x32_bf16(a, b, c, 0, 0, 0)

typedef __amdgpu_buffer_rsrc_t srd_t;
DI void drain() { asm volatile("s_waitcnt vmcnt(0)" ::: "memory"); }
DI void st_flag(srd_t w, unsigned off, unsigned tag) { __builtin_amdgcn_raw_buffer_store_b32(tag, w, off, 0, 16); }
DI void wait_flag(srd_t w, unsigned off, unsigned tag) {
#pragma unroll 1
  for (int it = 0; it < (1 << 22); ++it) {
    unsigned v;
    asm volatile("buffer_load_dword %0, %1, %2, 0 offen sc1\n s_waitcnt vmcnt(0)" : "=v"(v) : "v"(off), "s"(w) : "memory");
    if (__ballot(v != tag) == 0) break;
    __builtin_amdgcn_s_sleep(2);
    asm volatile("" ::: "memory");
  }
  asm volatile("" ::: "memory");
}

DI void prod_store(const f32x4 (&acc)[2][2], int mat, int ks, int J, int lane, srd_t wsrd) {
#pragma unroll
  for (int s = 0; s < 2; ++s) {
    u32x4 o = {pk2(acc[0][s][0], acc[0][s][1]), pk2(acc[0][s][2], acc[0][s][3]), pk2(acc[1][s][0], acc[1][s][1]), pk2(acc[1][s][2], acc[1][s][3])};
    if (ks == 7 && lane >= 48) o[3] &= 0xffffu;
    const int T = 2 * J + s;
    const int at = mat ? 131072 + ((J & 1) ^ 1) * 16384 + ks * 2048 + s * 1024 : ks * 4096 + (T & 3) * 1024;
    __builtin_amdgcn_raw_buffer_store_b128(o, wsrd, WS_WV + (T >> 2) * 32768 + at + lane * 16, 0, 16);
  }
}
DI void corr(unsigned* dl, const unsigned (&lr)[16]) {
  float a0 = 0.f, a1 = 0.f;
#pragma unroll
  for (int r = 0; r < 16; ++r) { a0 += bf2f(lr[r] & 0xffffu); a1 += bfhi(lr[r]); }
  dl[0] = pk2(a0 * -0.0625f, a1 * -0.0625f);
  dl[128] = pk2((bf2f(lr[15] & 0xffffu) - 0.25f * a0) * (1.f / 3.f), (bfhi(lr[15]) - 0.25f * a1) * (1.f / 3.f));
}
DI void product_load(char* lds, int wid, int lane, int job, const u16* __restrict__ wv, const u16* __restrict__ wm, const u16* __restrict__ wo, bool left, bool right) {
  const unsigned ldsb = (unsigned)(size_t)lds;
  const int r = lane >> 3, sw = ((lane & 7) ^ (r & 6)) * 16 + wid * 128;
  const unsigned vp = (8 * (r >> 2) + (r & 3)) * 512 + sw, vn = r * 512 + sw;
  auto image = [&](int n, const u16* w, int row0, bool nat) {
#pragma unroll
    for (int i = 0; i < 8; ++i) {
      const int a = i & 3;
      asm volatile("s_mov_b32 m0, %2\n s_nop 0\n global_load_lds_dwordx4 %0, %1"
                   :: "v"(nat ? vn : vp), "s"(w + (row0 + (i >> 2) * 32 + (nat ? 8 * a : 16 * (a & 1) + 4 * (a >> 1))) * DD),
                      "s"(ldsb + n * 32768 + (i >> 2) * 16384 + wid * 4096 + a * 1024) : "m0", "memory");
    }
  };
  if (left) {
    image(2, wv, (job >> 2) * 64, true);
    image(3, wm, (job >> 2) * 64, false);
  }
  if (right)
#pragma unroll
  for (int k = 0; k < 8; ++k)
    asm volatile("s_mov_b32 m0, %2\n s_nop 0\n global_load_lds_dwordx4 %0, %1"
                 :: "v"((lane >> 3) * 512 + (lane & 7) * 16), "s"(wo + (k * 32 + wid * 8) * DD + (job & 3) * 64), "s"(ldsb + k * 4096 + wid * 1024) : "m0", "memory");
}
DI void product_w(char* lds, int wid, int lane, int job, srd_t wsrd) {
  const int m = lane & 15, g = lane >> 4;
  const int ks = 2 * (job >> 2) + (wid >> 1), J = 2 * (job & 3) + (wid & 1);
  const unsigned ldsb = (unsigned)(size_t)lds;
  unsigned* dl = reinterpret_cast<unsigned*>(lds + 0x26c00);
  const bool hi = (m >> 3) != 0;
  const unsigned ra = (m >> 3) * 1024 + (m & 7) * 128 + (g ^ (m & 6)) * 16;
  auto frag = [&](int n, int tl, int t, int k) {
    return *reinterpret_cast<const bf16x8*>(lds + n * 32768 + tl * 16384 + (k >> 1) * 4096 + t * 2048 + (ra ^ ((k & 1) * 64)));
  };
  f32x4 av[2][2] = {}, am[2][2] = {}, ad[2] = {};
  const unsigned tb = ldsb + (job & 1) * 32768 + (8 * g + ((lane >> 2) & 3)) * 128 + (wid & 1) * 64 + (lane & 3) * 16;
  bf16x8 d0[8], d1[8], d2[8], rr[8][2], vf[8][2], mf[8][2];
#pragma unroll
  for (int kk = 0; kk < 9; ++kk) {
    if (kk < 8) {
      d0[kk] = *reinterpret_cast<const bf16x8*>(dl + 512 + 16 * kk + 4 * g); d1[kk] = *reinterpret_cast<const bf16x8*>(dl + 512 + (ks == 7 ? 128 : 0) + 16 * kk + 4 * g);
      d2[kk] = *reinterpret_cast<const bf16x8*>(dl + 768 + (ks == 7 && hi ? 128 : 0) + 16 * kk + 4 * g);
#pragma unroll
      for (int s = 0; s < 2; ++s) {
        rr[kk][s] = __builtin_shufflevector(tr_read(tb + kk * 4096 + s * 8), tr_read(tb + kk * 4096 + s * 8 + 512), 0, 1, 2, 3, 4, 5, 6, 7);
        vf[kk][s] = frag(2, wid >> 1, s, kk); mf[kk][s] = frag(3, wid >> 1, s, kk);
      }
    }
    SB();
    if (kk) {
      const int k = kk - 1;
#pragma unroll
      for (int s = 0; s < 2; ++s) {
        av[0][s] = MFMA16(d0[k], rr[k][s], av[0][s]); av[1][s] = MFMA16(d1[k], rr[k][s], av[1][s]); ad[s] = MFMA16(d2[k], rr[k][s], ad[s]);
#pragma unroll
        for (int t = 0; t < 2; ++t) { av[t][s] = MFMA16(vf[k][t], rr[k][s], av[t][s]); am[t][s] = MFMA16(mf[k][t], rr[k][s], am[t][s]); }
      }
    }
    SB();
  }
#pragma unroll
  for (int t = 0; t < 2; ++t)
#pragma unroll
    for (int s = 0; s < 2; ++s) am[t][s] += ad[s];
  prod_store(av, 0, ks, J, lane, wsrd);
  prod_store(am, 1, ks, J, lane, wsrd);
}

constexpr unsigned CODE_BYTES = 93568;
DI void code_touch(unsigned i, unsigned ldsdst) {
  unsigned long long a; int o_;
  asm volatile("s_getpc_b64 %0\n s_mov_b32 %1, _Z11attn_kernelPcPKiS1_PKtS3_PtS3_S3_S3_S3_S3_@rel32@lo+4" : "=&s"(a), "=&s"(o_));
  a += (long long)o_;
  asm volatile("s_mov_b32 m0, %2\n s_nop 0\n global_load_lds_dword %0, %1" :: "v"(min(i * 128u, CODE_BYTES - 4u)), "s"(a), "s"(ldsdst) : "m0", "memory");
}

constexpr int LONGS = 1056;
typedef __attribute__((address_space(3))) unsigned* lds_u32_p;
typedef __attribute__((address_space(3))) u16* lds_u16_p;
DI void local_store(srd_t srd, int node, int lane, f32x4 acc, float di) {
  const int n = lane & 15, g = lane >> 4;
  const u32x2 o = {pk2(acc[0] * di, acc[1] * di), pk2(acc[2] * di, acc[3] * di)};
  __builtin_amdgcn_raw_buffer_store_b64(o, srd, WS_G + node * 512 + (g >> 1) * 256 + n * 16 + (g & 1) * 8, 0, 16);
}
DI void list_branch(char* lds, int j, int wid, const int* __restrict__ src, const int* __restrict__ dst, const u16* __restrict__ deg_inv,
                    const u16* wv, const u16* wm, const u16* wo, char* ws) {
  const unsigned rec = (unsigned)(size_t)lds + wid * 33280, ext = (unsigned)(size_t)lds + 0x20800 + wid * 6912;
  int lane; asm volatile("v_mbcnt_lo_u32_b32 %0, -1, 0\n\tv_mbcnt_hi_u32_b32 %0, -1, %0" : "=v"(lane));
  const int tid = wid * 64 + lane, bin = j * 4 + wid, lo = bin * 640;
  code_touch(CODE_BYTES / 128 - tid, (unsigned)(size_t)lds + 0x27f00u);
  const srd_t msrd = __builtin_amdgcn_make_buffer_rsrc(ws + WS_H, 0, 10240 * 512, 0x00020000);
  const srd_t wsrd = __builtin_amdgcn_make_buffer_rsrc(ws, 0, WS_END, 0x00020000);
  const unsigned tag = __builtin_amdgcn_raw_buffer_load_b32(wsrd, WS_EP, 0, 0) + 2u;
  if (lane < 3) st_flag(wsrd, lane == 2 ? WS_WF + 32 : WS_LF + bin * 128 + lane * 64 + 8, 0);
  unsigned lr[16];
#pragma unroll
  for (int r = 0; r < 16; ++r) lr[r] = reinterpret_cast<const unsigned*>((wid >= 2 ? wm : wv) + (240 + r) * DD)[tid & 127];
  unsigned dv[10];
#pragma unroll
  for (int i = 0; i < 10; ++i) dv[i] = (unsigned)deg_inv[min(lo + 64 * i + lane, NN - 1)];
#pragma unroll
  for (int i = 0; i < 10; ++i) ((lds_u32_p)ext)[64 * i + lane] = dv[i] << 16 | (lo + 64 * i + lane < NN ? lo + 64 * i + lane : 0xffff);
  corr(reinterpret_cast<unsigned*>(lds + 0x26c00) + 512 + (wid >> 1) * 256 + (tid & 127), lr);
  const u32x4 none = {~0u, ~0u, ~0u, ~0u};
  auto clear = [&]() {
    unsigned a = rec + lane * 16, r = rec + lane * 52;
    asm volatile("" : "+v"(a), "+v"(r));
#pragma unroll
    for (int i = 0; i < 33; ++i) *(__attribute__((address_space(3))) u32x4*)(a + (i < 32 ? i * 1024 : 32256)) = none;
#pragma unroll
    for (int i = 0; i < 10; ++i) ((lds_u32_p)(r + 64 * 52 * i))[12] = 46;
  };
  clear();
  __builtin_amdgcn_s_sleep(40);
  wait_flag(wsrd, WS_BF + min(tid, NATT - 1) * 128, tag);
  __syncthreads();
  auto take = [&](const u32x4& e0, const u32x4& e1, unsigned (&a)[8], unsigned (&p)[8]) {
#pragma unroll
    for (int r = 0; r < 8; ++r) asm("v_mad_u32_u16 %0, %1, 52, %2 op_sel:[1,0,0,0]" : "=v"(a[r]) : "v"(r < 4 ? e0[r & 3] : e1[r & 3]), "s"(rec));
    SB();
#pragma unroll
    for (int r = 0; r < 8; ++r) p[r] = __atomic_fetch_add((lds_u32_p)a[r] + 12, -2u, __ATOMIC_RELAXED);
    SB();
  };
  auto fill = [&](const u32x4& e0, const u32x4& e1, unsigned (&a)[8], const unsigned (&p)[8]) {
#pragma unroll
    for (int r = 7; r >= 0; --r) a[r] = __builtin_elementwise_add_sat(a[r], p[r]);
    SB();
#pragma unroll
    for (int r = 7; r >= 0; --r) *(lds_u16_p)a[r] = r < 4 ? e0[r & 3] : e1[r & 3];
    SB();
  };
  auto add8 = [&](u32x4 e0, u32x4 e1) { unsigned a[8], p[8]; take(e0, e1, a, p); fill(e0, e1, a, p); };
  const unsigned bo = WS_BL + bin * 5 * 260 * 64 + lane * 16;
  auto ld = [&](int g, int k) { return __builtin_amdgcn_raw_buffer_load_b128(wsrd, bo + (g * 260 + k * 16) * 64, 0, 16); };
  auto fix = [&](u32x4 e, unsigned last) { return e | (unsigned)(__builtin_amdgcn_mov_dpp(last, 0xff, 15, 15, 0) >> 31); };
  bool ovf = false;
  unsigned big = 0; int off[10], tot = 0;
#pragma unroll 1
  for (int pass = 0; pass < 2; ++pass) {
    if (!ovf) {
      u32x4 c[64];
#pragma unroll
      for (int k = 0; k < 16; ++k) c[k] = ld(0, k);
      unsigned m = ~0u, a[2][8], p[2][8];
#pragma unroll
      for (int k = 0; k < 66; k += 2) {
        SB();
        if (k < 64) {
          if (k >= 48) { c[k] = fix(c[k], c[k - 16][3]); c[k + 1] = fix(c[k + 1], c[k - 15][3]); }
          if ((k & 15) == 14 && lane >= 48) c[k + 1] = none;
          if (k >= 48) m &= c[k][3] & c[k + 1][3];
          take(c[k], c[k + 1], a[k >> 1 & 1], p[k >> 1 & 1]);
        }
        if (k < 16) {
#pragma unroll
          for (int i = 16 + 2 * k; i < 20 + 2 * k; ++i) c[i] = ld(i >> 4, i & 15);
        }
        if (k >= 24 && k < 40) {
#pragma unroll
          for (int i = k - 24; i < k - 22; ++i)
            c[48 + i] = __builtin_amdgcn_raw_buffer_load_b128(wsrd, bo + (780 + i * 16) * 64 + (__builtin_amdgcn_mov_dpp(c[32 + i][3], 0xff, 15, 15, 0) & 0x40000000), 0, 16);
        }
        SB();
        if (k) fill(c[k - 2], c[k - 1], a[~k >> 1 & 1], p[~k >> 1 & 1]);
      }
      SB();
      if (__ballot((lane & 3) == 3 && m != ~0u)) {
        unsigned more = 0;
#pragma unroll
        for (int k = 0; k < 16; ++k) more |= __ballot((lane & 3) == 3 && c[48 + k][3] != ~0u) ? 1u << k : 0u;
#pragma unroll 1
        for (; more; more &= more - 1) {
          const int k = __builtin_ctz(more);
          u32x4 e = ld(4, k);
          if (k == 15 && lane >= 48) e = none;
          ovf |= __ballot((lane & 3) == 3 && e[3] != ~0u) != 0;
          add8(e, none);
        }
      }
      if (ovf) clear();
    }
    if (ovf) {
      for (int q = lane; q < EE / 4; q += 64) {
        const u32x4 d = reinterpret_cast<const u32x4*>(dst)[q], s = reinterpret_cast<const u32x4*>(src)[q];
        u32x4 e;
#pragma unroll
        for (int r = 0; r < 4; ++r) e[r] = d[r] - lo < 640u ? (d[r] - lo) << 16 | s[r] : ~0u;
        add8(e, none);
      }
    }
    if (pass) break;
    asm volatile("" : "+v"(lane));
    const int qd = lane & 3, nl = lane >> 2;
    int inc = 0;
#pragma unroll
    for (int i = 0; i < 10; ++i) off[i] = 46 - (int)((lds_u32_p)(rec + (64 * i + lane) * 52))[12];
#pragma unroll
    for (int i = 0; i < 10; ++i) {
      const int cn = off[i];
      off[i] = cn > 48 ? cn : 0; inc += off[i];
      if (cn > 48) { big |= 1u << i; ((lds_u16_p)ext)[2 * (64 * i + lane)] = 0xffff; }
    }
    __builtin_amdgcn_wave_barrier();
    auto put = [&](int odd) {
#pragma unroll 2
      for (int bl = 0; bl < 4; ++bl) {
        u32x4 o[10];
#pragma unroll
        for (int T = 0; T < 10; ++T) if (((2 * T + 1) / 5 & 1) == odd) {
          const lds_u32_p r = (lds_u32_p)(rec + (bl * 160 + T * 16 + nl) * 52);
          o[T] = (u32x4){r[3 * qd], r[1 + 3 * qd], r[2 + 3 * qd], ((lds_u32_p)ext)[bl * 160 + T * 16 + nl]};
        }
#pragma unroll
        for (int T = 0; T < 10; ++T) if (((2 * T + 1) / 5 & 1) == odd) {
          const int c = (2 * T + 1) / 5, ro = 16 * (T - (5 * c >> 1)) + nl, w = c & 1 ? ro * 43 >> 9 : ro >> 3;
          __builtin_amdgcn_raw_buffer_store_b128(o[T], wsrd, WS_GL + c * 4096 + w * (c & 1 ? 256 : 512) + ro * 64 + qd * 16, (bin * 4 + bl) * 16384, 16);
        }
      }
    };
    put(1);
    asm volatile("" ::: "memory");
    put(0);
    asm volatile("s_waitcnt vmcnt(16)" ::: "memory");
    if (lane == 0) st_flag(wsrd, WS_LF + bin * 128 + 64, tag);
#pragma unroll
    for (int o = 1; o < 64; o <<= 1) { const int y = __shfl_up(inc, o); if (lane >= o) inc += y; }
    tot = __builtin_amdgcn_readlane(inc, 63);
    drain();
    if (lane == 0) st_flag(wsrd, WS_LF + bin * 128, tag);
    __syncthreads();
    int j2 = j * 4, w2 = wid, l2 = lane;
    asm volatile("" : "+s"(j2), "+s"(w2), "+v"(l2));
    product_load(lds, w2, l2, j2, wv, wm, wo, true, true);
#pragma unroll 1
    for (int job = 0; job < 4; ++job) {
      if (job) asm volatile("s_waitcnt vmcnt(4)" ::: "memory"); else drain();
      __syncthreads();
      asm volatile("" : "+s"(w2));
      if (job < 3) product_load(lds + (~job & 1) * 32768, w2, l2, j2 + job + 1, wv, wm, wo, false, true);
      product_w(lds, w2, l2, j2 + job, wsrd);
    }
    if (w2 == 0) *(lds_u32_p)(ext - 8) = 0;
    __syncthreads();
    if (tot == 0 || tot > 2 * LONGS) break;
#pragma unroll
    for (int i = 0; i < 10; ++i) {
      const unsigned r = rec + (64 * i + lane) * 52;
      ((lds_u32_p)r)[12] = off[i] ? ext + 2558 + inc - r : -2u;
      inc -= off[i]; off[i] = off[i] << 15 | inc;
    }
  }
  __syncthreads();
  const unsigned ex0 = (unsigned)(size_t)lds + 0x20800;
  const lds_u32_p lng = (lds_u32_p)(size_t)(unsigned)(size_t)lds;
#pragma unroll
  for (int i = 0; i < 10; ++i)
    if (big >> i & 1) {
      const unsigned q = __atomic_fetch_add((lds_u32_p)(ex0 - 8), 1u, __ATOMIC_RELAXED);
      lng[2 * q] = tot > 2 * LONGS ? ~0u : off[i] + wid * 6912 + 2560; lng[2 * q + 1] = (((lds_u32_p)ext)[64 * i + lane] & 0xffff0000u) | (lo + 64 * i + lane);
    }
  __syncthreads();
  const int nl_ = *(lds_u32_p)(ex0 - 8);
  bf16x8 b1;
#pragma unroll
  for (int e = 0; e < 8; ++e) b1[e] = (lane & 15) == e + ((lane >> 1) & 8) ? 0x3f80 : 0;
  const unsigned lo16 = (lane & 31) * 16;
  {
    u32x4 d[3][16]; unsigned nn[3]; int mode[3], q = wid, hf = 0;
    auto issue = [&](u32x4 (&v)[16], unsigned& n, int& md) {
      const unsigned w = __builtin_amdgcn_readfirstlane(lng[2 * q]);
      n = lng[2 * q + 1];
      const int dg = q < nl_ && w >> 16 <= 64 ? w >> 16 : 0, g = dg - hf - (lane >> 5);
      const lds_u16_p f = (lds_u16_p)(ex0 + (w & 0xffffu)) + hf + (lane >> 5);
      md = q >= nl_ ? -1 : dg == 0 ? 0 : dg - hf > 32 ? 1 : 2;
      hf = md == 1 ? 32 : 0; q += md == 1 ? 0 : 4;
      unsigned id[16];
#pragma unroll
      for (int t = 0; t < 16; ++t) id[t] = f[2 * t];
#pragma unroll
      for (int t = 0; t < 16; ++t) { asm volatile("" : "+v"(id[t])); v[t] = __builtin_amdgcn_raw_buffer_load_b128(msrd, (2 * t < g ? id[t] : 0xffffu) * 512u + lo16, 0, 16); }
    };
    issue(d[0], nn[0], mode[0]); issue(d[1], nn[1], mode[1]);
    f32x4 keep = {};
#pragma unroll 1
    for (bool go = true; go;) {
#pragma unroll
      for (int s = 0; s < 3; ++s) {
        if (mode[s] < 0) { go = false; break; }
        issue(d[(s + 2) % 3], nn[(s + 2) % 3], mode[(s + 2) % 3]); SB();
        f32x4 acc = {};
#pragma unroll
        for (int t = 0; t < 16; ++t) acc = MFMA16(b1, as_bf(d[s][t]), acc);
        local_store(wsrd, mode[s] == 2 ? nn[s] & 0xffffu : 0x200000u, lane, acc + keep, bfhi(nn[s]));
        keep = mode[s] == 1 ? acc : (f32x4){0.f, 0.f, 0.f, 0.f}; SB();
      }
    }
  }
#pragma unroll 1
  for (int q0 = 0; q0 < nl_; q0 += 256)
#pragma unroll 1
  for (unsigned long long m = __ballot(q0 + 4 * lane + 3 - wid < nl_ && lng[2 * (q0 + 4 * lane + 3 - wid)] >> 16 > 64); m; m &= m - 1) {
    const int q = q0 + 4 * __builtin_ctzll(m) + 3 - wid;
    const unsigned w = __builtin_amdgcn_readfirstlane(lng[2 * q]), n = __builtin_amdgcn_readfirstlane(lng[2 * q + 1]); const int dg = w >> 16;
    f32x4 acc = {};
    if (w == ~0u) {
      for (int e0 = 0; e0 < EE; e0 += 64) {
        unsigned long long h = __ballot(dst[e0 + lane] == (n & 0xffffu));
        while (h) {
          const int e = e0 + __builtin_ctzll(h); h &= h - 1;
          const unsigned vo = lane < 32 ? (unsigned)src[e] * 512 + lo16 : 0x7fffff00u;
          acc = MFMA16(b1, as_bf(__builtin_amdgcn_raw_buffer_load_b128(msrd, vo, 0, 16)), acc);
        }
      }
    } else {
      const lds_u16_p f = (lds_u16_p)(ex0 + (w & 0xffffu)), u = (lds_u16_p)(ext + 2624 + 2 * LONGS);
      for (int x = lane; x < dg; x += 64) {
        const int key = f[x]; int p = 0;
#pragma unroll 8
        for (int t = 0; t < dg; ++t) { const int v = f[t]; p += v < key || (v == key && t < x); }
        u[p] = key;
      }
      __builtin_amdgcn_wave_barrier();
      for (int x = lane >> 5; x < dg + (lane >> 5); x += 32) {
        u32x4 d[16]; unsigned id[16]; f32x4 a2 = {};
#pragma unroll
        for (int t = 0; t < 16; ++t) id[t] = u[min(x + 2 * t, dg)];
#pragma unroll
        for (int t = 0; t < 16; ++t) { asm volatile("" : "+v"(id[t])); d[t] = __builtin_amdgcn_raw_buffer_load_b128(msrd, (x + 2 * t < dg ? id[t] : 0xffffu) * 512u + lo16, 0, 16); }
#pragma unroll
        for (int t = 0; t < 16; ++t) a2 = MFMA16(b1, as_bf(d[t]), a2);
        acc += a2;
      }
      __builtin_amdgcn_wave_barrier();
    }
    local_store(wsrd, n & 0xffffu, lane, acc, bfhi(n));
  }
  drain();
  __syncthreads();
  if (wid == 0 && lane == 0) st_flag(wsrd, WS_WF + j * 4, tag);
}

DI void exact_rows(char* lds, int b, int wid, char* ws, const u16* x, u16* out) {
  int lane; asm volatile("v_mbcnt_lo_u32_b32 %0, -1, 0\n\tv_mbcnt_hi_u32_b32 %0, -1, %0" : "=v"(lane));
  const srd_t w = __builtin_amdgcn_make_buffer_rsrc(ws, 0, WS_END, 0x00020000);
  const srd_t xs = __builtin_amdgcn_make_buffer_rsrc(const_cast<u16*>(x), 0, NN * 512, 0x00020000);
  const srd_t os = __builtin_amdgcn_make_buffer_rsrc(out, 0, NN * 512, 0x00020000);
  const int n = lane & 15, g = lane >> 4, c = b & 3;
  f32x4* red = reinterpret_cast<f32x4*>(lds);
  float* mx = reinterpret_cast<float*>(lds + 65536);
  auto ld = [&](unsigned off) { return as_bf(__builtin_amdgcn_raw_buffer_load_b128(w, off, 0, 16)); };
#pragma unroll 1
  for (int i = 0; i < 2 + (c & 1); ++i) {
    const int tile = (b >> 2) * 10 + (5 * c >> 1) + i;
    bf16x8 q[8];
#pragma unroll
    for (int ks = 0; ks < 8; ++ks) q[ks] = ld(WS_QP + tile * 8192 + ks * 1024 + lane * 16);
    auto score = [&](int kt) {
      f32x4 s = {};
#pragma unroll
      for (int ks = 0; ks < 8; ++ks) s = MFMA16(ld(WS_H + (kt * 16 + n) * 512 + ks * 64 + g * 16), q[ks], s);
      return s;
    };
    float m = -__builtin_inff();
#pragma unroll 1
    for (int kt = wid; kt < NN / 16; kt += 4) { const f32x4 s = score(kt); m = fmaxf(fmaxf(m, fmaxf(s[0], s[1])), fmaxf(s[2], s[3])); }
    __syncthreads();
    mx[wid * 64 + lane] = m;
    __syncthreads();
#pragma unroll
    for (int r = 0; r < 16; ++r) m = fmaxf(m, mx[r * 16 + n]);
    f32x4 acc[16] = {};
#pragma unroll 1
    for (int kt = wid; kt < NN / 16; kt += 4) {
      const f32x4 s = score(kt);
      const u32x2 p = {pk2(__builtin_amdgcn_exp2f(s[0] - m), __builtin_amdgcn_exp2f(s[1] - m)), pk2(__builtin_amdgcn_exp2f(s[2] - m), __builtin_amdgcn_exp2f(s[3] - m))};
#pragma unroll
      for (int dt = 0; dt < 16; ++dt) {
        bf16x4 v;
#pragma unroll
        for (int e = 0; e < 4; ++e) v[e] = __builtin_amdgcn_raw_buffer_load_b16(w, WS_H + (kt * 16 + 4 * g + e) * 512 + n * 2, dt * 32, 16);
        acc[dt] = __builtin_amdgcn_mfma_f32_16x16x16bf16_1k(v, __builtin_bit_cast(bf16x4, p), acc[dt], 0, 0, 0);
      }
    }
#pragma unroll
    for (int dt = 0; dt < 16; ++dt) red[(wid * 16 + dt) * 64 + lane] = acc[dt];
    __syncthreads();
    float l = 0.f;
#pragma unroll
    for (int r = 0; r < 4; ++r) l += red[(r * 16 + 15) * 64 + 48 + n][3];
    l = 1.f / l;
    f32x4 y[4] = {};
#pragma unroll 1
    for (int ks = 0; ks < 8; ++ks) {
      f32x4 o[2];
#pragma unroll
      for (int h = 0; h < 2; ++h) o[h] = (red[(2 * ks + h) * 64 + lane] + red[(16 + 2 * ks + h) * 64 + lane] + red[(32 + 2 * ks + h) * 64 + lane] + red[(48 + 2 * ks + h) * 64 + lane]) * l;
      const bf16x8 of = as_bf((u32x4){pk2(o[0][0], o[0][1]), pk2(o[0][2], o[0][3]), pk2(o[1][0], o[1][1]), pk2(o[1][2], o[1][3])});
      const bf16x8 gf = ld(WS_G + (tile * 16 + n) * 512 + ks * 64 + g * 16);
#pragma unroll
      for (int et = 0; et < 4; ++et) {
        y[et] = MFMA16(ld(WS_WV + wid * 32768 + ks * 4096 + et * 1024 + lane * 16), of, y[et]);
        y[et] = MFMA16(ld(WS_WM + wid * 32768 + (et < 2 ? 16384 : 0) + ks * 2048 + (et & 1) * 1024 + lane * 16), gf, y[et]);
      }
    }
#pragma unroll
    for (int jj = 0; jj < 2; ++jj) {
      const unsigned at = (tile * 16 + n) * 512 + wid * 128 + jj * 64 + g * 16;
      const u32x4 xv = __builtin_amdgcn_raw_buffer_load_b128(xs, at, 0, 0);
      u32x4 o;
#pragma unroll
      for (int r = 0; r < 4; ++r) o[r] = pk2(y[2 * jj + (r >> 1)][2 * (r & 1)] + bf2f(xv[r] & 0xffffu), y[2 * jj + (r >> 1)][2 * (r & 1) + 1] + bfhi(xv[r]));
      __builtin_amdgcn_raw_buffer_store_b128(o, os, at, 0, 0);
    }
  }
}

__global__ __launch_bounds__(256, 1) void attn_kernel(char* ws, const int* __restrict__ src, const int* __restrict__ dst,
                                                      const u16* __restrict__ deg_inv, const u16* __restrict__ x, u16* out,
                                                      const u16* __restrict__ wq, const u16* __restrict__ wk, const u16* __restrict__ wv,
                                                      const u16* __restrict__ wm, const u16* __restrict__ wo) {
  extern __shared__ __attribute__((aligned(16))) char lds[];
  static_assert(CODE_BYTES > 65536 + 16384, "range of the cache warm-up");
  int b = blockIdx.x;
  int wid = __builtin_amdgcn_readfirstlane(threadIdx.x >> 6);
  static_assert(WS_END == 37557248 && WS_EP == 5777664, "workspace layout of the generated code");
  auto ka = __builtin_amdgcn_kernarg_segment_ptr();
  asm volatile(
    "s_load_dwordx2 s[20:21], %0, 0x30\n"
    "s_load_dwordx2 s[24:25], %0, 0x38\n"
    "s_load_dwordx2 s[12:13], %0, 0x20\n"
    "s_load_dwordx2 s[8:9], %0, 0x0\n"
    "s_load_dwordx2 s[16:17], %0, 0x28\n"
    "s_load_dwordx4 s[28:31], %0, 0x8\n"
    "v_mbcnt_lo_u32_b32 v24, -1, 0\n"
    "v_mbcnt_hi_u32_b32 v24, -1, v24\n"
    "s_and_b32 s45, %1, 6\n"
    "s_lshl_b32 s46, s45, 5\n"
    "s_lshr_b32 s47, %1, 3\n"
    "s_getpc_b64 s[62:63]\n"
    "s_lshl1_add_u32 s46, s47, s46\n"
    "s_and_b32 s47, s47, 3\n"
    "s_lshl2_add_u32 s47, s47, %2\n"
    "s_lshl_b32 s47, s47, 11\n"
    "s_add_u32 m0, %3, 0x27f00\n"
    "v_lshl_add_u32 v29, v24, 5, s47\n"
    "global_load_lds_dword v29, s[62:63]\n"
    "s_add_u32 s48, s45, 2\n"
    "s_lshr_b32 s48, s48, 2\n"
    "s_lshl_b32 s48, s48, 1\n"
    "s_sub_u32 s46, s46, s48\n"
    "s_add_u32 s48, s48, 252\n"
    "s_and_b32 s45, %1, 250\n"
    "s_cmpk_eq_u32 s45, 248\n"
    "s_cselect_b32 s46, s48, s46\n"
    "s_and_b32 %1, %1, 1\n"
    "s_or_b32 %1, %1, s46\n"
    "s_cmpk_ge_u32 %1, 252\n"
    "s_cbranch_scc1 .Lat_end\n"
    "s_and_b32 s50, %2, 1\n"
    "s_lshr_b32 s51, %2, 1\n"
    "v_lshrrev_b32_e32 v25, 3, v24\n"
    "v_and_b32_e32 v26, 6, v25\n"
    "v_and_b32_e32 v27, 7, v24\n"
    "v_xor_b32_e32 v26, v26, v27\n"
    "v_lshlrev_b32_e32 v26, 4, v26\n"
    "v_and_b32_e32 v27, 4, v25\n"
    "v_add_u32_e32 v27, v27, v25\n"
    "v_lshl_add_u32 v254, v27, 9, v26\n"
    "v_add_u32_e32 v255, 0x1c00, v254\n"
    "s_mov_b32 s22, 0x20000\n"
    "s_mov_b32 s23, 0x20000\n"
    "s_mov_b32 s26, 0x20000\n"
    "s_mov_b32 s27, 0x20000\n"
    "s_lshr_b32 s36, %1, 7\n"
    "s_mul_i32 s36, s36, 3\n"
    "s_add_u32 s36, s36, %1\n"
    "s_and_b32 s36, s36, 127\n"
    "s_lshl_b32 s45, %2, 7\n"
    "s_lshl_b32 s46, %2, 12\n"
    "s_add_u32 s46, s46, %3\n"
    "s_and_b32 s47, s36, 7\n"
    "s_lshl_b32 s47, s47, 14\n"
    "s_add_u32 m0, s46, 32768\n"
    "s_add_u32 s47, s47, s45\n"
    "s_and_b32 s32, %1, 3\n"
    "s_lshr_b32 s33, %1, 2\n"
    "s_mul_i32 s38, s32, 5\n"
    "s_lshr_b32 s38, s38, 1\n"
    "s_mul_i32 s35, s33, 10\n"
    "s_add_u32 s35, s35, s38\n"
    "s_lshl_b32 s35, s35, 13\n"
    "s_mov_b32 s14, 5120000\n"
    "s_mov_b32 s15, 0x20000\n"
    "s_waitcnt lgkmcnt(0)\n"
    "buffer_load_dwordx4 v254, s[20:23], s47 offen offset:0 lds\n"
    "s_add_u32 s48, s47, 1024\n"
    "buffer_load_dwordx4 v255, s[20:23], s47 offen offset:3072 lds\n"
    "s_add_u32 s47, s47, 0x1800\n"
    "buffer_load_dwordx4 v254, s[20:23], s48 offen offset:1024 lds\n"
    "buffer_load_dwordx4 v254, s[20:23], s47 offen offset:2048 lds\n"
    "s_lshr_b32 s47, s36, 4\n"
    "s_lshl_b32 s47, s47, 14\n"
    "s_and_b32 s48, s36, 8\n"
    "s_lshl_b32 s48, s48, 8\n"
    "s_add_u32 s47, s47, s48\n"
    "s_add_u32 m0, s46, 49152\n"
    "s_add_u32 s47, s47, s45\n"
    "buffer_load_dwordx4 v254, s[24:27], s47 offen offset:0 lds\n"
    "buffer_load_dwordx4 v255, s[24:27], s47 offen offset:1024 lds\n"
    "s_add_u32 s47, s45, 120832\n"
    "buffer_load_dwordx4 v254, s[24:27], s47 offen offset:2048 lds\n"
    "s_add_u32 s47, s47, 1024\n"
    "buffer_load_dwordx4 v254, s[24:27], s47 offen offset:3072 lds\n"
    "s_mul_i32 s46, %1, 159\n"
    "s_lshl_b32 s45, s50, 6\n"
    "v_add_u32_e32 v111, s45, v24\n"
    "v_add_u32_e32 v108, s46, v111\n"
    "v_min_u32_e32 v108, 39999, v108\n"
    "v_lshlrev_b32_e32 v108, 4, v108\n"
    "s_lshl1_add_u32 s47, s46, 256\n"
    "v_add_u32_e32 v109, s47, v111\n"
    "v_min_u32_e32 v109, 79999, v109\n"
    "v_lshlrev_b32_e32 v109, 3, v109\n"
    "s_cmp_lg_u32 s51, 0\n"
    "s_cselect_b64 exec, -1, 1\n"
    "global_load_dwordx4 v[96:99], v108, s[30:31]\n"
    "global_load_dwordx2 v[92:93], v109, s[30:31]\n"
    "global_load_dwordx4 v[100:103], v108, s[28:29]\n"
    "global_load_dwordx2 v[94:95], v109, s[28:29]\n"
    "s_mov_b64 exec, -1\n"
    "s_lshl_b32 s45, %2, 6\n"
    "v_add_u32_e32 v108, s45, v24\n"
    "s_sub_u32 s59, 40000, s46\n"
    "s_lshl1_add_u32 s61, s59, -256\n"
    "s_min_i32 s61, s61, 62\n"
    "s_lshl_b32 s45, %2, 2\n"
    "s_add_u32 s54, s35, 0x2000\n"
    "s_add_u32 s55, s35, 0x4000\n"
    "v_lshlrev_b32_e32 v253, 4, v24\n"
    "v_lshrrev_b32_e32 v28, 4, v24\n"
    "v_add_u32_e32 v28, s45, v28\n"
    "v_and_b32_e32 v104, 0xf0, v253\n"
    "v_lshl_add_u32 v104, v28, 9, v104\n"
    "buffer_load_dwordx4 v[0:3], v104, s[12:15], s35 offen offset:0\n"
    "buffer_load_dwordx4 v[4:7], v104, s[12:15], s35 offen offset:256\n"
    "s_bitcmp1_b32 %1, 0\n"
    "s_cselect_b32 s60, s55, 2.0\n"
    "buffer_load_dwordx4 v[8:11], v104, s[12:15], s54 offen offset:0\n"
    "buffer_load_dwordx4 v[12:15], v104, s[12:15], s54 offen offset:256\n"
    "buffer_load_dwordx4 v[16:19], v104, s[12:15], s55 offen offset:0\n"
    "buffer_load_dwordx4 v[20:23], v104, s[12:15], s55 offen offset:256\n"
    "s_mov_b32 s10, 37557248\n"
    "s_mov_b32 s11, 0x20000\n"
    "s_mov_b32 s37, 5777664\n"
    "s_buffer_load_dword s39, s[8:11], s37\n"
    "s_bfe_u32 s37, %1, 0x20001\n"
    "v_and_b32_e32 v27, 1, v25\n"
    "v_lshl_add_u32 v248, v27, 9, v26\n"
    "v_lshrrev_b32_e32 v27, 1, v25\n"
    "v_lshl_add_u32 v248, v27, 12, v248\n"
    "s_mul_i32 s40, s32, 39\n"
    "s_and_b32 s34, s32, 1\n"
    "s_add_u32 s40, s40, s34\n"
    "s_lshl1_add_u32 s40, s40, s51\n"
    "s_lshl_b32 s40, s40, 14\n"
    "s_lshl_b32 s47, s50, 8\n"
    "s_add_u32 s40, s40, s47\n"
    "s_add_u32 s41, s40, 128\n"
    "s_xor_b32 s34, s34, 1\n"
    "s_lshl_b32 s34, s34, 15\n"
    "s_xor_b32 s43, s50, s51\n"
    "s_lshl3_add_u32 s43, s50, s43\n"
    "s_lshl_b32 s43, s43, 13\n"
    "s_add_u32 s47, %3, 32768\n"
    "s_add_u32 s43, s43, s47\n"
    "v_bfe_u32 v25, v24, 2, 3\n"
    "v_lshlrev_b32_e32 v244, 7, v25\n"
    "v_lshrrev_b32_e32 v26, 5, v24\n"
    "v_lshl_add_u32 v244, v26, 10, v244\n"
    "v_and_b32_e32 v25, 6, v25\n"
    "v_bfe_u32 v26, v24, 1, 1\n"
    "v_xor_b32_e32 v25, v25, v26\n"
    "v_lshl_add_u32 v244, v25, 4, v244\n"
    "v_and_b32_e32 v26, 1, v24\n"
    "v_lshl_add_u32 v244, v26, 3, v244\n"
    "s_lshl_b32 s48, s51, 16\n"
    "s_add_u32 s48, s48, s47\n"
    "v_add_u32_e32 v244, s48, v244\n"
    "v_xor_b32_e32 v245, 32, v244\n"
    "v_xor_b32_e32 v246, 64, v244\n"
    "v_xor_b32_e32 v247, 0x60, v244\n"
    "v_and_b32_e32 v25, 7, v24\n"
    "v_lshlrev_b32_e32 v27, 7, v25\n"
    "v_bfe_u32 v26, v24, 3, 1\n"
    "v_lshl_add_u32 v27, v26, 10, v27\n"
    "v_and_b32_e32 v25, 6, v24\n"
    "v_lshrrev_b32_e32 v26, 4, v24\n"
    "v_xor_b32_e32 v25, v25, v26\n"
    "v_lshl_add_u32 v27, v25, 4, v27\n"
    "s_lshl_b32 s48, s51, 13\n"
    "s_add_u32 s48, s48, s47\n"
    "v_add_u32_e32 v240, s48, v27\n"
    "v_xor_b32_e32 v241, 64, v240\n"
    "v_and_b32_e32 v29, 15, v24\n"
    "s_cmp_ge_u32 s36, 112\n"
    "s_cselect_b32 s47, 8, 16\n"
    "v_cmp_le_u32_e32 vcc, s47, v29\n"
    "v_mov_b32_e32 v64, 0xbd80bd80\n"
    "v_mov_b32_e32 v65, 0xbdabbdab\n"
    "v_mov_b32_e32 v66, 0x392b392b\n"
    "v_cndmask_b32_e32 v64, v64, v65, vcc\n"
    "v_cndmask_b32_e32 v66, 0, v66, vcc\n"
    "s_cselect_b32 s47, 56, 64\n"
    "v_cmp_le_u32_e32 vcc, s47, v24\n"
    "v_mov_b32_e32 v65, 0x3e80bdab\n"
    "v_mov_b32_e32 v67, 0x392b\n"
    "v_cndmask_b32_e32 v65, v64, v65, vcc\n"
    "v_cndmask_b32_e32 v67, v66, v67, vcc\n"
    "s_cmp_ge_u32 s36, 120\n"
    "s_cselect_b32 s47, 15, 16\n"
    "v_cmp_ne_u32_e32 vcc, s47, v29\n"
    "v_mov_b32_e32 v68, 0x3db8aa3b\n"
    "s_lshr_b32 s47, s36, 5\n"
    "v_cndmask_b32_e32 v68, 0, v68, vcc\n"
    "s_lshl_b32 s47, s47, 15\n"
    "s_and_b32 s48, s36, 7\n"
    "s_lshl_b32 s48, s48, 11\n"
    "s_add_u32 s47, s47, s48\n"
    "s_and_b32 s48, s36, 8\n"
    "s_lshl_b32 s48, s48, 7\n"
    "s_add_u32 s47, s47, s48\n"
    "s_and_b32 s48, s36, 16\n"
    "s_lshl_b32 s48, s48, 10\n"
    "s_add_u32 s47, s47, s48\n"
    "s_add_u32 s52, s47, 5242880\n"
    "s_lshl_b32 s48, s50, 9\n"
    "s_add_u32 s52, s52, s48\n"
    "s_lshl_b32 s48, s50, 11\n"
    "v_add_u32_e32 v72, s48, v240\n"
    "v_add_u32_e32 v73, s48, v241\n"
    "v_and_b32_e32 v74, 0x1f0, v253\n"
    "v_lshrrev_b32_e32 v29, 5, v24\n"
    "v_lshl_add_u32 v74, v29, 3, v74\n"
    "s_lshl_b32 s53, s36, 1\n"
    "s_add_u32 s53, s53, s50\n"
    "s_lshl_b32 s53, s53, 4\n"
    "s_add_u32 s53, s53, 5771264\n"
    "s_cmp_eq_u32 s51, 0\n"
    "s_cselect_b32 s52, s52, 2.0\n"
    "s_cselect_b32 s53, s53, 2.0\n"
    "s_lshl_b32 s47, %1, 4\n"
    "s_add_u32 s47, s47, 5701632\n"
    "s_lshl_b32 s48, %1, 7\n"
    "s_add_u32 s48, s48, 5705728\n"
    "s_cmp_eq_u32 s50, 0\n"
    "s_cselect_b32 s47, s47, s48\n"
    "s_cmp_eq_u32 s51, 0\n"
    "s_cselect_b32 s47, s53, s47\n"
    "buffer_store_dword v24, off, s[8:11], s47 offset:8 sc1\n"
    "v_lshrrev_b32_e32 v109, 2, v108\n"
    "v_mul_u32_u24_e32 v109, 0x4100, v109\n"
    "v_and_b32_e32 v110, 48, v253\n"
    "v_or_b32_e32 v109, v109, v110\n"
    "v_add_u32_e32 v110, 0x104000, v109\n"
    "v_cmp_gt_u32_e32 vcc, 64, v108\n"
    "s_add_u32 s44, %3, 24576\n"
    "v_lshl_add_u32 v108, v108, 4, s44\n"
    "v_cndmask_b32_e32 v110, 2.0, v110, vcc\n"
    "v_mov_b32_e32 v32, -1\n"
    "v_mov_b32_e32 v33, -1\n"
    "v_mov_b32_e32 v34, -1\n"
    "v_mov_b32_e32 v35, -1\n"
    "ds_write_b128 v108, v[32:35] offset:0\n"
    "ds_write_b128 v108, v[32:35] offset:1280\n"
    "v_and_b32_e32 v29, 6, v28\n"
    "v_xor_b32_e32 v29, v29, v24\n"
    "v_and_b32_e32 v29, 7, v29\n"
    "v_lshlrev_b32_e32 v105, 7, v28\n"
    "v_lshl_add_u32 v105, v29, 4, v105\n"
    "v_bfe_u32 v29, v24, 3, 1\n"
    "v_subrev_u32_e32 v29, s37, v29\n"
    "v_and_b32_e32 v29, 3, v29\n"
    "v_lshl_add_u32 v105, v29, 11, v105\n"
    "s_add_u32 s45, %3, 65536\n"
    "v_add_u32_e32 v105, s45, v105\n"
    "v_xor_b32_e32 v106, 0x1000, v105\n"
    "v_and_b32_e32 v29, 15, v24\n"
    "v_cmp_eq_u32_e32 vcc, 15, v29\n"
    "v_mov_b32_e32 v107, 0xffff\n"
    "s_mov_b32 s58, 0x3f803f80\n"
    "s_mov_b32 s57, 0x43c00000\n"
    "v_cndmask_b32_e32 v107, -1, v107, vcc\n"
    "s_mov_b32 s56, 0xc3c00000\n"
    "s_mov_b32 s62, 0xc000c000\n"
    "s_mov_b32 s63, 0xc000c000\n"
    "v_add_u32_e32 v250, s45, v27\n"
    "v_xor_b32_e32 v251, 64, v250\n"
    "s_lshl_b32 s47, %2, 10\n"
    "s_add_u32 s47, s47, 5771264\n"
    "v_lshl_add_u32 v252, v24, 4, s47\n"
    "s_waitcnt vmcnt(11)\n"
    "s_waitcnt lgkmcnt(0)\n"
    "s_barrier\n"
    "s_add_u32 s39, s39, 2\n"
    "s_cmp_lg_u32 s51, 0\n"
    "s_cbranch_scc1 .Lat_bin\n"
    "ds_read_b128 v[112:115], v72 offset:0\n"
    "ds_read_b128 v[208:211], v240 offset:18432\n"
    "ds_read_b128 v[176:179], v240 offset:16384\n"
    "ds_read_b128 v[116:119], v73 offset:0\n"
    "ds_read_b128 v[212:215], v241 offset:18432\n"
    "ds_read_b128 v[180:183], v241 offset:16384\n"
    "ds_read_b128 v[120:123], v72 offset:4096\n"
    "ds_read_b128 v[216:219], v240 offset:22528\n"
    "ds_read_b128 v[184:187], v240 offset:20480\n"
    "ds_read_b128 v[124:127], v73 offset:4096\n"
    "ds_read_b128 v[220:223], v241 offset:22528\n"
    "ds_read_b128 v[188:191], v241 offset:20480\n"
    "ds_read_b128 v[128:131], v72 offset:8192\n"
    "ds_read_b128 v[224:227], v240 offset:26624\n"
    "ds_read_b128 v[192:195], v240 offset:24576\n"
    "s_waitcnt lgkmcnt(12)\n"
    "v_mfma_f32_16x16x32_bf16 v[56:59], v[208:211], v[112:115], 0\n"
    "ds_read_b128 v[132:135], v73 offset:8192\n"
    "ds_read_b128 v[228:231], v241 offset:26624\n"
    "v_mfma_f32_16x16x32_bf16 v[48:51], v[112:115], v[176:179], 0\n"
    "ds_read_b128 v[196:199], v241 offset:24576\n"
    "s_waitcnt lgkmcnt(12)\n"
    "v_mfma_f32_16x16x32_bf16 v[56:59], v[212:215], v[116:119], v[56:59]\n"
    "ds_read_b128 v[136:139], v72 offset:12288\n"
    "ds_read_b128 v[232:235], v240 offset:30720\n"
    "v_mfma_f32_16x16x32_bf16 v[48:51], v[116:119], v[180:183], v[48:51]\n"
    "ds_read_b128 v[200:203], v240 offset:28672\n"
    "s_waitcnt lgkmcnt(12)\n"
    "v_mfma_f32_16x16x32_bf16 v[56:59], v[216:219], v[120:123], v[56:59]\n"
    "ds_read_b128 v[140:143], v73 offset:12288\n"
    "ds_read_b128 v[236:239], v241 offset:30720\n"
    "v_mfma_f32_16x16x32_bf16 v[48:51], v[120:123], v[184:187], v[48:51]\n"
    "ds_read_b128 v[204:207], v241 offset:28672\n"
    "s_waitcnt lgkmcnt(12)\n"
    "v_mfma_f32_16x16x32_bf16 v[56:59], v[220:223], v[124:127], v[56:59]\n"
    "v_mfma_f32_16x16x32_bf16 v[48:51], v[124:127], v[188:191], v[48:51]\n"
    "s_waitcnt lgkmcnt(9)\n"
    "v_mfma_f32_16x16x32_bf16 v[56:59], v[224:227], v[128:131], v[56:59]\n"
    "v_mfma_f32_16x16x32_bf16 v[48:51], v[128:131], v[192:195], v[48:51]\n"
    "s_waitcnt lgkmcnt(6)\n"
    "v_mfma_f32_16x16x32_bf16 v[56:59], v[228:231], v[132:135], v[56:59]\n"
    "v_mfma_f32_16x16x32_bf16 v[48:51], v[132:135], v[196:199], v[48:51]\n"
    "s_waitcnt lgkmcnt(3)\n"
    "v_mfma_f32_16x16x32_bf16 v[56:59], v[232:235], v[136:139], v[56:59]\n"
    "v_mfma_f32_16x16x32_bf16 v[48:51], v[136:139], v[200:203], v[48:51]\n"
    "s_waitcnt lgkmcnt(0)\n"
    "v_mfma_f32_16x16x32_bf16 v[56:59], v[236:239], v[140:143], v[56:59]\n"
    "v_mfma_f32_16x16x32_bf16 v[48:51], v[140:143], v[204:207], v[48:51]\n"
    "s_nop 3\n"
    "v_cvt_pk_bf16_f32 v56, v56, v57\n"
    "v_cvt_pk_bf16_f32 v57, v58, v59\n"
    "s_nop 1\n"
    "v_mfma_f32_16x16x16_bf16 v[48:51], v[56:57], v[64:65], v[48:51]\n"
    "v_mfma_f32_16x16x16_bf16 v[48:51], v[56:57], v[66:67], v[48:51]\n"
    "s_nop 7\n"
    "v_mul_f32_e32 v48, v48, v68\n"
    "v_mul_f32_e32 v49, v49, v68\n"
    "v_mul_f32_e32 v50, v50, v68\n"
    "v_mul_f32_e32 v51, v51, v68\n"
    "v_cvt_pk_bf16_f32 v48, v48, v49\n"
    "v_cvt_pk_bf16_f32 v49, v50, v51\n"
    "s_branch .Lat_noj2\n"
    ".Lat_apoll:\n"
    "s_mov_b32 s53, 0x400000\n"
    ".Lat_apoll_l:\n"
    "s_sleep 1\n"
    "buffer_load_dword v255, v252, s[8:11], 0 offen offset:0 sc1\n"
    "s_waitcnt vmcnt(0)\n"
    "v_cmp_ne_u32_e32 vcc, s39, v255\n"
    "s_sub_u32 s53, s53, 1\n"
    "s_cbranch_scc1 .Lat_apoll_ret\n"
    "s_nop 2\n"
    "s_cbranch_vccnz .Lat_apoll_l\n"
    "s_branch .Lat_apoll_ret\n"
    ".Lat_h1poll:\n"
    "s_mov_b32 s53, 0x400000\n"
    ".Lat_h1poll_l:\n"
    "s_sleep 1\n"
    "buffer_load_dword v0, v3, s[8:11], s59 offen offset:0 sc1\n"
    "s_waitcnt vmcnt(0)\n"
    "v_cmp_ne_u32_e32 vcc, s39, v0\n"
    "s_sub_u32 s53, s53, 1\n"
    "s_cbranch_scc1 .Lat_h1poll_ret\n"
    "s_nop 2\n"
    "s_cbranch_vccnz .Lat_h1poll_l\n"
    "s_branch .Lat_h1poll_ret\n"
    ".Lat_hpoll:\n"
    "s_mov_b32 s53, 0x400000\n"
    ".Lat_hpoll_l:\n"
    "s_sleep 1\n"
    "buffer_load_dword v0, v253, s[8:11], s59 offen offset:0 sc1\n"
    "buffer_load_dword v1, v2, s[8:11], s59 offen offset:0 sc1\n"
    "s_waitcnt vmcnt(0)\n"
    "v_xor_b32_e32 v1, s39, v1\n"
    "v_xor_b32_e32 v0, s39, v0\n"
    "v_or_b32_e32 v0, v0, v1\n"
    "v_cmp_ne_u32_e32 vcc, 0, v0\n"
    "s_sub_u32 s53, s53, 1\n"
    "s_cbranch_scc1 .Lat_hpoll_ret\n"
    "s_nop 2\n"
    "s_cbranch_vccnz .Lat_hpoll_l\n"
    "s_branch .Lat_hpoll_ret\n"
    ".Lat_qpoll:\n"
    "s_mov_b32 s53, 0x400000\n"
    ".Lat_qpoll_l:\n"
    "s_sleep 0\n"
    "buffer_load_dword v11, v5, s[8:11], s47 offen offset:0 sc1\n"
    "s_waitcnt vmcnt(0)\n"
    "v_cmp_ne_u32_e32 vcc, s39, v11\n"
    "s_sub_u32 s53, s53, 1\n"
    "s_cbranch_scc1 .Lat_qpoll_ret\n"
    "s_nop 2\n"
    "s_cbranch_vccnz .Lat_qpoll_l\n"
    "s_branch .Lat_qpoll_ret\n"
    ".Lat_bin:\n"
    "s_movk_i32 s45, -640\n"
    "s_movk_i32 s48, 80\n"
    "v_mov_b32_e32 v60, 1\n"
    "v_cmp_gt_u32_e32 vcc, s59, v111\n"
    "v_cmp_gt_i32_e64 s[46:47], s61, v111\n"
    "s_waitcnt vmcnt(9)\n"
    "v_mul_u32_u24_e32 v48, 0x6667, v96\n"
    "v_mul_u32_u24_e32 v49, 0x6667, v97\n"
    "v_mul_u32_u24_e32 v50, 0x6667, v98\n"
    "v_mul_u32_u24_e32 v51, 0x6667, v99\n"
    "v_mul_u32_u24_e32 v52, 0x6667, v92\n"
    "v_mul_u32_u24_e32 v53, 0x6667, v93\n"
    "v_lshrrev_b32_e32 v48, 24, v48\n"
    "v_lshrrev_b32_e32 v49, 24, v49\n"
    "v_lshrrev_b32_e32 v50, 24, v50\n"
    "v_lshrrev_b32_e32 v51, 24, v51\n"
    "v_lshrrev_b32_e32 v52, 24, v52\n"
    "v_lshrrev_b32_e32 v53, 24, v53\n"
    "v_min_u32_e32 v48, 15, v48\n"
    "v_min_u32_e32 v49, 15, v49\n"
    "v_min_u32_e32 v50, 15, v50\n"
    "v_min_u32_e32 v51, 15, v51\n"
    "v_min_u32_e32 v52, 15, v52\n"
    "v_min_u32_e32 v53, 15, v53\n"
    "v_lshl_add_u32 v54, v48, 2, s44\n"
    "v_lshl_add_u32 v55, v49, 2, s44\n"
    "v_lshl_add_u32 v56, v50, 2, s44\n"
    "v_lshl_add_u32 v57, v51, 2, s44\n"
    "v_lshl_add_u32 v58, v52, 2, s44\n"
    "v_lshl_add_u32 v59, v53, 2, s44\n"
    "s_mov_b64 exec, vcc\n"
    "ds_add_rtn_u32 v54, v54, v60 offset:5248\n"
    "ds_add_rtn_u32 v55, v55, v60 offset:5248\n"
    "ds_add_rtn_u32 v56, v56, v60 offset:5248\n"
    "ds_add_rtn_u32 v57, v57, v60 offset:5248\n"
    "s_mov_b64 exec, s[46:47]\n"
    "ds_add_rtn_u32 v58, v58, v60 offset:5248\n"
    "ds_add_rtn_u32 v59, v59, v60 offset:5248\n"
    "s_mov_b64 exec, -1\n"
    "s_waitcnt vmcnt(7)\n"
    "v_mad_i32_i24 v96, v48, s45, v96\n"
    "v_mad_i32_i24 v97, v49, s45, v97\n"
    "v_mad_i32_i24 v98, v50, s45, v98\n"
    "v_mad_i32_i24 v99, v51, s45, v99\n"
    "v_mad_i32_i24 v92, v52, s45, v92\n"
    "v_mad_i32_i24 v93, v53, s45, v93\n"
    "v_lshl_or_b32 v96, v96, 16, v100\n"
    "v_lshl_or_b32 v97, v97, 16, v101\n"
    "v_lshl_or_b32 v98, v98, 16, v102\n"
    "v_lshl_or_b32 v99, v99, 16, v103\n"
    "v_lshl_or_b32 v92, v92, 16, v94\n"
    "v_lshl_or_b32 v93, v93, 16, v95\n"
    "s_mov_b64 exec, vcc\n"
    "s_waitcnt lgkmcnt(0)\n"
    "v_min_i32_e32 v54, 78, v54\n"
    "v_min_i32_e32 v55, 78, v55\n"
    "v_min_i32_e32 v56, 78, v56\n"
    "v_min_i32_e32 v57, 78, v57\n"
    "v_mad_u32_u24 v48, v48, s48, v54\n"
    "v_mad_u32_u24 v49, v49, s48, v55\n"
    "v_mad_u32_u24 v50, v50, s48, v56\n"
    "v_mad_u32_u24 v51, v51, s48, v57\n"
    "v_lshl_add_u32 v48, v48, 2, s44\n"
    "v_lshl_add_u32 v49, v49, 2, s44\n"
    "v_lshl_add_u32 v50, v50, 2, s44\n"
    "v_lshl_add_u32 v51, v51, 2, s44\n"
    "ds_write_b32 v48, v96 offset:4\n"
    "ds_write_b32 v49, v97 offset:4\n"
    "ds_write_b32 v50, v98 offset:4\n"
    "ds_write_b32 v51, v99 offset:4\n"
    "s_mov_b64 exec, s[46:47]\n"
    "v_min_i32_e32 v58, 78, v58\n"
    "v_min_i32_e32 v59, 78, v59\n"
    "v_mad_u32_u24 v52, v52, s48, v58\n"
    "v_mad_u32_u24 v53, v53, s48, v59\n"
    "v_lshl_add_u32 v52, v52, 2, s44\n"
    "v_lshl_add_u32 v53, v53, 2, s44\n"
    "ds_write_b32 v52, v92 offset:4\n"
    "ds_write_b32 v53, v93 offset:4\n"
    "s_mov_b64 exec, -1\n"
    ".Lat_noj2:\n"
    "buffer_store_dwordx2 v[48:49], v74, s[8:11], s52 offen offset:0 sc1\n"
    "v_mov_b32_e32 v112, s39\n"
    "v_mov_b32_e32 v113, 0\n"
    "s_waitcnt vmcnt(2)\n"
    "v_dot2_f32_bf16 v24, v0, s58, 0\n"
    "v_dot2_f32_bf16 v32, v8, s58, 0\n"
    "v_dot2_f32_bf16 v40, v16, s58, 0\n"
    "v_dot2_f32_bf16 v25, v4, s58, 0\n"
    "v_dot2_f32_bf16 v33, v12, s58, 0\n"
    "v_dot2_f32_bf16 v41, v20, s58, 0\n"
    "v_dot2_f32_bf16 v26, v0, v0, 0\n"
    "v_dot2_f32_bf16 v34, v8, v8, 0\n"
    "v_dot2_f32_bf16 v42, v16, v16, 0\n"
    "v_dot2_f32_bf16 v27, v4, v4, 0\n"
    "v_dot2_f32_bf16 v35, v12, v12, 0\n"
    "v_dot2_f32_bf16 v43, v20, v20, 0\n"
    "v_dot2_f32_bf16 v24, v1, s58, v24\n"
    "v_dot2_f32_bf16 v32, v9, s58, v32\n"
    "v_dot2_f32_bf16 v40, v17, s58, v40\n"
    "v_dot2_f32_bf16 v25, v5, s58, v25\n"
    "v_dot2_f32_bf16 v33, v13, s58, v33\n"
    "v_dot2_f32_bf16 v41, v21, s58, v41\n"
    "v_dot2_f32_bf16 v26, v1, v1, v26\n"
    "v_dot2_f32_bf16 v34, v9, v9, v34\n"
    "v_dot2_f32_bf16 v42, v17, v17, v42\n"
    "v_dot2_f32_bf16 v27, v5, v5, v27\n"
    "v_dot2_f32_bf16 v35, v13, v13, v35\n"
    "v_dot2_f32_bf16 v43, v21, v21, v43\n"
    "v_dot2_f32_bf16 v24, v2, s58, v24\n"
    "v_dot2_f32_bf16 v32, v10, s58, v32\n"
    "v_dot2_f32_bf16 v40, v18, s58, v40\n"
    "v_dot2_f32_bf16 v25, v6, s58, v25\n"
    "v_dot2_f32_bf16 v33, v14, s58, v33\n"
    "v_dot2_f32_bf16 v41, v22, s58, v41\n"
    "v_dot2_f32_bf16 v26, v2, v2, v26\n"
    "v_dot2_f32_bf16 v34, v10, v10, v34\n"
    "v_dot2_f32_bf16 v42, v18, v18, v42\n"
    "v_dot2_f32_bf16 v27, v6, v6, v27\n"
    "v_dot2_f32_bf16 v35, v14, v14, v35\n"
    "v_dot2_f32_bf16 v43, v22, v22, v43\n"
    "v_dot2_f32_bf16 v24, v3, s58, v24\n"
    "v_dot2_f32_bf16 v32, v11, s58, v32\n"
    "v_dot2_f32_bf16 v40, v19, s58, v40\n"
    "v_dot2_f32_bf16 v25, v7, s58, v25\n"
    "v_dot2_f32_bf16 v33, v15, s58, v33\n"
    "v_dot2_f32_bf16 v41, v23, s58, v41\n"
    "v_dot2_f32_bf16 v26, v3, v3, v26\n"
    "v_dot2_f32_bf16 v34, v11, v11, v34\n"
    "v_dot2_f32_bf16 v42, v19, v19, v42\n"
    "v_dot2_f32_bf16 v27, v7, v7, v27\n"
    "v_dot2_f32_bf16 v35, v15, v15, v35\n"
    "v_dot2_f32_bf16 v43, v23, v23, v43\n"
    "v_and_b32_e32 v30, 0xffff0000, v7\n"
    "v_and_b32_e32 v38, 0xffff0000, v15\n"
    "v_and_b32_e32 v46, 0xffff0000, v23\n"
    "v_add_f32_e32 v24, v24, v25\n"
    "v_add_f32_e32 v32, v32, v33\n"
    "v_add_f32_e32 v40, v40, v41\n"
    "v_add_f32_e32 v26, v26, v27\n"
    "v_add_f32_e32 v34, v34, v35\n"
    "v_add_f32_e32 v42, v42, v43\n"
    "v_add_f32_dpp v25, v25, v25 quad_perm:[1,0,3,2] row_mask:0xf bank_mask:0xf bound_ctrl:1\n"
    "v_lshlrev_b32_e32 v48, 16, v0\n"
    "v_add_f32_dpp v33, v33, v33 quad_perm:[1,0,3,2] row_mask:0xf bank_mask:0xf bound_ctrl:1\n"
    "v_and_b32_e32 v49, 0xffff0000, v0\n"
    "v_add_f32_dpp v41, v41, v41 quad_perm:[1,0,3,2] row_mask:0xf bank_mask:0xf bound_ctrl:1\n"
    "v_lshlrev_b32_e32 v50, 16, v1\n"
    "v_mov_b32_dpp v30, v30 quad_perm:[3,3,3,3] row_mask:0xf bank_mask:0xf bound_ctrl:1\n"
    "v_and_b32_e32 v51, 0xffff0000, v1\n"
    "v_mov_b32_dpp v38, v38 quad_perm:[3,3,3,3] row_mask:0xf bank_mask:0xf bound_ctrl:1\n"
    "v_lshlrev_b32_e32 v52, 16, v2\n"
    "v_mov_b32_dpp v46, v46 quad_perm:[3,3,3,3] row_mask:0xf bank_mask:0xf bound_ctrl:1\n"
    "v_and_b32_e32 v53, 0xffff0000, v2\n"
    "v_add_f32_dpp v24, v24, v24 quad_perm:[1,0,3,2] row_mask:0xf bank_mask:0xf bound_ctrl:1\n"
    "v_lshlrev_b32_e32 v54, 16, v3\n"
    "v_add_f32_dpp v32, v32, v32 quad_perm:[1,0,3,2] row_mask:0xf bank_mask:0xf bound_ctrl:1\n"
    "v_and_b32_e32 v55, 0xffff0000, v3\n"
    "v_add_f32_dpp v40, v40, v40 quad_perm:[1,0,3,2] row_mask:0xf bank_mask:0xf bound_ctrl:1\n"
    "v_lshlrev_b32_e32 v56, 16, v4\n"
    "v_add_f32_dpp v26, v26, v26 quad_perm:[1,0,3,2] row_mask:0xf bank_mask:0xf bound_ctrl:1\n"
    "v_and_b32_e32 v57, 0xffff0000, v4\n"
    "v_add_f32_dpp v34, v34, v34 quad_perm:[1,0,3,2] row_mask:0xf bank_mask:0xf bound_ctrl:1\n"
    "v_lshlrev_b32_e32 v58, 16, v5\n"
    "s_waitcnt vmcnt(0)\n"
    "buffer_store_dword v112, v113, s[8:11], s53 offen offset:0 sc1\n"
    "v_add_f32_dpp v42, v42, v42 quad_perm:[1,0,3,2] row_mask:0xf bank_mask:0xf bound_ctrl:1\n"
    "v_and_b32_e32 v59, 0xffff0000, v5\n"
    "v_add_f32_dpp v24, v24, v24 quad_perm:[2,3,0,1] row_mask:0xf bank_mask:0xf bound_ctrl:1\n"
    "v_lshlrev_b32_e32 v60, 16, v6\n"
    "v_add_f32_dpp v32, v32, v32 quad_perm:[2,3,0,1] row_mask:0xf bank_mask:0xf bound_ctrl:1\n"
    "v_and_b32_e32 v61, 0xffff0000, v6\n"
    "s_waitcnt lgkmcnt(0)\n"
    "s_barrier\n"
    "ds_read_b128 v[96:99], v108 offset:0\n"
    "ds_read_b128 v[100:103], v108 offset:4096\n"
    "s_lshl_b32 s45, %1, 6\n"
    "s_add_u32 s45, s45, 35038208\n"
    "v_add_f32_dpp v40, v40, v40 quad_perm:[2,3,0,1] row_mask:0xf bank_mask:0xf bound_ctrl:1\n"
    "v_lshlrev_b32_e32 v62, 16, v7\n"
    "v_add_f32_dpp v26, v26, v26 quad_perm:[2,3,0,1] row_mask:0xf bank_mask:0xf bound_ctrl:1\n"
    "v_and_b32_e32 v63, 0xffff0000, v7\n"
    "v_add_f32_dpp v34, v34, v34 quad_perm:[2,3,0,1] row_mask:0xf bank_mask:0xf bound_ctrl:1\n"
    "v_lshlrev_b32_e32 v64, 16, v8\n"
    "v_add_f32_dpp v42, v42, v42 quad_perm:[2,3,0,1] row_mask:0xf bank_mask:0xf bound_ctrl:1\n"
    "v_and_b32_e32 v65, 0xffff0000, v8\n"
    "v_add_f32_dpp v24, v24, v24 row_half_mirror row_mask:0xf bank_mask:0xf bound_ctrl:1\n"
    "v_lshlrev_b32_e32 v66, 16, v9\n"
    "v_add_f32_dpp v32, v32, v32 row_half_mirror row_mask:0xf bank_mask:0xf bound_ctrl:1\n"
    "v_and_b32_e32 v67, 0xffff0000, v9\n"
    "v_add_f32_dpp v40, v40, v40 row_half_mirror row_mask:0xf bank_mask:0xf bound_ctrl:1\n"
    "v_lshlrev_b32_e32 v68, 16, v10\n"
    "v_add_f32_dpp v26, v26, v26 row_half_mirror row_mask:0xf bank_mask:0xf bound_ctrl:1\n"
    "v_and_b32_e32 v69, 0xffff0000, v10\n"
    "v_add_f32_dpp v34, v34, v34 row_half_mirror row_mask:0xf bank_mask:0xf bound_ctrl:1\n"
    "v_lshlrev_b32_e32 v70, 16, v11\n"
    "v_add_f32_dpp v42, v42, v42 row_half_mirror row_mask:0xf bank_mask:0xf bound_ctrl:1\n"
    "v_and_b32_e32 v71, 0xffff0000, v11\n"
    "v_add_f32_dpp v24, v24, v24 row_mirror row_mask:0xf bank_mask:0xf bound_ctrl:1\n"
    "v_lshlrev_b32_e32 v72, 16, v12\n"
    "v_add_f32_dpp v32, v32, v32 row_mirror row_mask:0xf bank_mask:0xf bound_ctrl:1\n"
    "v_and_b32_e32 v73, 0xffff0000, v12\n"
    "v_add_f32_dpp v40, v40, v40 row_mirror row_mask:0xf bank_mask:0xf bound_ctrl:1\n"
    "v_lshlrev_b32_e32 v74, 16, v13\n"
    "v_add_f32_dpp v26, v26, v26 row_mirror row_mask:0xf bank_mask:0xf bound_ctrl:1\n"
    "v_and_b32_e32 v75, 0xffff0000, v13\n"
    "v_add_f32_dpp v34, v34, v34 row_mirror row_mask:0xf bank_mask:0xf bound_ctrl:1\n"
    "v_lshlrev_b32_e32 v76, 16, v14\n"
    "s_waitcnt lgkmcnt(0)\n"
    "buffer_store_dwordx4 v[96:99], v109, s[8:11], s45 offen offset:0 sc1\n"
    "buffer_store_dwordx4 v[100:103], v110, s[8:11], s45 offen offset:0 sc1\n"
    "v_add_f32_dpp v42, v42, v42 row_mirror row_mask:0xf bank_mask:0xf bound_ctrl:1\n"
    "v_and_b32_e32 v77, 0xffff0000, v14\n"
    "v_mul_f32_e32 v24, 0x3b800000, v24\n"
    "v_lshlrev_b32_e32 v78, 16, v15\n"
    "v_mul_f32_e32 v32, 0x3b800000, v32\n"
    "v_and_b32_e32 v79, 0xffff0000, v15\n"
    "v_mul_f32_e32 v40, 0x3b800000, v40\n"
    "v_lshlrev_b32_e32 v80, 16, v16\n"
    "v_mul_f32_e32 v25, 0.25, v25\n"
    "v_and_b32_e32 v81, 0xffff0000, v16\n"
    "v_mul_f32_e32 v33, 0.25, v33\n"
    "v_lshlrev_b32_e32 v82, 16, v17\n"
    "v_mul_f32_e32 v41, 0.25, v41\n"
    "v_and_b32_e32 v83, 0xffff0000, v17\n"
    "v_mul_f32_e64 v27, v24, -v24\n"
    "v_lshlrev_b32_e32 v84, 16, v18\n"
    "v_mul_f32_e64 v35, v32, -v32\n"
    "v_and_b32_e32 v85, 0xffff0000, v18\n"
    "v_mul_f32_e64 v43, v40, -v40\n"
    "v_lshlrev_b32_e32 v86, 16, v19\n"
    "v_sub_f32_e32 v25, v25, v30\n"
    "v_and_b32_e32 v87, 0xffff0000, v19\n"
    "v_sub_f32_e32 v33, v33, v38\n"
    "v_lshlrev_b32_e32 v88, 16, v20\n"
    "v_sub_f32_e32 v41, v41, v46\n"
    "v_and_b32_e32 v89, 0xffff0000, v20\n"
    "v_fmac_f32_e32 v27, 0x3b800000, v26\n"
    "v_lshlrev_b32_e32 v90, 16, v21\n"
    "v_fmac_f32_e32 v35, 0x3b800000, v34\n"
    "v_and_b32_e32 v91, 0xffff0000, v21\n"
    "v_fmac_f32_e32 v43, 0x3b800000, v42\n"
    "v_lshlrev_b32_e32 v92, 16, v22\n"
    "v_mul_f32_e32 v25, 0x3eaaaaab, v25\n"
    "v_and_b32_e32 v93, 0xffff0000, v22\n"
    "v_mul_f32_e32 v33, 0x3eaaaaab, v33\n"
    "v_lshlrev_b32_e32 v94, 16, v23\n"
    "v_mul_f32_e32 v41, 0x3eaaaaab, v41\n"
    "v_and_b32_e32 v95, 0xffff0000, v23\n"
    "v_max_f32_e32 v27, 0, v27\n"
    "v_max_f32_e32 v35, 0, v35\n"
    "v_max_f32_e32 v43, 0, v43\n"
    "v_cndmask_b32_e64 v25, v24, v25, s[62:63]\n"
    "v_cndmask_b32_e64 v33, v32, v33, s[62:63]\n"
    "v_cndmask_b32_e64 v41, v40, v41, s[62:63]\n"
    "v_add_f32_e32 v27, 0x3727c5ac, v27\n"
    "v_add_f32_e32 v35, 0x3727c5ac, v35\n"
    "v_add_f32_e32 v43, 0x3727c5ac, v43\n"
    "v_rsq_f32_e32 v28, v27\n"
    "v_rsq_f32_e32 v36, v35\n"
    "v_rsq_f32_e32 v44, v43\n"
    "v_fma_f32 v29, -v24, v28, s57\n"
    "v_fma_f32 v37, -v32, v36, s57\n"
    "v_fma_f32 v45, -v40, v44, s57\n"
    "v_fma_f32 v31, -v25, v28, s57\n"
    "v_fma_f32 v39, -v33, v36, s57\n"
    "v_fma_f32 v47, -v41, v44, s57\n"
    "v_sub_f32_e32 v24, v29, v31\n"
    "v_sub_f32_e32 v32, v37, v39\n"
    "v_sub_f32_e32 v40, v45, v47\n"
    "v_pk_fma_f32 v[48:49], v[48:49], v[28:29], v[28:29] op_sel:[0,0,1] op_sel_hi:[1,0,1]\n"
    "v_pk_fma_f32 v[50:51], v[50:51], v[28:29], v[28:29] op_sel:[0,0,1] op_sel_hi:[1,0,1]\n"
    "v_pk_fma_f32 v[52:53], v[52:53], v[28:29], v[28:29] op_sel:[0,0,1] op_sel_hi:[1,0,1]\n"
    "v_pk_fma_f32 v[54:55], v[54:55], v[28:29], v[28:29] op_sel:[0,0,1] op_sel_hi:[1,0,1]\n"
    "v_pk_fma_f32 v[56:57], v[56:57], v[28:29], v[30:31] op_sel:[0,0,1] op_sel_hi:[1,0,1]\n"
    "v_pk_fma_f32 v[58:59], v[58:59], v[28:29], v[30:31] op_sel:[0,0,1] op_sel_hi:[1,0,1]\n"
    "v_pk_fma_f32 v[60:61], v[60:61], v[28:29], v[30:31] op_sel:[0,0,1] op_sel_hi:[1,0,1]\n"
    "v_pk_fma_f32 v[62:63], v[62:63], v[28:29], v[30:31] op_sel:[0,0,1] op_sel_hi:[1,0,1]\n"
    "v_pk_add_f32 v[48:49], v[48:49], s[56:57] op_sel_hi:[1,0]\n"
    "v_pk_add_f32 v[50:51], v[50:51], s[56:57] op_sel_hi:[1,0]\n"
    "v_pk_add_f32 v[52:53], v[52:53], s[56:57] op_sel_hi:[1,0]\n"
    "v_pk_add_f32 v[54:55], v[54:55], s[56:57] op_sel_hi:[1,0]\n"
    "v_pk_add_f32 v[56:57], v[56:57], s[56:57] op_sel_hi:[1,0]\n"
    "v_pk_add_f32 v[58:59], v[58:59], s[56:57] op_sel_hi:[1,0]\n"
    "v_pk_add_f32 v[60:61], v[60:61], s[56:57] op_sel_hi:[1,0]\n"
    "v_pk_add_f32 v[62:63], v[62:63], s[56:57] op_sel_hi:[1,0]\n"
    "v_cvt_pk_bf16_f32 v0, v48, v49\n"
    "v_cvt_pk_bf16_f32 v1, v50, v51\n"
    "v_cvt_pk_bf16_f32 v2, v52, v53\n"
    "v_cvt_pk_bf16_f32 v3, v54, v55\n"
    "v_cvt_pk_bf16_f32 v4, v56, v57\n"
    "v_cvt_pk_bf16_f32 v5, v58, v59\n"
    "v_cvt_pk_bf16_f32 v6, v60, v61\n"
    "v_cvt_pk_bf16_f32 v7, v62, v63\n"
    "buffer_store_dwordx4 v[0:3], v104, s[8:11], s35 offen offset:0 sc1\n"
    "v_bfi_b32 v7, v107, v7, 1.0\n"
    "ds_write_b128 v105, v[0:3] offset:0\n"
    "buffer_store_dwordx4 v[4:7], v104, s[8:11], s35 offen offset:256 sc1\n"
    "v_pk_fma_f32 v[64:65], v[64:65], v[36:37], v[36:37] op_sel:[0,0,1] op_sel_hi:[1,0,1]\n"
    "v_pk_fma_f32 v[66:67], v[66:67], v[36:37], v[36:37] op_sel:[0,0,1] op_sel_hi:[1,0,1]\n"
    "v_pk_fma_f32 v[68:69], v[68:69], v[36:37], v[36:37] op_sel:[0,0,1] op_sel_hi:[1,0,1]\n"
    "v_pk_fma_f32 v[70:71], v[70:71], v[36:37], v[36:37] op_sel:[0,0,1] op_sel_hi:[1,0,1]\n"
    "v_pk_fma_f32 v[72:73], v[72:73], v[36:37], v[38:39] op_sel:[0,0,1] op_sel_hi:[1,0,1]\n"
    "v_pk_fma_f32 v[74:75], v[74:75], v[36:37], v[38:39] op_sel:[0,0,1] op_sel_hi:[1,0,1]\n"
    "v_pk_fma_f32 v[76:77], v[76:77], v[36:37], v[38:39] op_sel:[0,0,1] op_sel_hi:[1,0,1]\n"
    "v_pk_fma_f32 v[78:79], v[78:79], v[36:37], v[38:39] op_sel:[0,0,1] op_sel_hi:[1,0,1]\n"
    "v_pk_add_f32 v[64:65], v[64:65], s[56:57] op_sel_hi:[1,0]\n"
    "v_pk_add_f32 v[66:67], v[66:67], s[56:57] op_sel_hi:[1,0]\n"
    "v_pk_add_f32 v[68:69], v[68:69], s[56:57] op_sel_hi:[1,0]\n"
    "v_pk_add_f32 v[70:71], v[70:71], s[56:57] op_sel_hi:[1,0]\n"
    "v_pk_add_f32 v[72:73], v[72:73], s[56:57] op_sel_hi:[1,0]\n"
    "v_pk_add_f32 v[74:75], v[74:75], s[56:57] op_sel_hi:[1,0]\n"
    "v_pk_add_f32 v[76:77], v[76:77], s[56:57] op_sel_hi:[1,0]\n"
    "v_pk_add_f32 v[78:79], v[78:79], s[56:57] op_sel_hi:[1,0]\n"
    "v_cvt_pk_bf16_f32 v8, v64, v65\n"
    "v_cvt_pk_bf16_f32 v9, v66, v67\n"
    "v_cvt_pk_bf16_f32 v10, v68, v69\n"
    "v_cvt_pk_bf16_f32 v11, v70, v71\n"
    "v_cvt_pk_bf16_f32 v12, v72, v73\n"
    "v_cvt_pk_bf16_f32 v13, v74, v75\n"
    "v_cvt_pk_bf16_f32 v14, v76, v77\n"
    "v_cvt_pk_bf16_f32 v15, v78, v79\n"
    "buffer_store_dwordx4 v[8:11], v104, s[8:11], s54 offen offset:0 sc1\n"
    "v_bfi_b32 v15, v107, v15, 1.0\n"
    "ds_write_b128 v105, v[8:11] offset:8192\n"
    "buffer_store_dwordx4 v[12:15], v104, s[8:11], s54 offen offset:256 sc1\n"
    "v_pk_fma_f32 v[80:81], v[80:81], v[44:45], v[44:45] op_sel:[0,0,1] op_sel_hi:[1,0,1]\n"
    "v_pk_fma_f32 v[82:83], v[82:83], v[44:45], v[44:45] op_sel:[0,0,1] op_sel_hi:[1,0,1]\n"
    "v_pk_fma_f32 v[84:85], v[84:85], v[44:45], v[44:45] op_sel:[0,0,1] op_sel_hi:[1,0,1]\n"
    "v_pk_fma_f32 v[86:87], v[86:87], v[44:45], v[44:45] op_sel:[0,0,1] op_sel_hi:[1,0,1]\n"
    "v_pk_fma_f32 v[88:89], v[88:89], v[44:45], v[46:47] op_sel:[0,0,1] op_sel_hi:[1,0,1]\n"
    "v_pk_fma_f32 v[90:91], v[90:91], v[44:45], v[46:47] op_sel:[0,0,1] op_sel_hi:[1,0,1]\n"
    "v_pk_fma_f32 v[92:93], v[92:93], v[44:45], v[46:47] op_sel:[0,0,1] op_sel_hi:[1,0,1]\n"
    "v_pk_fma_f32 v[94:95], v[94:95], v[44:45], v[46:47] op_sel:[0,0,1] op_sel_hi:[1,0,1]\n"
    "v_pk_add_f32 v[80:81], v[80:81], s[56:57] op_sel_hi:[1,0]\n"
    "v_pk_add_f32 v[82:83], v[82:83], s[56:57] op_sel_hi:[1,0]\n"
    "v_pk_add_f32 v[84:85], v[84:85], s[56:57] op_sel_hi:[1,0]\n"
    "v_pk_add_f32 v[86:87], v[86:87], s[56:57] op_sel_hi:[1,0]\n"
    "v_pk_add_f32 v[88:89], v[88:89], s[56:57] op_sel_hi:[1,0]\n"
    "v_pk_add_f32 v[90:91], v[90:91], s[56:57] op_sel_hi:[1,0]\n"
    "v_pk_add_f32 v[92:93], v[92:93], s[56:57] op_sel_hi:[1,0]\n"
    "v_pk_add_f32 v[94:95], v[94:95], s[56:57] op_sel_hi:[1,0]\n"
    "v_cvt_pk_bf16_f32 v16, v80, v81\n"
    "v_cvt_pk_bf16_f32 v17, v82, v83\n"
    "v_cvt_pk_bf16_f32 v18, v84, v85\n"
    "v_cvt_pk_bf16_f32 v19, v86, v87\n"
    "v_cvt_pk_bf16_f32 v20, v88, v89\n"
    "v_cvt_pk_bf16_f32 v21, v90, v91\n"
    "v_cvt_pk_bf16_f32 v22, v92, v93\n"
    "v_cvt_pk_bf16_f32 v23, v94, v95\n"
    "buffer_store_dwordx4 v[16:19], v104, s[8:11], s60 offen offset:0 sc1\n"
    "v_bfi_b32 v23, v107, v23, 1.0\n"
    "ds_write_b128 v105, v[16:19] offset:16384\n"
    "buffer_store_dwordx4 v[20:23], v104, s[8:11], s60 offen offset:256 sc1\n"
    "v_pk_add_f32 v[56:57], v[56:57], v[24:25] op_sel_hi:[1,0]\n"
    "v_pk_add_f32 v[58:59], v[58:59], v[24:25] op_sel_hi:[1,0]\n"
    "v_pk_add_f32 v[60:61], v[60:61], v[24:25] op_sel_hi:[1,0]\n"
    "v_pk_add_f32 v[62:63], v[62:63], v[24:25] op_sel_hi:[1,0]\n"
    "v_cvt_pk_bf16_f32 v56, v56, v57\n"
    "v_cvt_pk_bf16_f32 v57, v58, v59\n"
    "v_cvt_pk_bf16_f32 v58, v60, v61\n"
    "v_cvt_pk_bf16_f32 v59, v62, v63\n"
    "ds_write_b128 v106, v[56:59] offset:0\n"
    "v_pk_add_f32 v[72:73], v[72:73], v[32:33] op_sel_hi:[1,0]\n"
    "v_pk_add_f32 v[74:75], v[74:75], v[32:33] op_sel_hi:[1,0]\n"
    "v_pk_add_f32 v[76:77], v[76:77], v[32:33] op_sel_hi:[1,0]\n"
    "v_pk_add_f32 v[78:79], v[78:79], v[32:33] op_sel_hi:[1,0]\n"
    "v_cvt_pk_bf16_f32 v72, v72, v73\n"
    "v_cvt_pk_bf16_f32 v73, v74, v75\n"
    "v_cvt_pk_bf16_f32 v74, v76, v77\n"
    "v_cvt_pk_bf16_f32 v75, v78, v79\n"
    "ds_write_b128 v106, v[72:75] offset:8192\n"
    "v_pk_add_f32 v[88:89], v[88:89], v[40:41] op_sel_hi:[1,0]\n"
    "v_pk_add_f32 v[90:91], v[90:91], v[40:41] op_sel_hi:[1,0]\n"
    "v_pk_add_f32 v[92:93], v[92:93], v[40:41] op_sel_hi:[1,0]\n"
    "v_pk_add_f32 v[94:95], v[94:95], v[40:41] op_sel_hi:[1,0]\n"
    "v_cvt_pk_bf16_f32 v88, v88, v89\n"
    "v_cvt_pk_bf16_f32 v89, v90, v91\n"
    "v_cvt_pk_bf16_f32 v90, v92, v93\n"
    "v_cvt_pk_bf16_f32 v91, v94, v95\n"
    "ds_write_b128 v106, v[88:91] offset:16384\n"
    "s_waitcnt lgkmcnt(0)\n"
    "s_barrier\n"
    "ds_read_b128 a[0:3], v250 offset:0\n"
    "ds_read_b128 a[32:35], v250 offset:8192\n"
    "ds_read_b128 a[64:67], v250 offset:16384\n"
    "ds_read_b128 a[4:7], v251 offset:0\n"
    "ds_read_b128 a[36:39], v251 offset:8192\n"
    "ds_read_b128 a[68:71], v251 offset:16384\n"
    "ds_read_b128 a[8:11], v250 offset:2048\n"
    "ds_read_b128 a[40:43], v250 offset:10240\n"
    "ds_read_b128 a[72:75], v250 offset:18432\n"
    "ds_read_b128 a[12:15], v251 offset:2048\n"
    "ds_read_b128 a[44:47], v251 offset:10240\n"
    "ds_read_b128 a[76:79], v251 offset:18432\n"
    "buffer_load_dword v255, v252, s[8:11], 0 offen offset:0 sc1\n"
    "ds_read_b128 a[16:19], v250 offset:4096\n"
    "ds_read_b128 a[48:51], v250 offset:12288\n"
    "ds_read_b128 a[80:83], v250 offset:20480\n"
    "ds_read_b128 a[20:23], v251 offset:4096\n"
    "ds_read_b128 a[52:55], v251 offset:12288\n"
    "ds_read_b128 a[84:87], v251 offset:20480\n"
    "ds_read_b128 a[24:27], v250 offset:6144\n"
    "ds_read_b128 a[56:59], v250 offset:14336\n"
    "ds_read_b128 a[88:91], v250 offset:22528\n"
    "ds_read_b128 a[28:31], v251 offset:6144\n"
    "ds_read_b128 a[60:63], v251 offset:14336\n"
    "ds_read_b128 a[92:95], v251 offset:22528\n"
    "s_lshl_b32 s52, %2, 15\n"
    "s_add_u32 s52, s52, 5242880\n"
    "s_lshl_b32 s61, s37, 12\n"
    "s_lshl_b32 s46, %1, 4\n"
    "s_lshl_b32 s47, %1, 7\n"
    "s_add_u32 s47, s47, 4096\n"
    "s_cmp_eq_u32 s50, 0\n"
    "s_cselect_b32 s46, s46, s47\n"
    "s_add_u32 s46, s46, 5701632\n"
    "s_cmp_eq_u32 s51, 0\n"
    "s_cselect_b32 s46, s46, 2.0\n"
    "v_mov_b32_e32 v40, s39\n"
    "v_mov_b32_e32 v41, 0\n"
    "s_waitcnt vmcnt(1)\n"
    "s_waitcnt lgkmcnt(0)\n"
    "s_barrier\n"
    "buffer_store_dword v40, v41, s[8:11], s46 offen offset:0 sc1\n"
    "s_waitcnt vmcnt(1)\n"
    "v_cmp_ne_u32_e32 vcc, s39, v255\n"
    "s_nop 4\n"
    "s_cbranch_vccnz .Lat_apoll\n"
    ".Lat_apoll_ret:\n"
    "s_add_u32 s47, s52, s61\n"
    "buffer_load_dwordx4 v[112:115], v253, s[8:11], s47 offen offset:0 sc1\n"
    "buffer_load_dwordx4 v[116:119], v253, s[8:11], s47 offen offset:1024 sc1\n"
    "buffer_load_dwordx4 v[128:131], v253, s[8:11], s47 offen offset:2048 sc1\n"
    "s_add_u32 s61, s61, 0x1000\n"
    "buffer_load_dwordx4 v[132:135], v253, s[8:11], s47 offen offset:3072 sc1\n"
    "s_bitset0_b32 s61, 14\n"
    "s_add_u32 s47, s52, s61\n"
    "buffer_load_dwordx4 v[144:147], v253, s[8:11], s47 offen offset:0 sc1\n"
    "buffer_load_dwordx4 v[148:151], v253, s[8:11], s47 offen offset:1024 sc1\n"
    "buffer_load_dwordx4 v[160:163], v253, s[8:11], s47 offen offset:2048 sc1\n"
    "s_add_u32 s61, s61, 0x1000\n"
    "buffer_load_dwordx4 v[164:167], v253, s[8:11], s47 offen offset:3072 sc1\n"
    "s_bitset0_b32 s61, 14\n"
    "s_add_u32 s47, s52, s61\n"
    "buffer_load_dwordx4 v[176:179], v253, s[8:11], s47 offen offset:0 sc1\n"
    "buffer_load_dwordx4 v[180:183], v253, s[8:11], s47 offen offset:1024 sc1\n"
    "buffer_load_dwordx4 v[192:195], v253, s[8:11], s47 offen offset:2048 sc1\n"
    "s_add_u32 s61, s61, 0x1000\n"
    "s_lshl_b32 s59, s32, 4\n"
    "s_lshr_b32 s48, s32, 1\n"
    "s_sub_u32 s59, s59, s48\n"
    "s_lshl_b32 s59, s59, 6\n"
    "s_sub_u32 s48, 3984, s59\n"
    "v_cmp_le_u32_e32 vcc, 0x40, v253\n"
    "v_mov_b32_e32 v0, s48\n"
    "s_cmp_eq_u32 s32, 2\n"
    "s_cselect_b32 s48, 1024, 0\n"
    "v_mov_b32_e32 v2, s48\n"
    "s_add_u32 s59, s59, 5701632\n"
    "v_cndmask_b32_e32 v3, v253, v0, vcc\n"
    "s_lshl_b32 s45, %2, 11\n"
    "s_add_u32 s48, s45, %3\n"
    "v_add_u32_e32 v9, s48, v253\n"
    "s_add_u32 s45, s45, 11022336\n"
    "v_add_u32_e32 v8, s45, v253\n"
    "v_lshrrev_b32_e32 v13, 7, v253\n"
    "v_and_b32_e32 v12, 0x70, v254\n"
    "v_lshl_add_u32 v12, v13, 9, v12\n"
    "v_add_u32_e32 v13, 0xc00, v12\n"
    "v_add_u32_e32 v14, 0x1800, v12\n"
    "v_add_u32_e32 v15, 0x2400, v12\n"
    "s_lshl_b32 s57, s50, 8\n"
    "s_add_u32 s57, s57, 5111808\n"
    "s_add_u32 s62, s43, s34\n"
    "s_xor_b32 s63, s34, 0xc000\n"
    "s_add_u32 s63, s63, s43\n"
    "s_waitcnt vmcnt(9)\n"
    "v_mfma_f32_16x16x32_bf16 v[24:27], v[112:115], a[0:3], 0\n"
    "buffer_load_dwordx4 v[196:199], v253, s[8:11], s47 offen offset:3072 sc1\n"
    "v_mfma_f32_16x16x32_bf16 v[28:31], v[116:119], a[0:3], 0\n"
    "s_bitset0_b32 s61, 14\n"
    "s_mov_b32 s18, 5120000\n"
    "s_mov_b32 s19, 0x20000\n"
    "v_mfma_f32_16x16x32_bf16 v[48:51], v[112:115], a[32:35], 0\n"
    "s_add_u32 s47, s52, s61\n"
    "v_xor_b32_e32 v242, 0x12000, v240\n"
    "v_xor_b32_e32 v243, 0x12000, v241\n"
    "v_mfma_f32_16x16x32_bf16 v[52:55], v[116:119], a[32:35], 0\n"
    "buffer_load_dwordx4 v[208:211], v253, s[8:11], s47 offen offset:0 sc1\n"
    "v_mfma_f32_16x16x32_bf16 v[64:67], v[112:115], a[64:67], 0\n"
    "s_cmp_eq_u32 %2, 3\n"
    "s_cselect_b32 s88, 0x2ff, -1\n"
    "v_mov_b32_e32 v10, 0xc2980000\n"
    "v_mfma_f32_16x16x32_bf16 v[68:71], v[116:119], a[64:67], 0\n"
    "s_waitcnt vmcnt(9)\n"
    "v_cmp_lt_u32_e32 vcc, s88, v253\n"
    "s_nop 1\n"
    "v_cndmask_b32_e32 v10, 0, v10, vcc\n"
    "v_mfma_f32_16x16x32_bf16 v[24:27], v[128:131], a[4:7], v[24:27]\n"
    "buffer_load_dwordx4 v[212:215], v253, s[8:11], s47 offen offset:1024 sc1\n"
    "v_mfma_f32_16x16x32_bf16 v[28:31], v[132:135], a[4:7], v[28:31]\n"
    "s_add_u32 s52, s52, 0x4000\n"
    "v_lshrrev_b32_e32 v5, 4, v253\n"
    "v_min_u32_e32 v5, 2, v5\n"
    "v_mfma_f32_16x16x32_bf16 v[48:51], v[128:131], a[36:39], v[48:51]\n"
    "s_add_u32 s88, s32, 1\n"
    "v_add_u32_e32 v5, s88, v5\n"
    "v_mov_b32_e32 v80, 0\n"
    "v_mfma_f32_16x16x32_bf16 v[52:55], v[132:135], a[36:39], v[52:55]\n"
    "buffer_load_dwordx4 v[224:227], v253, s[8:11], s47 offen offset:2048 sc1\n"
    "v_mfma_f32_16x16x32_bf16 v[64:67], v[128:131], a[68:71], v[64:67]\n"
    "s_add_u32 s61, s61, 0x1000\n"
    "v_and_b32_e32 v5, 3, v5\n"
    "v_lshlrev_b32_e32 v5, 7, v5\n"
    "v_mfma_f32_16x16x32_bf16 v[68:71], v[132:135], a[68:71], v[68:71]\n"
    "s_waitcnt vmcnt(9)\n"
    "s_lshl_b32 s58, %1, 7\n"
    "s_add_u32 s58, s58, 5738432\n"
    "s_cmp_eq_u32 %2, 2\n"
    "s_cselect_b32 s58, s58, 2.0\n"
    "v_mfma_f32_16x16x32_bf16 v[24:27], v[144:147], a[8:11], v[24:27]\n"
    "buffer_load_dwordx4 v[228:231], v253, s[8:11], s47 offen offset:3072 sc1\n"
    "v_mfma_f32_16x16x32_bf16 v[28:31], v[148:151], a[8:11], v[28:31]\n"
    "s_bitset0_b32 s61, 14\n"
    "s_lshl_b32 s88, s50, 13\n"
    "s_add_u32 s88, s88, %3\n"
    "v_mfma_f32_16x16x32_bf16 v[48:51], v[144:147], a[40:43], v[48:51]\n"
    "s_add_u32 s47, s52, s61\n"
    "v_add_u32_e32 v6, s88, v253\n"
    "v_add_u32_e32 v7, %3, v253\n"
    "v_mfma_f32_16x16x32_bf16 v[52:55], v[148:151], a[40:43], v[52:55]\n"
    "buffer_load_dwordx4 v[120:123], v253, s[8:11], s47 offen offset:0 sc1\n"
    "v_mfma_f32_16x16x32_bf16 v[64:67], v[144:147], a[72:75], v[64:67]\n"
    "s_mul_i32 s88, %2, 1024\n"
    "s_add_u32 s88, s88, %3\n"
    "v_lshrrev_b32_e32 v250, 2, v253\n"
    "v_mfma_f32_16x16x32_bf16 v[68:71], v[148:151], a[72:75], v[68:71]\n"
    "s_waitcnt vmcnt(9)\n"
    "v_add_u32_e32 v249, s88, v250\n"
    "s_mov_b32 s49, s88\n"
    "v_mfma_f32_16x16x32_bf16 v[24:27], v[160:163], a[12:15], v[24:27]\n"
    "buffer_load_dwordx4 v[124:127], v253, s[8:11], s47 offen offset:1024 sc1\n"
    "v_mfma_f32_16x16x32_bf16 v[28:31], v[164:167], a[12:15], v[28:31]\n"
    "s_lshl_b32 s81, s88, 2\n"
    "s_lshl2_add_u32 s90, %1, %2\n"
    "s_lshl_b32 s90, s90, 10\n"
    "v_mfma_f32_16x16x32_bf16 v[48:51], v[160:163], a[44:47], v[48:51]\n"
    "s_sub_u32 s81, s90, s81\n"
    "s_add_u32 s81, s81, 36369408\n"
    "s_xor_b32 s88, %2, 2\n"
    "v_mfma_f32_16x16x32_bf16 v[52:55], v[164:167], a[44:47], v[52:55]\n"
    "buffer_load_dwordx4 v[136:139], v253, s[8:11], s47 offen offset:2048 sc1\n"
    "v_mfma_f32_16x16x32_bf16 v[64:67], v[160:163], a[76:79], v[64:67]\n"
    "s_add_u32 s61, s61, 0x1000\n"
    "s_mul_i32 s88, s88, 1024\n"
    "s_add_u32 s88, s88, %3\n"
    "v_mfma_f32_16x16x32_bf16 v[68:71], v[164:167], a[76:79], v[68:71]\n"
    "s_waitcnt vmcnt(9)\n"
    "v_add_u32_e32 v250, s88, v250\n"
    "v_and_b32_e32 v251, 0x1f0, v253\n"
    "v_mfma_f32_16x16x32_bf16 v[24:27], v[176:179], a[16:19], v[24:27]\n"
    "buffer_load_dwordx4 v[140:143], v253, s[8:11], s47 offen offset:3072 sc1\n"
    "v_mfma_f32_16x16x32_bf16 v[28:31], v[180:183], a[16:19], v[28:31]\n"
    "s_bitset0_b32 s61, 14\n"
    "s_lshl_b32 s82, %2, 10\n"
    "s_add_u32 s82, s82, %3\n"
    "v_mfma_f32_16x16x32_bf16 v[48:51], v[176:179], a[48:51], v[48:51]\n"
    "s_add_u32 s47, s52, s61\n"
    "s_add_u32 s82, s82, 20480\n"
    "v_lshrrev_b32_e32 v252, 9, v253\n"
    "v_mfma_f32_16x16x32_bf16 v[52:55], v[180:183], a[48:51], v[52:55]\n"
    "buffer_load_dwordx4 v[152:155], v253, s[8:11], s47 offen offset:0 sc1\n"
    "v_mfma_f32_16x16x32_bf16 v[64:67], v[176:179], a[80:83], v[64:67]\n"
    "s_lshr_b32 s80, %1, 4\n"
    "s_lshl_b32 s80, s80, 7\n"
    "v_lshl_add_u32 v252, v252, 4, s82\n"
    "v_mfma_f32_16x16x32_bf16 v[68:71], v[180:183], a[80:83], v[68:71]\n"
    "s_waitcnt vmcnt(9)\n"
    "s_add_u32 s80, s80, 5775616\n"
    "s_and_b32 s88, s32, 1\n"
    "v_mfma_f32_16x16x32_bf16 v[24:27], v[192:195], a[20:23], v[24:27]\n"
    "buffer_load_dwordx4 v[156:159], v253, s[8:11], s47 offen offset:1024 sc1\n"
    "v_mfma_f32_16x16x32_bf16 v[28:31], v[196:199], a[20:23], v[28:31]\n"
    "s_lshl_b32 s88, s88, 6\n"
    "s_add_u32 s80, s80, s88\n"
    "s_mov_b64 s[20:21], s[8:9]\n"
    "v_mfma_f32_16x16x32_bf16 v[48:51], v[192:195], a[52:55], v[48:51]\n"
    "s_mov_b32 s22, 5242880\n"
    "s_add_u32 s24, s8, 5779456\n"
    "s_addc_u32 s25, s9, 0\n"
    "v_mfma_f32_16x16x32_bf16 v[52:55], v[196:199], a[52:55], v[52:55]\n"
    "buffer_load_dwordx4 v[168:171], v253, s[8:11], s47 offen offset:2048 sc1\n"
    "v_mfma_f32_16x16x32_bf16 v[64:67], v[192:195], a[84:87], v[64:67]\n"
    "s_add_u32 s61, s61, 0x1000\n"
    "s_mov_b32 s26, 5242880\n"
    "s_movk_i32 s42, 512\n"
    "v_mfma_f32_16x16x32_bf16 v[68:71], v[196:199], a[84:87], v[68:71]\n"
    "s_waitcnt vmcnt(9)\n"
    "s_mov_b32 s37, 0\n"
    "s_add_u32 s38, s38, s50\n"
    "v_mfma_f32_16x16x32_bf16 v[24:27], v[208:211], a[24:27], v[24:27]\n"
    "buffer_load_dwordx4 v[172:175], v253, s[8:11], s47 offen offset:3072 sc1\n"
    "v_mfma_f32_16x16x32_bf16 v[28:31], v[212:215], a[24:27], v[28:31]\n"
    "s_bitset0_b32 s61, 14\n"
    "v_bfe_u32 v16, v253, 4, 2\n"
    "v_bfe_u32 v17, v253, 6, 2\n"
    "v_mov_b32_e32 v18, 0x3f80\n"
    "v_mfma_f32_16x16x32_bf16 v[48:51], v[208:211], a[56:59], v[48:51]\n"
    "s_add_u32 s47, s52, s61\n"
    "v_cmp_eq_u32_e32 vcc, 3, v16\n"
    "s_nop 1\n"
    "v_cndmask_b32_e64 v18, v18, 1.0, vcc\n"
    "v_cndmask_b32_e64 v16, v16, 0, vcc\n"
    "v_mfma_f32_16x16x32_bf16 v[52:55], v[212:215], a[56:59], v[52:55]\n"
    "buffer_load_dwordx4 v[184:187], v253, s[8:11], s47 offen offset:0 sc1\n"
    "v_mfma_f32_16x16x32_bf16 v[64:67], v[208:211], a[88:91], v[64:67]\n"
    "v_or_b32_e32 v16, 12, v16\n"
    "v_mul_u32_u24_e32 v16, 0x1111, v16\n"
    "v_mfma_f32_16x16x32_bf16 v[68:71], v[212:215], a[88:91], v[68:71]\n"
    "s_waitcnt vmcnt(9)\n"
    "v_cmp_eq_u32_e32 vcc, 0, v17\n"
    "s_nop 1\n"
    "v_cndmask_b32_e32 v19, 0, v18, vcc\n"
    "v_mfma_f32_16x16x32_bf16 v[24:27], v[224:227], a[28:31], v[24:27]\n"
    "buffer_load_dwordx4 v[188:191], v253, s[8:11], s47 offen offset:1024 sc1\n"
    "v_mfma_f32_16x16x32_bf16 v[28:31], v[228:231], a[28:31], v[28:31]\n"
    "v_accvgpr_write_b32 a192, v19\n"
    "v_mov_b32_e32 v81, 0\n"
    "v_mfma_f32_16x16x32_bf16 v[48:51], v[224:227], a[60:63], v[48:51]\n"
    "v_cmp_eq_u32_e32 vcc, 1, v17\n"
    "s_nop 1\n"
    "v_cndmask_b32_e32 v19, 0, v18, vcc\n"
    "v_mfma_f32_16x16x32_bf16 v[52:55], v[228:231], a[60:63], v[52:55]\n"
    "buffer_load_dwordx4 v[200:203], v253, s[8:11], s47 offen offset:2048 sc1\n"
    "v_mfma_f32_16x16x32_bf16 v[64:67], v[224:227], a[92:95], v[64:67]\n"
    "s_add_u32 s61, s61, 0x1000\n"
    "v_cvt_pk_bf16_f32 v24, v24, v25\n"
    "v_mov_b32_e32 v82, 0\n"
    "v_mfma_f32_16x16x32_bf16 v[68:71], v[228:231], a[92:95], v[68:71]\n"
    "s_waitcnt vmcnt(9)\n"
    "v_cvt_pk_bf16_f32 v25, v26, v27\n"
    "v_accvgpr_write_b32 a193, v19\n"
    "v_mfma_f32_16x16x32_bf16 v[32:35], v[120:123], a[0:3], 0\n"
    "buffer_load_dwordx4 v[204:207], v253, s[8:11], s47 offen offset:3072 sc1\n"
    "v_mfma_f32_16x16x32_bf16 v[36:39], v[124:127], a[0:3], 0\n"
    "s_bitset0_b32 s61, 14\n"
    "v_cvt_pk_bf16_f32 v26, v28, v29\n"
    "v_cmp_eq_u32_e32 vcc, 2, v17\n"
    "s_nop 1\n"
    "v_cndmask_b32_e32 v19, 0, v18, vcc\n"
    "v_mfma_f32_16x16x32_bf16 v[56:59], v[120:123], a[32:35], 0\n"
    "s_add_u32 s47, s52, s61\n"
    "v_cvt_pk_bf16_f32 v27, v30, v31\n"
    "v_accvgpr_write_b32 a194, v19\n"
    "v_mfma_f32_16x16x32_bf16 v[60:63], v[124:127], a[32:35], 0\n"
    "buffer_load_dwordx4 v[216:219], v253, s[8:11], s47 offen offset:0 sc1\n"
    "v_mfma_f32_16x16x32_bf16 v[72:75], v[120:123], a[64:67], 0\n"
    "buffer_store_dwordx4 v[24:27], v8, s[8:11], s35 offen offset:0 sc1\n"
    "v_cvt_pk_bf16_f32 v48, v48, v49\n"
    "v_cmp_eq_u32_e32 vcc, 3, v17\n"
    "s_nop 1\n"
    "v_cndmask_b32_e32 v19, 0, v18, vcc\n"
    "v_mfma_f32_16x16x32_bf16 v[76:79], v[124:127], a[64:67], 0\n"
    "s_waitcnt vmcnt(10)\n"
    "v_cvt_pk_bf16_f32 v49, v50, v51\n"
    "v_accvgpr_write_b32 a195, v19\n"
    "v_mfma_f32_16x16x32_bf16 v[32:35], v[136:139], a[4:7], v[32:35]\n"
    "buffer_load_dwordx4 v[220:223], v253, s[8:11], s47 offen offset:1024 sc1\n"
    "v_mfma_f32_16x16x32_bf16 v[36:39], v[140:143], a[4:7], v[36:39]\n"
    "s_add_u32 s52, s52, 0x4000\n"
    "v_cvt_pk_bf16_f32 v50, v52, v53\n"
    "v_cvt_pk_bf16_f32 v51, v54, v55\n"
    "v_mfma_f32_16x16x32_bf16 v[56:59], v[136:139], a[36:39], v[56:59]\n"
    "buffer_load_dword v0, v3, s[8:11], s59 offen offset:0 sc1\n"
    "v_mfma_f32_16x16x32_bf16 v[60:63], v[140:143], a[36:39], v[60:63]\n"
    "buffer_load_dwordx4 v[232:235], v253, s[8:11], s47 offen offset:2048 sc1\n"
    "buffer_store_dwordx4 v[48:51], v8, s[8:11], s54 offen offset:0 sc1\n"
    "v_mov_b32_e32 v83, 0\n"
    "v_mfma_f32_16x16x32_bf16 v[72:75], v[136:139], a[68:71], v[72:75]\n"
    "s_add_u32 s61, s61, 0x1000\n"
    "v_cvt_pk_bf16_f32 v64, v64, v65\n"
    "v_mov_b32_e32 v84, 0\n"
    "v_mfma_f32_16x16x32_bf16 v[76:79], v[140:143], a[68:71], v[76:79]\n"
    "s_waitcnt vmcnt(12)\n"
    "v_cvt_pk_bf16_f32 v65, v66, v67\n"
    "v_mov_b32_e32 v85, 0\n"
    "v_mfma_f32_16x16x32_bf16 v[32:35], v[152:155], a[8:11], v[32:35]\n"
    "buffer_load_dwordx4 v[236:239], v253, s[8:11], s47 offen offset:3072 sc1\n"
    "v_mfma_f32_16x16x32_bf16 v[36:39], v[156:159], a[8:11], v[36:39]\n"
    "s_bitset0_b32 s61, 14\n"
    "v_cvt_pk_bf16_f32 v66, v68, v69\n"
    "v_mov_b32_e32 v86, 0\n"
    "v_mfma_f32_16x16x32_bf16 v[56:59], v[152:155], a[40:43], v[56:59]\n"
    "s_add_u32 s28, s38, 2\n"
    "v_cvt_pk_bf16_f32 v67, v70, v71\n"
    "v_mov_b32_e32 v87, 0\n"
    "v_mfma_f32_16x16x32_bf16 v[60:63], v[156:159], a[40:43], v[60:63]\n"
    "buffer_store_dwordx4 v[64:67], v8, s[8:11], s60 offen offset:0 sc1\n"
    "v_mov_b32_e32 v88, 0\n"
    "v_mov_b32_e32 v89, 0\n"
    "v_mfma_f32_16x16x32_bf16 v[72:75], v[152:155], a[72:75], v[72:75]\n"
    "s_sub_u32 s88, s28, 10\n"
    "s_min_u32 s28, s28, s88\n"
    "v_mov_b32_e32 v90, 0\n"
    "v_mfma_f32_16x16x32_bf16 v[76:79], v[156:159], a[72:75], v[76:79]\n"
    "s_waitcnt vmcnt(12)\n"
    "s_lshl_b32 s88, s28, 0\n"
    "v_mov_b32_e32 v91, 0\n"
    "v_mfma_f32_16x16x32_bf16 v[32:35], v[168:171], a[12:15], v[32:35]\n"
    "s_or_b32 s36, s88, s88\n"
    "s_lshl1_add_u32 s88, s28, 1\n"
    "v_mov_b32_e32 v92, 0\n"
    "v_mfma_f32_16x16x32_bf16 v[36:39], v[172:175], a[12:15], v[36:39]\n"
    "s_mul_i32 s88, s88, 52\n"
    "s_lshr_b32 s88, s88, 8\n"
    "v_mov_b32_e32 v93, 0\n"
    "v_mfma_f32_16x16x32_bf16 v[56:59], v[168:171], a[44:47], v[56:59]\n"
    "s_mul_i32 s90, s88, 5\n"
    "ds_write_b128 v9, v[24:27] offset:0\n"
    "v_mfma_f32_16x16x32_bf16 v[60:63], v[172:175], a[44:47], v[60:63]\n"
    "s_lshr_b32 s90, s90, 1\n"
    "s_sub_u32 s28, s28, s90\n"
    "v_mov_b32_e32 v94, 0\n"
    "v_mfma_f32_16x16x32_bf16 v[72:75], v[168:171], a[76:79], v[72:75]\n"
    "s_sub_u32 s90, s32, s88\n"
    "s_sub_u32 s90, s90, 1\n"
    "v_mov_b32_e32 v95, 0\n"
    "v_mfma_f32_16x16x32_bf16 v[76:79], v[172:175], a[76:79], v[76:79]\n"
    "s_waitcnt vmcnt(10)\n"
    "s_and_b32 s90, s90, 3\n"
    "v_mov_b32_e32 v96, 0\n"
    "v_mfma_f32_16x16x32_bf16 v[32:35], v[184:187], a[16:19], v[32:35]\n"
    "s_lshl2_add_u32 s88, s33, s88\n"
    "s_mul_i32 s88, s88, 3\n"
    "v_mov_b32_e32 v97, 0\n"
    "v_mfma_f32_16x16x32_bf16 v[36:39], v[188:191], a[16:19], v[36:39]\n"
    "s_add_u32 s88, s88, s90\n"
    "v_mov_b32_e32 v98, 0\n"
    "v_mov_b32_e32 v99, 0\n"
    "v_mfma_f32_16x16x32_bf16 v[56:59], v[184:187], a[48:51], v[56:59]\n"
    "s_mul_i32 s88, s88, 24832\n"
    "s_add_u32 s88, s88, 16265216\n"
    "v_mov_b32_e32 v100, 0\n"
    "v_mfma_f32_16x16x32_bf16 v[60:63], v[188:191], a[48:51], v[60:63]\n"
    "s_cmp_eq_u32 s90, 3\n"
    "s_cselect_b32 s88, 2.0, s88\n"
    "s_cselect_b32 s89, 2, 3\n"
    "v_mfma_f32_16x16x32_bf16 v[72:75], v[184:187], a[80:83], v[72:75]\n"
    "s_lshl1_add_u32 s83, s28, s51\n"
    "s_lshl_b32 s83, s83, 12\n"
    "v_mov_b32_e32 v101, 0\n"
    "v_mfma_f32_16x16x32_bf16 v[76:79], v[188:191], a[80:83], v[76:79]\n"
    "s_waitcnt vmcnt(8)\n"
    "ds_write_b128 v9, v[48:51] offset:8192\n"
    "v_mfma_f32_16x16x32_bf16 v[32:35], v[200:203], a[20:23], v[32:35]\n"
    "s_add_u32 s83, s83, s88\n"
    "s_lshl2_add_u32 s28, s28, s88\n"
    "v_mov_b32_e32 v102, 0\n"
    "v_mfma_f32_16x16x32_bf16 v[36:39], v[204:207], a[20:23], v[36:39]\n"
    "s_add_u32 s29, s38, 4\n"
    "s_sub_u32 s88, s29, 10\n"
    "v_mov_b32_e32 v103, 0\n"
    "v_mfma_f32_16x16x32_bf16 v[56:59], v[200:203], a[52:55], v[56:59]\n"
    "s_min_u32 s29, s29, s88\n"
    "s_lshl_b32 s88, s29, 4\n"
    "v_mov_b32_e32 v104, 0\n"
    "v_mfma_f32_16x16x32_bf16 v[60:63], v[204:207], a[52:55], v[60:63]\n"
    "s_or_b32 s36, s88, s36\n"
    "s_lshl1_add_u32 s88, s29, 1\n"
    "v_mov_b32_e32 v105, 0\n"
    "v_mfma_f32_16x16x32_bf16 v[72:75], v[200:203], a[84:87], v[72:75]\n"
    "s_waitcnt vmcnt(4)\n"
    "v_cmp_ne_u32_e32 vcc, s39, v0\n"
    "v_mov_b32_e32 v106, 0\n"
    "v_mfma_f32_16x16x32_bf16 v[76:79], v[204:207], a[84:87], v[76:79]\n"
    "s_waitcnt vmcnt(5)\n"
    "s_mul_i32 s88, s88, 52\n"
    "v_mov_b32_e32 v107, 0\n"
    "v_mfma_f32_16x16x32_bf16 v[32:35], v[216:219], a[24:27], v[32:35]\n"
    "s_lshr_b32 s88, s88, 8\n"
    "s_mul_i32 s90, s88, 5\n"
    "v_mfma_f32_16x16x32_bf16 v[36:39], v[220:223], a[24:27], v[36:39]\n"
    "s_lshr_b32 s90, s90, 1\n"
    "ds_write_b128 v9, v[64:67] offset:16384\n"
    "v_mfma_f32_16x16x32_bf16 v[56:59], v[216:219], a[56:59], v[56:59]\n"
    "s_cbranch_vccnz .Lat_h1poll\n"
    ".Lat_h1poll_ret:\n"
    "v_mfma_f32_16x16x32_bf16 v[60:63], v[220:223], a[56:59], v[60:63]\n"
    "s_mov_b32 m0, s63\n"
    "s_sub_u32 s29, s29, s90\n"
    "v_mov_b32_e32 v108, 0\n"
    "v_mfma_f32_16x16x32_bf16 v[72:75], v[216:219], a[88:91], v[72:75]\n"
    "buffer_load_dwordx4 v254, s[8:11], s57 offen offset:0 sc1 lds\n"
    "v_mfma_f32_16x16x32_bf16 v[76:79], v[220:223], a[88:91], v[76:79]\n"
    "s_waitcnt vmcnt(2)\n"
    "s_sub_u32 m0, s63, 1024\n"
    "v_mov_b32_e32 v109, 0\n"
    "v_mfma_f32_16x16x32_bf16 v[32:35], v[232:235], a[28:31], v[32:35]\n"
    "buffer_load_dwordx4 v254, s[8:11], s57 offen offset:2048 sc1 lds\n"
    "v_mfma_f32_16x16x32_bf16 v[36:39], v[236:239], a[28:31], v[36:39]\n"
    "s_add_u32 m0, s63, 3968\n"
    "s_sub_u32 s90, s32, s88\n"
    "v_mov_b32_e32 v110, 0\n"
    "v_mfma_f32_16x16x32_bf16 v[56:59], v[232:235], a[60:63], v[56:59]\n"
    "buffer_load_dwordx4 v254, s[8:11], s57 offen offset:128 sc1 lds\n"
    "v_mfma_f32_16x16x32_bf16 v[60:63], v[236:239], a[60:63], v[60:63]\n"
    "s_add_u32 m0, s63, 2944\n"
    "s_sub_u32 s90, s90, 1\n"
    "s_and_b32 s90, s90, 3\n"
    "v_mfma_f32_16x16x32_bf16 v[72:75], v[232:235], a[92:95], v[72:75]\n"
    "buffer_load_dwordx4 v254, s[8:11], s57 offen offset:2176 sc1 lds\n"
    "v_mfma_f32_16x16x32_bf16 v[76:79], v[236:239], a[92:95], v[76:79]\n"
    "s_lshl2_add_u32 s88, s33, s88\n"
    "s_mul_i32 s88, s88, 3\n"
    "v_mov_b32_e32 v111, 0\n"
    "v_cvt_pk_bf16_f32 v32, v32, v33\n"
    "v_cvt_pk_bf16_f32 v33, v34, v35\n"
    "v_cvt_pk_bf16_f32 v34, v36, v37\n"
    "v_cvt_pk_bf16_f32 v35, v38, v39\n"
    "v_or_b32_e32 v35, v10, v35\n"
    "ds_write_b128 v9, v[32:35] offset:1024\n"
    "buffer_store_dwordx4 v[32:35], v8, s[8:11], s35 offen offset:1024 sc1\n"
    "v_cvt_pk_bf16_f32 v56, v56, v57\n"
    "v_cvt_pk_bf16_f32 v57, v58, v59\n"
    "v_cvt_pk_bf16_f32 v58, v60, v61\n"
    "v_cvt_pk_bf16_f32 v59, v62, v63\n"
    "v_or_b32_e32 v59, v10, v59\n"
    "ds_write_b128 v9, v[56:59] offset:9216\n"
    "buffer_store_dwordx4 v[56:59], v8, s[8:11], s54 offen offset:1024 sc1\n"
    "v_cvt_pk_bf16_f32 v72, v72, v73\n"
    "v_cvt_pk_bf16_f32 v73, v74, v75\n"
    "v_cvt_pk_bf16_f32 v74, v76, v77\n"
    "v_cvt_pk_bf16_f32 v75, v78, v79\n"
    "v_or_b32_e32 v75, v10, v75\n"
    "ds_write_b128 v9, v[72:75] offset:17408\n"
    "buffer_store_dwordx4 v[72:75], v8, s[8:11], s60 offen offset:1024 sc1\n"
    "v_mov_b32_e32 v112, 0\n"
    "v_mov_b32_e32 v113, 0\n"
    "v_mov_b32_e32 v114, 0\n"
    "s_add_u32 m0, s62, 0\n"
    "v_mov_b32_e32 v115, 0\n"
    "v_mov_b32_e32 v116, 0\n"
    "v_mov_b32_e32 v117, 0\n"
    "buffer_load_dwordx4 v12, s[8:11], s40 offen offset:0 sc1 lds\n"
    "v_mov_b32_e32 v118, 0\n"
    "v_mov_b32_e32 v119, 0\n"
    "v_mov_b32_e32 v120, 0\n"
    "buffer_load_dwordx4 v13, s[8:11], s40 offen offset:1024 sc1 lds\n"
    "v_mov_b32_e32 v121, 0\n"
    "v_mov_b32_e32 v122, 0\n"
    "v_mov_b32_e32 v123, 0\n"
    "buffer_load_dwordx4 v14, s[8:11], s40 offen offset:2048 sc1 lds\n"
    "v_mov_b32_e32 v124, 0\n"
    "v_mov_b32_e32 v125, 0\n"
    "v_mov_b32_e32 v126, 0\n"
    "buffer_load_dwordx4 v15, s[8:11], s40 offen offset:3072 sc1 lds\n"
    "v_mov_b32_e32 v127, 0\n"
    "v_mov_b32_e32 v128, 0\n"
    "v_mov_b32_e32 v129, 0\n"
    "s_add_u32 s40, s40, 0x8000\n"
    "v_mov_b32_e32 v130, 0\n"
    "v_mov_b32_e32 v131, 0\n"
    "v_mov_b32_e32 v132, 0\n"
    "s_add_u32 m0, s62, 4096\n"
    "v_mov_b32_e32 v133, 0\n"
    "v_mov_b32_e32 v134, 0\n"
    "v_mov_b32_e32 v135, 0\n"
    "buffer_load_dwordx4 v12, s[8:11], s41 offen offset:0 sc1 lds\n"
    "v_mov_b32_e32 v136, 0\n"
    "v_mov_b32_e32 v137, 0\n"
    "v_mov_b32_e32 v138, 0\n"
    "buffer_load_dwordx4 v13, s[8:11], s41 offen offset:1024 sc1 lds\n"
    "v_mov_b32_e32 v139, 0\n"
    "v_mov_b32_e32 v140, 0\n"
    "v_mov_b32_e32 v141, 0\n"
    "buffer_load_dwordx4 v14, s[8:11], s41 offen offset:2048 sc1 lds\n"
    "v_mov_b32_e32 v142, 0\n"
    "v_mov_b32_e32 v143, 0\n"
    "v_mov_b32_e32 v144, 0\n"
    "buffer_load_dwordx4 v15, s[8:11], s41 offen offset:3072 sc1 lds\n"
    "v_mov_b32_e32 v145, 0\n"
    "v_mov_b32_e32 v146, 0\n"
    "v_mov_b32_e32 v147, 0\n"
    "s_add_u32 s41, s41, 0x8000\n"
    "v_mov_b32_e32 v148, 0\n"
    "v_mov_b32_e32 v149, 0\n"
    "v_mov_b32_e32 v150, 0\n"
    "v_mov_b32_e32 v151, 0\n"
    "s_xor_b32 s45, s34, 0xc000\n"
    "v_add_u32_e32 v204, s45, v240\n"
    "v_add_u32_e32 v205, s45, v241\n"
    "v_add_u32_e32 v206, s45, v242\n"
    "v_add_u32_e32 v207, s45, v243\n"
    "v_add_u32_e32 v208, s45, v244\n"
    "v_add_u32_e32 v209, s45, v245\n"
    "v_add_u32_e32 v210, s45, v246\n"
    "buffer_load_dword v0, v253, s[8:11], s59 offen offset:0 sc1\n"
    "buffer_load_dword v1, v2, s[8:11], s59 offen offset:0 sc1\n"
    "v_add_u32_e32 v211, s45, v247\n"
    "v_add_u32_e32 v200, s34, v240\n"
    "v_add_u32_e32 v201, s34, v241\n"
    "v_add_u32_e32 v202, s34, v242\n"
    "v_add_u32_e32 v203, s34, v243\n"
    "v_mov_b32_e32 v152, v16\n"
    "v_and_b32_e32 v153, 0xf0, v253\n"
    "v_lshrrev_b32_e32 v154, 9, v253\n"
    "v_lshl_add_u32 v153, v154, 8, v153\n"
    "v_bfe_u32 v154, v253, 8, 1\n"
    "v_lshl_add_u32 v153, v154, 3, v153\n"
    "v_mov_b32_e32 v156, 0\n"
    "v_mov_b32_e32 v157, 0\n"
    "v_mov_b32_e32 v158, 0\n"
    "v_mov_b32_e32 v159, 0\n"
    "v_mov_b32_e32 v56, 0\n"
    "v_mov_b32_e32 v57, 0\n"
    "v_mov_b32_e32 v58, 0\n"
    "v_mov_b32_e32 v59, 0\n"
    "v_mov_b32_e32 v60, 0\n"
    "v_mov_b32_e32 v61, 0\n"
    "v_mov_b32_e32 v62, 0\n"
    "v_mov_b32_e32 v63, 0\n"
    "v_mov_b32_e32 v64, 0\n"
    "v_mov_b32_e32 v65, 0\n"
    "v_mov_b32_e32 v66, 0\n"
    "v_mov_b32_e32 v67, 0\n"
    "v_mov_b32_e32 v68, 0\n"
    "v_mov_b32_e32 v69, 0\n"
    "v_mov_b32_e32 v70, 0\n"
    "v_mov_b32_e32 v71, 0\n"
    "v_mov_b32_e32 v72, 0\n"
    "v_mov_b32_e32 v73, 0\n"
    "v_mov_b32_e32 v74, 0\n"
    "v_mov_b32_e32 v75, 0\n"
    "v_mov_b32_e32 v76, 0\n"
    "v_mov_b32_e32 v77, 0\n"
    "v_mov_b32_e32 v78, 0\n"
    "v_mov_b32_e32 v79, 0\n"
    "s_add_u32 s88, s88, s90\n"
    "s_mul_i32 s88, s88, 24832\n"
    "s_add_u32 s88, s88, 16265216\n"
    "s_lshl1_add_u32 s84, s29, s51\n"
    "s_lshl_b32 s84, s84, 12\n"
    "s_add_u32 s84, s84, s88\n"
    "s_lshl2_add_u32 s29, s29, s88\n"
    "s_add_u32 s30, s38, 6\n"
    "s_sub_u32 s88, s30, 10\n"
    "s_min_u32 s30, s30, s88\n"
    "s_lshl_b32 s88, s30, 8\n"
    "s_or_b32 s36, s88, s36\n"
    "s_lshl1_add_u32 s88, s30, 1\n"
    "s_mul_i32 s88, s88, 52\n"
    "s_lshr_b32 s88, s88, 8\n"
    "s_mul_i32 s90, s88, 5\n"
    "s_lshr_b32 s90, s90, 1\n"
    "s_sub_u32 s30, s30, s90\n"
    "s_sub_u32 s90, s32, s88\n"
    "s_sub_u32 s90, s90, 1\n"
    "s_and_b32 s90, s90, 3\n"
    "s_lshl2_add_u32 s88, s33, s88\n"
    "s_mul_i32 s88, s88, 3\n"
    "s_add_u32 s88, s88, s90\n"
    "s_mul_i32 s88, s88, 24832\n"
    "s_add_u32 s88, s88, 16265216\n"
    "s_lshl1_add_u32 s85, s30, s51\n"
    "s_lshl_b32 s85, s85, 12\n"
    "s_add_u32 s85, s85, s88\n"
    "s_lshl2_add_u32 s30, s30, s88\n"
    "s_add_u32 s31, s38, 8\n"
    "s_sub_u32 s88, s31, 10\n"
    "s_min_u32 s31, s31, s88\n"
    "s_lshl_b32 s88, s31, 12\n"
    "s_or_b32 s36, s88, s36\n"
    "s_lshl1_add_u32 s88, s31, 1\n"
    "s_mul_i32 s88, s88, 52\n"
    "s_lshr_b32 s88, s88, 8\n"
    "s_mul_i32 s90, s88, 5\n"
    "s_lshr_b32 s90, s90, 1\n"
    "s_sub_u32 s31, s31, s90\n"
    "s_sub_u32 s90, s32, s88\n"
    "s_sub_u32 s90, s90, 1\n"
    "s_and_b32 s90, s90, 3\n"
    "s_lshl2_add_u32 s88, s33, s88\n"
    "s_mul_i32 s88, s88, 3\n"
    "s_add_u32 s88, s88, s90\n"
    "s_mul_i32 s88, s88, 24832\n"
    "s_add_u32 s88, s88, 16265216\n"
    "s_lshl1_add_u32 s86, s31, s51\n"
    "s_lshl_b32 s86, s86, 12\n"
    "s_add_u32 s86, s86, s88\n"
    "s_lshl2_add_u32 s31, s31, s88\n"
    "s_lshl_b32 s87, %1, 6\n"
    "s_add_u32 s87, s87, %2\n"
    "s_lshl2_add_u32 s87, s87, 5636096\n"
    "s_or_b32 s88, %1, %2\n"
    "s_cselect_b32 s88, 2.0, 5777664\n"
    "s_lshl_b32 s47, s33, 9\n"
    "s_add_u32 s47, s47, 5738496\n"
    "v_lshlrev_b32_e32 v9, 3, v253\n"
    "s_cmp_eq_u32 s50, 0\n"
    "s_cselect_b32 s46, s35, s54\n"
    "s_cselect_b32 s45, s60, 2.0\n"
    "s_cmp_eq_u32 s51, 0\n"
    "s_cselect_b32 s46, s46, s45\n"
    "s_add_u32 s46, s46, 11022336\n"
    "s_waitcnt vmcnt(10)\n"
    "s_waitcnt lgkmcnt(0)\n"
    "s_barrier\n"
    "buffer_store_dword v40, v41, s[8:11], s58 offen offset:64 sc1\n"
    "buffer_load_dword v228, v9, s[8:11], s46 offen offset:0 sc1\n"
    "ds_read_b128 a[0:3], v6 offset:0\n"
    "ds_read_b128 a[4:7], v6 offset:1024\n"
    "ds_read_b128 a[8:11], v6 offset:2048\n"
    "ds_read_b128 a[12:15], v6 offset:3072\n"
    "ds_read_b128 a[16:19], v6 offset:4096\n"
    "ds_read_b128 a[20:23], v6 offset:5120\n"
    "ds_read_b128 a[24:27], v6 offset:6144\n"
    "ds_read_b128 a[28:31], v6 offset:7168\n"
    "s_waitcnt vmcnt(4)\n"
    "s_barrier\n"
    "ds_read_b128 a[160:163], v204 offset:0\n"
    "ds_read_b128 a[164:167], v205 offset:0\n"
    "ds_read_b128 a[168:171], v204 offset:4096\n"
    "ds_read_b128 a[172:175], v205 offset:4096\n"
    "ds_read_b128 a[176:179], v206 offset:0\n"
    "ds_read_b128 a[180:183], v207 offset:0\n"
    "ds_read_b128 a[184:187], v206 offset:4096\n"
    "ds_read_b128 a[188:191], v207 offset:4096\n"
    "s_add_u32 m0, s62, 16384\n"
    "s_nop 0\n"
    "buffer_load_dwordx4 v12, s[8:11], s40 offen offset:0 sc1 lds\n"
    "buffer_load_dwordx4 v13, s[8:11], s40 offen offset:1024 sc1 lds\n"
    "buffer_load_dwordx4 v14, s[8:11], s40 offen offset:2048 sc1 lds\n"
    "buffer_load_dwordx4 v15, s[8:11], s40 offen offset:3072 sc1 lds\n"
    "s_add_u32 s40, s40, 0x8000\n"
    "s_add_u32 m0, s62, 20480\n"
    "s_nop 0\n"
    "buffer_load_dwordx4 v12, s[8:11], s41 offen offset:0 sc1 lds\n"
    "buffer_load_dwordx4 v13, s[8:11], s41 offen offset:1024 sc1 lds\n"
    "buffer_load_dwordx4 v14, s[8:11], s41 offen offset:2048 sc1 lds\n"
    "buffer_load_dwordx4 v15, s[8:11], s41 offen offset:3072 sc1 lds\n"
    "s_add_u32 s41, s41, 0x8000\n"
    "s_waitcnt lgkmcnt(4)\n"
    "v_mfma_f32_16x16x32_bf16 v[220:223], a[160:163], a[0:3], 0\n"
    "ds_read_b128 a[32:35], v7 offset:16384\n"
    "ds_read_b128 a[36:39], v7 offset:17408\n"
    "v_mfma_f32_16x16x32_bf16 v[220:223], a[164:167], a[4:7], v[220:223]\n"
    "ds_read_b128 a[40:43], v7 offset:18432\n"
    "ds_read_b128 a[44:47], v7 offset:19456\n"
    "v_mfma_f32_16x16x32_bf16 v[220:223], a[168:171], a[8:11], v[220:223]\n"
    "ds_read_b128 a[48:51], v7 offset:20480\n"
    "ds_read_b128 a[52:55], v7 offset:21504\n"
    "v_mfma_f32_16x16x32_bf16 v[220:223], a[172:175], a[12:15], v[220:223]\n"
    "ds_read_b128 a[56:59], v7 offset:22528\n"
    "ds_read_b128 a[60:63], v7 offset:23552\n"
    "s_waitcnt lgkmcnt(8)\n"
    "v_mfma_f32_16x16x32_bf16 v[220:223], a[176:179], a[16:19], v[220:223]\n"
    "ds_read_b64_tr_b16 a[232:233], v208 offset:0\n"
    "ds_read_b64_tr_b16 a[234:235], v209 offset:0\n"
    "v_mfma_f32_16x16x32_bf16 v[220:223], a[180:183], a[20:23], v[220:223]\n"
    "ds_read_b64_tr_b16 a[236:237], v210 offset:0\n"
    "ds_read_b64_tr_b16 a[238:239], v211 offset:0\n"
    "v_mfma_f32_16x16x32_bf16 v[220:223], a[184:187], a[24:27], v[220:223]\n"
    "ds_read_b64_tr_b16 a[240:241], v208 offset:4096\n"
    "ds_read_b64_tr_b16 a[242:243], v209 offset:4096\n"
    "v_mfma_f32_16x16x32_bf16 v[220:223], a[188:191], a[28:31], v[220:223]\n"
    "buffer_load_dword v11, v5, s[8:11], s47 offen offset:0 sc1\n"
    "ds_read_b64_tr_b16 a[244:245], v210 offset:4096\n"
    "s_waitcnt lgkmcnt(11)\n"
    "v_mfma_f32_16x16x32_bf16 v[224:227], a[160:163], a[32:35], 0\n"
    "ds_read_b64_tr_b16 a[246:247], v211 offset:4096\n"
    "ds_read_b128 a[200:203], v203 offset:6144\n"
    "v_mfma_f32_16x16x32_bf16 v[224:227], a[164:167], a[36:39], v[224:227]\n"
    "ds_read_b128 a[204:207], v200 offset:2048\n"
    "ds_read_b128 a[208:211], v201 offset:2048\n"
    "v_mfma_f32_16x16x32_bf16 v[224:227], a[168:171], a[40:43], v[224:227]\n"
    "ds_read_b128 a[212:215], v200 offset:6144\n"
    "ds_read_b128 a[216:219], v201 offset:6144\n"
    "v_mfma_f32_16x16x32_bf16 v[224:227], a[172:175], a[44:47], v[224:227]\n"
    "ds_read_b128 a[220:223], v202 offset:2048\n"
    "ds_read_b128 a[224:227], v203 offset:2048\n"
    "s_waitcnt lgkmcnt(15)\n"
    "v_mfma_f32_16x16x32_bf16 v[224:227], a[176:179], a[48:51], v[224:227]\n"
    "ds_read_b128 a[228:231], v202 offset:6144\n"
    "v_mfma_f32_16x16x32_bf16 v[224:227], a[180:183], a[52:55], v[224:227]\n"
    "v_mfma_f32_16x16x32_bf16 v[224:227], a[184:187], a[56:59], v[224:227]\n"
    "v_mfma_f32_16x16x32_bf16 v[224:227], a[188:191], a[60:63], v[224:227]\n"
    "s_andn2_b32 s61, s32, s50\n"
    "s_bfe_i32 s61, s61, 0x10000\n"
    "ds_read_b128 a[160:163], v203 offset:4096\n"
    "ds_read_b128 a[164:167], v200 offset:0\n"
    "ds_read_b128 a[168:171], v201 offset:0\n"
    "ds_read_b128 a[172:175], v200 offset:4096\n"
    "ds_read_b128 a[176:179], v201 offset:4096\n"
    "ds_read_b128 a[180:183], v202 offset:0\n"
    "ds_read_b128 a[184:187], v203 offset:0\n"
    "ds_read_b128 a[188:191], v202 offset:4096\n"
    "s_and_b32 s57, s63, 0xffffdfff\n"
    "s_sub_u32 s57, s57, 16384\n"
    "v_add_u32_e32 v216, s57, v253\n"
    "s_lshl_b32 s45, s51, 12\n"
    "s_add_u32 s57, s57, s45\n"
    "s_mul_i32 s48, s33, 81920\n"
    "s_add_u32 s48, s48, s45\n"
    "s_add_u32 s48, s48, 11022336\n"
    "v_exp_f32_e32 v220, v220\n"
    "v_exp_f32_e32 v221, v221\n"
    "v_exp_f32_e32 v222, v222\n"
    "v_exp_f32_e32 v223, v223\n"
    "v_exp_f32_e32 v224, v224\n"
    "v_exp_f32_e32 v225, v225\n"
    "v_exp_f32_e32 v226, v226\n"
    "v_exp_f32_e32 v227, v227\n"
    "v_cvt_pk_bf16_f32 v220, v220, v221\n"
    "v_cvt_pk_bf16_f32 v221, v222, v223\n"
    "v_cvt_pk_bf16_f32 v222, v224, v225\n"
    "v_cvt_pk_bf16_f32 v223, v226, v227\n"
    "v_and_b32_e32 v222, s61, v222\n"
    "v_and_b32_e32 v223, s61, v223\n"
    "s_waitcnt vmcnt(11)\n"
    "s_waitcnt lgkmcnt(0)\n"
    "s_barrier\n"
    "v_xor_b32_e32 v1, s39, v1\n"
    "v_xor_b32_e32 v0, s39, v0\n"
    "v_or_b32_e32 v0, v0, v1\n"
    "v_cmp_ne_u32_e32 vcc, 0, v0\n"
    "s_nop 3\n"
    "s_cbranch_vccnz .Lat_hpoll\n"
    ".Lat_hpoll_ret:\n"
    "s_waitcnt vmcnt(0)\n"
    "v_cmp_ne_u32_e32 vcc, s39, v11\n"
    "s_nop 3\n"
    "s_cbranch_vccnz .Lat_qpoll\n"
    ".Lat_qpoll_ret:\n"
    "s_cmp_eq_u32 s50, 0\n"
    "s_cbranch_scc1 .Lat_qx\n"
    "s_bfe_u32 s45, s36, 0x40000\n"
    "s_add_u32 m0, s57, 0\n"
    "s_lshl_b32 s45, s45, 13\n"
    "s_add_u32 s45, s45, s48\n"
    "buffer_load_dwordx4 v253, s[8:11], s45 offen offset:0 sc1 lds\n"
    "buffer_load_dwordx4 v253, s[8:11], s45 offen offset:1024 sc1 lds\n"
    "buffer_load_dwordx4 v253, s[8:11], s45 offen offset:2048 sc1 lds\n"
    "buffer_load_dwordx4 v253, s[8:11], s45 offen offset:3072 sc1 lds\n"
    ".Lat_qx:\n"
    "s_bfe_u32 s45, s36, 0x40004\n"
    "s_add_u32 m0, s57, 8192\n"
    "s_lshl_b32 s45, s45, 13\n"
    "s_add_u32 s45, s45, s48\n"
    "buffer_load_dwordx4 v253, s[8:11], s45 offen offset:0 sc1 lds\n"
    "buffer_load_dwordx4 v253, s[8:11], s45 offen offset:1024 sc1 lds\n"
    "buffer_load_dwordx4 v253, s[8:11], s45 offen offset:2048 sc1 lds\n"
    "buffer_load_dwordx4 v253, s[8:11], s45 offen offset:3072 sc1 lds\n"
    "v_mfma_f32_16x16x16_bf16 v[52:55], a[246:247], v[222:223], 0\n"
    "v_mfma_f32_16x16x16_bf16 v[48:51], a[244:245], v[222:223], 0\n"
    "s_bfe_u32 s45, s36, 0x40008\n"
    "s_add_u32 m0, s57, 16384\n"
    "s_lshl_b32 s45, s45, 13\n"
    "s_add_u32 s45, s45, s48\n"
    "v_mfma_f32_16x16x16_bf16 v[44:47], a[242:243], v[222:223], 0\n"
    "buffer_load_dwordx4 v253, s[8:11], s45 offen offset:0 sc1 lds\n"
    "v_mfma_f32_16x16x16_bf16 v[40:43], a[240:241], v[222:223], 0\n"
    "v_mfma_f32_16x16x16_bf16 v[36:39], a[238:239], v[222:223], 0\n"
    "buffer_load_dwordx4 v253, s[8:11], s45 offen offset:1024 sc1 lds\n"
    "v_mfma_f32_16x16x16_bf16 v[32:35], a[236:237], v[222:223], 0\n"
    "v_mfma_f32_16x16x16_bf16 a[252:255], a[234:235], v[222:223], 0\n"
    "buffer_load_dwordx4 v253, s[8:11], s45 offen offset:2048 sc1 lds\n"
    "v_mfma_f32_16x16x16_bf16 a[248:251], a[232:233], v[222:223], 0\n"
    "v_mfma_f32_16x16x16_bf16 v[28:31], a[246:247], v[220:221], 0\n"
    "buffer_load_dwordx4 v253, s[8:11], s45 offen offset:3072 sc1 lds\n"
    "v_mfma_f32_16x16x16_bf16 v[24:27], a[244:245], v[220:221], 0\n"
    "s_bfe_u32 s45, s36, 0x4000c\n"
    "s_add_u32 m0, s57, 24576\n"
    "s_lshl_b32 s45, s45, 13\n"
    "s_add_u32 s45, s45, s48\n"
    "v_mfma_f32_16x16x16_bf16 v[20:23], a[242:243], v[220:221], 0\n"
    "buffer_load_dwordx4 v253, s[8:11], s45 offen offset:0 sc1 lds\n"
    "v_mfma_f32_16x16x16_bf16 v[16:19], a[240:241], v[220:221], 0\n"
    "v_mfma_f32_16x16x16_bf16 v[12:15], a[238:239], v[220:221], 0\n"
    "buffer_load_dwordx4 v253, s[8:11], s45 offen offset:1024 sc1 lds\n"
    "v_mfma_f32_16x16x16_bf16 v[8:11], a[236:237], v[220:221], 0\n"
    "v_mfma_f32_16x16x16_bf16 v[4:7], a[234:235], v[220:221], 0\n"
    "buffer_load_dwordx4 v253, s[8:11], s45 offen offset:2048 sc1 lds\n"
    "v_mfma_f32_16x16x16_bf16 v[0:3], a[232:233], v[220:221], 0\n"
    "v_mfma_f32_16x16x32_bf16 v[160:163], a[160:163], a[28:31], 0\n"
    "buffer_load_dwordx4 v253, s[8:11], s45 offen offset:3072 sc1 lds\n"
    "v_mfma_f32_16x16x32_bf16 v[160:163], a[164:167], a[0:3], v[160:163]\n"
    "s_mov_b32 m0, s49\n"
    "v_mfma_f32_16x16x32_bf16 v[160:163], a[168:171], a[4:7], v[160:163]\n"
    "v_mfma_f32_16x16x32_bf16 v[160:163], a[172:175], a[8:11], v[160:163]\n"
    "v_mfma_f32_16x16x32_bf16 v[160:163], a[176:179], a[12:15], v[160:163]\n"
    "v_mfma_f32_16x16x32_bf16 v[160:163], a[180:183], a[16:19], v[160:163]\n"
    "v_mfma_f32_16x16x32_bf16 v[160:163], a[184:187], a[20:23], v[160:163]\n"
    "v_mfma_f32_16x16x32_bf16 v[160:163], a[188:191], a[24:27], v[160:163]\n"
    "v_mfma_f32_16x16x32_bf16 v[164:167], a[200:203], a[28:31], 0\n"
    "v_mfma_f32_16x16x32_bf16 v[164:167], a[204:207], a[0:3], v[164:167]\n"
    "v_mfma_f32_16x16x32_bf16 v[164:167], a[208:211], a[4:7], v[164:167]\n"
    "v_exp_f32_e32 v160, v160\n"
    "v_mfma_f32_16x16x32_bf16 v[164:167], a[212:215], a[8:11], v[164:167]\n"
    "v_exp_f32_e32 v161, v161\n"
    "v_mfma_f32_16x16x32_bf16 v[164:167], a[216:219], a[12:15], v[164:167]\n"
    "v_exp_f32_e32 v162, v162\n"
    "v_mfma_f32_16x16x32_bf16 v[164:167], a[220:223], a[16:19], v[164:167]\n"
    "v_exp_f32_e32 v163, v163\n"
    "v_mfma_f32_16x16x32_bf16 v[164:167], a[224:227], a[20:23], v[164:167]\n"
    "v_cvt_pk_bf16_f32 v160, v160, v161\n"
    "v_mfma_f32_16x16x32_bf16 v[164:167], a[228:231], a[24:27], v[164:167]\n"
    "s_waitcnt vmcnt(8)\n"
    "s_barrier\n"
    "s_cmp_eq_u32 s50, 0\n"
    "s_cbranch_scc1 .Lat_qy\n"
    "ds_read_b128 a[60:63], v216 offset:7168\n"
    "ds_read_b128 a[32:35], v216 offset:0\n"
    "ds_read_b128 a[36:39], v216 offset:1024\n"
    "ds_read_b128 a[40:43], v216 offset:2048\n"
    "ds_read_b128 a[44:47], v216 offset:3072\n"
    "ds_read_b128 a[48:51], v216 offset:4096\n"
    "ds_read_b128 a[52:55], v216 offset:5120\n"
    "ds_read_b128 a[56:59], v216 offset:6144\n"
    ".Lat_qy:\n"
    "ds_read_b128 a[92:95], v216 offset:15360\n"
    "ds_read_b128 a[64:67], v216 offset:8192\n"
    "s_waitcnt lgkmcnt(8)\n"
    "v_mfma_f32_16x16x32_bf16 v[168:171], a[160:163], a[60:63], 0\n"
    "v_cvt_pk_bf16_f32 v161, v162, v163\n"
    "v_mfma_f32_16x16x32_bf16 v[168:171], a[164:167], a[32:35], v[168:171]\n"
    "s_waitcnt lgkmcnt(6)\n"
    "ds_read_b128 a[68:71], v216 offset:9216\n"
    "v_mfma_f32_16x16x32_bf16 v[168:171], a[168:171], a[36:39], v[168:171]\n"
    "ds_read_b128 a[72:75], v216 offset:10240\n"
    "v_mfma_f32_16x16x32_bf16 v[168:171], a[172:175], a[40:43], v[168:171]\n"
    "s_waitcnt lgkmcnt(6)\n"
    "ds_read_b128 a[76:79], v216 offset:11264\n"
    "ds_write_addtid_b32 v160 offset:0\n"
    "v_mfma_f32_16x16x32_bf16 v[168:171], a[176:179], a[44:47], v[168:171]\n"
    "v_exp_f32_e32 v164, v164\n"
    "v_mfma_f32_16x16x32_bf16 v[168:171], a[180:183], a[48:51], v[168:171]\n"
    "s_waitcnt lgkmcnt(6)\n"
    "ds_read_b128 a[80:83], v216 offset:12288\n"
    "v_mfma_f32_16x16x32_bf16 v[168:171], a[184:187], a[52:55], v[168:171]\n"
    "ds_read_b128 a[84:87], v216 offset:13312\n"
    "v_mfma_f32_16x16x32_bf16 v[168:171], a[188:191], a[56:59], v[168:171]\n"
    "ds_read_b128 a[88:91], v216 offset:14336\n"
    "ds_write_addtid_b32 v161 offset:256\n"
    "v_mfma_f32_16x16x32_bf16 v[172:175], a[200:203], a[60:63], 0\n"
    "v_exp_f32_e32 v165, v165\n"
    "v_mfma_f32_16x16x32_bf16 v[172:175], a[204:207], a[32:35], v[172:175]\n"
    "v_exp_f32_e32 v166, v166\n"
    "v_mfma_f32_16x16x32_bf16 v[172:175], a[208:211], a[36:39], v[172:175]\n"
    "v_exp_f32_e32 v167, v167\n"
    "v_mfma_f32_16x16x32_bf16 v[172:175], a[212:215], a[40:43], v[172:175]\n"
    "v_cvt_pk_bf16_f32 v162, v164, v165\n"
    "v_cvt_pk_bf16_f32 v163, v166, v167\n"
    "v_mfma_f32_16x16x32_bf16 v[172:175], a[216:219], a[44:47], v[172:175]\n"
    "ds_write_addtid_b32 v162 offset:512\n"
    "v_exp_f32_e32 v168, v168\n"
    "v_mfma_f32_16x16x32_bf16 v[172:175], a[220:223], a[48:51], v[172:175]\n"
    "v_exp_f32_e32 v169, v169\n"
    "v_mfma_f32_16x16x32_bf16 v[172:175], a[224:227], a[52:55], v[172:175]\n"
    "v_exp_f32_e32 v170, v170\n"
    "v_mfma_f32_16x16x32_bf16 v[172:175], a[228:231], a[56:59], v[172:175]\n"
    "s_waitcnt lgkmcnt(9)\n"
    "v_exp_f32_e32 v171, v171\n"
    "v_mfma_f32_16x16x32_bf16 v[176:179], a[160:163], a[92:95], 0\n"
    "ds_write_addtid_b32 v163 offset:768\n"
    "v_cvt_pk_bf16_f32 v168, v168, v169\n"
    "v_mfma_f32_16x16x32_bf16 v[176:179], a[164:167], a[64:67], v[176:179]\n"
    "s_waitcnt lgkmcnt(8)\n"
    "ds_write_addtid_b32 v168 offset:4096\n"
    "v_cvt_pk_bf16_f32 v169, v170, v171\n"
    "v_mfma_f32_16x16x32_bf16 v[176:179], a[168:171], a[68:71], v[176:179]\n"
    "v_exp_f32_e32 v172, v172\n"
    "v_mfma_f32_16x16x32_bf16 v[176:179], a[172:175], a[72:75], v[176:179]\n"
    "s_waitcnt lgkmcnt(6)\n"
    "v_exp_f32_e32 v173, v173\n"
    "v_mfma_f32_16x16x32_bf16 v[176:179], a[176:179], a[76:79], v[176:179]\n"
    "v_exp_f32_e32 v174, v174\n"
    "v_mfma_f32_16x16x32_bf16 v[176:179], a[180:183], a[80:83], v[176:179]\n"
    "s_waitcnt vmcnt(0)\n"
    "s_barrier\n"
    "s_waitcnt lgkmcnt(4)\n"
    "v_mfma_f32_16x16x32_bf16 v[176:179], a[184:187], a[84:87], v[176:179]\n"
    "ds_write_addtid_b32 v169 offset:4352\n"
    "v_exp_f32_e32 v175, v175\n"
    "v_mfma_f32_16x16x32_bf16 v[176:179], a[188:191], a[88:91], v[176:179]\n"
    "ds_read_b128 a[124:127], v216 offset:23552\n"
    "v_cvt_pk_bf16_f32 v170, v172, v173\n"
    "v_mfma_f32_16x16x32_bf16 v[180:183], a[200:203], a[92:95], 0\n"
    "ds_read_b128 a[96:99], v216 offset:16384\n"
    "v_cvt_pk_bf16_f32 v171, v174, v175\n"
    "v_mfma_f32_16x16x32_bf16 v[180:183], a[204:207], a[64:67], v[180:183]\n"
    "ds_read_b128 a[100:103], v216 offset:17408\n"
    "v_exp_f32_e32 v176, v176\n"
    "v_mfma_f32_16x16x32_bf16 v[180:183], a[208:211], a[68:71], v[180:183]\n"
    "ds_read_b128 a[104:107], v216 offset:18432\n"
    "ds_write_addtid_b32 v170 offset:4608\n"
    "v_mfma_f32_16x16x32_bf16 v[180:183], a[212:215], a[72:75], v[180:183]\n"
    "ds_read_b128 a[108:111], v216 offset:19456\n"
    "v_exp_f32_e32 v177, v177\n"
    "v_mfma_f32_16x16x32_bf16 v[180:183], a[216:219], a[76:79], v[180:183]\n"
    "ds_read_b128 a[112:115], v216 offset:20480\n"
    "v_exp_f32_e32 v178, v178\n"
    "v_mfma_f32_16x16x32_bf16 v[180:183], a[220:223], a[80:83], v[180:183]\n"
    "ds_read_b128 a[116:119], v216 offset:21504\n"
    "ds_write_addtid_b32 v171 offset:4864\n"
    "v_mfma_f32_16x16x32_bf16 v[180:183], a[224:227], a[84:87], v[180:183]\n"
    "ds_read_b128 a[120:123], v216 offset:22528\n"
    "v_mfma_f32_16x16x32_bf16 v[180:183], a[228:231], a[88:91], v[180:183]\n"
    "s_waitcnt lgkmcnt(8)\n"
    "s_sub_u32 m0, s63, 16384\n"
    "v_exp_f32_e32 v179, v179\n"
    "v_mfma_f32_16x16x32_bf16 v[184:187], a[160:163], a[124:127], 0\n"
    "ds_read_b128 a[156:159], v216 offset:31744\n"
    "buffer_load_dwordx4 v248, s[8:11], s40 offen offset:0 sc1 lds\n"
    "v_cvt_pk_bf16_f32 v176, v176, v177\n"
    "v_mfma_f32_16x16x32_bf16 v[184:187], a[164:167], a[96:99], v[184:187]\n"
    "s_waitcnt lgkmcnt(7)\n"
    "ds_read_b128 a[128:131], v216 offset:24576\n"
    "v_cvt_pk_bf16_f32 v177, v178, v179\n"
    "v_mfma_f32_16x16x32_bf16 v[184:187], a[168:171], a[100:103], v[184:187]\n"
    "ds_read_b128 a[132:135], v216 offset:25600\n"
    "v_mfma_f32_16x16x32_bf16 v[184:187], a[172:175], a[104:107], v[184:187]\n"
    "s_waitcnt lgkmcnt(6)\n"
    "ds_read_b128 a[136:139], v216 offset:26624\n"
    "v_mfma_f32_16x16x32_bf16 v[184:187], a[176:179], a[108:111], v[184:187]\n"
    "ds_read_b128 a[140:143], v216 offset:27648\n"
    "buffer_load_dwordx4 v248, s[8:11], s40 offen offset:1024 sc1 lds\n"
    "v_mfma_f32_16x16x32_bf16 v[184:187], a[180:183], a[112:115], v[184:187]\n"
    "s_waitcnt lgkmcnt(5)\n"
    "ds_read_b128 a[144:147], v216 offset:28672\n"
    "v_mfma_f32_16x16x32_bf16 v[184:187], a[184:187], a[116:119], v[184:187]\n"
    "ds_read_b128 a[148:151], v216 offset:29696\n"
    "v_mfma_f32_16x16x32_bf16 v[184:187], a[188:191], a[120:123], v[184:187]\n"
    "ds_read_b128 a[152:155], v216 offset:30720\n"
    "v_mfma_f32_16x16x32_bf16 v[188:191], a[200:203], a[124:127], 0\n"
    "buffer_load_dwordx4 v248, s[8:11], s40 offen offset:2048 sc1 lds\n"
    "v_exp_f32_e32 v180, v180\n"
    "v_mfma_f32_16x16x32_bf16 v[188:191], a[204:207], a[96:99], v[188:191]\n"
    "ds_write_b32 v249, v176 offset:8192\n"
    "v_exp_f32_e32 v181, v181\n"
    "v_mfma_f32_16x16x32_bf16 v[188:191], a[208:211], a[100:103], v[188:191]\n"
    "ds_write_b32 v249, v177 offset:8448\n"
    "v_exp_f32_e32 v182, v182\n"
    "v_mfma_f32_16x16x32_bf16 v[188:191], a[212:215], a[104:107], v[188:191]\n"
    "v_exp_f32_e32 v183, v183\n"
    "v_mfma_f32_16x16x32_bf16 v[188:191], a[216:219], a[108:111], v[188:191]\n"
    "buffer_load_dwordx4 v248, s[8:11], s40 offen offset:3072 sc1 lds\n"
    "v_cvt_pk_bf16_f32 v178, v180, v181\n"
    "v_cvt_pk_bf16_f32 v179, v182, v183\n"
    "v_mfma_f32_16x16x32_bf16 v[188:191], a[220:223], a[112:115], v[188:191]\n"
    "s_add_u32 s40, s40, 0x8000\n"
    "ds_write_b32 v249, v178 offset:8704\n"
    "v_exp_f32_e32 v184, v184\n"
    "v_mfma_f32_16x16x32_bf16 v[188:191], a[224:227], a[116:119], v[188:191]\n"
    "s_sub_u32 m0, s63, 12288\n"
    "ds_write_b32 v249, v179 offset:8960\n"
    "v_exp_f32_e32 v185, v185\n"
    "v_mfma_f32_16x16x32_bf16 v[188:191], a[228:231], a[120:123], v[188:191]\n"
    "s_waitcnt lgkmcnt(10)\n"
    "v_exp_f32_e32 v186, v186\n"
    "v_mfma_f32_16x16x32_bf16 v[192:195], a[160:163], a[156:159], 0\n"
    "buffer_load_dwordx4 v248, s[8:11], s41 offen offset:0 sc1 lds\n"
    "v_exp_f32_e32 v187, v187\n"
    "v_mfma_f32_16x16x32_bf16 v[192:195], a[164:167], a[128:131], v[192:195]\n"
    "s_waitcnt lgkmcnt(8)\n"
    "ds_read_b128 a[160:163], v203 offset:20480\n"
    "v_cvt_pk_bf16_f32 v184, v184, v185\n"
    "v_mfma_f32_16x16x32_bf16 v[192:195], a[168:171], a[132:135], v[192:195]\n"
    "ds_read_b128 a[164:167], v200 offset:16384\n"
    "v_cvt_pk_bf16_f32 v185, v186, v187\n"
    "v_mfma_f32_16x16x32_bf16 v[192:195], a[172:175], a[136:139], v[192:195]\n"
    "s_waitcnt lgkmcnt(8)\n"
    "ds_read_b128 a[168:171], v201 offset:16384\n"
    "v_exp_f32_e32 v188, v188\n"
    "v_mfma_f32_16x16x32_bf16 v[192:195], a[176:179], a[140:143], v[192:195]\n"
    "ds_read_b128 a[172:175], v200 offset:20480\n"
    "buffer_load_dwordx4 v248, s[8:11], s41 offen offset:1024 sc1 lds\n"
    "v_exp_f32_e32 v189, v189\n"
    "v_mfma_f32_16x16x32_bf16 v[192:195], a[180:183], a[144:147], v[192:195]\n"
    "s_waitcnt lgkmcnt(8)\n"
    "ds_read_b128 a[176:179], v201 offset:20480\n"
    "v_exp_f32_e32 v190, v190\n"
    "v_mfma_f32_16x16x32_bf16 v[192:195], a[184:187], a[148:151], v[192:195]\n"
    "ds_read_b128 a[180:183], v202 offset:16384\n"
    "v_mfma_f32_16x16x32_bf16 v[192:195], a[188:191], a[152:155], v[192:195]\n"
    "ds_read_b128 a[184:187], v203 offset:16384\n"
    "v_mfma_f32_16x16x32_bf16 v[196:199], a[200:203], a[156:159], 0\n"
    "ds_read_b128 a[188:191], v202 offset:20480\n"
    "buffer_load_dwordx4 v248, s[8:11], s41 offen offset:2048 sc1 lds\n"
    "v_mfma_f32_16x16x32_bf16 v[196:199], a[204:207], a[128:131], v[196:199]\n"
    "ds_write_b32 v249, v184 offset:12288\n"
    "v_exp_f32_e32 v191, v191\n"
    "v_mfma_f32_16x16x32_bf16 v[196:199], a[208:211], a[132:135], v[196:199]\n"
    "ds_write_b32 v249, v185 offset:12544\n"
    "v_cvt_pk_bf16_f32 v186, v188, v189\n"
    "v_cvt_pk_bf16_f32 v187, v190, v191\n"
    "v_mfma_f32_16x16x32_bf16 v[196:199], a[212:215], a[136:139], v[196:199]\n"
    "ds_write_b32 v249, v186 offset:12800\n"
    "v_exp_f32_e32 v192, v192\n"
    "v_mfma_f32_16x16x32_bf16 v[196:199], a[216:219], a[140:143], v[196:199]\n"
    "buffer_load_dwordx4 v248, s[8:11], s41 offen offset:3072 sc1 lds\n"
    "v_exp_f32_e32 v193, v193\n"
    "v_mfma_f32_16x16x32_bf16 v[196:199], a[220:223], a[144:147], v[196:199]\n"
    "s_add_u32 s41, s41, 0x8000\n"
    "ds_write_b32 v249, v187 offset:13056\n"
    "v_exp_f32_e32 v194, v194\n"
    "v_mfma_f32_16x16x32_bf16 v[196:199], a[224:227], a[148:151], v[196:199]\n"
    "v_exp_f32_e32 v195, v195\n"
    "v_mfma_f32_16x16x32_bf16 v[196:199], a[228:231], a[152:155], v[196:199]\n"
    "v_cvt_pk_bf16_f32 v192, v192, v193\n"
    "ds_write_b32 v249, v192 offset:16384\n"
    "v_cvt_pk_bf16_f32 v193, v194, v195\n"
    "ds_write_b32 v249, v193 offset:16640\n"
    "s_cmp_lg_u32 s34, 0\n"
    "s_cselect_b32 s44, 2, 1\n"
    "s_waitcnt lgkmcnt(4)\n"
    "s_nop 0\n"
    "v_exp_f32_e32 v196, v196\n"
    "v_exp_f32_e32 v197, v197\n"
    "v_exp_f32_e32 v198, v198\n"
    "v_exp_f32_e32 v199, v199\n"
    "v_cvt_pk_bf16_f32 v194, v196, v197\n"
    "v_cvt_pk_bf16_f32 v195, v198, v199\n"
    "ds_write_b32 v249, v194 offset:16896\n"
    "ds_write_b32 v249, v195 offset:17152\n"
    "s_cbranch_scc1 .Lat_p2e\n"
    ".p2align 6\n"
    ".Lat_p:\n"
    "v_mfma_f32_16x16x32_bf16 v[200:203], a[160:163], a[28:31], 0\n"
    "v_mfma_f32_16x16x32_bf16 v[200:203], a[164:167], a[0:3], v[200:203]\n"
    "v_mfma_f32_16x16x32_bf16 v[208:211], a[164:167], a[32:35], 0\n"
    "v_mfma_f32_16x16x32_bf16 v[208:211], a[160:163], a[60:63], v[208:211]\n"
    "v_mfma_f32_16x16x32_bf16 v[216:219], a[160:163], a[92:95], 0\n"
    "v_mfma_f32_16x16x32_bf16 v[216:219], a[164:167], a[64:67], v[216:219]\n"
    "s_waitcnt lgkmcnt(0)\n"
    "v_mfma_f32_16x16x32_bf16 v[224:227], a[164:167], a[96:99], 0\n"
    "s_barrier\n"
    "v_mfma_f32_16x16x32_bf16 v[224:227], a[160:163], a[124:127], v[224:227]\n"
    "s_add_u32 m0, s43, 49152\n"
    "ds_read2st64_b32 v[164:165], v250 offset0:0 offset1:1\n"
    "v_mfma_f32_16x16x32_bf16 v[232:235], a[160:163], a[156:159], 0\n"
    "ds_read_b128 a[160:163], v243 offset:22528\n"
    "v_mfma_f32_16x16x32_bf16 v[232:235], a[164:167], a[128:131], v[232:235]\n"
    "ds_read_b128 a[164:167], v240 offset:18432\n"
    "v_mfma_f32_16x16x32_bf16 v[232:235], a[168:171], a[132:135], v[232:235]\n"
    "buffer_load_dwordx4 v248, s[8:11], s40 offen offset:0 sc1 lds\n"
    "v_mfma_f32_16x16x32_bf16 v[232:235], a[172:175], a[136:139], v[232:235]\n"
    "v_mfma_f32_16x16x32_bf16 v[224:227], a[172:175], a[104:107], v[224:227]\n"
    "v_mfma_f32_16x16x32_bf16 v[224:227], a[168:171], a[100:103], v[224:227]\n"
    "ds_read2st64_b32 v[166:167], v250 offset0:2 offset1:3\n"
    "v_mfma_f32_16x16x32_bf16 v[216:219], a[168:171], a[68:71], v[216:219]\n"
    "v_mfma_f32_16x16x32_bf16 v[216:219], a[172:175], a[72:75], v[216:219]\n"
    "buffer_load_dwordx4 v248, s[8:11], s40 offen offset:1024 sc1 lds\n"
    "v_mfma_f32_16x16x32_bf16 v[208:211], a[172:175], a[40:43], v[208:211]\n"
    "v_mfma_f32_16x16x32_bf16 v[208:211], a[168:171], a[36:39], v[208:211]\n"
    "ds_read2st64_b32 v[172:173], v250 offset0:16 offset1:17\n"
    "v_mfma_f32_16x16x32_bf16 v[200:203], a[168:171], a[4:7], v[200:203]\n"
    "ds_read_b128 a[168:171], v241 offset:18432\n"
    "v_mfma_f32_16x16x32_bf16 v[200:203], a[172:175], a[8:11], v[200:203]\n"
    "ds_read_b128 a[172:175], v240 offset:22528\n"
    "v_mfma_f32_16x16x32_bf16 v[200:203], a[176:179], a[12:15], v[200:203]\n"
    "buffer_load_dwordx4 v248, s[8:11], s40 offen offset:2048 sc1 lds\n"
    "v_mfma_f32_16x16x32_bf16 v[200:203], a[180:183], a[16:19], v[200:203]\n"
    "ds_read2st64_b32 v[174:175], v250 offset0:18 offset1:19\n"
    "v_mfma_f32_16x16x32_bf16 v[208:211], a[180:183], a[48:51], v[208:211]\n"
    "v_mfma_f32_16x16x32_bf16 v[208:211], a[176:179], a[44:47], v[208:211]\n"
    "v_mfma_f32_16x16x32_bf16 v[216:219], a[176:179], a[76:79], v[216:219]\n"
    "ds_read2st64_b32 v[180:181], v250 offset0:32 offset1:33\n"
    "v_mfma_f32_16x16x32_bf16 v[216:219], a[180:183], a[80:83], v[216:219]\n"
    "buffer_load_dwordx4 v248, s[8:11], s40 offen offset:3072 sc1 lds\n"
    "v_mfma_f32_16x16x32_bf16 v[224:227], a[180:183], a[112:115], v[224:227]\n"
    "s_add_u32 m0, s43, 53248\n"
    "v_mfma_f32_16x16x32_bf16 v[224:227], a[176:179], a[108:111], v[224:227]\n"
    "s_add_u32 s40, s40, 0x8000\n"
    "ds_read2st64_b32 v[182:183], v250 offset0:34 offset1:35\n"
    "v_mfma_f32_16x16x32_bf16 v[232:235], a[176:179], a[140:143], v[232:235]\n"
    "ds_read_b128 a[176:179], v241 offset:22528\n"
    "v_mfma_f32_16x16x32_bf16 v[232:235], a[180:183], a[144:147], v[232:235]\n"
    "ds_read_b128 a[180:183], v242 offset:18432\n"
    "v_mfma_f32_16x16x32_bf16 v[232:235], a[184:187], a[148:151], v[232:235]\n"
    "buffer_load_dwordx4 v248, s[8:11], s41 offen offset:0 sc1 lds\n"
    "v_mfma_f32_16x16x32_bf16 v[232:235], a[188:191], a[152:155], v[232:235]\n"
    "v_mfma_f32_16x16x32_bf16 v[224:227], a[188:191], a[120:123], v[224:227]\n"
    "ds_read2st64_b32 v[188:189], v250 offset0:48 offset1:49\n"
    "v_mfma_f32_16x16x32_bf16 v[224:227], a[184:187], a[116:119], v[224:227]\n"
    "v_mfma_f32_16x16x32_bf16 v[216:219], a[184:187], a[84:87], v[216:219]\n"
    "v_exp_f32_e32 v232, v232\n"
    "v_mfma_f32_16x16x32_bf16 v[216:219], a[188:191], a[88:91], v[216:219]\n"
    "buffer_load_dwordx4 v248, s[8:11], s41 offen offset:1024 sc1 lds\n"
    "v_mfma_f32_16x16x32_bf16 v[208:211], a[188:191], a[56:59], v[208:211]\n"
    "ds_read2st64_b32 v[190:191], v250 offset0:50 offset1:51\n"
    "v_mfma_f32_16x16x32_bf16 v[208:211], a[184:187], a[52:55], v[208:211]\n"
    "v_exp_f32_e32 v233, v233\n"
    "v_mfma_f32_16x16x32_bf16 v[200:203], a[184:187], a[20:23], v[200:203]\n"
    "ds_read_b128 a[184:187], v243 offset:18432\n"
    "v_mfma_f32_16x16x32_bf16 v[200:203], a[188:191], a[24:27], v[200:203]\n"
    "s_waitcnt lgkmcnt(8)\n"
    "ds_read_b128 a[188:191], v242 offset:22528\n"
    "v_mfma_f32_16x16x32_bf16 v[204:207], a[160:163], a[28:31], 0\n"
    "buffer_load_dwordx4 v248, s[8:11], s41 offen offset:2048 sc1 lds\n"
    "v_mfma_f32_16x16x32_bf16 v[204:207], a[164:167], a[0:3], v[204:207]\n"
    "ds_read2st64_b32 v[196:197], v250 offset0:64 offset1:65\n"
    "v_mfma_f32_16x16x32_bf16 v[212:215], a[164:167], a[32:35], 0\n"
    "v_exp_f32_e32 v234, v234\n"
    "v_mfma_f32_16x16x32_bf16 v[212:215], a[160:163], a[60:63], v[212:215]\n"
    "ds_read2st64_b32 v[198:199], v250 offset0:66 offset1:67\n"
    "v_mfma_f32_16x16x32_bf16 v[220:223], a[160:163], a[92:95], 0\n"
    "v_exp_f32_e32 v235, v235\n"
    "v_mfma_f32_16x16x32_bf16 v[220:223], a[164:167], a[64:67], v[220:223]\n"
    "buffer_load_dwordx4 v248, s[8:11], s41 offen offset:3072 sc1 lds\n"
    "v_mfma_f32_16x16x32_bf16 v[228:231], a[164:167], a[96:99], 0\n"
    "s_add_u32 s41, s41, 0x8000\n"
    "v_cvt_pk_bf16_f32 v232, v232, v233\n"
    "v_mfma_f32_16x16x32_bf16 v[228:231], a[160:163], a[124:127], v[228:231]\n"
    "v_exp_f32_e32 v224, v224\n"
    "v_mfma_f32_16x16x32_bf16 v[236:239], a[160:163], a[156:159], 0\n"
    "ds_read_b64_tr_b16 a[160:161], v244 offset:0\n"
    "v_mfma_f32_16x16x32_bf16 v[236:239], a[164:167], a[128:131], v[236:239]\n"
    "ds_read_b64_tr_b16 a[162:163], v244 offset:2048\n"
    "v_mfma_f32_16x16x32_bf16 v[236:239], a[168:171], a[132:135], v[236:239]\n"
    "ds_read_b64_tr_b16 a[164:165], v244 offset:8192\n"
    "v_mfma_f32_16x16x32_bf16 v[236:239], a[172:175], a[136:139], v[236:239]\n"
    "ds_read_b64_tr_b16 a[166:167], v244 offset:10240\n"
    "v_mfma_f32_16x16x32_bf16 v[228:231], a[172:175], a[104:107], v[228:231]\n"
    "v_exp_f32_e32 v225, v225\n"
    "v_mfma_f32_16x16x32_bf16 v[228:231], a[168:171], a[100:103], v[228:231]\n"
    "v_cvt_pk_bf16_f32 v233, v234, v235\n"
    "v_mfma_f32_16x16x32_bf16 v[220:223], a[168:171], a[68:71], v[220:223]\n"
    "v_exp_f32_e32 v226, v226\n"
    "v_mfma_f32_16x16x32_bf16 v[220:223], a[172:175], a[72:75], v[220:223]\n"
    "v_exp_f32_e32 v227, v227\n"
    "v_mfma_f32_16x16x32_bf16 v[212:215], a[172:175], a[40:43], v[212:215]\n"
    "v_cvt_pk_bf16_f32 v224, v224, v225\n"
    "v_mfma_f32_16x16x32_bf16 v[212:215], a[168:171], a[36:39], v[212:215]\n"
    "v_exp_f32_e32 v216, v216\n"
    "v_mfma_f32_16x16x32_bf16 v[204:207], a[168:171], a[4:7], v[204:207]\n"
    "ds_read_b64_tr_b16 a[168:169], v245 offset:0\n"
    "v_mfma_f32_16x16x32_bf16 v[204:207], a[172:175], a[8:11], v[204:207]\n"
    "s_waitcnt lgkmcnt(7)\n"
    "ds_read_b64_tr_b16 a[170:171], v245 offset:2048\n"
    "v_mfma_f32_16x16x32_bf16 v[204:207], a[176:179], a[12:15], v[204:207]\n"
    "ds_read_b64_tr_b16 a[172:173], v245 offset:8192\n"
    "v_mfma_f32_16x16x32_bf16 v[204:207], a[180:183], a[16:19], v[204:207]\n"
    "ds_read_b64_tr_b16 a[174:175], v245 offset:10240\n"
    "v_mfma_f32_16x16x32_bf16 v[212:215], a[180:183], a[48:51], v[212:215]\n"
    "v_exp_f32_e32 v217, v217\n"
    "v_mfma_f32_16x16x32_bf16 v[212:215], a[176:179], a[44:47], v[212:215]\n"
    "v_cvt_pk_bf16_f32 v225, v226, v227\n"
    "v_mfma_f32_16x16x32_bf16 v[220:223], a[176:179], a[76:79], v[220:223]\n"
    "v_exp_f32_e32 v218, v218\n"
    "v_mfma_f32_16x16x32_bf16 v[220:223], a[180:183], a[80:83], v[220:223]\n"
    "v_exp_f32_e32 v219, v219\n"
    "v_mfma_f32_16x16x32_bf16 v[228:231], a[180:183], a[112:115], v[228:231]\n"
    "v_cvt_pk_bf16_f32 v216, v216, v217\n"
    "v_mfma_f32_16x16x32_bf16 v[228:231], a[176:179], a[108:111], v[228:231]\n"
    "v_exp_f32_e32 v208, v208\n"
    "v_mfma_f32_16x16x32_bf16 v[236:239], a[176:179], a[140:143], v[236:239]\n"
    "ds_read_b64_tr_b16 a[176:177], v246 offset:0\n"
    "v_mfma_f32_16x16x32_bf16 v[236:239], a[180:183], a[144:147], v[236:239]\n"
    "ds_read_b64_tr_b16 a[178:179], v246 offset:2048\n"
    "v_mfma_f32_16x16x32_bf16 v[236:239], a[184:187], a[148:151], v[236:239]\n"
    "ds_read_b64_tr_b16 a[180:181], v246 offset:8192\n"
    "v_mfma_f32_16x16x32_bf16 v[236:239], a[188:191], a[152:155], v[236:239]\n"
    "ds_read_b64_tr_b16 a[182:183], v246 offset:10240\n"
    "v_mfma_f32_16x16x32_bf16 v[228:231], a[188:191], a[120:123], v[228:231]\n"
    "v_exp_f32_e32 v209, v209\n"
    "v_mfma_f32_16x16x32_bf16 v[228:231], a[184:187], a[116:119], v[228:231]\n"
    "v_cvt_pk_bf16_f32 v217, v218, v219\n"
    "v_mfma_f32_16x16x32_bf16 v[220:223], a[184:187], a[84:87], v[220:223]\n"
    "v_exp_f32_e32 v210, v210\n"
    "v_mfma_f32_16x16x32_bf16 v[220:223], a[188:191], a[88:91], v[220:223]\n"
    "v_exp_f32_e32 v211, v211\n"
    "v_mfma_f32_16x16x32_bf16 v[212:215], a[188:191], a[56:59], v[212:215]\n"
    "v_cvt_pk_bf16_f32 v208, v208, v209\n"
    "v_mfma_f32_16x16x32_bf16 v[212:215], a[184:187], a[52:55], v[212:215]\n"
    "v_exp_f32_e32 v200, v200\n"
    "v_mfma_f32_16x16x32_bf16 v[204:207], a[184:187], a[20:23], v[204:207]\n"
    "ds_read_b64_tr_b16 a[184:185], v247 offset:0\n"
    "v_mfma_f32_16x16x32_bf16 v[204:207], a[188:191], a[24:27], v[204:207]\n"
    "s_waitcnt lgkmcnt(5)\n"
    "ds_read_b64_tr_b16 a[186:187], v247 offset:2048\n"
    "v_mfma_f32_16x16x32_bf16 v[0:3], a[160:163], v[160:163], v[0:3]\n"
    "ds_read_b64_tr_b16 a[188:189], v247 offset:8192\n"
    "v_mfma_f32_16x16x32_bf16 v[0:3], a[164:167], v[164:167], v[0:3]\n"
    "ds_read_b64_tr_b16 a[190:191], v247 offset:10240\n"
    "v_mfma_f32_16x16x32_bf16 a[248:251], a[164:167], v[172:175], a[248:251]\n"
    "v_exp_f32_e32 v201, v201\n"
    "v_mfma_f32_16x16x32_bf16 a[248:251], a[160:163], v[168:171], a[248:251]\n"
    "v_cvt_pk_bf16_f32 v209, v210, v211\n"
    "v_mfma_f32_16x16x32_bf16 v[56:59], a[160:163], v[176:179], v[56:59]\n"
    "v_exp_f32_e32 v202, v202\n"
    "v_mfma_f32_16x16x32_bf16 v[56:59], a[164:167], v[180:183], v[56:59]\n"
    "v_exp_f32_e32 v203, v203\n"
    "v_mfma_f32_16x16x32_bf16 v[60:63], a[164:167], v[188:191], v[60:63]\n"
    "v_cvt_pk_bf16_f32 v200, v200, v201\n"
    "v_mfma_f32_16x16x32_bf16 v[60:63], a[160:163], v[184:187], v[60:63]\n"
    "v_cvt_pk_bf16_f32 v201, v202, v203\n"
    "v_mfma_f32_16x16x32_bf16 v[64:67], a[160:163], v[192:195], v[64:67]\n"
    "ds_read_b64_tr_b16 a[160:161], v244 offset:4096\n"
    "v_mfma_f32_16x16x32_bf16 v[64:67], a[164:167], v[196:199], v[64:67]\n"
    "ds_read_b64_tr_b16 a[162:163], v244 offset:6144\n"
    "v_mfma_f32_16x16x32_bf16 v[76:79], a[168:171], v[192:195], v[76:79]\n"
    "ds_read_b64_tr_b16 a[164:165], v244 offset:12288\n"
    "v_mfma_f32_16x16x32_bf16 v[76:79], a[172:175], v[196:199], v[76:79]\n"
    "ds_read_b64_tr_b16 a[166:167], v244 offset:14336\n"
    "v_mfma_f32_16x16x32_bf16 v[72:75], a[172:175], v[188:191], v[72:75]\n"
    "v_exp_f32_e32 v236, v236\n"
    "v_mfma_f32_16x16x32_bf16 v[72:75], a[168:171], v[184:187], v[72:75]\n"
    "v_exp_f32_e32 v237, v237\n"
    "v_mfma_f32_16x16x32_bf16 v[68:71], a[168:171], v[176:179], v[68:71]\n"
    "v_exp_f32_e32 v238, v238\n"
    "v_mfma_f32_16x16x32_bf16 v[68:71], a[172:175], v[180:183], v[68:71]\n"
    "v_exp_f32_e32 v239, v239\n"
    "v_mfma_f32_16x16x32_bf16 a[252:255], a[172:175], v[172:175], a[252:255]\n"
    "v_cvt_pk_bf16_f32 v234, v236, v237\n"
    "v_mfma_f32_16x16x32_bf16 a[252:255], a[168:171], v[168:171], a[252:255]\n"
    "v_exp_f32_e32 v228, v228\n"
    "v_mfma_f32_16x16x32_bf16 v[4:7], a[168:171], v[160:163], v[4:7]\n"
    "ds_read_b64_tr_b16 a[168:169], v245 offset:4096\n"
    "v_mfma_f32_16x16x32_bf16 v[4:7], a[172:175], v[164:167], v[4:7]\n"
    "s_waitcnt lgkmcnt(5)\n"
    "ds_read_b64_tr_b16 a[170:171], v245 offset:6144\n"
    "v_mfma_f32_16x16x32_bf16 v[8:11], a[176:179], v[160:163], v[8:11]\n"
    "ds_read_b64_tr_b16 a[172:173], v245 offset:12288\n"
    "v_mfma_f32_16x16x32_bf16 v[8:11], a[180:183], v[164:167], v[8:11]\n"
    "ds_read_b64_tr_b16 a[174:175], v245 offset:14336\n"
    "v_mfma_f32_16x16x32_bf16 v[32:35], a[180:183], v[172:175], v[32:35]\n"
    "v_exp_f32_e32 v229, v229\n"
    "v_mfma_f32_16x16x32_bf16 v[32:35], a[176:179], v[168:171], v[32:35]\n"
    "v_cvt_pk_bf16_f32 v235, v238, v239\n"
    "v_mfma_f32_16x16x32_bf16 v[80:83], a[176:179], v[176:179], v[80:83]\n"
    "v_exp_f32_e32 v230, v230\n"
    "v_mfma_f32_16x16x32_bf16 v[80:83], a[180:183], v[180:183], v[80:83]\n"
    "v_exp_f32_e32 v231, v231\n"
    "v_mfma_f32_16x16x32_bf16 v[84:87], a[180:183], v[188:191], v[84:87]\n"
    "v_cvt_pk_bf16_f32 v226, v228, v229\n"
    "v_mfma_f32_16x16x32_bf16 v[84:87], a[176:179], v[184:187], v[84:87]\n"
    "v_exp_f32_e32 v220, v220\n"
    "v_mfma_f32_16x16x32_bf16 v[88:91], a[176:179], v[192:195], v[88:91]\n"
    "ds_read_b64_tr_b16 a[176:177], v246 offset:4096\n"
    "v_mfma_f32_16x16x32_bf16 v[88:91], a[180:183], v[196:199], v[88:91]\n"
    "ds_read_b64_tr_b16 a[178:179], v246 offset:6144\n"
    "v_mfma_f32_16x16x32_bf16 v[100:103], a[184:187], v[192:195], v[100:103]\n"
    "ds_read_b64_tr_b16 a[180:181], v246 offset:12288\n"
    "v_mfma_f32_16x16x32_bf16 v[100:103], a[188:191], v[196:199], v[100:103]\n"
    "ds_read_b64_tr_b16 a[182:183], v246 offset:14336\n"
    "v_mfma_f32_16x16x32_bf16 v[96:99], a[188:191], v[188:191], v[96:99]\n"
    "v_exp_f32_e32 v221, v221\n"
    "v_mfma_f32_16x16x32_bf16 v[96:99], a[184:187], v[184:187], v[96:99]\n"
    "v_cvt_pk_bf16_f32 v227, v230, v231\n"
    "v_mfma_f32_16x16x32_bf16 v[92:95], a[184:187], v[176:179], v[92:95]\n"
    "v_exp_f32_e32 v222, v222\n"
    "v_mfma_f32_16x16x32_bf16 v[92:95], a[188:191], v[180:183], v[92:95]\n"
    "v_exp_f32_e32 v223, v223\n"
    "v_mfma_f32_16x16x32_bf16 v[36:39], a[188:191], v[172:175], v[36:39]\n"
    "v_cvt_pk_bf16_f32 v218, v220, v221\n"
    "v_mfma_f32_16x16x32_bf16 v[36:39], a[184:187], v[168:171], v[36:39]\n"
    "v_exp_f32_e32 v212, v212\n"
    "v_mfma_f32_16x16x32_bf16 v[12:15], a[184:187], v[160:163], v[12:15]\n"
    "s_mov_b32 m0, s49\n"
    "ds_read_b64_tr_b16 a[184:185], v247 offset:4096\n"
    "v_mfma_f32_16x16x32_bf16 v[12:15], a[188:191], v[164:167], v[12:15]\n"
    "s_waitcnt lgkmcnt(5)\n"
    "ds_read_b64_tr_b16 a[186:187], v247 offset:6144\n"
    "v_mfma_f32_16x16x32_bf16 v[16:19], a[160:163], v[160:163], v[16:19]\n"
    "ds_read_b64_tr_b16 a[188:189], v247 offset:12288\n"
    "v_mfma_f32_16x16x32_bf16 v[16:19], a[164:167], v[164:167], v[16:19]\n"
    "ds_read_b64_tr_b16 a[190:191], v247 offset:14336\n"
    "v_mfma_f32_16x16x32_bf16 v[40:43], a[164:167], v[172:175], v[40:43]\n"
    "v_exp_f32_e32 v213, v213\n"
    "v_mfma_f32_16x16x32_bf16 v[40:43], a[160:163], v[168:171], v[40:43]\n"
    "v_cvt_pk_bf16_f32 v219, v222, v223\n"
    "v_mfma_f32_16x16x32_bf16 v[104:107], a[160:163], v[176:179], v[104:107]\n"
    "v_exp_f32_e32 v214, v214\n"
    "v_mfma_f32_16x16x32_bf16 v[104:107], a[164:167], v[180:183], v[104:107]\n"
    "s_waitcnt vmcnt(8)\n"
    "v_mfma_f32_16x16x32_bf16 v[108:111], a[164:167], v[188:191], v[108:111]\n"
    "s_barrier\n"
    "v_mfma_f32_16x16x32_bf16 v[108:111], a[160:163], v[184:187], v[108:111]\n"
    "v_exp_f32_e32 v215, v215\n"
    "v_mfma_f32_16x16x32_bf16 v[112:115], a[160:163], v[192:195], v[112:115]\n"
    "ds_read_b128 a[160:163], v243 offset:36864\n"
    "v_mfma_f32_16x16x32_bf16 v[112:115], a[164:167], v[196:199], v[112:115]\n"
    "ds_read_b128 a[164:167], v240 offset:32768\n"
    "v_mfma_f32_16x16x32_bf16 v[124:127], a[168:171], v[192:195], v[124:127]\n"
    "ds_write_addtid_b32 v232 offset:16384\n"
    "v_cvt_pk_bf16_f32 v210, v212, v213\n"
    "v_mfma_f32_16x16x32_bf16 v[124:127], a[172:175], v[196:199], v[124:127]\n"
    "v_exp_f32_e32 v204, v204\n"
    "v_mfma_f32_16x16x32_bf16 v[120:123], a[172:175], v[188:191], v[120:123]\n"
    "v_exp_f32_e32 v205, v205\n"
    "v_mfma_f32_16x16x32_bf16 v[120:123], a[168:171], v[184:187], v[120:123]\n"
    "ds_write_addtid_b32 v233 offset:16640\n"
    "v_cvt_pk_bf16_f32 v211, v214, v215\n"
    "v_mfma_f32_16x16x32_bf16 v[116:119], a[168:171], v[176:179], v[116:119]\n"
    "v_exp_f32_e32 v206, v206\n"
    "v_mfma_f32_16x16x32_bf16 v[116:119], a[172:175], v[180:183], v[116:119]\n"
    "v_exp_f32_e32 v207, v207\n"
    "v_mfma_f32_16x16x32_bf16 v[44:47], a[172:175], v[172:175], v[44:47]\n"
    "ds_write_addtid_b32 v224 offset:12288\n"
    "v_cvt_pk_bf16_f32 v202, v204, v205\n"
    "v_mfma_f32_16x16x32_bf16 v[44:47], a[168:171], v[168:171], v[44:47]\n"
    "ds_write_addtid_b32 v225 offset:12544\n"
    "v_cvt_pk_bf16_f32 v203, v206, v207\n"
    "v_mfma_f32_16x16x32_bf16 v[20:23], a[168:171], v[160:163], v[20:23]\n"
    "ds_read_b128 a[168:171], v241 offset:32768\n"
    "v_mfma_f32_16x16x32_bf16 v[20:23], a[172:175], v[164:167], v[20:23]\n"
    "s_waitcnt lgkmcnt(7)\n"
    "ds_read_b128 a[172:175], v240 offset:36864\n"
    "v_mfma_f32_16x16x32_bf16 v[24:27], a[176:179], v[160:163], v[24:27]\n"
    "ds_write_addtid_b32 v216 offset:8192\n"
    "v_mfma_f32_16x16x32_bf16 v[24:27], a[180:183], v[164:167], v[24:27]\n"
    "ds_write_addtid_b32 v217 offset:8448\n"
    "v_mfma_f32_16x16x32_bf16 v[48:51], a[180:183], v[172:175], v[48:51]\n"
    "ds_write_addtid_b32 v208 offset:4096\n"
    "v_mfma_f32_16x16x32_bf16 v[48:51], a[176:179], v[168:171], v[48:51]\n"
    "ds_write_addtid_b32 v209 offset:4352\n"
    "v_mfma_f32_16x16x32_bf16 v[128:131], a[176:179], v[176:179], v[128:131]\n"
    "ds_write_addtid_b32 v200 offset:0\n"
    "v_mfma_f32_16x16x32_bf16 v[128:131], a[180:183], v[180:183], v[128:131]\n"
    "ds_write_addtid_b32 v201 offset:256\n"
    "v_mfma_f32_16x16x32_bf16 v[132:135], a[180:183], v[188:191], v[132:135]\n"
    "ds_write_addtid_b32 v234 offset:16896\n"
    "v_mfma_f32_16x16x32_bf16 v[132:135], a[176:179], v[184:187], v[132:135]\n"
    "ds_write_addtid_b32 v235 offset:17152\n"
    "v_mfma_f32_16x16x32_bf16 v[136:139], a[176:179], v[192:195], v[136:139]\n"
    "ds_read_b128 a[176:179], v241 offset:36864\n"
    "v_mfma_f32_16x16x32_bf16 v[136:139], a[180:183], v[196:199], v[136:139]\n"
    "ds_read_b128 a[180:183], v242 offset:32768\n"
    "v_mfma_f32_16x16x32_bf16 v[148:151], a[184:187], v[192:195], v[148:151]\n"
    "ds_write_addtid_b32 v226 offset:12800\n"
    "v_mfma_f32_16x16x32_bf16 v[148:151], a[188:191], v[196:199], v[148:151]\n"
    "ds_write_addtid_b32 v227 offset:13056\n"
    "v_mfma_f32_16x16x32_bf16 v[144:147], a[188:191], v[188:191], v[144:147]\n"
    "ds_write_addtid_b32 v218 offset:8704\n"
    "v_mfma_f32_16x16x32_bf16 v[144:147], a[184:187], v[184:187], v[144:147]\n"
    "ds_write_addtid_b32 v219 offset:8960\n"
    "v_mfma_f32_16x16x32_bf16 v[140:143], a[184:187], v[176:179], v[140:143]\n"
    "ds_write_addtid_b32 v210 offset:4608\n"
    "v_mfma_f32_16x16x32_bf16 v[140:143], a[188:191], v[180:183], v[140:143]\n"
    "ds_write_addtid_b32 v211 offset:4864\n"
    "v_mfma_f32_16x16x32_bf16 v[52:55], a[188:191], v[172:175], v[52:55]\n"
    "ds_write_addtid_b32 v202 offset:512\n"
    "v_mfma_f32_16x16x32_bf16 v[52:55], a[184:187], v[168:171], v[52:55]\n"
    "ds_write_addtid_b32 v203 offset:768\n"
    "v_mfma_f32_16x16x32_bf16 v[28:31], a[184:187], v[160:163], v[28:31]\n"
    "ds_read_b128 a[184:187], v243 offset:32768\n"
    "v_mfma_f32_16x16x32_bf16 v[28:31], a[188:191], v[164:167], v[28:31]\n"
    "s_waitcnt lgkmcnt(15)\n"
    "ds_read_b128 a[188:191], v242 offset:36864\n"
    "v_mfma_f32_16x16x32_bf16 v[160:163], a[160:163], a[28:31], 0\n"
    "v_mfma_f32_16x16x32_bf16 v[160:163], a[164:167], a[0:3], v[160:163]\n"
    "v_mfma_f32_16x16x32_bf16 v[168:171], a[164:167], a[32:35], 0\n"
    "v_mfma_f32_16x16x32_bf16 v[168:171], a[160:163], a[60:63], v[168:171]\n"
    "v_mfma_f32_16x16x32_bf16 v[176:179], a[160:163], a[92:95], 0\n"
    "v_mfma_f32_16x16x32_bf16 v[176:179], a[164:167], a[64:67], v[176:179]\n"
    "s_waitcnt lgkmcnt(0)\n"
    "v_mfma_f32_16x16x32_bf16 v[184:187], a[164:167], a[96:99], 0\n"
    "s_barrier\n"
    "v_mfma_f32_16x16x32_bf16 v[184:187], a[160:163], a[124:127], v[184:187]\n"
    "s_add_u32 m0, s43, 0\n"
    "ds_read2st64_b32 v[204:205], v250 offset0:0 offset1:1\n"
    "v_mfma_f32_16x16x32_bf16 v[192:195], a[160:163], a[156:159], 0\n"
    "ds_read_b128 a[160:163], v243 offset:38912\n"
    "v_mfma_f32_16x16x32_bf16 v[192:195], a[164:167], a[128:131], v[192:195]\n"
    "ds_read_b128 a[164:167], v240 offset:34816\n"
    "v_mfma_f32_16x16x32_bf16 v[192:195], a[168:171], a[132:135], v[192:195]\n"
    "buffer_load_dwordx4 v248, s[8:11], s40 offen offset:0 sc1 lds\n"
    "v_mfma_f32_16x16x32_bf16 v[192:195], a[172:175], a[136:139], v[192:195]\n"
    "v_mfma_f32_16x16x32_bf16 v[184:187], a[172:175], a[104:107], v[184:187]\n"
    "v_mfma_f32_16x16x32_bf16 v[184:187], a[168:171], a[100:103], v[184:187]\n"
    "ds_read2st64_b32 v[206:207], v250 offset0:2 offset1:3\n"
    "v_mfma_f32_16x16x32_bf16 v[176:179], a[168:171], a[68:71], v[176:179]\n"
    "v_mfma_f32_16x16x32_bf16 v[176:179], a[172:175], a[72:75], v[176:179]\n"
    "buffer_load_dwordx4 v248, s[8:11], s40 offen offset:1024 sc1 lds\n"
    "v_mfma_f32_16x16x32_bf16 v[168:171], a[172:175], a[40:43], v[168:171]\n"
    "v_mfma_f32_16x16x32_bf16 v[168:171], a[168:171], a[36:39], v[168:171]\n"
    "ds_read2st64_b32 v[212:213], v250 offset0:16 offset1:17\n"
    "v_mfma_f32_16x16x32_bf16 v[160:163], a[168:171], a[4:7], v[160:163]\n"
    "ds_read_b128 a[168:171], v241 offset:34816\n"
    "v_mfma_f32_16x16x32_bf16 v[160:163], a[172:175], a[8:11], v[160:163]\n"
    "ds_read_b128 a[172:175], v240 offset:38912\n"
    "v_mfma_f32_16x16x32_bf16 v[160:163], a[176:179], a[12:15], v[160:163]\n"
    "buffer_load_dwordx4 v248, s[8:11], s40 offen offset:2048 sc1 lds\n"
    "v_mfma_f32_16x16x32_bf16 v[160:163], a[180:183], a[16:19], v[160:163]\n"
    "ds_read2st64_b32 v[214:215], v250 offset0:18 offset1:19\n"
    "v_mfma_f32_16x16x32_bf16 v[168:171], a[180:183], a[48:51], v[168:171]\n"
    "v_mfma_f32_16x16x32_bf16 v[168:171], a[176:179], a[44:47], v[168:171]\n"
    "v_mfma_f32_16x16x32_bf16 v[176:179], a[176:179], a[76:79], v[176:179]\n"
    "ds_read2st64_b32 v[220:221], v250 offset0:32 offset1:33\n"
    "v_mfma_f32_16x16x32_bf16 v[176:179], a[180:183], a[80:83], v[176:179]\n"
    "buffer_load_dwordx4 v248, s[8:11], s40 offen offset:3072 sc1 lds\n"
    "v_mfma_f32_16x16x32_bf16 v[184:187], a[180:183], a[112:115], v[184:187]\n"
    "s_add_u32 m0, s43, 4096\n"
    "v_mfma_f32_16x16x32_bf16 v[184:187], a[176:179], a[108:111], v[184:187]\n"
    "s_add_u32 s40, s40, 0x8000\n"
    "ds_read2st64_b32 v[222:223], v250 offset0:34 offset1:35\n"
    "v_mfma_f32_16x16x32_bf16 v[192:195], a[176:179], a[140:143], v[192:195]\n"
    "ds_read_b128 a[176:179], v241 offset:38912\n"
    "v_mfma_f32_16x16x32_bf16 v[192:195], a[180:183], a[144:147], v[192:195]\n"
    "ds_read_b128 a[180:183], v242 offset:34816\n"
    "v_mfma_f32_16x16x32_bf16 v[192:195], a[184:187], a[148:151], v[192:195]\n"
    "buffer_load_dwordx4 v248, s[8:11], s41 offen offset:0 sc1 lds\n"
    "v_mfma_f32_16x16x32_bf16 v[192:195], a[188:191], a[152:155], v[192:195]\n"
    "v_mfma_f32_16x16x32_bf16 v[184:187], a[188:191], a[120:123], v[184:187]\n"
    "ds_read2st64_b32 v[228:229], v250 offset0:48 offset1:49\n"
    "v_mfma_f32_16x16x32_bf16 v[184:187], a[184:187], a[116:119], v[184:187]\n"
    "v_mfma_f32_16x16x32_bf16 v[176:179], a[184:187], a[84:87], v[176:179]\n"
    "v_exp_f32_e32 v192, v192\n"
    "v_mfma_f32_16x16x32_bf16 v[176:179], a[188:191], a[88:91], v[176:179]\n"
    "buffer_load_dwordx4 v248, s[8:11], s41 offen offset:1024 sc1 lds\n"
    "v_mfma_f32_16x16x32_bf16 v[168:171], a[188:191], a[56:59], v[168:171]\n"
    "ds_read2st64_b32 v[230:231], v250 offset0:50 offset1:51\n"
    "v_mfma_f32_16x16x32_bf16 v[168:171], a[184:187], a[52:55], v[168:171]\n"
    "v_exp_f32_e32 v193, v193\n"
    "v_mfma_f32_16x16x32_bf16 v[160:163], a[184:187], a[20:23], v[160:163]\n"
    "ds_read_b128 a[184:187], v243 offset:34816\n"
    "v_mfma_f32_16x16x32_bf16 v[160:163], a[188:191], a[24:27], v[160:163]\n"
    "s_waitcnt lgkmcnt(8)\n"
    "ds_read_b128 a[188:191], v242 offset:38912\n"
    "v_mfma_f32_16x16x32_bf16 v[164:167], a[160:163], a[28:31], 0\n"
    "buffer_load_dwordx4 v248, s[8:11], s41 offen offset:2048 sc1 lds\n"
    "v_mfma_f32_16x16x32_bf16 v[164:167], a[164:167], a[0:3], v[164:167]\n"
    "ds_read2st64_b32 v[236:237], v250 offset0:64 offset1:65\n"
    "v_mfma_f32_16x16x32_bf16 v[172:175], a[164:167], a[32:35], 0\n"
    "v_exp_f32_e32 v194, v194\n"
    "v_mfma_f32_16x16x32_bf16 v[172:175], a[160:163], a[60:63], v[172:175]\n"
    "ds_read2st64_b32 v[238:239], v250 offset0:66 offset1:67\n"
    "v_mfma_f32_16x16x32_bf16 v[180:183], a[160:163], a[92:95], 0\n"
    "v_exp_f32_e32 v195, v195\n"
    "v_mfma_f32_16x16x32_bf16 v[180:183], a[164:167], a[64:67], v[180:183]\n"
    "buffer_load_dwordx4 v248, s[8:11], s41 offen offset:3072 sc1 lds\n"
    "v_mfma_f32_16x16x32_bf16 v[188:191], a[164:167], a[96:99], 0\n"
    "s_add_u32 s41, s41, 0x8000\n"
    "v_cvt_pk_bf16_f32 v192, v192, v193\n"
    "v_mfma_f32_16x16x32_bf16 v[188:191], a[160:163], a[124:127], v[188:191]\n"
    "v_exp_f32_e32 v184, v184\n"
    "v_mfma_f32_16x16x32_bf16 v[196:199], a[160:163], a[156:159], 0\n"
    "ds_read_b64_tr_b16 a[160:161], v244 offset:16384\n"
    "v_mfma_f32_16x16x32_bf16 v[196:199], a[164:167], a[128:131], v[196:199]\n"
    "ds_read_b64_tr_b16 a[162:163], v244 offset:18432\n"
    "v_mfma_f32_16x16x32_bf16 v[196:199], a[168:171], a[132:135], v[196:199]\n"
    "ds_read_b64_tr_b16 a[164:165], v244 offset:24576\n"
    "v_mfma_f32_16x16x32_bf16 v[196:199], a[172:175], a[136:139], v[196:199]\n"
    "ds_read_b64_tr_b16 a[166:167], v244 offset:26624\n"
    "v_mfma_f32_16x16x32_bf16 v[188:191], a[172:175], a[104:107], v[188:191]\n"
    "v_exp_f32_e32 v185, v185\n"
    "v_mfma_f32_16x16x32_bf16 v[188:191], a[168:171], a[100:103], v[188:191]\n"
    "v_cvt_pk_bf16_f32 v193, v194, v195\n"
    "v_mfma_f32_16x16x32_bf16 v[180:183], a[168:171], a[68:71], v[180:183]\n"
    "v_exp_f32_e32 v186, v186\n"
    "v_mfma_f32_16x16x32_bf16 v[180:183], a[172:175], a[72:75], v[180:183]\n"
    "v_exp_f32_e32 v187, v187\n"
    "v_mfma_f32_16x16x32_bf16 v[172:175], a[172:175], a[40:43], v[172:175]\n"
    "v_cvt_pk_bf16_f32 v184, v184, v185\n"
    "v_mfma_f32_16x16x32_bf16 v[172:175], a[168:171], a[36:39], v[172:175]\n"
    "v_exp_f32_e32 v176, v176\n"
    "v_mfma_f32_16x16x32_bf16 v[164:167], a[168:171], a[4:7], v[164:167]\n"
    "ds_read_b64_tr_b16 a[168:169], v245 offset:16384\n"
    "v_mfma_f32_16x16x32_bf16 v[164:167], a[172:175], a[8:11], v[164:167]\n"
    "s_waitcnt lgkmcnt(7)\n"
    "ds_read_b64_tr_b16 a[170:171], v245 offset:18432\n"
    "v_mfma_f32_16x16x32_bf16 v[164:167], a[176:179], a[12:15], v[164:167]\n"
    "ds_read_b64_tr_b16 a[172:173], v245 offset:24576\n"
    "v_mfma_f32_16x16x32_bf16 v[164:167], a[180:183], a[16:19], v[164:167]\n"
    "ds_read_b64_tr_b16 a[174:175], v245 offset:26624\n"
    "v_mfma_f32_16x16x32_bf16 v[172:175], a[180:183], a[48:51], v[172:175]\n"
    "v_exp_f32_e32 v177, v177\n"
    "v_mfma_f32_16x16x32_bf16 v[172:175], a[176:179], a[44:47], v[172:175]\n"
    "v_cvt_pk_bf16_f32 v185, v186, v187\n"
    "v_mfma_f32_16x16x32_bf16 v[180:183], a[176:179], a[76:79], v[180:183]\n"
    "v_exp_f32_e32 v178, v178\n"
    "v_mfma_f32_16x16x32_bf16 v[180:183], a[180:183], a[80:83], v[180:183]\n"
    "v_exp_f32_e32 v179, v179\n"
    "v_mfma_f32_16x16x32_bf16 v[188:191], a[180:183], a[112:115], v[188:191]\n"
    "v_cvt_pk_bf16_f32 v176, v176, v177\n"
    "v_mfma_f32_16x16x32_bf16 v[188:191], a[176:179], a[108:111], v[188:191]\n"
    "v_exp_f32_e32 v168, v168\n"
    "v_mfma_f32_16x16x32_bf16 v[196:199], a[176:179], a[140:143], v[196:199]\n"
    "ds_read_b64_tr_b16 a[176:177], v246 offset:16384\n"
    "v_mfma_f32_16x16x32_bf16 v[196:199], a[180:183], a[144:147], v[196:199]\n"
    "ds_read_b64_tr_b16 a[178:179], v246 offset:18432\n"
    "v_mfma_f32_16x16x32_bf16 v[196:199], a[184:187], a[148:151], v[196:199]\n"
    "ds_read_b64_tr_b16 a[180:181], v246 offset:24576\n"
    "v_mfma_f32_16x16x32_bf16 v[196:199], a[188:191], a[152:155], v[196:199]\n"
    "ds_read_b64_tr_b16 a[182:183], v246 offset:26624\n"
    "v_mfma_f32_16x16x32_bf16 v[188:191], a[188:191], a[120:123], v[188:191]\n"
    "v_exp_f32_e32 v169, v169\n"
    "v_mfma_f32_16x16x32_bf16 v[188:191], a[184:187], a[116:119], v[188:191]\n"
    "v_cvt_pk_bf16_f32 v177, v178, v179\n"
    "v_mfma_f32_16x16x32_bf16 v[180:183], a[184:187], a[84:87], v[180:183]\n"
    "v_exp_f32_e32 v170, v170\n"
    "v_mfma_f32_16x16x32_bf16 v[180:183], a[188:191], a[88:91], v[180:183]\n"
    "v_exp_f32_e32 v171, v171\n"
    "v_mfma_f32_16x16x32_bf16 v[172:175], a[188:191], a[56:59], v[172:175]\n"
    "v_cvt_pk_bf16_f32 v168, v168, v169\n"
    "v_mfma_f32_16x16x32_bf16 v[172:175], a[184:187], a[52:55], v[172:175]\n"
    "v_exp_f32_e32 v160, v160\n"
    "v_mfma_f32_16x16x32_bf16 v[164:167], a[184:187], a[20:23], v[164:167]\n"
    "ds_read_b64_tr_b16 a[184:185], v247 offset:16384\n"
    "v_mfma_f32_16x16x32_bf16 v[164:167], a[188:191], a[24:27], v[164:167]\n"
    "s_waitcnt lgkmcnt(5)\n"
    "ds_read_b64_tr_b16 a[186:187], v247 offset:18432\n"
    "v_mfma_f32_16x16x32_bf16 v[0:3], a[160:163], v[200:203], v[0:3]\n"
    "ds_read_b64_tr_b16 a[188:189], v247 offset:24576\n"
    "v_mfma_f32_16x16x32_bf16 v[0:3], a[164:167], v[204:207], v[0:3]\n"
    "ds_read_b64_tr_b16 a[190:191], v247 offset:26624\n"
    "v_mfma_f32_16x16x32_bf16 a[248:251], a[164:167], v[212:215], a[248:251]\n"
    "v_exp_f32_e32 v161, v161\n"
    "v_mfma_f32_16x16x32_bf16 a[248:251], a[160:163], v[208:211], a[248:251]\n"
    "v_cvt_pk_bf16_f32 v169, v170, v171\n"
    "v_mfma_f32_16x16x32_bf16 v[56:59], a[160:163], v[216:219], v[56:59]\n"
    "v_exp_f32_e32 v162, v162\n"
    "v_mfma_f32_16x16x32_bf16 v[56:59], a[164:167], v[220:223], v[56:59]\n"
    "v_exp_f32_e32 v163, v163\n"
    "v_mfma_f32_16x16x32_bf16 v[60:63], a[164:167], v[228:231], v[60:63]\n"
    "v_cvt_pk_bf16_f32 v160, v160, v161\n"
    "v_mfma_f32_16x16x32_bf16 v[60:63], a[160:163], v[224:227], v[60:63]\n"
    "v_cvt_pk_bf16_f32 v161, v162, v163\n"
    "v_mfma_f32_16x16x32_bf16 v[64:67], a[160:163], v[232:235], v[64:67]\n"
    "ds_read_b64_tr_b16 a[160:161], v244 offset:20480\n"
    "v_mfma_f32_16x16x32_bf16 v[64:67], a[164:167], v[236:239], v[64:67]\n"
    "ds_read_b64_tr_b16 a[162:163], v244 offset:22528\n"
    "v_mfma_f32_16x16x32_bf16 v[76:79], a[168:171], v[232:235], v[76:79]\n"
    "ds_read_b64_tr_b16 a[164:165], v244 offset:28672\n"
    "v_mfma_f32_16x16x32_bf16 v[76:79], a[172:175], v[236:239], v[76:79]\n"
    "ds_read_b64_tr_b16 a[166:167], v244 offset:30720\n"
    "v_mfma_f32_16x16x32_bf16 v[72:75], a[172:175], v[228:231], v[72:75]\n"
    "v_exp_f32_e32 v196, v196\n"
    "v_mfma_f32_16x16x32_bf16 v[72:75], a[168:171], v[224:227], v[72:75]\n"
    "v_exp_f32_e32 v197, v197\n"
    "v_mfma_f32_16x16x32_bf16 v[68:71], a[168:171], v[216:219], v[68:71]\n"
    "v_exp_f32_e32 v198, v198\n"
    "v_mfma_f32_16x16x32_bf16 v[68:71], a[172:175], v[220:223], v[68:71]\n"
    "v_exp_f32_e32 v199, v199\n"
    "v_mfma_f32_16x16x32_bf16 a[252:255], a[172:175], v[212:215], a[252:255]\n"
    "v_cvt_pk_bf16_f32 v194, v196, v197\n"
    "v_mfma_f32_16x16x32_bf16 a[252:255], a[168:171], v[208:211], a[252:255]\n"
    "v_exp_f32_e32 v188, v188\n"
    "v_mfma_f32_16x16x32_bf16 v[4:7], a[168:171], v[200:203], v[4:7]\n"
    "ds_read_b64_tr_b16 a[168:169], v245 offset:20480\n"
    "v_mfma_f32_16x16x32_bf16 v[4:7], a[172:175], v[204:207], v[4:7]\n"
    "s_waitcnt lgkmcnt(5)\n"
    "ds_read_b64_tr_b16 a[170:171], v245 offset:22528\n"
    "v_mfma_f32_16x16x32_bf16 v[8:11], a[176:179], v[200:203], v[8:11]\n"
    "ds_read_b64_tr_b16 a[172:173], v245 offset:28672\n"
    "v_mfma_f32_16x16x32_bf16 v[8:11], a[180:183], v[204:207], v[8:11]\n"
    "ds_read_b64_tr_b16 a[174:175], v245 offset:30720\n"
    "v_mfma_f32_16x16x32_bf16 v[32:35], a[180:183], v[212:215], v[32:35]\n"
    "v_exp_f32_e32 v189, v189\n"
    "v_mfma_f32_16x16x32_bf16 v[32:35], a[176:179], v[208:211], v[32:35]\n"
    "v_cvt_pk_bf16_f32 v195, v198, v199\n"
    "v_mfma_f32_16x16x32_bf16 v[80:83], a[176:179], v[216:219], v[80:83]\n"
    "v_exp_f32_e32 v190, v190\n"
    "v_mfma_f32_16x16x32_bf16 v[80:83], a[180:183], v[220:223], v[80:83]\n"
    "v_exp_f32_e32 v191, v191\n"
    "v_mfma_f32_16x16x32_bf16 v[84:87], a[180:183], v[228:231], v[84:87]\n"
    "v_cvt_pk_bf16_f32 v186, v188, v189\n"
    "v_mfma_f32_16x16x32_bf16 v[84:87], a[176:179], v[224:227], v[84:87]\n"
    "v_exp_f32_e32 v180, v180\n"
    "v_mfma_f32_16x16x32_bf16 v[88:91], a[176:179], v[232:235], v[88:91]\n"
    "ds_read_b64_tr_b16 a[176:177], v246 offset:20480\n"
    "v_mfma_f32_16x16x32_bf16 v[88:91], a[180:183], v[236:239], v[88:91]\n"
    "ds_read_b64_tr_b16 a[178:179], v246 offset:22528\n"
    "v_mfma_f32_16x16x32_bf16 v[100:103], a[184:187], v[232:235], v[100:103]\n"
    "ds_read_b64_tr_b16 a[180:181], v246 offset:28672\n"
    "v_mfma_f32_16x16x32_bf16 v[100:103], a[188:191], v[236:239], v[100:103]\n"
    "ds_read_b64_tr_b16 a[182:183], v246 offset:30720\n"
    "v_mfma_f32_16x16x32_bf16 v[96:99], a[188:191], v[228:231], v[96:99]\n"
    "v_exp_f32_e32 v181, v181\n"
    "v_mfma_f32_16x16x32_bf16 v[96:99], a[184:187], v[224:227], v[96:99]\n"
    "v_cvt_pk_bf16_f32 v187, v190, v191\n"
    "v_mfma_f32_16x16x32_bf16 v[92:95], a[184:187], v[216:219], v[92:95]\n"
    "v_exp_f32_e32 v182, v182\n"
    "v_mfma_f32_16x16x32_bf16 v[92:95], a[188:191], v[220:223], v[92:95]\n"
    "v_exp_f32_e32 v183, v183\n"
    "v_mfma_f32_16x16x32_bf16 v[36:39], a[188:191], v[212:215], v[36:39]\n"
    "v_cvt_pk_bf16_f32 v178, v180, v181\n"
    "v_mfma_f32_16x16x32_bf16 v[36:39], a[184:187], v[208:211], v[36:39]\n"
    "v_exp_f32_e32 v172, v172\n"
    "v_mfma_f32_16x16x32_bf16 v[12:15], a[184:187], v[200:203], v[12:15]\n"
    "s_mov_b32 m0, s49\n"
    "ds_read_b64_tr_b16 a[184:185], v247 offset:20480\n"
    "v_mfma_f32_16x16x32_bf16 v[12:15], a[188:191], v[204:207], v[12:15]\n"
    "s_waitcnt lgkmcnt(5)\n"
    "ds_read_b64_tr_b16 a[186:187], v247 offset:22528\n"
    "v_mfma_f32_16x16x32_bf16 v[16:19], a[160:163], v[200:203], v[16:19]\n"
    "ds_read_b64_tr_b16 a[188:189], v247 offset:28672\n"
    "v_mfma_f32_16x16x32_bf16 v[16:19], a[164:167], v[204:207], v[16:19]\n"
    "ds_read_b64_tr_b16 a[190:191], v247 offset:30720\n"
    "v_mfma_f32_16x16x32_bf16 v[40:43], a[164:167], v[212:215], v[40:43]\n"
    "v_exp_f32_e32 v173, v173\n"
    "v_mfma_f32_16x16x32_bf16 v[40:43], a[160:163], v[208:211], v[40:43]\n"
    "v_cvt_pk_bf16_f32 v179, v182, v183\n"
    "v_mfma_f32_16x16x32_bf16 v[104:107], a[160:163], v[216:219], v[104:107]\n"
    "v_exp_f32_e32 v174, v174\n"
    "v_mfma_f32_16x16x32_bf16 v[104:107], a[164:167], v[220:223], v[104:107]\n"
    "s_waitcnt vmcnt(8)\n"
    "v_mfma_f32_16x16x32_bf16 v[108:111], a[164:167], v[228:231], v[108:111]\n"
    "s_barrier\n"
    "v_mfma_f32_16x16x32_bf16 v[108:111], a[160:163], v[224:227], v[108:111]\n"
    "v_exp_f32_e32 v175, v175\n"
    "v_mfma_f32_16x16x32_bf16 v[112:115], a[160:163], v[232:235], v[112:115]\n"
    "ds_read_b128 a[160:163], v243 offset:53248\n"
    "v_mfma_f32_16x16x32_bf16 v[112:115], a[164:167], v[236:239], v[112:115]\n"
    "ds_read_b128 a[164:167], v240 offset:49152\n"
    "v_mfma_f32_16x16x32_bf16 v[124:127], a[168:171], v[232:235], v[124:127]\n"
    "ds_write_addtid_b32 v192 offset:16384\n"
    "v_cvt_pk_bf16_f32 v170, v172, v173\n"
    "v_mfma_f32_16x16x32_bf16 v[124:127], a[172:175], v[236:239], v[124:127]\n"
    "v_exp_f32_e32 v164, v164\n"
    "v_mfma_f32_16x16x32_bf16 v[120:123], a[172:175], v[228:231], v[120:123]\n"
    "v_exp_f32_e32 v165, v165\n"
    "v_mfma_f32_16x16x32_bf16 v[120:123], a[168:171], v[224:227], v[120:123]\n"
    "ds_write_addtid_b32 v193 offset:16640\n"
    "v_cvt_pk_bf16_f32 v171, v174, v175\n"
    "v_mfma_f32_16x16x32_bf16 v[116:119], a[168:171], v[216:219], v[116:119]\n"
    "v_exp_f32_e32 v166, v166\n"
    "v_mfma_f32_16x16x32_bf16 v[116:119], a[172:175], v[220:223], v[116:119]\n"
    "v_exp_f32_e32 v167, v167\n"
    "v_mfma_f32_16x16x32_bf16 v[44:47], a[172:175], v[212:215], v[44:47]\n"
    "ds_write_addtid_b32 v184 offset:12288\n"
    "v_cvt_pk_bf16_f32 v162, v164, v165\n"
    "v_mfma_f32_16x16x32_bf16 v[44:47], a[168:171], v[208:211], v[44:47]\n"
    "ds_write_addtid_b32 v185 offset:12544\n"
    "v_cvt_pk_bf16_f32 v163, v166, v167\n"
    "v_mfma_f32_16x16x32_bf16 v[20:23], a[168:171], v[200:203], v[20:23]\n"
    "ds_read_b128 a[168:171], v241 offset:49152\n"
    "v_mfma_f32_16x16x32_bf16 v[20:23], a[172:175], v[204:207], v[20:23]\n"
    "s_waitcnt lgkmcnt(7)\n"
    "ds_read_b128 a[172:175], v240 offset:53248\n"
    "v_mfma_f32_16x16x32_bf16 v[24:27], a[176:179], v[200:203], v[24:27]\n"
    "ds_write_addtid_b32 v176 offset:8192\n"
    "v_mfma_f32_16x16x32_bf16 v[24:27], a[180:183], v[204:207], v[24:27]\n"
    "ds_write_addtid_b32 v177 offset:8448\n"
    "v_mfma_f32_16x16x32_bf16 v[48:51], a[180:183], v[212:215], v[48:51]\n"
    "ds_write_addtid_b32 v168 offset:4096\n"
    "v_mfma_f32_16x16x32_bf16 v[48:51], a[176:179], v[208:211], v[48:51]\n"
    "ds_write_addtid_b32 v169 offset:4352\n"
    "v_mfma_f32_16x16x32_bf16 v[128:131], a[176:179], v[216:219], v[128:131]\n"
    "ds_write_addtid_b32 v160 offset:0\n"
    "v_mfma_f32_16x16x32_bf16 v[128:131], a[180:183], v[220:223], v[128:131]\n"
    "ds_write_addtid_b32 v161 offset:256\n"
    "v_mfma_f32_16x16x32_bf16 v[132:135], a[180:183], v[228:231], v[132:135]\n"
    "ds_write_addtid_b32 v194 offset:16896\n"
    "v_mfma_f32_16x16x32_bf16 v[132:135], a[176:179], v[224:227], v[132:135]\n"
    "ds_write_addtid_b32 v195 offset:17152\n"
    "v_mfma_f32_16x16x32_bf16 v[136:139], a[176:179], v[232:235], v[136:139]\n"
    "ds_read_b128 a[176:179], v241 offset:53248\n"
    "v_mfma_f32_16x16x32_bf16 v[136:139], a[180:183], v[236:239], v[136:139]\n"
    "ds_read_b128 a[180:183], v242 offset:49152\n"
    "v_mfma_f32_16x16x32_bf16 v[148:151], a[184:187], v[232:235], v[148:151]\n"
    "ds_write_addtid_b32 v186 offset:12800\n"
    "v_mfma_f32_16x16x32_bf16 v[148:151], a[188:191], v[236:239], v[148:151]\n"
    "ds_write_addtid_b32 v187 offset:13056\n"
    "v_mfma_f32_16x16x32_bf16 v[144:147], a[188:191], v[228:231], v[144:147]\n"
    "ds_write_addtid_b32 v178 offset:8704\n"
    "v_mfma_f32_16x16x32_bf16 v[144:147], a[184:187], v[224:227], v[144:147]\n"
    "ds_write_addtid_b32 v179 offset:8960\n"
    "v_mfma_f32_16x16x32_bf16 v[140:143], a[184:187], v[216:219], v[140:143]\n"
    "ds_write_addtid_b32 v170 offset:4608\n"
    "v_mfma_f32_16x16x32_bf16 v[140:143], a[188:191], v[220:223], v[140:143]\n"
    "ds_write_addtid_b32 v171 offset:4864\n"
    "v_mfma_f32_16x16x32_bf16 v[52:55], a[188:191], v[212:215], v[52:55]\n"
    "ds_write_addtid_b32 v162 offset:512\n"
    "v_mfma_f32_16x16x32_bf16 v[52:55], a[184:187], v[208:211], v[52:55]\n"
    "ds_write_addtid_b32 v163 offset:768\n"
    "v_mfma_f32_16x16x32_bf16 v[28:31], a[184:187], v[200:203], v[28:31]\n"
    "ds_read_b128 a[184:187], v243 offset:49152\n"
    "v_mfma_f32_16x16x32_bf16 v[28:31], a[188:191], v[204:207], v[28:31]\n"
    "s_waitcnt lgkmcnt(15)\n"
    "ds_read_b128 a[188:191], v242 offset:53248\n"
    ".Lat_p2e:\n"
    "v_mfma_f32_16x16x32_bf16 v[200:203], a[160:163], a[28:31], 0\n"
    "v_mfma_f32_16x16x32_bf16 v[200:203], a[164:167], a[0:3], v[200:203]\n"
    "v_mfma_f32_16x16x32_bf16 v[208:211], a[164:167], a[32:35], 0\n"
    "v_mfma_f32_16x16x32_bf16 v[208:211], a[160:163], a[60:63], v[208:211]\n"
    "v_mfma_f32_16x16x32_bf16 v[216:219], a[160:163], a[92:95], 0\n"
    "v_mfma_f32_16x16x32_bf16 v[216:219], a[164:167], a[64:67], v[216:219]\n"
    "s_waitcnt lgkmcnt(0)\n"
    "v_mfma_f32_16x16x32_bf16 v[224:227], a[164:167], a[96:99], 0\n"
    "s_barrier\n"
    "v_mfma_f32_16x16x32_bf16 v[224:227], a[160:163], a[124:127], v[224:227]\n"
    "s_add_u32 m0, s43, 16384\n"
    "ds_read2st64_b32 v[164:165], v250 offset0:0 offset1:1\n"
    "v_mfma_f32_16x16x32_bf16 v[232:235], a[160:163], a[156:159], 0\n"
    "ds_read_b128 a[160:163], v243 offset:55296\n"
    "v_mfma_f32_16x16x32_bf16 v[232:235], a[164:167], a[128:131], v[232:235]\n"
    "ds_read_b128 a[164:167], v240 offset:51200\n"
    "v_mfma_f32_16x16x32_bf16 v[232:235], a[168:171], a[132:135], v[232:235]\n"
    "buffer_load_dwordx4 v248, s[8:11], s40 offen offset:0 sc1 lds\n"
    "v_mfma_f32_16x16x32_bf16 v[232:235], a[172:175], a[136:139], v[232:235]\n"
    "v_mfma_f32_16x16x32_bf16 v[224:227], a[172:175], a[104:107], v[224:227]\n"
    "v_mfma_f32_16x16x32_bf16 v[224:227], a[168:171], a[100:103], v[224:227]\n"
    "ds_read2st64_b32 v[166:167], v250 offset0:2 offset1:3\n"
    "v_mfma_f32_16x16x32_bf16 v[216:219], a[168:171], a[68:71], v[216:219]\n"
    "v_mfma_f32_16x16x32_bf16 v[216:219], a[172:175], a[72:75], v[216:219]\n"
    "buffer_load_dwordx4 v248, s[8:11], s40 offen offset:1024 sc1 lds\n"
    "v_mfma_f32_16x16x32_bf16 v[208:211], a[172:175], a[40:43], v[208:211]\n"
    "v_mfma_f32_16x16x32_bf16 v[208:211], a[168:171], a[36:39], v[208:211]\n"
    "ds_read2st64_b32 v[172:173], v250 offset0:16 offset1:17\n"
    "v_mfma_f32_16x16x32_bf16 v[200:203], a[168:171], a[4:7], v[200:203]\n"
    "ds_read_b128 a[168:171], v241 offset:51200\n"
    "v_mfma_f32_16x16x32_bf16 v[200:203], a[172:175], a[8:11], v[200:203]\n"
    "ds_read_b128 a[172:175], v240 offset:55296\n"
    "v_mfma_f32_16x16x32_bf16 v[200:203], a[176:179], a[12:15], v[200:203]\n"
    "buffer_load_dwordx4 v248, s[8:11], s40 offen offset:2048 sc1 lds\n"
    "v_mfma_f32_16x16x32_bf16 v[200:203], a[180:183], a[16:19], v[200:203]\n"
    "ds_read2st64_b32 v[174:175], v250 offset0:18 offset1:19\n"
    "v_mfma_f32_16x16x32_bf16 v[208:211], a[180:183], a[48:51], v[208:211]\n"
    "v_mfma_f32_16x16x32_bf16 v[208:211], a[176:179], a[44:47], v[208:211]\n"
    "v_mfma_f32_16x16x32_bf16 v[216:219], a[176:179], a[76:79], v[216:219]\n"
    "ds_read2st64_b32 v[180:181], v250 offset0:32 offset1:33\n"
    "v_mfma_f32_16x16x32_bf16 v[216:219], a[180:183], a[80:83], v[216:219]\n"
    "buffer_load_dwordx4 v248, s[8:11], s40 offen offset:3072 sc1 lds\n"
    "v_mfma_f32_16x16x32_bf16 v[224:227], a[180:183], a[112:115], v[224:227]\n"
    "s_add_u32 m0, s43, 20480\n"
    "v_mfma_f32_16x16x32_bf16 v[224:227], a[176:179], a[108:111], v[224:227]\n"
    "s_add_u32 s40, s40, 0x8000\n"
    "ds_read2st64_b32 v[182:183], v250 offset0:34 offset1:35\n"
    "v_mfma_f32_16x16x32_bf16 v[232:235], a[176:179], a[140:143], v[232:235]\n"
    "ds_read_b128 a[176:179], v241 offset:55296\n"
    "v_mfma_f32_16x16x32_bf16 v[232:235], a[180:183], a[144:147], v[232:235]\n"
    "ds_read_b128 a[180:183], v242 offset:51200\n"
    "v_mfma_f32_16x16x32_bf16 v[232:235], a[184:187], a[148:151], v[232:235]\n"
    "buffer_load_dwordx4 v248, s[8:11], s41 offen offset:0 sc1 lds\n"
    "v_mfma_f32_16x16x32_bf16 v[232:235], a[188:191], a[152:155], v[232:235]\n"
    "v_mfma_f32_16x16x32_bf16 v[224:227], a[188:191], a[120:123], v[224:227]\n"
    "ds_read2st64_b32 v[188:189], v250 offset0:48 offset1:49\n"
    "v_mfma_f32_16x16x32_bf16 v[224:227], a[184:187], a[116:119], v[224:227]\n"
    "v_mfma_f32_16x16x32_bf16 v[216:219], a[184:187], a[84:87], v[216:219]\n"
    "v_exp_f32_e32 v232, v232\n"
    "v_mfma_f32_16x16x32_bf16 v[216:219], a[188:191], a[88:91], v[216:219]\n"
    "buffer_load_dwordx4 v248, s[8:11], s41 offen offset:1024 sc1 lds\n"
    "v_mfma_f32_16x16x32_bf16 v[208:211], a[188:191], a[56:59], v[208:211]\n"
    "ds_read2st64_b32 v[190:191], v250 offset0:50 offset1:51\n"
    "v_mfma_f32_16x16x32_bf16 v[208:211], a[184:187], a[52:55], v[208:211]\n"
    "v_exp_f32_e32 v233, v233\n"
    "v_mfma_f32_16x16x32_bf16 v[200:203], a[184:187], a[20:23], v[200:203]\n"
    "ds_read_b128 a[184:187], v243 offset:51200\n"
    "v_mfma_f32_16x16x32_bf16 v[200:203], a[188:191], a[24:27], v[200:203]\n"
    "s_waitcnt lgkmcnt(8)\n"
    "ds_read_b128 a[188:191], v242 offset:55296\n"
    "v_mfma_f32_16x16x32_bf16 v[204:207], a[160:163], a[28:31], 0\n"
    "buffer_load_dwordx4 v248, s[8:11], s41 offen offset:2048 sc1 lds\n"
    "v_mfma_f32_16x16x32_bf16 v[204:207], a[164:167], a[0:3], v[204:207]\n"
    "ds_read2st64_b32 v[196:197], v250 offset0:64 offset1:65\n"
    "v_mfma_f32_16x16x32_bf16 v[212:215], a[164:167], a[32:35], 0\n"
    "v_exp_f32_e32 v234, v234\n"
    "v_mfma_f32_16x16x32_bf16 v[212:215], a[160:163], a[60:63], v[212:215]\n"
    "ds_read2st64_b32 v[198:199], v250 offset0:66 offset1:67\n"
    "v_mfma_f32_16x16x32_bf16 v[220:223], a[160:163], a[92:95], 0\n"
    "v_exp_f32_e32 v235, v235\n"
    "v_mfma_f32_16x16x32_bf16 v[220:223], a[164:167], a[64:67], v[220:223]\n"
    "buffer_load_dwordx4 v248, s[8:11], s41 offen offset:3072 sc1 lds\n"
    "v_cvt_pk_bf16_f32 v232, v232, v233\n"
    "v_mfma_f32_16x16x32_bf16 v[228:231], a[164:167], a[96:99], 0\n"
    "s_add_u32 s41, s41, 0x8000\n"
    "v_exp_f32_e32 v224, v224\n"
    "v_mfma_f32_16x16x32_bf16 v[228:231], a[160:163], a[124:127], v[228:231]\n"
    "v_exp_f32_e32 v225, v225\n"
    "v_mfma_f32_16x16x32_bf16 v[236:239], a[160:163], a[156:159], 0\n"
    "ds_read_b64_tr_b16 a[160:161], v244 offset:32768\n"
    "v_cvt_pk_bf16_f32 v233, v234, v235\n"
    "v_mfma_f32_16x16x32_bf16 v[236:239], a[164:167], a[128:131], v[236:239]\n"
    "ds_read_b64_tr_b16 a[162:163], v244 offset:34816\n"
    "v_mfma_f32_16x16x32_bf16 v[236:239], a[168:171], a[132:135], v[236:239]\n"
    "ds_read_b64_tr_b16 a[164:165], v244 offset:40960\n"
    "v_mfma_f32_16x16x32_bf16 v[236:239], a[172:175], a[136:139], v[236:239]\n"
    "ds_read_b64_tr_b16 a[166:167], v244 offset:43008\n"
    "v_mfma_f32_16x16x32_bf16 v[228:231], a[172:175], a[104:107], v[228:231]\n"
    "v_exp_f32_e32 v226, v226\n"
    "v_mfma_f32_16x16x32_bf16 v[228:231], a[168:171], a[100:103], v[228:231]\n"
    "v_exp_f32_e32 v227, v227\n"
    "v_mfma_f32_16x16x32_bf16 v[220:223], a[168:171], a[68:71], v[220:223]\n"
    "v_cvt_pk_bf16_f32 v224, v224, v225\n"
    "v_mfma_f32_16x16x32_bf16 v[220:223], a[172:175], a[72:75], v[220:223]\n"
    "v_exp_f32_e32 v216, v216\n"
    "v_mfma_f32_16x16x32_bf16 v[212:215], a[172:175], a[40:43], v[212:215]\n"
    "v_exp_f32_e32 v217, v217\n"
    "v_mfma_f32_16x16x32_bf16 v[212:215], a[168:171], a[36:39], v[212:215]\n"
    "v_cvt_pk_bf16_f32 v225, v226, v227\n"
    "v_mfma_f32_16x16x32_bf16 v[204:207], a[168:171], a[4:7], v[204:207]\n"
    "ds_read_b64_tr_b16 a[168:169], v245 offset:32768\n"
    "v_mfma_f32_16x16x32_bf16 v[204:207], a[172:175], a[8:11], v[204:207]\n"
    "s_waitcnt lgkmcnt(7)\n"
    "ds_read_b64_tr_b16 a[170:171], v245 offset:34816\n"
    "v_mfma_f32_16x16x32_bf16 v[204:207], a[176:179], a[12:15], v[204:207]\n"
    "ds_read_b64_tr_b16 a[172:173], v245 offset:40960\n"
    "v_mfma_f32_16x16x32_bf16 v[204:207], a[180:183], a[16:19], v[204:207]\n"
    "ds_read_b64_tr_b16 a[174:175], v245 offset:43008\n"
    "v_mfma_f32_16x16x32_bf16 v[212:215], a[180:183], a[48:51], v[212:215]\n"
    "v_exp_f32_e32 v218, v218\n"
    "v_mfma_f32_16x16x32_bf16 v[212:215], a[176:179], a[44:47], v[212:215]\n"
    "v_exp_f32_e32 v219, v219\n"
    "v_mfma_f32_16x16x32_bf16 v[220:223], a[176:179], a[76:79], v[220:223]\n"
    "v_cvt_pk_bf16_f32 v216, v216, v217\n"
    "v_mfma_f32_16x16x32_bf16 v[220:223], a[180:183], a[80:83], v[220:223]\n"
    "v_exp_f32_e32 v208, v208\n"
    "v_mfma_f32_16x16x32_bf16 v[228:231], a[180:183], a[112:115], v[228:231]\n"
    "v_exp_f32_e32 v209, v209\n"
    "v_mfma_f32_16x16x32_bf16 v[228:231], a[176:179], a[108:111], v[228:231]\n"
    "v_cvt_pk_bf16_f32 v217, v218, v219\n"
    "v_mfma_f32_16x16x32_bf16 v[236:239], a[176:179], a[140:143], v[236:239]\n"
    "ds_read_b64_tr_b16 a[176:177], v246 offset:32768\n"
    "v_mfma_f32_16x16x32_bf16 v[236:239], a[180:183], a[144:147], v[236:239]\n"
    "ds_read_b64_tr_b16 a[178:179], v246 offset:34816\n"
    "v_mfma_f32_16x16x32_bf16 v[236:239], a[184:187], a[148:151], v[236:239]\n"
    "ds_read_b64_tr_b16 a[180:181], v246 offset:40960\n"
    "v_mfma_f32_16x16x32_bf16 v[236:239], a[188:191], a[152:155], v[236:239]\n"
    "ds_read_b64_tr_b16 a[182:183], v246 offset:43008\n"
    "v_mfma_f32_16x16x32_bf16 v[228:231], a[188:191], a[120:123], v[228:231]\n"
    "v_exp_f32_e32 v210, v210\n"
    "v_mfma_f32_16x16x32_bf16 v[228:231], a[184:187], a[116:119], v[228:231]\n"
    "v_exp_f32_e32 v211, v211\n"
    "v_mfma_f32_16x16x32_bf16 v[220:223], a[184:187], a[84:87], v[220:223]\n"
    "v_cvt_pk_bf16_f32 v208, v208, v209\n"
    "v_mfma_f32_16x16x32_bf16 v[220:223], a[188:191], a[88:91], v[220:223]\n"
    "v_exp_f32_e32 v200, v200\n"
    "v_mfma_f32_16x16x32_bf16 v[212:215], a[188:191], a[56:59], v[212:215]\n"
    "v_exp_f32_e32 v201, v201\n"
    "v_mfma_f32_16x16x32_bf16 v[212:215], a[184:187], a[52:55], v[212:215]\n"
    "v_cvt_pk_bf16_f32 v209, v210, v211\n"
    "v_mfma_f32_16x16x32_bf16 v[204:207], a[184:187], a[20:23], v[204:207]\n"
    "ds_read_b64_tr_b16 a[184:185], v247 offset:32768\n"
    "v_mfma_f32_16x16x32_bf16 v[204:207], a[188:191], a[24:27], v[204:207]\n"
    "s_waitcnt lgkmcnt(5)\n"
    "ds_read_b64_tr_b16 a[186:187], v247 offset:34816\n"
    "v_mfma_f32_16x16x32_bf16 v[0:3], a[160:163], v[160:163], v[0:3]\n"
    "ds_read_b64_tr_b16 a[188:189], v247 offset:40960\n"
    "v_mfma_f32_16x16x32_bf16 v[0:3], a[164:167], v[164:167], v[0:3]\n"
    "ds_read_b64_tr_b16 a[190:191], v247 offset:43008\n"
    "v_mfma_f32_16x16x32_bf16 a[248:251], a[164:167], v[172:175], a[248:251]\n"
    "v_exp_f32_e32 v202, v202\n"
    "v_mfma_f32_16x16x32_bf16 a[248:251], a[160:163], v[168:171], a[248:251]\n"
    "v_exp_f32_e32 v203, v203\n"
    "v_mfma_f32_16x16x32_bf16 v[56:59], a[160:163], v[176:179], v[56:59]\n"
    "v_cvt_pk_bf16_f32 v200, v200, v201\n"
    "v_mfma_f32_16x16x32_bf16 v[56:59], a[164:167], v[180:183], v[56:59]\n"
    "v_cvt_pk_bf16_f32 v201, v202, v203\n"
    "v_mfma_f32_16x16x32_bf16 v[60:63], a[164:167], v[188:191], v[60:63]\n"
    "v_exp_f32_e32 v236, v236\n"
    "v_mfma_f32_16x16x32_bf16 v[60:63], a[160:163], v[184:187], v[60:63]\n"
    "v_exp_f32_e32 v237, v237\n"
    "v_mfma_f32_16x16x32_bf16 v[64:67], a[160:163], v[192:195], v[64:67]\n"
    "ds_read_b64_tr_b16 a[160:161], v244 offset:36864\n"
    "v_mfma_f32_16x16x32_bf16 v[64:67], a[164:167], v[196:199], v[64:67]\n"
    "ds_read_b64_tr_b16 a[162:163], v244 offset:38912\n"
    "v_mfma_f32_16x16x32_bf16 v[76:79], a[168:171], v[192:195], v[76:79]\n"
    "ds_read_b64_tr_b16 a[164:165], v244 offset:45056\n"
    "v_mfma_f32_16x16x32_bf16 v[76:79], a[172:175], v[196:199], v[76:79]\n"
    "ds_read_b64_tr_b16 a[166:167], v244 offset:47104\n"
    "v_mfma_f32_16x16x32_bf16 v[72:75], a[172:175], v[188:191], v[72:75]\n"
    "v_exp_f32_e32 v238, v238\n"
    "v_mfma_f32_16x16x32_bf16 v[72:75], a[168:171], v[184:187], v[72:75]\n"
    "v_exp_f32_e32 v239, v239\n"
    "v_mfma_f32_16x16x32_bf16 v[68:71], a[168:171], v[176:179], v[68:71]\n"
    "v_cvt_pk_bf16_f32 v234, v236, v237\n"
    "v_mfma_f32_16x16x32_bf16 v[68:71], a[172:175], v[180:183], v[68:71]\n"
    "v_exp_f32_e32 v228, v228\n"
    "v_mfma_f32_16x16x32_bf16 a[252:255], a[172:175], v[172:175], a[252:255]\n"
    "v_exp_f32_e32 v229, v229\n"
    "v_mfma_f32_16x16x32_bf16 a[252:255], a[168:171], v[168:171], a[252:255]\n"
    "v_cvt_pk_bf16_f32 v235, v238, v239\n"
    "v_mfma_f32_16x16x32_bf16 v[4:7], a[168:171], v[160:163], v[4:7]\n"
    "ds_read_b64_tr_b16 a[168:169], v245 offset:36864\n"
    "v_mfma_f32_16x16x32_bf16 v[4:7], a[172:175], v[164:167], v[4:7]\n"
    "s_waitcnt lgkmcnt(5)\n"
    "ds_read_b64_tr_b16 a[170:171], v245 offset:38912\n"
    "v_mfma_f32_16x16x32_bf16 v[8:11], a[176:179], v[160:163], v[8:11]\n"
    "ds_read_b64_tr_b16 a[172:173], v245 offset:45056\n"
    "v_mfma_f32_16x16x32_bf16 v[8:11], a[180:183], v[164:167], v[8:11]\n"
    "ds_read_b64_tr_b16 a[174:175], v245 offset:47104\n"
    "v_mfma_f32_16x16x32_bf16 v[32:35], a[180:183], v[172:175], v[32:35]\n"
    "s_or_b32 s45, s44, s37\n"
    "s_cselect_b32 s46, 2.0, s81\n"
    "s_cselect_b32 s45, 2.0, s80\n"
    "v_mfma_f32_16x16x32_bf16 v[32:35], a[176:179], v[168:171], v[32:35]\n"
    "v_exp_f32_e32 v230, v230\n"
    "v_mfma_f32_16x16x32_bf16 v[80:83], a[176:179], v[176:179], v[80:83]\n"
    "v_exp_f32_e32 v231, v231\n"
    "v_mfma_f32_16x16x32_bf16 v[80:83], a[180:183], v[180:183], v[80:83]\n"
    "v_cvt_pk_bf16_f32 v226, v228, v229\n"
    "v_mfma_f32_16x16x32_bf16 v[84:87], a[180:183], v[188:191], v[84:87]\n"
    "v_exp_f32_e32 v220, v220\n"
    "v_mfma_f32_16x16x32_bf16 v[84:87], a[176:179], v[184:187], v[84:87]\n"
    "v_exp_f32_e32 v221, v221\n"
    "v_mfma_f32_16x16x32_bf16 v[88:91], a[176:179], v[192:195], v[88:91]\n"
    "ds_read_b64_tr_b16 a[176:177], v246 offset:36864\n"
    "v_mfma_f32_16x16x32_bf16 v[88:91], a[180:183], v[196:199], v[88:91]\n"
    "ds_read_b64_tr_b16 a[178:179], v246 offset:38912\n"
    "v_mfma_f32_16x16x32_bf16 v[100:103], a[184:187], v[192:195], v[100:103]\n"
    "ds_read_b64_tr_b16 a[180:181], v246 offset:45056\n"
    "v_mfma_f32_16x16x32_bf16 v[100:103], a[188:191], v[196:199], v[100:103]\n"
    "ds_read_b64_tr_b16 a[182:183], v246 offset:47104\n"
    "v_mfma_f32_16x16x32_bf16 v[96:99], a[188:191], v[188:191], v[96:99]\n"
    "v_cvt_pk_bf16_f32 v227, v230, v231\n"
    "v_mfma_f32_16x16x32_bf16 v[96:99], a[184:187], v[184:187], v[96:99]\n"
    "v_exp_f32_e32 v222, v222\n"
    "v_mfma_f32_16x16x32_bf16 v[92:95], a[184:187], v[176:179], v[92:95]\n"
    "v_exp_f32_e32 v223, v223\n"
    "v_mfma_f32_16x16x32_bf16 v[92:95], a[188:191], v[180:183], v[92:95]\n"
    "v_cvt_pk_bf16_f32 v218, v220, v221\n"
    "v_mfma_f32_16x16x32_bf16 v[36:39], a[188:191], v[172:175], v[36:39]\n"
    "v_exp_f32_e32 v212, v212\n"
    "v_mfma_f32_16x16x32_bf16 v[36:39], a[184:187], v[168:171], v[36:39]\n"
    "v_exp_f32_e32 v213, v213\n"
    "v_mfma_f32_16x16x32_bf16 v[12:15], a[184:187], v[160:163], v[12:15]\n"
    "s_mov_b32 m0, s49\n"
    "ds_read_b64_tr_b16 a[184:185], v247 offset:36864\n"
    "v_mfma_f32_16x16x32_bf16 v[12:15], a[188:191], v[164:167], v[12:15]\n"
    "s_waitcnt lgkmcnt(5)\n"
    "ds_read_b64_tr_b16 a[186:187], v247 offset:38912\n"
    "v_mfma_f32_16x16x32_bf16 v[16:19], a[160:163], v[160:163], v[16:19]\n"
    "ds_read_b64_tr_b16 a[188:189], v247 offset:45056\n"
    "v_mfma_f32_16x16x32_bf16 v[16:19], a[164:167], v[164:167], v[16:19]\n"
    "ds_read_b64_tr_b16 a[190:191], v247 offset:47104\n"
    "v_mfma_f32_16x16x32_bf16 v[40:43], a[164:167], v[172:175], v[40:43]\n"
    "buffer_load_dword v154, off, s[8:11], s45 sc1\n"
    "v_mfma_f32_16x16x32_bf16 v[40:43], a[160:163], v[168:171], v[40:43]\n"
    "v_cvt_pk_bf16_f32 v219, v222, v223\n"
    "v_mfma_f32_16x16x32_bf16 v[104:107], a[160:163], v[176:179], v[104:107]\n"
    "v_exp_f32_e32 v214, v214\n"
    "v_mfma_f32_16x16x32_bf16 v[104:107], a[164:167], v[180:183], v[104:107]\n"
    "s_waitcnt vmcnt(9)\n"
    "v_mfma_f32_16x16x32_bf16 v[108:111], a[164:167], v[188:191], v[108:111]\n"
    "s_barrier\n"
    "v_mfma_f32_16x16x32_bf16 v[108:111], a[160:163], v[184:187], v[108:111]\n"
    "v_exp_f32_e32 v215, v215\n"
    "v_mfma_f32_16x16x32_bf16 v[112:115], a[160:163], v[192:195], v[112:115]\n"
    "ds_read_b128 a[160:163], v243 offset:4096\n"
    "v_mfma_f32_16x16x32_bf16 v[112:115], a[164:167], v[196:199], v[112:115]\n"
    "ds_read_b128 a[164:167], v240 offset:0\n"
    "v_mfma_f32_16x16x32_bf16 v[124:127], a[168:171], v[192:195], v[124:127]\n"
    "ds_write_addtid_b32 v232 offset:16384\n"
    "v_cvt_pk_bf16_f32 v210, v212, v213\n"
    "v_mfma_f32_16x16x32_bf16 v[124:127], a[172:175], v[196:199], v[124:127]\n"
    "v_exp_f32_e32 v204, v204\n"
    "v_mfma_f32_16x16x32_bf16 v[120:123], a[172:175], v[188:191], v[120:123]\n"
    "v_exp_f32_e32 v205, v205\n"
    "v_mfma_f32_16x16x32_bf16 v[120:123], a[168:171], v[184:187], v[120:123]\n"
    "ds_write_addtid_b32 v233 offset:16640\n"
    "v_cvt_pk_bf16_f32 v211, v214, v215\n"
    "v_mfma_f32_16x16x32_bf16 v[116:119], a[168:171], v[176:179], v[116:119]\n"
    "v_exp_f32_e32 v206, v206\n"
    "v_mfma_f32_16x16x32_bf16 v[116:119], a[172:175], v[180:183], v[116:119]\n"
    "v_exp_f32_e32 v207, v207\n"
    "v_mfma_f32_16x16x32_bf16 v[44:47], a[172:175], v[172:175], v[44:47]\n"
    "ds_write_addtid_b32 v224 offset:12288\n"
    "v_cvt_pk_bf16_f32 v202, v204, v205\n"
    "v_mfma_f32_16x16x32_bf16 v[44:47], a[168:171], v[168:171], v[44:47]\n"
    "ds_write_addtid_b32 v225 offset:12544\n"
    "v_cvt_pk_bf16_f32 v203, v206, v207\n"
    "v_mfma_f32_16x16x32_bf16 v[20:23], a[168:171], v[160:163], v[20:23]\n"
    "ds_read_b128 a[168:171], v241 offset:0\n"
    "v_mfma_f32_16x16x32_bf16 v[20:23], a[172:175], v[164:167], v[20:23]\n"
    "s_waitcnt lgkmcnt(7)\n"
    "ds_read_b128 a[172:175], v240 offset:4096\n"
    "v_mfma_f32_16x16x32_bf16 v[24:27], a[176:179], v[160:163], v[24:27]\n"
    "ds_write_addtid_b32 v216 offset:8192\n"
    "v_mfma_f32_16x16x32_bf16 v[24:27], a[180:183], v[164:167], v[24:27]\n"
    "ds_write_addtid_b32 v217 offset:8448\n"
    "v_mfma_f32_16x16x32_bf16 v[48:51], a[180:183], v[172:175], v[48:51]\n"
    "ds_write_addtid_b32 v208 offset:4096\n"
    "v_mfma_f32_16x16x32_bf16 v[48:51], a[176:179], v[168:171], v[48:51]\n"
    "ds_write_addtid_b32 v209 offset:4352\n"
    "v_mfma_f32_16x16x32_bf16 v[128:131], a[176:179], v[176:179], v[128:131]\n"
    "ds_write_addtid_b32 v200 offset:0\n"
    "v_mfma_f32_16x16x32_bf16 v[128:131], a[180:183], v[180:183], v[128:131]\n"
    "ds_write_addtid_b32 v201 offset:256\n"
    "v_mfma_f32_16x16x32_bf16 v[132:135], a[180:183], v[188:191], v[132:135]\n"
    "ds_write_addtid_b32 v234 offset:16896\n"
    "v_mfma_f32_16x16x32_bf16 v[132:135], a[176:179], v[184:187], v[132:135]\n"
    "ds_write_addtid_b32 v235 offset:17152\n"
    "v_mfma_f32_16x16x32_bf16 v[136:139], a[176:179], v[192:195], v[136:139]\n"
    "ds_read_b128 a[176:179], v241 offset:4096\n"
    "v_mfma_f32_16x16x32_bf16 v[136:139], a[180:183], v[196:199], v[136:139]\n"
    "ds_read_b128 a[180:183], v242 offset:0\n"
    "v_mfma_f32_16x16x32_bf16 v[148:151], a[184:187], v[192:195], v[148:151]\n"
    "ds_write_addtid_b32 v226 offset:12800\n"
    "v_mfma_f32_16x16x32_bf16 v[148:151], a[188:191], v[196:199], v[148:151]\n"
    "ds_write_addtid_b32 v227 offset:13056\n"
    "v_mfma_f32_16x16x32_bf16 v[144:147], a[188:191], v[188:191], v[144:147]\n"
    "ds_write_addtid_b32 v218 offset:8704\n"
    "v_mfma_f32_16x16x32_bf16 v[144:147], a[184:187], v[184:187], v[144:147]\n"
    "ds_write_addtid_b32 v219 offset:8960\n"
    "v_mfma_f32_16x16x32_bf16 v[140:143], a[184:187], v[176:179], v[140:143]\n"
    "ds_write_addtid_b32 v210 offset:4608\n"
    "v_mfma_f32_16x16x32_bf16 v[140:143], a[188:191], v[180:183], v[140:143]\n"
    "ds_write_addtid_b32 v211 offset:4864\n"
    "v_mfma_f32_16x16x32_bf16 v[52:55], a[188:191], v[172:175], v[52:55]\n"
    "ds_write_addtid_b32 v202 offset:512\n"
    "v_mfma_f32_16x16x32_bf16 v[52:55], a[184:187], v[168:171], v[52:55]\n"
    "ds_write_addtid_b32 v203 offset:768\n"
    "v_mfma_f32_16x16x32_bf16 v[28:31], a[184:187], v[160:163], v[28:31]\n"
    "ds_read_b128 a[184:187], v243 offset:0\n"
    "v_mfma_f32_16x16x32_bf16 v[28:31], a[188:191], v[164:167], v[28:31]\n"
    "s_waitcnt lgkmcnt(15)\n"
    "ds_read_b128 a[188:191], v242 offset:4096\n"
    "v_mfma_f32_16x16x32_bf16 v[160:163], a[160:163], a[28:31], 0\n"
    "v_lshlrev_b32_e32 v253, 2, v249\n"
    "v_mfma_f32_16x16x32_bf16 v[160:163], a[164:167], a[0:3], v[160:163]\n"
    "v_mfma_f32_16x16x32_bf16 v[168:171], a[164:167], a[32:35], 0\n"
    "v_mfma_f32_16x16x32_bf16 v[168:171], a[160:163], a[60:63], v[168:171]\n"
    "v_mfma_f32_16x16x32_bf16 v[176:179], a[160:163], a[92:95], 0\n"
    "v_mfma_f32_16x16x32_bf16 v[176:179], a[164:167], a[64:67], v[176:179]\n"
    "s_waitcnt lgkmcnt(0)\n"
    "v_mfma_f32_16x16x32_bf16 v[184:187], a[164:167], a[96:99], 0\n"
    "s_barrier\n"
    "v_mfma_f32_16x16x32_bf16 v[184:187], a[160:163], a[124:127], v[184:187]\n"
    "s_add_u32 m0, s43, 32768\n"
    "ds_read2st64_b32 v[204:205], v250 offset0:0 offset1:1\n"
    "v_mfma_f32_16x16x32_bf16 v[192:195], a[160:163], a[156:159], 0\n"
    "ds_read_b128 a[160:163], v243 offset:6144\n"
    "v_mfma_f32_16x16x32_bf16 v[192:195], a[164:167], a[128:131], v[192:195]\n"
    "ds_read_b128 a[164:167], v240 offset:2048\n"
    "v_mfma_f32_16x16x32_bf16 v[192:195], a[168:171], a[132:135], v[192:195]\n"
    "buffer_load_dwordx4 v248, s[8:11], s40 offen offset:0 sc1 lds\n"
    "v_mfma_f32_16x16x32_bf16 v[192:195], a[172:175], a[136:139], v[192:195]\n"
    "v_mfma_f32_16x16x32_bf16 v[184:187], a[172:175], a[104:107], v[184:187]\n"
    "v_mfma_f32_16x16x32_bf16 v[184:187], a[168:171], a[100:103], v[184:187]\n"
    "ds_read2st64_b32 v[206:207], v250 offset0:2 offset1:3\n"
    "v_mfma_f32_16x16x32_bf16 v[176:179], a[168:171], a[68:71], v[176:179]\n"
    "v_mfma_f32_16x16x32_bf16 v[176:179], a[172:175], a[72:75], v[176:179]\n"
    "buffer_load_dwordx4 v248, s[8:11], s40 offen offset:1024 sc1 lds\n"
    "v_mfma_f32_16x16x32_bf16 v[168:171], a[172:175], a[40:43], v[168:171]\n"
    "v_mfma_f32_16x16x32_bf16 v[168:171], a[168:171], a[36:39], v[168:171]\n"
    "ds_read2st64_b32 v[212:213], v250 offset0:16 offset1:17\n"
    "v_mfma_f32_16x16x32_bf16 v[160:163], a[168:171], a[4:7], v[160:163]\n"
    "ds_read_b128 a[168:171], v241 offset:2048\n"
    "v_mfma_f32_16x16x32_bf16 v[160:163], a[172:175], a[8:11], v[160:163]\n"
    "ds_read_b128 a[172:175], v240 offset:6144\n"
    "v_mfma_f32_16x16x32_bf16 v[160:163], a[176:179], a[12:15], v[160:163]\n"
    "buffer_load_dwordx4 v248, s[8:11], s40 offen offset:2048 sc1 lds\n"
    "v_mfma_f32_16x16x32_bf16 v[160:163], a[180:183], a[16:19], v[160:163]\n"
    "ds_read2st64_b32 v[214:215], v250 offset0:18 offset1:19\n"
    "v_mfma_f32_16x16x32_bf16 v[168:171], a[180:183], a[48:51], v[168:171]\n"
    "v_mfma_f32_16x16x32_bf16 v[168:171], a[176:179], a[44:47], v[168:171]\n"
    "v_mfma_f32_16x16x32_bf16 v[176:179], a[176:179], a[76:79], v[176:179]\n"
    "ds_read2st64_b32 v[220:221], v250 offset0:32 offset1:33\n"
    "v_mfma_f32_16x16x32_bf16 v[176:179], a[180:183], a[80:83], v[176:179]\n"
    "buffer_load_dwordx4 v248, s[8:11], s40 offen offset:3072 sc1 lds\n"
    "v_mfma_f32_16x16x32_bf16 v[184:187], a[180:183], a[112:115], v[184:187]\n"
    "s_add_u32 m0, s43, 36864\n"
    "v_mfma_f32_16x16x32_bf16 v[184:187], a[176:179], a[108:111], v[184:187]\n"
    "s_add_u32 s40, s40, 0x8000\n"
    "ds_read2st64_b32 v[222:223], v250 offset0:34 offset1:35\n"
    "v_mfma_f32_16x16x32_bf16 v[192:195], a[176:179], a[140:143], v[192:195]\n"
    "ds_read_b128 a[176:179], v241 offset:6144\n"
    "v_mfma_f32_16x16x32_bf16 v[192:195], a[180:183], a[144:147], v[192:195]\n"
    "ds_read_b128 a[180:183], v242 offset:2048\n"
    "v_mfma_f32_16x16x32_bf16 v[192:195], a[184:187], a[148:151], v[192:195]\n"
    "buffer_load_dwordx4 v248, s[8:11], s41 offen offset:0 sc1 lds\n"
    "v_mfma_f32_16x16x32_bf16 v[192:195], a[188:191], a[152:155], v[192:195]\n"
    "v_mfma_f32_16x16x32_bf16 v[184:187], a[188:191], a[120:123], v[184:187]\n"
    "ds_read2st64_b32 v[228:229], v250 offset0:48 offset1:49\n"
    "v_mfma_f32_16x16x32_bf16 v[184:187], a[184:187], a[116:119], v[184:187]\n"
    "v_mfma_f32_16x16x32_bf16 v[176:179], a[184:187], a[84:87], v[176:179]\n"
    "v_exp_f32_e32 v192, v192\n"
    "v_mfma_f32_16x16x32_bf16 v[176:179], a[188:191], a[88:91], v[176:179]\n"
    "buffer_load_dwordx4 v248, s[8:11], s41 offen offset:1024 sc1 lds\n"
    "v_mfma_f32_16x16x32_bf16 v[168:171], a[188:191], a[56:59], v[168:171]\n"
    "ds_read2st64_b32 v[230:231], v250 offset0:50 offset1:51\n"
    "v_mfma_f32_16x16x32_bf16 v[168:171], a[184:187], a[52:55], v[168:171]\n"
    "v_exp_f32_e32 v193, v193\n"
    "v_mfma_f32_16x16x32_bf16 v[160:163], a[184:187], a[20:23], v[160:163]\n"
    "ds_read_b128 a[184:187], v243 offset:2048\n"
    "v_mfma_f32_16x16x32_bf16 v[160:163], a[188:191], a[24:27], v[160:163]\n"
    "s_waitcnt lgkmcnt(8)\n"
    "ds_read_b128 a[188:191], v242 offset:6144\n"
    "v_mfma_f32_16x16x32_bf16 v[164:167], a[160:163], a[28:31], 0\n"
    "buffer_load_dwordx4 v248, s[8:11], s41 offen offset:2048 sc1 lds\n"
    "v_mfma_f32_16x16x32_bf16 v[164:167], a[164:167], a[0:3], v[164:167]\n"
    "ds_read2st64_b32 v[236:237], v250 offset0:64 offset1:65\n"
    "v_mfma_f32_16x16x32_bf16 v[172:175], a[164:167], a[32:35], 0\n"
    "v_exp_f32_e32 v194, v194\n"
    "v_mfma_f32_16x16x32_bf16 v[172:175], a[160:163], a[60:63], v[172:175]\n"
    "ds_read2st64_b32 v[238:239], v250 offset0:66 offset1:67\n"
    "v_mfma_f32_16x16x32_bf16 v[180:183], a[160:163], a[92:95], 0\n"
    "v_exp_f32_e32 v195, v195\n"
    "v_mfma_f32_16x16x32_bf16 v[180:183], a[164:167], a[64:67], v[180:183]\n"
    "buffer_load_dwordx4 v248, s[8:11], s41 offen offset:3072 sc1 lds\n"
    "v_cvt_pk_bf16_f32 v192, v192, v193\n"
    "v_mfma_f32_16x16x32_bf16 v[188:191], a[164:167], a[96:99], 0\n"
    "s_add_u32 s41, s41, 0x8000\n"
    "v_exp_f32_e32 v184, v184\n"
    "v_mfma_f32_16x16x32_bf16 v[188:191], a[160:163], a[124:127], v[188:191]\n"
    "v_exp_f32_e32 v185, v185\n"
    "v_mfma_f32_16x16x32_bf16 v[196:199], a[160:163], a[156:159], 0\n"
    "ds_read_b64_tr_b16 a[160:161], v244 offset:49152\n"
    "v_mfma_f32_16x16x32_bf16 v[196:199], a[164:167], a[128:131], v[196:199]\n"
    "ds_read_b64_tr_b16 a[162:163], v244 offset:51200\n"
    "v_mfma_f32_16x16x32_bf16 v[196:199], a[168:171], a[132:135], v[196:199]\n"
    "ds_read_b64_tr_b16 a[164:165], v244 offset:57344\n"
    "v_mfma_f32_16x16x32_bf16 v[196:199], a[172:175], a[136:139], v[196:199]\n"
    "ds_read_b64_tr_b16 a[166:167], v244 offset:59392\n"
    "v_mfma_f32_16x16x32_bf16 v[188:191], a[172:175], a[104:107], v[188:191]\n"
    "v_cvt_pk_bf16_f32 v193, v194, v195\n"
    "v_mfma_f32_16x16x32_bf16 v[188:191], a[168:171], a[100:103], v[188:191]\n"
    "v_exp_f32_e32 v186, v186\n"
    "v_mfma_f32_16x16x32_bf16 v[180:183], a[168:171], a[68:71], v[180:183]\n"
    "v_exp_f32_e32 v187, v187\n"
    "v_mfma_f32_16x16x32_bf16 v[180:183], a[172:175], a[72:75], v[180:183]\n"
    "v_cvt_pk_bf16_f32 v184, v184, v185\n"
    "v_mfma_f32_16x16x32_bf16 v[172:175], a[172:175], a[40:43], v[172:175]\n"
    "v_exp_f32_e32 v176, v176\n"
    "v_mfma_f32_16x16x32_bf16 v[172:175], a[168:171], a[36:39], v[172:175]\n"
    "v_exp_f32_e32 v177, v177\n"
    "v_mfma_f32_16x16x32_bf16 v[164:167], a[168:171], a[4:7], v[164:167]\n"
    "ds_read_b64_tr_b16 a[168:169], v245 offset:49152\n"
    "v_mfma_f32_16x16x32_bf16 v[164:167], a[172:175], a[8:11], v[164:167]\n"
    "s_waitcnt lgkmcnt(7)\n"
    "ds_read_b64_tr_b16 a[170:171], v245 offset:51200\n"
    "v_mfma_f32_16x16x32_bf16 v[164:167], a[176:179], a[12:15], v[164:167]\n"
    "s_mov_b32 m0, s82\n"
    "ds_read_b64_tr_b16 a[172:173], v245 offset:57344\n"
    "v_mfma_f32_16x16x32_bf16 v[164:167], a[180:183], a[16:19], v[164:167]\n"
    "s_waitcnt vmcnt(8)\n"
    "ds_read_b64_tr_b16 a[174:175], v245 offset:59392\n"
    "v_mfma_f32_16x16x32_bf16 v[172:175], a[180:183], a[48:51], v[172:175]\n"
    "buffer_load_dwordx4 v253, s[8:11], s46 offen offset:0 sc1 lds\n"
    "v_mfma_f32_16x16x32_bf16 v[172:175], a[176:179], a[44:47], v[172:175]\n"
    "v_cvt_pk_bf16_f32 v185, v186, v187\n"
    "v_mfma_f32_16x16x32_bf16 v[180:183], a[176:179], a[76:79], v[180:183]\n"
    "v_exp_f32_e32 v178, v178\n"
    "v_mfma_f32_16x16x32_bf16 v[180:183], a[180:183], a[80:83], v[180:183]\n"
    "v_exp_f32_e32 v179, v179\n"
    "v_mfma_f32_16x16x32_bf16 v[188:191], a[180:183], a[112:115], v[188:191]\n"
    "v_cvt_pk_bf16_f32 v176, v176, v177\n"
    "v_mfma_f32_16x16x32_bf16 v[188:191], a[176:179], a[108:111], v[188:191]\n"
    "v_exp_f32_e32 v168, v168\n"
    "v_mfma_f32_16x16x32_bf16 v[196:199], a[176:179], a[140:143], v[196:199]\n"
    "ds_read_b64_tr_b16 a[176:177], v246 offset:49152\n"
    "v_mfma_f32_16x16x32_bf16 v[196:199], a[180:183], a[144:147], v[196:199]\n"
    "ds_read_b64_tr_b16 a[178:179], v246 offset:51200\n"
    "v_mfma_f32_16x16x32_bf16 v[196:199], a[184:187], a[148:151], v[196:199]\n"
    "ds_read_b64_tr_b16 a[180:181], v246 offset:57344\n"
    "v_mfma_f32_16x16x32_bf16 v[196:199], a[188:191], a[152:155], v[196:199]\n"
    "ds_read_b64_tr_b16 a[182:183], v246 offset:59392\n"
    "v_mfma_f32_16x16x32_bf16 v[188:191], a[188:191], a[120:123], v[188:191]\n"
    "v_exp_f32_e32 v169, v169\n"
    "v_mfma_f32_16x16x32_bf16 v[188:191], a[184:187], a[116:119], v[188:191]\n"
    "v_cvt_pk_bf16_f32 v177, v178, v179\n"
    "v_mfma_f32_16x16x32_bf16 v[180:183], a[184:187], a[84:87], v[180:183]\n"
    "v_exp_f32_e32 v170, v170\n"
    "v_mfma_f32_16x16x32_bf16 v[180:183], a[188:191], a[88:91], v[180:183]\n"
    "v_exp_f32_e32 v171, v171\n"
    "v_mfma_f32_16x16x32_bf16 v[172:175], a[188:191], a[56:59], v[172:175]\n"
    "v_cvt_pk_bf16_f32 v168, v168, v169\n"
    "v_mfma_f32_16x16x32_bf16 v[172:175], a[184:187], a[52:55], v[172:175]\n"
    "v_exp_f32_e32 v160, v160\n"
    "v_mfma_f32_16x16x32_bf16 v[164:167], a[184:187], a[20:23], v[164:167]\n"
    "ds_read_b64_tr_b16 a[184:185], v247 offset:49152\n"
    "v_mfma_f32_16x16x32_bf16 v[164:167], a[188:191], a[24:27], v[164:167]\n"
    "s_waitcnt lgkmcnt(5)\n"
    "ds_read_b64_tr_b16 a[186:187], v247 offset:51200\n"
    "v_mfma_f32_16x16x32_bf16 v[0:3], a[160:163], v[200:203], v[0:3]\n"
    "ds_read_b64_tr_b16 a[188:189], v247 offset:57344\n"
    "v_mfma_f32_16x16x32_bf16 v[0:3], a[164:167], v[204:207], v[0:3]\n"
    "ds_read_b64_tr_b16 a[190:191], v247 offset:59392\n"
    "v_mfma_f32_16x16x32_bf16 a[248:251], a[164:167], v[212:215], a[248:251]\n"
    "v_exp_f32_e32 v161, v161\n"
    "v_mfma_f32_16x16x32_bf16 a[248:251], a[160:163], v[208:211], a[248:251]\n"
    "v_cvt_pk_bf16_f32 v169, v170, v171\n"
    "v_mfma_f32_16x16x32_bf16 v[56:59], a[160:163], v[216:219], v[56:59]\n"
    "v_exp_f32_e32 v162, v162\n"
    "v_mfma_f32_16x16x32_bf16 v[56:59], a[164:167], v[220:223], v[56:59]\n"
    "v_exp_f32_e32 v163, v163\n"
    "v_mfma_f32_16x16x32_bf16 v[60:63], a[164:167], v[228:231], v[60:63]\n"
    "v_cvt_pk_bf16_f32 v160, v160, v161\n"
    "v_mfma_f32_16x16x32_bf16 v[60:63], a[160:163], v[224:227], v[60:63]\n"
    "v_cvt_pk_bf16_f32 v161, v162, v163\n"
    "v_mfma_f32_16x16x32_bf16 v[64:67], a[160:163], v[232:235], v[64:67]\n"
    "ds_read_b64_tr_b16 a[160:161], v244 offset:53248\n"
    "v_mfma_f32_16x16x32_bf16 v[64:67], a[164:167], v[236:239], v[64:67]\n"
    "ds_read_b64_tr_b16 a[162:163], v244 offset:55296\n"
    "v_mfma_f32_16x16x32_bf16 v[76:79], a[168:171], v[232:235], v[76:79]\n"
    "ds_read_b64_tr_b16 a[164:165], v244 offset:61440\n"
    "v_mfma_f32_16x16x32_bf16 v[76:79], a[172:175], v[236:239], v[76:79]\n"
    "ds_read_b64_tr_b16 a[166:167], v244 offset:63488\n"
    "v_mfma_f32_16x16x32_bf16 v[72:75], a[172:175], v[228:231], v[72:75]\n"
    "v_exp_f32_e32 v196, v196\n"
    "v_mfma_f32_16x16x32_bf16 v[72:75], a[168:171], v[224:227], v[72:75]\n"
    "v_exp_f32_e32 v197, v197\n"
    "v_mfma_f32_16x16x32_bf16 v[68:71], a[168:171], v[216:219], v[68:71]\n"
    "v_exp_f32_e32 v198, v198\n"
    "v_mfma_f32_16x16x32_bf16 v[68:71], a[172:175], v[220:223], v[68:71]\n"
    "v_exp_f32_e32 v199, v199\n"
    "v_mfma_f32_16x16x32_bf16 a[252:255], a[172:175], v[212:215], a[252:255]\n"
    "v_cvt_pk_bf16_f32 v194, v196, v197\n"
    "v_mfma_f32_16x16x32_bf16 a[252:255], a[168:171], v[208:211], a[252:255]\n"
    "v_exp_f32_e32 v188, v188\n"
    "v_mfma_f32_16x16x32_bf16 v[4:7], a[168:171], v[200:203], v[4:7]\n"
    "ds_read_b64_tr_b16 a[168:169], v245 offset:53248\n"
    "v_mfma_f32_16x16x32_bf16 v[4:7], a[172:175], v[204:207], v[4:7]\n"
    "s_waitcnt lgkmcnt(5)\n"
    "ds_read_b64_tr_b16 a[170:171], v245 offset:55296\n"
    "v_mfma_f32_16x16x32_bf16 v[8:11], a[176:179], v[200:203], v[8:11]\n"
    "ds_read_b64_tr_b16 a[172:173], v245 offset:61440\n"
    "v_mfma_f32_16x16x32_bf16 v[8:11], a[180:183], v[204:207], v[8:11]\n"
    "ds_read_b64_tr_b16 a[174:175], v245 offset:63488\n"
    "v_mfma_f32_16x16x32_bf16 v[32:35], a[180:183], v[212:215], v[32:35]\n"
    "v_exp_f32_e32 v189, v189\n"
    "v_mfma_f32_16x16x32_bf16 v[32:35], a[176:179], v[208:211], v[32:35]\n"
    "v_cvt_pk_bf16_f32 v195, v198, v199\n"
    "v_mfma_f32_16x16x32_bf16 v[80:83], a[176:179], v[216:219], v[80:83]\n"
    "v_exp_f32_e32 v190, v190\n"
    "v_mfma_f32_16x16x32_bf16 v[80:83], a[180:183], v[220:223], v[80:83]\n"
    "v_exp_f32_e32 v191, v191\n"
    "v_mfma_f32_16x16x32_bf16 v[84:87], a[180:183], v[228:231], v[84:87]\n"
    "v_cvt_pk_bf16_f32 v186, v188, v189\n"
    "v_mfma_f32_16x16x32_bf16 v[84:87], a[176:179], v[224:227], v[84:87]\n"
    "v_exp_f32_e32 v180, v180\n"
    "v_mfma_f32_16x16x32_bf16 v[88:91], a[176:179], v[232:235], v[88:91]\n"
    "ds_read_b64_tr_b16 a[176:177], v246 offset:53248\n"
    "v_mfma_f32_16x16x32_bf16 v[88:91], a[180:183], v[236:239], v[88:91]\n"
    "ds_read_b64_tr_b16 a[178:179], v246 offset:55296\n"
    "v_mfma_f32_16x16x32_bf16 v[100:103], a[184:187], v[232:235], v[100:103]\n"
    "ds_read_b64_tr_b16 a[180:181], v246 offset:61440\n"
    "v_mfma_f32_16x16x32_bf16 v[100:103], a[188:191], v[236:239], v[100:103]\n"
    "ds_read_b64_tr_b16 a[182:183], v246 offset:63488\n"
    "v_mfma_f32_16x16x32_bf16 v[96:99], a[188:191], v[228:231], v[96:99]\n"
    "v_exp_f32_e32 v181, v181\n"
    "v_mfma_f32_16x16x32_bf16 v[96:99], a[184:187], v[224:227], v[96:99]\n"
    "v_cvt_pk_bf16_f32 v187, v190, v191\n"
    "v_mfma_f32_16x16x32_bf16 v[92:95], a[184:187], v[216:219], v[92:95]\n"
    "v_exp_f32_e32 v182, v182\n"
    "v_mfma_f32_16x16x32_bf16 v[92:95], a[188:191], v[220:223], v[92:95]\n"
    "v_exp_f32_e32 v183, v183\n"
    "v_mfma_f32_16x16x32_bf16 v[36:39], a[188:191], v[212:215], v[36:39]\n"
    "v_cvt_pk_bf16_f32 v178, v180, v181\n"
    "v_mfma_f32_16x16x32_bf16 v[36:39], a[184:187], v[208:211], v[36:39]\n"
    "v_exp_f32_e32 v172, v172\n"
    "v_mfma_f32_16x16x32_bf16 v[12:15], a[184:187], v[200:203], v[12:15]\n"
    "s_mov_b32 m0, s49\n"
    "ds_read_b64_tr_b16 a[184:185], v247 offset:53248\n"
    "v_mfma_f32_16x16x32_bf16 v[12:15], a[188:191], v[204:207], v[12:15]\n"
    "s_waitcnt lgkmcnt(5)\n"
    "ds_read_b64_tr_b16 a[186:187], v247 offset:55296\n"
    "v_mfma_f32_16x16x32_bf16 v[16:19], a[160:163], v[200:203], v[16:19]\n"
    "ds_read_b64_tr_b16 a[188:189], v247 offset:61440\n"
    "v_mfma_f32_16x16x32_bf16 v[16:19], a[164:167], v[204:207], v[16:19]\n"
    "ds_read_b64_tr_b16 a[190:191], v247 offset:63488\n"
    "v_mfma_f32_16x16x32_bf16 v[40:43], a[164:167], v[212:215], v[40:43]\n"
    "v_exp_f32_e32 v173, v173\n"
    "v_mfma_f32_16x16x32_bf16 v[40:43], a[160:163], v[208:211], v[40:43]\n"
    "v_cvt_pk_bf16_f32 v179, v182, v183\n"
    "v_mfma_f32_16x16x32_bf16 v[104:107], a[160:163], v[216:219], v[104:107]\n"
    "v_exp_f32_e32 v174, v174\n"
    "v_mfma_f32_16x16x32_bf16 v[104:107], a[164:167], v[220:223], v[104:107]\n"
    "s_waitcnt vmcnt(9)\n"
    "v_mfma_f32_16x16x32_bf16 v[108:111], a[164:167], v[228:231], v[108:111]\n"
    "s_barrier\n"
    "v_mfma_f32_16x16x32_bf16 v[108:111], a[160:163], v[224:227], v[108:111]\n"
    "v_exp_f32_e32 v175, v175\n"
    "v_mfma_f32_16x16x32_bf16 v[112:115], a[160:163], v[232:235], v[112:115]\n"
    "ds_read_b128 a[160:163], v243 offset:20480\n"
    "v_mfma_f32_16x16x32_bf16 v[112:115], a[164:167], v[236:239], v[112:115]\n"
    "ds_read_b128 a[164:167], v240 offset:16384\n"
    "v_mfma_f32_16x16x32_bf16 v[124:127], a[168:171], v[232:235], v[124:127]\n"
    "ds_write_addtid_b32 v192 offset:16384\n"
    "v_cvt_pk_bf16_f32 v170, v172, v173\n"
    "v_mfma_f32_16x16x32_bf16 v[124:127], a[172:175], v[236:239], v[124:127]\n"
    "v_exp_f32_e32 v164, v164\n"
    "v_mfma_f32_16x16x32_bf16 v[120:123], a[172:175], v[228:231], v[120:123]\n"
    "v_exp_f32_e32 v165, v165\n"
    "v_mfma_f32_16x16x32_bf16 v[120:123], a[168:171], v[224:227], v[120:123]\n"
    "ds_write_addtid_b32 v193 offset:16640\n"
    "v_cvt_pk_bf16_f32 v171, v174, v175\n"
    "v_mfma_f32_16x16x32_bf16 v[116:119], a[168:171], v[216:219], v[116:119]\n"
    "v_exp_f32_e32 v166, v166\n"
    "v_mfma_f32_16x16x32_bf16 v[116:119], a[172:175], v[220:223], v[116:119]\n"
    "v_exp_f32_e32 v167, v167\n"
    "v_mfma_f32_16x16x32_bf16 v[44:47], a[172:175], v[212:215], v[44:47]\n"
    "ds_write_addtid_b32 v184 offset:12288\n"
    "v_cvt_pk_bf16_f32 v162, v164, v165\n"
    "v_mfma_f32_16x16x32_bf16 v[44:47], a[168:171], v[208:211], v[44:47]\n"
    "ds_write_addtid_b32 v185 offset:12544\n"
    "v_cvt_pk_bf16_f32 v163, v166, v167\n"
    "v_mfma_f32_16x16x32_bf16 v[20:23], a[168:171], v[200:203], v[20:23]\n"
    "ds_read_b128 a[168:171], v241 offset:16384\n"
    "v_mfma_f32_16x16x32_bf16 v[20:23], a[172:175], v[204:207], v[20:23]\n"
    "s_waitcnt lgkmcnt(7)\n"
    "ds_read_b128 a[172:175], v240 offset:20480\n"
    "v_mfma_f32_16x16x32_bf16 v[24:27], a[176:179], v[200:203], v[24:27]\n"
    "ds_write_addtid_b32 v176 offset:8192\n"
    "v_mfma_f32_16x16x32_bf16 v[24:27], a[180:183], v[204:207], v[24:27]\n"
    "ds_write_addtid_b32 v177 offset:8448\n"
    "v_mfma_f32_16x16x32_bf16 v[48:51], a[180:183], v[212:215], v[48:51]\n"
    "ds_write_addtid_b32 v168 offset:4096\n"
    "v_mfma_f32_16x16x32_bf16 v[48:51], a[176:179], v[208:211], v[48:51]\n"
    "ds_write_addtid_b32 v169 offset:4352\n"
    "v_mfma_f32_16x16x32_bf16 v[128:131], a[176:179], v[216:219], v[128:131]\n"
    "ds_write_addtid_b32 v160 offset:0\n"
    "v_mfma_f32_16x16x32_bf16 v[128:131], a[180:183], v[220:223], v[128:131]\n"
    "ds_write_addtid_b32 v161 offset:256\n"
    "v_mfma_f32_16x16x32_bf16 v[132:135], a[180:183], v[228:231], v[132:135]\n"
    "ds_write_addtid_b32 v194 offset:16896\n"
    "v_mfma_f32_16x16x32_bf16 v[132:135], a[176:179], v[224:227], v[132:135]\n"
    "ds_write_addtid_b32 v195 offset:17152\n"
    "v_mfma_f32_16x16x32_bf16 v[136:139], a[176:179], v[232:235], v[136:139]\n"
    "ds_read_b128 a[176:179], v241 offset:20480\n"
    "v_mfma_f32_16x16x32_bf16 v[136:139], a[180:183], v[236:239], v[136:139]\n"
    "ds_read_b128 a[180:183], v242 offset:16384\n"
    "v_mfma_f32_16x16x32_bf16 v[148:151], a[184:187], v[232:235], v[148:151]\n"
    "ds_write_addtid_b32 v186 offset:12800\n"
    "v_mfma_f32_16x16x32_bf16 v[148:151], a[188:191], v[236:239], v[148:151]\n"
    "ds_write_addtid_b32 v187 offset:13056\n"
    "v_mfma_f32_16x16x32_bf16 v[144:147], a[188:191], v[228:231], v[144:147]\n"
    "ds_write_addtid_b32 v178 offset:8704\n"
    "v_mfma_f32_16x16x32_bf16 v[144:147], a[184:187], v[224:227], v[144:147]\n"
    "ds_write_addtid_b32 v179 offset:8960\n"
    "v_mfma_f32_16x16x32_bf16 v[140:143], a[184:187], v[216:219], v[140:143]\n"
    "s_sub_u32 s44, s44, 1\n"
    "ds_write_addtid_b32 v170 offset:4608\n"
    "v_mfma_f32_16x16x32_bf16 v[140:143], a[188:191], v[220:223], v[140:143]\n"
    "ds_write_addtid_b32 v171 offset:4864\n"
    "v_mfma_f32_16x16x32_bf16 v[52:55], a[188:191], v[212:215], v[52:55]\n"
    "ds_write_addtid_b32 v162 offset:512\n"
    "v_mfma_f32_16x16x32_bf16 v[52:55], a[184:187], v[208:211], v[52:55]\n"
    "ds_write_addtid_b32 v163 offset:768\n"
    "v_mfma_f32_16x16x32_bf16 v[28:31], a[184:187], v[200:203], v[28:31]\n"
    "ds_read_b128 a[184:187], v243 offset:16384\n"
    "v_mfma_f32_16x16x32_bf16 v[28:31], a[188:191], v[204:207], v[28:31]\n"
    "s_waitcnt lgkmcnt(15)\n"
    "ds_read_b128 a[188:191], v242 offset:20480\n"
    "s_cbranch_scc0 .Lat_p\n"
    "s_cmp_lg_u32 s37, 0\n"
    "s_cbranch_scc1 .Lat_tail\n"
    "s_waitcnt vmcnt(0)\n"
    "v_cmp_ne_u32_e32 vcc, s39, v154\n"
    "s_mov_b32 s37, 1\n"
    "s_bitcmp1_b32 %1, 0\n"
    "s_cselect_b32 s44, 5, 3\n"
    "s_nop 1\n"
    "s_cbranch_vccnz .Lat_lpoll\n"
    ".p2align 4\n"
    ".Lat_g:\n"
    "v_mfma_f32_16x16x32_bf16 v[200:203], a[160:163], a[28:31], 0\n"
    "v_mfma_f32_16x16x32_bf16 v[200:203], a[164:167], a[0:3], v[200:203]\n"
    "v_mfma_f32_16x16x32_bf16 v[208:211], a[164:167], a[32:35], 0\n"
    "v_mfma_f32_16x16x32_bf16 v[208:211], a[160:163], a[60:63], v[208:211]\n"
    "v_mfma_f32_16x16x32_bf16 v[216:219], a[160:163], a[92:95], 0\n"
    "v_mfma_f32_16x16x32_bf16 v[216:219], a[164:167], a[64:67], v[216:219]\n"
    "s_waitcnt lgkmcnt(0)\n"
    "v_mfma_f32_16x16x32_bf16 v[224:227], a[164:167], a[96:99], 0\n"
    "s_barrier\n"
    "v_mfma_f32_16x16x32_bf16 v[224:227], a[160:163], a[124:127], v[224:227]\n"
    "s_add_u32 m0, s43, 49152\n"
    "ds_read2st64_b32 v[164:165], v250 offset0:0 offset1:1\n"
    "v_mfma_f32_16x16x32_bf16 v[232:235], a[160:163], a[156:159], 0\n"
    "ds_read_b128 a[160:163], v243 offset:22528\n"
    "v_mfma_f32_16x16x32_bf16 v[232:235], a[164:167], a[128:131], v[232:235]\n"
    "ds_read_b128 a[164:167], v240 offset:18432\n"
    "v_mfma_f32_16x16x32_bf16 v[232:235], a[168:171], a[132:135], v[232:235]\n"
    "buffer_load_dwordx4 v248, s[8:11], s40 offen offset:0 sc1 lds\n"
    "v_mfma_f32_16x16x32_bf16 v[232:235], a[172:175], a[136:139], v[232:235]\n"
    "v_mfma_f32_16x16x32_bf16 v[224:227], a[172:175], a[104:107], v[224:227]\n"
    "v_mfma_f32_16x16x32_bf16 v[224:227], a[168:171], a[100:103], v[224:227]\n"
    "ds_read2st64_b32 v[166:167], v250 offset0:2 offset1:3\n"
    "v_mfma_f32_16x16x32_bf16 v[216:219], a[168:171], a[68:71], v[216:219]\n"
    "buffer_load_dwordx4 v248, s[8:11], s40 offen offset:1024 sc1 lds\n"
    "v_mfma_f32_16x16x32_bf16 v[216:219], a[172:175], a[72:75], v[216:219]\n"
    "v_mfma_f32_16x16x32_bf16 v[208:211], a[172:175], a[40:43], v[208:211]\n"
    "v_mfma_f32_16x16x32_bf16 v[208:211], a[168:171], a[36:39], v[208:211]\n"
    "ds_read2st64_b32 v[172:173], v250 offset0:16 offset1:17\n"
    "v_mfma_f32_16x16x32_bf16 v[200:203], a[168:171], a[4:7], v[200:203]\n"
    "ds_read_b128 a[168:171], v241 offset:18432\n"
    "buffer_load_dwordx4 v248, s[8:11], s40 offen offset:2048 sc1 lds\n"
    "v_mfma_f32_16x16x32_bf16 v[200:203], a[172:175], a[8:11], v[200:203]\n"
    "ds_read_b128 a[172:175], v240 offset:22528\n"
    "v_mfma_f32_16x16x32_bf16 v[200:203], a[176:179], a[12:15], v[200:203]\n"
    "ds_read2st64_b32 v[174:175], v250 offset0:18 offset1:19\n"
    "v_mfma_f32_16x16x32_bf16 v[200:203], a[180:183], a[16:19], v[200:203]\n"
    "v_mfma_f32_16x16x32_bf16 v[208:211], a[180:183], a[48:51], v[208:211]\n"
    "buffer_load_dwordx4 v248, s[8:11], s40 offen offset:3072 sc1 lds\n"
    "v_mfma_f32_16x16x32_bf16 v[208:211], a[176:179], a[44:47], v[208:211]\n"
    "s_add_u32 m0, s43, 53248\n"
    "v_mfma_f32_16x16x32_bf16 v[216:219], a[176:179], a[76:79], v[216:219]\n"
    "s_add_u32 s40, s40, 0x8000\n"
    "ds_read2st64_b32 v[180:181], v250 offset0:32 offset1:33\n"
    "v_mfma_f32_16x16x32_bf16 v[216:219], a[180:183], a[80:83], v[216:219]\n"
    "v_mfma_f32_16x16x32_bf16 v[224:227], a[180:183], a[112:115], v[224:227]\n"
    "buffer_load_dwordx4 v248, s[8:11], s41 offen offset:0 sc1 lds\n"
    "v_mfma_f32_16x16x32_bf16 v[224:227], a[176:179], a[108:111], v[224:227]\n"
    "ds_read2st64_b32 v[182:183], v250 offset0:34 offset1:35\n"
    "v_mfma_f32_16x16x32_bf16 v[232:235], a[176:179], a[140:143], v[232:235]\n"
    "ds_read_b128 a[176:179], v241 offset:22528\n"
    "v_mfma_f32_16x16x32_bf16 v[232:235], a[180:183], a[144:147], v[232:235]\n"
    "ds_read_b128 a[180:183], v242 offset:18432\n"
    "v_mfma_f32_16x16x32_bf16 v[232:235], a[184:187], a[148:151], v[232:235]\n"
    "buffer_load_dwordx4 v248, s[8:11], s41 offen offset:1024 sc1 lds\n"
    "v_mfma_f32_16x16x32_bf16 v[232:235], a[188:191], a[152:155], v[232:235]\n"
    "v_mfma_f32_16x16x32_bf16 v[224:227], a[188:191], a[120:123], v[224:227]\n"
    "ds_read2st64_b32 v[188:189], v250 offset0:48 offset1:49\n"
    "v_mfma_f32_16x16x32_bf16 v[224:227], a[184:187], a[116:119], v[224:227]\n"
    "v_mfma_f32_16x16x32_bf16 v[216:219], a[184:187], a[84:87], v[216:219]\n"
    "buffer_load_dwordx4 v248, s[8:11], s41 offen offset:2048 sc1 lds\n"
    "v_mfma_f32_16x16x32_bf16 v[216:219], a[188:191], a[88:91], v[216:219]\n"
    "ds_read2st64_b32 v[190:191], v250 offset0:50 offset1:51\n"
    "v_mfma_f32_16x16x32_bf16 v[208:211], a[188:191], a[56:59], v[208:211]\n"
    "ds_read_b64 v[254:255], v252 offset:0\n"
    "v_mfma_f32_16x16x32_bf16 v[208:211], a[184:187], a[52:55], v[208:211]\n"
    "ds_read2st64_b32 v[196:197], v250 offset0:64 offset1:65\n"
    "v_mfma_f32_16x16x32_bf16 v[200:203], a[184:187], a[20:23], v[200:203]\n"
    "ds_read_b128 a[184:187], v243 offset:18432\n"
    "buffer_load_dwordx4 v248, s[8:11], s41 offen offset:3072 sc1 lds\n"
    "v_mfma_f32_16x16x32_bf16 v[200:203], a[188:191], a[24:27], v[200:203]\n"
    "s_waitcnt lgkmcnt(15)\n"
    "s_add_u32 s41, s41, 0x8000\n"
    "ds_read_b128 a[188:191], v242 offset:22528\n"
    "v_mfma_f32_16x16x32_bf16 v[204:207], a[160:163], a[28:31], 0\n"
    "s_waitcnt lgkmcnt(3)\n"
    "v_mad_u32_u16 v253, v254, s42, v251 op_sel:[0,0,0,0]\n"
    "v_mfma_f32_16x16x32_bf16 v[204:207], a[164:167], a[0:3], v[204:207]\n"
    "buffer_load_dwordx4 a[200:203], v253, s[20:23], 0 offen offset:0 sc1\n"
    "v_mfma_f32_16x16x32_bf16 v[212:215], a[164:167], a[32:35], 0\n"
    "v_exp_f32_e32 v232, v232\n"
    "v_mfma_f32_16x16x32_bf16 v[212:215], a[160:163], a[60:63], v[212:215]\n"
    "ds_read2st64_b32 v[198:199], v250 offset0:66 offset1:67\n"
    "v_mfma_f32_16x16x32_bf16 v[220:223], a[160:163], a[92:95], 0\n"
    "v_mad_u32_u16 v253, v254, s42, v251 op_sel:[1,0,0,0]\n"
    "v_mfma_f32_16x16x32_bf16 v[220:223], a[164:167], a[64:67], v[220:223]\n"
    "buffer_load_dwordx4 a[204:207], v253, s[20:23], 0 offen offset:0 sc1\n"
    "v_mfma_f32_16x16x32_bf16 v[228:231], a[164:167], a[96:99], 0\n"
    "v_exp_f32_e32 v233, v233\n"
    "v_mfma_f32_16x16x32_bf16 v[228:231], a[160:163], a[124:127], v[228:231]\n"
    "v_exp_f32_e32 v234, v234\n"
    "v_mfma_f32_16x16x32_bf16 v[236:239], a[160:163], a[156:159], 0\n"
    "ds_read_b64_tr_b16 a[160:161], v244 offset:0\n"
    "v_mad_u32_u16 v253, v255, s42, v251 op_sel:[0,0,0,0]\n"
    "v_mfma_f32_16x16x32_bf16 v[236:239], a[164:167], a[128:131], v[236:239]\n"
    "ds_read_b64_tr_b16 a[162:163], v244 offset:2048\n"
    "buffer_load_dwordx4 a[208:211], v253, s[20:23], 0 offen offset:0 sc1\n"
    "v_mfma_f32_16x16x32_bf16 v[236:239], a[168:171], a[132:135], v[236:239]\n"
    "ds_read_b64_tr_b16 a[164:165], v244 offset:8192\n"
    "v_mfma_f32_16x16x32_bf16 v[236:239], a[172:175], a[136:139], v[236:239]\n"
    "ds_read_b64_tr_b16 a[166:167], v244 offset:10240\n"
    "v_mfma_f32_16x16x32_bf16 v[228:231], a[172:175], a[104:107], v[228:231]\n"
    "v_mad_u32_u16 v253, v255, s42, v251 op_sel:[1,0,0,0]\n"
    "v_mfma_f32_16x16x32_bf16 v[228:231], a[168:171], a[100:103], v[228:231]\n"
    "buffer_load_dwordx4 a[212:215], v253, s[20:23], 0 offen offset:0 sc1\n"
    "ds_read_b64 v[254:255], v252 offset:8\n"
    "v_mfma_f32_16x16x32_bf16 v[220:223], a[168:171], a[68:71], v[220:223]\n"
    "v_exp_f32_e32 v235, v235\n"
    "v_mfma_f32_16x16x32_bf16 v[220:223], a[172:175], a[72:75], v[220:223]\n"
    "v_cvt_pk_bf16_f32 v232, v232, v233\n"
    "v_mfma_f32_16x16x32_bf16 v[212:215], a[172:175], a[40:43], v[212:215]\n"
    "v_exp_f32_e32 v224, v224\n"
    "v_mfma_f32_16x16x32_bf16 v[212:215], a[168:171], a[36:39], v[212:215]\n"
    "v_exp_f32_e32 v225, v225\n"
    "v_mfma_f32_16x16x32_bf16 v[204:207], a[168:171], a[4:7], v[204:207]\n"
    "ds_read_b64_tr_b16 a[168:169], v245 offset:0\n"
    "v_cvt_pk_bf16_f32 v233, v234, v235\n"
    "v_mfma_f32_16x16x32_bf16 v[204:207], a[172:175], a[8:11], v[204:207]\n"
    "ds_read_b64_tr_b16 a[170:171], v245 offset:2048\n"
    "v_mfma_f32_16x16x32_bf16 v[204:207], a[176:179], a[12:15], v[204:207]\n"
    "s_waitcnt lgkmcnt(2)\n"
    "ds_read_b64_tr_b16 a[172:173], v245 offset:8192\n"
    "v_mad_u32_u16 v253, v254, s42, v251 op_sel:[0,0,0,0]\n"
    "v_mfma_f32_16x16x32_bf16 v[204:207], a[180:183], a[16:19], v[204:207]\n"
    "ds_read_b64_tr_b16 a[174:175], v245 offset:10240\n"
    "buffer_load_dwordx4 a[216:219], v253, s[20:23], 0 offen offset:0 sc1\n"
    "v_mfma_f32_16x16x32_bf16 v[212:215], a[180:183], a[48:51], v[212:215]\n"
    "v_exp_f32_e32 v226, v226\n"
    "v_mfma_f32_16x16x32_bf16 v[212:215], a[176:179], a[44:47], v[212:215]\n"
    "v_exp_f32_e32 v227, v227\n"
    "v_mfma_f32_16x16x32_bf16 v[220:223], a[176:179], a[76:79], v[220:223]\n"
    "v_mad_u32_u16 v253, v254, s42, v251 op_sel:[1,0,0,0]\n"
    "v_cvt_pk_bf16_f32 v224, v224, v225\n"
    "v_mfma_f32_16x16x32_bf16 v[220:223], a[180:183], a[80:83], v[220:223]\n"
    "buffer_load_dwordx4 a[220:223], v253, s[20:23], 0 offen offset:0 sc1\n"
    "v_mfma_f32_16x16x32_bf16 v[228:231], a[180:183], a[112:115], v[228:231]\n"
    "v_exp_f32_e32 v216, v216\n"
    "v_mfma_f32_16x16x32_bf16 v[228:231], a[176:179], a[108:111], v[228:231]\n"
    "v_exp_f32_e32 v217, v217\n"
    "v_mfma_f32_16x16x32_bf16 v[236:239], a[176:179], a[140:143], v[236:239]\n"
    "ds_read_b64_tr_b16 a[176:177], v246 offset:0\n"
    "v_cvt_pk_bf16_f32 v225, v226, v227\n"
    "v_mfma_f32_16x16x32_bf16 v[236:239], a[180:183], a[144:147], v[236:239]\n"
    "ds_read_b64_tr_b16 a[178:179], v246 offset:2048\n"
    "v_mfma_f32_16x16x32_bf16 v[236:239], a[184:187], a[148:151], v[236:239]\n"
    "ds_read_b64_tr_b16 a[180:181], v246 offset:8192\n"
    "v_mfma_f32_16x16x32_bf16 v[236:239], a[188:191], a[152:155], v[236:239]\n"
    "ds_read_b64_tr_b16 a[182:183], v246 offset:10240\n"
    "v_mfma_f32_16x16x32_bf16 v[228:231], a[188:191], a[120:123], v[228:231]\n"
    "ds_read_b64 v[254:255], v252 offset:32\n"
    "v_mfma_f32_16x16x32_bf16 v[228:231], a[184:187], a[116:119], v[228:231]\n"
    "v_exp_f32_e32 v218, v218\n"
    "v_mfma_f32_16x16x32_bf16 v[220:223], a[184:187], a[84:87], v[220:223]\n"
    "v_exp_f32_e32 v219, v219\n"
    "v_mfma_f32_16x16x32_bf16 v[220:223], a[188:191], a[88:91], v[220:223]\n"
    "v_cvt_pk_bf16_f32 v216, v216, v217\n"
    "v_mfma_f32_16x16x32_bf16 v[212:215], a[188:191], a[56:59], v[212:215]\n"
    "s_waitcnt lgkmcnt(0)\n"
    "v_mad_u32_u16 v253, v254, s42, v251 op_sel:[0,0,0,0]\n"
    "v_mfma_f32_16x16x32_bf16 v[212:215], a[184:187], a[52:55], v[212:215]\n"
    "buffer_load_dwordx4 a[224:227], v253, s[20:23], 0 offen offset:0 sc1\n"
    "v_mfma_f32_16x16x32_bf16 v[204:207], a[184:187], a[20:23], v[204:207]\n"
    "ds_read_b64_tr_b16 a[184:185], v247 offset:0\n"
    "v_mfma_f32_16x16x32_bf16 v[204:207], a[188:191], a[24:27], v[204:207]\n"
    "ds_read_b64_tr_b16 a[186:187], v247 offset:2048\n"
    "v_mfma_f32_16x16x32_bf16 v[0:3], a[160:163], v[160:163], v[0:3]\n"
    "ds_read_b64_tr_b16 a[188:189], v247 offset:8192\n"
    "v_mad_u32_u16 v253, v254, s42, v251 op_sel:[1,0,0,0]\n"
    "v_mfma_f32_16x16x32_bf16 v[0:3], a[164:167], v[164:167], v[0:3]\n"
    "ds_read_b64_tr_b16 a[190:191], v247 offset:10240\n"
    "buffer_load_dwordx4 a[228:231], v253, s[20:23], 0 offen offset:0 sc1\n"
    "v_mfma_f32_16x16x32_bf16 a[248:251], a[164:167], v[172:175], a[248:251]\n"
    "v_exp_f32_e32 v208, v208\n"
    "v_mfma_f32_16x16x32_bf16 a[248:251], a[160:163], v[168:171], a[248:251]\n"
    "v_exp_f32_e32 v209, v209\n"
    "v_mfma_f32_16x16x32_bf16 v[56:59], a[160:163], v[176:179], v[56:59]\n"
    "v_mad_u32_u16 v253, v255, s42, v251 op_sel:[0,0,0,0]\n"
    "v_cvt_pk_bf16_f32 v217, v218, v219\n"
    "v_mfma_f32_16x16x32_bf16 v[56:59], a[164:167], v[180:183], v[56:59]\n"
    "buffer_load_dwordx4 a[232:235], v253, s[20:23], 0 offen offset:0 sc1\n"
    "v_mfma_f32_16x16x32_bf16 v[60:63], a[164:167], v[188:191], v[60:63]\n"
    "v_exp_f32_e32 v210, v210\n"
    "v_mfma_f32_16x16x32_bf16 v[60:63], a[160:163], v[184:187], v[60:63]\n"
    "v_exp_f32_e32 v211, v211\n"
    "v_mfma_f32_16x16x32_bf16 v[64:67], a[160:163], v[192:195], v[64:67]\n"
    "ds_read_b64_tr_b16 a[160:161], v244 offset:4096\n"
    "v_mad_u32_u16 v253, v255, s42, v251 op_sel:[1,0,0,0]\n"
    "v_mfma_f32_16x16x32_bf16 v[64:67], a[164:167], v[196:199], v[64:67]\n"
    "ds_read_b64_tr_b16 a[162:163], v244 offset:6144\n"
    "buffer_load_dwordx4 a[236:239], v253, s[20:23], 0 offen offset:0 sc1\n"
    "v_mfma_f32_16x16x32_bf16 v[76:79], a[168:171], v[192:195], v[76:79]\n"
    "ds_read_b64_tr_b16 a[164:165], v244 offset:12288\n"
    "v_cvt_pk_bf16_f32 v208, v208, v209\n"
    "v_mfma_f32_16x16x32_bf16 v[76:79], a[172:175], v[196:199], v[76:79]\n"
    "ds_read_b64_tr_b16 a[166:167], v244 offset:14336\n"
    "v_mfma_f32_16x16x32_bf16 v[72:75], a[172:175], v[188:191], v[72:75]\n"
    "ds_read_b64 v[254:255], v252 offset:40\n"
    "v_mfma_f32_16x16x32_bf16 v[72:75], a[168:171], v[184:187], v[72:75]\n"
    "v_exp_f32_e32 v200, v200\n"
    "v_mfma_f32_16x16x32_bf16 v[68:71], a[168:171], v[176:179], v[68:71]\n"
    "v_exp_f32_e32 v201, v201\n"
    "v_mfma_f32_16x16x32_bf16 v[68:71], a[172:175], v[180:183], v[68:71]\n"
    "v_cvt_pk_bf16_f32 v209, v210, v211\n"
    "v_mfma_f32_16x16x32_bf16 a[252:255], a[172:175], v[172:175], a[252:255]\n"
    "v_exp_f32_e32 v202, v202\n"
    "v_mfma_f32_16x16x32_bf16 a[252:255], a[168:171], v[168:171], a[252:255]\n"
    "v_exp_f32_e32 v203, v203\n"
    "v_mfma_f32_16x16x32_bf16 v[4:7], a[168:171], v[160:163], v[4:7]\n"
    "s_waitcnt lgkmcnt(0)\n"
    "ds_read_b64_tr_b16 a[168:169], v245 offset:4096\n"
    "v_mad_u32_u16 v253, v254, s42, v251 op_sel:[0,0,0,0]\n"
    "v_mfma_f32_16x16x32_bf16 v[4:7], a[172:175], v[164:167], v[4:7]\n"
    "ds_read_b64_tr_b16 a[170:171], v245 offset:6144\n"
    "v_cvt_pk_bf16_f32 v200, v200, v201\n"
    "v_mfma_f32_16x16x32_bf16 v[8:11], a[176:179], v[160:163], v[8:11]\n"
    "ds_read_b64_tr_b16 a[172:173], v245 offset:12288\n"
    "buffer_load_dwordx4 a[240:243], v253, s[20:23], 0 offen offset:0 sc1\n"
    "v_mfma_f32_16x16x32_bf16 v[8:11], a[180:183], v[164:167], v[8:11]\n"
    "ds_read_b64_tr_b16 a[174:175], v245 offset:14336\n"
    "v_cvt_pk_bf16_f32 v201, v202, v203\n"
    "v_mfma_f32_16x16x32_bf16 v[32:35], a[180:183], v[172:175], v[32:35]\n"
    "v_exp_f32_e32 v236, v236\n"
    "v_mfma_f32_16x16x32_bf16 v[32:35], a[176:179], v[168:171], v[32:35]\n"
    "v_mad_u32_u16 v253, v254, s42, v251 op_sel:[1,0,0,0]\n"
    "v_mfma_f32_16x16x32_bf16 v[80:83], a[176:179], v[176:179], v[80:83]\n"
    "buffer_load_dwordx4 a[244:247], v253, s[20:23], 0 offen offset:0 sc1\n"
    "v_mfma_f32_16x16x32_bf16 v[80:83], a[180:183], v[180:183], v[80:83]\n"
    "v_exp_f32_e32 v237, v237\n"
    "v_mfma_f32_16x16x32_bf16 v[84:87], a[180:183], v[188:191], v[84:87]\n"
    "v_exp_f32_e32 v238, v238\n"
    "v_mfma_f32_16x16x32_bf16 v[84:87], a[176:179], v[184:187], v[84:87]\n"
    "v_exp_f32_e32 v239, v239\n"
    "v_mfma_f32_16x16x32_bf16 v[88:91], a[176:179], v[192:195], v[88:91]\n"
    "ds_read_b64_tr_b16 a[176:177], v246 offset:4096\n"
    "v_cvt_pk_bf16_f32 v234, v236, v237\n"
    "v_mfma_f32_16x16x32_bf16 v[88:91], a[180:183], v[196:199], v[88:91]\n"
    "ds_read_b64_tr_b16 a[178:179], v246 offset:6144\n"
    "v_exp_f32_e32 v228, v228\n"
    "v_mfma_f32_16x16x32_bf16 v[100:103], a[184:187], v[192:195], v[100:103]\n"
    "ds_read_b64_tr_b16 a[180:181], v246 offset:12288\n"
    "v_exp_f32_e32 v229, v229\n"
    "v_mfma_f32_16x16x32_bf16 v[100:103], a[188:191], v[196:199], v[100:103]\n"
    "ds_read_b64_tr_b16 a[182:183], v246 offset:14336\n"
    "v_cvt_pk_bf16_f32 v235, v238, v239\n"
    "v_mfma_f32_16x16x32_bf16 v[96:99], a[188:191], v[188:191], v[96:99]\n"
    "v_exp_f32_e32 v230, v230\n"
    "v_mfma_f32_16x16x32_bf16 v[96:99], a[184:187], v[184:187], v[96:99]\n"
    "v_exp_f32_e32 v231, v231\n"
    "v_cvt_pk_bf16_f32 v226, v228, v229\n"
    "v_mfma_f32_16x16x32_bf16 v[92:95], a[184:187], v[176:179], v[92:95]\n"
    "v_exp_f32_e32 v220, v220\n"
    "v_mfma_f32_16x16x32_bf16 v[92:95], a[188:191], v[180:183], v[92:95]\n"
    "v_exp_f32_e32 v221, v221\n"
    "v_mfma_f32_16x16x32_bf16 v[36:39], a[188:191], v[172:175], v[36:39]\n"
    "v_cvt_pk_bf16_f32 v227, v230, v231\n"
    "v_mfma_f32_16x16x32_bf16 v[36:39], a[184:187], v[168:171], v[36:39]\n"
    "v_exp_f32_e32 v222, v222\n"
    "v_mfma_f32_16x16x32_bf16 v[12:15], a[184:187], v[160:163], v[12:15]\n"
    "s_mov_b32 m0, s49\n"
    "ds_read_b64_tr_b16 a[184:185], v247 offset:4096\n"
    "v_mfma_f32_16x16x32_bf16 v[12:15], a[188:191], v[164:167], v[12:15]\n"
    "s_waitcnt lgkmcnt(5)\n"
    "ds_read_b64_tr_b16 a[186:187], v247 offset:6144\n"
    "v_mfma_f32_16x16x32_bf16 v[16:19], a[160:163], v[160:163], v[16:19]\n"
    "ds_read_b64_tr_b16 a[188:189], v247 offset:12288\n"
    "v_mfma_f32_16x16x32_bf16 v[16:19], a[164:167], v[164:167], v[16:19]\n"
    "ds_read_b64_tr_b16 a[190:191], v247 offset:14336\n"
    "v_mfma_f32_16x16x32_bf16 v[40:43], a[164:167], v[172:175], v[40:43]\n"
    "v_exp_f32_e32 v223, v223\n"
    "v_mfma_f32_16x16x32_bf16 v[40:43], a[160:163], v[168:171], v[40:43]\n"
    "v_cvt_pk_bf16_f32 v218, v220, v221\n"
    "v_mfma_f32_16x16x32_bf16 v[104:107], a[160:163], v[176:179], v[104:107]\n"
    "v_exp_f32_e32 v212, v212\n"
    "v_mfma_f32_16x16x32_bf16 v[104:107], a[164:167], v[180:183], v[104:107]\n"
    "s_waitcnt vmcnt(20)\n"
    "v_mfma_f32_16x16x32_bf16 v[108:111], a[164:167], v[188:191], v[108:111]\n"
    "s_barrier\n"
    "v_mfma_f32_16x16x32_bf16 v[108:111], a[160:163], v[184:187], v[108:111]\n"
    "v_exp_f32_e32 v213, v213\n"
    "v_mfma_f32_16x16x32_bf16 v[112:115], a[160:163], v[192:195], v[112:115]\n"
    "ds_read_b128 a[160:163], v243 offset:36864\n"
    "v_cvt_pk_bf16_f32 v219, v222, v223\n"
    "v_mfma_f32_16x16x32_bf16 v[112:115], a[164:167], v[196:199], v[112:115]\n"
    "ds_read_b128 a[164:167], v240 offset:32768\n"
    "ds_write_addtid_b32 v232 offset:16384\n"
    "v_mfma_f32_16x16x32_bf16 v[124:127], a[168:171], v[192:195], v[124:127]\n"
    "v_exp_f32_e32 v214, v214\n"
    "v_mfma_f32_16x16x32_bf16 v[124:127], a[172:175], v[196:199], v[124:127]\n"
    "v_exp_f32_e32 v215, v215\n"
    "v_mfma_f32_16x16x32_bf16 v[120:123], a[172:175], v[188:191], v[120:123]\n"
    "ds_write_addtid_b32 v233 offset:16640\n"
    "v_cvt_pk_bf16_f32 v210, v212, v213\n"
    "v_mfma_f32_16x16x32_bf16 v[120:123], a[168:171], v[184:187], v[120:123]\n"
    "v_exp_f32_e32 v204, v204\n"
    "v_mfma_f32_16x16x32_bf16 v[116:119], a[168:171], v[176:179], v[116:119]\n"
    "v_exp_f32_e32 v205, v205\n"
    "v_mfma_f32_16x16x32_bf16 v[116:119], a[172:175], v[180:183], v[116:119]\n"
    "ds_write_addtid_b32 v224 offset:12288\n"
    "v_cvt_pk_bf16_f32 v211, v214, v215\n"
    "v_mfma_f32_16x16x32_bf16 v[44:47], a[172:175], v[172:175], v[44:47]\n"
    "v_exp_f32_e32 v206, v206\n"
    "v_mfma_f32_16x16x32_bf16 v[44:47], a[168:171], v[168:171], v[44:47]\n"
    "v_exp_f32_e32 v207, v207\n"
    "v_mfma_f32_16x16x32_bf16 v[20:23], a[168:171], v[160:163], v[20:23]\n"
    "ds_read_b128 a[168:171], v241 offset:32768\n"
    "v_cvt_pk_bf16_f32 v202, v204, v205\n"
    "v_mfma_f32_16x16x32_bf16 v[20:23], a[172:175], v[164:167], v[20:23]\n"
    "s_waitcnt lgkmcnt(6)\n"
    "ds_read_b128 a[172:175], v240 offset:36864\n"
    "v_cvt_pk_bf16_f32 v203, v206, v207\n"
    "v_mfma_f32_16x16x32_bf16 v[24:27], a[176:179], v[160:163], v[24:27]\n"
    "ds_write_addtid_b32 v225 offset:12544\n"
    "ds_write_addtid_b32 v216 offset:8192\n"
    "v_mfma_f32_16x16x32_bf16 v[24:27], a[180:183], v[164:167], v[24:27]\n"
    "ds_write_addtid_b32 v217 offset:8448\n"
    "v_mfma_f32_16x16x32_bf16 v[48:51], a[180:183], v[172:175], v[48:51]\n"
    "ds_write_addtid_b32 v208 offset:4096\n"
    "v_mfma_f32_16x16x32_bf16 v[48:51], a[176:179], v[168:171], v[48:51]\n"
    "ds_write_addtid_b32 v209 offset:4352\n"
    "v_mfma_f32_16x16x32_bf16 v[128:131], a[176:179], v[176:179], v[128:131]\n"
    "ds_write_addtid_b32 v200 offset:0\n"
    "v_mfma_f32_16x16x32_bf16 v[128:131], a[180:183], v[180:183], v[128:131]\n"
    "ds_write_addtid_b32 v201 offset:256\n"
    "v_mfma_f32_16x16x32_bf16 v[132:135], a[180:183], v[188:191], v[132:135]\n"
    "ds_write_addtid_b32 v234 offset:16896\n"
    "v_mfma_f32_16x16x32_bf16 v[132:135], a[176:179], v[184:187], v[132:135]\n"
    "ds_write_addtid_b32 v235 offset:17152\n"
    "v_mfma_f32_16x16x32_bf16 v[136:139], a[176:179], v[192:195], v[136:139]\n"
    "ds_read_b128 a[176:179], v241 offset:36864\n"
    "v_mfma_f32_16x16x32_bf16 v[136:139], a[180:183], v[196:199], v[136:139]\n"
    "ds_read_b128 a[180:183], v242 offset:32768\n"
    "v_mfma_f32_16x16x32_bf16 v[148:151], a[184:187], v[192:195], v[148:151]\n"
    "ds_write_addtid_b32 v226 offset:12800\n"
    "v_mfma_f32_16x16x32_bf16 v[148:151], a[188:191], v[196:199], v[148:151]\n"
    "ds_write_addtid_b32 v227 offset:13056\n"
    "v_mfma_f32_16x16x32_bf16 v[144:147], a[188:191], v[188:191], v[144:147]\n"
    "ds_write_addtid_b32 v218 offset:8704\n"
    "v_mfma_f32_16x16x32_bf16 v[144:147], a[184:187], v[184:187], v[144:147]\n"
    "ds_write_addtid_b32 v219 offset:8960\n"
    "v_mfma_f32_16x16x32_bf16 v[140:143], a[184:187], v[176:179], v[140:143]\n"
    "ds_write_addtid_b32 v210 offset:4608\n"
    "v_mfma_f32_16x16x32_bf16 v[140:143], a[188:191], v[180:183], v[140:143]\n"
    "ds_write_addtid_b32 v211 offset:4864\n"
    "v_mfma_f32_16x16x32_bf16 v[52:55], a[188:191], v[172:175], v[52:55]\n"
    "ds_write_addtid_b32 v202 offset:512\n"
    "v_mfma_f32_16x16x32_bf16 v[52:55], a[184:187], v[168:171], v[52:55]\n"
    "ds_write_addtid_b32 v203 offset:768\n"
    "v_mfma_f32_16x16x32_bf16 v[28:31], a[184:187], v[160:163], v[28:31]\n"
    "ds_read_b128 a[184:187], v243 offset:32768\n"
    "v_mfma_f32_16x16x32_bf16 v[28:31], a[188:191], v[164:167], v[28:31]\n"
    "s_waitcnt lgkmcnt(15)\n"
    "ds_read_b128 a[188:191], v242 offset:36864\n"
    "v_mfma_f32_16x16x32_bf16 v[160:163], a[160:163], a[28:31], 0\n"
    "v_mfma_f32_16x16x32_bf16 v[160:163], a[164:167], a[0:3], v[160:163]\n"
    "v_mfma_f32_16x16x32_bf16 v[168:171], a[164:167], a[32:35], 0\n"
    "v_mfma_f32_16x16x32_bf16 v[168:171], a[160:163], a[60:63], v[168:171]\n"
    "v_mfma_f32_16x16x32_bf16 v[176:179], a[160:163], a[92:95], 0\n"
    "v_mfma_f32_16x16x32_bf16 v[176:179], a[164:167], a[64:67], v[176:179]\n"
    "s_waitcnt lgkmcnt(0)\n"
    "v_mfma_f32_16x16x32_bf16 v[184:187], a[164:167], a[96:99], 0\n"
    "s_barrier\n"
    "v_mfma_f32_16x16x32_bf16 v[184:187], a[160:163], a[124:127], v[184:187]\n"
    "s_add_u32 m0, s43, 0\n"
    "v_mfma_f32_16x16x32_bf16 v[192:195], a[160:163], a[156:159], 0\n"
    "ds_read_b128 a[160:163], v243 offset:38912\n"
    "v_mfma_f32_16x16x32_bf16 v[192:195], a[164:167], a[128:131], v[192:195]\n"
    "ds_read_b128 a[164:167], v240 offset:34816\n"
    "v_mfma_f32_16x16x32_bf16 v[192:195], a[168:171], a[132:135], v[192:195]\n"
    "buffer_load_dwordx4 v248, s[8:11], s40 offen offset:0 sc1 lds\n"
    "v_mfma_f32_16x16x32_bf16 v[192:195], a[172:175], a[136:139], v[192:195]\n"
    "ds_read2st64_b32 v[204:205], v250 offset0:0 offset1:1\n"
    "v_mfma_f32_16x16x32_bf16 v[184:187], a[172:175], a[104:107], v[184:187]\n"
    "s_waitcnt vmcnt(11)\n"
    "v_smfmac_f32_16x16x64_bf16 v[156:159], a[192:195], a[200:207], v152\n"
    "v_mfma_f32_16x16x32_bf16 v[184:187], a[168:171], a[100:103], v[184:187]\n"
    "ds_read2st64_b32 v[206:207], v250 offset0:2 offset1:3\n"
    "v_mfma_f32_16x16x32_bf16 v[176:179], a[168:171], a[68:71], v[176:179]\n"
    "buffer_load_dwordx4 v248, s[8:11], s40 offen offset:1024 sc1 lds\n"
    "v_mfma_f32_16x16x32_bf16 v[176:179], a[172:175], a[72:75], v[176:179]\n"
    "v_mfma_f32_16x16x32_bf16 v[168:171], a[172:175], a[40:43], v[168:171]\n"
    "s_waitcnt vmcnt(10)\n"
    "v_smfmac_f32_16x16x64_bf16 v[156:159], a[192:195], a[208:215], v152\n"
    "ds_read2st64_b32 v[212:213], v250 offset0:16 offset1:17\n"
    "v_mfma_f32_16x16x32_bf16 v[168:171], a[168:171], a[36:39], v[168:171]\n"
    "v_mfma_f32_16x16x32_bf16 v[160:163], a[168:171], a[4:7], v[160:163]\n"
    "ds_read_b128 a[168:171], v241 offset:34816\n"
    "buffer_load_dwordx4 v248, s[8:11], s40 offen offset:2048 sc1 lds\n"
    "v_mfma_f32_16x16x32_bf16 v[160:163], a[172:175], a[8:11], v[160:163]\n"
    "ds_read_b128 a[172:175], v240 offset:38912\n"
    "v_mfma_f32_16x16x32_bf16 v[160:163], a[176:179], a[12:15], v[160:163]\n"
    "s_waitcnt vmcnt(9)\n"
    "ds_read2st64_b32 v[214:215], v250 offset0:18 offset1:19\n"
    "v_smfmac_f32_16x16x64_bf16 v[156:159], a[192:195], a[216:223], v152\n"
    "v_mfma_f32_16x16x32_bf16 v[160:163], a[180:183], a[16:19], v[160:163]\n"
    "v_mfma_f32_16x16x32_bf16 v[168:171], a[180:183], a[48:51], v[168:171]\n"
    "buffer_load_dwordx4 v248, s[8:11], s40 offen offset:3072 sc1 lds\n"
    "v_mfma_f32_16x16x32_bf16 v[168:171], a[176:179], a[44:47], v[168:171]\n"
    "s_add_u32 m0, s43, 4096\n"
    "v_mfma_f32_16x16x32_bf16 v[176:179], a[176:179], a[76:79], v[176:179]\n"
    "s_add_u32 s40, s40, 0x8000\n"
    "ds_read2st64_b32 v[220:221], v250 offset0:32 offset1:33\n"
    "v_mfma_f32_16x16x32_bf16 v[176:179], a[180:183], a[80:83], v[176:179]\n"
    "v_mfma_f32_16x16x32_bf16 v[184:187], a[180:183], a[112:115], v[184:187]\n"
    "v_mfma_f32_16x16x32_bf16 v[184:187], a[176:179], a[108:111], v[184:187]\n"
    "buffer_load_dwordx4 v248, s[8:11], s41 offen offset:0 sc1 lds\n"
    "v_mfma_f32_16x16x32_bf16 v[192:195], a[176:179], a[140:143], v[192:195]\n"
    "ds_read_b128 a[176:179], v241 offset:38912\n"
    "v_mfma_f32_16x16x32_bf16 v[192:195], a[180:183], a[144:147], v[192:195]\n"
    "ds_read_b128 a[180:183], v242 offset:34816\n"
    "v_mfma_f32_16x16x32_bf16 v[192:195], a[184:187], a[148:151], v[192:195]\n"
    "ds_read2st64_b32 v[222:223], v250 offset0:34 offset1:35\n"
    "v_mfma_f32_16x16x32_bf16 v[192:195], a[188:191], a[152:155], v[192:195]\n"
    "v_mfma_f32_16x16x32_bf16 v[184:187], a[188:191], a[120:123], v[184:187]\n"
    "buffer_load_dwordx4 v248, s[8:11], s41 offen offset:1024 sc1 lds\n"
    "v_mfma_f32_16x16x32_bf16 v[184:187], a[184:187], a[116:119], v[184:187]\n"
    "ds_read2st64_b32 v[228:229], v250 offset0:48 offset1:49\n"
    "v_mfma_f32_16x16x32_bf16 v[176:179], a[184:187], a[84:87], v[176:179]\n"
    "s_waitcnt vmcnt(10)\n"
    "v_exp_f32_e32 v192, v192\n"
    "v_smfmac_f32_16x16x64_bf16 v[156:159], a[192:195], a[224:231], v152\n"
    "v_exp_f32_e32 v193, v193\n"
    "v_mfma_f32_16x16x32_bf16 v[176:179], a[188:191], a[88:91], v[176:179]\n"
    "v_exp_f32_e32 v194, v194\n"
    "v_mfma_f32_16x16x32_bf16 v[168:171], a[188:191], a[56:59], v[168:171]\n"
    "buffer_load_dwordx4 v248, s[8:11], s41 offen offset:2048 sc1 lds\n"
    "v_mfma_f32_16x16x32_bf16 v[168:171], a[184:187], a[52:55], v[168:171]\n"
    "ds_read2st64_b32 v[230:231], v250 offset0:50 offset1:51\n"
    "v_mfma_f32_16x16x32_bf16 v[160:163], a[184:187], a[20:23], v[160:163]\n"
    "s_waitcnt vmcnt(9)\n"
    "ds_read_b128 a[184:187], v243 offset:34816\n"
    "v_smfmac_f32_16x16x64_bf16 v[156:159], a[192:195], a[232:239], v152\n"
    "ds_read2st64_b32 v[236:237], v250 offset0:64 offset1:65\n"
    "v_mfma_f32_16x16x32_bf16 v[160:163], a[188:191], a[24:27], v[160:163]\n"
    "s_waitcnt lgkmcnt(9)\n"
    "ds_read_b128 a[188:191], v242 offset:38912\n"
    "v_mfma_f32_16x16x32_bf16 v[164:167], a[160:163], a[28:31], 0\n"
    "buffer_load_dwordx4 v248, s[8:11], s41 offen offset:3072 sc1 lds\n"
    "v_mfma_f32_16x16x32_bf16 v[164:167], a[164:167], a[0:3], v[164:167]\n"
    "s_add_u32 s41, s41, 0x8000\n"
    "v_exp_f32_e32 v195, v195\n"
    "v_mfma_f32_16x16x32_bf16 v[172:175], a[164:167], a[32:35], 0\n"
    "s_waitcnt vmcnt(8)\n"
    "v_cvt_pk_bf16_f32 v192, v192, v193\n"
    "v_smfmac_f32_16x16x64_bf16 v[156:159], a[192:195], a[240:247], v152\n"
    "v_exp_f32_e32 v184, v184\n"
    "v_mfma_f32_16x16x32_bf16 v[172:175], a[160:163], a[60:63], v[172:175]\n"
    "ds_read2st64_b32 v[238:239], v250 offset0:66 offset1:67\n"
    "v_mfma_f32_16x16x32_bf16 v[180:183], a[160:163], a[92:95], 0\n"
    "v_exp_f32_e32 v185, v185\n"
    "v_mfma_f32_16x16x32_bf16 v[180:183], a[164:167], a[64:67], v[180:183]\n"
    "v_cvt_pk_bf16_f32 v193, v194, v195\n"
    "v_mfma_f32_16x16x32_bf16 v[188:191], a[164:167], a[96:99], 0\n"
    "v_exp_f32_e32 v186, v186\n"
    "v_mfma_f32_16x16x32_bf16 v[188:191], a[160:163], a[124:127], v[188:191]\n"
    "v_exp_f32_e32 v187, v187\n"
    "v_mfma_f32_16x16x32_bf16 v[196:199], a[160:163], a[156:159], 0\n"
    "ds_read_b64_tr_b16 a[160:161], v244 offset:16384\n"
    "v_and_b32_e32 v253, 0xffff0000, v255\n"
    "v_mfma_f32_16x16x32_bf16 v[196:199], a[164:167], a[128:131], v[196:199]\n"
    "ds_read_b64_tr_b16 a[162:163], v244 offset:18432\n"
    "v_mfma_f32_16x16x32_bf16 v[196:199], a[168:171], a[132:135], v[196:199]\n"
    "ds_read_b64_tr_b16 a[164:165], v244 offset:24576\n"
    "v_mfma_f32_16x16x32_bf16 v[196:199], a[172:175], a[136:139], v[196:199]\n"
    "ds_read_b64_tr_b16 a[166:167], v244 offset:26624\n"
    "v_mfma_f32_16x16x32_bf16 v[188:191], a[172:175], a[104:107], v[188:191]\n"
    "v_cvt_pk_bf16_f32 v184, v184, v185\n"
    "v_mfma_f32_16x16x32_bf16 v[188:191], a[168:171], a[100:103], v[188:191]\n"
    "v_exp_f32_e32 v176, v176\n"
    "v_mfma_f32_16x16x32_bf16 v[180:183], a[168:171], a[68:71], v[180:183]\n"
    "v_exp_f32_e32 v177, v177\n"
    "v_mfma_f32_16x16x32_bf16 v[180:183], a[172:175], a[72:75], v[180:183]\n"
    "v_cvt_pk_bf16_f32 v185, v186, v187\n"
    "v_mul_f32_e32 v156, v253, v156\n"
    "v_mfma_f32_16x16x32_bf16 v[172:175], a[172:175], a[40:43], v[172:175]\n"
    "v_exp_f32_e32 v178, v178\n"
    "v_mfma_f32_16x16x32_bf16 v[172:175], a[168:171], a[36:39], v[172:175]\n"
    "v_exp_f32_e32 v179, v179\n"
    "v_mfma_f32_16x16x32_bf16 v[164:167], a[168:171], a[4:7], v[164:167]\n"
    "ds_read_b64_tr_b16 a[168:169], v245 offset:16384\n"
    "v_mfma_f32_16x16x32_bf16 v[164:167], a[172:175], a[8:11], v[164:167]\n"
    "s_waitcnt lgkmcnt(6)\n"
    "ds_read_b64_tr_b16 a[170:171], v245 offset:18432\n"
    "v_mfma_f32_16x16x32_bf16 v[164:167], a[176:179], a[12:15], v[164:167]\n"
    "ds_read_b64_tr_b16 a[172:173], v245 offset:24576\n"
    "v_mfma_f32_16x16x32_bf16 v[164:167], a[180:183], a[16:19], v[164:167]\n"
    "ds_read_b64_tr_b16 a[174:175], v245 offset:26624\n"
    "v_mfma_f32_16x16x32_bf16 v[172:175], a[180:183], a[48:51], v[172:175]\n"
    "v_cvt_pk_bf16_f32 v176, v176, v177\n"
    "v_mfma_f32_16x16x32_bf16 v[172:175], a[176:179], a[44:47], v[172:175]\n"
    "v_exp_f32_e32 v168, v168\n"
    "v_mfma_f32_16x16x32_bf16 v[180:183], a[176:179], a[76:79], v[180:183]\n"
    "v_mul_f32_e32 v157, v253, v157\n"
    "v_mfma_f32_16x16x32_bf16 v[180:183], a[180:183], a[80:83], v[180:183]\n"
    "v_exp_f32_e32 v169, v169\n"
    "v_mfma_f32_16x16x32_bf16 v[188:191], a[180:183], a[112:115], v[188:191]\n"
    "v_cvt_pk_bf16_f32 v177, v178, v179\n"
    "v_mfma_f32_16x16x32_bf16 v[188:191], a[176:179], a[108:111], v[188:191]\n"
    "v_exp_f32_e32 v170, v170\n"
    "v_mfma_f32_16x16x32_bf16 v[196:199], a[176:179], a[140:143], v[196:199]\n"
    "ds_read_b64_tr_b16 a[176:177], v246 offset:16384\n"
    "v_mul_f32_e32 v158, v253, v158\n"
    "v_mfma_f32_16x16x32_bf16 v[196:199], a[180:183], a[144:147], v[196:199]\n"
    "ds_read_b64_tr_b16 a[178:179], v246 offset:18432\n"
    "v_mfma_f32_16x16x32_bf16 v[196:199], a[184:187], a[148:151], v[196:199]\n"
    "ds_read_b64_tr_b16 a[180:181], v246 offset:24576\n"
    "v_mfma_f32_16x16x32_bf16 v[196:199], a[188:191], a[152:155], v[196:199]\n"
    "ds_read_b64_tr_b16 a[182:183], v246 offset:26624\n"
    "v_mfma_f32_16x16x32_bf16 v[188:191], a[188:191], a[120:123], v[188:191]\n"
    "v_exp_f32_e32 v171, v171\n"
    "v_mfma_f32_16x16x32_bf16 v[188:191], a[184:187], a[116:119], v[188:191]\n"
    "v_cvt_pk_bf16_f32 v168, v168, v169\n"
    "v_mul_f32_e32 v159, v253, v159\n"
    "v_mfma_f32_16x16x32_bf16 v[180:183], a[184:187], a[84:87], v[180:183]\n"
    "v_exp_f32_e32 v160, v160\n"
    "v_mfma_f32_16x16x32_bf16 v[180:183], a[188:191], a[88:91], v[180:183]\n"
    "v_exp_f32_e32 v161, v161\n"
    "v_mfma_f32_16x16x32_bf16 v[172:175], a[188:191], a[56:59], v[172:175]\n"
    "v_cvt_pk_bf16_f32 v169, v170, v171\n"
    "v_mfma_f32_16x16x32_bf16 v[172:175], a[184:187], a[52:55], v[172:175]\n"
    "v_exp_f32_e32 v162, v162\n"
    "v_mfma_f32_16x16x32_bf16 v[164:167], a[184:187], a[20:23], v[164:167]\n"
    "ds_read_b64_tr_b16 a[184:185], v247 offset:16384\n"
    "v_mfma_f32_16x16x32_bf16 v[164:167], a[188:191], a[24:27], v[164:167]\n"
    "s_waitcnt lgkmcnt(5)\n"
    "ds_read_b64_tr_b16 a[186:187], v247 offset:18432\n"
    "v_mfma_f32_16x16x32_bf16 v[0:3], a[160:163], v[200:203], v[0:3]\n"
    "ds_read_b64_tr_b16 a[188:189], v247 offset:24576\n"
    "v_mad_u32_u16 v253, v255, s42, v153 op_sel:[0,0,0,0]\n"
    "v_mfma_f32_16x16x32_bf16 v[0:3], a[164:167], v[204:207], v[0:3]\n"
    "ds_read_b64_tr_b16 a[190:191], v247 offset:26624\n"
    "v_mfma_f32_16x16x32_bf16 a[248:251], a[164:167], v[212:215], a[248:251]\n"
    "v_exp_f32_e32 v163, v163\n"
    "v_mfma_f32_16x16x32_bf16 a[248:251], a[160:163], v[208:211], a[248:251]\n"
    "v_cvt_pk_bf16_f32 v160, v160, v161\n"
    "v_mfma_f32_16x16x32_bf16 v[56:59], a[160:163], v[216:219], v[56:59]\n"
    "v_cvt_pk_bf16_f32 v154, v156, v157\n"
    "v_mfma_f32_16x16x32_bf16 v[56:59], a[164:167], v[220:223], v[56:59]\n"
    "v_cvt_pk_bf16_f32 v161, v162, v163\n"
    "v_mfma_f32_16x16x32_bf16 v[60:63], a[164:167], v[228:231], v[60:63]\n"
    "v_exp_f32_e32 v196, v196\n"
    "v_mfma_f32_16x16x32_bf16 v[60:63], a[160:163], v[224:227], v[60:63]\n"
    "v_exp_f32_e32 v197, v197\n"
    "v_mfma_f32_16x16x32_bf16 v[64:67], a[160:163], v[232:235], v[64:67]\n"
    "ds_read_b64_tr_b16 a[160:161], v244 offset:20480\n"
    "v_mfma_f32_16x16x32_bf16 v[64:67], a[164:167], v[236:239], v[64:67]\n"
    "ds_read_b64_tr_b16 a[162:163], v244 offset:22528\n"
    "v_mfma_f32_16x16x32_bf16 v[76:79], a[168:171], v[232:235], v[76:79]\n"
    "ds_read_b64_tr_b16 a[164:165], v244 offset:28672\n"
    "v_mfma_f32_16x16x32_bf16 v[76:79], a[172:175], v[236:239], v[76:79]\n"
    "ds_read_b64_tr_b16 a[166:167], v244 offset:30720\n"
    "v_mfma_f32_16x16x32_bf16 v[72:75], a[172:175], v[228:231], v[72:75]\n"
    "v_exp_f32_e32 v198, v198\n"
    "v_mfma_f32_16x16x32_bf16 v[72:75], a[168:171], v[224:227], v[72:75]\n"
    "v_exp_f32_e32 v199, v199\n"
    "v_mfma_f32_16x16x32_bf16 v[68:71], a[168:171], v[216:219], v[68:71]\n"
    "v_cvt_pk_bf16_f32 v194, v196, v197\n"
    "v_cvt_pk_bf16_f32 v155, v158, v159\n"
    "v_mfma_f32_16x16x32_bf16 v[68:71], a[172:175], v[220:223], v[68:71]\n"
    "v_exp_f32_e32 v188, v188\n"
    "v_mfma_f32_16x16x32_bf16 a[252:255], a[172:175], v[212:215], a[252:255]\n"
    "v_exp_f32_e32 v189, v189\n"
    "v_mfma_f32_16x16x32_bf16 a[252:255], a[168:171], v[208:211], a[252:255]\n"
    "v_cvt_pk_bf16_f32 v195, v198, v199\n"
    "v_mfma_f32_16x16x32_bf16 v[4:7], a[168:171], v[200:203], v[4:7]\n"
    "ds_read_b64_tr_b16 a[168:169], v245 offset:20480\n"
    "v_mfma_f32_16x16x32_bf16 v[4:7], a[172:175], v[204:207], v[4:7]\n"
    "s_waitcnt lgkmcnt(5)\n"
    "ds_read_b64_tr_b16 a[170:171], v245 offset:22528\n"
    "v_mfma_f32_16x16x32_bf16 v[8:11], a[176:179], v[200:203], v[8:11]\n"
    "ds_read_b64_tr_b16 a[172:173], v245 offset:28672\n"
    "v_mov_b32_e32 v156, 0\n"
    "v_mfma_f32_16x16x32_bf16 v[8:11], a[180:183], v[204:207], v[8:11]\n"
    "ds_read_b64_tr_b16 a[174:175], v245 offset:30720\n"
    "v_mfma_f32_16x16x32_bf16 v[32:35], a[180:183], v[212:215], v[32:35]\n"
    "v_exp_f32_e32 v190, v190\n"
    "v_mfma_f32_16x16x32_bf16 v[32:35], a[176:179], v[208:211], v[32:35]\n"
    "v_exp_f32_e32 v191, v191\n"
    "v_mfma_f32_16x16x32_bf16 v[80:83], a[176:179], v[216:219], v[80:83]\n"
    "v_mov_b32_e32 v157, 0\n"
    "v_mfma_f32_16x16x32_bf16 v[80:83], a[180:183], v[220:223], v[80:83]\n"
    "v_cvt_pk_bf16_f32 v186, v188, v189\n"
    "v_mfma_f32_16x16x32_bf16 v[84:87], a[180:183], v[228:231], v[84:87]\n"
    "v_exp_f32_e32 v180, v180\n"
    "v_mfma_f32_16x16x32_bf16 v[84:87], a[176:179], v[224:227], v[84:87]\n"
    "v_exp_f32_e32 v181, v181\n"
    "v_mfma_f32_16x16x32_bf16 v[88:91], a[176:179], v[232:235], v[88:91]\n"
    "ds_read_b64_tr_b16 a[176:177], v246 offset:20480\n"
    "v_mfma_f32_16x16x32_bf16 v[88:91], a[180:183], v[236:239], v[88:91]\n"
    "ds_read_b64_tr_b16 a[178:179], v246 offset:22528\n"
    "v_mfma_f32_16x16x32_bf16 v[100:103], a[184:187], v[232:235], v[100:103]\n"
    "ds_read_b64_tr_b16 a[180:181], v246 offset:28672\n"
    "v_mfma_f32_16x16x32_bf16 v[100:103], a[188:191], v[236:239], v[100:103]\n"
    "ds_read_b64_tr_b16 a[182:183], v246 offset:30720\n"
    "v_mfma_f32_16x16x32_bf16 v[96:99], a[188:191], v[228:231], v[96:99]\n"
    "v_cvt_pk_bf16_f32 v187, v190, v191\n"
    "v_mov_b32_e32 v158, 0\n"
    "v_mfma_f32_16x16x32_bf16 v[96:99], a[184:187], v[224:227], v[96:99]\n"
    "v_exp_f32_e32 v182, v182\n"
    "v_mfma_f32_16x16x32_bf16 v[92:95], a[184:187], v[216:219], v[92:95]\n"
    "v_exp_f32_e32 v183, v183\n"
    "v_mfma_f32_16x16x32_bf16 v[92:95], a[188:191], v[220:223], v[92:95]\n"
    "v_cvt_pk_bf16_f32 v178, v180, v181\n"
    "v_mfma_f32_16x16x32_bf16 v[36:39], a[188:191], v[212:215], v[36:39]\n"
    "v_exp_f32_e32 v172, v172\n"
    "v_mfma_f32_16x16x32_bf16 v[36:39], a[184:187], v[208:211], v[36:39]\n"
    "v_exp_f32_e32 v173, v173\n"
    "v_mfma_f32_16x16x32_bf16 v[12:15], a[184:187], v[200:203], v[12:15]\n"
    "s_mov_b32 m0, s49\n"
    "ds_read_b64_tr_b16 a[184:185], v247 offset:20480\n"
    "v_mfma_f32_16x16x32_bf16 v[12:15], a[188:191], v[204:207], v[12:15]\n"
    "s_waitcnt lgkmcnt(5)\n"
    "ds_read_b64_tr_b16 a[186:187], v247 offset:22528\n"
    "v_mov_b32_e32 v159, 0\n"
    "v_mfma_f32_16x16x32_bf16 v[16:19], a[160:163], v[200:203], v[16:19]\n"
    "ds_read_b64_tr_b16 a[188:189], v247 offset:28672\n"
    "v_mfma_f32_16x16x32_bf16 v[16:19], a[164:167], v[204:207], v[16:19]\n"
    "ds_read_b64_tr_b16 a[190:191], v247 offset:30720\n"
    "v_mfma_f32_16x16x32_bf16 v[40:43], a[164:167], v[212:215], v[40:43]\n"
    "buffer_store_dwordx2 v[154:155], v253, s[24:27], 0 offen offset:0 sc1\n"
    "v_cvt_pk_bf16_f32 v179, v182, v183\n"
    "v_mfma_f32_16x16x32_bf16 v[40:43], a[160:163], v[208:211], v[40:43]\n"
    "v_exp_f32_e32 v174, v174\n"
    "v_mfma_f32_16x16x32_bf16 v[104:107], a[160:163], v[216:219], v[104:107]\n"
    "v_exp_f32_e32 v175, v175\n"
    "v_mfma_f32_16x16x32_bf16 v[104:107], a[164:167], v[220:223], v[104:107]\n"
    "s_waitcnt vmcnt(9)\n"
    "v_mfma_f32_16x16x32_bf16 v[108:111], a[164:167], v[228:231], v[108:111]\n"
    "s_barrier\n"
    "v_mfma_f32_16x16x32_bf16 v[108:111], a[160:163], v[224:227], v[108:111]\n"
    "ds_write_addtid_b32 v192 offset:16384\n"
    "v_cvt_pk_bf16_f32 v170, v172, v173\n"
    "v_mfma_f32_16x16x32_bf16 v[112:115], a[160:163], v[232:235], v[112:115]\n"
    "ds_read_b128 a[160:163], v243 offset:53248\n"
    "v_mfma_f32_16x16x32_bf16 v[112:115], a[164:167], v[236:239], v[112:115]\n"
    "ds_read_b128 a[164:167], v240 offset:49152\n"
    "v_mfma_f32_16x16x32_bf16 v[124:127], a[168:171], v[232:235], v[124:127]\n"
    "v_exp_f32_e32 v164, v164\n"
    "v_mfma_f32_16x16x32_bf16 v[124:127], a[172:175], v[236:239], v[124:127]\n"
    "v_exp_f32_e32 v165, v165\n"
    "v_mfma_f32_16x16x32_bf16 v[120:123], a[172:175], v[228:231], v[120:123]\n"
    "ds_write_addtid_b32 v193 offset:16640\n"
    "v_cvt_pk_bf16_f32 v171, v174, v175\n"
    "v_mfma_f32_16x16x32_bf16 v[120:123], a[168:171], v[224:227], v[120:123]\n"
    "ds_write_addtid_b32 v184 offset:12288\n"
    "v_mfma_f32_16x16x32_bf16 v[116:119], a[168:171], v[216:219], v[116:119]\n"
    "v_exp_f32_e32 v166, v166\n"
    "v_mfma_f32_16x16x32_bf16 v[116:119], a[172:175], v[220:223], v[116:119]\n"
    "v_exp_f32_e32 v167, v167\n"
    "v_mfma_f32_16x16x32_bf16 v[44:47], a[172:175], v[212:215], v[44:47]\n"
    "ds_write_addtid_b32 v185 offset:12544\n"
    "v_cvt_pk_bf16_f32 v162, v164, v165\n"
    "v_mfma_f32_16x16x32_bf16 v[44:47], a[168:171], v[208:211], v[44:47]\n"
    "v_cvt_pk_bf16_f32 v163, v166, v167\n"
    "v_mfma_f32_16x16x32_bf16 v[20:23], a[168:171], v[200:203], v[20:23]\n"
    "ds_read_b128 a[168:171], v241 offset:49152\n"
    "v_mfma_f32_16x16x32_bf16 v[20:23], a[172:175], v[204:207], v[20:23]\n"
    "s_waitcnt lgkmcnt(7)\n"
    "ds_read_b128 a[172:175], v240 offset:53248\n"
    "v_mfma_f32_16x16x32_bf16 v[24:27], a[176:179], v[200:203], v[24:27]\n"
    "ds_write_addtid_b32 v176 offset:8192\n"
    "v_mfma_f32_16x16x32_bf16 v[24:27], a[180:183], v[204:207], v[24:27]\n"
    "ds_write_addtid_b32 v177 offset:8448\n"
    "v_mfma_f32_16x16x32_bf16 v[48:51], a[180:183], v[212:215], v[48:51]\n"
    "ds_write_addtid_b32 v168 offset:4096\n"
    "v_mfma_f32_16x16x32_bf16 v[48:51], a[176:179], v[208:211], v[48:51]\n"
    "ds_write_addtid_b32 v169 offset:4352\n"
    "v_mfma_f32_16x16x32_bf16 v[128:131], a[176:179], v[216:219], v[128:131]\n"
    "ds_write_addtid_b32 v160 offset:0\n"
    "v_mfma_f32_16x16x32_bf16 v[128:131], a[180:183], v[220:223], v[128:131]\n"
    "ds_write_addtid_b32 v161 offset:256\n"
    "v_mfma_f32_16x16x32_bf16 v[132:135], a[180:183], v[228:231], v[132:135]\n"
    "ds_write_addtid_b32 v194 offset:16896\n"
    "v_mfma_f32_16x16x32_bf16 v[132:135], a[176:179], v[224:227], v[132:135]\n"
    "ds_write_addtid_b32 v195 offset:17152\n"
    "v_mfma_f32_16x16x32_bf16 v[136:139], a[176:179], v[232:235], v[136:139]\n"
    "ds_read_b128 a[176:179], v241 offset:53248\n"
    "v_mfma_f32_16x16x32_bf16 v[136:139], a[180:183], v[236:239], v[136:139]\n"
    "ds_read_b128 a[180:183], v242 offset:49152\n"
    "v_mfma_f32_16x16x32_bf16 v[148:151], a[184:187], v[232:235], v[148:151]\n"
    "ds_write_addtid_b32 v186 offset:12800\n"
    "v_mfma_f32_16x16x32_bf16 v[148:151], a[188:191], v[236:239], v[148:151]\n"
    "ds_write_addtid_b32 v187 offset:13056\n"
    "v_mfma_f32_16x16x32_bf16 v[144:147], a[188:191], v[228:231], v[144:147]\n"
    "ds_write_addtid_b32 v178 offset:8704\n"
    "v_mfma_f32_16x16x32_bf16 v[144:147], a[184:187], v[224:227], v[144:147]\n"
    "ds_write_addtid_b32 v179 offset:8960\n"
    "v_mfma_f32_16x16x32_bf16 v[140:143], a[184:187], v[216:219], v[140:143]\n"
    "ds_write_addtid_b32 v170 offset:4608\n"
    "v_mfma_f32_16x16x32_bf16 v[140:143], a[188:191], v[220:223], v[140:143]\n"
    "ds_write_addtid_b32 v171 offset:4864\n"
    "v_mfma_f32_16x16x32_bf16 v[52:55], a[188:191], v[212:215], v[52:55]\n"
    "ds_write_addtid_b32 v162 offset:512\n"
    "v_mfma_f32_16x16x32_bf16 v[52:55], a[184:187], v[208:211], v[52:55]\n"
    "ds_write_addtid_b32 v163 offset:768\n"
    "v_mfma_f32_16x16x32_bf16 v[28:31], a[184:187], v[200:203], v[28:31]\n"
    "ds_read_b128 a[184:187], v243 offset:49152\n"
    "v_mfma_f32_16x16x32_bf16 v[28:31], a[188:191], v[204:207], v[28:31]\n"
    "s_waitcnt lgkmcnt(15)\n"
    "ds_read_b128 a[188:191], v242 offset:53248\n"
    "v_mfma_f32_16x16x32_bf16 v[200:203], a[160:163], a[28:31], 0\n"
    "v_mfma_f32_16x16x32_bf16 v[200:203], a[164:167], a[0:3], v[200:203]\n"
    "v_mfma_f32_16x16x32_bf16 v[208:211], a[164:167], a[32:35], 0\n"
    "v_mfma_f32_16x16x32_bf16 v[208:211], a[160:163], a[60:63], v[208:211]\n"
    "v_mfma_f32_16x16x32_bf16 v[216:219], a[160:163], a[92:95], 0\n"
    "v_mfma_f32_16x16x32_bf16 v[216:219], a[164:167], a[64:67], v[216:219]\n"
    "s_waitcnt lgkmcnt(0)\n"
    "v_mfma_f32_16x16x32_bf16 v[224:227], a[164:167], a[96:99], 0\n"
    "s_barrier\n"
    "v_mfma_f32_16x16x32_bf16 v[224:227], a[160:163], a[124:127], v[224:227]\n"
    "s_add_u32 m0, s43, 16384\n"
    "ds_read2st64_b32 v[164:165], v250 offset0:0 offset1:1\n"
    "v_mfma_f32_16x16x32_bf16 v[232:235], a[160:163], a[156:159], 0\n"
    "ds_read_b128 a[160:163], v243 offset:55296\n"
    "v_mfma_f32_16x16x32_bf16 v[232:235], a[164:167], a[128:131], v[232:235]\n"
    "ds_read_b128 a[164:167], v240 offset:51200\n"
    "v_mfma_f32_16x16x32_bf16 v[232:235], a[168:171], a[132:135], v[232:235]\n"
    "buffer_load_dwordx4 v248, s[8:11], s40 offen offset:0 sc1 lds\n"
    "v_mfma_f32_16x16x32_bf16 v[232:235], a[172:175], a[136:139], v[232:235]\n"
    "v_mfma_f32_16x16x32_bf16 v[224:227], a[172:175], a[104:107], v[224:227]\n"
    "v_mfma_f32_16x16x32_bf16 v[224:227], a[168:171], a[100:103], v[224:227]\n"
    "ds_read2st64_b32 v[166:167], v250 offset0:2 offset1:3\n"
    "v_mfma_f32_16x16x32_bf16 v[216:219], a[168:171], a[68:71], v[216:219]\n"
    "buffer_load_dwordx4 v248, s[8:11], s40 offen offset:1024 sc1 lds\n"
    "v_mfma_f32_16x16x32_bf16 v[216:219], a[172:175], a[72:75], v[216:219]\n"
    "v_mfma_f32_16x16x32_bf16 v[208:211], a[172:175], a[40:43], v[208:211]\n"
    "v_mfma_f32_16x16x32_bf16 v[208:211], a[168:171], a[36:39], v[208:211]\n"
    "ds_read2st64_b32 v[172:173], v250 offset0:16 offset1:17\n"
    "v_mfma_f32_16x16x32_bf16 v[200:203], a[168:171], a[4:7], v[200:203]\n"
    "ds_read_b128 a[168:171], v241 offset:51200\n"
    "buffer_load_dwordx4 v248, s[8:11], s40 offen offset:2048 sc1 lds\n"
    "v_mfma_f32_16x16x32_bf16 v[200:203], a[172:175], a[8:11], v[200:203]\n"
    "ds_read_b128 a[172:175], v240 offset:55296\n"
    "v_mfma_f32_16x16x32_bf16 v[200:203], a[176:179], a[12:15], v[200:203]\n"
    "ds_read2st64_b32 v[174:175], v250 offset0:18 offset1:19\n"
    "v_mfma_f32_16x16x32_bf16 v[200:203], a[180:183], a[16:19], v[200:203]\n"
    "v_mfma_f32_16x16x32_bf16 v[208:211], a[180:183], a[48:51], v[208:211]\n"
    "buffer_load_dwordx4 v248, s[8:11], s40 offen offset:3072 sc1 lds\n"
    "v_mfma_f32_16x16x32_bf16 v[208:211], a[176:179], a[44:47], v[208:211]\n"
    "s_add_u32 m0, s43, 20480\n"
    "v_mfma_f32_16x16x32_bf16 v[216:219], a[176:179], a[76:79], v[216:219]\n"
    "s_add_u32 s40, s40, 0x8000\n"
    "ds_read2st64_b32 v[180:181], v250 offset0:32 offset1:33\n"
    "v_mfma_f32_16x16x32_bf16 v[216:219], a[180:183], a[80:83], v[216:219]\n"
    "v_mfma_f32_16x16x32_bf16 v[224:227], a[180:183], a[112:115], v[224:227]\n"
    "buffer_load_dwordx4 v248, s[8:11], s41 offen offset:0 sc1 lds\n"
    "v_mfma_f32_16x16x32_bf16 v[224:227], a[176:179], a[108:111], v[224:227]\n"
    "ds_read2st64_b32 v[182:183], v250 offset0:34 offset1:35\n"
    "v_mfma_f32_16x16x32_bf16 v[232:235], a[176:179], a[140:143], v[232:235]\n"
    "ds_read_b128 a[176:179], v241 offset:55296\n"
    "v_mfma_f32_16x16x32_bf16 v[232:235], a[180:183], a[144:147], v[232:235]\n"
    "ds_read_b128 a[180:183], v242 offset:51200\n"
    "v_mfma_f32_16x16x32_bf16 v[232:235], a[184:187], a[148:151], v[232:235]\n"
    "buffer_load_dwordx4 v248, s[8:11], s41 offen offset:1024 sc1 lds\n"
    "v_mfma_f32_16x16x32_bf16 v[232:235], a[188:191], a[152:155], v[232:235]\n"
    "v_mfma_f32_16x16x32_bf16 v[224:227], a[188:191], a[120:123], v[224:227]\n"
    "ds_read2st64_b32 v[188:189], v250 offset0:48 offset1:49\n"
    "v_mfma_f32_16x16x32_bf16 v[224:227], a[184:187], a[116:119], v[224:227]\n"
    "v_mfma_f32_16x16x32_bf16 v[216:219], a[184:187], a[84:87], v[216:219]\n"
    "buffer_load_dwordx4 v248, s[8:11], s41 offen offset:2048 sc1 lds\n"
    "v_mfma_f32_16x16x32_bf16 v[216:219], a[188:191], a[88:91], v[216:219]\n"
    "ds_read2st64_b32 v[190:191], v250 offset0:50 offset1:51\n"
    "v_mfma_f32_16x16x32_bf16 v[208:211], a[188:191], a[56:59], v[208:211]\n"
    "ds_read_b64 v[254:255], v252 offset:64\n"
    "v_mfma_f32_16x16x32_bf16 v[208:211], a[184:187], a[52:55], v[208:211]\n"
    "ds_read2st64_b32 v[196:197], v250 offset0:64 offset1:65\n"
    "v_mfma_f32_16x16x32_bf16 v[200:203], a[184:187], a[20:23], v[200:203]\n"
    "ds_read_b128 a[184:187], v243 offset:51200\n"
    "buffer_load_dwordx4 v248, s[8:11], s41 offen offset:3072 sc1 lds\n"
    "v_mfma_f32_16x16x32_bf16 v[200:203], a[188:191], a[24:27], v[200:203]\n"
    "s_waitcnt lgkmcnt(15)\n"
    "s_add_u32 s41, s41, 0x8000\n"
    "ds_read_b128 a[188:191], v242 offset:55296\n"
    "v_mfma_f32_16x16x32_bf16 v[204:207], a[160:163], a[28:31], 0\n"
    "s_waitcnt lgkmcnt(3)\n"
    "v_mad_u32_u16 v253, v254, s42, v251 op_sel:[0,0,0,0]\n"
    "v_mfma_f32_16x16x32_bf16 v[204:207], a[164:167], a[0:3], v[204:207]\n"
    "buffer_load_dwordx4 a[200:203], v253, s[20:23], 0 offen offset:0 sc1\n"
    "v_mfma_f32_16x16x32_bf16 v[212:215], a[164:167], a[32:35], 0\n"
    "v_exp_f32_e32 v232, v232\n"
    "v_mfma_f32_16x16x32_bf16 v[212:215], a[160:163], a[60:63], v[212:215]\n"
    "ds_read2st64_b32 v[198:199], v250 offset0:66 offset1:67\n"
    "v_mfma_f32_16x16x32_bf16 v[220:223], a[160:163], a[92:95], 0\n"
    "v_mad_u32_u16 v253, v254, s42, v251 op_sel:[1,0,0,0]\n"
    "v_mfma_f32_16x16x32_bf16 v[220:223], a[164:167], a[64:67], v[220:223]\n"
    "buffer_load_dwordx4 a[204:207], v253, s[20:23], 0 offen offset:0 sc1\n"
    "v_mfma_f32_16x16x32_bf16 v[228:231], a[164:167], a[96:99], 0\n"
    "v_exp_f32_e32 v233, v233\n"
    "v_mfma_f32_16x16x32_bf16 v[228:231], a[160:163], a[124:127], v[228:231]\n"
    "v_exp_f32_e32 v234, v234\n"
    "v_mfma_f32_16x16x32_bf16 v[236:239], a[160:163], a[156:159], 0\n"
    "ds_read_b64_tr_b16 a[160:161], v244 offset:32768\n"
    "v_mad_u32_u16 v253, v255, s42, v251 op_sel:[0,0,0,0]\n"
    "v_mfma_f32_16x16x32_bf16 v[236:239], a[164:167], a[128:131], v[236:239]\n"
    "ds_read_b64_tr_b16 a[162:163], v244 offset:34816\n"
    "buffer_load_dwordx4 a[208:211], v253, s[20:23], 0 offen offset:0 sc1\n"
    "v_mfma_f32_16x16x32_bf16 v[236:239], a[168:171], a[132:135], v[236:239]\n"
    "ds_read_b64_tr_b16 a[164:165], v244 offset:40960\n"
    "v_mfma_f32_16x16x32_bf16 v[236:239], a[172:175], a[136:139], v[236:239]\n"
    "ds_read_b64_tr_b16 a[166:167], v244 offset:43008\n"
    "v_mfma_f32_16x16x32_bf16 v[228:231], a[172:175], a[104:107], v[228:231]\n"
    "v_mad_u32_u16 v253, v255, s42, v251 op_sel:[1,0,0,0]\n"
    "v_mfma_f32_16x16x32_bf16 v[228:231], a[168:171], a[100:103], v[228:231]\n"
    "buffer_load_dwordx4 a[212:215], v253, s[20:23], 0 offen offset:0 sc1\n"
    "ds_read_b64 v[254:255], v252 offset:72\n"
    "v_mfma_f32_16x16x32_bf16 v[220:223], a[168:171], a[68:71], v[220:223]\n"
    "v_exp_f32_e32 v235, v235\n"
    "v_mfma_f32_16x16x32_bf16 v[220:223], a[172:175], a[72:75], v[220:223]\n"
    "v_cvt_pk_bf16_f32 v232, v232, v233\n"
    "v_mfma_f32_16x16x32_bf16 v[212:215], a[172:175], a[40:43], v[212:215]\n"
    "v_exp_f32_e32 v224, v224\n"
    "v_mfma_f32_16x16x32_bf16 v[212:215], a[168:171], a[36:39], v[212:215]\n"
    "v_exp_f32_e32 v225, v225\n"
    "v_mfma_f32_16x16x32_bf16 v[204:207], a[168:171], a[4:7], v[204:207]\n"
    "ds_read_b64_tr_b16 a[168:169], v245 offset:32768\n"
    "v_cvt_pk_bf16_f32 v233, v234, v235\n"
    "v_mfma_f32_16x16x32_bf16 v[204:207], a[172:175], a[8:11], v[204:207]\n"
    "ds_read_b64_tr_b16 a[170:171], v245 offset:34816\n"
    "v_mfma_f32_16x16x32_bf16 v[204:207], a[176:179], a[12:15], v[204:207]\n"
    "s_waitcnt lgkmcnt(2)\n"
    "ds_read_b64_tr_b16 a[172:173], v245 offset:40960\n"
    "v_mad_u32_u16 v253, v254, s42, v251 op_sel:[0,0,0,0]\n"
    "v_mfma_f32_16x16x32_bf16 v[204:207], a[180:183], a[16:19], v[204:207]\n"
    "ds_read_b64_tr_b16 a[174:175], v245 offset:43008\n"
    "buffer_load_dwordx4 a[216:219], v253, s[20:23], 0 offen offset:0 sc1\n"
    "v_mfma_f32_16x16x32_bf16 v[212:215], a[180:183], a[48:51], v[212:215]\n"
    "v_exp_f32_e32 v226, v226\n"
    "v_mfma_f32_16x16x32_bf16 v[212:215], a[176:179], a[44:47], v[212:215]\n"
    "v_exp_f32_e32 v227, v227\n"
    "v_mfma_f32_16x16x32_bf16 v[220:223], a[176:179], a[76:79], v[220:223]\n"
    "v_mad_u32_u16 v253, v254, s42, v251 op_sel:[1,0,0,0]\n"
    "v_cvt_pk_bf16_f32 v224, v224, v225\n"
    "v_mfma_f32_16x16x32_bf16 v[220:223], a[180:183], a[80:83], v[220:223]\n"
    "buffer_load_dwordx4 a[220:223], v253, s[20:23], 0 offen offset:0 sc1\n"
    "v_mfma_f32_16x16x32_bf16 v[228:231], a[180:183], a[112:115], v[228:231]\n"
    "v_exp_f32_e32 v216, v216\n"
    "v_mfma_f32_16x16x32_bf16 v[228:231], a[176:179], a[108:111], v[228:231]\n"
    "v_exp_f32_e32 v217, v217\n"
    "v_mfma_f32_16x16x32_bf16 v[236:239], a[176:179], a[140:143], v[236:239]\n"
    "ds_read_b64_tr_b16 a[176:177], v246 offset:32768\n"
    "v_cvt_pk_bf16_f32 v225, v226, v227\n"
    "v_mfma_f32_16x16x32_bf16 v[236:239], a[180:183], a[144:147], v[236:239]\n"
    "ds_read_b64_tr_b16 a[178:179], v246 offset:34816\n"
    "v_mfma_f32_16x16x32_bf16 v[236:239], a[184:187], a[148:151], v[236:239]\n"
    "ds_read_b64_tr_b16 a[180:181], v246 offset:40960\n"
    "v_mfma_f32_16x16x32_bf16 v[236:239], a[188:191], a[152:155], v[236:239]\n"
    "ds_read_b64_tr_b16 a[182:183], v246 offset:43008\n"
    "v_mfma_f32_16x16x32_bf16 v[228:231], a[188:191], a[120:123], v[228:231]\n"
    "ds_read_b64 v[254:255], v252 offset:96\n"
    "v_mfma_f32_16x16x32_bf16 v[228:231], a[184:187], a[116:119], v[228:231]\n"
    "v_exp_f32_e32 v218, v218\n"
    "v_mfma_f32_16x16x32_bf16 v[220:223], a[184:187], a[84:87], v[220:223]\n"
    "v_exp_f32_e32 v219, v219\n"
    "v_mfma_f32_16x16x32_bf16 v[220:223], a[188:191], a[88:91], v[220:223]\n"
    "v_cvt_pk_bf16_f32 v216, v216, v217\n"
    "v_mfma_f32_16x16x32_bf16 v[212:215], a[188:191], a[56:59], v[212:215]\n"
    "s_waitcnt lgkmcnt(0)\n"
    "v_mad_u32_u16 v253, v254, s42, v251 op_sel:[0,0,0,0]\n"
    "v_mfma_f32_16x16x32_bf16 v[212:215], a[184:187], a[52:55], v[212:215]\n"
    "buffer_load_dwordx4 a[224:227], v253, s[20:23], 0 offen offset:0 sc1\n"
    "v_mfma_f32_16x16x32_bf16 v[204:207], a[184:187], a[20:23], v[204:207]\n"
    "ds_read_b64_tr_b16 a[184:185], v247 offset:32768\n"
    "v_mfma_f32_16x16x32_bf16 v[204:207], a[188:191], a[24:27], v[204:207]\n"
    "ds_read_b64_tr_b16 a[186:187], v247 offset:34816\n"
    "v_mfma_f32_16x16x32_bf16 v[0:3], a[160:163], v[160:163], v[0:3]\n"
    "ds_read_b64_tr_b16 a[188:189], v247 offset:40960\n"
    "v_mad_u32_u16 v253, v254, s42, v251 op_sel:[1,0,0,0]\n"
    "v_mfma_f32_16x16x32_bf16 v[0:3], a[164:167], v[164:167], v[0:3]\n"
    "ds_read_b64_tr_b16 a[190:191], v247 offset:43008\n"
    "buffer_load_dwordx4 a[228:231], v253, s[20:23], 0 offen offset:0 sc1\n"
    "v_mfma_f32_16x16x32_bf16 a[248:251], a[164:167], v[172:175], a[248:251]\n"
    "v_exp_f32_e32 v208, v208\n"
    "v_mfma_f32_16x16x32_bf16 a[248:251], a[160:163], v[168:171], a[248:251]\n"
    "v_exp_f32_e32 v209, v209\n"
    "v_mfma_f32_16x16x32_bf16 v[56:59], a[160:163], v[176:179], v[56:59]\n"
    "v_mad_u32_u16 v253, v255, s42, v251 op_sel:[0,0,0,0]\n"
    "v_cvt_pk_bf16_f32 v217, v218, v219\n"
    "v_mfma_f32_16x16x32_bf16 v[56:59], a[164:167], v[180:183], v[56:59]\n"
    "buffer_load_dwordx4 a[232:235], v253, s[20:23], 0 offen offset:0 sc1\n"
    "v_mfma_f32_16x16x32_bf16 v[60:63], a[164:167], v[188:191], v[60:63]\n"
    "v_exp_f32_e32 v210, v210\n"
    "v_mfma_f32_16x16x32_bf16 v[60:63], a[160:163], v[184:187], v[60:63]\n"
    "v_exp_f32_e32 v211, v211\n"
    "v_mfma_f32_16x16x32_bf16 v[64:67], a[160:163], v[192:195], v[64:67]\n"
    "ds_read_b64_tr_b16 a[160:161], v244 offset:36864\n"
    "v_mad_u32_u16 v253, v255, s42, v251 op_sel:[1,0,0,0]\n"
    "v_mfma_f32_16x16x32_bf16 v[64:67], a[164:167], v[196:199], v[64:67]\n"
    "ds_read_b64_tr_b16 a[162:163], v244 offset:38912\n"
    "buffer_load_dwordx4 a[236:239], v253, s[20:23], 0 offen offset:0 sc1\n"
    "v_mfma_f32_16x16x32_bf16 v[76:79], a[168:171], v[192:195], v[76:79]\n"
    "ds_read_b64_tr_b16 a[164:165], v244 offset:45056\n"
    "v_cvt_pk_bf16_f32 v208, v208, v209\n"
    "v_mfma_f32_16x16x32_bf16 v[76:79], a[172:175], v[196:199], v[76:79]\n"
    "ds_read_b64_tr_b16 a[166:167], v244 offset:47104\n"
    "v_mfma_f32_16x16x32_bf16 v[72:75], a[172:175], v[188:191], v[72:75]\n"
    "ds_read_b64 v[254:255], v252 offset:104\n"
    "v_mfma_f32_16x16x32_bf16 v[72:75], a[168:171], v[184:187], v[72:75]\n"
    "v_exp_f32_e32 v200, v200\n"
    "v_mfma_f32_16x16x32_bf16 v[68:71], a[168:171], v[176:179], v[68:71]\n"
    "v_exp_f32_e32 v201, v201\n"
    "v_mfma_f32_16x16x32_bf16 v[68:71], a[172:175], v[180:183], v[68:71]\n"
    "v_cvt_pk_bf16_f32 v209, v210, v211\n"
    "v_mfma_f32_16x16x32_bf16 a[252:255], a[172:175], v[172:175], a[252:255]\n"
    "v_exp_f32_e32 v202, v202\n"
    "v_mfma_f32_16x16x32_bf16 a[252:255], a[168:171], v[168:171], a[252:255]\n"
    "v_exp_f32_e32 v203, v203\n"
    "v_mfma_f32_16x16x32_bf16 v[4:7], a[168:171], v[160:163], v[4:7]\n"
    "s_waitcnt lgkmcnt(0)\n"
    "ds_read_b64_tr_b16 a[168:169], v245 offset:36864\n"
    "v_mad_u32_u16 v253, v254, s42, v251 op_sel:[0,0,0,0]\n"
    "v_mfma_f32_16x16x32_bf16 v[4:7], a[172:175], v[164:167], v[4:7]\n"
    "ds_read_b64_tr_b16 a[170:171], v245 offset:38912\n"
    "v_cvt_pk_bf16_f32 v200, v200, v201\n"
    "v_mfma_f32_16x16x32_bf16 v[8:11], a[176:179], v[160:163], v[8:11]\n"
    "ds_read_b64_tr_b16 a[172:173], v245 offset:45056\n"
    "buffer_load_dwordx4 a[240:243], v253, s[20:23], 0 offen offset:0 sc1\n"
    "v_mfma_f32_16x16x32_bf16 v[8:11], a[180:183], v[164:167], v[8:11]\n"
    "ds_read_b64_tr_b16 a[174:175], v245 offset:47104\n"
    "v_cvt_pk_bf16_f32 v201, v202, v203\n"
    "v_mfma_f32_16x16x32_bf16 v[32:35], a[180:183], v[172:175], v[32:35]\n"
    "v_exp_f32_e32 v236, v236\n"
    "v_mfma_f32_16x16x32_bf16 v[32:35], a[176:179], v[168:171], v[32:35]\n"
    "v_mad_u32_u16 v253, v254, s42, v251 op_sel:[1,0,0,0]\n"
    "v_mfma_f32_16x16x32_bf16 v[80:83], a[176:179], v[176:179], v[80:83]\n"
    "buffer_load_dwordx4 a[244:247], v253, s[20:23], 0 offen offset:0 sc1\n"
    "v_mfma_f32_16x16x32_bf16 v[80:83], a[180:183], v[180:183], v[80:83]\n"
    "v_exp_f32_e32 v237, v237\n"
    "v_mfma_f32_16x16x32_bf16 v[84:87], a[180:183], v[188:191], v[84:87]\n"
    "v_exp_f32_e32 v238, v238\n"
    "v_mfma_f32_16x16x32_bf16 v[84:87], a[176:179], v[184:187], v[84:87]\n"
    "v_exp_f32_e32 v239, v239\n"
    "v_mfma_f32_16x16x32_bf16 v[88:91], a[176:179], v[192:195], v[88:91]\n"
    "ds_read_b64_tr_b16 a[176:177], v246 offset:36864\n"
    "v_cvt_pk_bf16_f32 v234, v236, v237\n"
    "v_mfma_f32_16x16x32_bf16 v[88:91], a[180:183], v[196:199], v[88:91]\n"
    "ds_read_b64_tr_b16 a[178:179], v246 offset:38912\n"
    "v_exp_f32_e32 v228, v228\n"
    "v_mfma_f32_16x16x32_bf16 v[100:103], a[184:187], v[192:195], v[100:103]\n"
    "ds_read_b64_tr_b16 a[180:181], v246 offset:45056\n"
    "v_exp_f32_e32 v229, v229\n"
    "v_mfma_f32_16x16x32_bf16 v[100:103], a[188:191], v[196:199], v[100:103]\n"
    "ds_read_b64_tr_b16 a[182:183], v246 offset:47104\n"
    "v_cvt_pk_bf16_f32 v235, v238, v239\n"
    "v_mfma_f32_16x16x32_bf16 v[96:99], a[188:191], v[188:191], v[96:99]\n"
    "v_exp_f32_e32 v230, v230\n"
    "v_mfma_f32_16x16x32_bf16 v[96:99], a[184:187], v[184:187], v[96:99]\n"
    "v_exp_f32_e32 v231, v231\n"
    "v_cvt_pk_bf16_f32 v226, v228, v229\n"
    "v_mfma_f32_16x16x32_bf16 v[92:95], a[184:187], v[176:179], v[92:95]\n"
    "v_exp_f32_e32 v220, v220\n"
    "v_mfma_f32_16x16x32_bf16 v[92:95], a[188:191], v[180:183], v[92:95]\n"
    "v_exp_f32_e32 v221, v221\n"
    "v_mfma_f32_16x16x32_bf16 v[36:39], a[188:191], v[172:175], v[36:39]\n"
    "v_cvt_pk_bf16_f32 v227, v230, v231\n"
    "v_mfma_f32_16x16x32_bf16 v[36:39], a[184:187], v[168:171], v[36:39]\n"
    "v_exp_f32_e32 v222, v222\n"
    "v_mfma_f32_16x16x32_bf16 v[12:15], a[184:187], v[160:163], v[12:15]\n"
    "s_mov_b32 m0, s49\n"
    "ds_read_b64_tr_b16 a[184:185], v247 offset:36864\n"
    "v_mfma_f32_16x16x32_bf16 v[12:15], a[188:191], v[164:167], v[12:15]\n"
    "s_waitcnt lgkmcnt(5)\n"
    "ds_read_b64_tr_b16 a[186:187], v247 offset:38912\n"
    "v_mfma_f32_16x16x32_bf16 v[16:19], a[160:163], v[160:163], v[16:19]\n"
    "ds_read_b64_tr_b16 a[188:189], v247 offset:45056\n"
    "v_mfma_f32_16x16x32_bf16 v[16:19], a[164:167], v[164:167], v[16:19]\n"
    "ds_read_b64_tr_b16 a[190:191], v247 offset:47104\n"
    "v_mfma_f32_16x16x32_bf16 v[40:43], a[164:167], v[172:175], v[40:43]\n"
    "v_exp_f32_e32 v223, v223\n"
    "v_mfma_f32_16x16x32_bf16 v[40:43], a[160:163], v[168:171], v[40:43]\n"
    "v_cvt_pk_bf16_f32 v218, v220, v221\n"
    "v_mfma_f32_16x16x32_bf16 v[104:107], a[160:163], v[176:179], v[104:107]\n"
    "v_exp_f32_e32 v212, v212\n"
    "v_mfma_f32_16x16x32_bf16 v[104:107], a[164:167], v[180:183], v[104:107]\n"
    "s_waitcnt vmcnt(20)\n"
    "v_mfma_f32_16x16x32_bf16 v[108:111], a[164:167], v[188:191], v[108:111]\n"
    "s_barrier\n"
    "v_mfma_f32_16x16x32_bf16 v[108:111], a[160:163], v[184:187], v[108:111]\n"
    "v_exp_f32_e32 v213, v213\n"
    "v_mfma_f32_16x16x32_bf16 v[112:115], a[160:163], v[192:195], v[112:115]\n"
    "ds_read_b128 a[160:163], v243 offset:4096\n"
    "v_cvt_pk_bf16_f32 v219, v222, v223\n"
    "v_mfma_f32_16x16x32_bf16 v[112:115], a[164:167], v[196:199], v[112:115]\n"
    "ds_read_b128 a[164:167], v240 offset:0\n"
    "ds_write_addtid_b32 v232 offset:16384\n"
    "v_mfma_f32_16x16x32_bf16 v[124:127], a[168:171], v[192:195], v[124:127]\n"
    "v_exp_f32_e32 v214, v214\n"
    "v_mfma_f32_16x16x32_bf16 v[124:127], a[172:175], v[196:199], v[124:127]\n"
    "v_exp_f32_e32 v215, v215\n"
    "v_mfma_f32_16x16x32_bf16 v[120:123], a[172:175], v[188:191], v[120:123]\n"
    "ds_write_addtid_b32 v233 offset:16640\n"
    "v_cvt_pk_bf16_f32 v210, v212, v213\n"
    "v_mfma_f32_16x16x32_bf16 v[120:123], a[168:171], v[184:187], v[120:123]\n"
    "v_exp_f32_e32 v204, v204\n"
    "v_mfma_f32_16x16x32_bf16 v[116:119], a[168:171], v[176:179], v[116:119]\n"
    "v_exp_f32_e32 v205, v205\n"
    "v_mfma_f32_16x16x32_bf16 v[116:119], a[172:175], v[180:183], v[116:119]\n"
    "ds_write_addtid_b32 v224 offset:12288\n"
    "v_cvt_pk_bf16_f32 v211, v214, v215\n"
    "v_mfma_f32_16x16x32_bf16 v[44:47], a[172:175], v[172:175], v[44:47]\n"
    "v_exp_f32_e32 v206, v206\n"
    "v_mfma_f32_16x16x32_bf16 v[44:47], a[168:171], v[168:171], v[44:47]\n"
    "v_exp_f32_e32 v207, v207\n"
    "v_mfma_f32_16x16x32_bf16 v[20:23], a[168:171], v[160:163], v[20:23]\n"
    "ds_read_b128 a[168:171], v241 offset:0\n"
    "v_cvt_pk_bf16_f32 v202, v204, v205\n"
    "v_mfma_f32_16x16x32_bf16 v[20:23], a[172:175], v[164:167], v[20:23]\n"
    "s_waitcnt lgkmcnt(6)\n"
    "ds_read_b128 a[172:175], v240 offset:4096\n"
    "v_cvt_pk_bf16_f32 v203, v206, v207\n"
    "v_mfma_f32_16x16x32_bf16 v[24:27], a[176:179], v[160:163], v[24:27]\n"
    "ds_write_addtid_b32 v225 offset:12544\n"
    "ds_write_addtid_b32 v216 offset:8192\n"
    "v_mfma_f32_16x16x32_bf16 v[24:27], a[180:183], v[164:167], v[24:27]\n"
    "ds_write_addtid_b32 v217 offset:8448\n"
    "v_mfma_f32_16x16x32_bf16 v[48:51], a[180:183], v[172:175], v[48:51]\n"
    "ds_write_addtid_b32 v208 offset:4096\n"
    "v_mfma_f32_16x16x32_bf16 v[48:51], a[176:179], v[168:171], v[48:51]\n"
    "ds_write_addtid_b32 v209 offset:4352\n"
    "v_mfma_f32_16x16x32_bf16 v[128:131], a[176:179], v[176:179], v[128:131]\n"
    "ds_write_addtid_b32 v200 offset:0\n"
    "v_mfma_f32_16x16x32_bf16 v[128:131], a[180:183], v[180:183], v[128:131]\n"
    "ds_write_addtid_b32 v201 offset:256\n"
    "v_mfma_f32_16x16x32_bf16 v[132:135], a[180:183], v[188:191], v[132:135]\n"
    "ds_write_addtid_b32 v234 offset:16896\n"
    "v_mfma_f32_16x16x32_bf16 v[132:135], a[176:179], v[184:187], v[132:135]\n"
    "ds_write_addtid_b32 v235 offset:17152\n"
    "v_mfma_f32_16x16x32_bf16 v[136:139], a[176:179], v[192:195], v[136:139]\n"
    "ds_read_b128 a[176:179], v241 offset:4096\n"
    "v_mfma_f32_16x16x32_bf16 v[136:139], a[180:183], v[196:199], v[136:139]\n"
    "ds_read_b128 a[180:183], v242 offset:0\n"
    "v_mfma_f32_16x16x32_bf16 v[148:151], a[184:187], v[192:195], v[148:151]\n"
    "ds_write_addtid_b32 v226 offset:12800\n"
    "v_mfma_f32_16x16x32_bf16 v[148:151], a[188:191], v[196:199], v[148:151]\n"
    "ds_write_addtid_b32 v227 offset:13056\n"
    "v_mfma_f32_16x16x32_bf16 v[144:147], a[188:191], v[188:191], v[144:147]\n"
    "ds_write_addtid_b32 v218 offset:8704\n"
    "v_mfma_f32_16x16x32_bf16 v[144:147], a[184:187], v[184:187], v[144:147]\n"
    "ds_write_addtid_b32 v219 offset:8960\n"
    "v_mfma_f32_16x16x32_bf16 v[140:143], a[184:187], v[176:179], v[140:143]\n"
    "ds_write_addtid_b32 v210 offset:4608\n"
    "v_mfma_f32_16x16x32_bf16 v[140:143], a[188:191], v[180:183], v[140:143]\n"
    "ds_write_addtid_b32 v211 offset:4864\n"
    "v_mfma_f32_16x16x32_bf16 v[52:55], a[188:191], v[172:175], v[52:55]\n"
    "ds_write_addtid_b32 v202 offset:512\n"
    "v_mfma_f32_16x16x32_bf16 v[52:55], a[184:187], v[168:171], v[52:55]\n"
    "ds_write_addtid_b32 v203 offset:768\n"
    "v_mfma_f32_16x16x32_bf16 v[28:31], a[184:187], v[160:163], v[28:31]\n"
    "ds_read_b128 a[184:187], v243 offset:0\n"
    "v_mfma_f32_16x16x32_bf16 v[28:31], a[188:191], v[164:167], v[28:31]\n"
    "s_waitcnt lgkmcnt(15)\n"
    "ds_read_b128 a[188:191], v242 offset:4096\n"
    "v_mfma_f32_16x16x32_bf16 v[160:163], a[160:163], a[28:31], 0\n"
    "v_mfma_f32_16x16x32_bf16 v[160:163], a[164:167], a[0:3], v[160:163]\n"
    "v_mfma_f32_16x16x32_bf16 v[168:171], a[164:167], a[32:35], 0\n"
    "v_mfma_f32_16x16x32_bf16 v[168:171], a[160:163], a[60:63], v[168:171]\n"
    "v_mfma_f32_16x16x32_bf16 v[176:179], a[160:163], a[92:95], 0\n"
    "v_mfma_f32_16x16x32_bf16 v[176:179], a[164:167], a[64:67], v[176:179]\n"
    "s_waitcnt lgkmcnt(0)\n"
    "v_mfma_f32_16x16x32_bf16 v[184:187], a[164:167], a[96:99], 0\n"
    "s_barrier\n"
    "v_mfma_f32_16x16x32_bf16 v[184:187], a[160:163], a[124:127], v[184:187]\n"
    "s_add_u32 m0, s43, 32768\n"
    "v_mfma_f32_16x16x32_bf16 v[192:195], a[160:163], a[156:159], 0\n"
    "ds_read_b128 a[160:163], v243 offset:6144\n"
    "v_mfma_f32_16x16x32_bf16 v[192:195], a[164:167], a[128:131], v[192:195]\n"
    "ds_read_b128 a[164:167], v240 offset:2048\n"
    "v_mfma_f32_16x16x32_bf16 v[192:195], a[168:171], a[132:135], v[192:195]\n"
    "buffer_load_dwordx4 v248, s[8:11], s40 offen offset:0 sc1 lds\n"
    "v_mfma_f32_16x16x32_bf16 v[192:195], a[172:175], a[136:139], v[192:195]\n"
    "ds_read2st64_b32 v[204:205], v250 offset0:0 offset1:1\n"
    "v_mfma_f32_16x16x32_bf16 v[184:187], a[172:175], a[104:107], v[184:187]\n"
    "s_waitcnt vmcnt(11)\n"
    "v_smfmac_f32_16x16x64_bf16 v[156:159], a[192:195], a[200:207], v152\n"
    "v_mfma_f32_16x16x32_bf16 v[184:187], a[168:171], a[100:103], v[184:187]\n"
    "ds_read2st64_b32 v[206:207], v250 offset0:2 offset1:3\n"
    "v_mfma_f32_16x16x32_bf16 v[176:179], a[168:171], a[68:71], v[176:179]\n"
    "buffer_load_dwordx4 v248, s[8:11], s40 offen offset:1024 sc1 lds\n"
    "v_mfma_f32_16x16x32_bf16 v[176:179], a[172:175], a[72:75], v[176:179]\n"
    "v_mfma_f32_16x16x32_bf16 v[168:171], a[172:175], a[40:43], v[168:171]\n"
    "s_waitcnt vmcnt(10)\n"
    "v_smfmac_f32_16x16x64_bf16 v[156:159], a[192:195], a[208:215], v152\n"
    "ds_read2st64_b32 v[212:213], v250 offset0:16 offset1:17\n"
    "v_mfma_f32_16x16x32_bf16 v[168:171], a[168:171], a[36:39], v[168:171]\n"
    "v_mfma_f32_16x16x32_bf16 v[160:163], a[168:171], a[4:7], v[160:163]\n"
    "ds_read_b128 a[168:171], v241 offset:2048\n"
    "buffer_load_dwordx4 v248, s[8:11], s40 offen offset:2048 sc1 lds\n"
    "v_mfma_f32_16x16x32_bf16 v[160:163], a[172:175], a[8:11], v[160:163]\n"
    "ds_read_b128 a[172:175], v240 offset:6144\n"
    "v_mfma_f32_16x16x32_bf16 v[160:163], a[176:179], a[12:15], v[160:163]\n"
    "s_waitcnt vmcnt(9)\n"
    "ds_read2st64_b32 v[214:215], v250 offset0:18 offset1:19\n"
    "v_smfmac_f32_16x16x64_bf16 v[156:159], a[192:195], a[216:223], v152\n"
    "v_mfma_f32_16x16x32_bf16 v[160:163], a[180:183], a[16:19], v[160:163]\n"
    "v_mfma_f32_16x16x32_bf16 v[168:171], a[180:183], a[48:51], v[168:171]\n"
    "buffer_load_dwordx4 v248, s[8:11], s40 offen offset:3072 sc1 lds\n"
    "v_mfma_f32_16x16x32_bf16 v[168:171], a[176:179], a[44:47], v[168:171]\n"
    "s_add_u32 m0, s43, 36864\n"
    "v_mfma_f32_16x16x32_bf16 v[176:179], a[176:179], a[76:79], v[176:179]\n"
    "s_add_u32 s40, s40, 0x8000\n"
    "ds_read2st64_b32 v[220:221], v250 offset0:32 offset1:33\n"
    "v_mfma_f32_16x16x32_bf16 v[176:179], a[180:183], a[80:83], v[176:179]\n"
    "v_mfma_f32_16x16x32_bf16 v[184:187], a[180:183], a[112:115], v[184:187]\n"
    "v_mfma_f32_16x16x32_bf16 v[184:187], a[176:179], a[108:111], v[184:187]\n"
    "buffer_load_dwordx4 v248, s[8:11], s41 offen offset:0 sc1 lds\n"
    "v_mfma_f32_16x16x32_bf16 v[192:195], a[176:179], a[140:143], v[192:195]\n"
    "ds_read_b128 a[176:179], v241 offset:6144\n"
    "v_mfma_f32_16x16x32_bf16 v[192:195], a[180:183], a[144:147], v[192:195]\n"
    "ds_read_b128 a[180:183], v242 offset:2048\n"
    "v_mfma_f32_16x16x32_bf16 v[192:195], a[184:187], a[148:151], v[192:195]\n"
    "ds_read2st64_b32 v[222:223], v250 offset0:34 offset1:35\n"
    "v_mfma_f32_16x16x32_bf16 v[192:195], a[188:191], a[152:155], v[192:195]\n"
    "v_mfma_f32_16x16x32_bf16 v[184:187], a[188:191], a[120:123], v[184:187]\n"
    "buffer_load_dwordx4 v248, s[8:11], s41 offen offset:1024 sc1 lds\n"
    "v_mfma_f32_16x16x32_bf16 v[184:187], a[184:187], a[116:119], v[184:187]\n"
    "ds_read2st64_b32 v[228:229], v250 offset0:48 offset1:49\n"
    "v_mfma_f32_16x16x32_bf16 v[176:179], a[184:187], a[84:87], v[176:179]\n"
    "s_waitcnt vmcnt(10)\n"
    "v_exp_f32_e32 v192, v192\n"
    "v_smfmac_f32_16x16x64_bf16 v[156:159], a[192:195], a[224:231], v152\n"
    "v_exp_f32_e32 v193, v193\n"
    "v_mfma_f32_16x16x32_bf16 v[176:179], a[188:191], a[88:91], v[176:179]\n"
    "v_exp_f32_e32 v194, v194\n"
    "v_mfma_f32_16x16x32_bf16 v[168:171], a[188:191], a[56:59], v[168:171]\n"
    "buffer_load_dwordx4 v248, s[8:11], s41 offen offset:2048 sc1 lds\n"
    "v_mfma_f32_16x16x32_bf16 v[168:171], a[184:187], a[52:55], v[168:171]\n"
    "ds_read2st64_b32 v[230:231], v250 offset0:50 offset1:51\n"
    "v_mfma_f32_16x16x32_bf16 v[160:163], a[184:187], a[20:23], v[160:163]\n"
    "s_waitcnt vmcnt(9)\n"
    "ds_read_b128 a[184:187], v243 offset:2048\n"
    "v_smfmac_f32_16x16x64_bf16 v[156:159], a[192:195], a[232:239], v152\n"
    "ds_read2st64_b32 v[236:237], v250 offset0:64 offset1:65\n"
    "v_mfma_f32_16x16x32_bf16 v[160:163], a[188:191], a[24:27], v[160:163]\n"
    "s_waitcnt lgkmcnt(9)\n"
    "ds_read_b128 a[188:191], v242 offset:6144\n"
    "v_mfma_f32_16x16x32_bf16 v[164:167], a[160:163], a[28:31], 0\n"
    "buffer_load_dwordx4 v248, s[8:11], s41 offen offset:3072 sc1 lds\n"
    "v_mfma_f32_16x16x32_bf16 v[164:167], a[164:167], a[0:3], v[164:167]\n"
    "s_add_u32 s41, s41, 0x8000\n"
    "v_exp_f32_e32 v195, v195\n"
    "v_mfma_f32_16x16x32_bf16 v[172:175], a[164:167], a[32:35], 0\n"
    "s_waitcnt vmcnt(8)\n"
    "v_cvt_pk_bf16_f32 v192, v192, v193\n"
    "v_smfmac_f32_16x16x64_bf16 v[156:159], a[192:195], a[240:247], v152\n"
    "v_exp_f32_e32 v184, v184\n"
    "v_mfma_f32_16x16x32_bf16 v[172:175], a[160:163], a[60:63], v[172:175]\n"
    "ds_read2st64_b32 v[238:239], v250 offset0:66 offset1:67\n"
    "v_mfma_f32_16x16x32_bf16 v[180:183], a[160:163], a[92:95], 0\n"
    "v_exp_f32_e32 v185, v185\n"
    "v_mfma_f32_16x16x32_bf16 v[180:183], a[164:167], a[64:67], v[180:183]\n"
    "v_cvt_pk_bf16_f32 v193, v194, v195\n"
    "v_mfma_f32_16x16x32_bf16 v[188:191], a[164:167], a[96:99], 0\n"
    "v_exp_f32_e32 v186, v186\n"
    "v_mfma_f32_16x16x32_bf16 v[188:191], a[160:163], a[124:127], v[188:191]\n"
    "v_exp_f32_e32 v187, v187\n"
    "v_mfma_f32_16x16x32_bf16 v[196:199], a[160:163], a[156:159], 0\n"
    "ds_read_b64_tr_b16 a[160:161], v244 offset:49152\n"
    "v_and_b32_e32 v253, 0xffff0000, v255\n"
    "v_mfma_f32_16x16x32_bf16 v[196:199], a[164:167], a[128:131], v[196:199]\n"
    "ds_read_b64_tr_b16 a[162:163], v244 offset:51200\n"
    "v_mfma_f32_16x16x32_bf16 v[196:199], a[168:171], a[132:135], v[196:199]\n"
    "ds_read_b64_tr_b16 a[164:165], v244 offset:57344\n"
    "v_mfma_f32_16x16x32_bf16 v[196:199], a[172:175], a[136:139], v[196:199]\n"
    "ds_read_b64_tr_b16 a[166:167], v244 offset:59392\n"
    "v_mfma_f32_16x16x32_bf16 v[188:191], a[172:175], a[104:107], v[188:191]\n"
    "v_cvt_pk_bf16_f32 v184, v184, v185\n"
    "v_mfma_f32_16x16x32_bf16 v[188:191], a[168:171], a[100:103], v[188:191]\n"
    "v_exp_f32_e32 v176, v176\n"
    "v_mfma_f32_16x16x32_bf16 v[180:183], a[168:171], a[68:71], v[180:183]\n"
    "v_exp_f32_e32 v177, v177\n"
    "v_mfma_f32_16x16x32_bf16 v[180:183], a[172:175], a[72:75], v[180:183]\n"
    "v_cvt_pk_bf16_f32 v185, v186, v187\n"
    "v_mul_f32_e32 v156, v253, v156\n"
    "v_mfma_f32_16x16x32_bf16 v[172:175], a[172:175], a[40:43], v[172:175]\n"
    "v_exp_f32_e32 v178, v178\n"
    "v_mfma_f32_16x16x32_bf16 v[172:175], a[168:171], a[36:39], v[172:175]\n"
    "v_exp_f32_e32 v179, v179\n"
    "v_mfma_f32_16x16x32_bf16 v[164:167], a[168:171], a[4:7], v[164:167]\n"
    "ds_read_b64_tr_b16 a[168:169], v245 offset:49152\n"
    "v_mfma_f32_16x16x32_bf16 v[164:167], a[172:175], a[8:11], v[164:167]\n"
    "s_waitcnt lgkmcnt(6)\n"
    "ds_read_b64_tr_b16 a[170:171], v245 offset:51200\n"
    "v_mfma_f32_16x16x32_bf16 v[164:167], a[176:179], a[12:15], v[164:167]\n"
    "ds_read_b64_tr_b16 a[172:173], v245 offset:57344\n"
    "v_mul_f32_e32 v157, v253, v157\n"
    "v_mfma_f32_16x16x32_bf16 v[164:167], a[180:183], a[16:19], v[164:167]\n"
    "ds_read_b64_tr_b16 a[174:175], v245 offset:59392\n"
    "v_mfma_f32_16x16x32_bf16 v[172:175], a[180:183], a[48:51], v[172:175]\n"
    "v_cvt_pk_bf16_f32 v176, v176, v177\n"
    "v_mfma_f32_16x16x32_bf16 v[172:175], a[176:179], a[44:47], v[172:175]\n"
    "v_exp_f32_e32 v168, v168\n"
    "v_mfma_f32_16x16x32_bf16 v[180:183], a[176:179], a[76:79], v[180:183]\n"
    "v_mul_f32_e32 v158, v253, v158\n"
    "v_mfma_f32_16x16x32_bf16 v[180:183], a[180:183], a[80:83], v[180:183]\n"
    "v_exp_f32_e32 v169, v169\n"
    "v_mfma_f32_16x16x32_bf16 v[188:191], a[180:183], a[112:115], v[188:191]\n"
    "v_cvt_pk_bf16_f32 v177, v178, v179\n"
    "v_mfma_f32_16x16x32_bf16 v[188:191], a[176:179], a[108:111], v[188:191]\n"
    "v_exp_f32_e32 v170, v170\n"
    "v_mfma_f32_16x16x32_bf16 v[196:199], a[176:179], a[140:143], v[196:199]\n"
    "ds_read_b64_tr_b16 a[176:177], v246 offset:49152\n"
    "v_mfma_f32_16x16x32_bf16 v[196:199], a[180:183], a[144:147], v[196:199]\n"
    "ds_read_b64_tr_b16 a[178:179], v246 offset:51200\n"
    "v_mfma_f32_16x16x32_bf16 v[196:199], a[184:187], a[148:151], v[196:199]\n"
    "ds_read_b64_tr_b16 a[180:181], v246 offset:57344\n"
    "v_mfma_f32_16x16x32_bf16 v[196:199], a[188:191], a[152:155], v[196:199]\n"
    "ds_read_b64_tr_b16 a[182:183], v246 offset:59392\n"
    "v_mul_f32_e32 v159, v253, v159\n"
    "v_mfma_f32_16x16x32_bf16 v[188:191], a[188:191], a[120:123], v[188:191]\n"
    "v_exp_f32_e32 v171, v171\n"
    "v_mfma_f32_16x16x32_bf16 v[188:191], a[184:187], a[116:119], v[188:191]\n"
    "v_cvt_pk_bf16_f32 v168, v168, v169\n"
    "v_mfma_f32_16x16x32_bf16 v[180:183], a[184:187], a[84:87], v[180:183]\n"
    "v_exp_f32_e32 v160, v160\n"
    "v_mfma_f32_16x16x32_bf16 v[180:183], a[188:191], a[88:91], v[180:183]\n"
    "v_exp_f32_e32 v161, v161\n"
    "v_mfma_f32_16x16x32_bf16 v[172:175], a[188:191], a[56:59], v[172:175]\n"
    "v_cvt_pk_bf16_f32 v169, v170, v171\n"
    "v_mfma_f32_16x16x32_bf16 v[172:175], a[184:187], a[52:55], v[172:175]\n"
    "v_exp_f32_e32 v162, v162\n"
    "v_mfma_f32_16x16x32_bf16 v[164:167], a[184:187], a[20:23], v[164:167]\n"
    "ds_read_b64_tr_b16 a[184:185], v247 offset:49152\n"
    "v_mad_u32_u16 v253, v255, s42, v153 op_sel:[0,0,0,0]\n"
    "v_mfma_f32_16x16x32_bf16 v[164:167], a[188:191], a[24:27], v[164:167]\n"
    "s_waitcnt lgkmcnt(5)\n"
    "ds_read_b64_tr_b16 a[186:187], v247 offset:51200\n"
    "v_mfma_f32_16x16x32_bf16 v[0:3], a[160:163], v[200:203], v[0:3]\n"
    "ds_read_b64_tr_b16 a[188:189], v247 offset:57344\n"
    "v_mfma_f32_16x16x32_bf16 v[0:3], a[164:167], v[204:207], v[0:3]\n"
    "ds_read_b64_tr_b16 a[190:191], v247 offset:59392\n"
    "v_mfma_f32_16x16x32_bf16 a[248:251], a[164:167], v[212:215], a[248:251]\n"
    "v_exp_f32_e32 v163, v163\n"
    "v_mfma_f32_16x16x32_bf16 a[248:251], a[160:163], v[208:211], a[248:251]\n"
    "v_cvt_pk_bf16_f32 v160, v160, v161\n"
    "v_mfma_f32_16x16x32_bf16 v[56:59], a[160:163], v[216:219], v[56:59]\n"
    "v_cvt_pk_bf16_f32 v154, v156, v157\n"
    "v_mfma_f32_16x16x32_bf16 v[56:59], a[164:167], v[220:223], v[56:59]\n"
    "v_cvt_pk_bf16_f32 v161, v162, v163\n"
    "v_mfma_f32_16x16x32_bf16 v[60:63], a[164:167], v[228:231], v[60:63]\n"
    "v_exp_f32_e32 v196, v196\n"
    "v_mfma_f32_16x16x32_bf16 v[60:63], a[160:163], v[224:227], v[60:63]\n"
    "v_exp_f32_e32 v197, v197\n"
    "v_mfma_f32_16x16x32_bf16 v[64:67], a[160:163], v[232:235], v[64:67]\n"
    "ds_read_b64_tr_b16 a[160:161], v244 offset:53248\n"
    "v_mfma_f32_16x16x32_bf16 v[64:67], a[164:167], v[236:239], v[64:67]\n"
    "ds_read_b64_tr_b16 a[162:163], v244 offset:55296\n"
    "v_cvt_pk_bf16_f32 v155, v158, v159\n"
    "v_mfma_f32_16x16x32_bf16 v[76:79], a[168:171], v[232:235], v[76:79]\n"
    "ds_read_b64_tr_b16 a[164:165], v244 offset:61440\n"
    "v_mfma_f32_16x16x32_bf16 v[76:79], a[172:175], v[236:239], v[76:79]\n"
    "ds_read_b64_tr_b16 a[166:167], v244 offset:63488\n"
    "v_mfma_f32_16x16x32_bf16 v[72:75], a[172:175], v[228:231], v[72:75]\n"
    "v_exp_f32_e32 v198, v198\n"
    "v_mfma_f32_16x16x32_bf16 v[72:75], a[168:171], v[224:227], v[72:75]\n"
    "v_exp_f32_e32 v199, v199\n"
    "v_mfma_f32_16x16x32_bf16 v[68:71], a[168:171], v[216:219], v[68:71]\n"
    "v_cvt_pk_bf16_f32 v194, v196, v197\n"
    "v_mov_b32_e32 v156, 0\n"
    "v_mfma_f32_16x16x32_bf16 v[68:71], a[172:175], v[220:223], v[68:71]\n"
    "v_exp_f32_e32 v188, v188\n"
    "v_mfma_f32_16x16x32_bf16 a[252:255], a[172:175], v[212:215], a[252:255]\n"
    "v_exp_f32_e32 v189, v189\n"
    "v_mfma_f32_16x16x32_bf16 a[252:255], a[168:171], v[208:211], a[252:255]\n"
    "v_cvt_pk_bf16_f32 v195, v198, v199\n"
    "v_mfma_f32_16x16x32_bf16 v[4:7], a[168:171], v[200:203], v[4:7]\n"
    "ds_read_b64_tr_b16 a[168:169], v245 offset:53248\n"
    "v_mfma_f32_16x16x32_bf16 v[4:7], a[172:175], v[204:207], v[4:7]\n"
    "s_waitcnt lgkmcnt(5)\n"
    "ds_read_b64_tr_b16 a[170:171], v245 offset:55296\n"
    "v_mfma_f32_16x16x32_bf16 v[8:11], a[176:179], v[200:203], v[8:11]\n"
    "ds_read_b64_tr_b16 a[172:173], v245 offset:61440\n"
    "v_mfma_f32_16x16x32_bf16 v[8:11], a[180:183], v[204:207], v[8:11]\n"
    "ds_read_b64_tr_b16 a[174:175], v245 offset:63488\n"
    "v_mov_b32_e32 v157, 0\n"
    "v_mfma_f32_16x16x32_bf16 v[32:35], a[180:183], v[212:215], v[32:35]\n"
    "v_exp_f32_e32 v190, v190\n"
    "v_mfma_f32_16x16x32_bf16 v[32:35], a[176:179], v[208:211], v[32:35]\n"
    "v_exp_f32_e32 v191, v191\n"
    "v_mfma_f32_16x16x32_bf16 v[80:83], a[176:179], v[216:219], v[80:83]\n"
    "v_mov_b32_e32 v158, 0\n"
    "v_mfma_f32_16x16x32_bf16 v[80:83], a[180:183], v[220:223], v[80:83]\n"
    "v_cvt_pk_bf16_f32 v186, v188, v189\n"
    "v_mfma_f32_16x16x32_bf16 v[84:87], a[180:183], v[228:231], v[84:87]\n"
    "v_exp_f32_e32 v180, v180\n"
    "v_mfma_f32_16x16x32_bf16 v[84:87], a[176:179], v[224:227], v[84:87]\n"
    "v_exp_f32_e32 v181, v181\n"
    "v_mfma_f32_16x16x32_bf16 v[88:91], a[176:179], v[232:235], v[88:91]\n"
    "ds_read_b64_tr_b16 a[176:177], v246 offset:53248\n"
    "v_mfma_f32_16x16x32_bf16 v[88:91], a[180:183], v[236:239], v[88:91]\n"
    "ds_read_b64_tr_b16 a[178:179], v246 offset:55296\n"
    "v_mfma_f32_16x16x32_bf16 v[100:103], a[184:187], v[232:235], v[100:103]\n"
    "ds_read_b64_tr_b16 a[180:181], v246 offset:61440\n"
    "v_mfma_f32_16x16x32_bf16 v[100:103], a[188:191], v[236:239], v[100:103]\n"
    "ds_read_b64_tr_b16 a[182:183], v246 offset:63488\n"
    "v_mfma_f32_16x16x32_bf16 v[96:99], a[188:191], v[228:231], v[96:99]\n"
    "v_cvt_pk_bf16_f32 v187, v190, v191\n"
    "v_mov_b32_e32 v159, 0\n"
    "v_mfma_f32_16x16x32_bf16 v[96:99], a[184:187], v[224:227], v[96:99]\n"
    "v_exp_f32_e32 v182, v182\n"
    "v_mfma_f32_16x16x32_bf16 v[92:95], a[184:187], v[216:219], v[92:95]\n"
    "v_exp_f32_e32 v183, v183\n"
    "v_mfma_f32_16x16x32_bf16 v[92:95], a[188:191], v[220:223], v[92:95]\n"
    "v_cvt_pk_bf16_f32 v178, v180, v181\n"
    "v_mfma_f32_16x16x32_bf16 v[36:39], a[188:191], v[212:215], v[36:39]\n"
    "v_exp_f32_e32 v172, v172\n"
    "v_mfma_f32_16x16x32_bf16 v[36:39], a[184:187], v[208:211], v[36:39]\n"
    "v_exp_f32_e32 v173, v173\n"
    "v_mfma_f32_16x16x32_bf16 v[12:15], a[184:187], v[200:203], v[12:15]\n"
    "s_mov_b32 m0, s49\n"
    "ds_read_b64_tr_b16 a[184:185], v247 offset:53248\n"
    "v_mfma_f32_16x16x32_bf16 v[12:15], a[188:191], v[204:207], v[12:15]\n"
    "s_waitcnt lgkmcnt(5)\n"
    "ds_read_b64_tr_b16 a[186:187], v247 offset:55296\n"
    "v_add_u32_e32 v252, 0x80, v252\n"
    "v_mfma_f32_16x16x32_bf16 v[16:19], a[160:163], v[200:203], v[16:19]\n"
    "ds_read_b64_tr_b16 a[188:189], v247 offset:61440\n"
    "v_mfma_f32_16x16x32_bf16 v[16:19], a[164:167], v[204:207], v[16:19]\n"
    "ds_read_b64_tr_b16 a[190:191], v247 offset:63488\n"
    "v_mfma_f32_16x16x32_bf16 v[40:43], a[164:167], v[212:215], v[40:43]\n"
    "buffer_store_dwordx2 v[154:155], v253, s[24:27], 0 offen offset:0 sc1\n"
    "v_cvt_pk_bf16_f32 v179, v182, v183\n"
    "v_mfma_f32_16x16x32_bf16 v[40:43], a[160:163], v[208:211], v[40:43]\n"
    "v_exp_f32_e32 v174, v174\n"
    "v_mfma_f32_16x16x32_bf16 v[104:107], a[160:163], v[216:219], v[104:107]\n"
    "v_exp_f32_e32 v175, v175\n"
    "v_mfma_f32_16x16x32_bf16 v[104:107], a[164:167], v[220:223], v[104:107]\n"
    "s_waitcnt vmcnt(9)\n"
    "v_mfma_f32_16x16x32_bf16 v[108:111], a[164:167], v[228:231], v[108:111]\n"
    "s_barrier\n"
    "v_mfma_f32_16x16x32_bf16 v[108:111], a[160:163], v[224:227], v[108:111]\n"
    "ds_write_addtid_b32 v192 offset:16384\n"
    "v_cvt_pk_bf16_f32 v170, v172, v173\n"
    "v_mfma_f32_16x16x32_bf16 v[112:115], a[160:163], v[232:235], v[112:115]\n"
    "ds_read_b128 a[160:163], v243 offset:20480\n"
    "v_mfma_f32_16x16x32_bf16 v[112:115], a[164:167], v[236:239], v[112:115]\n"
    "ds_read_b128 a[164:167], v240 offset:16384\n"
    "v_mfma_f32_16x16x32_bf16 v[124:127], a[168:171], v[232:235], v[124:127]\n"
    "v_exp_f32_e32 v164, v164\n"
    "v_mfma_f32_16x16x32_bf16 v[124:127], a[172:175], v[236:239], v[124:127]\n"
    "v_exp_f32_e32 v165, v165\n"
    "v_mfma_f32_16x16x32_bf16 v[120:123], a[172:175], v[228:231], v[120:123]\n"
    "ds_write_addtid_b32 v193 offset:16640\n"
    "v_cvt_pk_bf16_f32 v171, v174, v175\n"
    "v_mfma_f32_16x16x32_bf16 v[120:123], a[168:171], v[224:227], v[120:123]\n"
    "ds_write_addtid_b32 v184 offset:12288\n"
    "v_mfma_f32_16x16x32_bf16 v[116:119], a[168:171], v[216:219], v[116:119]\n"
    "v_exp_f32_e32 v166, v166\n"
    "v_mfma_f32_16x16x32_bf16 v[116:119], a[172:175], v[220:223], v[116:119]\n"
    "v_exp_f32_e32 v167, v167\n"
    "v_mfma_f32_16x16x32_bf16 v[44:47], a[172:175], v[212:215], v[44:47]\n"
    "ds_write_addtid_b32 v185 offset:12544\n"
    "v_cvt_pk_bf16_f32 v162, v164, v165\n"
    "v_mfma_f32_16x16x32_bf16 v[44:47], a[168:171], v[208:211], v[44:47]\n"
    "v_cvt_pk_bf16_f32 v163, v166, v167\n"
    "v_mfma_f32_16x16x32_bf16 v[20:23], a[168:171], v[200:203], v[20:23]\n"
    "ds_read_b128 a[168:171], v241 offset:16384\n"
    "v_mfma_f32_16x16x32_bf16 v[20:23], a[172:175], v[204:207], v[20:23]\n"
    "s_waitcnt lgkmcnt(7)\n"
    "ds_read_b128 a[172:175], v240 offset:20480\n"
    "v_mfma_f32_16x16x32_bf16 v[24:27], a[176:179], v[200:203], v[24:27]\n"
    "ds_write_addtid_b32 v176 offset:8192\n"
    "v_mfma_f32_16x16x32_bf16 v[24:27], a[180:183], v[204:207], v[24:27]\n"
    "ds_write_addtid_b32 v177 offset:8448\n"
    "v_mfma_f32_16x16x32_bf16 v[48:51], a[180:183], v[212:215], v[48:51]\n"
    "ds_write_addtid_b32 v168 offset:4096\n"
    "v_mfma_f32_16x16x32_bf16 v[48:51], a[176:179], v[208:211], v[48:51]\n"
    "ds_write_addtid_b32 v169 offset:4352\n"
    "v_mfma_f32_16x16x32_bf16 v[128:131], a[176:179], v[216:219], v[128:131]\n"
    "ds_write_addtid_b32 v160 offset:0\n"
    "v_mfma_f32_16x16x32_bf16 v[128:131], a[180:183], v[220:223], v[128:131]\n"
    "ds_write_addtid_b32 v161 offset:256\n"
    "v_mfma_f32_16x16x32_bf16 v[132:135], a[180:183], v[228:231], v[132:135]\n"
    "ds_write_addtid_b32 v194 offset:16896\n"
    "v_mfma_f32_16x16x32_bf16 v[132:135], a[176:179], v[224:227], v[132:135]\n"
    "ds_write_addtid_b32 v195 offset:17152\n"
    "v_mfma_f32_16x16x32_bf16 v[136:139], a[176:179], v[232:235], v[136:139]\n"
    "ds_read_b128 a[176:179], v241 offset:20480\n"
    "v_mfma_f32_16x16x32_bf16 v[136:139], a[180:183], v[236:239], v[136:139]\n"
    "ds_read_b128 a[180:183], v242 offset:16384\n"
    "v_mfma_f32_16x16x32_bf16 v[148:151], a[184:187], v[232:235], v[148:151]\n"
    "ds_write_addtid_b32 v186 offset:12800\n"
    "v_mfma_f32_16x16x32_bf16 v[148:151], a[188:191], v[236:239], v[148:151]\n"
    "ds_write_addtid_b32 v187 offset:13056\n"
    "v_mfma_f32_16x16x32_bf16 v[144:147], a[188:191], v[228:231], v[144:147]\n"
    "ds_write_addtid_b32 v178 offset:8704\n"
    "v_mfma_f32_16x16x32_bf16 v[144:147], a[184:187], v[224:227], v[144:147]\n"
    "ds_write_addtid_b32 v179 offset:8960\n"
    "v_mfma_f32_16x16x32_bf16 v[140:143], a[184:187], v[216:219], v[140:143]\n"
    "s_sub_u32 s44, s44, 1\n"
    "ds_write_addtid_b32 v170 offset:4608\n"
    "v_mfma_f32_16x16x32_bf16 v[140:143], a[188:191], v[220:223], v[140:143]\n"
    "ds_write_addtid_b32 v171 offset:4864\n"
    "v_mfma_f32_16x16x32_bf16 v[52:55], a[188:191], v[212:215], v[52:55]\n"
    "ds_write_addtid_b32 v162 offset:512\n"
    "v_mfma_f32_16x16x32_bf16 v[52:55], a[184:187], v[208:211], v[52:55]\n"
    "ds_write_addtid_b32 v163 offset:768\n"
    "v_mfma_f32_16x16x32_bf16 v[28:31], a[184:187], v[200:203], v[28:31]\n"
    "ds_read_b128 a[184:187], v243 offset:16384\n"
    "v_mfma_f32_16x16x32_bf16 v[28:31], a[188:191], v[204:207], v[28:31]\n"
    "s_waitcnt lgkmcnt(15)\n"
    "ds_read_b128 a[188:191], v242 offset:20480\n"
    "s_cbranch_scc0 .Lat_g\n"
    "s_mov_b32 s44, 1\n"
    "s_bitcmp0_b32 %1, 0\n"
    "s_cbranch_scc1 .Lat_p\n"
    ".Lat_tail:\n"
    "v_mfma_f32_16x16x32_bf16 v[216:219], a[160:163], a[92:95], 0\n"
    "s_getpc_b64 s[62:63]\n"
    "v_and_b32_e32 v159, 0x3ff, v249\n"
    "v_sub_u32_e32 v157, v249, v159\n"
    "v_mfma_f32_16x16x32_bf16 v[216:219], a[164:167], a[64:67], v[216:219]\n"
    "s_add_u32 m0, s43, 49152\n"
    "v_lshlrev_b32_e32 v157, 1, v157\n"
    "v_lshl_add_u32 v159, v159, 3, v157\n"
    "v_mfma_f32_16x16x32_bf16 v[224:227], a[164:167], a[96:99], 0\n"
    "buffer_load_dwordx4 v248, s[8:11], s40 offen offset:0 sc1 lds\n"
    "v_mbcnt_lo_u32_b32 v253, -1, 0\n"
    "v_mfma_f32_16x16x32_bf16 v[224:227], a[160:163], a[124:127], v[224:227]\n"
    "global_load_dword v157, v159, s[62:63]\n"
    "v_mfma_f32_16x16x32_bf16 v[232:235], a[160:163], a[156:159], 0\n"
    "s_mov_b32 s45, 5775360\n"
    "ds_read_b128 a[160:163], v243 offset:22528\n"
    "v_mfma_f32_16x16x32_bf16 v[232:235], a[164:167], a[128:131], v[232:235]\n"
    "s_waitcnt lgkmcnt(1)\n"
    "ds_read_b128 a[164:167], v240 offset:18432\n"
    "v_mfma_f32_16x16x32_bf16 v[232:235], a[168:171], a[132:135], v[232:235]\n"
    "s_barrier\n"
    "v_mfma_f32_16x16x32_bf16 v[232:235], a[172:175], a[136:139], v[232:235]\n"
    "buffer_load_dwordx4 v248, s[8:11], s40 offen offset:1024 sc1 lds\n"
    "v_mfma_f32_16x16x32_bf16 v[224:227], a[172:175], a[104:107], v[224:227]\n"
    "s_lshl_b32 s56, %2, 15\n"
    "v_mbcnt_hi_u32_b32 v253, -1, v253\n"
    "v_mfma_f32_16x16x32_bf16 v[224:227], a[168:171], a[100:103], v[224:227]\n"
    "s_add_u32 s57, s56, 5505024\n"
    "ds_read2st64_b32 v[164:165], v250 offset0:0 offset1:1\n"
    "v_and_b32_e32 v254, 3, v253\n"
    "v_mfma_f32_16x16x32_bf16 v[216:219], a[168:171], a[68:71], v[216:219]\n"
    "ds_read_b128 a[168:171], v241 offset:18432\n"
    "v_mfma_f32_16x16x32_bf16 v[216:219], a[172:175], a[72:75], v[216:219]\n"
    "ds_read_b128 a[172:175], v240 offset:22528\n"
    "v_mfma_f32_16x16x32_bf16 v[216:219], a[176:179], a[76:79], v[216:219]\n"
    "buffer_load_dwordx4 v248, s[8:11], s40 offen offset:2048 sc1 lds\n"
    "ds_read2st64_b32 v[166:167], v250 offset0:2 offset1:3\n"
    "v_mfma_f32_16x16x32_bf16 v[216:219], a[180:183], a[80:83], v[216:219]\n"
    "s_add_u32 s56, s56, 5373952\n"
    "v_lshlrev_b32_e32 v254, 2, v254\n"
    "v_mfma_f32_16x16x32_bf16 v[224:227], a[180:183], a[112:115], v[224:227]\n"
    "s_lshl_b32 s47, %2, 7\n"
    "v_and_b32_e32 v158, 15, v253\n"
    "v_mfma_f32_16x16x32_bf16 v[224:227], a[176:179], a[108:111], v[224:227]\n"
    "s_add_u32 s47, s47, s35\n"
    "ds_read2st64_b32 v[172:173], v250 offset0:16 offset1:17\n"
    "v_lshlrev_b32_e32 v158, 9, v158\n"
    "v_mfma_f32_16x16x32_bf16 v[232:235], a[176:179], a[140:143], v[232:235]\n"
    "ds_read_b128 a[176:179], v241 offset:22528\n"
    "buffer_load_dwordx4 v248, s[8:11], s40 offen offset:3072 sc1 lds\n"
    "v_mfma_f32_16x16x32_bf16 v[232:235], a[180:183], a[144:147], v[232:235]\n"
    "s_add_u32 s40, s40, 32768\n"
    "ds_read_b128 a[180:183], v242 offset:18432\n"
    "v_mfma_f32_16x16x32_bf16 v[232:235], a[184:187], a[148:151], v[232:235]\n"
    "s_add_u32 m0, s43, 53248\n"
    "v_and_or_b32 v158, v253, 48, v158\n"
    "v_mfma_f32_16x16x32_bf16 v[232:235], a[188:191], a[152:155], v[232:235]\n"
    "ds_read2st64_b32 v[174:175], v250 offset0:18 offset1:19\n"
    "v_bfe_u32 v252, v253, 4, 2\n"
    "v_mfma_f32_16x16x32_bf16 v[224:227], a[188:191], a[120:123], v[224:227]\n"
    "buffer_load_dwordx4 v248, s[8:11], s41 offen offset:0 sc1 lds\n"
    "v_mfma_f32_16x16x32_bf16 v[224:227], a[184:187], a[116:119], v[224:227]\n"
    "v_lshlrev_b32_e32 v252, 1, v252\n"
    "v_mfma_f32_16x16x32_bf16 v[216:219], a[184:187], a[84:87], v[216:219]\n"
    "ds_read_b128 a[184:187], v243 offset:18432\n"
    "v_mfma_f32_16x16x32_bf16 v[216:219], a[188:191], a[88:91], v[216:219]\n"
    "s_waitcnt lgkmcnt(9)\n"
    "ds_read_b128 a[188:191], v242 offset:22528\n"
    "v_mfma_f32_16x16x32_bf16 v[220:223], a[160:163], a[92:95], 0\n"
    "buffer_load_dwordx4 v248, s[8:11], s41 offen offset:1024 sc1 lds\n"
    "ds_read2st64_b32 v[180:181], v250 offset0:32 offset1:33\n"
    "v_mfma_f32_16x16x32_bf16 v[220:223], a[164:167], a[64:67], v[220:223]\n"
    "ds_read2st64_b32 v[182:183], v250 offset0:34 offset1:35\n"
    "v_xor_b32_e32 v252, v252, v253\n"
    "v_mfma_f32_16x16x32_bf16 v[228:231], a[164:167], a[96:99], 0\n"
    "ds_read2st64_b32 v[188:189], v250 offset0:48 offset1:49\n"
    "v_and_b32_e32 v252, 7, v252\n"
    "v_mfma_f32_16x16x32_bf16 v[228:231], a[160:163], a[124:127], v[228:231]\n"
    "ds_read2st64_b32 v[190:191], v250 offset0:50 offset1:51\n"
    "v_lshlrev_b32_e32 v252, 4, v252\n"
    "v_mfma_f32_16x16x32_bf16 v[236:239], a[160:163], a[156:159], 0\n"
    "ds_read_b64_tr_b16 a[160:161], v244 offset:0\n"
    "buffer_load_dwordx4 v248, s[8:11], s41 offen offset:2048 sc1 lds\n"
    "v_mfma_f32_16x16x32_bf16 v[236:239], a[164:167], a[128:131], v[236:239]\n"
    "s_waitcnt lgkmcnt(12)\n"
    "ds_read_b64_tr_b16 a[162:163], v244 offset:2048\n"
    "v_mfma_f32_16x16x32_bf16 v[236:239], a[168:171], a[132:135], v[236:239]\n"
    "ds_read_b64_tr_b16 a[164:165], v244 offset:8192\n"
    "v_lshrrev_b32_e32 v255, 3, v253\n"
    "v_mfma_f32_16x16x32_bf16 v[236:239], a[172:175], a[136:139], v[236:239]\n"
    "ds_read_b64_tr_b16 a[166:167], v244 offset:10240\n"
    "v_mfma_f32_16x16x32_bf16 v[228:231], a[172:175], a[104:107], v[228:231]\n"
    "buffer_load_dwordx4 v248, s[8:11], s41 offen offset:3072 sc1 lds\n"
    "v_lshl_add_u32 v252, v255, 9, v252\n"
    "v_mfma_f32_16x16x32_bf16 v[228:231], a[168:171], a[100:103], v[228:231]\n"
    "s_add_u32 s41, s41, 32768\n"
    "s_add_u32 s61, s47, 0\n"
    "v_lshlrev_b32_e32 v253, 4, v253\n"
    "v_mfma_f32_16x16x32_bf16 v[220:223], a[168:171], a[68:71], v[220:223]\n"
    "s_mov_b32 m0, s49\n"
    "ds_read_b64_tr_b16 a[168:169], v245 offset:0\n"
    "v_mfma_f32_16x16x32_bf16 v[220:223], a[172:175], a[72:75], v[220:223]\n"
    "s_waitcnt lgkmcnt(12)\n"
    "ds_read_b64_tr_b16 a[170:171], v245 offset:2048\n"
    "v_mfma_f32_16x16x32_bf16 v[220:223], a[176:179], a[76:79], v[220:223]\n"
    "ds_read_b64_tr_b16 a[172:173], v245 offset:8192\n"
    "buffer_load_dwordx4 a[224:227], v158, s[12:15], s61 offen offset:0\n"
    "v_mfma_f32_16x16x32_bf16 v[220:223], a[180:183], a[80:83], v[220:223]\n"
    "ds_read_b64_tr_b16 a[174:175], v245 offset:10240\n"
    "v_mfma_f32_16x16x32_bf16 v[228:231], a[180:183], a[112:115], v[228:231]\n"
    "ds_read2st64_b32 v[196:197], v250 offset0:64 offset1:65\n"
    "v_mfma_f32_16x16x32_bf16 v[228:231], a[176:179], a[108:111], v[228:231]\n"
    "ds_read2st64_b32 v[198:199], v250 offset0:66 offset1:67\n"
    "v_mfma_f32_16x16x32_bf16 v[236:239], a[176:179], a[140:143], v[236:239]\n"
    "ds_read_b64_tr_b16 a[176:177], v246 offset:0\n"
    "v_mfma_f32_16x16x32_bf16 v[236:239], a[180:183], a[144:147], v[236:239]\n"
    "s_waitcnt lgkmcnt(15)\n"
    "ds_read_b64_tr_b16 a[178:179], v246 offset:2048\n"
    "v_mfma_f32_16x16x32_bf16 v[236:239], a[184:187], a[148:151], v[236:239]\n"
    "ds_read_b64_tr_b16 a[180:181], v246 offset:8192\n"
    "v_mfma_f32_16x16x32_bf16 v[236:239], a[188:191], a[152:155], v[236:239]\n"
    "ds_read_b64_tr_b16 a[182:183], v246 offset:10240\n"
    "v_mfma_f32_16x16x32_bf16 v[228:231], a[188:191], a[120:123], v[228:231]\n"
    "v_exp_f32_e32 v232, v232\n"
    "v_mfma_f32_16x16x32_bf16 v[228:231], a[184:187], a[116:119], v[228:231]\n"
    "v_exp_f32_e32 v233, v233\n"
    "v_mfma_f32_16x16x32_bf16 v[220:223], a[184:187], a[84:87], v[220:223]\n"
    "ds_read_b64_tr_b16 a[184:185], v247 offset:0\n"
    "v_mfma_f32_16x16x32_bf16 v[220:223], a[188:191], a[88:91], v[220:223]\n"
    "s_waitcnt lgkmcnt(5)\n"
    "ds_read_b64_tr_b16 a[186:187], v247 offset:2048\n"
    "v_mfma_f32_16x16x32_bf16 v[0:3], a[160:163], v[160:163], v[0:3]\n"
    "ds_read_b64_tr_b16 a[188:189], v247 offset:8192\n"
    "buffer_load_dwordx4 a[228:231], v158, s[12:15], s61 offen offset:64\n"
    "v_mfma_f32_16x16x32_bf16 v[0:3], a[164:167], v[164:167], v[0:3]\n"
    "s_add_u32 s61, s47, 8192\n"
    "ds_read_b64_tr_b16 a[190:191], v247 offset:10240\n"
    "v_mfma_f32_16x16x32_bf16 a[248:251], a[164:167], v[172:175], a[248:251]\n"
    "v_exp_f32_e32 v234, v234\n"
    "v_mfma_f32_16x16x32_bf16 a[248:251], a[160:163], v[168:171], a[248:251]\n"
    "v_exp_f32_e32 v235, v235\n"
    "v_mfma_f32_16x16x32_bf16 v[56:59], a[160:163], v[176:179], v[56:59]\n"
    "v_cvt_pk_bf16_f32 v232, v232, v233\n"
    "v_mfma_f32_16x16x32_bf16 v[56:59], a[164:167], v[180:183], v[56:59]\n"
    "v_exp_f32_e32 v224, v224\n"
    "v_mfma_f32_16x16x32_bf16 v[60:63], a[164:167], v[188:191], v[60:63]\n"
    "v_exp_f32_e32 v225, v225\n"
    "v_mfma_f32_16x16x32_bf16 v[60:63], a[160:163], v[184:187], v[60:63]\n"
    "v_cvt_pk_bf16_f32 v233, v234, v235\n"
    "v_mfma_f32_16x16x32_bf16 v[64:67], a[160:163], v[192:195], v[64:67]\n"
    "ds_read_b64_tr_b16 a[160:161], v244 offset:4096\n"
    "v_mfma_f32_16x16x32_bf16 v[64:67], a[164:167], v[196:199], v[64:67]\n"
    "ds_read_b64_tr_b16 a[162:163], v244 offset:6144\n"
    "v_mfma_f32_16x16x32_bf16 v[76:79], a[168:171], v[192:195], v[76:79]\n"
    "ds_read_b64_tr_b16 a[164:165], v244 offset:12288\n"
    "v_mfma_f32_16x16x32_bf16 v[76:79], a[172:175], v[196:199], v[76:79]\n"
    "ds_read_b64_tr_b16 a[166:167], v244 offset:14336\n"
    "v_mfma_f32_16x16x32_bf16 v[72:75], a[172:175], v[188:191], v[72:75]\n"
    "buffer_load_dwordx4 a[232:235], v158, s[12:15], s61 offen offset:0\n"
    "v_mfma_f32_16x16x32_bf16 v[72:75], a[168:171], v[184:187], v[72:75]\n"
    "s_waitcnt vmcnt(12)\n"
    "v_exp_f32_e32 v226, v226\n"
    "v_mfma_f32_16x16x32_bf16 v[68:71], a[168:171], v[176:179], v[68:71]\n"
    "s_barrier\n"
    "v_mfma_f32_16x16x32_bf16 v[68:71], a[172:175], v[180:183], v[68:71]\n"
    "v_exp_f32_e32 v227, v227\n"
    "v_mfma_f32_16x16x32_bf16 a[252:255], a[172:175], v[172:175], a[252:255]\n"
    "v_cvt_pk_bf16_f32 v224, v224, v225\n"
    "v_mfma_f32_16x16x32_bf16 a[252:255], a[168:171], v[168:171], a[252:255]\n"
    "v_exp_f32_e32 v216, v216\n"
    "v_mfma_f32_16x16x32_bf16 v[4:7], a[168:171], v[160:163], v[4:7]\n"
    "ds_read_b64_tr_b16 a[168:169], v245 offset:4096\n"
    "v_mfma_f32_16x16x32_bf16 v[4:7], a[172:175], v[164:167], v[4:7]\n"
    "s_waitcnt lgkmcnt(9)\n"
    "ds_read_b64_tr_b16 a[170:171], v245 offset:6144\n"
    "v_mfma_f32_16x16x32_bf16 v[8:11], a[176:179], v[160:163], v[8:11]\n"
    "ds_read_b64_tr_b16 a[172:173], v245 offset:12288\n"
    "v_mfma_f32_16x16x32_bf16 v[8:11], a[180:183], v[164:167], v[8:11]\n"
    "ds_read_b64_tr_b16 a[174:175], v245 offset:14336\n"
    "v_mfma_f32_16x16x32_bf16 v[32:35], a[180:183], v[172:175], v[32:35]\n"
    "v_exp_f32_e32 v217, v217\n"
    "v_mfma_f32_16x16x32_bf16 v[32:35], a[176:179], v[168:171], v[32:35]\n"
    "v_cvt_pk_bf16_f32 v225, v226, v227\n"
    "v_mfma_f32_16x16x32_bf16 v[80:83], a[176:179], v[176:179], v[80:83]\n"
    "buffer_load_dwordx4 a[236:239], v158, s[12:15], s61 offen offset:64\n"
    "v_mfma_f32_16x16x32_bf16 v[80:83], a[180:183], v[180:183], v[80:83]\n"
    "s_add_u32 s61, s47, 16384\n"
    "v_exp_f32_e32 v218, v218\n"
    "v_mfma_f32_16x16x32_bf16 v[84:87], a[180:183], v[188:191], v[84:87]\n"
    "v_exp_f32_e32 v219, v219\n"
    "v_mfma_f32_16x16x32_bf16 v[84:87], a[176:179], v[184:187], v[84:87]\n"
    "v_cvt_pk_bf16_f32 v216, v216, v217\n"
    "v_mfma_f32_16x16x32_bf16 v[88:91], a[176:179], v[192:195], v[88:91]\n"
    "ds_read_b64_tr_b16 a[176:177], v246 offset:4096\n"
    "v_mfma_f32_16x16x32_bf16 v[88:91], a[180:183], v[196:199], v[88:91]\n"
    "s_waitcnt lgkmcnt(9)\n"
    "ds_read_b64_tr_b16 a[178:179], v246 offset:6144\n"
    "v_mfma_f32_16x16x32_bf16 v[100:103], a[184:187], v[192:195], v[100:103]\n"
    "ds_read_b64_tr_b16 a[180:181], v246 offset:12288\n"
    "v_mfma_f32_16x16x32_bf16 v[100:103], a[188:191], v[196:199], v[100:103]\n"
    "ds_read_b64_tr_b16 a[182:183], v246 offset:14336\n"
    "v_mfma_f32_16x16x32_bf16 v[96:99], a[188:191], v[188:191], v[96:99]\n"
    "v_cvt_pk_bf16_f32 v217, v218, v219\n"
    "v_mfma_f32_16x16x32_bf16 v[96:99], a[184:187], v[184:187], v[96:99]\n"
    "v_exp_f32_e32 v236, v236\n"
    "v_mfma_f32_16x16x32_bf16 v[92:95], a[184:187], v[176:179], v[92:95]\n"
    "v_exp_f32_e32 v237, v237\n"
    "v_mfma_f32_16x16x32_bf16 v[92:95], a[188:191], v[180:183], v[92:95]\n"
    "v_exp_f32_e32 v238, v238\n"
    "v_mfma_f32_16x16x32_bf16 v[36:39], a[188:191], v[172:175], v[36:39]\n"
    "buffer_load_dwordx4 a[240:243], v158, s[12:15], s61 offen offset:0\n"
    "v_mfma_f32_16x16x32_bf16 v[36:39], a[184:187], v[168:171], v[36:39]\n"
    "v_exp_f32_e32 v239, v239\n"
    "v_mfma_f32_16x16x32_bf16 v[12:15], a[184:187], v[160:163], v[12:15]\n"
    "ds_read_b64_tr_b16 a[184:185], v247 offset:4096\n"
    "v_mfma_f32_16x16x32_bf16 v[12:15], a[188:191], v[164:167], v[12:15]\n"
    "s_waitcnt lgkmcnt(9)\n"
    "ds_read_b64_tr_b16 a[186:187], v247 offset:6144\n"
    "v_mfma_f32_16x16x32_bf16 v[16:19], a[160:163], v[160:163], v[16:19]\n"
    "ds_read_b64_tr_b16 a[188:189], v247 offset:12288\n"
    "v_mfma_f32_16x16x32_bf16 v[16:19], a[164:167], v[164:167], v[16:19]\n"
    "ds_read_b64_tr_b16 a[190:191], v247 offset:14336\n"
    "v_mfma_f32_16x16x32_bf16 v[40:43], a[164:167], v[172:175], v[40:43]\n"
    "v_cvt_pk_bf16_f32 v234, v236, v237\n"
    "v_mfma_f32_16x16x32_bf16 v[40:43], a[160:163], v[168:171], v[40:43]\n"
    "v_exp_f32_e32 v228, v228\n"
    "v_mfma_f32_16x16x32_bf16 v[104:107], a[160:163], v[176:179], v[104:107]\n"
    "ds_write_addtid_b32 v232 offset:16384\n"
    "v_mfma_f32_16x16x32_bf16 v[104:107], a[164:167], v[180:183], v[104:107]\n"
    "ds_write_addtid_b32 v233 offset:16640\n"
    "v_mfma_f32_16x16x32_bf16 v[108:111], a[164:167], v[188:191], v[108:111]\n"
    "v_exp_f32_e32 v229, v229\n"
    "v_mfma_f32_16x16x32_bf16 v[108:111], a[160:163], v[184:187], v[108:111]\n"
    "ds_write_addtid_b32 v224 offset:12288\n"
    "v_mfma_f32_16x16x32_bf16 v[112:115], a[160:163], v[192:195], v[112:115]\n"
    "ds_read_b128 a[160:163], v243 offset:36864\n"
    "buffer_load_dwordx4 a[244:247], v158, s[12:15], s61 offen offset:64\n"
    "v_mfma_f32_16x16x32_bf16 v[112:115], a[164:167], v[196:199], v[112:115]\n"
    "s_waitcnt lgkmcnt(12)\n"
    "ds_read_b128 a[164:167], v240 offset:32768\n"
    "v_mfma_f32_16x16x32_bf16 v[124:127], a[168:171], v[192:195], v[124:127]\n"
    "v_cvt_pk_bf16_f32 v235, v238, v239\n"
    "v_mfma_f32_16x16x32_bf16 v[124:127], a[172:175], v[196:199], v[124:127]\n"
    "ds_write_addtid_b32 v225 offset:12544\n"
    "v_mfma_f32_16x16x32_bf16 v[120:123], a[172:175], v[188:191], v[120:123]\n"
    "v_exp_f32_e32 v230, v230\n"
    "v_mfma_f32_16x16x32_bf16 v[120:123], a[168:171], v[184:187], v[120:123]\n"
    "ds_write_addtid_b32 v216 offset:8192\n"
    "v_mfma_f32_16x16x32_bf16 v[116:119], a[168:171], v[176:179], v[116:119]\n"
    "ds_write_addtid_b32 v217 offset:8448\n"
    "v_mfma_f32_16x16x32_bf16 v[116:119], a[172:175], v[180:183], v[116:119]\n"
    "v_exp_f32_e32 v231, v231\n"
    "v_mfma_f32_16x16x32_bf16 v[44:47], a[172:175], v[172:175], v[44:47]\n"
    "ds_write_addtid_b32 v234 offset:16896\n"
    "v_mfma_f32_16x16x32_bf16 v[44:47], a[168:171], v[168:171], v[44:47]\n"
    "v_cvt_pk_bf16_f32 v226, v228, v229\n"
    "v_mfma_f32_16x16x32_bf16 v[20:23], a[168:171], v[160:163], v[20:23]\n"
    "ds_read_b128 a[168:171], v241 offset:32768\n"
    "v_mfma_f32_16x16x32_bf16 v[20:23], a[172:175], v[164:167], v[20:23]\n"
    "s_waitcnt lgkmcnt(14)\n"
    "ds_read_b128 a[172:175], v240 offset:36864\n"
    "v_mfma_f32_16x16x32_bf16 v[24:27], a[176:179], v[160:163], v[24:27]\n"
    "v_mfma_f32_16x16x32_bf16 v[24:27], a[180:183], v[164:167], v[24:27]\n"
    "v_exp_f32_e32 v220, v220\n"
    "v_mfma_f32_16x16x32_bf16 v[48:51], a[180:183], v[172:175], v[48:51]\n"
    "v_mfma_f32_16x16x32_bf16 v[48:51], a[176:179], v[168:171], v[48:51]\n"
    "ds_write_addtid_b32 v235 offset:17152\n"
    "v_mfma_f32_16x16x32_bf16 v[128:131], a[176:179], v[176:179], v[128:131]\n"
    "v_exp_f32_e32 v221, v221\n"
    "v_mfma_f32_16x16x32_bf16 v[128:131], a[180:183], v[180:183], v[128:131]\n"
    "v_mfma_f32_16x16x32_bf16 v[132:135], a[180:183], v[188:191], v[132:135]\n"
    "ds_write_addtid_b32 v226 offset:12800\n"
    "v_mfma_f32_16x16x32_bf16 v[132:135], a[176:179], v[184:187], v[132:135]\n"
    "v_cvt_pk_bf16_f32 v227, v230, v231\n"
    "v_mfma_f32_16x16x32_bf16 v[136:139], a[176:179], v[192:195], v[136:139]\n"
    "ds_read_b128 a[176:179], v241 offset:36864\n"
    "v_mfma_f32_16x16x32_bf16 v[136:139], a[180:183], v[196:199], v[136:139]\n"
    "s_waitcnt lgkmcnt(14)\n"
    "ds_read_b128 a[180:183], v242 offset:32768\n"
    "v_mfma_f32_16x16x32_bf16 v[148:151], a[184:187], v[192:195], v[148:151]\n"
    "v_exp_f32_e32 v222, v222\n"
    "v_mfma_f32_16x16x32_bf16 v[148:151], a[188:191], v[196:199], v[148:151]\n"
    "v_mfma_f32_16x16x32_bf16 v[144:147], a[188:191], v[188:191], v[144:147]\n"
    "ds_write_addtid_b32 v227 offset:13056\n"
    "v_mfma_f32_16x16x32_bf16 v[144:147], a[184:187], v[184:187], v[144:147]\n"
    "v_exp_f32_e32 v223, v223\n"
    "v_mfma_f32_16x16x32_bf16 v[140:143], a[184:187], v[176:179], v[140:143]\n"
    "v_mfma_f32_16x16x32_bf16 v[140:143], a[188:191], v[180:183], v[140:143]\n"
    "v_mfma_f32_16x16x32_bf16 v[52:55], a[188:191], v[172:175], v[52:55]\n"
    "v_cvt_pk_bf16_f32 v218, v220, v221\n"
    "v_mfma_f32_16x16x32_bf16 v[52:55], a[184:187], v[168:171], v[52:55]\n"
    "v_cvt_pk_bf16_f32 v219, v222, v223\n"
    "v_mfma_f32_16x16x32_bf16 v[28:31], a[184:187], v[160:163], v[28:31]\n"
    "ds_read_b128 a[184:187], v243 offset:32768\n"
    "v_mfma_f32_16x16x32_bf16 v[28:31], a[188:191], v[164:167], v[28:31]\n"
    "s_waitcnt lgkmcnt(12)\n"
    "ds_read_b128 a[188:191], v242 offset:36864\n"
    "v_mfma_f32_16x16x32_bf16 v[176:179], a[160:163], a[92:95], 0\n"
    "ds_write_addtid_b32 v218 offset:8704\n"
    "v_mfma_f32_16x16x32_bf16 v[176:179], a[164:167], a[64:67], v[176:179]\n"
    "v_mfma_f32_16x16x32_bf16 v[184:187], a[164:167], a[96:99], 0\n"
    "v_mfma_f32_16x16x32_bf16 v[184:187], a[160:163], a[124:127], v[184:187]\n"
    "ds_write_addtid_b32 v219 offset:8960\n"
    "v_mfma_f32_16x16x32_bf16 v[192:195], a[160:163], a[156:159], 0\n"
    "ds_read_b128 a[160:163], v243 offset:38912\n"
    "v_mfma_f32_16x16x32_bf16 v[192:195], a[164:167], a[128:131], v[192:195]\n"
    "s_waitcnt lgkmcnt(10)\n"
    "ds_read_b128 a[164:167], v240 offset:34816\n"
    "v_mfma_f32_16x16x32_bf16 v[192:195], a[168:171], a[132:135], v[192:195]\n"
    "v_mfma_f32_16x16x32_bf16 v[192:195], a[172:175], a[136:139], v[192:195]\n"
    "s_waitcnt lgkmcnt(2)\n"
    "v_mfma_f32_16x16x32_bf16 v[184:187], a[172:175], a[104:107], v[184:187]\n"
    "s_barrier\n"
    "v_mfma_f32_16x16x32_bf16 v[184:187], a[168:171], a[100:103], v[184:187]\n"
    "s_add_u32 m0, s43, 0\n"
    "v_mfma_f32_16x16x32_bf16 v[176:179], a[168:171], a[68:71], v[176:179]\n"
    "ds_read_b128 a[168:171], v241 offset:34816\n"
    "buffer_load_dwordx4 v248, s[8:11], s40 offen offset:0 sc1 lds\n"
    "v_mfma_f32_16x16x32_bf16 v[176:179], a[172:175], a[72:75], v[176:179]\n"
    "ds_read_b128 a[172:175], v240 offset:38912\n"
    "v_mfma_f32_16x16x32_bf16 v[176:179], a[176:179], a[76:79], v[176:179]\n"
    "ds_read2st64_b32 v[220:221], v250 offset0:32 offset1:33\n"
    "v_mfma_f32_16x16x32_bf16 v[176:179], a[180:183], a[80:83], v[176:179]\n"
    "v_mfma_f32_16x16x32_bf16 v[184:187], a[180:183], a[112:115], v[184:187]\n"
    "buffer_load_dwordx4 v248, s[8:11], s40 offen offset:1024 sc1 lds\n"
    "v_mfma_f32_16x16x32_bf16 v[184:187], a[176:179], a[108:111], v[184:187]\n"
    "v_mfma_f32_16x16x32_bf16 v[192:195], a[176:179], a[140:143], v[192:195]\n"
    "ds_read_b128 a[176:179], v241 offset:38912\n"
    "v_mfma_f32_16x16x32_bf16 v[192:195], a[180:183], a[144:147], v[192:195]\n"
    "ds_read_b128 a[180:183], v242 offset:34816\n"
    "v_mfma_f32_16x16x32_bf16 v[192:195], a[184:187], a[148:151], v[192:195]\n"
    "buffer_load_dwordx4 v248, s[8:11], s40 offen offset:2048 sc1 lds\n"
    "v_mfma_f32_16x16x32_bf16 v[192:195], a[188:191], a[152:155], v[192:195]\n"
    "ds_read2st64_b32 v[222:223], v250 offset0:34 offset1:35\n"
    "v_mfma_f32_16x16x32_bf16 v[184:187], a[188:191], a[120:123], v[184:187]\n"
    "v_mfma_f32_16x16x32_bf16 v[184:187], a[184:187], a[116:119], v[184:187]\n"
    "v_exp_f32_e32 v192, v192\n"
    "v_mfma_f32_16x16x32_bf16 v[176:179], a[184:187], a[84:87], v[176:179]\n"
    "ds_read_b128 a[184:187], v243 offset:34816\n"
    "buffer_load_dwordx4 v248, s[8:11], s40 offen offset:3072 sc1 lds\n"
    "v_mfma_f32_16x16x32_bf16 v[176:179], a[188:191], a[88:91], v[176:179]\n"
    "s_waitcnt lgkmcnt(7)\n"
    "s_add_u32 s40, s40, 32768\n"
    "ds_read_b128 a[188:191], v242 offset:38912\n"
    "v_mfma_f32_16x16x32_bf16 v[180:183], a[160:163], a[92:95], 0\n"
    "s_add_u32 m0, s43, 4096\n"
    "ds_read2st64_b32 v[228:229], v250 offset0:48 offset1:49\n"
    "v_mfma_f32_16x16x32_bf16 v[180:183], a[164:167], a[64:67], v[180:183]\n"
    "v_exp_f32_e32 v193, v193\n"
    "v_mfma_f32_16x16x32_bf16 v[188:191], a[164:167], a[96:99], 0\n"
    "buffer_load_dwordx4 v248, s[8:11], s41 offen offset:0 sc1 lds\n"
    "v_exp_f32_e32 v194, v194\n"
    "v_mfma_f32_16x16x32_bf16 v[188:191], a[160:163], a[124:127], v[188:191]\n"
    "ds_read2st64_b32 v[230:231], v250 offset0:50 offset1:51\n"
    "v_mfma_f32_16x16x32_bf16 v[196:199], a[160:163], a[156:159], 0\n"
    "ds_read_b64_tr_b16 a[160:161], v244 offset:16384\n"
    "v_mfma_f32_16x16x32_bf16 v[196:199], a[164:167], a[128:131], v[196:199]\n"
    "s_waitcnt lgkmcnt(9)\n"
    "ds_read_b64_tr_b16 a[162:163], v244 offset:18432\n"
    "v_mfma_f32_16x16x32_bf16 v[196:199], a[168:171], a[132:135], v[196:199]\n"
    "ds_read_b64_tr_b16 a[164:165], v244 offset:24576\n"
    "buffer_load_dwordx4 v248, s[8:11], s41 offen offset:1024 sc1 lds\n"
    "v_mfma_f32_16x16x32_bf16 v[196:199], a[172:175], a[136:139], v[196:199]\n"
    "ds_read_b64_tr_b16 a[166:167], v244 offset:26624\n"
    "v_mfma_f32_16x16x32_bf16 v[188:191], a[172:175], a[104:107], v[188:191]\n"
    "v_exp_f32_e32 v195, v195\n"
    "v_mfma_f32_16x16x32_bf16 v[188:191], a[168:171], a[100:103], v[188:191]\n"
    "ds_read2st64_b32 v[236:237], v250 offset0:64 offset1:65\n"
    "v_cvt_pk_bf16_f32 v192, v192, v193\n"
    "v_mfma_f32_16x16x32_bf16 v[180:183], a[168:171], a[68:71], v[180:183]\n"
    "ds_read_b64_tr_b16 a[168:169], v245 offset:16384\n"
    "buffer_load_dwordx4 v248, s[8:11], s41 offen offset:2048 sc1 lds\n"
    "v_mfma_f32_16x16x32_bf16 v[180:183], a[172:175], a[72:75], v[180:183]\n"
    "s_waitcnt lgkmcnt(11)\n"
    "ds_read_b64_tr_b16 a[170:171], v245 offset:18432\n"
    "v_mfma_f32_16x16x32_bf16 v[180:183], a[176:179], a[76:79], v[180:183]\n"
    "ds_read_b64_tr_b16 a[172:173], v245 offset:24576\n"
    "v_mfma_f32_16x16x32_bf16 v[180:183], a[180:183], a[80:83], v[180:183]\n"
    "ds_read_b64_tr_b16 a[174:175], v245 offset:26624\n"
    "v_mfma_f32_16x16x32_bf16 v[188:191], a[180:183], a[112:115], v[188:191]\n"
    "buffer_load_dwordx4 v248, s[8:11], s41 offen offset:3072 sc1 lds\n"
    "v_mfma_f32_16x16x32_bf16 v[188:191], a[176:179], a[108:111], v[188:191]\n"
    "s_add_u32 s41, s41, 32768\n"
    "ds_read2st64_b32 v[238:239], v250 offset0:66 offset1:67\n"
    "v_mfma_f32_16x16x32_bf16 v[196:199], a[176:179], a[140:143], v[196:199]\n"
    "s_mov_b32 m0, s49\n"
    "ds_read_b64_tr_b16 a[176:177], v246 offset:16384\n"
    "v_mfma_f32_16x16x32_bf16 v[196:199], a[180:183], a[144:147], v[196:199]\n"
    "s_waitcnt lgkmcnt(13)\n"
    "ds_read_b64_tr_b16 a[178:179], v246 offset:18432\n"
    "v_mfma_f32_16x16x32_bf16 v[196:199], a[184:187], a[148:151], v[196:199]\n"
    "ds_read_b64_tr_b16 a[180:181], v246 offset:24576\n"
    "v_mfma_f32_16x16x32_bf16 v[196:199], a[188:191], a[152:155], v[196:199]\n"
    "ds_read_b64_tr_b16 a[182:183], v246 offset:26624\n"
    "v_mfma_f32_16x16x32_bf16 v[188:191], a[188:191], a[120:123], v[188:191]\n"
    "v_exp_f32_e32 v184, v184\n"
    "v_mfma_f32_16x16x32_bf16 v[188:191], a[184:187], a[116:119], v[188:191]\n"
    "v_exp_f32_e32 v185, v185\n"
    "v_mfma_f32_16x16x32_bf16 v[180:183], a[184:187], a[84:87], v[180:183]\n"
    "ds_read_b64_tr_b16 a[184:185], v247 offset:16384\n"
    "v_cvt_pk_bf16_f32 v193, v194, v195\n"
    "v_mfma_f32_16x16x32_bf16 v[180:183], a[188:191], a[88:91], v[180:183]\n"
    "s_waitcnt lgkmcnt(5)\n"
    "ds_read_b64_tr_b16 a[186:187], v247 offset:18432\n"
    "v_mfma_f32_16x16x32_bf16 v[56:59], a[160:163], v[216:219], v[56:59]\n"
    "ds_read_b64_tr_b16 a[188:189], v247 offset:24576\n"
    "v_mfma_f32_16x16x32_bf16 v[56:59], a[164:167], v[220:223], v[56:59]\n"
    "ds_read_b64_tr_b16 a[190:191], v247 offset:26624\n"
    "v_mfma_f32_16x16x32_bf16 v[60:63], a[164:167], v[228:231], v[60:63]\n"
    "v_exp_f32_e32 v186, v186\n"
    "v_mfma_f32_16x16x32_bf16 v[60:63], a[160:163], v[224:227], v[60:63]\n"
    "v_exp_f32_e32 v187, v187\n"
    "v_mfma_f32_16x16x32_bf16 v[64:67], a[160:163], v[232:235], v[64:67]\n"
    "ds_read_b64_tr_b16 a[160:161], v244 offset:20480\n"
    "v_cvt_pk_bf16_f32 v184, v184, v185\n"
    "v_mfma_f32_16x16x32_bf16 v[64:67], a[164:167], v[236:239], v[64:67]\n"
    "ds_read_b64_tr_b16 a[162:163], v244 offset:22528\n"
    "v_mfma_f32_16x16x32_bf16 v[76:79], a[168:171], v[232:235], v[76:79]\n"
    "ds_read_b64_tr_b16 a[164:165], v244 offset:28672\n"
    "v_mfma_f32_16x16x32_bf16 v[76:79], a[172:175], v[236:239], v[76:79]\n"
    "ds_read_b64_tr_b16 a[166:167], v244 offset:30720\n"
    "v_mfma_f32_16x16x32_bf16 v[72:75], a[172:175], v[228:231], v[72:75]\n"
    "v_exp_f32_e32 v176, v176\n"
    "v_mfma_f32_16x16x32_bf16 v[72:75], a[168:171], v[224:227], v[72:75]\n"
    "v_exp_f32_e32 v177, v177\n"
    "v_mfma_f32_16x16x32_bf16 v[68:71], a[168:171], v[216:219], v[68:71]\n"
    "ds_read_b64_tr_b16 a[168:169], v245 offset:20480\n"
    "v_cvt_pk_bf16_f32 v185, v186, v187\n"
    "v_mfma_f32_16x16x32_bf16 v[68:71], a[172:175], v[220:223], v[68:71]\n"
    "s_waitcnt lgkmcnt(9)\n"
    "ds_read_b64_tr_b16 a[170:171], v245 offset:22528\n"
    "v_mfma_f32_16x16x32_bf16 v[80:83], a[176:179], v[216:219], v[80:83]\n"
    "ds_read_b64_tr_b16 a[172:173], v245 offset:28672\n"
    "v_mfma_f32_16x16x32_bf16 v[80:83], a[180:183], v[220:223], v[80:83]\n"
    "ds_read_b64_tr_b16 a[174:175], v245 offset:30720\n"
    "v_mfma_f32_16x16x32_bf16 v[84:87], a[180:183], v[228:231], v[84:87]\n"
    "v_exp_f32_e32 v178, v178\n"
    "v_mfma_f32_16x16x32_bf16 v[84:87], a[176:179], v[224:227], v[84:87]\n"
    "s_waitcnt vmcnt(14)\n"
    "v_exp_f32_e32 v179, v179\n"
    "v_mfma_f32_16x16x32_bf16 v[88:91], a[176:179], v[232:235], v[88:91]\n"
    "s_barrier\n"
    "v_mfma_f32_16x16x32_bf16 v[88:91], a[180:183], v[236:239], v[88:91]\n"
    "s_waitcnt lgkmcnt(8)\n"
    "ds_read_b64_tr_b16 a[176:177], v246 offset:20480\n"
    "v_cvt_pk_bf16_f32 v176, v176, v177\n"
    "v_mfma_f32_16x16x32_bf16 v[100:103], a[184:187], v[232:235], v[100:103]\n"
    "ds_read_b64_tr_b16 a[178:179], v246 offset:22528\n"
    "v_cvt_pk_bf16_f32 v177, v178, v179\n"
    "v_mfma_f32_16x16x32_bf16 v[100:103], a[188:191], v[236:239], v[100:103]\n"
    "ds_read_b64_tr_b16 a[180:181], v246 offset:28672\n"
    "v_mfma_f32_16x16x32_bf16 v[96:99], a[188:191], v[228:231], v[96:99]\n"
    "ds_read_b64_tr_b16 a[182:183], v246 offset:30720\n"
    "v_mfma_f32_16x16x32_bf16 v[96:99], a[184:187], v[224:227], v[96:99]\n"
    "v_exp_f32_e32 v196, v196\n"
    "v_mfma_f32_16x16x32_bf16 v[92:95], a[184:187], v[216:219], v[92:95]\n"
    "ds_read_b64_tr_b16 a[184:185], v247 offset:20480\n"
    "ds_write_addtid_b32 v192 offset:16384\n"
    "v_mfma_f32_16x16x32_bf16 v[92:95], a[188:191], v[220:223], v[92:95]\n"
    "s_waitcnt lgkmcnt(10)\n"
    "ds_read_b64_tr_b16 a[186:187], v247 offset:22528\n"
    "v_mfma_f32_16x16x32_bf16 v[104:107], a[160:163], v[216:219], v[104:107]\n"
    "ds_read_b64_tr_b16 a[188:189], v247 offset:28672\n"
    "ds_write_addtid_b32 v193 offset:16640\n"
    "v_mfma_f32_16x16x32_bf16 v[104:107], a[164:167], v[220:223], v[104:107]\n"
    "ds_read_b64_tr_b16 a[190:191], v247 offset:30720\n"
    "v_mfma_f32_16x16x32_bf16 v[108:111], a[164:167], v[228:231], v[108:111]\n"
    "v_exp_f32_e32 v197, v197\n"
    "v_mfma_f32_16x16x32_bf16 v[108:111], a[160:163], v[224:227], v[108:111]\n"
    "v_exp_f32_e32 v198, v198\n"
    "v_mfma_f32_16x16x32_bf16 v[112:115], a[160:163], v[232:235], v[112:115]\n"
    "ds_read_b128 a[160:163], v243 offset:53248\n"
    "ds_write_addtid_b32 v184 offset:12288\n"
    "v_mfma_f32_16x16x32_bf16 v[112:115], a[164:167], v[236:239], v[112:115]\n"
    "s_waitcnt lgkmcnt(12)\n"
    "ds_read_b128 a[164:167], v240 offset:49152\n"
    "ds_write_addtid_b32 v185 offset:12544\n"
    "v_mfma_f32_16x16x32_bf16 v[124:127], a[168:171], v[232:235], v[124:127]\n"
    "v_exp_f32_e32 v199, v199\n"
    "v_mfma_f32_16x16x32_bf16 v[124:127], a[172:175], v[236:239], v[124:127]\n"
    "ds_write_addtid_b32 v176 offset:8192\n"
    "v_cvt_pk_bf16_f32 v194, v196, v197\n"
    "v_mfma_f32_16x16x32_bf16 v[120:123], a[172:175], v[228:231], v[120:123]\n"
    "v_exp_f32_e32 v188, v188\n"
    "v_mfma_f32_16x16x32_bf16 v[120:123], a[168:171], v[224:227], v[120:123]\n"
    "v_exp_f32_e32 v189, v189\n"
    "v_mfma_f32_16x16x32_bf16 v[116:119], a[168:171], v[216:219], v[116:119]\n"
    "ds_read_b128 a[168:171], v241 offset:49152\n"
    "v_cvt_pk_bf16_f32 v195, v198, v199\n"
    "v_mfma_f32_16x16x32_bf16 v[116:119], a[172:175], v[220:223], v[116:119]\n"
    "s_waitcnt lgkmcnt(12)\n"
    "ds_read_b128 a[172:175], v240 offset:53248\n"
    "ds_write_addtid_b32 v177 offset:8448\n"
    "v_mfma_f32_16x16x32_bf16 v[128:131], a[176:179], v[216:219], v[128:131]\n"
    "v_exp_f32_e32 v190, v190\n"
    "v_mfma_f32_16x16x32_bf16 v[128:131], a[180:183], v[220:223], v[128:131]\n"
    "v_exp_f32_e32 v191, v191\n"
    "v_mfma_f32_16x16x32_bf16 v[132:135], a[180:183], v[228:231], v[132:135]\n"
    "ds_write_addtid_b32 v194 offset:16896\n"
    "v_cvt_pk_bf16_f32 v186, v188, v189\n"
    "v_mfma_f32_16x16x32_bf16 v[132:135], a[176:179], v[224:227], v[132:135]\n"
    "v_exp_f32_e32 v180, v180\n"
    "v_mfma_f32_16x16x32_bf16 v[136:139], a[176:179], v[232:235], v[136:139]\n"
    "ds_read_b128 a[176:179], v241 offset:53248\n"
    "v_mfma_f32_16x16x32_bf16 v[136:139], a[180:183], v[236:239], v[136:139]\n"
    "s_waitcnt lgkmcnt(10)\n"
    "ds_read_b128 a[180:183], v242 offset:49152\n"
    "v_mfma_f32_16x16x32_bf16 v[148:151], a[184:187], v[232:235], v[148:151]\n"
    "v_exp_f32_e32 v181, v181\n"
    "v_mfma_f32_16x16x32_bf16 v[148:151], a[188:191], v[236:239], v[148:151]\n"
    "ds_write_addtid_b32 v195 offset:17152\n"
    "v_cvt_pk_bf16_f32 v187, v190, v191\n"
    "v_mfma_f32_16x16x32_bf16 v[144:147], a[188:191], v[228:231], v[144:147]\n"
    "v_exp_f32_e32 v182, v182\n"
    "v_mfma_f32_16x16x32_bf16 v[144:147], a[184:187], v[224:227], v[144:147]\n"
    "v_exp_f32_e32 v183, v183\n"
    "v_mfma_f32_16x16x32_bf16 v[140:143], a[184:187], v[216:219], v[140:143]\n"
    "ds_read_b128 a[184:187], v243 offset:49152\n"
    "v_cvt_pk_bf16_f32 v178, v180, v181\n"
    "v_mfma_f32_16x16x32_bf16 v[140:143], a[188:191], v[220:223], v[140:143]\n"
    "s_waitcnt lgkmcnt(10)\n"
    "ds_read_b128 a[188:191], v242 offset:53248\n"
    "v_cvt_pk_bf16_f32 v179, v182, v183\n"
    "v_mfma_f32_16x16x32_bf16 v[216:219], a[160:163], a[92:95], 0\n"
    "ds_write_addtid_b32 v186 offset:12800\n"
    "v_mfma_f32_16x16x32_bf16 v[216:219], a[164:167], a[64:67], v[216:219]\n"
    "ds_write_addtid_b32 v187 offset:13056\n"
    "v_mfma_f32_16x16x32_bf16 v[224:227], a[164:167], a[96:99], 0\n"
    "ds_write_addtid_b32 v178 offset:8704\n"
    "v_mfma_f32_16x16x32_bf16 v[224:227], a[160:163], a[124:127], v[224:227]\n"
    "ds_write_addtid_b32 v179 offset:8960\n"
    "v_mfma_f32_16x16x32_bf16 v[232:235], a[160:163], a[156:159], 0\n"
    "ds_read_b128 a[160:163], v243 offset:55296\n"
    "v_mfma_f32_16x16x32_bf16 v[232:235], a[164:167], a[128:131], v[232:235]\n"
    "s_waitcnt lgkmcnt(12)\n"
    "ds_read_b128 a[164:167], v240 offset:51200\n"
    "v_mfma_f32_16x16x32_bf16 v[232:235], a[168:171], a[132:135], v[232:235]\n"
    "v_mfma_f32_16x16x32_bf16 v[232:235], a[172:175], a[136:139], v[232:235]\n"
    "s_waitcnt lgkmcnt(2)\n"
    "v_mfma_f32_16x16x32_bf16 v[224:227], a[172:175], a[104:107], v[224:227]\n"
    "s_barrier\n"
    "v_mfma_f32_16x16x32_bf16 v[224:227], a[168:171], a[100:103], v[224:227]\n"
    "s_add_u32 m0, s43, 16384\n"
    "v_mfma_f32_16x16x32_bf16 v[216:219], a[168:171], a[68:71], v[216:219]\n"
    "ds_read_b128 a[168:171], v241 offset:51200\n"
    "buffer_load_dwordx4 v248, s[8:11], s40 offen offset:0 sc1 lds\n"
    "v_mfma_f32_16x16x32_bf16 v[216:219], a[172:175], a[72:75], v[216:219]\n"
    "ds_read_b128 a[172:175], v240 offset:55296\n"
    "v_mfma_f32_16x16x32_bf16 v[216:219], a[176:179], a[76:79], v[216:219]\n"
    "ds_read2st64_b32 v[180:181], v250 offset0:32 offset1:33\n"
    "v_mfma_f32_16x16x32_bf16 v[216:219], a[180:183], a[80:83], v[216:219]\n"
    "v_mfma_f32_16x16x32_bf16 v[224:227], a[180:183], a[112:115], v[224:227]\n"
    "buffer_load_dwordx4 v248, s[8:11], s40 offen offset:1024 sc1 lds\n"
    "v_mfma_f32_16x16x32_bf16 v[224:227], a[176:179], a[108:111], v[224:227]\n"
    "v_mfma_f32_16x16x32_bf16 v[232:235], a[176:179], a[140:143], v[232:235]\n"
    "ds_read_b128 a[176:179], v241 offset:55296\n"
    "v_mfma_f32_16x16x32_bf16 v[232:235], a[180:183], a[144:147], v[232:235]\n"
    "ds_read_b128 a[180:183], v242 offset:51200\n"
    "v_mfma_f32_16x16x32_bf16 v[232:235], a[184:187], a[148:151], v[232:235]\n"
    "buffer_load_dwordx4 v248, s[8:11], s40 offen offset:2048 sc1 lds\n"
    "v_mfma_f32_16x16x32_bf16 v[232:235], a[188:191], a[152:155], v[232:235]\n"
    "ds_read2st64_b32 v[182:183], v250 offset0:34 offset1:35\n"
    "v_mfma_f32_16x16x32_bf16 v[224:227], a[188:191], a[120:123], v[224:227]\n"
    "v_mfma_f32_16x16x32_bf16 v[224:227], a[184:187], a[116:119], v[224:227]\n"
    "v_exp_f32_e32 v232, v232\n"
    "v_mfma_f32_16x16x32_bf16 v[216:219], a[184:187], a[84:87], v[216:219]\n"
    "ds_read_b128 a[184:187], v243 offset:51200\n"
    "buffer_load_dwordx4 v248, s[8:11], s40 offen offset:3072 sc1 lds\n"
    "v_mfma_f32_16x16x32_bf16 v[216:219], a[188:191], a[88:91], v[216:219]\n"
    "s_waitcnt lgkmcnt(7)\n"
    "s_add_u32 s40, s40, -131072\n"
    "ds_read_b128 a[188:191], v242 offset:55296\n"
    "v_mfma_f32_16x16x32_bf16 v[220:223], a[160:163], a[92:95], 0\n"
    "s_add_u32 m0, s43, 20480\n"
    "ds_read2st64_b32 v[188:189], v250 offset0:48 offset1:49\n"
    "v_mfma_f32_16x16x32_bf16 v[220:223], a[164:167], a[64:67], v[220:223]\n"
    "v_exp_f32_e32 v233, v233\n"
    "v_mfma_f32_16x16x32_bf16 v[228:231], a[164:167], a[96:99], 0\n"
    "buffer_load_dwordx4 v248, s[8:11], s41 offen offset:0 sc1 lds\n"
    "v_exp_f32_e32 v234, v234\n"
    "v_mfma_f32_16x16x32_bf16 v[228:231], a[160:163], a[124:127], v[228:231]\n"
    "ds_read2st64_b32 v[190:191], v250 offset0:50 offset1:51\n"
    "v_mfma_f32_16x16x32_bf16 v[236:239], a[160:163], a[156:159], 0\n"
    "ds_read_b64_tr_b16 a[160:161], v244 offset:32768\n"
    "v_mfma_f32_16x16x32_bf16 v[236:239], a[164:167], a[128:131], v[236:239]\n"
    "s_waitcnt lgkmcnt(9)\n"
    "ds_read_b64_tr_b16 a[162:163], v244 offset:34816\n"
    "v_mfma_f32_16x16x32_bf16 v[236:239], a[168:171], a[132:135], v[236:239]\n"
    "ds_read_b64_tr_b16 a[164:165], v244 offset:40960\n"
    "buffer_load_dwordx4 v248, s[8:11], s41 offen offset:1024 sc1 lds\n"
    "v_mfma_f32_16x16x32_bf16 v[236:239], a[172:175], a[136:139], v[236:239]\n"
    "ds_read_b64_tr_b16 a[166:167], v244 offset:43008\n"
    "v_mfma_f32_16x16x32_bf16 v[228:231], a[172:175], a[104:107], v[228:231]\n"
    "v_exp_f32_e32 v235, v235\n"
    "v_mfma_f32_16x16x32_bf16 v[228:231], a[168:171], a[100:103], v[228:231]\n"
    "ds_read2st64_b32 v[196:197], v250 offset0:64 offset1:65\n"
    "v_cvt_pk_bf16_f32 v232, v232, v233\n"
    "v_mfma_f32_16x16x32_bf16 v[220:223], a[168:171], a[68:71], v[220:223]\n"
    "ds_read_b64_tr_b16 a[168:169], v245 offset:32768\n"
    "buffer_load_dwordx4 v248, s[8:11], s41 offen offset:2048 sc1 lds\n"
    "v_mfma_f32_16x16x32_bf16 v[220:223], a[172:175], a[72:75], v[220:223]\n"
    "s_waitcnt lgkmcnt(11)\n"
    "ds_read_b64_tr_b16 a[170:171], v245 offset:34816\n"
    "v_mfma_f32_16x16x32_bf16 v[220:223], a[176:179], a[76:79], v[220:223]\n"
    "ds_read_b64_tr_b16 a[172:173], v245 offset:40960\n"
    "v_mfma_f32_16x16x32_bf16 v[220:223], a[180:183], a[80:83], v[220:223]\n"
    "ds_read_b64_tr_b16 a[174:175], v245 offset:43008\n"
    "v_mfma_f32_16x16x32_bf16 v[228:231], a[180:183], a[112:115], v[228:231]\n"
    "buffer_load_dwordx4 v248, s[8:11], s41 offen offset:3072 sc1 lds\n"
    "v_mfma_f32_16x16x32_bf16 v[228:231], a[176:179], a[108:111], v[228:231]\n"
    "s_add_u32 s41, s41, -131072\n"
    "ds_read2st64_b32 v[198:199], v250 offset0:66 offset1:67\n"
    "v_mfma_f32_16x16x32_bf16 v[236:239], a[176:179], a[140:143], v[236:239]\n"
    "s_mov_b32 m0, s49\n"
    "ds_read_b64_tr_b16 a[176:177], v246 offset:32768\n"
    "v_mfma_f32_16x16x32_bf16 v[236:239], a[180:183], a[144:147], v[236:239]\n"
    "s_waitcnt lgkmcnt(13)\n"
    "ds_read_b64_tr_b16 a[178:179], v246 offset:34816\n"
    "v_mfma_f32_16x16x32_bf16 v[236:239], a[184:187], a[148:151], v[236:239]\n"
    "ds_read_b64_tr_b16 a[180:181], v246 offset:40960\n"
    "buffer_load_dword v255, v254, s[8:11], s45 offen offset:0 sc1\n"
    "v_mfma_f32_16x16x32_bf16 v[236:239], a[188:191], a[152:155], v[236:239]\n"
    "ds_read_b64_tr_b16 a[182:183], v246 offset:43008\n"
    "v_mfma_f32_16x16x32_bf16 v[228:231], a[188:191], a[120:123], v[228:231]\n"
    "v_exp_f32_e32 v224, v224\n"
    "v_mfma_f32_16x16x32_bf16 v[228:231], a[184:187], a[116:119], v[228:231]\n"
    "v_exp_f32_e32 v225, v225\n"
    "v_mfma_f32_16x16x32_bf16 v[220:223], a[184:187], a[84:87], v[220:223]\n"
    "ds_read_b64_tr_b16 a[184:185], v247 offset:32768\n"
    "v_cvt_pk_bf16_f32 v233, v234, v235\n"
    "v_mfma_f32_16x16x32_bf16 v[220:223], a[188:191], a[88:91], v[220:223]\n"
    "s_waitcnt lgkmcnt(5)\n"
    "ds_read_b64_tr_b16 a[186:187], v247 offset:34816\n"
    "v_mfma_f32_16x16x32_bf16 v[56:59], a[160:163], v[176:179], v[56:59]\n"
    "ds_read_b64_tr_b16 a[188:189], v247 offset:40960\n"
    "v_mfma_f32_16x16x32_bf16 v[56:59], a[164:167], v[180:183], v[56:59]\n"
    "ds_read_b64_tr_b16 a[190:191], v247 offset:43008\n"
    "v_mfma_f32_16x16x32_bf16 v[60:63], a[164:167], v[188:191], v[60:63]\n"
    "v_exp_f32_e32 v226, v226\n"
    "v_mfma_f32_16x16x32_bf16 v[60:63], a[160:163], v[184:187], v[60:63]\n"
    "v_exp_f32_e32 v227, v227\n"
    "v_mfma_f32_16x16x32_bf16 v[64:67], a[160:163], v[192:195], v[64:67]\n"
    "ds_read_b64_tr_b16 a[160:161], v244 offset:36864\n"
    "v_cvt_pk_bf16_f32 v224, v224, v225\n"
    "v_mfma_f32_16x16x32_bf16 v[64:67], a[164:167], v[196:199], v[64:67]\n"
    "ds_read_b64_tr_b16 a[162:163], v244 offset:38912\n"
    "v_mfma_f32_16x16x32_bf16 v[76:79], a[168:171], v[192:195], v[76:79]\n"
    "ds_read_b64_tr_b16 a[164:165], v244 offset:45056\n"
    "v_mfma_f32_16x16x32_bf16 v[76:79], a[172:175], v[196:199], v[76:79]\n"
    "ds_read_b64_tr_b16 a[166:167], v244 offset:47104\n"
    "v_mfma_f32_16x16x32_bf16 v[72:75], a[172:175], v[188:191], v[72:75]\n"
    "v_exp_f32_e32 v216, v216\n"
    "v_mfma_f32_16x16x32_bf16 v[72:75], a[168:171], v[184:187], v[72:75]\n"
    "v_exp_f32_e32 v217, v217\n"
    "v_mfma_f32_16x16x32_bf16 v[68:71], a[168:171], v[176:179], v[68:71]\n"
    "ds_read_b64_tr_b16 a[168:169], v245 offset:36864\n"
    "v_cvt_pk_bf16_f32 v225, v226, v227\n"
    "v_mfma_f32_16x16x32_bf16 v[68:71], a[172:175], v[180:183], v[68:71]\n"
    "s_waitcnt lgkmcnt(9)\n"
    "ds_read_b64_tr_b16 a[170:171], v245 offset:38912\n"
    "v_mfma_f32_16x16x32_bf16 v[80:83], a[176:179], v[176:179], v[80:83]\n"
    "ds_read_b64_tr_b16 a[172:173], v245 offset:45056\n"
    "v_mfma_f32_16x16x32_bf16 v[80:83], a[180:183], v[180:183], v[80:83]\n"
    "ds_read_b64_tr_b16 a[174:175], v245 offset:47104\n"
    "v_mfma_f32_16x16x32_bf16 v[84:87], a[180:183], v[188:191], v[84:87]\n"
    "v_exp_f32_e32 v218, v218\n"
    "v_mfma_f32_16x16x32_bf16 v[84:87], a[176:179], v[184:187], v[84:87]\n"
    "s_waitcnt vmcnt(9)\n"
    "v_exp_f32_e32 v219, v219\n"
    "v_mfma_f32_16x16x32_bf16 v[88:91], a[176:179], v[192:195], v[88:91]\n"
    "s_barrier\n"
    "v_mfma_f32_16x16x32_bf16 v[88:91], a[180:183], v[196:199], v[88:91]\n"
    "s_waitcnt lgkmcnt(8)\n"
    "ds_read_b64_tr_b16 a[176:177], v246 offset:36864\n"
    "v_cvt_pk_bf16_f32 v216, v216, v217\n"
    "v_mfma_f32_16x16x32_bf16 v[100:103], a[184:187], v[192:195], v[100:103]\n"
    "ds_read_b64_tr_b16 a[178:179], v246 offset:38912\n"
    "v_cvt_pk_bf16_f32 v217, v218, v219\n"
    "v_mfma_f32_16x16x32_bf16 v[100:103], a[188:191], v[196:199], v[100:103]\n"
    "ds_read_b64_tr_b16 a[180:181], v246 offset:45056\n"
    "v_mfma_f32_16x16x32_bf16 v[96:99], a[188:191], v[188:191], v[96:99]\n"
    "ds_read_b64_tr_b16 a[182:183], v246 offset:47104\n"
    "v_mfma_f32_16x16x32_bf16 v[96:99], a[184:187], v[184:187], v[96:99]\n"
    "v_exp_f32_e32 v236, v236\n"
    "v_mfma_f32_16x16x32_bf16 v[92:95], a[184:187], v[176:179], v[92:95]\n"
    "ds_read_b64_tr_b16 a[184:185], v247 offset:36864\n"
    "ds_write_addtid_b32 v232 offset:16384\n"
    "v_mfma_f32_16x16x32_bf16 v[92:95], a[188:191], v[180:183], v[92:95]\n"
    "s_waitcnt lgkmcnt(10)\n"
    "ds_read_b64_tr_b16 a[186:187], v247 offset:38912\n"
    "v_mfma_f32_16x16x32_bf16 v[104:107], a[160:163], v[176:179], v[104:107]\n"
    "ds_read_b64_tr_b16 a[188:189], v247 offset:45056\n"
    "ds_write_addtid_b32 v233 offset:16640\n"
    "v_mfma_f32_16x16x32_bf16 v[104:107], a[164:167], v[180:183], v[104:107]\n"
    "ds_read_b64_tr_b16 a[190:191], v247 offset:47104\n"
    "v_mfma_f32_16x16x32_bf16 v[108:111], a[164:167], v[188:191], v[108:111]\n"
    "v_exp_f32_e32 v237, v237\n"
    "v_mfma_f32_16x16x32_bf16 v[108:111], a[160:163], v[184:187], v[108:111]\n"
    "v_exp_f32_e32 v238, v238\n"
    "v_mfma_f32_16x16x32_bf16 v[112:115], a[160:163], v[192:195], v[112:115]\n"
    "ds_read_b128 a[160:163], v243 offset:4096\n"
    "ds_write_addtid_b32 v224 offset:12288\n"
    "v_mfma_f32_16x16x32_bf16 v[112:115], a[164:167], v[196:199], v[112:115]\n"
    "s_waitcnt lgkmcnt(12)\n"
    "ds_read_b128 a[164:167], v240 offset:0\n"
    "ds_write_addtid_b32 v225 offset:12544\n"
    "v_mfma_f32_16x16x32_bf16 v[124:127], a[168:171], v[192:195], v[124:127]\n"
    "v_exp_f32_e32 v239, v239\n"
    "v_mfma_f32_16x16x32_bf16 v[124:127], a[172:175], v[196:199], v[124:127]\n"
    "ds_write_addtid_b32 v216 offset:8192\n"
    "v_cvt_pk_bf16_f32 v234, v236, v237\n"
    "v_mfma_f32_16x16x32_bf16 v[120:123], a[172:175], v[188:191], v[120:123]\n"
    "v_exp_f32_e32 v228, v228\n"
    "v_mfma_f32_16x16x32_bf16 v[120:123], a[168:171], v[184:187], v[120:123]\n"
    "v_exp_f32_e32 v229, v229\n"
    "v_mfma_f32_16x16x32_bf16 v[116:119], a[168:171], v[176:179], v[116:119]\n"
    "ds_read_b128 a[168:171], v241 offset:0\n"
    "v_cvt_pk_bf16_f32 v235, v238, v239\n"
    "v_mfma_f32_16x16x32_bf16 v[116:119], a[172:175], v[180:183], v[116:119]\n"
    "s_waitcnt lgkmcnt(12)\n"
    "ds_read_b128 a[172:175], v240 offset:4096\n"
    "ds_write_addtid_b32 v217 offset:8448\n"
    "v_mfma_f32_16x16x32_bf16 v[128:131], a[176:179], v[176:179], v[128:131]\n"
    "v_exp_f32_e32 v230, v230\n"
    "v_mfma_f32_16x16x32_bf16 v[128:131], a[180:183], v[180:183], v[128:131]\n"
    "v_exp_f32_e32 v231, v231\n"
    "v_mfma_f32_16x16x32_bf16 v[132:135], a[180:183], v[188:191], v[132:135]\n"
    "ds_write_addtid_b32 v234 offset:16896\n"
    "v_cvt_pk_bf16_f32 v226, v228, v229\n"
    "v_mfma_f32_16x16x32_bf16 v[132:135], a[176:179], v[184:187], v[132:135]\n"
    "v_exp_f32_e32 v220, v220\n"
    "v_mfma_f32_16x16x32_bf16 v[136:139], a[176:179], v[192:195], v[136:139]\n"
    "ds_read_b128 a[176:179], v241 offset:4096\n"
    "v_mfma_f32_16x16x32_bf16 v[136:139], a[180:183], v[196:199], v[136:139]\n"
    "s_waitcnt lgkmcnt(10)\n"
    "ds_read_b128 a[180:183], v242 offset:0\n"
    "ds_write_addtid_b32 v235 offset:17152\n"
    "v_mfma_f32_16x16x32_bf16 v[148:151], a[184:187], v[192:195], v[148:151]\n"
    "v_exp_f32_e32 v221, v221\n"
    "v_mfma_f32_16x16x32_bf16 v[148:151], a[188:191], v[196:199], v[148:151]\n"
    "ds_write_addtid_b32 v226 offset:12800\n"
    "v_cvt_pk_bf16_f32 v227, v230, v231\n"
    "v_mfma_f32_16x16x32_bf16 v[144:147], a[188:191], v[188:191], v[144:147]\n"
    "v_exp_f32_e32 v222, v222\n"
    "v_mfma_f32_16x16x32_bf16 v[144:147], a[184:187], v[184:187], v[144:147]\n"
    "v_exp_f32_e32 v223, v223\n"
    "v_mfma_f32_16x16x32_bf16 v[140:143], a[184:187], v[176:179], v[140:143]\n"
    "ds_read_b128 a[184:187], v243 offset:0\n"
    "v_cvt_pk_bf16_f32 v218, v220, v221\n"
    "v_mfma_f32_16x16x32_bf16 v[140:143], a[188:191], v[180:183], v[140:143]\n"
    "s_waitcnt lgkmcnt(11)\n"
    "ds_read_b128 a[188:191], v242 offset:4096\n"
    "v_cvt_pk_bf16_f32 v219, v222, v223\n"
    "v_mfma_f32_16x16x32_bf16 v[176:179], a[160:163], a[92:95], 0\n"
    "s_waitcnt vmcnt(0)\n"
    "ds_write_addtid_b32 v227 offset:13056\n"
    "v_cmp_ne_u32_e32 vcc, s39, v255\n"
    "v_mfma_f32_16x16x32_bf16 v[176:179], a[164:167], a[64:67], v[176:179]\n"
    "ds_write_addtid_b32 v218 offset:8704\n"
    "v_mfma_f32_16x16x32_bf16 v[184:187], a[164:167], a[96:99], 0\n"
    "s_cbranch_vccnz .Lat_wpoll\n"
    ".Lat_wpoll_ret:\n"
    "v_mfma_f32_16x16x32_bf16 v[184:187], a[160:163], a[124:127], v[184:187]\n"
    "ds_write_addtid_b32 v219 offset:8960\n"
    "v_mfma_f32_16x16x32_bf16 v[192:195], a[160:163], a[156:159], 0\n"
    "ds_read_b128 a[160:163], v243 offset:6144\n"
    "v_mfma_f32_16x16x32_bf16 v[192:195], a[164:167], a[128:131], v[192:195]\n"
    "s_waitcnt lgkmcnt(12)\n"
    "ds_read_b128 a[164:167], v240 offset:2048\n"
    "v_mfma_f32_16x16x32_bf16 v[192:195], a[168:171], a[132:135], v[192:195]\n"
    "v_mfma_f32_16x16x32_bf16 v[192:195], a[172:175], a[136:139], v[192:195]\n"
    "s_waitcnt lgkmcnt(2)\n"
    "v_mfma_f32_16x16x32_bf16 v[184:187], a[172:175], a[104:107], v[184:187]\n"
    "s_barrier\n"
    "v_mfma_f32_16x16x32_bf16 v[184:187], a[168:171], a[100:103], v[184:187]\n"
    "s_add_u32 s61, s47, 5791744\n"
    "v_mfma_f32_16x16x32_bf16 v[176:179], a[168:171], a[68:71], v[176:179]\n"
    "s_add_u32 m0, s49, 20480\n"
    "ds_read_b128 a[168:171], v241 offset:2048\n"
    "v_mfma_f32_16x16x32_bf16 v[176:179], a[172:175], a[72:75], v[176:179]\n"
    "ds_read_b128 a[172:175], v240 offset:6144\n"
    "v_mfma_f32_16x16x32_bf16 v[176:179], a[176:179], a[76:79], v[176:179]\n"
    "buffer_load_dwordx4 v252, s[8:11], s61 offen offset:0 sc1 lds\n"
    "v_mfma_f32_16x16x32_bf16 v[176:179], a[180:183], a[80:83], v[176:179]\n"
    "s_add_u32 s61, s47, 5795840\n"
    "ds_read2st64_b32 v[220:221], v250 offset0:32 offset1:33\n"
    "v_mfma_f32_16x16x32_bf16 v[184:187], a[180:183], a[112:115], v[184:187]\n"
    "s_add_u32 m0, s49, 24576\n"
    "v_mfma_f32_16x16x32_bf16 v[184:187], a[176:179], a[108:111], v[184:187]\n"
    "v_mfma_f32_16x16x32_bf16 v[192:195], a[176:179], a[140:143], v[192:195]\n"
    "ds_read_b128 a[176:179], v241 offset:6144\n"
    "buffer_load_dwordx4 v252, s[8:11], s61 offen offset:0 sc1 lds\n"
    "v_mfma_f32_16x16x32_bf16 v[192:195], a[180:183], a[144:147], v[192:195]\n"
    "s_add_u32 s61, s47, 5799936\n"
    "ds_read_b128 a[180:183], v242 offset:2048\n"
    "v_mfma_f32_16x16x32_bf16 v[192:195], a[184:187], a[148:151], v[192:195]\n"
    "s_add_u32 m0, s49, 28672\n"
    "ds_read2st64_b32 v[222:223], v250 offset0:34 offset1:35\n"
    "v_mfma_f32_16x16x32_bf16 v[192:195], a[188:191], a[152:155], v[192:195]\n"
    "v_mfma_f32_16x16x32_bf16 v[184:187], a[188:191], a[120:123], v[184:187]\n"
    "buffer_load_dwordx4 v252, s[8:11], s61 offen offset:0 sc1 lds\n"
    "v_mfma_f32_16x16x32_bf16 v[184:187], a[184:187], a[116:119], v[184:187]\n"
    "s_mov_b32 m0, s49\n"
    "ds_read2st64_b32 v[228:229], v250 offset0:48 offset1:49\n"
    "v_mfma_f32_16x16x32_bf16 v[176:179], a[184:187], a[84:87], v[176:179]\n"
    "s_add_u32 s48, s57, 30720\n"
    "ds_read_b128 a[184:187], v243 offset:2048\n"
    "v_exp_f32_e32 v192, v192\n"
    "v_mfma_f32_16x16x32_bf16 v[176:179], a[188:191], a[88:91], v[176:179]\n"
    "s_waitcnt lgkmcnt(8)\n"
    "ds_read_b128 a[188:191], v242 offset:6144\n"
    "v_mfma_f32_16x16x32_bf16 v[180:183], a[160:163], a[92:95], 0\n"
    "buffer_load_dwordx4 a[192:195], v253, s[8:11], s48 offen offset:0 sc1\n"
    "v_mfma_f32_16x16x32_bf16 v[180:183], a[164:167], a[64:67], v[180:183]\n"
    "v_exp_f32_e32 v193, v193\n"
    "v_mfma_f32_16x16x32_bf16 v[188:191], a[164:167], a[96:99], 0\n"
    "v_exp_f32_e32 v194, v194\n"
    "v_mfma_f32_16x16x32_bf16 v[188:191], a[160:163], a[124:127], v[188:191]\n"
    "ds_read2st64_b32 v[230:231], v250 offset0:50 offset1:51\n"
    "v_exp_f32_e32 v195, v195\n"
    "v_mfma_f32_16x16x32_bf16 v[196:199], a[160:163], a[156:159], 0\n"
    "ds_read_b64_tr_b16 a[160:161], v244 offset:49152\n"
    "buffer_load_dwordx4 a[196:199], v253, s[8:11], s48 offen offset:1024 sc1\n"
    "v_mfma_f32_16x16x32_bf16 v[196:199], a[164:167], a[128:131], v[196:199]\n"
    "s_waitcnt lgkmcnt(9)\n"
    "s_add_u32 s48, s57, 14336\n"
    "ds_read_b64_tr_b16 a[162:163], v244 offset:51200\n"
    "v_mfma_f32_16x16x32_bf16 v[196:199], a[168:171], a[132:135], v[196:199]\n"
    "ds_read_b64_tr_b16 a[164:165], v244 offset:57344\n"
    "v_cvt_pk_bf16_f32 v192, v192, v193\n"
    "v_mfma_f32_16x16x32_bf16 v[196:199], a[172:175], a[136:139], v[196:199]\n"
    "ds_read_b64_tr_b16 a[166:167], v244 offset:59392\n"
    "v_mfma_f32_16x16x32_bf16 v[188:191], a[172:175], a[104:107], v[188:191]\n"
    "buffer_load_dwordx4 a[200:203], v253, s[8:11], s48 offen offset:0 sc1\n"
    "v_mfma_f32_16x16x32_bf16 v[188:191], a[168:171], a[100:103], v[188:191]\n"
    "ds_read2st64_b32 v[236:237], v250 offset0:64 offset1:65\n"
    "v_mfma_f32_16x16x32_bf16 v[180:183], a[168:171], a[68:71], v[180:183]\n"
    "ds_read_b64_tr_b16 a[168:169], v245 offset:49152\n"
    "v_mfma_f32_16x16x32_bf16 v[180:183], a[172:175], a[72:75], v[180:183]\n"
    "s_waitcnt lgkmcnt(11)\n"
    "ds_read_b64_tr_b16 a[170:171], v245 offset:51200\n"
    "v_mfma_f32_16x16x32_bf16 v[180:183], a[176:179], a[76:79], v[180:183]\n"
    "ds_read_b64_tr_b16 a[172:173], v245 offset:57344\n"
    "buffer_load_dwordx4 a[204:207], v253, s[8:11], s48 offen offset:1024 sc1\n"
    "v_mfma_f32_16x16x32_bf16 v[180:183], a[180:183], a[80:83], v[180:183]\n"
    "s_add_u32 s48, s57, 16384\n"
    "ds_read_b64_tr_b16 a[174:175], v245 offset:59392\n"
    "v_mfma_f32_16x16x32_bf16 v[188:191], a[180:183], a[112:115], v[188:191]\n"
    "ds_read2st64_b32 v[238:239], v250 offset0:66 offset1:67\n"
    "v_mfma_f32_16x16x32_bf16 v[188:191], a[176:179], a[108:111], v[188:191]\n"
    "v_exp_f32_e32 v184, v184\n"
    "v_mfma_f32_16x16x32_bf16 v[196:199], a[176:179], a[140:143], v[196:199]\n"
    "ds_read_b64_tr_b16 a[176:177], v246 offset:49152\n"
    "buffer_load_dwordx4 a[208:211], v253, s[8:11], s48 offen offset:0 sc1\n"
    "v_mfma_f32_16x16x32_bf16 v[196:199], a[180:183], a[144:147], v[196:199]\n"
    "s_waitcnt lgkmcnt(12)\n"
    "ds_read_b64_tr_b16 a[178:179], v246 offset:51200\n"
    "v_mfma_f32_16x16x32_bf16 v[196:199], a[184:187], a[148:151], v[196:199]\n"
    "ds_read_b64_tr_b16 a[180:181], v246 offset:57344\n"
    "v_mfma_f32_16x16x32_bf16 v[196:199], a[188:191], a[152:155], v[196:199]\n"
    "ds_read_b64_tr_b16 a[182:183], v246 offset:59392\n"
    "v_mfma_f32_16x16x32_bf16 v[188:191], a[188:191], a[120:123], v[188:191]\n"
    "buffer_load_dwordx4 a[212:215], v253, s[8:11], s48 offen offset:1024 sc1\n"
    "v_mfma_f32_16x16x32_bf16 v[188:191], a[184:187], a[116:119], v[188:191]\n"
    "s_add_u32 s48, s57, 0\n"
    "v_exp_f32_e32 v185, v185\n"
    "v_mfma_f32_16x16x32_bf16 v[180:183], a[184:187], a[84:87], v[180:183]\n"
    "ds_read_b64_tr_b16 a[184:185], v247 offset:49152\n"
    "v_cvt_pk_bf16_f32 v193, v194, v195\n"
    "v_mfma_f32_16x16x32_bf16 v[180:183], a[188:191], a[88:91], v[180:183]\n"
    "s_waitcnt lgkmcnt(5)\n"
    "ds_read_b64_tr_b16 a[186:187], v247 offset:51200\n"
    "v_mfma_f32_16x16x32_bf16 v[56:59], a[160:163], v[216:219], v[56:59]\n"
    "ds_read_b64_tr_b16 a[188:189], v247 offset:57344\n"
    "buffer_load_dwordx4 a[216:219], v253, s[8:11], s48 offen offset:0 sc1\n"
    "v_mfma_f32_16x16x32_bf16 v[56:59], a[164:167], v[220:223], v[56:59]\n"
    "ds_read_b64_tr_b16 a[190:191], v247 offset:59392\n"
    "v_mfma_f32_16x16x32_bf16 v[60:63], a[164:167], v[228:231], v[60:63]\n"
    "v_exp_f32_e32 v186, v186\n"
    "v_mfma_f32_16x16x32_bf16 v[60:63], a[160:163], v[224:227], v[60:63]\n"
    "v_exp_f32_e32 v187, v187\n"
    "v_mfma_f32_16x16x32_bf16 v[64:67], a[160:163], v[232:235], v[64:67]\n"
    "ds_read_b64_tr_b16 a[160:161], v244 offset:53248\n"
    "buffer_load_dwordx4 a[220:223], v253, s[8:11], s48 offen offset:1024 sc1\n"
    "v_mfma_f32_16x16x32_bf16 v[64:67], a[164:167], v[236:239], v[64:67]\n"
    "ds_read_b64_tr_b16 a[162:163], v244 offset:55296\n"
    "v_cvt_pk_bf16_f32 v184, v184, v185\n"
    "v_mfma_f32_16x16x32_bf16 v[76:79], a[168:171], v[232:235], v[76:79]\n"
    "ds_read_b64_tr_b16 a[164:165], v244 offset:61440\n"
    "v_exp_f32_e32 v176, v176\n"
    "v_mfma_f32_16x16x32_bf16 v[76:79], a[172:175], v[236:239], v[76:79]\n"
    "s_add_u32 s61, s28, 24576\n"
    "ds_read_b64_tr_b16 a[166:167], v244 offset:63488\n"
    "v_mfma_f32_16x16x32_bf16 v[72:75], a[172:175], v[228:231], v[72:75]\n"
    "v_and_b32_e32 v254, 0x80, v253\n"
    "v_mfma_f32_16x16x32_bf16 v[72:75], a[168:171], v[224:227], v[72:75]\n"
    "v_exp_f32_e32 v177, v177\n"
    "v_mfma_f32_16x16x32_bf16 v[68:71], a[168:171], v[216:219], v[68:71]\n"
    "ds_read_b64_tr_b16 a[168:169], v245 offset:53248\n"
    "buffer_load_dword v249, v254, s[8:11], s61 offen offset:0 sc1\n"
    "v_mfma_f32_16x16x32_bf16 v[68:71], a[172:175], v[220:223], v[68:71]\n"
    "s_waitcnt lgkmcnt(9)\n"
    "s_add_u32 s61, s29, 24576\n"
    "ds_read_b64_tr_b16 a[170:171], v245 offset:55296\n"
    "v_mfma_f32_16x16x32_bf16 v[80:83], a[176:179], v[216:219], v[80:83]\n"
    "ds_read_b64_tr_b16 a[172:173], v245 offset:61440\n"
    "v_cvt_pk_bf16_f32 v185, v186, v187\n"
    "v_mfma_f32_16x16x32_bf16 v[80:83], a[180:183], v[220:223], v[80:83]\n"
    "ds_read_b64_tr_b16 a[174:175], v245 offset:63488\n"
    "buffer_load_dword v249, v254, s[8:11], s61 offen offset:0 sc1\n"
    "v_mfma_f32_16x16x32_bf16 v[84:87], a[180:183], v[228:231], v[84:87]\n"
    "s_add_u32 s61, s30, 24576\n"
    "v_exp_f32_e32 v178, v178\n"
    "v_mfma_f32_16x16x32_bf16 v[84:87], a[176:179], v[224:227], v[84:87]\n"
    "s_waitcnt vmcnt(14)\n"
    "v_exp_f32_e32 v179, v179\n"
    "v_mfma_f32_16x16x32_bf16 v[88:91], a[176:179], v[232:235], v[88:91]\n"
    "s_barrier\n"
    "v_mfma_f32_16x16x32_bf16 v[88:91], a[180:183], v[236:239], v[88:91]\n"
    "s_waitcnt lgkmcnt(8)\n"
    "ds_read_b64_tr_b16 a[176:177], v246 offset:53248\n"
    "buffer_load_dword v249, v254, s[8:11], s61 offen offset:0 sc1\n"
    "v_mfma_f32_16x16x32_bf16 v[100:103], a[184:187], v[232:235], v[100:103]\n"
    "s_add_u32 s61, s31, 24576\n"
    "ds_read_b64_tr_b16 a[178:179], v246 offset:55296\n"
    "v_cvt_pk_bf16_f32 v176, v176, v177\n"
    "v_mfma_f32_16x16x32_bf16 v[100:103], a[188:191], v[236:239], v[100:103]\n"
    "ds_read_b64_tr_b16 a[180:181], v246 offset:61440\n"
    "v_cvt_pk_bf16_f32 v177, v178, v179\n"
    "v_mfma_f32_16x16x32_bf16 v[96:99], a[188:191], v[228:231], v[96:99]\n"
    "ds_read_b64_tr_b16 a[182:183], v246 offset:63488\n"
    "buffer_load_dword v249, v254, s[8:11], s61 offen offset:0 sc1\n"
    "v_mfma_f32_16x16x32_bf16 v[96:99], a[184:187], v[224:227], v[96:99]\n"
    "v_exp_f32_e32 v196, v196\n"
    "v_mfma_f32_16x16x32_bf16 v[92:95], a[184:187], v[216:219], v[92:95]\n"
    "ds_read_b64_tr_b16 a[184:185], v247 offset:53248\n"
    "v_exp_f32_e32 v197, v197\n"
    "v_mfma_f32_16x16x32_bf16 v[92:95], a[188:191], v[220:223], v[92:95]\n"
    "s_waitcnt lgkmcnt(9)\n"
    "ds_read_b64_tr_b16 a[186:187], v247 offset:55296\n"
    "ds_write_addtid_b32 v192 offset:16384\n"
    "v_mfma_f32_16x16x32_bf16 v[104:107], a[160:163], v[216:219], v[104:107]\n"
    "ds_read_b64_tr_b16 a[188:189], v247 offset:61440\n"
    "buffer_load_dword v249, v254, s[8:11], s87 offen offset:0 sc1\n"
    "v_mfma_f32_16x16x32_bf16 v[104:107], a[164:167], v[220:223], v[104:107]\n"
    "ds_read_b64_tr_b16 a[190:191], v247 offset:63488\n"
    "ds_write_addtid_b32 v193 offset:16640\n"
    "v_mfma_f32_16x16x32_bf16 v[108:111], a[164:167], v[228:231], v[108:111]\n"
    "v_exp_f32_e32 v198, v198\n"
    "v_mfma_f32_16x16x32_bf16 v[108:111], a[160:163], v[224:227], v[108:111]\n"
    "s_getpc_b64 s[62:63]\n"
    "v_exp_f32_e32 v199, v199\n"
    "v_mfma_f32_16x16x32_bf16 v[112:115], a[160:163], v[232:235], v[112:115]\n"
    "s_bfe_u32 s61, %1, 0x20001\n"
    "ds_read_b128 a[160:163], v243 offset:20480\n"
    "v_cvt_pk_bf16_f32 v194, v196, v197\n"
    "v_mfma_f32_16x16x32_bf16 v[112:115], a[164:167], v[236:239], v[112:115]\n"
    "s_waitcnt lgkmcnt(11)\n"
    "ds_read_b128 a[164:167], v240 offset:16384\n"
    "ds_write_addtid_b32 v184 offset:12288\n"
    "v_mfma_f32_16x16x32_bf16 v[124:127], a[168:171], v[232:235], v[124:127]\n"
    "s_lshl2_add_u32 s61, s61, %2\n"
    "v_exp_f32_e32 v188, v188\n"
    "v_mfma_f32_16x16x32_bf16 v[124:127], a[172:175], v[236:239], v[124:127]\n"
    "s_lshl_b32 s61, s61, 11\n"
    "v_exp_f32_e32 v189, v189\n"
    "v_mfma_f32_16x16x32_bf16 v[120:123], a[172:175], v[228:231], v[120:123]\n"
    "v_lshl_add_u32 v158, v253, 1, s61\n"
    "v_cvt_pk_bf16_f32 v195, v198, v199\n"
    "v_mfma_f32_16x16x32_bf16 v[120:123], a[168:171], v[224:227], v[120:123]\n"
    "v_exp_f32_e32 v190, v190\n"
    "v_mfma_f32_16x16x32_bf16 v[116:119], a[168:171], v[216:219], v[116:119]\n"
    "ds_read_b128 a[168:171], v241 offset:16384\n"
    "global_load_dword v249, v158, s[62:63]\n"
    "v_mfma_f32_16x16x32_bf16 v[116:119], a[172:175], v[220:223], v[116:119]\n"
    "s_waitcnt lgkmcnt(10)\n"
    "ds_read_b128 a[172:175], v240 offset:20480\n"
    "ds_write_addtid_b32 v185 offset:12544\n"
    "v_mfma_f32_16x16x32_bf16 v[128:131], a[176:179], v[216:219], v[128:131]\n"
    "v_exp_f32_e32 v191, v191\n"
    "v_mfma_f32_16x16x32_bf16 v[128:131], a[180:183], v[220:223], v[128:131]\n"
    "ds_write_addtid_b32 v176 offset:8192\n"
    "v_cvt_pk_bf16_f32 v186, v188, v189\n"
    "v_mfma_f32_16x16x32_bf16 v[132:135], a[180:183], v[228:231], v[132:135]\n"
    "s_bfe_u32 s61, %1, 0x40001\n"
    "v_exp_f32_e32 v180, v180\n"
    "v_mfma_f32_16x16x32_bf16 v[132:135], a[176:179], v[224:227], v[132:135]\n"
    "s_lshl2_add_u32 s61, s61, %2\n"
    "v_exp_f32_e32 v181, v181\n"
    "v_mfma_f32_16x16x32_bf16 v[136:139], a[176:179], v[232:235], v[136:139]\n"
    "s_lshl_b32 s61, s61, 11\n"
    "ds_read_b128 a[176:179], v241 offset:20480\n"
    "v_cvt_pk_bf16_f32 v187, v190, v191\n"
    "v_mfma_f32_16x16x32_bf16 v[136:139], a[180:183], v[236:239], v[136:139]\n"
    "s_waitcnt lgkmcnt(9)\n"
    "ds_read_b128 a[180:183], v242 offset:16384\n"
    "ds_write_addtid_b32 v177 offset:8448\n"
    "v_mfma_f32_16x16x32_bf16 v[148:151], a[184:187], v[232:235], v[148:151]\n"
    "s_add_u32 s61, s61, 5373952\n"
    "ds_write_addtid_b32 v194 offset:16896\n"
    "v_lshlrev_b32_e32 v158, 1, v253\n"
    "v_mfma_f32_16x16x32_bf16 v[148:151], a[188:191], v[236:239], v[148:151]\n"
    "v_exp_f32_e32 v182, v182\n"
    "v_mfma_f32_16x16x32_bf16 v[144:147], a[188:191], v[228:231], v[144:147]\n"
    "buffer_load_dword v249, v158, s[8:11], s61 offen offset:0 sc1\n"
    "ds_write_addtid_b32 v195 offset:17152\n"
    "v_mfma_f32_16x16x32_bf16 v[144:147], a[184:187], v[224:227], v[144:147]\n"
    "v_exp_f32_e32 v183, v183\n"
    "v_mfma_f32_16x16x32_bf16 v[140:143], a[184:187], v[216:219], v[140:143]\n"
    "ds_read_b128 a[184:187], v243 offset:16384\n"
    "v_cvt_pk_bf16_f32 v178, v180, v181\n"
    "v_mfma_f32_16x16x32_bf16 v[140:143], a[188:191], v[220:223], v[140:143]\n"
    "s_waitcnt lgkmcnt(11)\n"
    "ds_read_b128 a[188:191], v242 offset:20480\n"
    "v_cvt_pk_bf16_f32 v179, v182, v183\n"
    "v_mfma_f32_16x16x32_bf16 v[216:219], a[160:163], a[92:95], 0\n"
    "ds_write_addtid_b32 v186 offset:12800\n"
    "v_mfma_f32_16x16x32_bf16 v[216:219], a[164:167], a[64:67], v[216:219]\n"
    "ds_write_addtid_b32 v187 offset:13056\n"
    "v_mfma_f32_16x16x32_bf16 v[224:227], a[164:167], a[96:99], 0\n"
    "ds_write_addtid_b32 v178 offset:8704\n"
    "v_mfma_f32_16x16x32_bf16 v[224:227], a[160:163], a[124:127], v[224:227]\n"
    "ds_write_addtid_b32 v179 offset:8960\n"
    "v_mfma_f32_16x16x32_bf16 v[232:235], a[160:163], a[156:159], 0\n"
    "ds_read_b128 a[160:163], v243 offset:22528\n"
    "v_mfma_f32_16x16x32_bf16 v[232:235], a[164:167], a[128:131], v[232:235]\n"
    "s_waitcnt lgkmcnt(14)\n"
    "ds_read_b128 a[164:167], v240 offset:18432\n"
    "v_mfma_f32_16x16x32_bf16 v[232:235], a[168:171], a[132:135], v[232:235]\n"
    "v_mfma_f32_16x16x32_bf16 v[232:235], a[172:175], a[136:139], v[232:235]\n"
    "s_waitcnt lgkmcnt(2)\n"
    "v_mfma_f32_16x16x32_bf16 v[224:227], a[172:175], a[104:107], v[224:227]\n"
    "s_barrier\n"
    "v_mfma_f32_16x16x32_bf16 v[224:227], a[168:171], a[100:103], v[224:227]\n"
    "v_mfma_f32_16x16x32_bf16 v[216:219], a[168:171], a[68:71], v[216:219]\n"
    "ds_read_b128 a[168:171], v241 offset:18432\n"
    "v_mfma_f32_16x16x32_bf16 v[216:219], a[172:175], a[72:75], v[216:219]\n"
    "ds_read_b128 a[172:175], v240 offset:22528\n"
    "v_mfma_f32_16x16x32_bf16 v[216:219], a[176:179], a[76:79], v[216:219]\n"
    "ds_read2st64_b32 v[180:181], v250 offset0:32 offset1:33\n"
    "v_mfma_f32_16x16x32_bf16 v[216:219], a[180:183], a[80:83], v[216:219]\n"
    "v_mfma_f32_16x16x32_bf16 v[224:227], a[180:183], a[112:115], v[224:227]\n"
    "v_mfma_f32_16x16x32_bf16 v[224:227], a[176:179], a[108:111], v[224:227]\n"
    "v_mfma_f32_16x16x32_bf16 v[232:235], a[176:179], a[140:143], v[232:235]\n"
    "ds_read_b128 a[176:179], v241 offset:22528\n"
    "v_mfma_f32_16x16x32_bf16 v[232:235], a[180:183], a[144:147], v[232:235]\n"
    "ds_read_b128 a[180:183], v242 offset:18432\n"
    "v_mfma_f32_16x16x32_bf16 v[232:235], a[184:187], a[148:151], v[232:235]\n"
    "ds_read2st64_b32 v[182:183], v250 offset0:34 offset1:35\n"
    "v_mfma_f32_16x16x32_bf16 v[232:235], a[188:191], a[152:155], v[232:235]\n"
    "v_mfma_f32_16x16x32_bf16 v[224:227], a[188:191], a[120:123], v[224:227]\n"
    "v_mfma_f32_16x16x32_bf16 v[224:227], a[184:187], a[116:119], v[224:227]\n"
    "ds_read2st64_b32 v[188:189], v250 offset0:48 offset1:49\n"
    "v_mfma_f32_16x16x32_bf16 v[216:219], a[184:187], a[84:87], v[216:219]\n"
    "ds_read_b128 a[184:187], v243 offset:18432\n"
    "v_mfma_f32_16x16x32_bf16 v[216:219], a[188:191], a[88:91], v[216:219]\n"
    "s_waitcnt lgkmcnt(8)\n"
    "ds_read_b128 a[188:191], v242 offset:22528\n"
    "v_mfma_f32_16x16x32_bf16 v[220:223], a[160:163], a[92:95], 0\n"
    "v_exp_f32_e32 v232, v232\n"
    "v_mfma_f32_16x16x32_bf16 v[220:223], a[164:167], a[64:67], v[220:223]\n"
    "v_exp_f32_e32 v233, v233\n"
    "v_mfma_f32_16x16x32_bf16 v[228:231], a[164:167], a[96:99], 0\n"
    "s_add_u32 s48, s57, 18432\n"
    "v_exp_f32_e32 v234, v234\n"
    "v_mfma_f32_16x16x32_bf16 v[228:231], a[160:163], a[124:127], v[228:231]\n"
    "ds_read2st64_b32 v[190:191], v250 offset0:50 offset1:51\n"
    "v_mfma_f32_16x16x32_bf16 v[236:239], a[160:163], a[156:159], 0\n"
    "ds_read_b64_tr_b16 a[160:161], v244 offset:0\n"
    "v_exp_f32_e32 v235, v235\n"
    "v_mfma_f32_16x16x32_bf16 v[236:239], a[164:167], a[128:131], v[236:239]\n"
    "s_waitcnt lgkmcnt(9)\n"
    "ds_read_b64_tr_b16 a[162:163], v244 offset:2048\n"
    "v_mfma_f32_16x16x32_bf16 v[236:239], a[168:171], a[132:135], v[236:239]\n"
    "ds_read_b64_tr_b16 a[164:165], v244 offset:8192\n"
    "buffer_load_dwordx4 a[92:95], v253, s[8:11], s48 offen offset:0 sc1\n"
    "v_mfma_f32_16x16x32_bf16 v[236:239], a[172:175], a[136:139], v[236:239]\n"
    "ds_read_b64_tr_b16 a[166:167], v244 offset:10240\n"
    "v_cvt_pk_bf16_f32 v232, v232, v233\n"
    "v_mfma_f32_16x16x32_bf16 v[228:231], a[172:175], a[104:107], v[228:231]\n"
    "ds_read2st64_b32 v[196:197], v250 offset0:64 offset1:65\n"
    "v_mfma_f32_16x16x32_bf16 v[228:231], a[168:171], a[100:103], v[228:231]\n"
    "v_exp_f32_e32 v224, v224\n"
    "v_mfma_f32_16x16x32_bf16 v[220:223], a[168:171], a[68:71], v[220:223]\n"
    "ds_read_b64_tr_b16 a[168:169], v245 offset:0\n"
    "buffer_load_dwordx4 a[124:127], v253, s[8:11], s48 offen offset:1024 sc1\n"
    "v_mfma_f32_16x16x32_bf16 v[220:223], a[172:175], a[72:75], v[220:223]\n"
    "s_waitcnt lgkmcnt(11)\n"
    "s_add_u32 s48, s57, 2048\n"
    "ds_read_b64_tr_b16 a[170:171], v245 offset:2048\n"
    "v_mfma_f32_16x16x32_bf16 v[220:223], a[176:179], a[76:79], v[220:223]\n"
    "ds_read_b64_tr_b16 a[172:173], v245 offset:8192\n"
    "v_exp_f32_e32 v225, v225\n"
    "v_mfma_f32_16x16x32_bf16 v[220:223], a[180:183], a[80:83], v[220:223]\n"
    "ds_read_b64_tr_b16 a[174:175], v245 offset:10240\n"
    "v_cvt_pk_bf16_f32 v233, v234, v235\n"
    "v_mfma_f32_16x16x32_bf16 v[228:231], a[180:183], a[112:115], v[228:231]\n"
    "buffer_load_dwordx4 a[156:159], v253, s[8:11], s48 offen offset:0 sc1\n"
    "v_mfma_f32_16x16x32_bf16 v[228:231], a[176:179], a[108:111], v[228:231]\n"
    "ds_read2st64_b32 v[198:199], v250 offset0:66 offset1:67\n"
    "v_exp_f32_e32 v226, v226\n"
    "v_mfma_f32_16x16x32_bf16 v[236:239], a[176:179], a[140:143], v[236:239]\n"
    "ds_read_b64_tr_b16 a[176:177], v246 offset:0\n"
    "v_mfma_f32_16x16x32_bf16 v[236:239], a[180:183], a[144:147], v[236:239]\n"
    "s_waitcnt lgkmcnt(12)\n"
    "ds_read_b64_tr_b16 a[178:179], v246 offset:2048\n"
    "v_exp_f32_e32 v227, v227\n"
    "v_mfma_f32_16x16x32_bf16 v[236:239], a[184:187], a[148:151], v[236:239]\n"
    "ds_read_b64_tr_b16 a[180:181], v246 offset:8192\n"
    "buffer_load_dwordx4 a[64:67], v253, s[8:11], s48 offen offset:1024 sc1\n"
    "v_mfma_f32_16x16x32_bf16 v[236:239], a[188:191], a[152:155], v[236:239]\n"
    "s_add_u32 s48, s57, 20480\n"
    "ds_read_b64_tr_b16 a[182:183], v246 offset:10240\n"
    "v_cvt_pk_bf16_f32 v224, v224, v225\n"
    "v_mfma_f32_16x16x32_bf16 v[228:231], a[188:191], a[120:123], v[228:231]\n"
    "v_exp_f32_e32 v216, v216\n"
    "v_mfma_f32_16x16x32_bf16 v[228:231], a[184:187], a[116:119], v[228:231]\n"
    "v_exp_f32_e32 v217, v217\n"
    "v_mfma_f32_16x16x32_bf16 v[220:223], a[184:187], a[84:87], v[220:223]\n"
    "ds_read_b64_tr_b16 a[184:185], v247 offset:0\n"
    "buffer_load_dwordx4 a[96:99], v253, s[8:11], s48 offen offset:0 sc1\n"
    "v_mfma_f32_16x16x32_bf16 v[220:223], a[188:191], a[88:91], v[220:223]\n"
    "s_waitcnt lgkmcnt(5)\n"
    "ds_read_b64_tr_b16 a[186:187], v247 offset:2048\n"
    "v_mfma_f32_16x16x32_bf16 v[56:59], a[160:163], v[176:179], v[56:59]\n"
    "ds_read_b64_tr_b16 a[188:189], v247 offset:8192\n"
    "v_mfma_f32_16x16x32_bf16 v[56:59], a[164:167], v[180:183], v[56:59]\n"
    "ds_read_b64_tr_b16 a[190:191], v247 offset:10240\n"
    "v_mfma_f32_16x16x32_bf16 v[60:63], a[164:167], v[188:191], v[60:63]\n"
    "buffer_load_dwordx4 a[128:131], v253, s[8:11], s48 offen offset:1024 sc1\n"
    "v_cvt_pk_bf16_f32 v225, v226, v227\n"
    "v_mfma_f32_16x16x32_bf16 v[60:63], a[160:163], v[184:187], v[60:63]\n"
    "s_add_u32 s48, s57, 4096\n"
    "v_exp_f32_e32 v218, v218\n"
    "v_mfma_f32_16x16x32_bf16 v[64:67], a[160:163], v[192:195], v[64:67]\n"
    "ds_read_b64_tr_b16 a[160:161], v244 offset:4096\n"
    "v_mfma_f32_16x16x32_bf16 v[64:67], a[164:167], v[196:199], v[64:67]\n"
    "ds_read_b64_tr_b16 a[162:163], v244 offset:6144\n"
    "v_mfma_f32_16x16x32_bf16 v[76:79], a[168:171], v[192:195], v[76:79]\n"
    "ds_read_b64_tr_b16 a[164:165], v244 offset:12288\n"
    "buffer_load_dwordx4 a[68:71], v253, s[8:11], s48 offen offset:0 sc1\n"
    "v_mfma_f32_16x16x32_bf16 v[76:79], a[172:175], v[196:199], v[76:79]\n"
    "ds_read_b64_tr_b16 a[166:167], v244 offset:14336\n"
    "v_mfma_f32_16x16x32_bf16 v[72:75], a[172:175], v[188:191], v[72:75]\n"
    "s_barrier\n"
    "v_mfma_f32_16x16x32_bf16 v[72:75], a[168:171], v[184:187], v[72:75]\n"
    "v_exp_f32_e32 v219, v219\n"
    "v_mfma_f32_16x16x32_bf16 v[68:71], a[168:171], v[176:179], v[68:71]\n"
    "ds_read_b64_tr_b16 a[168:169], v245 offset:4096\n"
    "buffer_load_dwordx4 a[100:103], v253, s[8:11], s48 offen offset:1024 sc1\n"
    "v_mfma_f32_16x16x32_bf16 v[68:71], a[172:175], v[180:183], v[68:71]\n"
    "s_waitcnt lgkmcnt(9)\n"
    "s_add_u32 s48, s57, 22528\n"
    "ds_read_b64_tr_b16 a[170:171], v245 offset:6144\n"
    "v_mfma_f32_16x16x32_bf16 v[80:83], a[176:179], v[176:179], v[80:83]\n"
    "ds_read_b64_tr_b16 a[172:173], v245 offset:12288\n"
    "v_cvt_pk_bf16_f32 v216, v216, v217\n"
    "v_mfma_f32_16x16x32_bf16 v[80:83], a[180:183], v[180:183], v[80:83]\n"
    "ds_read_b64_tr_b16 a[174:175], v245 offset:14336\n"
    "v_cvt_pk_bf16_f32 v217, v218, v219\n"
    "v_mfma_f32_16x16x32_bf16 v[84:87], a[180:183], v[188:191], v[84:87]\n"
    "buffer_load_dwordx4 a[132:135], v253, s[8:11], s48 offen offset:0 sc1\n"
    "ds_write_addtid_b32 v232 offset:16384\n"
    "v_mfma_f32_16x16x32_bf16 v[84:87], a[176:179], v[184:187], v[84:87]\n"
    "v_exp_f32_e32 v236, v236\n"
    "v_mfma_f32_16x16x32_bf16 v[88:91], a[176:179], v[192:195], v[88:91]\n"
    "ds_read_b64_tr_b16 a[176:177], v246 offset:4096\n"
    "ds_write_addtid_b32 v233 offset:16640\n"
    "v_mfma_f32_16x16x32_bf16 v[88:91], a[180:183], v[196:199], v[88:91]\n"
    "s_waitcnt lgkmcnt(11)\n"
    "ds_read_b64_tr_b16 a[178:179], v246 offset:6144\n"
    "v_mfma_f32_16x16x32_bf16 v[100:103], a[184:187], v[192:195], v[100:103]\n"
    "ds_read_b64_tr_b16 a[180:181], v246 offset:12288\n"
    "buffer_load_dwordx4 a[72:75], v253, s[8:11], s48 offen offset:1024 sc1\n"
    "v_mfma_f32_16x16x32_bf16 v[100:103], a[188:191], v[196:199], v[100:103]\n"
    "s_add_u32 s48, s57, 6144\n"
    "ds_read_b64_tr_b16 a[182:183], v246 offset:14336\n"
    "ds_write_addtid_b32 v224 offset:12288\n"
    "v_mfma_f32_16x16x32_bf16 v[96:99], a[188:191], v[188:191], v[96:99]\n"
    "v_exp_f32_e32 v237, v237\n"
    "v_mfma_f32_16x16x32_bf16 v[96:99], a[184:187], v[184:187], v[96:99]\n"
    "v_exp_f32_e32 v238, v238\n"
    "v_mfma_f32_16x16x32_bf16 v[92:95], a[184:187], v[176:179], v[92:95]\n"
    "ds_read_b64_tr_b16 a[184:185], v247 offset:4096\n"
    "buffer_load_dwordx4 a[104:107], v253, s[8:11], s48 offen offset:0 sc1\n"
    "v_mfma_f32_16x16x32_bf16 v[92:95], a[188:191], v[180:183], v[92:95]\n"
    "s_waitcnt lgkmcnt(12)\n"
    "ds_read_b64_tr_b16 a[186:187], v247 offset:6144\n"
    "ds_write_addtid_b32 v225 offset:12544\n"
    "v_mfma_f32_16x16x32_bf16 v[104:107], a[160:163], v[176:179], v[104:107]\n"
    "ds_read_b64_tr_b16 a[188:189], v247 offset:12288\n"
    "v_mfma_f32_16x16x32_bf16 v[104:107], a[164:167], v[180:183], v[104:107]\n"
    "ds_read_b64_tr_b16 a[190:191], v247 offset:14336\n"
    "v_mfma_f32_16x16x32_bf16 v[108:111], a[164:167], v[188:191], v[108:111]\n"
    "buffer_load_dwordx4 a[136:139], v253, s[8:11], s48 offen offset:1024 sc1\n"
    "ds_write_addtid_b32 v216 offset:8192\n"
    "v_mfma_f32_16x16x32_bf16 v[108:111], a[160:163], v[184:187], v[108:111]\n"
    "s_add_u32 s48, s57, 24576\n"
    "v_exp_f32_e32 v239, v239\n"
    "v_mfma_f32_16x16x32_bf16 v[112:115], a[160:163], v[192:195], v[112:115]\n"
    "ds_read_b128 a[160:163], v243 offset:36864\n"
    "v_cvt_pk_bf16_f32 v234, v236, v237\n"
    "v_mfma_f32_16x16x32_bf16 v[112:115], a[164:167], v[196:199], v[112:115]\n"
    "s_waitcnt lgkmcnt(14)\n"
    "ds_read_b128 a[164:167], v240 offset:32768\n"
    "v_mfma_f32_16x16x32_bf16 v[124:127], a[168:171], v[192:195], v[124:127]\n"
    "buffer_load_dwordx4 a[76:79], v253, s[8:11], s48 offen offset:0 sc1\n"
    "ds_write_addtid_b32 v217 offset:8448\n"
    "v_mfma_f32_16x16x32_bf16 v[124:127], a[172:175], v[196:199], v[124:127]\n"
    "v_exp_f32_e32 v228, v228\n"
    "v_mfma_f32_16x16x32_bf16 v[120:123], a[172:175], v[188:191], v[120:123]\n"
    "v_exp_f32_e32 v229, v229\n"
    "v_mfma_f32_16x16x32_bf16 v[120:123], a[168:171], v[184:187], v[120:123]\n"
    "ds_write_addtid_b32 v234 offset:16896\n"
    "v_cvt_pk_bf16_f32 v235, v238, v239\n"
    "v_mfma_f32_16x16x32_bf16 v[116:119], a[168:171], v[176:179], v[116:119]\n"
    "ds_read_b128 a[168:171], v241 offset:32768\n"
    "buffer_load_dwordx4 a[108:111], v253, s[8:11], s48 offen offset:1024 sc1\n"
    "v_mfma_f32_16x16x32_bf16 v[116:119], a[172:175], v[180:183], v[116:119]\n"
    "s_waitcnt lgkmcnt(12)\n"
    "s_add_u32 s48, s57, 8192\n"
    "ds_read_b128 a[172:175], v240 offset:36864\n"
    "v_mfma_f32_16x16x32_bf16 v[128:131], a[176:179], v[176:179], v[128:131]\n"
    "v_exp_f32_e32 v230, v230\n"
    "v_mfma_f32_16x16x32_bf16 v[128:131], a[180:183], v[180:183], v[128:131]\n"
    "v_exp_f32_e32 v231, v231\n"
    "v_mfma_f32_16x16x32_bf16 v[132:135], a[180:183], v[188:191], v[132:135]\n"
    "buffer_load_dwordx4 a[140:143], v253, s[8:11], s48 offen offset:0 sc1\n"
    "v_cvt_pk_bf16_f32 v226, v228, v229\n"
    "v_mfma_f32_16x16x32_bf16 v[132:135], a[176:179], v[184:187], v[132:135]\n"
    "v_exp_f32_e32 v220, v220\n"
    "v_mfma_f32_16x16x32_bf16 v[136:139], a[176:179], v[192:195], v[136:139]\n"
    "ds_read_b128 a[176:179], v241 offset:36864\n"
    "v_mfma_f32_16x16x32_bf16 v[136:139], a[180:183], v[196:199], v[136:139]\n"
    "s_waitcnt lgkmcnt(8)\n"
    "ds_read_b128 a[180:183], v242 offset:32768\n"
    "v_mfma_f32_16x16x32_bf16 v[148:151], a[184:187], v[192:195], v[148:151]\n"
    "buffer_load_dwordx4 a[80:83], v253, s[8:11], s48 offen offset:1024 sc1\n"
    "ds_write_addtid_b32 v235 offset:17152\n"
    "v_mfma_f32_16x16x32_bf16 v[148:151], a[188:191], v[196:199], v[148:151]\n"
    "s_add_u32 s48, s57, 26624\n"
    "v_exp_f32_e32 v221, v221\n"
    "v_mfma_f32_16x16x32_bf16 v[144:147], a[188:191], v[188:191], v[144:147]\n"
    "ds_write_addtid_b32 v226 offset:12800\n"
    "v_cvt_pk_bf16_f32 v227, v230, v231\n"
    "v_mfma_f32_16x16x32_bf16 v[144:147], a[184:187], v[184:187], v[144:147]\n"
    "v_exp_f32_e32 v222, v222\n"
    "v_mfma_f32_16x16x32_bf16 v[140:143], a[184:187], v[176:179], v[140:143]\n"
    "ds_read_b128 a[184:187], v243 offset:32768\n"
    "buffer_load_dwordx4 a[112:115], v253, s[8:11], s48 offen offset:0 sc1\n"
    "v_mfma_f32_16x16x32_bf16 v[140:143], a[188:191], v[180:183], v[140:143]\n"
    "s_waitcnt lgkmcnt(5)\n"
    "ds_read_b128 a[188:191], v242 offset:36864\n"
    "v_mfma_f32_16x16x32_bf16 v[160:163], a[160:163], a[28:31], 0\n"
    "v_exp_f32_e32 v223, v223\n"
    "v_mfma_f32_16x16x32_bf16 v[160:163], a[164:167], a[0:3], v[160:163]\n"
    "v_cvt_pk_bf16_f32 v218, v220, v221\n"
    "v_cvt_pk_bf16_f32 v219, v222, v223\n"
    "v_mfma_f32_16x16x32_bf16 v[168:171], a[164:167], a[32:35], 0\n"
    "buffer_load_dwordx4 a[144:147], v253, s[8:11], s48 offen offset:1024 sc1\n"
    "ds_write_addtid_b32 v227 offset:13056\n"
    "v_mfma_f32_16x16x32_bf16 v[168:171], a[160:163], a[60:63], v[168:171]\n"
    "s_add_u32 s48, s57, 10240\n"
    "ds_read_b128 a[160:163], v243 offset:38912\n"
    "v_mfma_f32_16x16x32_bf16 v[168:171], a[168:171], a[36:39], v[168:171]\n"
    "ds_read_b128 a[164:167], v240 offset:34816\n"
    "v_mfma_f32_16x16x32_bf16 v[168:171], a[172:175], a[40:43], v[168:171]\n"
    "ds_write_addtid_b32 v218 offset:8704\n"
    "v_mfma_f32_16x16x32_bf16 v[160:163], a[172:175], a[8:11], v[160:163]\n"
    "buffer_load_dwordx4 a[84:87], v253, s[8:11], s48 offen offset:0 sc1\n"
    "ds_write_addtid_b32 v219 offset:8960\n"
    "v_mfma_f32_16x16x32_bf16 v[160:163], a[168:171], a[4:7], v[160:163]\n"
    "s_waitcnt lgkmcnt(5)\n"
    "ds_read_b128 a[168:171], v241 offset:34816\n"
    "v_mfma_f32_16x16x32_bf16 v[160:163], a[176:179], a[12:15], v[160:163]\n"
    "ds_read_b128 a[172:175], v240 offset:38912\n"
    "v_mfma_f32_16x16x32_bf16 v[160:163], a[180:183], a[16:19], v[160:163]\n"
    "v_mfma_f32_16x16x32_bf16 v[168:171], a[180:183], a[48:51], v[168:171]\n"
    "buffer_load_dwordx4 a[116:119], v253, s[8:11], s48 offen offset:1024 sc1\n"
    "v_mfma_f32_16x16x32_bf16 v[168:171], a[176:179], a[44:47], v[168:171]\n"
    "s_waitcnt lgkmcnt(2)\n"
    "s_add_u32 s48, s57, 28672\n"
    "ds_read_b128 a[176:179], v241 offset:38912\n"
    "v_mfma_f32_16x16x32_bf16 v[168:171], a[184:187], a[52:55], v[168:171]\n"
    "s_barrier\n"
    "v_mfma_f32_16x16x32_bf16 v[168:171], a[188:191], a[56:59], v[168:171]\n"
    "ds_read_b128 a[180:183], v242 offset:34816\n"
    "v_mfma_f32_16x16x32_bf16 v[160:163], a[188:191], a[24:27], v[160:163]\n"
    "buffer_load_dwordx4 a[148:151], v253, s[8:11], s48 offen offset:0 sc1\n"
    "ds_read2st64_b32 v[220:221], v250 offset0:32 offset1:33\n"
    "v_mfma_f32_16x16x32_bf16 v[160:163], a[184:187], a[20:23], v[160:163]\n"
    "s_waitcnt lgkmcnt(3)\n"
    "ds_read_b128 a[184:187], v243 offset:34816\n"
    "v_mfma_f32_16x16x32_bf16 v[164:167], a[160:163], a[28:31], 0\n"
    "ds_read_b128 a[188:191], v242 offset:38912\n"
    "v_mfma_f32_16x16x32_bf16 v[164:167], a[164:167], a[0:3], v[164:167]\n"
    "ds_read2st64_b32 v[222:223], v250 offset0:34 offset1:35\n"
    "v_mfma_f32_16x16x32_bf16 v[172:175], a[164:167], a[32:35], 0\n"
    "buffer_load_dwordx4 a[88:91], v253, s[8:11], s48 offen offset:1024 sc1\n"
    "ds_read2st64_b32 v[228:229], v250 offset0:48 offset1:49\n"
    "v_mfma_f32_16x16x32_bf16 v[172:175], a[160:163], a[60:63], v[172:175]\n"
    "s_add_u32 s48, s57, 12288\n"
    "ds_read_b64_tr_b16 a[160:161], v244 offset:16384\n"
    "ds_read2st64_b32 v[230:231], v250 offset0:50 offset1:51\n"
    "v_mfma_f32_16x16x32_bf16 v[172:175], a[168:171], a[36:39], v[172:175]\n"
    "ds_read_b64_tr_b16 a[162:163], v244 offset:18432\n"
    "v_mfma_f32_16x16x32_bf16 v[172:175], a[172:175], a[40:43], v[172:175]\n"
    "ds_read_b64_tr_b16 a[164:165], v244 offset:24576\n"
    "ds_read2st64_b32 v[236:237], v250 offset0:64 offset1:65\n"
    "v_mfma_f32_16x16x32_bf16 v[164:167], a[172:175], a[8:11], v[164:167]\n"
    "ds_read_b64_tr_b16 a[166:167], v244 offset:26624\n"
    "buffer_load_dwordx4 a[120:123], v253, s[8:11], s48 offen offset:0 sc1\n"
    "v_mfma_f32_16x16x32_bf16 v[164:167], a[168:171], a[4:7], v[164:167]\n"
    "s_waitcnt lgkmcnt(8)\n"
    "ds_read_b64_tr_b16 a[168:169], v245 offset:16384\n"
    "ds_read2st64_b32 v[238:239], v250 offset0:66 offset1:67\n"
    "v_mfma_f32_16x16x32_bf16 v[164:167], a[176:179], a[12:15], v[164:167]\n"
    "ds_read_b64_tr_b16 a[170:171], v245 offset:18432\n"
    "v_mfma_f32_16x16x32_bf16 v[164:167], a[180:183], a[16:19], v[164:167]\n"
    "ds_read_b64_tr_b16 a[172:173], v245 offset:24576\n"
    "v_mfma_f32_16x16x32_bf16 v[172:175], a[180:183], a[48:51], v[172:175]\n"
    "ds_read_b64_tr_b16 a[174:175], v245 offset:26624\n"
    "buffer_load_dwordx4 a[152:155], v253, s[8:11], s48 offen offset:1024 sc1\n"
    "v_mfma_f32_16x16x32_bf16 v[172:175], a[176:179], a[44:47], v[172:175]\n"
    "s_waitcnt lgkmcnt(3)\n"
    "ds_read_b64_tr_b16 a[176:177], v246 offset:16384\n"
    "v_mfma_f32_16x16x32_bf16 v[172:175], a[184:187], a[52:55], v[172:175]\n"
    "s_barrier\n"
    "v_mfma_f32_16x16x32_bf16 v[172:175], a[188:191], a[56:59], v[172:175]\n"
    "s_add_u32 s61, s47, 5779456\n"
    "ds_read_b64_tr_b16 a[178:179], v246 offset:18432\n"
    "v_mfma_f32_16x16x32_bf16 v[164:167], a[188:191], a[24:27], v[164:167]\n"
    "s_add_u32 m0, s49, 8192\n"
    "ds_read_b64_tr_b16 a[180:181], v246 offset:24576\n"
    "v_mfma_f32_16x16x32_bf16 v[164:167], a[184:187], a[20:23], v[164:167]\n"
    "ds_read_b64_tr_b16 a[182:183], v246 offset:26624\n"
    "buffer_load_dwordx4 v252, s[8:11], s61 offen offset:0 sc1 lds\n"
    "v_mfma_f32_16x16x32_bf16 v[56:59], a[160:163], v[216:219], v[56:59]\n"
    "s_add_u32 s61, s47, 5783552\n"
    "ds_read_b64_tr_b16 a[184:185], v247 offset:16384\n"
    "v_mfma_f32_16x16x32_bf16 v[56:59], a[164:167], v[220:223], v[56:59]\n"
    "s_add_u32 m0, s49, 12288\n"
    "ds_read_b64_tr_b16 a[186:187], v247 offset:18432\n"
    "v_mfma_f32_16x16x32_bf16 v[60:63], a[164:167], v[228:231], v[60:63]\n"
    "ds_read_b64_tr_b16 a[188:189], v247 offset:24576\n"
    "buffer_load_dwordx4 v252, s[8:11], s61 offen offset:0 sc1 lds\n"
    "v_mfma_f32_16x16x32_bf16 v[60:63], a[160:163], v[224:227], v[60:63]\n"
    "s_add_u32 s61, s47, 5787648\n"
    "ds_read_b64_tr_b16 a[190:191], v247 offset:26624\n"
    "v_mfma_f32_16x16x32_bf16 v[64:67], a[160:163], v[232:235], v[64:67]\n"
    "s_add_u32 m0, s49, 16384\n"
    "ds_read_b64_tr_b16 a[160:161], v244 offset:20480\n"
    "v_lshrrev_b32_e32 v158, 4, v253\n"
    "v_mfma_f32_16x16x32_bf16 v[64:67], a[164:167], v[236:239], v[64:67]\n"
    "s_waitcnt lgkmcnt(9)\n"
    "ds_read_b64_tr_b16 a[162:163], v244 offset:22528\n"
    "buffer_load_dwordx4 v252, s[8:11], s61 offen offset:0 sc1 lds\n"
    "v_mfma_f32_16x16x32_bf16 v[76:79], a[168:171], v[232:235], v[76:79]\n"
    "s_mov_b32 m0, s49\n"
    "ds_read_b64_tr_b16 a[164:165], v244 offset:28672\n"
    "v_lshrrev_b32_e32 v159, 4, v158\n"
    "v_mfma_f32_16x16x32_bf16 v[76:79], a[172:175], v[236:239], v[76:79]\n"
    "ds_read_b64_tr_b16 a[166:167], v244 offset:30720\n"
    "v_cvt_pk_bf16_f32 v64, v64, v65\n"
    "v_mfma_f32_16x16x32_bf16 v[72:75], a[172:175], v[228:231], v[72:75]\n"
    "v_exp_f32_e32 v168, v168\n"
    "v_mfma_f32_16x16x32_bf16 v[72:75], a[168:171], v[224:227], v[72:75]\n"
    "v_exp_f32_e32 v169, v169\n"
    "v_mfma_f32_16x16x32_bf16 v[68:71], a[168:171], v[216:219], v[68:71]\n"
    "ds_read_b64_tr_b16 a[168:169], v245 offset:20480\n"
    "v_cvt_pk_bf16_f32 v65, v66, v67\n"
    "v_mfma_f32_16x16x32_bf16 v[68:71], a[172:175], v[220:223], v[68:71]\n"
    "s_waitcnt lgkmcnt(9)\n"
    "ds_read_b64_tr_b16 a[170:171], v245 offset:22528\n"
    "v_cvt_pk_bf16_f32 v66, v76, v77\n"
    "v_mfma_f32_16x16x32_bf16 v[80:83], a[176:179], v[216:219], v[80:83]\n"
    "ds_read_b64_tr_b16 a[172:173], v245 offset:28672\n"
    "v_cvt_pk_bf16_f32 v67, v78, v79\n"
    "v_mfma_f32_16x16x32_bf16 v[80:83], a[180:183], v[220:223], v[80:83]\n"
    "ds_read_b64_tr_b16 a[174:175], v245 offset:30720\n"
    "v_cvt_pk_bf16_f32 v60, v60, v61\n"
    "v_mfma_f32_16x16x32_bf16 v[84:87], a[180:183], v[228:231], v[84:87]\n"
    "v_exp_f32_e32 v170, v170\n"
    "v_mfma_f32_16x16x32_bf16 v[84:87], a[176:179], v[224:227], v[84:87]\n"
    "v_exp_f32_e32 v171, v171\n"
    "v_mfma_f32_16x16x32_bf16 v[88:91], a[176:179], v[232:235], v[88:91]\n"
    "ds_read_b64_tr_b16 a[176:177], v246 offset:20480\n"
    "buffer_store_dwordx4 v[64:67], v253, s[8:11], s86 offen offset:0 sc1\n"
    "v_cvt_pk_bf16_f32 v168, v168, v169\n"
    "v_cvt_pk_bf16_f32 v61, v62, v63\n"
    "v_bfe_u32 v255, v158, 2, 2\n"
    "v_mfma_f32_16x16x32_bf16 v[88:91], a[180:183], v[236:239], v[88:91]\n"
    "s_waitcnt lgkmcnt(9)\n"
    "ds_read_b64_tr_b16 a[178:179], v246 offset:22528\n"
    "v_cvt_pk_bf16_f32 v62, v72, v73\n"
    "v_mfma_f32_16x16x32_bf16 v[100:103], a[184:187], v[232:235], v[100:103]\n"
    "ds_read_b64_tr_b16 a[180:181], v246 offset:28672\n"
    "v_cvt_pk_bf16_f32 v63, v74, v75\n"
    "v_mfma_f32_16x16x32_bf16 v[100:103], a[188:191], v[236:239], v[100:103]\n"
    "ds_read_b64_tr_b16 a[182:183], v246 offset:30720\n"
    "v_cvt_pk_bf16_f32 v56, v56, v57\n"
    "v_mfma_f32_16x16x32_bf16 v[96:99], a[188:191], v[228:231], v[96:99]\n"
    "v_exp_f32_e32 v160, v160\n"
    "v_mfma_f32_16x16x32_bf16 v[96:99], a[184:187], v[224:227], v[96:99]\n"
    "v_exp_f32_e32 v161, v161\n"
    "v_and_b32_e32 v158, 3, v158\n"
    "v_mfma_f32_16x16x32_bf16 v[92:95], a[184:187], v[216:219], v[92:95]\n"
    "ds_read_b64_tr_b16 a[184:185], v247 offset:20480\n"
    "v_cvt_pk_bf16_f32 v169, v170, v171\n"
    "v_mfma_f32_16x16x32_bf16 v[92:95], a[188:191], v[220:223], v[92:95]\n"
    "s_waitcnt lgkmcnt(9)\n"
    "ds_read_b64_tr_b16 a[186:187], v247 offset:22528\n"
    "v_cvt_pk_bf16_f32 v57, v58, v59\n"
    "v_mfma_f32_16x16x32_bf16 v[104:107], a[160:163], v[216:219], v[104:107]\n"
    "ds_read_b64_tr_b16 a[188:189], v247 offset:28672\n"
    "v_cvt_pk_bf16_f32 v58, v68, v69\n"
    "v_mfma_f32_16x16x32_bf16 v[104:107], a[164:167], v[220:223], v[104:107]\n"
    "ds_read_b64_tr_b16 a[190:191], v247 offset:30720\n"
    "v_cvt_pk_bf16_f32 v59, v70, v71\n"
    "v_xor_b32_e32 v159, v159, v255\n"
    "v_mfma_f32_16x16x32_bf16 v[108:111], a[164:167], v[228:231], v[108:111]\n"
    "buffer_store_dwordx4 v[60:63], v253, s[8:11], s85 offen offset:0 sc1\n"
    "v_exp_f32_e32 v162, v162\n"
    "v_mfma_f32_16x16x32_bf16 v[108:111], a[160:163], v[224:227], v[108:111]\n"
    "v_exp_f32_e32 v163, v163\n"
    "v_mfma_f32_16x16x32_bf16 v[112:115], a[160:163], v[232:235], v[112:115]\n"
    "ds_read_b128 a[160:163], v243 offset:53248\n"
    "v_cvt_pk_bf16_f32 v160, v160, v161\n"
    "v_mfma_f32_16x16x32_bf16 v[112:115], a[164:167], v[236:239], v[112:115]\n"
    "s_waitcnt lgkmcnt(9)\n"
    "ds_read_b128 a[164:167], v240 offset:49152\n"
    "v_cvt_pk_bf16_f32 v161, v162, v163\n"
    "v_mfma_f32_16x16x32_bf16 v[124:127], a[168:171], v[232:235], v[124:127]\n"
    "v_exp_f32_e32 v172, v172\n"
    "v_lshl_or_b32 v158, v159, 2, v158\n"
    "v_mfma_f32_16x16x32_bf16 v[124:127], a[172:175], v[236:239], v[124:127]\n"
    "v_exp_f32_e32 v173, v173\n"
    "v_mfma_f32_16x16x32_bf16 v[120:123], a[172:175], v[228:231], v[120:123]\n"
    "v_exp_f32_e32 v174, v174\n"
    "v_mfma_f32_16x16x32_bf16 v[120:123], a[168:171], v[224:227], v[120:123]\n"
    "v_exp_f32_e32 v175, v175\n"
    "v_mfma_f32_16x16x32_bf16 v[116:119], a[168:171], v[216:219], v[116:119]\n"
    "ds_read_b128 a[168:171], v241 offset:49152\n"
    "ds_write_addtid_b32 v168 offset:4096\n"
    "v_mfma_f32_16x16x32_bf16 v[116:119], a[172:175], v[220:223], v[116:119]\n"
    "s_waitcnt lgkmcnt(8)\n"
    "ds_read_b128 a[172:175], v240 offset:53248\n"
    "v_cvt_pk_bf16_f32 v88, v88, v89\n"
    "v_mov_b32_e32 v159, 0x3f80\n"
    "v_mfma_f32_16x16x32_bf16 v[128:131], a[176:179], v[216:219], v[128:131]\n"
    "ds_write_addtid_b32 v169 offset:4352\n"
    "v_cvt_pk_bf16_f32 v170, v172, v173\n"
    "v_mfma_f32_16x16x32_bf16 v[128:131], a[180:183], v[220:223], v[128:131]\n"
    "buffer_store_dwordx4 v[56:59], v253, s[8:11], s84 offen offset:0 sc1\n"
    "v_exp_f32_e32 v164, v164\n"
    "v_mfma_f32_16x16x32_bf16 v[132:135], a[180:183], v[228:231], v[132:135]\n"
    "v_exp_f32_e32 v165, v165\n"
    "v_mfma_f32_16x16x32_bf16 v[132:135], a[176:179], v[224:227], v[132:135]\n"
    "ds_write_addtid_b32 v160 offset:0\n"
    "v_cvt_pk_bf16_f32 v171, v174, v175\n"
    "v_mfma_f32_16x16x32_bf16 v[136:139], a[176:179], v[232:235], v[136:139]\n"
    "ds_read_b128 a[176:179], v241 offset:53248\n"
    "v_cvt_pk_bf16_f32 v89, v90, v91\n"
    "v_mfma_f32_16x16x32_bf16 v[136:139], a[180:183], v[236:239], v[136:139]\n"
    "s_waitcnt lgkmcnt(8)\n"
    "ds_read_b128 a[180:183], v242 offset:49152\n"
    "v_cvt_pk_bf16_f32 v90, v100, v101\n"
    "v_mfma_f32_16x16x32_bf16 v[148:151], a[184:187], v[232:235], v[148:151]\n"
    "v_exp_f32_e32 v166, v166\n"
    "v_mfma_f32_16x16x32_bf16 v[148:151], a[188:191], v[236:239], v[148:151]\n"
    "v_exp_f32_e32 v167, v167\n"
    "v_mfma_f32_16x16x32_bf16 v[144:147], a[188:191], v[228:231], v[144:147]\n"
    "ds_write_addtid_b32 v161 offset:256\n"
    "v_cvt_pk_bf16_f32 v162, v164, v165\n"
    "v_mfma_f32_16x16x32_bf16 v[144:147], a[184:187], v[224:227], v[144:147]\n"
    "ds_write_addtid_b32 v170 offset:4608\n"
    "v_cvt_pk_bf16_f32 v163, v166, v167\n"
    "v_cvt_pk_bf16_f32 v91, v102, v103\n"
    "v_mfma_f32_16x16x32_bf16 v[140:143], a[184:187], v[216:219], v[140:143]\n"
    "ds_read_b128 a[184:187], v243 offset:49152\n"
    "buffer_store_dwordx4 v[88:91], v253, s[8:11], s86 offen offset:1024 sc1\n"
    "v_cvt_pk_bf16_f32 v84, v84, v85\n"
    "v_mov_b32_e32 v152, 0\n"
    "v_mfma_f32_16x16x32_bf16 v[140:143], a[188:191], v[220:223], v[140:143]\n"
    "s_waitcnt lgkmcnt(7)\n"
    "ds_read_b128 a[188:191], v242 offset:53248\n"
    "v_mov_b32_e32 v153, 0\n"
    "v_mfma_f32_16x16x32_bf16 v[200:203], a[160:163], a[28:31], 0\n"
    "ds_write_addtid_b32 v171 offset:4864\n"
    "v_mov_b32_e32 v156, 0\n"
    "v_mfma_f32_16x16x32_bf16 v[200:203], a[164:167], a[0:3], v[200:203]\n"
    "ds_write_addtid_b32 v162 offset:512\n"
    "v_mov_b32_e32 v157, 0\n"
    "v_mfma_f32_16x16x32_bf16 v[208:211], a[164:167], a[32:35], 0\n"
    "ds_write_addtid_b32 v163 offset:768\n"
    "v_cmp_eq_u32_e32 vcc, 0, v158\n"
    "v_mfma_f32_16x16x32_bf16 v[208:211], a[160:163], a[60:63], v[208:211]\n"
    "ds_read_b128 a[160:163], v243 offset:55296\n"
    "v_cndmask_b32_e32 v154, 0, v159, vcc\n"
    "v_mfma_f32_16x16x32_bf16 v[208:211], a[168:171], a[36:39], v[208:211]\n"
    "ds_read_b128 a[164:167], v240 offset:51200\n"
    "v_cmp_eq_u32_e32 vcc, 1, v158\n"
    "v_mfma_f32_16x16x32_bf16 v[208:211], a[172:175], a[40:43], v[208:211]\n"
    "s_waitcnt lgkmcnt(2)\n"
    "v_cvt_pk_bf16_f32 v85, v86, v87\n"
    "v_cndmask_b32_e64 v154, v154, 1.0, vcc\n"
    "v_mfma_f32_16x16x32_bf16 v[200:203], a[172:175], a[8:11], v[200:203]\n"
    "s_barrier\n"
    "v_mfma_f32_16x16x32_bf16 v[200:203], a[168:171], a[4:7], v[200:203]\n"
    "ds_read_b128 a[168:171], v241 offset:51200\n"
    "v_cmp_eq_u32_e32 vcc, 2, v158\n"
    "v_mfma_f32_16x16x32_bf16 v[200:203], a[176:179], a[12:15], v[200:203]\n"
    "ds_read_b128 a[172:175], v240 offset:55296\n"
    "v_cndmask_b32_e32 v155, 0, v159, vcc\n"
    "v_mfma_f32_16x16x32_bf16 v[200:203], a[180:183], a[16:19], v[200:203]\n"
    "ds_read2st64_b32 v[164:165], v250 offset0:0 offset1:1\n"
    "v_cmp_eq_u32_e32 vcc, 3, v158\n"
    "s_nop 1\n"
    "v_cndmask_b32_e64 v155, v155, 1.0, vcc\n"
    "v_mfma_f32_16x16x32_bf16 v[208:211], a[180:183], a[48:51], v[208:211]\n"
    "v_cvt_pk_bf16_f32 v86, v96, v97\n"
    "v_and_b32_e32 v251, 0xf0, v253\n"
    "v_mov_b32_e32 v254, 2.0\n"
    "v_mfma_f32_16x16x32_bf16 v[208:211], a[176:179], a[44:47], v[208:211]\n"
    "ds_read_b128 a[176:179], v241 offset:55296\n"
    "v_cmp_le_u32_e32 vcc, 0x100, v253\n"
    "v_mov_b32_e32 v255, s29\n"
    "v_mfma_f32_16x16x32_bf16 v[208:211], a[184:187], a[52:55], v[208:211]\n"
    "ds_read_b128 a[180:183], v242 offset:51200\n"
    "v_cvt_pk_bf16_f32 v87, v98, v99\n"
    "v_mfma_f32_16x16x32_bf16 v[208:211], a[188:191], a[56:59], v[208:211]\n"
    "v_cvt_pk_bf16_f32 v80, v80, v81\n"
    "v_cvt_pk_bf16_f32 v81, v82, v83\n"
    "v_mfma_f32_16x16x32_bf16 v[200:203], a[188:191], a[24:27], v[200:203]\n"
    "ds_read2st64_b32 v[166:167], v250 offset0:2 offset1:3\n"
    "v_cvt_pk_bf16_f32 v82, v92, v93\n"
    "v_mfma_f32_16x16x32_bf16 v[200:203], a[184:187], a[20:23], v[200:203]\n"
    "s_waitcnt lgkmcnt(4)\n"
    "ds_read_b128 a[184:187], v243 offset:51200\n"
    "v_cvt_pk_bf16_f32 v83, v94, v95\n"
    "v_mfma_f32_16x16x32_bf16 v[204:207], a[160:163], a[28:31], 0\n"
    "ds_read_b128 a[188:191], v242 offset:55296\n"
    "v_cvt_pk_bf16_f32 v112, v112, v113\n"
    "v_mfma_f32_16x16x32_bf16 v[204:207], a[164:167], a[0:3], v[204:207]\n"
    "v_exp_f32_e32 v208, v208\n"
    "v_mfma_f32_16x16x32_bf16 v[212:215], a[164:167], a[32:35], 0\n"
    "ds_read2st64_b32 v[172:173], v250 offset0:16 offset1:17\n"
    "v_cvt_pk_bf16_f32 v113, v114, v115\n"
    "v_mfma_f32_16x16x32_bf16 v[212:215], a[160:163], a[60:63], v[212:215]\n"
    "ds_read_b64_tr_b16 a[160:161], v244 offset:32768\n"
    "ds_read_b64_tr_b16 a[162:163], v244 offset:34816\n"
    "v_mfma_f32_16x16x32_bf16 v[212:215], a[168:171], a[36:39], v[212:215]\n"
    "v_exp_f32_e32 v209, v209\n"
    "v_mfma_f32_16x16x32_bf16 v[212:215], a[172:175], a[40:43], v[212:215]\n"
    "ds_read_b64_tr_b16 a[164:165], v244 offset:40960\n"
    "ds_read_b64_tr_b16 a[166:167], v244 offset:43008\n"
    "v_mfma_f32_16x16x32_bf16 v[204:207], a[172:175], a[8:11], v[204:207]\n"
    "v_exp_f32_e32 v210, v210\n"
    "v_mfma_f32_16x16x32_bf16 v[204:207], a[168:171], a[4:7], v[204:207]\n"
    "s_waitcnt lgkmcnt(5)\n"
    "ds_read_b64_tr_b16 a[168:169], v245 offset:32768\n"
    "ds_read_b64_tr_b16 a[170:171], v245 offset:34816\n"
    "v_mfma_f32_16x16x32_bf16 v[204:207], a[176:179], a[12:15], v[204:207]\n"
    "ds_read2st64_b32 v[174:175], v250 offset0:18 offset1:19\n"
    "v_cvt_pk_bf16_f32 v114, v124, v125\n"
    "v_mfma_f32_16x16x32_bf16 v[204:207], a[180:183], a[16:19], v[204:207]\n"
    "ds_read_b64_tr_b16 a[172:173], v245 offset:40960\n"
    "ds_read_b64_tr_b16 a[174:175], v245 offset:43008\n"
    "v_mfma_f32_16x16x32_bf16 v[212:215], a[180:183], a[48:51], v[212:215]\n"
    "v_exp_f32_e32 v211, v211\n"
    "v_mfma_f32_16x16x32_bf16 v[212:215], a[176:179], a[44:47], v[212:215]\n"
    "ds_read_b64_tr_b16 a[176:177], v246 offset:32768\n"
    "ds_read_b64_tr_b16 a[178:179], v246 offset:34816\n"
    "v_mfma_f32_16x16x32_bf16 v[212:215], a[184:187], a[52:55], v[212:215]\n"
    "v_cvt_pk_bf16_f32 v208, v208, v209\n"
    "v_cvt_pk_bf16_f32 v115, v126, v127\n"
    "v_mfma_f32_16x16x32_bf16 v[212:215], a[188:191], a[56:59], v[212:215]\n"
    "ds_read_b64_tr_b16 a[180:181], v246 offset:40960\n"
    "ds_read_b64_tr_b16 a[182:183], v246 offset:43008\n"
    "v_mfma_f32_16x16x32_bf16 v[204:207], a[188:191], a[24:27], v[204:207]\n"
    "v_exp_f32_e32 v200, v200\n"
    "v_mfma_f32_16x16x32_bf16 v[204:207], a[184:187], a[20:23], v[204:207]\n"
    "s_waitcnt lgkmcnt(4)\n"
    "ds_read_b64_tr_b16 a[184:185], v247 offset:32768\n"
    "ds_read_b64_tr_b16 a[186:187], v247 offset:34816\n"
    "v_mfma_f32_16x16x32_bf16 v[0:3], a[160:163], v[160:163], v[0:3]\n"
    "v_exp_f32_e32 v201, v201\n"
    "v_mfma_f32_16x16x32_bf16 v[0:3], a[164:167], v[164:167], v[0:3]\n"
    "ds_read_b64_tr_b16 a[188:189], v247 offset:40960\n"
    "ds_read_b64_tr_b16 a[190:191], v247 offset:43008\n"
    "v_mfma_f32_16x16x32_bf16 a[248:251], a[164:167], v[172:175], a[248:251]\n"
    "v_cvt_pk_bf16_f32 v209, v210, v211\n"
    "v_cvt_pk_bf16_f32 v108, v108, v109\n"
    "v_mfma_f32_16x16x32_bf16 a[248:251], a[160:163], v[168:171], a[248:251]\n"
    "ds_read_b64_tr_b16 a[160:161], v244 offset:36864\n"
    "ds_read_b64_tr_b16 a[162:163], v244 offset:38912\n"
    "v_mfma_f32_16x16x32_bf16 a[252:255], a[168:171], v[168:171], a[252:255]\n"
    "v_exp_f32_e32 v202, v202\n"
    "v_mfma_f32_16x16x32_bf16 a[252:255], a[172:175], v[172:175], a[252:255]\n"
    "s_barrier\n"
    "v_mfma_f32_16x16x32_bf16 v[4:7], a[172:175], v[164:167], v[4:7]\n"
    "ds_read_b64_tr_b16 a[164:165], v244 offset:45056\n"
    "ds_read_b64_tr_b16 a[166:167], v244 offset:47104\n"
    "v_mfma_f32_16x16x32_bf16 v[4:7], a[168:171], v[160:163], v[4:7]\n"
    "s_waitcnt lgkmcnt(4)\n"
    "v_exp_f32_e32 v203, v203\n"
    "v_mfma_f32_16x16x32_bf16 v[8:11], a[176:179], v[160:163], v[8:11]\n"
    "ds_read_b64_tr_b16 a[168:169], v245 offset:36864\n"
    "ds_read_b64_tr_b16 a[170:171], v245 offset:38912\n"
    "v_mfma_f32_16x16x32_bf16 v[8:11], a[180:183], v[164:167], v[8:11]\n"
    "buffer_store_dwordx4 v[84:87], v253, s[8:11], s85 offen offset:1024 sc1\n"
    "v_cvt_pk_bf16_f32 v200, v200, v201\n"
    "v_cvt_pk_bf16_f32 v201, v202, v203\n"
    "v_mfma_f32_16x16x32_bf16 v[32:35], a[180:183], v[172:175], v[32:35]\n"
    "ds_read_b64_tr_b16 a[172:173], v245 offset:45056\n"
    "ds_read_b64_tr_b16 a[174:175], v245 offset:47104\n"
    "v_mfma_f32_16x16x32_bf16 v[32:35], a[176:179], v[168:171], v[32:35]\n"
    "v_exp_f32_e32 v212, v212\n"
    "v_mfma_f32_16x16x32_bf16 v[36:39], a[184:187], v[168:171], v[36:39]\n"
    "ds_read_b64_tr_b16 a[176:177], v246 offset:36864\n"
    "ds_read_b64_tr_b16 a[178:179], v246 offset:38912\n"
    "v_mfma_f32_16x16x32_bf16 v[36:39], a[188:191], v[172:175], v[36:39]\n"
    "buffer_store_dwordx4 v[80:83], v253, s[8:11], s84 offen offset:1024 sc1\n"
    "v_exp_f32_e32 v213, v213\n"
    "v_mfma_f32_16x16x32_bf16 v[12:15], a[188:191], v[164:167], v[12:15]\n"
    "ds_read_b64_tr_b16 a[180:181], v246 offset:45056\n"
    "ds_read_b64_tr_b16 a[182:183], v246 offset:47104\n"
    "v_mfma_f32_16x16x32_bf16 v[12:15], a[184:187], v[160:163], v[12:15]\n"
    "s_waitcnt lgkmcnt(4)\n"
    "v_exp_f32_e32 v214, v214\n"
    "v_mfma_f32_16x16x32_bf16 v[16:19], a[160:163], v[160:163], v[16:19]\n"
    "ds_read_b64_tr_b16 a[184:185], v247 offset:36864\n"
    "ds_read_b64_tr_b16 a[186:187], v247 offset:38912\n"
    "v_mfma_f32_16x16x32_bf16 v[16:19], a[164:167], v[164:167], v[16:19]\n"
    "v_exp_f32_e32 v215, v215\n"
    "v_mfma_f32_16x16x32_bf16 v[40:43], a[164:167], v[172:175], v[40:43]\n"
    "ds_read_b64_tr_b16 a[188:189], v247 offset:45056\n"
    "ds_read_b64_tr_b16 a[190:191], v247 offset:47104\n"
    "v_mfma_f32_16x16x32_bf16 v[40:43], a[160:163], v[168:171], v[40:43]\n"
    "ds_write_addtid_b32 v208 offset:4096\n"
    "buffer_store_dwordx4 v[112:115], v253, s[8:11], s86 offen offset:2048 sc1\n"
    "v_cvt_pk_bf16_f32 v210, v212, v213\n"
    "v_cvt_pk_bf16_f32 v109, v110, v111\n"
    "v_mfma_f32_16x16x32_bf16 v[44:47], a[168:171], v[168:171], v[44:47]\n"
    "ds_read_b128 a[160:163], v243 offset:4096\n"
    "ds_write_addtid_b32 v209 offset:4352\n"
    "v_mfma_f32_16x16x32_bf16 v[44:47], a[172:175], v[172:175], v[44:47]\n"
    "ds_read_b128 a[164:167], v240 offset:0\n"
    "ds_write_addtid_b32 v200 offset:0\n"
    "v_mfma_f32_16x16x32_bf16 v[20:23], a[172:175], v[164:167], v[20:23]\n"
    "v_exp_f32_e32 v204, v204\n"
    "v_mfma_f32_16x16x32_bf16 v[20:23], a[168:171], v[160:163], v[20:23]\n"
    "s_waitcnt lgkmcnt(5)\n"
    "ds_read_b128 a[168:171], v241 offset:0\n"
    "v_cvt_pk_bf16_f32 v110, v120, v121\n"
    "v_mfma_f32_16x16x32_bf16 v[24:27], a[176:179], v[160:163], v[24:27]\n"
    "ds_read_b128 a[172:175], v240 offset:4096\n"
    "v_cvt_pk_bf16_f32 v111, v122, v123\n"
    "v_mfma_f32_16x16x32_bf16 v[24:27], a[180:183], v[164:167], v[24:27]\n"
    "buffer_store_dwordx4 v[108:111], v253, s[8:11], s85 offen offset:2048 sc1\n"
    "v_exp_f32_e32 v205, v205\n"
    "v_mfma_f32_16x16x32_bf16 v[48:51], a[180:183], v[172:175], v[48:51]\n"
    "ds_write_addtid_b32 v201 offset:256\n"
    "v_cvt_pk_bf16_f32 v211, v214, v215\n"
    "v_mfma_f32_16x16x32_bf16 v[48:51], a[176:179], v[168:171], v[48:51]\n"
    "ds_read_b128 a[176:179], v241 offset:4096\n"
    "v_cndmask_b32_e32 v254, v254, v255, vcc\n"
    "v_mfma_f32_16x16x32_bf16 v[52:55], a[184:187], v[168:171], v[52:55]\n"
    "ds_read_b128 a[180:183], v242 offset:0\n"
    "v_cmp_le_u32_e32 vcc, 0x200, v253\n"
    "v_mov_b32_e32 v255, s30\n"
    "v_mfma_f32_16x16x32_bf16 v[52:55], a[188:191], v[172:175], v[52:55]\n"
    "v_exp_f32_e32 v206, v206\n"
    "v_mfma_f32_16x16x32_bf16 v[28:31], a[188:191], v[164:167], v[28:31]\n"
    "v_exp_f32_e32 v207, v207\n"
    "v_mfma_f32_16x16x32_bf16 v[28:31], a[184:187], v[160:163], v[28:31]\n"
    "s_waitcnt lgkmcnt(3)\n"
    "ds_read_b128 a[184:187], v243 offset:0\n"
    "v_cvt_pk_bf16_f32 v202, v204, v205\n"
    "v_cndmask_b32_e32 v254, v254, v255, vcc\n"
    "v_mfma_f32_16x16x32_bf16 v[160:163], a[160:163], a[28:31], 0\n"
    "ds_read_b128 a[188:191], v242 offset:4096\n"
    "v_cmp_le_u32_e32 vcc, 0x300, v253\n"
    "v_mov_b32_e32 v255, s31\n"
    "v_mfma_f32_16x16x32_bf16 v[160:163], a[164:167], a[0:3], v[160:163]\n"
    "ds_write_addtid_b32 v210 offset:4608\n"
    "v_cvt_pk_bf16_f32 v203, v206, v207\n"
    "v_cndmask_b32_e32 v254, v254, v255, vcc\n"
    "v_mfma_f32_16x16x32_bf16 v[168:171], a[164:167], a[32:35], 0\n"
    "s_cmp_eq_u32 s51, 0\n"
    "s_cselect_b32 s60, 2.0, 24576\n"
    "ds_write_addtid_b32 v211 offset:4864\n"
    "v_add_u32_e32 v251, v251, v254\n"
    "v_mfma_f32_16x16x32_bf16 v[168:171], a[160:163], a[60:63], v[168:171]\n"
    "s_lshl_b32 s45, s51, 16\n"
    "s_add_u32 s45, s45, %3\n"
    "s_add_u32 s45, s45, 73728\n"
    "ds_read_b128 a[160:163], v243 offset:6144\n"
    "v_mfma_f32_16x16x32_bf16 v[168:171], a[168:171], a[36:39], v[168:171]\n"
    "s_lshl_b32 s46, s89, 12\n"
    "s_add_u32 s46, s46, s45\n"
    "ds_read_b128 a[164:167], v240 offset:2048\n"
    "v_add_u32_e32 v159, s46, v253\n"
    "v_mfma_f32_16x16x32_bf16 v[168:171], a[172:175], a[40:43], v[168:171]\n"
    "ds_write_addtid_b32 v202 offset:512\n"
    "v_cvt_pk_bf16_f32 v104, v104, v105\n"
    "v_mfma_f32_16x16x32_bf16 v[160:163], a[172:175], a[8:11], v[160:163]\n"
    "ds_write_addtid_b32 v203 offset:768\n"
    "v_cvt_pk_bf16_f32 v105, v106, v107\n"
    "v_mfma_f32_16x16x32_bf16 v[160:163], a[168:171], a[4:7], v[160:163]\n"
    "s_waitcnt lgkmcnt(6)\n"
    "ds_read_b128 a[168:171], v241 offset:2048\n"
    "v_cvt_pk_bf16_f32 v106, v116, v117\n"
    "v_mfma_f32_16x16x32_bf16 v[160:163], a[176:179], a[12:15], v[160:163]\n"
    "ds_read_b128 a[172:175], v240 offset:6144\n"
    "v_cvt_pk_bf16_f32 v107, v118, v119\n"
    "v_mfma_f32_16x16x32_bf16 v[160:163], a[180:183], a[16:19], v[160:163]\n"
    "s_waitcnt lgkmcnt(2)\n"
    "v_cvt_pk_bf16_f32 v136, v136, v137\n"
    "v_cvt_pk_bf16_f32 v137, v138, v139\n"
    "v_mfma_f32_16x16x32_bf16 v[168:171], a[180:183], a[48:51], v[168:171]\n"
    "s_barrier\n"
    "v_mfma_f32_16x16x32_bf16 v[168:171], a[176:179], a[44:47], v[168:171]\n"
    "s_add_u32 m0, s43, 32768\n"
    "ds_read_b128 a[176:179], v241 offset:6144\n"
    "v_cvt_pk_bf16_f32 v138, v148, v149\n"
    "v_mfma_f32_16x16x32_bf16 v[168:171], a[184:187], a[52:55], v[168:171]\n"
    "ds_read_b128 a[180:183], v242 offset:2048\n"
    "buffer_load_dwordx4 v248, s[8:11], s40 offen offset:0 sc1 lds\n"
    "v_mfma_f32_16x16x32_bf16 v[168:171], a[188:191], a[56:59], v[168:171]\n"
    "ds_read2st64_b32 v[204:205], v250 offset0:0 offset1:1\n"
    "v_cvt_pk_bf16_f32 v139, v150, v151\n"
    "v_mfma_f32_16x16x32_bf16 v[160:163], a[188:191], a[24:27], v[160:163]\n"
    "v_cvt_pk_bf16_f32 v132, v132, v133\n"
    "v_cvt_pk_bf16_f32 v133, v134, v135\n"
    "v_mfma_f32_16x16x32_bf16 v[160:163], a[184:187], a[20:23], v[160:163]\n"
    "s_waitcnt lgkmcnt(3)\n"
    "ds_read_b128 a[184:187], v243 offset:2048\n"
    "buffer_load_dwordx4 v248, s[8:11], s40 offen offset:1024 sc1 lds\n"
    "v_mfma_f32_16x16x32_bf16 v[164:167], a[160:163], a[28:31], 0\n"
    "ds_read_b128 a[188:191], v242 offset:6144\n"
    "v_exp_f32_e32 v168, v168\n"
    "v_mfma_f32_16x16x32_bf16 v[164:167], a[164:167], a[0:3], v[164:167]\n"
    "ds_read2st64_b32 v[206:207], v250 offset0:2 offset1:3\n"
    "v_cvt_pk_bf16_f32 v134, v144, v145\n"
    "v_mfma_f32_16x16x32_bf16 v[172:175], a[164:167], a[32:35], 0\n"
    "buffer_load_dwordx4 v248, s[8:11], s40 offen offset:2048 sc1 lds\n"
    "v_exp_f32_e32 v169, v169\n"
    "v_mfma_f32_16x16x32_bf16 v[172:175], a[160:163], a[60:63], v[172:175]\n"
    "ds_read_b64_tr_b16 a[160:161], v244 offset:49152\n"
    "ds_read_b64_tr_b16 a[162:163], v244 offset:51200\n"
    "v_mfma_f32_16x16x32_bf16 v[172:175], a[168:171], a[36:39], v[172:175]\n"
    "ds_read2st64_b32 v[212:213], v250 offset0:16 offset1:17\n"
    "v_cvt_pk_bf16_f32 v135, v146, v147\n"
    "v_mfma_f32_16x16x32_bf16 v[172:175], a[172:175], a[40:43], v[172:175]\n"
    "ds_read_b64_tr_b16 a[164:165], v244 offset:57344\n"
    "ds_read_b64_tr_b16 a[166:167], v244 offset:59392\n"
    "buffer_load_dwordx4 v248, s[8:11], s40 offen offset:3072 sc1 lds\n"
    "v_mfma_f32_16x16x32_bf16 v[164:167], a[172:175], a[8:11], v[164:167]\n"
    "s_add_u32 s40, s40, 32768\n"
    "ds_read2st64_b32 v[214:215], v250 offset0:18 offset1:19\n"
    "v_cvt_pk_bf16_f32 v128, v128, v129\n"
    "v_mfma_f32_16x16x32_bf16 v[164:167], a[168:171], a[4:7], v[164:167]\n"
    "s_waitcnt lgkmcnt(7)\n"
    "s_add_u32 m0, s43, 36864\n"
    "ds_read_b64_tr_b16 a[168:169], v245 offset:49152\n"
    "ds_read_b64_tr_b16 a[170:171], v245 offset:51200\n"
    "v_mfma_f32_16x16x32_bf16 v[164:167], a[176:179], a[12:15], v[164:167]\n"
    "buffer_load_dwordx4 v248, s[8:11], s41 offen offset:0 sc1 lds\n"
    "v_cvt_pk_bf16_f32 v129, v130, v131\n"
    "v_mfma_f32_16x16x32_bf16 v[164:167], a[180:183], a[16:19], v[164:167]\n"
    "ds_read_b64_tr_b16 a[172:173], v245 offset:57344\n"
    "ds_read_b64_tr_b16 a[174:175], v245 offset:59392\n"
    "v_mfma_f32_16x16x32_bf16 v[172:175], a[180:183], a[48:51], v[172:175]\n"
    "v_exp_f32_e32 v170, v170\n"
    "v_mfma_f32_16x16x32_bf16 v[172:175], a[176:179], a[44:47], v[172:175]\n"
    "ds_read_b64_tr_b16 a[176:177], v246 offset:49152\n"
    "ds_read_b64_tr_b16 a[178:179], v246 offset:51200\n"
    "buffer_load_dwordx4 v248, s[8:11], s41 offen offset:1024 sc1 lds\n"
    "v_mfma_f32_16x16x32_bf16 v[172:175], a[184:187], a[52:55], v[172:175]\n"
    "v_exp_f32_e32 v171, v171\n"
    "v_mfma_f32_16x16x32_bf16 v[172:175], a[188:191], a[56:59], v[172:175]\n"
    "ds_read_b64_tr_b16 a[180:181], v246 offset:57344\n"
    "ds_read_b64_tr_b16 a[182:183], v246 offset:59392\n"
    "v_mfma_f32_16x16x32_bf16 v[164:167], a[188:191], a[24:27], v[164:167]\n"
    "buffer_load_dwordx4 v248, s[8:11], s41 offen offset:2048 sc1 lds\n"
    "v_cvt_pk_bf16_f32 v168, v168, v169\n"
    "v_mfma_f32_16x16x32_bf16 v[164:167], a[184:187], a[20:23], v[164:167]\n"
    "s_waitcnt lgkmcnt(4)\n"
    "ds_read_b64_tr_b16 a[184:185], v247 offset:49152\n"
    "ds_read_b64_tr_b16 a[186:187], v247 offset:51200\n"
    "v_mfma_f32_16x16x32_bf16 v[0:3], a[160:163], v[200:203], v[0:3]\n"
    "v_exp_f32_e32 v160, v160\n"
    "v_mfma_f32_16x16x32_bf16 v[0:3], a[164:167], v[204:207], v[0:3]\n"
    "ds_read_b64_tr_b16 a[188:189], v247 offset:57344\n"
    "ds_read_b64_tr_b16 a[190:191], v247 offset:59392\n"
    "buffer_load_dwordx4 v248, s[8:11], s41 offen offset:3072 sc1 lds\n"
    "v_mfma_f32_16x16x32_bf16 a[248:251], a[164:167], v[212:215], a[248:251]\n"
    "s_add_u32 s41, s41, 32768\n"
    "v_exp_f32_e32 v161, v161\n"
    "v_mfma_f32_16x16x32_bf16 a[248:251], a[160:163], v[208:211], a[248:251]\n"
    "s_mov_b32 m0, s49\n"
    "ds_read_b64_tr_b16 a[160:161], v244 offset:53248\n"
    "ds_read_b64_tr_b16 a[162:163], v244 offset:55296\n"
    "v_mfma_f32_16x16x32_bf16 a[252:255], a[168:171], v[208:211], a[252:255]\n"
    "v_cvt_pk_bf16_f32 v169, v170, v171\n"
    "v_cvt_pk_bf16_f32 v130, v140, v141\n"
    "v_mfma_f32_16x16x32_bf16 a[252:255], a[172:175], v[212:215], a[252:255]\n"
    "s_barrier\n"
    "v_mfma_f32_16x16x32_bf16 v[4:7], a[172:175], v[204:207], v[4:7]\n"
    "ds_read_b64_tr_b16 a[164:165], v244 offset:61440\n"
    "ds_read_b64_tr_b16 a[166:167], v244 offset:63488\n"
    "v_mfma_f32_16x16x32_bf16 v[4:7], a[168:171], v[200:203], v[4:7]\n"
    "s_waitcnt lgkmcnt(4)\n"
    "v_exp_f32_e32 v162, v162\n"
    "v_mfma_f32_16x16x32_bf16 v[8:11], a[176:179], v[200:203], v[8:11]\n"
    "s_add_u32 s45, s45, 16384\n"
    "ds_read_b64_tr_b16 a[168:169], v245 offset:53248\n"
    "ds_read_b64_tr_b16 a[170:171], v245 offset:55296\n"
    "v_lshrrev_b32_e32 v255, 2, v253\n"
    "v_mfma_f32_16x16x32_bf16 v[8:11], a[180:183], v[204:207], v[8:11]\n"
    "buffer_store_dwordx4 v[104:107], v253, s[8:11], s84 offen offset:2048 sc1\n"
    "v_exp_f32_e32 v163, v163\n"
    "v_mfma_f32_16x16x32_bf16 v[32:35], a[180:183], v[212:215], v[32:35]\n"
    "ds_read_b64_tr_b16 a[172:173], v245 offset:61440\n"
    "ds_read_b64_tr_b16 a[174:175], v245 offset:63488\n"
    "v_mfma_f32_16x16x32_bf16 v[32:35], a[176:179], v[208:211], v[32:35]\n"
    "v_cvt_pk_bf16_f32 v160, v160, v161\n"
    "v_cvt_pk_bf16_f32 v161, v162, v163\n"
    "v_mfma_f32_16x16x32_bf16 v[36:39], a[184:187], v[208:211], v[36:39]\n"
    "ds_read_b64_tr_b16 a[176:177], v246 offset:53248\n"
    "ds_read_b64_tr_b16 a[178:179], v246 offset:55296\n"
    "v_mfma_f32_16x16x32_bf16 v[36:39], a[188:191], v[212:215], v[36:39]\n"
    "buffer_store_dwordx4 v[136:139], v253, s[8:11], s86 offen offset:3072 sc1\n"
    "v_exp_f32_e32 v172, v172\n"
    "v_add_u32_e32 v255, s45, v255\n"
    "v_mfma_f32_16x16x32_bf16 v[12:15], a[188:191], v[204:207], v[12:15]\n"
    "ds_read_b64_tr_b16 a[180:181], v246 offset:61440\n"
    "ds_read_b64_tr_b16 a[182:183], v246 offset:63488\n"
    "v_mfma_f32_16x16x32_bf16 v[12:15], a[184:187], v[200:203], v[12:15]\n"
    "s_waitcnt lgkmcnt(4)\n"
    "v_exp_f32_e32 v173, v173\n"
    "v_mfma_f32_16x16x32_bf16 v[16:19], a[160:163], v[200:203], v[16:19]\n"
    "ds_read_b64_tr_b16 a[184:185], v247 offset:53248\n"
    "ds_read_b64_tr_b16 a[186:187], v247 offset:55296\n"
    "v_mfma_f32_16x16x32_bf16 v[16:19], a[164:167], v[204:207], v[16:19]\n"
    "v_exp_f32_e32 v174, v174\n"
    "v_mfma_f32_16x16x32_bf16 v[40:43], a[164:167], v[212:215], v[40:43]\n"
    "s_lshl_b32 s45, s89, 8\n"
    "ds_read_b64_tr_b16 a[188:189], v247 offset:61440\n"
    "ds_read_b64_tr_b16 a[190:191], v247 offset:63488\n"
    "v_add_u32_e32 v254, s45, v255\n"
    "v_mfma_f32_16x16x32_bf16 v[40:43], a[160:163], v[208:211], v[40:43]\n"
    "buffer_store_dwordx4 v[132:135], v253, s[8:11], s85 offen offset:3072 sc1\n"
    "v_exp_f32_e32 v175, v175\n"
    "v_mfma_f32_16x16x32_bf16 v[44:47], a[168:171], v[208:211], v[44:47]\n"
    "ds_read_b128 a[160:163], v243 offset:20480\n"
    "v_cvt_pk_bf16_f32 v170, v172, v173\n"
    "v_mfma_f32_16x16x32_bf16 v[44:47], a[172:175], v[212:215], v[44:47]\n"
    "ds_read_b128 a[164:167], v240 offset:16384\n"
    "ds_write_addtid_b32 v168 offset:4096\n"
    "v_mfma_f32_16x16x32_bf16 v[20:23], a[172:175], v[204:207], v[20:23]\n"
    "s_lshl_b32 s45, s50, 8\n"
    "v_exp_f32_e32 v164, v164\n"
    "v_add_u32_e32 v255, s45, v255\n"
    "v_mfma_f32_16x16x32_bf16 v[20:23], a[168:171], v[200:203], v[20:23]\n"
    "s_waitcnt lgkmcnt(3)\n"
    "ds_read_b128 a[168:171], v241 offset:16384\n"
    "ds_write_addtid_b32 v169 offset:4352\n"
    "v_mfma_f32_16x16x32_bf16 v[24:27], a[176:179], v[200:203], v[24:27]\n"
    "ds_read_b128 a[172:175], v240 offset:20480\n"
    "v_cvt_pk_bf16_f32 v131, v142, v143\n"
    "v_mfma_f32_16x16x32_bf16 v[24:27], a[180:183], v[204:207], v[24:27]\n"
    "buffer_store_dwordx4 v[128:131], v253, s[8:11], s84 offen offset:3072 sc1\n"
    "v_exp_f32_e32 v165, v165\n"
    "v_mfma_f32_16x16x32_bf16 v[48:51], a[180:183], v[212:215], v[48:51]\n"
    "ds_write_addtid_b32 v160 offset:0\n"
    "v_cvt_pk_bf16_f32 v171, v174, v175\n"
    "v_permlane16_swap_b32_e32 v147, v151\n"
    "v_mfma_f32_16x16x32_bf16 v[48:51], a[176:179], v[208:211], v[48:51]\n"
    "ds_read_b128 a[176:179], v241 offset:20480\n"
    "v_mov_b32_e32 v148, s39\n"
    "v_mfma_f32_16x16x32_bf16 v[52:55], a[184:187], v[208:211], v[52:55]\n"
    "ds_read_b128 a[180:183], v242 offset:16384\n"
    "ds_write_addtid_b32 v161 offset:256\n"
    "v_mfma_f32_16x16x32_bf16 v[52:55], a[188:191], v[212:215], v[52:55]\n"
    "v_exp_f32_e32 v166, v166\n"
    "v_mov_b32_e32 v149, 0\n"
    "v_mfma_f32_16x16x32_bf16 v[28:31], a[188:191], v[204:207], v[28:31]\n"
    "v_exp_f32_e32 v167, v167\n"
    "v_permlane32_swap_b32_e32 v143, v151\n"
    "v_mfma_f32_16x16x32_bf16 v[28:31], a[184:187], v[200:203], v[28:31]\n"
    "s_waitcnt lgkmcnt(4)\n"
    "ds_read_b128 a[184:187], v243 offset:16384\n"
    "v_cvt_pk_bf16_f32 v162, v164, v165\n"
    "v_mfma_f32_16x16x32_bf16 v[200:203], a[160:163], a[28:31], 0\n"
    "ds_read_b128 a[188:191], v242 offset:20480\n"
    "v_mfma_f32_16x16x32_bf16 v[200:203], a[164:167], a[0:3], v[200:203]\n"
    "ds_write_addtid_b32 v170 offset:4608\n"
    "buffer_store_dword v151, v251, s[8:11], s60 offen offset:0 sc1\n"
    "v_cvt_pk_bf16_f32 v163, v166, v167\n"
    "v_mfma_f32_16x16x32_bf16 v[208:211], a[164:167], a[32:35], 0\n"
    "ds_write_addtid_b32 v171 offset:4864\n"
    "v_mfma_f32_16x16x32_bf16 v[208:211], a[160:163], a[60:63], v[208:211]\n"
    "ds_read_b128 a[160:163], v243 offset:22528\n"
    "v_lshrrev_b32_e32 v147, 4, v253\n"
    "v_mfma_f32_16x16x32_bf16 v[208:211], a[168:171], a[36:39], v[208:211]\n"
    "ds_read_b128 a[164:167], v240 offset:18432\n"
    "v_mfma_f32_16x16x32_bf16 v[208:211], a[172:175], a[40:43], v[208:211]\n"
    "ds_write_addtid_b32 v162 offset:512\n"
    "v_mfma_f32_16x16x32_bf16 v[200:203], a[172:175], a[8:11], v[200:203]\n"
    "ds_write_addtid_b32 v163 offset:768\n"
    "v_bfe_u32 v145, v147, 2, 2\n"
    "v_mfma_f32_16x16x32_bf16 v[200:203], a[168:171], a[4:7], v[200:203]\n"
    "s_waitcnt lgkmcnt(6)\n"
    "ds_read_b128 a[168:171], v241 offset:18432\n"
    "v_mfma_f32_16x16x32_bf16 v[200:203], a[176:179], a[12:15], v[200:203]\n"
    "ds_read_b128 a[172:175], v240 offset:22528\n"
    "v_mfma_f32_16x16x32_bf16 v[200:203], a[180:183], a[16:19], v[200:203]\n"
    "s_waitcnt lgkmcnt(2)\n"
    "v_min_u32_e32 v145, 2, v145\n"
    "v_mfma_f32_16x16x32_bf16 v[208:211], a[180:183], a[48:51], v[208:211]\n"
    "s_barrier\n"
    "v_mfma_f32_16x16x32_bf16 v[208:211], a[176:179], a[44:47], v[208:211]\n"
    "s_add_u32 s46, s32, 1\n"
    "ds_read_b128 a[176:179], v241 offset:22528\n"
    "v_mfma_f32_16x16x32_bf16 v[208:211], a[184:187], a[52:55], v[208:211]\n"
    "ds_read_b128 a[180:183], v242 offset:18432\n"
    "v_mfma_f32_16x16x32_bf16 v[208:211], a[188:191], a[56:59], v[208:211]\n"
    "ds_read2st64_b32 v[164:165], v250 offset0:0 offset1:1\n"
    "v_mfma_f32_16x16x32_bf16 v[200:203], a[188:191], a[24:27], v[200:203]\n"
    "v_add_u32_e32 v145, s46, v145\n"
    "v_mfma_f32_16x16x32_bf16 v[200:203], a[184:187], a[20:23], v[200:203]\n"
    "s_waitcnt lgkmcnt(3)\n"
    "ds_read_b128 a[184:187], v243 offset:18432\n"
    "v_and_b32_e32 v145, 3, v145\n"
    "v_mfma_f32_16x16x32_bf16 v[204:207], a[160:163], a[28:31], 0\n"
    "ds_read_b128 a[188:191], v242 offset:22528\n"
    "v_lshlrev_b32_e32 v145, 6, v145\n"
    "v_mfma_f32_16x16x32_bf16 v[204:207], a[164:167], a[0:3], v[204:207]\n"
    "ds_read2st64_b32 v[166:167], v250 offset0:2 offset1:3\n"
    "v_and_or_b32 v145, v147, 3, v145\n"
    "v_mfma_f32_16x16x32_bf16 v[212:215], a[164:167], a[32:35], 0\n"
    "v_exp_f32_e32 v208, v208\n"
    "v_mfma_f32_16x16x32_bf16 v[212:215], a[160:163], a[60:63], v[212:215]\n"
    "ds_read_b64_tr_b16 a[160:161], v244 offset:0\n"
    "ds_read_b64_tr_b16 a[162:163], v244 offset:2048\n"
    "v_mfma_f32_16x16x32_bf16 v[212:215], a[168:171], a[36:39], v[212:215]\n"
    "ds_read2st64_b32 v[172:173], v250 offset0:16 offset1:17\n"
    "v_lshlrev_b32_e32 v145, 2, v145\n"
    "v_mfma_f32_16x16x32_bf16 v[212:215], a[172:175], a[40:43], v[212:215]\n"
    "ds_read_b64_tr_b16 a[164:165], v244 offset:8192\n"
    "ds_read_b64_tr_b16 a[166:167], v244 offset:10240\n"
    "v_mfma_f32_16x16x32_bf16 v[204:207], a[172:175], a[8:11], v[204:207]\n"
    "s_lshl_b32 s47, s33, 10\n"
    "v_exp_f32_e32 v209, v209\n"
    "v_mfma_f32_16x16x32_bf16 v[204:207], a[168:171], a[4:7], v[204:207]\n"
    "s_waitcnt lgkmcnt(6)\n"
    "ds_read_b64_tr_b16 a[168:169], v245 offset:0\n"
    "ds_read_b64_tr_b16 a[170:171], v245 offset:2048\n"
    "v_mfma_f32_16x16x32_bf16 v[204:207], a[176:179], a[12:15], v[204:207]\n"
    "s_add_u32 s47, s47, 5636096\n"
    "ds_read2st64_b32 v[174:175], v250 offset0:18 offset1:19\n"
    "v_cmp_gt_u32_e32 vcc, 0x300, v253\n"
    "v_mfma_f32_16x16x32_bf16 v[204:207], a[180:183], a[16:19], v[204:207]\n"
    "s_add_u32 s48, s56, 0\n"
    "ds_read_b64_tr_b16 a[172:173], v245 offset:8192\n"
    "ds_read_b64_tr_b16 a[174:175], v245 offset:10240\n"
    "v_mfma_f32_16x16x32_bf16 v[212:215], a[180:183], a[48:51], v[212:215]\n"
    "v_exp_f32_e32 v210, v210\n"
    "v_mfma_f32_16x16x32_bf16 v[212:215], a[176:179], a[44:47], v[212:215]\n"
    "ds_read_b64_tr_b16 a[176:177], v246 offset:0\n"
    "ds_read_b64_tr_b16 a[178:179], v246 offset:2048\n"
    "v_mfma_f32_16x16x32_bf16 v[212:215], a[184:187], a[52:55], v[212:215]\n"
    "buffer_load_dwordx4 v[56:59], v253, s[8:11], s48 offen offset:0 sc1\n"
    "v_exp_f32_e32 v211, v211\n"
    "v_mfma_f32_16x16x32_bf16 v[212:215], a[188:191], a[56:59], v[212:215]\n"
    "ds_read_b64_tr_b16 a[180:181], v246 offset:8192\n"
    "ds_read_b64_tr_b16 a[182:183], v246 offset:10240\n"
    "v_mfma_f32_16x16x32_bf16 v[204:207], a[188:191], a[24:27], v[204:207]\n"
    "s_add_u32 s60, s60, s28\n"
    "v_cvt_pk_bf16_f32 v208, v208, v209\n"
    "v_and_b32_e32 v251, 0xf0, v253\n"
    "v_mfma_f32_16x16x32_bf16 v[204:207], a[184:187], a[20:23], v[204:207]\n"
    "s_waitcnt lgkmcnt(4)\n"
    "ds_read_b64_tr_b16 a[184:185], v247 offset:0\n"
    "ds_read_b64_tr_b16 a[186:187], v247 offset:2048\n"
    "v_mfma_f32_16x16x32_bf16 v[0:3], a[160:163], v[160:163], v[0:3]\n"
    "buffer_load_dwordx4 v[60:63], v253, s[8:11], s48 offen offset:1024 sc1\n"
    "v_exp_f32_e32 v200, v200\n"
    "v_mfma_f32_16x16x32_bf16 v[0:3], a[164:167], v[164:167], v[0:3]\n"
    "ds_read_b64_tr_b16 a[188:189], v247 offset:8192\n"
    "ds_read_b64_tr_b16 a[190:191], v247 offset:10240\n"
    "v_mfma_f32_16x16x32_bf16 a[248:251], a[164:167], v[172:175], a[248:251]\n"
    "v_exp_f32_e32 v201, v201\n"
    "v_mfma_f32_16x16x32_bf16 a[248:251], a[160:163], v[168:171], a[248:251]\n"
    "ds_read_b64_tr_b16 a[160:161], v244 offset:4096\n"
    "ds_read_b64_tr_b16 a[162:163], v244 offset:6144\n"
    "v_mfma_f32_16x16x32_bf16 a[252:255], a[168:171], v[168:171], a[252:255]\n"
    "buffer_load_dwordx4 v[64:67], v253, s[8:11], s48 offen offset:2048 sc1\n"
    "v_cvt_pk_bf16_f32 v209, v210, v211\n"
    "v_cndmask_b32_e64 v251, v251, 2.0, vcc\n"
    "v_mfma_f32_16x16x32_bf16 a[252:255], a[172:175], v[172:175], a[252:255]\n"
    "s_waitcnt vmcnt(8)\n"
    "s_barrier\n"
    "v_mfma_f32_16x16x32_bf16 v[4:7], a[172:175], v[164:167], v[4:7]\n"
    "ds_read_b64_tr_b16 a[164:165], v244 offset:12288\n"
    "ds_read_b64_tr_b16 a[166:167], v244 offset:14336\n"
    "v_mfma_f32_16x16x32_bf16 v[4:7], a[168:171], v[160:163], v[4:7]\n"
    "s_waitcnt lgkmcnt(4)\n"
    "v_exp_f32_e32 v202, v202\n"
    "v_mfma_f32_16x16x32_bf16 v[8:11], a[176:179], v[160:163], v[8:11]\n"
    "ds_read_b64_tr_b16 a[168:169], v245 offset:4096\n"
    "ds_read_b64_tr_b16 a[170:171], v245 offset:6144\n"
    "buffer_load_dwordx4 v[68:71], v253, s[8:11], s48 offen offset:3072 sc1\n"
    "v_mfma_f32_16x16x32_bf16 v[8:11], a[180:183], v[164:167], v[8:11]\n"
    "s_add_u32 s48, s56, 4096\n"
    "v_exp_f32_e32 v203, v203\n"
    "v_mfma_f32_16x16x32_bf16 v[32:35], a[180:183], v[172:175], v[32:35]\n"
    "ds_read_b64_tr_b16 a[172:173], v245 offset:12288\n"
    "ds_read_b64_tr_b16 a[174:175], v245 offset:14336\n"
    "v_mfma_f32_16x16x32_bf16 v[32:35], a[176:179], v[168:171], v[32:35]\n"
    "v_cvt_pk_bf16_f32 v200, v200, v201\n"
    "v_cvt_pk_bf16_f32 v201, v202, v203\n"
    "v_mfma_f32_16x16x32_bf16 v[36:39], a[184:187], v[168:171], v[36:39]\n"
    "ds_read_b64_tr_b16 a[176:177], v246 offset:4096\n"
    "ds_read_b64_tr_b16 a[178:179], v246 offset:6144\n"
    "buffer_load_dwordx4 v[72:75], v253, s[8:11], s48 offen offset:0 sc1\n"
    "v_mfma_f32_16x16x32_bf16 v[36:39], a[188:191], v[172:175], v[36:39]\n"
    "v_exp_f32_e32 v212, v212\n"
    "v_mfma_f32_16x16x32_bf16 v[12:15], a[188:191], v[164:167], v[12:15]\n"
    "ds_read_b64_tr_b16 a[180:181], v246 offset:12288\n"
    "ds_read_b64_tr_b16 a[182:183], v246 offset:14336\n"
    "v_mfma_f32_16x16x32_bf16 v[12:15], a[184:187], v[160:163], v[12:15]\n"
    "s_waitcnt lgkmcnt(4)\n"
    "v_exp_f32_e32 v213, v213\n"
    "v_mfma_f32_16x16x32_bf16 v[16:19], a[160:163], v[160:163], v[16:19]\n"
    "ds_read_b64_tr_b16 a[184:185], v247 offset:4096\n"
    "ds_read_b64_tr_b16 a[186:187], v247 offset:6144\n"
    "buffer_load_dwordx4 v[76:79], v253, s[8:11], s48 offen offset:1024 sc1\n"
    "v_mfma_f32_16x16x32_bf16 v[16:19], a[164:167], v[164:167], v[16:19]\n"
    "v_exp_f32_e32 v214, v214\n"
    "v_mfma_f32_16x16x32_bf16 v[40:43], a[164:167], v[172:175], v[40:43]\n"
    "ds_read_b64_tr_b16 a[188:189], v247 offset:12288\n"
    "ds_read_b64_tr_b16 a[190:191], v247 offset:14336\n"
    "v_mfma_f32_16x16x32_bf16 v[40:43], a[160:163], v[168:171], v[40:43]\n"
    "v_exp_f32_e32 v215, v215\n"
    "v_mfma_f32_16x16x32_bf16 v[44:47], a[168:171], v[168:171], v[44:47]\n"
    "ds_read_b128 a[160:163], v243 offset:36864\n"
    "buffer_load_dwordx4 v[80:83], v253, s[8:11], s48 offen offset:2048 sc1\n"
    "v_cvt_pk_bf16_f32 v210, v212, v213\n"
    "v_mfma_f32_16x16x32_bf16 v[44:47], a[172:175], v[172:175], v[44:47]\n"
    "ds_read_b128 a[164:167], v240 offset:32768\n"
    "ds_write_addtid_b32 v208 offset:4096\n"
    "v_mfma_f32_16x16x32_bf16 v[20:23], a[172:175], v[164:167], v[20:23]\n"
    "v_exp_f32_e32 v204, v204\n"
    "v_mfma_f32_16x16x32_bf16 v[20:23], a[168:171], v[160:163], v[20:23]\n"
    "s_waitcnt lgkmcnt(3)\n"
    "ds_read_b128 a[168:171], v241 offset:32768\n"
    "ds_write_addtid_b32 v209 offset:4352\n"
    "v_mfma_f32_16x16x32_bf16 v[24:27], a[176:179], v[160:163], v[24:27]\n"
    "ds_read_b128 a[172:175], v240 offset:36864\n"
    "ds_write_addtid_b32 v200 offset:0\n"
    "buffer_load_dwordx4 v[84:87], v253, s[8:11], s48 offen offset:3072 sc1\n"
    "v_mfma_f32_16x16x32_bf16 v[24:27], a[180:183], v[164:167], v[24:27]\n"
    "s_add_u32 s48, s56, 8192\n"
    "v_exp_f32_e32 v205, v205\n"
    "v_mfma_f32_16x16x32_bf16 v[48:51], a[180:183], v[172:175], v[48:51]\n"
    "s_waitcnt vmcnt(8)\n"
    "ds_write_addtid_b32 v201 offset:256\n"
    "buffer_store_dword v148, v149, s[8:11], s87 offen offset:128 sc1\n"
    "v_cvt_pk_bf16_f32 v211, v214, v215\n"
    "v_mfma_f32_16x16x32_bf16 v[48:51], a[176:179], v[168:171], v[48:51]\n"
    "ds_read_b128 a[176:179], v241 offset:36864\n"
    "v_mfma_f32_16x16x32_bf16 v[52:55], a[184:187], v[168:171], v[52:55]\n"
    "ds_read_b128 a[180:183], v242 offset:32768\n"
    "buffer_load_dwordx4 v[88:91], v253, s[8:11], s48 offen offset:0 sc1\n"
    "v_mfma_f32_16x16x32_bf16 v[52:55], a[188:191], v[172:175], v[52:55]\n"
    "v_exp_f32_e32 v206, v206\n"
    "v_mfma_f32_16x16x32_bf16 v[28:31], a[188:191], v[164:167], v[28:31]\n"
    "v_exp_f32_e32 v207, v207\n"
    "v_mfma_f32_16x16x32_bf16 v[28:31], a[184:187], v[160:163], v[28:31]\n"
    "s_waitcnt lgkmcnt(4)\n"
    "ds_read_b128 a[184:187], v243 offset:32768\n"
    "v_cvt_pk_bf16_f32 v202, v204, v205\n"
    "v_mfma_f32_16x16x32_bf16 v[160:163], a[160:163], a[28:31], 0\n"
    "ds_read_b128 a[188:191], v242 offset:36864\n"
    "buffer_load_dwordx4 v[92:95], v253, s[8:11], s48 offen offset:1024 sc1\n"
    "v_mfma_f32_16x16x32_bf16 v[160:163], a[164:167], a[0:3], v[160:163]\n"
    "ds_write_addtid_b32 v210 offset:4608\n"
    "v_cvt_pk_bf16_f32 v203, v206, v207\n"
    "v_mfma_f32_16x16x32_bf16 v[168:171], a[164:167], a[32:35], 0\n"
    "ds_write_addtid_b32 v211 offset:4864\n"
    "v_mfma_f32_16x16x32_bf16 v[168:171], a[160:163], a[60:63], v[168:171]\n"
    "ds_read_b128 a[160:163], v243 offset:38912\n"
    "v_mfma_f32_16x16x32_bf16 v[168:171], a[168:171], a[36:39], v[168:171]\n"
    "ds_read_b128 a[164:167], v240 offset:34816\n"
    "buffer_load_dwordx4 v[96:99], v253, s[8:11], s48 offen offset:2048 sc1\n"
    "v_mfma_f32_16x16x32_bf16 v[168:171], a[172:175], a[40:43], v[168:171]\n"
    "ds_write_addtid_b32 v202 offset:512\n"
    "v_mfma_f32_16x16x32_bf16 v[160:163], a[172:175], a[8:11], v[160:163]\n"
    "ds_write_addtid_b32 v203 offset:768\n"
    "v_mfma_f32_16x16x32_bf16 v[160:163], a[168:171], a[4:7], v[160:163]\n"
    "s_waitcnt lgkmcnt(6)\n"
    "ds_read_b128 a[168:171], v241 offset:34816\n"
    "v_mfma_f32_16x16x32_bf16 v[160:163], a[176:179], a[12:15], v[160:163]\n"
    "ds_read_b128 a[172:175], v240 offset:38912\n"
    "buffer_load_dwordx4 v[100:103], v253, s[8:11], s48 offen offset:3072 sc1\n"
    "v_mfma_f32_16x16x32_bf16 v[160:163], a[180:183], a[16:19], v[160:163]\n"
    "s_waitcnt lgkmcnt(2)\n"
    "s_add_u32 s48, s56, 12288\n"
    "v_mfma_f32_16x16x32_bf16 v[168:171], a[180:183], a[48:51], v[168:171]\n"
    "s_barrier\n"
    "v_mfma_f32_16x16x32_bf16 v[168:171], a[176:179], a[44:47], v[168:171]\n"
    "ds_read_b128 a[176:179], v241 offset:38912\n"
    "v_mfma_f32_16x16x32_bf16 v[168:171], a[184:187], a[52:55], v[168:171]\n"
    "ds_read_b128 a[180:183], v242 offset:34816\n"
    "buffer_load_dwordx4 v[104:107], v253, s[8:11], s48 offen offset:0 sc1\n"
    "v_mfma_f32_16x16x32_bf16 v[168:171], a[188:191], a[56:59], v[168:171]\n"
    "ds_read2st64_b32 v[204:205], v250 offset0:0 offset1:1\n"
    "v_mfma_f32_16x16x32_bf16 v[160:163], a[188:191], a[24:27], v[160:163]\n"
    "v_mfma_f32_16x16x32_bf16 v[160:163], a[184:187], a[20:23], v[160:163]\n"
    "s_waitcnt lgkmcnt(3)\n"
    "ds_read_b128 a[184:187], v243 offset:34816\n"
    "v_exp_f32_e32 v168, v168\n"
    "v_mfma_f32_16x16x32_bf16 v[164:167], a[160:163], a[28:31], 0\n"
    "ds_read_b128 a[188:191], v242 offset:38912\n"
    "buffer_load_dwordx4 v[108:111], v253, s[8:11], s48 offen offset:1024 sc1\n"
    "v_exp_f32_e32 v169, v169\n"
    "v_mfma_f32_16x16x32_bf16 v[164:167], a[164:167], a[0:3], v[164:167]\n"
    "ds_read2st64_b32 v[206:207], v250 offset0:2 offset1:3\n"
    "v_exp_f32_e32 v170, v170\n"
    "v_mfma_f32_16x16x32_bf16 v[172:175], a[164:167], a[32:35], 0\n"
    "v_exp_f32_e32 v171, v171\n"
    "v_mfma_f32_16x16x32_bf16 v[172:175], a[160:163], a[60:63], v[172:175]\n"
    "ds_read_b64_tr_b16 a[160:161], v244 offset:16384\n"
    "ds_read_b64_tr_b16 a[162:163], v244 offset:18432\n"
    "v_mfma_f32_16x16x32_bf16 v[172:175], a[168:171], a[36:39], v[172:175]\n"
    "ds_read2st64_b32 v[212:213], v250 offset0:16 offset1:17\n"
    "buffer_load_dwordx4 v[112:115], v253, s[8:11], s48 offen offset:2048 sc1\n"
    "v_cvt_pk_bf16_f32 v168, v168, v169\n"
    "v_mfma_f32_16x16x32_bf16 v[172:175], a[172:175], a[40:43], v[172:175]\n"
    "ds_read_b64_tr_b16 a[164:165], v244 offset:24576\n"
    "ds_read_b64_tr_b16 a[166:167], v244 offset:26624\n"
    "v_mfma_f32_16x16x32_bf16 v[164:167], a[172:175], a[8:11], v[164:167]\n"
    "v_exp_f32_e32 v160, v160\n"
    "v_mfma_f32_16x16x32_bf16 v[164:167], a[168:171], a[4:7], v[164:167]\n"
    "s_waitcnt lgkmcnt(6)\n"
    "ds_read_b64_tr_b16 a[168:169], v245 offset:16384\n"
    "ds_read_b64_tr_b16 a[170:171], v245 offset:18432\n"
    "v_mfma_f32_16x16x32_bf16 v[164:167], a[176:179], a[12:15], v[164:167]\n"
    "ds_read2st64_b32 v[214:215], v250 offset0:18 offset1:19\n"
    "buffer_load_dwordx4 v[116:119], v253, s[8:11], s48 offen offset:3072 sc1\n"
    "v_mfma_f32_16x16x32_bf16 v[164:167], a[180:183], a[16:19], v[164:167]\n"
    "s_add_u32 s48, s56, 16384\n"
    "ds_read_b64_tr_b16 a[172:173], v245 offset:24576\n"
    "ds_read_b64_tr_b16 a[174:175], v245 offset:26624\n"
    "v_mfma_f32_16x16x32_bf16 v[172:175], a[180:183], a[48:51], v[172:175]\n"
    "v_exp_f32_e32 v161, v161\n"
    "v_mfma_f32_16x16x32_bf16 v[172:175], a[176:179], a[44:47], v[172:175]\n"
    "ds_read_b64_tr_b16 a[176:177], v246 offset:16384\n"
    "ds_read_b64_tr_b16 a[178:179], v246 offset:18432\n"
    "v_cvt_pk_bf16_f32 v169, v170, v171\n"
    "v_mfma_f32_16x16x32_bf16 v[172:175], a[184:187], a[52:55], v[172:175]\n"
    "buffer_load_dwordx4 a[28:31], v253, s[8:11], s48 offen offset:0 sc1\n"
    "v_exp_f32_e32 v162, v162\n"
    "v_mfma_f32_16x16x32_bf16 v[172:175], a[188:191], a[56:59], v[172:175]\n"
    "ds_read_b64_tr_b16 a[180:181], v246 offset:24576\n"
    "ds_read_b64_tr_b16 a[182:183], v246 offset:26624\n"
    "v_mfma_f32_16x16x32_bf16 v[164:167], a[188:191], a[24:27], v[164:167]\n"
    "v_exp_f32_e32 v163, v163\n"
    "v_mfma_f32_16x16x32_bf16 v[164:167], a[184:187], a[20:23], v[164:167]\n"
    "s_waitcnt lgkmcnt(4)\n"
    "ds_read_b64_tr_b16 a[184:185], v247 offset:16384\n"
    "ds_read_b64_tr_b16 a[186:187], v247 offset:18432\n"
    "v_mfma_f32_16x16x32_bf16 v[0:3], a[160:163], v[200:203], v[0:3]\n"
    "buffer_load_dwordx4 a[60:63], v253, s[8:11], s48 offen offset:1024 sc1\n"
    "v_cvt_pk_bf16_f32 v160, v160, v161\n"
    "v_cvt_pk_bf16_f32 v161, v162, v163\n"
    "v_mfma_f32_16x16x32_bf16 v[0:3], a[164:167], v[204:207], v[0:3]\n"
    "ds_read_b64_tr_b16 a[188:189], v247 offset:24576\n"
    "ds_read_b64_tr_b16 a[190:191], v247 offset:26624\n"
    "v_mfma_f32_16x16x32_bf16 a[248:251], a[164:167], v[212:215], a[248:251]\n"
    "v_exp_f32_e32 v172, v172\n"
    "v_mfma_f32_16x16x32_bf16 a[248:251], a[160:163], v[208:211], a[248:251]\n"
    "ds_read_b64_tr_b16 a[160:161], v244 offset:20480\n"
    "ds_read_b64_tr_b16 a[162:163], v244 offset:22528\n"
    "v_mfma_f32_16x16x32_bf16 a[252:255], a[168:171], v[208:211], a[252:255]\n"
    "buffer_load_dwordx4 a[0:3], v253, s[8:11], s48 offen offset:2048 sc1\n"
    "v_exp_f32_e32 v173, v173\n"
    "v_mfma_f32_16x16x32_bf16 a[252:255], a[172:175], v[212:215], a[252:255]\n"
    "s_waitcnt vmcnt(41)\n"
    "s_barrier\n"
    "v_mfma_f32_16x16x32_bf16 v[4:7], a[172:175], v[204:207], v[4:7]\n"
    "ds_read_b64_tr_b16 a[164:165], v244 offset:28672\n"
    "ds_read_b64_tr_b16 a[166:167], v244 offset:30720\n"
    "v_mfma_f32_16x16x32_bf16 v[4:7], a[168:171], v[200:203], v[4:7]\n"
    "s_waitcnt lgkmcnt(4)\n"
    "ds_write_addtid_b32 v168 offset:4096\n"
    "v_exp_f32_e32 v174, v174\n"
    "v_mfma_f32_16x16x32_bf16 v[8:11], a[176:179], v[200:203], v[8:11]\n"
    "ds_read_b64_tr_b16 a[168:169], v245 offset:20480\n"
    "ds_read_b64_tr_b16 a[170:171], v245 offset:22528\n"
    "buffer_load_dwordx4 a[32:35], v253, s[8:11], s48 offen offset:3072 sc1\n"
    "v_mfma_f32_16x16x32_bf16 v[8:11], a[180:183], v[204:207], v[8:11]\n"
    "s_add_u32 s48, s56, 20480\n"
    "ds_write_addtid_b32 v169 offset:4352\n"
    "v_exp_f32_e32 v175, v175\n"
    "v_mfma_f32_16x16x32_bf16 v[32:35], a[180:183], v[212:215], v[32:35]\n"
    "ds_read_b64_tr_b16 a[172:173], v245 offset:28672\n"
    "ds_read_b64_tr_b16 a[174:175], v245 offset:30720\n"
    "v_cvt_pk_bf16_f32 v170, v172, v173\n"
    "v_mfma_f32_16x16x32_bf16 v[32:35], a[176:179], v[208:211], v[32:35]\n"
    "v_exp_f32_e32 v164, v164\n"
    "v_mfma_f32_16x16x32_bf16 v[36:39], a[184:187], v[208:211], v[36:39]\n"
    "ds_read_b64_tr_b16 a[176:177], v246 offset:20480\n"
    "ds_read_b64_tr_b16 a[178:179], v246 offset:22528\n"
    "buffer_load_dwordx4 a[4:7], v253, s[8:11], s48 offen offset:0 sc1\n"
    "v_mfma_f32_16x16x32_bf16 v[36:39], a[188:191], v[212:215], v[36:39]\n"
    "v_exp_f32_e32 v165, v165\n"
    "v_mfma_f32_16x16x32_bf16 v[12:15], a[188:191], v[204:207], v[12:15]\n"
    "ds_read_b64_tr_b16 a[180:181], v246 offset:28672\n"
    "ds_read_b64_tr_b16 a[182:183], v246 offset:30720\n"
    "v_mfma_f32_16x16x32_bf16 v[12:15], a[184:187], v[200:203], v[12:15]\n"
    "s_waitcnt lgkmcnt(4)\n"
    "ds_write_addtid_b32 v160 offset:0\n"
    "v_cvt_pk_bf16_f32 v171, v174, v175\n"
    "v_mfma_f32_16x16x32_bf16 v[16:19], a[160:163], v[200:203], v[16:19]\n"
    "ds_read_b64_tr_b16 a[184:185], v247 offset:20480\n"
    "ds_read_b64_tr_b16 a[186:187], v247 offset:22528\n"
    "buffer_load_dwordx4 a[36:39], v253, s[8:11], s48 offen offset:1024 sc1\n"
    "v_mfma_f32_16x16x32_bf16 v[16:19], a[164:167], v[204:207], v[16:19]\n"
    "v_exp_f32_e32 v166, v166\n"
    "v_mfma_f32_16x16x32_bf16 v[40:43], a[164:167], v[212:215], v[40:43]\n"
    "ds_read_b64_tr_b16 a[188:189], v247 offset:28672\n"
    "ds_read_b64_tr_b16 a[190:191], v247 offset:30720\n"
    "v_mfma_f32_16x16x32_bf16 v[40:43], a[160:163], v[208:211], v[40:43]\n"
    "v_exp_f32_e32 v167, v167\n"
    "v_mfma_f32_16x16x32_bf16 v[44:47], a[168:171], v[208:211], v[44:47]\n"
    "ds_read_b64_tr_b16 a[160:161], v244 offset:32768\n"
    "buffer_load_dwordx4 a[8:11], v253, s[8:11], s48 offen offset:2048 sc1\n"
    "v_cvt_pk_bf16_f32 v162, v164, v165\n"
    "v_mfma_f32_16x16x32_bf16 v[44:47], a[172:175], v[212:215], v[44:47]\n"
    "ds_read_b64_tr_b16 a[162:163], v244 offset:34816\n"
    "v_cvt_pk_bf16_f32 v163, v166, v167\n"
    "v_mfma_f32_16x16x32_bf16 v[20:23], a[172:175], v[204:207], v[20:23]\n"
    "ds_read_b64_tr_b16 a[164:165], v244 offset:40960\n"
    "ds_write_addtid_b32 v161 offset:256\n"
    "v_mfma_f32_16x16x32_bf16 v[20:23], a[168:171], v[200:203], v[20:23]\n"
    "s_waitcnt lgkmcnt(4)\n"
    "ds_read_b64_tr_b16 a[166:167], v244 offset:43008\n"
    "ds_write_addtid_b32 v170 offset:4608\n"
    "v_mfma_f32_16x16x32_bf16 v[24:27], a[176:179], v[200:203], v[24:27]\n"
    "ds_read_b64_tr_b16 a[168:169], v245 offset:32768\n"
    "ds_write_addtid_b32 v171 offset:4864\n"
    "buffer_load_dwordx4 a[40:43], v253, s[8:11], s48 offen offset:3072 sc1\n"
    "v_mfma_f32_16x16x32_bf16 v[24:27], a[180:183], v[204:207], v[24:27]\n"
    "s_add_u32 s48, s56, 24576\n"
    "ds_read_b64_tr_b16 a[170:171], v245 offset:34816\n"
    "ds_write_addtid_b32 v162 offset:512\n"
    "v_mfma_f32_16x16x32_bf16 v[48:51], a[180:183], v[212:215], v[48:51]\n"
    "ds_read_b64_tr_b16 a[172:173], v245 offset:40960\n"
    "ds_write_addtid_b32 v163 offset:768\n"
    "v_mfma_f32_16x16x32_bf16 v[48:51], a[176:179], v[208:211], v[48:51]\n"
    "ds_read_b64_tr_b16 a[174:175], v245 offset:43008\n"
    "v_and_b32_e32 v242, 0x400, v240\n"
    "v_mfma_f32_16x16x32_bf16 v[52:55], a[184:187], v[208:211], v[52:55]\n"
    "ds_read_b64_tr_b16 a[176:177], v246 offset:32768\n"
    "buffer_load_dwordx4 a[12:15], v253, s[8:11], s48 offen offset:0 sc1\n"
    "v_and_b32_e32 v240, 0x3ff, v240\n"
    "v_mfma_f32_16x16x32_bf16 v[52:55], a[188:191], v[212:215], v[52:55]\n"
    "s_waitcnt lgkmcnt(2)\n"
    "s_barrier\n"
    "v_mfma_f32_16x16x32_bf16 v[28:31], a[188:191], v[204:207], v[28:31]\n"
    "ds_read_b64_tr_b16 a[178:179], v246 offset:34816\n"
    "v_lshl_or_b32 v240, v242, 2, v240\n"
    "v_mfma_f32_16x16x32_bf16 v[28:31], a[184:187], v[200:203], v[28:31]\n"
    "s_waitcnt lgkmcnt(0)\n"
    "ds_read_b64_tr_b16 a[180:181], v246 offset:40960\n"
    "ds_read2st64_b32 v[164:165], v250 offset0:0 offset1:1\n"
    "v_mfma_f32_16x16x32_bf16 v[0:3], a[160:163], v[160:163], v[0:3]\n"
    "ds_read_b64_tr_b16 a[182:183], v246 offset:43008\n"
    "ds_read2st64_b32 v[166:167], v250 offset0:2 offset1:3\n"
    "buffer_load_dwordx4 a[44:47], v253, s[8:11], s48 offen offset:1024 sc1\n"
    "v_mfma_f32_16x16x32_bf16 a[248:251], a[160:163], v[168:171], a[248:251]\n"
    "ds_read_b64_tr_b16 a[184:185], v247 offset:32768\n"
    "ds_read2st64_b32 v[172:173], v250 offset0:16 offset1:17\n"
    "v_mfma_f32_16x16x32_bf16 a[252:255], a[168:171], v[168:171], a[252:255]\n"
    "ds_read_b64_tr_b16 a[186:187], v247 offset:34816\n"
    "ds_read2st64_b32 v[174:175], v250 offset0:18 offset1:19\n"
    "v_mfma_f32_16x16x32_bf16 v[4:7], a[168:171], v[160:163], v[4:7]\n"
    "ds_read_b64_tr_b16 a[188:189], v247 offset:40960\n"
    "v_mfma_f32_16x16x32_bf16 v[8:11], a[176:179], v[160:163], v[8:11]\n"
    "ds_read_b64_tr_b16 a[190:191], v247 offset:43008\n"
    "buffer_load_dwordx4 a[16:19], v253, s[8:11], s48 offen offset:2048 sc1\n"
    "v_mfma_f32_16x16x32_bf16 v[32:35], a[176:179], v[168:171], v[32:35]\n"
    "s_waitcnt lgkmcnt(2)\n"
    "ds_read_b64_tr_b16 a[160:161], v244 offset:36864\n"
    "v_xor_b32_e32 v241, 64, v240\n"
    "v_mfma_f32_16x16x32_bf16 v[0:3], a[164:167], v[164:167], v[0:3]\n"
    "ds_read_b64_tr_b16 a[162:163], v244 offset:38912\n"
    "v_mfma_f32_16x16x32_bf16 a[248:251], a[164:167], v[172:175], a[248:251]\n"
    "s_waitcnt lgkmcnt(2)\n"
    "ds_read_b64_tr_b16 a[164:165], v244 offset:45056\n"
    "v_mfma_f32_16x16x32_bf16 a[252:255], a[172:175], v[172:175], a[252:255]\n"
    "ds_read_b64_tr_b16 a[166:167], v244 offset:47104\n"
    "buffer_load_dwordx4 a[48:51], v253, s[8:11], s48 offen offset:3072 sc1\n"
    "v_mfma_f32_16x16x32_bf16 v[4:7], a[172:175], v[164:167], v[4:7]\n"
    "s_add_u32 s48, s56, 28672\n"
    "ds_read_b64_tr_b16 a[168:169], v245 offset:36864\n"
    "v_mfma_f32_16x16x32_bf16 v[8:11], a[180:183], v[164:167], v[8:11]\n"
    "ds_read_b64_tr_b16 a[170:171], v245 offset:38912\n"
    "v_mfma_f32_16x16x32_bf16 v[32:35], a[180:183], v[172:175], v[32:35]\n"
    "ds_read_b64_tr_b16 a[172:173], v245 offset:45056\n"
    "v_accvgpr_read_b32 v216, a248\n"
    "v_mfma_f32_16x16x32_bf16 v[36:39], a[184:187], v[168:171], v[36:39]\n"
    "ds_read_b64_tr_b16 a[174:175], v245 offset:47104\n"
    "buffer_load_dwordx4 a[20:23], v253, s[8:11], s48 offen offset:0 sc1\n"
    "v_mfma_f32_16x16x32_bf16 v[36:39], a[188:191], v[172:175], v[36:39]\n"
    "s_lshl_b32 s45, s51, 16\n"
    "ds_read_b64_tr_b16 a[176:177], v246 offset:36864\n"
    "v_accvgpr_read_b32 v217, a249\n"
    "v_mfma_f32_16x16x32_bf16 v[12:15], a[188:191], v[164:167], v[12:15]\n"
    "ds_read_b64_tr_b16 a[178:179], v246 offset:38912\n"
    "buffer_load_dword v146, v145, s[8:11], s47 offen offset:128 sc1\n"
    "v_accvgpr_read_b32 v218, a250\n"
    "v_mfma_f32_16x16x32_bf16 v[12:15], a[184:187], v[160:163], v[12:15]\n"
    "s_waitcnt lgkmcnt(2)\n"
    "ds_read_b64_tr_b16 a[180:181], v246 offset:45056\n"
    "v_accvgpr_read_b32 v219, a251\n"
    "v_mfma_f32_16x16x32_bf16 v[16:19], a[160:163], v[160:163], v[16:19]\n"
    "ds_read_b64_tr_b16 a[182:183], v246 offset:47104\n"
    "buffer_load_dwordx4 a[52:55], v253, s[8:11], s48 offen offset:1024 sc1\n"
    "v_mfma_f32_16x16x32_bf16 v[16:19], a[164:167], v[164:167], v[16:19]\n"
    "s_lshl_b32 s46, s50, 11\n"
    "ds_read_b64_tr_b16 a[184:185], v247 offset:36864\n"
    "v_accvgpr_read_b32 v220, a252\n"
    "v_mfma_f32_16x16x32_bf16 v[40:43], a[164:167], v[172:175], v[40:43]\n"
    "s_add_u32 s46, s46, s45\n"
    "ds_read_b64_tr_b16 a[186:187], v247 offset:38912\n"
    "v_accvgpr_read_b32 v221, a253\n"
    "v_mfma_f32_16x16x32_bf16 v[40:43], a[160:163], v[168:171], v[40:43]\n"
    "s_add_u32 s46, s46, %3\n"
    "ds_read_b64_tr_b16 a[188:189], v247 offset:45056\n"
    "v_accvgpr_read_b32 v222, a254\n"
    "v_mfma_f32_16x16x32_bf16 v[44:47], a[168:171], v[168:171], v[44:47]\n"
    "ds_read_b64_tr_b16 a[190:191], v247 offset:47104\n"
    "buffer_load_dwordx4 a[24:27], v253, s[8:11], s48 offen offset:2048 sc1\n"
    "v_mfma_f32_16x16x32_bf16 v[44:47], a[172:175], v[172:175], v[44:47]\n"
    "s_bitcmp0_b32 %1, 0\n"
    "s_cbranch_scc1 .Lat_two\n"
    "s_add_u32 s46, s46, 81920\n"
    "ds_read_b128 a[160:163], v241 offset:11264\n"
    "v_accvgpr_read_b32 v223, a255\n"
    "v_mfma_f32_16x16x32_bf16 v[20:23], a[172:175], v[164:167], v[20:23]\n"
    "ds_read_b128 a[164:167], v241 offset:19456\n"
    "v_cvt_pk_bf16_f32 v216, v216, v217\n"
    "v_mfma_f32_16x16x32_bf16 v[20:23], a[168:171], v[160:163], v[20:23]\n"
    "s_waitcnt lgkmcnt(2)\n"
    "ds_read_b128 a[168:171], v241 offset:27648\n"
    "v_cvt_pk_bf16_f32 v217, v218, v219\n"
    "v_mfma_f32_16x16x32_bf16 v[24:27], a[176:179], v[160:163], v[24:27]\n"
    "ds_read_b128 a[172:175], v240 offset:8192\n"
    "buffer_load_dwordx4 a[56:59], v253, s[8:11], s48 offen offset:3072 sc1\n"
    "v_mfma_f32_16x16x32_bf16 v[24:27], a[180:183], v[164:167], v[24:27]\n"
    "v_cvt_pk_bf16_f32 v218, v220, v221\n"
    "v_cvt_pk_bf16_f32 v219, v222, v223\n"
    "v_mfma_f32_16x16x32_bf16 v[48:51], a[180:183], v[172:175], v[48:51]\n"
    "buffer_store_dwordx4 v[216:219], v253, s[8:11], s83 offen offset:0 sc1\n"
    "v_cvt_pk_bf16_f32 v32, v32, v33\n"
    "v_cvt_pk_bf16_f32 v33, v34, v35\n"
    "v_mfma_f32_16x16x32_bf16 v[48:51], a[176:179], v[168:171], v[48:51]\n"
    "ds_read_b128 a[176:179], v240 offset:16384\n"
    "v_cvt_pk_bf16_f32 v34, v36, v37\n"
    "v_mfma_f32_16x16x32_bf16 v[52:55], a[184:187], v[168:171], v[52:55]\n"
    "ds_read_b128 a[180:183], v240 offset:24576\n"
    "v_cvt_pk_bf16_f32 v35, v38, v39\n"
    "v_mfma_f32_16x16x32_bf16 v[52:55], a[188:191], v[172:175], v[52:55]\n"
    "buffer_store_dwordx4 v[32:35], v253, s[8:11], s83 offen offset:1024 sc1\n"
    "v_cvt_pk_bf16_f32 v40, v40, v41\n"
    "v_cvt_pk_bf16_f32 v41, v42, v43\n"
    "v_mfma_f32_16x16x32_bf16 v[28:31], a[188:191], v[164:167], v[28:31]\n"
    "v_cvt_pk_bf16_f32 v42, v44, v45\n"
    "v_cvt_pk_bf16_f32 v43, v46, v47\n"
    "v_mfma_f32_16x16x32_bf16 v[28:31], a[184:187], v[160:163], v[28:31]\n"
    "ds_read_b128 a[184:187], v241 offset:8192\n"
    "buffer_store_dwordx4 v[40:43], v253, s[8:11], s83 offen offset:2048 sc1\n"
    "v_cvt_pk_bf16_f32 v48, v48, v49\n"
    "v_add_u32_e32 v158, s46, v253\n"
    "v_mfma_f32_16x16x32_bf16 v[160:163], v[154:157], a[224:227], 0\n"
    "ds_read_b128 a[188:191], v241 offset:16384\n"
    "v_cvt_pk_bf16_f32 v49, v50, v51\n"
    "v_mfma_f32_16x16x32_bf16 v[164:167], v[152:155], a[224:227], 0\n"
    "v_cvt_pk_bf16_f32 v50, v52, v53\n"
    "v_cvt_pk_bf16_f32 v51, v54, v55\n"
    "v_mfma_f32_16x16x32_bf16 v[168:171], v[154:157], a[228:231], 0\n"
    "s_add_u32 s45, %3, 155840\n"
    "buffer_store_dwordx4 v[48:51], v253, s[8:11], s83 offen offset:3072 sc1\n"
    "v_and_b32_e32 v150, 15, v147\n"
    "v_lshlrev_b32_e32 v151, 9, v150\n"
    "v_mfma_f32_16x16x32_bf16 v[172:175], v[152:155], a[228:231], 0\n"
    "buffer_store_dword v55, v251, s[8:11], s60 offen offset:0 sc1\n"
    "v_lshl_add_u32 v150, v150, 2, s45\n"
    "v_and_or_b32 v151, v147, 48, v151\n"
    "v_mfma_f32_16x16x32_bf16 v[176:179], v[154:157], a[232:235], 0\n"
    "s_lshl_b32 s46, %2, 7\n"
    "s_add_u32 s58, s35, s46\n"
    "s_mul_i32 s45, %1, 74496\n"
    "v_mfma_f32_16x16x32_bf16 v[180:183], v[152:155], a[232:235], 0\n"
    "s_add_u32 s45, s45, 16265216\n"
    "ds_write_b128 v159, v[216:219] offset:0\n"
    "v_mfma_f32_16x16x32_bf16 v[184:187], v[154:157], a[236:239], 0\n"
    "s_lshl_b32 s54, %2, 11\n"
    "v_and_b32_e32 v147, 0xf0, v253\n"
    "v_add_u32_e32 v252, %3, v253\n"
    "v_mfma_f32_16x16x32_bf16 v[188:191], v[152:155], a[236:239], 0\n"
    "s_add_u32 s54, s54, s45\n"
    "ds_write_b128 v159, v[32:35] offset:1024\n"
    "v_mfma_f32_16x16x32_bf16 v[192:195], v[154:157], a[240:243], 0\n"
    "s_add_u32 s54, s54, 16384\n"
    "ds_write_b128 v159, v[40:43] offset:2048\n"
    "v_mfma_f32_16x16x32_bf16 v[196:199], v[152:155], a[240:243], 0\n"
    "s_lshl1_add_u32 s59, s50, s51\n"
    "s_lshl_b32 s59, s59, 12\n"
    "v_mfma_f32_16x16x32_bf16 v[200:203], v[154:157], a[244:247], 0\n"
    "s_add_u32 s46, s59, s45\n"
    "ds_write_b128 v159, v[48:51] offset:3072\n"
    "v_mfma_f32_16x16x32_bf16 v[204:207], v[152:155], a[244:247], 0\n"
    "s_waitcnt lgkmcnt(10)\n"
    "s_bitcmp1_b32 %1, 0\n"
    "s_cselect_b32 s54, s54, 2.0\n"
    "s_cselect_b32 s55, s58, 2.0\n"
    "v_mfma_f32_16x16x32_bf16 v[160:163], a[192:195], a[160:163], v[160:163]\n"
    "ds_write_b32 v254, v55 offset:0\n"
    "v_mfma_f32_16x16x32_bf16 v[164:167], a[196:199], a[160:163], v[164:167]\n"
    "s_add_u32 s61, s54, 0\n"
    "ds_write_b32 v255, v31 offset:0\n"
    "v_mfma_f32_16x16x32_bf16 v[168:171], a[200:203], a[160:163], v[168:171]\n"
    "s_waitcnt vmcnt(8)\n"
    "v_cmp_ne_u32_e32 vcc, s39, v146\n"
    "v_mfma_f32_16x16x32_bf16 v[172:175], a[204:207], a[160:163], v[172:175]\n"
    "ds_read_b128 a[160:163], v241 offset:24576\n"
    "v_mfma_f32_16x16x32_bf16 v[176:179], a[192:195], a[164:167], v[176:179]\n"
    "s_cbranch_vccnz .Lat_xpoll\n"
    ".Lat_xpoll_ret:\n"
    "v_mfma_f32_16x16x32_bf16 v[180:183], a[196:199], a[164:167], v[180:183]\n"
    "v_mfma_f32_16x16x32_bf16 v[184:187], a[200:203], a[164:167], v[184:187]\n"
    "buffer_load_dwordx4 a[224:227], v253, s[8:11], s61 offen offset:0 sc1\n"
    "v_mfma_f32_16x16x32_bf16 v[188:191], a[204:207], a[164:167], v[188:191]\n"
    "s_waitcnt lgkmcnt(11)\n"
    "ds_read_b128 a[164:167], v240 offset:9216\n"
    "v_mfma_f32_16x16x32_bf16 v[192:195], a[192:195], a[168:171], v[192:195]\n"
    "v_mfma_f32_16x16x32_bf16 v[196:199], a[196:199], a[168:171], v[196:199]\n"
    "v_mfma_f32_16x16x32_bf16 v[200:203], a[200:203], a[168:171], v[200:203]\n"
    "v_mfma_f32_16x16x32_bf16 v[204:207], a[204:207], a[168:171], v[204:207]\n"
    "ds_read_b128 a[168:171], v240 offset:17408\n"
    "buffer_load_dwordx4 a[228:231], v253, s[8:11], s61 offen offset:1024 sc1\n"
    "v_mfma_f32_16x16x32_bf16 v[160:163], a[208:211], a[172:175], v[160:163]\n"
    "s_waitcnt lgkmcnt(3)\n"
    "s_barrier\n"
    "v_mfma_f32_16x16x32_bf16 v[164:167], a[212:215], a[172:175], v[164:167]\n"
    "s_add_u32 s61, s54, 24832\n"
    "v_mfma_f32_16x16x32_bf16 v[168:171], a[216:219], a[172:175], v[168:171]\n"
    "v_mfma_f32_16x16x32_bf16 v[172:175], a[220:223], a[172:175], v[172:175]\n"
    "ds_read_b128 a[172:175], v240 offset:25600\n"
    "v_mfma_f32_16x16x32_bf16 v[176:179], a[208:211], a[176:179], v[176:179]\n"
    "buffer_load_dwordx4 a[232:235], v253, s[8:11], s61 offen offset:0 sc1\n"
    "v_mfma_f32_16x16x32_bf16 v[180:183], a[212:215], a[176:179], v[180:183]\n"
    "ds_read_b32 v232, v150 offset:0\n"
    "v_mfma_f32_16x16x32_bf16 v[184:187], a[216:219], a[176:179], v[184:187]\n"
    "v_mfma_f32_16x16x32_bf16 v[188:191], a[220:223], a[176:179], v[188:191]\n"
    "ds_read_b128 a[176:179], v241 offset:9216\n"
    "v_mfma_f32_16x16x32_bf16 v[192:195], a[208:211], a[180:183], v[192:195]\n"
    "v_mfma_f32_16x16x32_bf16 v[196:199], a[212:215], a[180:183], v[196:199]\n"
    "buffer_load_dwordx4 a[236:239], v253, s[8:11], s61 offen offset:1024 sc1\n"
    "v_mfma_f32_16x16x32_bf16 v[200:203], a[216:219], a[180:183], v[200:203]\n"
    "s_add_u32 s61, s54, 49664\n"
    "ds_read_b32 v233, v150 offset:256\n"
    "v_mfma_f32_16x16x32_bf16 v[204:207], a[220:223], a[180:183], v[204:207]\n"
    "ds_read_b128 a[180:183], v241 offset:17408\n"
    "v_mfma_f32_16x16x32_bf16 v[160:163], a[92:95], a[184:187], v[160:163]\n"
    "v_mfma_f32_16x16x32_bf16 v[164:167], a[124:127], a[184:187], v[164:167]\n"
    "v_mfma_f32_16x16x32_bf16 v[168:171], a[156:159], a[184:187], v[168:171]\n"
    "buffer_load_dwordx4 a[240:243], v253, s[8:11], s61 offen offset:0 sc1\n"
    "v_mfma_f32_16x16x32_bf16 v[172:175], a[64:67], a[184:187], v[172:175]\n"
    "ds_read_b128 a[184:187], v241 offset:25600\n"
    "v_mfma_f32_16x16x32_bf16 v[176:179], a[92:95], a[188:191], v[176:179]\n"
    "ds_read_b32 v234, v150 offset:512\n"
    "v_mfma_f32_16x16x32_bf16 v[180:183], a[124:127], a[188:191], v[180:183]\n"
    "v_mfma_f32_16x16x32_bf16 v[184:187], a[156:159], a[188:191], v[184:187]\n"
    "v_mfma_f32_16x16x32_bf16 v[188:191], a[64:67], a[188:191], v[188:191]\n"
    "s_waitcnt lgkmcnt(8)\n"
    "ds_read_b128 a[188:191], v240 offset:10240\n"
    "v_mfma_f32_16x16x32_bf16 v[192:195], a[92:95], a[160:163], v[192:195]\n"
    "buffer_load_dwordx4 a[244:247], v253, s[8:11], s61 offen offset:1024 sc1\n"
    "v_mfma_f32_16x16x32_bf16 v[196:199], a[124:127], a[160:163], v[196:199]\n"
    "s_add_u32 s61, s46, 0\n"
    "ds_read_b128 a[248:251], v158 offset:0\n"
    "v_mfma_f32_16x16x32_bf16 v[200:203], a[156:159], a[160:163], v[200:203]\n"
    "v_mfma_f32_16x16x32_bf16 v[204:207], a[64:67], a[160:163], v[204:207]\n"
    "ds_read_b128 a[160:163], v240 offset:18432\n"
    "v_mfma_f32_16x16x32_bf16 v[160:163], a[96:99], a[164:167], v[160:163]\n"
    "s_waitcnt vmcnt(6)\n"
    "buffer_store_dword v148, v149, s[8:11], s87 offen offset:0 sc1\n"
    "v_mfma_f32_16x16x32_bf16 v[164:167], a[128:131], a[164:167], v[164:167]\n"
    "buffer_load_dwordx4 a[192:195], v253, s[8:11], s61 offen offset:0 sc1\n"
    "v_mfma_f32_16x16x32_bf16 v[168:171], a[68:71], a[164:167], v[168:171]\n"
    "ds_read_b128 a[252:255], v158 offset:1024\n"
    "v_mfma_f32_16x16x32_bf16 v[172:175], a[100:103], a[164:167], v[172:175]\n"
    "s_waitcnt lgkmcnt(10)\n"
    "ds_read_b128 a[164:167], v240 offset:26624\n"
    "v_mfma_f32_16x16x32_bf16 v[176:179], a[96:99], a[168:171], v[176:179]\n"
    "v_mfma_f32_16x16x32_bf16 v[180:183], a[128:131], a[168:171], v[180:183]\n"
    "v_mfma_f32_16x16x32_bf16 v[184:187], a[68:71], a[168:171], v[184:187]\n"
    "buffer_load_dwordx4 a[196:199], v253, s[8:11], s61 offen offset:1024 sc1\n"
    "v_mfma_f32_16x16x32_bf16 v[188:191], a[100:103], a[168:171], v[188:191]\n"
    "s_lshl_b32 s52, %2, 11\n"
    "ds_read_b128 a[168:171], v241 offset:10240\n"
    "v_mfma_f32_16x16x32_bf16 v[192:195], a[96:99], a[172:175], v[192:195]\n"
    "v_mfma_f32_16x16x32_bf16 v[196:199], a[128:131], a[172:175], v[196:199]\n"
    "v_mfma_f32_16x16x32_bf16 v[200:203], a[68:71], a[172:175], v[200:203]\n"
    "v_mfma_f32_16x16x32_bf16 v[204:207], a[100:103], a[172:175], v[204:207]\n"
    "s_waitcnt lgkmcnt(8)\n"
    "ds_read_b128 a[172:175], v241 offset:18432\n"
    "v_mfma_f32_16x16x32_bf16 v[160:163], a[132:135], a[176:179], v[160:163]\n"
    "buffer_load_dwordx4 a[200:203], v253, s[8:11], s61 offen offset:2048 sc1\n"
    "v_mfma_f32_16x16x32_bf16 v[164:167], a[72:75], a[176:179], v[164:167]\n"
    "v_add_u32_e32 v251, s52, v252\n"
    "v_mfma_f32_16x16x32_bf16 v[168:171], a[104:107], a[176:179], v[168:171]\n"
    "v_mfma_f32_16x16x32_bf16 v[172:175], a[136:139], a[176:179], v[172:175]\n"
    "ds_read_b128 a[176:179], v241 offset:26624\n"
    "v_mfma_f32_16x16x32_bf16 v[176:179], a[132:135], a[180:183], v[176:179]\n"
    "v_mfma_f32_16x16x32_bf16 v[180:183], a[72:75], a[180:183], v[180:183]\n"
    "buffer_load_dwordx4 a[204:207], v253, s[8:11], s61 offen offset:3072 sc1\n"
    "v_mfma_f32_16x16x32_bf16 v[184:187], a[104:107], a[180:183], v[184:187]\n"
    "s_add_u32 s61, s46, 24832\n"
    "v_add_u32_e32 v158, s59, v252\n"
    "v_mfma_f32_16x16x32_bf16 v[188:191], a[136:139], a[180:183], v[188:191]\n"
    "s_waitcnt lgkmcnt(7)\n"
    "ds_read_b128 a[180:183], v240 offset:11264\n"
    "v_mfma_f32_16x16x32_bf16 v[192:195], a[132:135], a[184:187], v[192:195]\n"
    "v_mfma_f32_16x16x32_bf16 v[196:199], a[72:75], a[184:187], v[196:199]\n"
    "v_mfma_f32_16x16x32_bf16 v[200:203], a[104:107], a[184:187], v[200:203]\n"
    "buffer_load_dwordx4 a[208:211], v253, s[8:11], s61 offen offset:0 sc1\n"
    "v_mfma_f32_16x16x32_bf16 v[204:207], a[136:139], a[184:187], v[204:207]\n"
    "ds_read_b128 a[184:187], v240 offset:19456\n"
    "v_mfma_f32_16x16x32_bf16 v[160:163], a[76:79], a[188:191], v[160:163]\n"
    "v_and_b32_e32 v145, 12, v145\n"
    "v_mfma_f32_16x16x32_bf16 v[164:167], a[108:111], a[188:191], v[164:167]\n"
    "v_mfma_f32_16x16x32_bf16 v[168:171], a[140:143], a[188:191], v[168:171]\n"
    "v_mfma_f32_16x16x32_bf16 v[172:175], a[80:83], a[188:191], v[172:175]\n"
    "s_waitcnt lgkmcnt(5)\n"
    "ds_read_b128 a[188:191], v240 offset:27648\n"
    "v_mfma_f32_16x16x32_bf16 v[176:179], a[76:79], a[160:163], v[176:179]\n"
    "buffer_load_dwordx4 a[212:215], v253, s[8:11], s61 offen offset:1024 sc1\n"
    "v_mfma_f32_16x16x32_bf16 v[180:183], a[108:111], a[160:163], v[180:183]\n"
    "s_add_u32 s52, s32, 3\n"
    "v_mfma_f32_16x16x32_bf16 v[184:187], a[140:143], a[160:163], v[184:187]\n"
    "v_mfma_f32_16x16x32_bf16 v[188:191], a[80:83], a[160:163], v[188:191]\n"
    "v_mfma_f32_16x16x32_bf16 v[192:195], a[76:79], a[164:167], v[192:195]\n"
    "v_mfma_f32_16x16x32_bf16 v[196:199], a[108:111], a[164:167], v[196:199]\n"
    "buffer_load_dwordx4 a[216:219], v253, s[8:11], s61 offen offset:2048 sc1\n"
    "v_mfma_f32_16x16x32_bf16 v[200:203], a[140:143], a[164:167], v[200:203]\n"
    "s_and_b32 s52, s52, 3\n"
    "v_mfma_f32_16x16x32_bf16 v[204:207], a[80:83], a[164:167], v[204:207]\n"
    "s_waitcnt lgkmcnt(4)\n"
    "v_mfma_f32_16x16x32_bf16 v[160:163], a[112:115], a[168:171], v[160:163]\n"
    "v_mfma_f32_16x16x32_bf16 v[164:167], a[144:147], a[168:171], v[164:167]\n"
    "v_mfma_f32_16x16x32_bf16 v[168:171], a[84:87], a[168:171], v[168:171]\n"
    "buffer_load_dwordx4 a[220:223], v253, s[8:11], s61 offen offset:3072 sc1\n"
    "v_mfma_f32_16x16x32_bf16 v[172:175], a[116:119], a[168:171], v[172:175]\n"
    "s_add_u32 s61, s45, 24576\n"
    "v_mfma_f32_16x16x32_bf16 v[176:179], a[112:115], a[172:175], v[176:179]\n"
    "s_lshl_b32 s52, s52, 8\n"
    "v_mfma_f32_16x16x32_bf16 v[180:183], a[144:147], a[172:175], v[180:183]\n"
    "v_mfma_f32_16x16x32_bf16 v[184:187], a[84:87], a[172:175], v[184:187]\n"
    "v_mfma_f32_16x16x32_bf16 v[188:191], a[116:119], a[172:175], v[188:191]\n"
    "s_waitcnt lgkmcnt(2)\n"
    "buffer_load_dwordx3 v[236:238], v147, s[8:11], s61 offen offset:0 sc1\n"
    "v_mfma_f32_16x16x32_bf16 v[192:195], a[112:115], a[176:179], v[192:195]\n"
    "s_add_u32 s61, s45, 49408\n"
    "v_mfma_f32_16x16x32_bf16 v[196:199], a[144:147], a[176:179], v[196:199]\n"
    "s_add_u32 s52, s52, s47\n"
    "v_mfma_f32_16x16x32_bf16 v[200:203], a[84:87], a[176:179], v[200:203]\n"
    "v_mfma_f32_16x16x32_bf16 v[204:207], a[116:119], a[176:179], v[204:207]\n"
    "v_mfma_f32_16x16x32_bf16 v[160:163], a[148:151], a[180:183], v[160:163]\n"
    "buffer_load_dwordx3 v[248:250], v147, s[8:11], s61 offen offset:0 sc1\n"
    "v_mfma_f32_16x16x32_bf16 v[164:167], a[88:91], a[180:183], v[164:167]\n"
    "v_mfma_f32_16x16x32_bf16 v[168:171], a[120:123], a[180:183], v[168:171]\n"
    "v_mfma_f32_16x16x32_bf16 v[172:175], a[152:155], a[180:183], v[172:175]\n"
    "s_waitcnt lgkmcnt(0)\n"
    "v_mfma_f32_16x16x32_bf16 v[176:179], a[148:151], a[184:187], v[176:179]\n"
    "v_mfma_f32_16x16x32_bf16 v[180:183], a[88:91], a[184:187], v[180:183]\n"
    "v_mfma_f32_16x16x32_bf16 v[184:187], a[120:123], a[184:187], v[184:187]\n"
    "v_mfma_f32_16x16x32_bf16 v[188:191], a[152:155], a[184:187], v[188:191]\n"
    "v_mfma_f32_16x16x32_bf16 v[192:195], a[148:151], a[188:191], v[192:195]\n"
    "v_mfma_f32_16x16x32_bf16 v[196:199], a[88:91], a[188:191], v[196:199]\n"
    "v_mfma_f32_16x16x32_bf16 v[200:203], a[120:123], a[188:191], v[200:203]\n"
    "v_mfma_f32_16x16x32_bf16 v[204:207], a[152:155], a[188:191], v[204:207]\n"
    "v_mfma_f32_16x16x32_bf16 v[128:131], v[154:157], a[248:251], 0\n"
    "v_mfma_f32_16x16x32_bf16 v[132:135], v[152:155], a[248:251], 0\n"
    "v_mfma_f32_16x16x32_bf16 v[136:139], v[154:157], a[252:255], 0\n"
    "v_mfma_f32_16x16x32_bf16 v[140:143], v[152:155], a[252:255], 0\n"
    "s_waitcnt vmcnt(15)\n"
    "v_mfma_f32_16x16x32_bf16 v[128:131], v[154:157], a[224:227], v[128:131]\n"
    "v_mfma_f32_16x16x32_bf16 v[132:135], v[152:155], a[224:227], v[132:135]\n"
    "v_mfma_f32_16x16x32_bf16 v[136:139], v[154:157], a[228:231], v[136:139]\n"
    "buffer_load_dword v144, v145, s[8:11], s52 offen offset:0 sc1\n"
    "v_mfma_f32_16x16x32_bf16 v[140:143], v[152:155], a[228:231], v[140:143]\n"
    "s_waitcnt vmcnt(14)\n"
    "v_mfma_f32_16x16x32_bf16 v[128:131], v[154:157], a[232:235], v[128:131]\n"
    "v_mfma_f32_16x16x32_bf16 v[132:135], v[152:155], a[232:235], v[132:135]\n"
    "v_mfma_f32_16x16x32_bf16 v[136:139], v[154:157], a[236:239], v[136:139]\n"
    "v_mfma_f32_16x16x32_bf16 v[140:143], v[152:155], a[236:239], v[140:143]\n"
    "s_waitcnt vmcnt(12)\n"
    "v_mfma_f32_16x16x32_bf16 v[128:131], v[154:157], a[240:243], v[128:131]\n"
    "v_mfma_f32_16x16x32_bf16 v[132:135], v[152:155], a[240:243], v[132:135]\n"
    "v_mfma_f32_16x16x32_bf16 v[136:139], v[154:157], a[244:247], v[136:139]\n"
    "v_mfma_f32_16x16x32_bf16 v[140:143], v[152:155], a[244:247], v[140:143]\n"
    "s_waitcnt vmcnt(9)\n"
    "v_mfma_f32_16x16x32_bf16 v[0:3], v[154:157], a[192:195], v[0:3]\n"
    "v_cvt_pk_bf16_f32 v128, v128, v129\n"
    "v_mfma_f32_16x16x32_bf16 v[4:7], v[152:155], a[192:195], v[4:7]\n"
    "v_cvt_pk_bf16_f32 v129, v130, v131\n"
    "v_mfma_f32_16x16x32_bf16 v[8:11], v[154:157], a[196:199], v[8:11]\n"
    "v_cvt_pk_bf16_f32 v130, v132, v133\n"
    "v_mfma_f32_16x16x32_bf16 v[12:15], v[152:155], a[196:199], v[12:15]\n"
    "s_waitcnt vmcnt(7)\n"
    "v_cvt_pk_bf16_f32 v131, v134, v135\n"
    "v_mfma_f32_16x16x32_bf16 v[16:19], v[154:157], a[200:203], v[16:19]\n"
    "ds_write_b128 v251, v[128:131] offset:49152\n"
    "v_mfma_f32_16x16x32_bf16 v[20:23], v[152:155], a[200:203], v[20:23]\n"
    "v_cvt_pk_bf16_f32 v136, v136, v137\n"
    "v_mfma_f32_16x16x32_bf16 v[24:27], v[154:157], a[204:207], v[24:27]\n"
    "v_cvt_pk_bf16_f32 v137, v138, v139\n"
    "v_mfma_f32_16x16x32_bf16 v[28:31], v[152:155], a[204:207], v[28:31]\n"
    "s_waitcnt vmcnt(5)\n"
    "v_cvt_pk_bf16_f32 v138, v140, v141\n"
    "v_cvt_pk_bf16_f32 v139, v142, v143\n"
    "v_mfma_f32_16x16x32_bf16 v[0:3], v[154:157], a[208:211], v[0:3]\n"
    "ds_write_b128 v251, v[136:139] offset:50176\n"
    "v_mfma_f32_16x16x32_bf16 v[4:7], v[152:155], a[208:211], v[4:7]\n"
    "v_mfma_f32_16x16x32_bf16 v[8:11], v[154:157], a[212:215], v[8:11]\n"
    "s_waitcnt lgkmcnt(0)\n"
    "s_barrier\n"
    "ds_read_b128 a[128:131], v252 offset:49152\n"
    "ds_read_b128 a[132:135], v252 offset:50176\n"
    "ds_read_b128 a[136:139], v252 offset:51200\n"
    "v_mfma_f32_16x16x32_bf16 v[12:15], v[152:155], a[212:215], v[12:15]\n"
    "s_waitcnt vmcnt(3)\n"
    "v_mfma_f32_16x16x32_bf16 v[16:19], v[154:157], a[216:219], v[16:19]\n"
    "ds_read_b128 a[140:143], v252 offset:52224\n"
    "v_mfma_f32_16x16x32_bf16 v[20:23], v[152:155], a[216:219], v[20:23]\n"
    "v_mfma_f32_16x16x32_bf16 v[24:27], v[154:157], a[220:223], v[24:27]\n"
    "ds_read_b128 a[144:147], v252 offset:53248\n"
    "v_mfma_f32_16x16x32_bf16 v[28:31], v[152:155], a[220:223], v[28:31]\n"
    "s_waitcnt lgkmcnt(3)\n"
    "v_mfma_f32_16x16x32_bf16 v[216:219], v[56:59], a[128:131], 0\n"
    "ds_read_b128 a[148:151], v252 offset:54272\n"
    "v_mfma_f32_16x16x32_bf16 v[220:223], v[60:63], a[128:131], 0\n"
    "ds_read_b128 a[152:155], v252 offset:55296\n"
    "v_mfma_f32_16x16x32_bf16 v[224:227], v[64:67], a[128:131], 0\n"
    "s_waitcnt vmcnt(0)\n"
    "v_cmp_ne_u32_e32 vcc, s39, v144\n"
    "s_nop 3\n"
    "s_cbranch_vccnz .Lat_ypoll\n"
    ".Lat_ypoll_ret:\n"
    "s_add_u32 s61, s46, 49664\n"
    "buffer_load_dwordx4 a[160:163], v253, s[8:11], s61 offen offset:0 sc1\n"
    "buffer_load_dwordx4 a[164:167], v253, s[8:11], s61 offen offset:1024 sc1\n"
    "buffer_load_dwordx4 a[168:171], v253, s[8:11], s61 offen offset:2048 sc1\n"
    "buffer_load_dwordx4 a[172:175], v253, s[8:11], s61 offen offset:3072 sc1\n"
    "s_add_u32 s61, s45, 74240\n"
    "buffer_load_dwordx3 v[244:246], v147, s[8:11], s61 offen offset:0 sc1\n"
    "v_mfma_f32_16x16x32_bf16 v[228:231], v[68:71], a[128:131], 0\n"
    "ds_read_b128 a[156:159], v252 offset:56320\n"
    "v_mfma_f32_16x16x32_bf16 v[216:219], v[72:75], a[132:135], v[216:219]\n"
    "v_mfma_f32_16x16x32_bf16 v[220:223], v[76:79], a[132:135], v[220:223]\n"
    "s_waitcnt vmcnt(6)\n"
    "v_mfma_f32_16x16x32_bf16 v[224:227], v[80:83], a[132:135], v[224:227]\n"
    "v_mfma_f32_16x16x32_bf16 v[228:231], v[84:87], a[132:135], v[228:231]\n"
    "s_waitcnt lgkmcnt(4)\n"
    "v_mfma_f32_16x16x32_bf16 v[216:219], v[88:91], a[136:139], v[216:219]\n"
    "v_mfma_f32_16x16x32_bf16 v[220:223], v[92:95], a[136:139], v[220:223]\n"
    "v_add_f32_e32 v232, v232, v236\n"
    "v_mfma_f32_16x16x32_bf16 v[224:227], v[96:99], a[136:139], v[224:227]\n"
    "v_mfma_f32_16x16x32_bf16 v[228:231], v[100:103], a[136:139], v[228:231]\n"
    "v_mfma_f32_16x16x32_bf16 v[216:219], v[104:107], a[140:143], v[216:219]\n"
    "v_mfma_f32_16x16x32_bf16 v[220:223], v[108:111], a[140:143], v[220:223]\n"
    "v_add_f32_e32 v233, v233, v237\n"
    "v_mfma_f32_16x16x32_bf16 v[224:227], v[112:115], a[140:143], v[224:227]\n"
    "v_mfma_f32_16x16x32_bf16 v[228:231], v[116:119], a[140:143], v[228:231]\n"
    "s_waitcnt lgkmcnt(2)\n"
    "v_mfma_f32_16x16x32_bf16 v[216:219], a[28:31], a[144:147], v[216:219]\n"
    "v_mfma_f32_16x16x32_bf16 v[220:223], a[60:63], a[144:147], v[220:223]\n"
    "v_add_f32_e32 v234, v234, v238\n"
    "v_mfma_f32_16x16x32_bf16 v[224:227], a[0:3], a[144:147], v[224:227]\n"
    "v_mfma_f32_16x16x32_bf16 v[228:231], a[32:35], a[144:147], v[228:231]\n"
    "v_mfma_f32_16x16x32_bf16 v[216:219], a[4:7], a[148:151], v[216:219]\n"
    "v_mfma_f32_16x16x32_bf16 v[220:223], a[36:39], a[148:151], v[220:223]\n"
    "v_add_f32_e32 v232, v232, v248\n"
    "v_mfma_f32_16x16x32_bf16 v[224:227], a[8:11], a[148:151], v[224:227]\n"
    "v_mfma_f32_16x16x32_bf16 v[228:231], a[40:43], a[148:151], v[228:231]\n"
    "s_waitcnt lgkmcnt(0)\n"
    "v_mfma_f32_16x16x32_bf16 v[216:219], a[12:15], a[152:155], v[216:219]\n"
    "v_mfma_f32_16x16x32_bf16 v[220:223], a[44:47], a[152:155], v[220:223]\n"
    "v_add_f32_e32 v233, v233, v249\n"
    "v_mfma_f32_16x16x32_bf16 v[224:227], a[16:19], a[152:155], v[224:227]\n"
    "v_mfma_f32_16x16x32_bf16 v[228:231], a[48:51], a[152:155], v[228:231]\n"
    "v_mfma_f32_16x16x32_bf16 v[216:219], a[20:23], a[156:159], v[216:219]\n"
    "v_mfma_f32_16x16x32_bf16 v[220:223], a[52:55], a[156:159], v[220:223]\n"
    "v_add_f32_e32 v234, v234, v250\n"
    "v_mfma_f32_16x16x32_bf16 v[224:227], a[24:27], a[156:159], v[224:227]\n"
    "v_mfma_f32_16x16x32_bf16 v[228:231], a[56:59], a[156:159], v[228:231]\n"
    "s_waitcnt vmcnt(4)\n"
    "v_mfma_f32_16x16x32_bf16 v[0:3], v[154:157], a[160:163], v[0:3]\n"
    "v_mfma_f32_16x16x32_bf16 v[4:7], v[152:155], a[160:163], v[4:7]\n"
    "s_waitcnt vmcnt(3)\n"
    "v_mfma_f32_16x16x32_bf16 v[8:11], v[154:157], a[164:167], v[8:11]\n"
    "v_mfma_f32_16x16x32_bf16 v[12:15], v[152:155], a[164:167], v[12:15]\n"
    "s_waitcnt vmcnt(2)\n"
    "v_cvt_pk_bf16_f32 v0, v0, v1\n"
    "v_cvt_pk_bf16_f32 v1, v2, v3\n"
    "v_mfma_f32_16x16x32_bf16 v[16:19], v[154:157], a[168:171], v[16:19]\n"
    "v_cvt_pk_bf16_f32 v2, v4, v5\n"
    "v_cvt_pk_bf16_f32 v3, v6, v7\n"
    "v_mfma_f32_16x16x32_bf16 v[20:23], v[152:155], a[168:171], v[20:23]\n"
    "s_waitcnt vmcnt(1)\n"
    "ds_write_b128 v158, v[0:3] offset:32768\n"
    "v_mfma_f32_16x16x32_bf16 v[24:27], v[154:157], a[172:175], v[24:27]\n"
    "v_cvt_pk_bf16_f32 v8, v8, v9\n"
    "v_cvt_pk_bf16_f32 v9, v10, v11\n"
    "v_cvt_pk_bf16_f32 v10, v12, v13\n"
    "v_cvt_pk_bf16_f32 v11, v14, v15\n"
    "v_mfma_f32_16x16x32_bf16 v[28:31], v[152:155], a[172:175], v[28:31]\n"
    "ds_write_b128 v158, v[8:11] offset:33792\n"
    "v_cvt_pk_bf16_f32 v16, v16, v17\n"
    "v_cvt_pk_bf16_f32 v17, v18, v19\n"
    "v_cvt_pk_bf16_f32 v18, v20, v21\n"
    "v_cvt_pk_bf16_f32 v19, v22, v23\n"
    "ds_write_b128 v158, v[16:19] offset:34816\n"
    "v_cvt_pk_bf16_f32 v24, v24, v25\n"
    "v_cvt_pk_bf16_f32 v25, v26, v27\n"
    "v_cvt_pk_bf16_f32 v26, v28, v29\n"
    "v_cvt_pk_bf16_f32 v27, v30, v31\n"
    "ds_write_b128 v158, v[24:27] offset:35840\n"
    "s_waitcnt lgkmcnt(0)\n"
    "s_barrier\n"
    "ds_read_b128 a[96:99], v252 offset:40960\n"
    "ds_read_b128 a[100:103], v252 offset:41984\n"
    "ds_read_b128 a[104:107], v252 offset:43008\n"
    "ds_read_b128 a[108:111], v252 offset:44032\n"
    "ds_read_b128 a[112:115], v252 offset:45056\n"
    "ds_read_b128 a[116:119], v252 offset:46080\n"
    "ds_read_b128 a[120:123], v252 offset:47104\n"
    "ds_read_b128 a[124:127], v252 offset:48128\n"
    "s_waitcnt lgkmcnt(6)\n"
    "v_mfma_f32_16x16x32_bf16 v[120:123], v[56:59], a[96:99], 0\n"
    "s_waitcnt vmcnt(0)\n"
    "v_add_f32_e32 v234, v234, v246\n"
    "v_mfma_f32_16x16x32_bf16 v[124:127], v[60:63], a[96:99], 0\n"
    "ds_read_b128 a[64:67], v252 offset:32768\n"
    "v_mfma_f32_16x16x32_bf16 v[128:131], v[64:67], a[96:99], 0\n"
    "v_add_f32_e32 v233, v233, v245\n"
    "v_add_f32_e32 v232, v232, v244\n"
    "v_mfma_f32_16x16x32_bf16 v[132:135], v[68:71], a[96:99], 0\n"
    "v_rcp_f32_e32 v242, v234\n"
    "v_mfma_f32_16x16x32_bf16 v[120:123], v[72:75], a[100:103], v[120:123]\n"
    "s_add_u32 s48, s55, 16384\n"
    "ds_read_b128 a[68:71], v252 offset:33792\n"
    "v_mfma_f32_16x16x32_bf16 v[124:127], v[76:79], a[100:103], v[124:127]\n"
    "v_fma_f32 v216, v216, v242, v192\n"
    "v_fma_f32 v217, v217, v242, v193\n"
    "v_mfma_f32_16x16x32_bf16 v[128:131], v[80:83], a[100:103], v[128:131]\n"
    "ds_read_b128 a[72:75], v252 offset:34816\n"
    "v_fma_f32 v218, v218, v242, v194\n"
    "v_mfma_f32_16x16x32_bf16 v[132:135], v[84:87], a[100:103], v[132:135]\n"
    "s_waitcnt lgkmcnt(7)\n"
    "v_fma_f32 v219, v219, v242, v195\n"
    "v_fma_f32 v220, v220, v242, v196\n"
    "v_mfma_f32_16x16x32_bf16 v[120:123], v[88:91], a[104:107], v[120:123]\n"
    "v_fma_f32 v221, v221, v242, v197\n"
    "v_mfma_f32_16x16x32_bf16 v[124:127], v[92:95], a[104:107], v[124:127]\n"
    "ds_read_b128 a[76:79], v252 offset:35840\n"
    "v_fma_f32 v222, v222, v242, v198\n"
    "v_mfma_f32_16x16x32_bf16 v[128:131], v[96:99], a[104:107], v[128:131]\n"
    "v_fma_f32 v223, v223, v242, v199\n"
    "v_cvt_pk_bf16_f32 v216, v216, v217\n"
    "v_mfma_f32_16x16x32_bf16 v[132:135], v[100:103], a[104:107], v[132:135]\n"
    "ds_read_b128 a[80:83], v252 offset:36864\n"
    "v_cvt_pk_bf16_f32 v217, v218, v219\n"
    "v_mfma_f32_16x16x32_bf16 v[120:123], v[104:107], a[108:111], v[120:123]\n"
    "v_cvt_pk_bf16_f32 v218, v220, v221\n"
    "v_cvt_pk_bf16_f32 v219, v222, v223\n"
    "v_mfma_f32_16x16x32_bf16 v[124:127], v[108:111], a[108:111], v[124:127]\n"
    "buffer_store_dwordx4 v[216:219], v151, s[16:19], s48 offen offset:0 sc0 sc1\n"
    "v_fma_f32 v224, v224, v242, v200\n"
    "v_mfma_f32_16x16x32_bf16 v[128:131], v[112:115], a[108:111], v[128:131]\n"
    "ds_read_b128 a[84:87], v252 offset:37888\n"
    "v_fma_f32 v225, v225, v242, v201\n"
    "v_mfma_f32_16x16x32_bf16 v[132:135], v[116:119], a[108:111], v[132:135]\n"
    "s_waitcnt lgkmcnt(8)\n"
    "v_fma_f32 v226, v226, v242, v202\n"
    "v_fma_f32 v227, v227, v242, v203\n"
    "v_mfma_f32_16x16x32_bf16 v[120:123], a[28:31], a[112:115], v[120:123]\n"
    "ds_read_b128 a[88:91], v252 offset:38912\n"
    "v_fma_f32 v228, v228, v242, v204\n"
    "v_mfma_f32_16x16x32_bf16 v[124:127], a[60:63], a[112:115], v[124:127]\n"
    "v_fma_f32 v229, v229, v242, v205\n"
    "v_fma_f32 v230, v230, v242, v206\n"
    "v_mfma_f32_16x16x32_bf16 v[128:131], a[0:3], a[112:115], v[128:131]\n"
    "v_fma_f32 v231, v231, v242, v207\n"
    "v_cvt_pk_bf16_f32 v224, v224, v225\n"
    "v_mfma_f32_16x16x32_bf16 v[132:135], a[32:35], a[112:115], v[132:135]\n"
    "ds_read_b128 a[92:95], v252 offset:39936\n"
    "v_cvt_pk_bf16_f32 v225, v226, v227\n"
    "v_mfma_f32_16x16x32_bf16 v[120:123], a[4:7], a[116:119], v[120:123]\n"
    "v_cvt_pk_bf16_f32 v226, v228, v229\n"
    "v_cvt_pk_bf16_f32 v227, v230, v231\n"
    "v_mfma_f32_16x16x32_bf16 v[124:127], a[36:39], a[116:119], v[124:127]\n"
    "buffer_store_dwordx4 v[224:227], v151, s[16:19], s48 offen offset:64 sc0 sc1\n"
    "v_mfma_f32_16x16x32_bf16 v[128:131], a[8:11], a[116:119], v[128:131]\n"
    "s_bitcmp1_b32 %1, 0\n"
    "s_cselect_b64 vcc, -1, 0\n"
    "v_mfma_f32_16x16x32_bf16 v[132:135], a[40:43], a[116:119], v[132:135]\n"
    "s_waitcnt lgkmcnt(8)\n"
    "v_cndmask_b32_e32 v234, v233, v234, vcc\n"
    "v_mfma_f32_16x16x32_bf16 v[120:123], a[12:15], a[120:123], v[120:123]\n"
    "s_mov_b32 s61, 0x12800000\n"
    "v_rcp_f32_e32 v241, v233\n"
    "v_mfma_f32_16x16x32_bf16 v[124:127], a[44:47], a[120:123], v[124:127]\n"
    "v_rcp_f32_e32 v240, v232\n"
    "v_mfma_f32_16x16x32_bf16 v[128:131], a[16:19], a[120:123], v[128:131]\n"
    "v_subrev_u32_e32 v236, s61, v232\n"
    "v_mfma_f32_16x16x32_bf16 v[132:135], a[48:51], a[120:123], v[132:135]\n"
    "v_subrev_u32_e32 v237, s61, v233\n"
    "v_mfma_f32_16x16x32_bf16 v[120:123], a[20:23], a[124:127], v[120:123]\n"
    "v_subrev_u32_e32 v238, s61, v234\n"
    "v_max3_u32 v236, v236, v237, v238\n"
    "v_mfma_f32_16x16x32_bf16 v[124:127], a[52:55], a[124:127], v[124:127]\n"
    "buffer_store_dword v148, v149, s[8:11], s88 offen offset:0 sc1\n"
    "v_cmp_le_u32_e32 vcc, 0x5f000000, v236\n"
    "v_mfma_f32_16x16x32_bf16 v[128:131], a[24:27], a[124:127], v[128:131]\n"
    "v_mfma_f32_16x16x32_bf16 v[132:135], a[56:59], a[124:127], v[132:135]\n"
    "s_waitcnt lgkmcnt(0)\n"
    "v_mfma_f32_16x16x32_bf16 v[32:35], v[56:59], a[64:67], 0\n"
    "s_cbranch_vccnz .Lat_exit\n"
    "v_mfma_f32_16x16x32_bf16 v[36:39], v[60:63], a[64:67], 0\n"
    "s_add_u32 s48, s58, 8192\n"
    "v_fma_f32 v120, v120, v241, v176\n"
    "v_mfma_f32_16x16x32_bf16 v[32:35], v[72:75], a[68:71], v[32:35]\n"
    "v_fma_f32 v121, v121, v241, v177\n"
    "v_mfma_f32_16x16x32_bf16 v[36:39], v[76:79], a[68:71], v[36:39]\n"
    "v_fma_f32 v122, v122, v241, v178\n"
    "v_mfma_f32_16x16x32_bf16 v[32:35], v[88:91], a[72:75], v[32:35]\n"
    "v_fma_f32 v123, v123, v241, v179\n"
    "v_mfma_f32_16x16x32_bf16 v[36:39], v[92:95], a[72:75], v[36:39]\n"
    "v_fma_f32 v124, v124, v241, v180\n"
    "v_mfma_f32_16x16x32_bf16 v[32:35], v[104:107], a[76:79], v[32:35]\n"
    "v_fma_f32 v125, v125, v241, v181\n"
    "v_mfma_f32_16x16x32_bf16 v[36:39], v[108:111], a[76:79], v[36:39]\n"
    "v_fma_f32 v126, v126, v241, v182\n"
    "v_fma_f32 v127, v127, v241, v183\n"
    "v_mfma_f32_16x16x32_bf16 v[32:35], a[28:31], a[80:83], v[32:35]\n"
    "v_cvt_pk_bf16_f32 v120, v120, v121\n"
    "v_mfma_f32_16x16x32_bf16 v[36:39], a[60:63], a[80:83], v[36:39]\n"
    "v_cvt_pk_bf16_f32 v121, v122, v123\n"
    "v_mfma_f32_16x16x32_bf16 v[32:35], a[4:7], a[84:87], v[32:35]\n"
    "v_cvt_pk_bf16_f32 v122, v124, v125\n"
    "v_mfma_f32_16x16x32_bf16 v[36:39], a[36:39], a[84:87], v[36:39]\n"
    "v_cvt_pk_bf16_f32 v123, v126, v127\n"
    "v_mfma_f32_16x16x32_bf16 v[32:35], a[12:15], a[88:91], v[32:35]\n"
    "buffer_store_dwordx4 v[120:123], v151, s[16:19], s48 offen offset:0 sc0 sc1\n"
    "v_fma_f32 v128, v128, v241, v184\n"
    "v_mfma_f32_16x16x32_bf16 v[36:39], a[44:47], a[88:91], v[36:39]\n"
    "v_fma_f32 v129, v129, v241, v185\n"
    "v_fma_f32 v130, v130, v241, v186\n"
    "v_mfma_f32_16x16x32_bf16 v[32:35], a[20:23], a[92:95], v[32:35]\n"
    "v_fma_f32 v131, v131, v241, v187\n"
    "v_mfma_f32_16x16x32_bf16 v[36:39], a[52:55], a[92:95], v[36:39]\n"
    "v_fma_f32 v132, v132, v241, v188\n"
    "v_mfma_f32_16x16x32_bf16 v[40:43], v[64:67], a[64:67], 0\n"
    "v_fma_f32 v133, v133, v241, v189\n"
    "v_mfma_f32_16x16x32_bf16 v[44:47], v[68:71], a[64:67], 0\n"
    "v_fma_f32 v134, v134, v241, v190\n"
    "v_fma_f32 v135, v135, v241, v191\n"
    "v_mfma_f32_16x16x32_bf16 v[40:43], v[80:83], a[68:71], v[40:43]\n"
    "v_cvt_pk_bf16_f32 v128, v128, v129\n"
    "v_mfma_f32_16x16x32_bf16 v[44:47], v[84:87], a[68:71], v[44:47]\n"
    "v_cvt_pk_bf16_f32 v129, v130, v131\n"
    "v_mfma_f32_16x16x32_bf16 v[40:43], v[96:99], a[72:75], v[40:43]\n"
    "v_cvt_pk_bf16_f32 v130, v132, v133\n"
    "v_cvt_pk_bf16_f32 v131, v134, v135\n"
    "v_mfma_f32_16x16x32_bf16 v[44:47], v[100:103], a[72:75], v[44:47]\n"
    "buffer_store_dwordx4 v[128:131], v151, s[16:19], s48 offen offset:64 sc0 sc1\n"
    "v_mfma_f32_16x16x32_bf16 v[40:43], v[112:115], a[76:79], v[40:43]\n"
    "s_add_u32 s48, s58, 0\n"
    "v_fma_f32 v32, v32, v240, v160\n"
    "v_mfma_f32_16x16x32_bf16 v[44:47], v[116:119], a[76:79], v[44:47]\n"
    "v_fma_f32 v33, v33, v240, v161\n"
    "v_mfma_f32_16x16x32_bf16 v[40:43], a[0:3], a[80:83], v[40:43]\n"
    "v_fma_f32 v34, v34, v240, v162\n"
    "v_mfma_f32_16x16x32_bf16 v[44:47], a[32:35], a[80:83], v[44:47]\n"
    "v_fma_f32 v35, v35, v240, v163\n"
    "v_mfma_f32_16x16x32_bf16 v[40:43], a[8:11], a[84:87], v[40:43]\n"
    "v_fma_f32 v36, v36, v240, v164\n"
    "v_fma_f32 v37, v37, v240, v165\n"
    "v_mfma_f32_16x16x32_bf16 v[44:47], a[40:43], a[84:87], v[44:47]\n"
    "v_fma_f32 v38, v38, v240, v166\n"
    "v_mfma_f32_16x16x32_bf16 v[40:43], a[16:19], a[88:91], v[40:43]\n"
    "v_fma_f32 v39, v39, v240, v167\n"
    "v_mfma_f32_16x16x32_bf16 v[44:47], a[48:51], a[88:91], v[44:47]\n"
    "v_cvt_pk_bf16_f32 v32, v32, v33\n"
    "v_cvt_pk_bf16_f32 v33, v34, v35\n"
    "v_mfma_f32_16x16x32_bf16 v[40:43], a[24:27], a[92:95], v[40:43]\n"
    "v_cvt_pk_bf16_f32 v34, v36, v37\n"
    "v_cvt_pk_bf16_f32 v35, v38, v39\n"
    "v_mfma_f32_16x16x32_bf16 v[44:47], a[56:59], a[92:95], v[44:47]\n"
    "buffer_store_dwordx4 v[32:35], v151, s[16:19], s48 offen offset:0 sc0 sc1\n"
    "s_nop 2\n"
    "v_fma_f32 v40, v40, v240, v168\n"
    "v_fma_f32 v41, v41, v240, v169\n"
    "v_fma_f32 v42, v42, v240, v170\n"
    "v_fma_f32 v43, v43, v240, v171\n"
    "v_fma_f32 v44, v44, v240, v172\n"
    "v_fma_f32 v45, v45, v240, v173\n"
    "v_fma_f32 v46, v46, v240, v174\n"
    "v_fma_f32 v47, v47, v240, v175\n"
    "v_cvt_pk_bf16_f32 v40, v40, v41\n"
    "v_cvt_pk_bf16_f32 v41, v42, v43\n"
    "v_cvt_pk_bf16_f32 v42, v44, v45\n"
    "v_cvt_pk_bf16_f32 v43, v46, v47\n"
    "buffer_store_dwordx4 v[40:43], v151, s[16:19], s48 offen offset:64 sc0 sc1\n"
    "s_endpgm\n"
    ".Lat_two:\n"
    "s_add_u32 s46, s46, 81920\n"
    "ds_read_b128 a[160:163], v241 offset:11264\n"
    "v_accvgpr_read_b32 v223, a255\n"
    "v_add_u32_e32 v158, s46, v253\n"
    "v_mfma_f32_16x16x32_bf16 v[20:23], a[172:175], v[164:167], v[20:23]\n"
    "s_add_u32 s45, %3, 155840\n"
    "ds_read_b128 a[164:167], v241 offset:19456\n"
    "v_cvt_pk_bf16_f32 v216, v216, v217\n"
    "v_and_b32_e32 v150, 15, v147\n"
    "v_mfma_f32_16x16x32_bf16 v[20:23], a[168:171], v[160:163], v[20:23]\n"
    "s_waitcnt lgkmcnt(2)\n"
    "ds_read_b128 a[168:171], v240 offset:8192\n"
    "v_cvt_pk_bf16_f32 v217, v218, v219\n"
    "v_lshlrev_b32_e32 v151, 9, v150\n"
    "v_mfma_f32_16x16x32_bf16 v[24:27], a[176:179], v[160:163], v[24:27]\n"
    "ds_read_b128 a[172:175], v240 offset:16384\n"
    "buffer_load_dwordx4 a[56:59], v253, s[8:11], s48 offen offset:3072 sc1\n"
    "v_lshl_add_u32 v150, v150, 2, s45\n"
    "v_mfma_f32_16x16x32_bf16 v[24:27], a[180:183], v[164:167], v[24:27]\n"
    "s_lshl_b32 s46, %2, 7\n"
    "v_cvt_pk_bf16_f32 v218, v220, v221\n"
    "v_cvt_pk_bf16_f32 v219, v222, v223\n"
    "v_and_or_b32 v151, v147, 48, v151\n"
    "v_mfma_f32_16x16x32_bf16 v[48:51], a[180:183], v[172:175], v[48:51]\n"
    "buffer_store_dwordx4 v[216:219], v253, s[8:11], s83 offen offset:0 sc1\n"
    "v_cvt_pk_bf16_f32 v32, v32, v33\n"
    "v_cvt_pk_bf16_f32 v33, v34, v35\n"
    "v_mfma_f32_16x16x32_bf16 v[48:51], a[176:179], v[168:171], v[48:51]\n"
    "s_add_u32 s58, s35, s46\n"
    "ds_read_b128 a[176:179], v241 offset:8192\n"
    "v_cvt_pk_bf16_f32 v34, v36, v37\n"
    "v_mfma_f32_16x16x32_bf16 v[52:55], a[184:187], v[168:171], v[52:55]\n"
    "s_mul_i32 s45, %1, 74496\n"
    "ds_read_b128 a[180:183], v241 offset:16384\n"
    "v_cvt_pk_bf16_f32 v35, v38, v39\n"
    "v_mfma_f32_16x16x32_bf16 v[52:55], a[188:191], v[172:175], v[52:55]\n"
    "buffer_store_dwordx4 v[32:35], v253, s[8:11], s83 offen offset:1024 sc1\n"
    "v_cvt_pk_bf16_f32 v40, v40, v41\n"
    "v_cvt_pk_bf16_f32 v41, v42, v43\n"
    "v_mfma_f32_16x16x32_bf16 v[28:31], a[188:191], v[164:167], v[28:31]\n"
    "s_add_u32 s45, s45, 16265216\n"
    "v_cvt_pk_bf16_f32 v42, v44, v45\n"
    "v_cvt_pk_bf16_f32 v43, v46, v47\n"
    "v_mfma_f32_16x16x32_bf16 v[28:31], a[184:187], v[160:163], v[28:31]\n"
    "ds_read_b128 a[184:187], v240 offset:9216\n"
    "buffer_store_dwordx4 v[40:43], v253, s[8:11], s83 offen offset:2048 sc1\n"
    "v_cvt_pk_bf16_f32 v48, v48, v49\n"
    "v_mfma_f32_16x16x32_bf16 v[160:163], v[154:157], a[224:227], 0\n"
    "s_lshl_b32 s54, %2, 11\n"
    "ds_read_b128 a[188:191], v240 offset:17408\n"
    "v_cvt_pk_bf16_f32 v49, v50, v51\n"
    "v_mfma_f32_16x16x32_bf16 v[164:167], v[152:155], a[224:227], 0\n"
    "v_cvt_pk_bf16_f32 v50, v52, v53\n"
    "v_cvt_pk_bf16_f32 v51, v54, v55\n"
    "v_mfma_f32_16x16x32_bf16 v[168:171], v[154:157], a[228:231], 0\n"
    "buffer_store_dwordx4 v[48:51], v253, s[8:11], s83 offen offset:3072 sc1\n"
    "v_and_b32_e32 v147, 0xf0, v253\n"
    "v_mfma_f32_16x16x32_bf16 v[172:175], v[152:155], a[228:231], 0\n"
    "buffer_store_dword v55, v251, s[8:11], s60 offen offset:0 sc1\n"
    "v_add_u32_e32 v252, %3, v253\n"
    "v_mfma_f32_16x16x32_bf16 v[176:179], v[154:157], a[232:235], 0\n"
    "s_add_u32 s54, s54, s45\n"
    "s_add_u32 s54, s54, 16384\n"
    "s_lshl1_add_u32 s59, s50, s51\n"
    "v_mfma_f32_16x16x32_bf16 v[180:183], v[152:155], a[232:235], 0\n"
    "s_lshl_b32 s59, s59, 12\n"
    "s_add_u32 s46, s59, s45\n"
    "v_mfma_f32_16x16x32_bf16 v[184:187], v[154:157], a[236:239], 0\n"
    "s_bitcmp1_b32 %1, 0\n"
    "s_cselect_b32 s54, s54, 2.0\n"
    "s_cselect_b32 s55, s58, 2.0\n"
    "v_mfma_f32_16x16x32_bf16 v[188:191], v[152:155], a[236:239], 0\n"
    "s_waitcnt lgkmcnt(6)\n"
    "ds_write_b32 v255, v31 offset:0\n"
    "v_mfma_f32_16x16x32_bf16 v[160:163], a[192:195], a[160:163], v[160:163]\n"
    "s_waitcnt vmcnt(8)\n"
    "v_cmp_ne_u32_e32 vcc, s39, v146\n"
    "v_mfma_f32_16x16x32_bf16 v[164:167], a[196:199], a[160:163], v[164:167]\n"
    "s_add_u32 s61, s46, 0\n"
    "v_mfma_f32_16x16x32_bf16 v[168:171], a[200:203], a[160:163], v[168:171]\n"
    "s_cbranch_vccnz .Lat_two_xpoll\n"
    ".Lat_two_xpoll_ret:\n"
    "v_mfma_f32_16x16x32_bf16 v[172:175], a[204:207], a[160:163], v[172:175]\n"
    "ds_read_b128 a[160:163], v241 offset:9216\n"
    "v_mfma_f32_16x16x32_bf16 v[176:179], a[192:195], a[164:167], v[176:179]\n"
    "buffer_load_dwordx4 a[224:227], v253, s[8:11], s61 offen offset:0 sc1\n"
    "v_mfma_f32_16x16x32_bf16 v[180:183], a[196:199], a[164:167], v[180:183]\n"
    "v_mfma_f32_16x16x32_bf16 v[184:187], a[200:203], a[164:167], v[184:187]\n"
    "v_mfma_f32_16x16x32_bf16 v[188:191], a[204:207], a[164:167], v[188:191]\n"
    "s_waitcnt lgkmcnt(6)\n"
    "ds_read_b128 a[164:167], v241 offset:17408\n"
    "v_mfma_f32_16x16x32_bf16 v[160:163], a[208:211], a[168:171], v[160:163]\n"
    "buffer_load_dwordx4 a[228:231], v253, s[8:11], s61 offen offset:1024 sc1\n"
    "v_mfma_f32_16x16x32_bf16 v[164:167], a[212:215], a[168:171], v[164:167]\n"
    "v_mfma_f32_16x16x32_bf16 v[168:171], a[216:219], a[168:171], v[168:171]\n"
    "v_mfma_f32_16x16x32_bf16 v[172:175], a[220:223], a[168:171], v[172:175]\n"
    "ds_read_b128 a[168:171], v240 offset:10240\n"
    "buffer_load_dwordx4 a[232:235], v253, s[8:11], s61 offen offset:2048 sc1\n"
    "v_mfma_f32_16x16x32_bf16 v[176:179], a[208:211], a[172:175], v[176:179]\n"
    "s_waitcnt lgkmcnt(3)\n"
    "s_barrier\n"
    "v_mfma_f32_16x16x32_bf16 v[180:183], a[212:215], a[172:175], v[180:183]\n"
    "ds_read_b32 v232, v150 offset:0\n"
    "v_mfma_f32_16x16x32_bf16 v[184:187], a[216:219], a[172:175], v[184:187]\n"
    "buffer_load_dwordx4 a[236:239], v253, s[8:11], s61 offen offset:3072 sc1\n"
    "v_mfma_f32_16x16x32_bf16 v[188:191], a[220:223], a[172:175], v[188:191]\n"
    "s_add_u32 s61, s46, 24832\n"
    "ds_read_b128 a[172:175], v240 offset:18432\n"
    "v_mfma_f32_16x16x32_bf16 v[160:163], a[92:95], a[176:179], v[160:163]\n"
    "ds_read_b32 v233, v150 offset:256\n"
    "v_mfma_f32_16x16x32_bf16 v[164:167], a[124:127], a[176:179], v[164:167]\n"
    "buffer_load_dwordx4 a[240:243], v253, s[8:11], s61 offen offset:0 sc1\n"
    "v_mfma_f32_16x16x32_bf16 v[168:171], a[156:159], a[176:179], v[168:171]\n"
    "ds_read_b32 v234, v150 offset:512\n"
    "v_mfma_f32_16x16x32_bf16 v[172:175], a[64:67], a[176:179], v[172:175]\n"
    "ds_read_b128 a[176:179], v241 offset:10240\n"
    "v_mfma_f32_16x16x32_bf16 v[176:179], a[92:95], a[180:183], v[176:179]\n"
    "buffer_load_dwordx4 a[244:247], v253, s[8:11], s61 offen offset:1024 sc1\n"
    "v_mfma_f32_16x16x32_bf16 v[180:183], a[124:127], a[180:183], v[180:183]\n"
    "s_lshl_b32 s52, %2, 11\n"
    "v_mfma_f32_16x16x32_bf16 v[184:187], a[156:159], a[180:183], v[184:187]\n"
    "v_mfma_f32_16x16x32_bf16 v[188:191], a[64:67], a[180:183], v[188:191]\n"
    "ds_read_b128 a[180:183], v241 offset:18432\n"
    "v_mfma_f32_16x16x32_bf16 v[160:163], a[96:99], a[184:187], v[160:163]\n"
    "buffer_load_dwordx4 a[248:251], v253, s[8:11], s61 offen offset:2048 sc1\n"
    "v_mfma_f32_16x16x32_bf16 v[164:167], a[128:131], a[184:187], v[164:167]\n"
    "v_add_u32_e32 v251, s52, v252\n"
    "v_mfma_f32_16x16x32_bf16 v[168:171], a[68:71], a[184:187], v[168:171]\n"
    "v_add_u32_e32 v158, s59, v252\n"
    "v_mfma_f32_16x16x32_bf16 v[172:175], a[100:103], a[184:187], v[172:175]\n"
    "ds_read_b128 a[184:187], v240 offset:11264\n"
    "buffer_load_dwordx4 a[252:255], v253, s[8:11], s61 offen offset:3072 sc1\n"
    "v_mfma_f32_16x16x32_bf16 v[176:179], a[96:99], a[188:191], v[176:179]\n"
    "s_add_u32 s61, s45, 24576\n"
    "v_and_b32_e32 v145, 12, v145\n"
    "v_mfma_f32_16x16x32_bf16 v[180:183], a[128:131], a[188:191], v[180:183]\n"
    "v_mfma_f32_16x16x32_bf16 v[184:187], a[68:71], a[188:191], v[184:187]\n"
    "buffer_load_dwordx3 v[236:238], v147, s[8:11], s61 offen offset:0 sc1\n"
    "v_mfma_f32_16x16x32_bf16 v[188:191], a[100:103], a[188:191], v[188:191]\n"
    "s_waitcnt lgkmcnt(8)\n"
    "s_add_u32 s61, s45, 49408\n"
    "ds_read_b128 a[188:191], v240 offset:19456\n"
    "v_mfma_f32_16x16x32_bf16 v[160:163], a[132:135], a[160:163], v[160:163]\n"
    "s_add_u32 s52, s32, 3\n"
    "v_mfma_f32_16x16x32_bf16 v[164:167], a[72:75], a[160:163], v[164:167]\n"
    "buffer_load_dwordx3 v[248:250], v147, s[8:11], s61 offen offset:0 sc1\n"
    "v_mfma_f32_16x16x32_bf16 v[168:171], a[104:107], a[160:163], v[168:171]\n"
    "s_and_b32 s52, s52, 3\n"
    "v_mfma_f32_16x16x32_bf16 v[172:175], a[136:139], a[160:163], v[172:175]\n"
    "s_lshl_b32 s52, s52, 8\n"
    "v_mfma_f32_16x16x32_bf16 v[176:179], a[132:135], a[164:167], v[176:179]\n"
    "s_waitcnt vmcnt(10)\n"
    "buffer_store_dword v148, v149, s[8:11], s87 offen offset:0 sc1\n"
    "v_mfma_f32_16x16x32_bf16 v[180:183], a[72:75], a[164:167], v[180:183]\n"
    "v_mfma_f32_16x16x32_bf16 v[184:187], a[104:107], a[164:167], v[184:187]\n"
    "s_add_u32 s52, s52, s47\n"
    "v_mfma_f32_16x16x32_bf16 v[188:191], a[136:139], a[164:167], v[188:191]\n"
    "s_waitcnt lgkmcnt(6)\n"
    "v_mfma_f32_16x16x32_bf16 v[160:163], a[76:79], a[168:171], v[160:163]\n"
    "buffer_load_dword v144, v145, s[8:11], s52 offen offset:0 sc1\n"
    "v_mfma_f32_16x16x32_bf16 v[164:167], a[108:111], a[168:171], v[164:167]\n"
    "v_mfma_f32_16x16x32_bf16 v[168:171], a[140:143], a[168:171], v[168:171]\n"
    "v_mfma_f32_16x16x32_bf16 v[172:175], a[80:83], a[168:171], v[172:175]\n"
    "v_mfma_f32_16x16x32_bf16 v[176:179], a[76:79], a[172:175], v[176:179]\n"
    "v_mfma_f32_16x16x32_bf16 v[180:183], a[108:111], a[172:175], v[180:183]\n"
    "v_mfma_f32_16x16x32_bf16 v[184:187], a[140:143], a[172:175], v[184:187]\n"
    "v_mfma_f32_16x16x32_bf16 v[188:191], a[80:83], a[172:175], v[188:191]\n"
    "s_waitcnt lgkmcnt(2)\n"
    "v_mfma_f32_16x16x32_bf16 v[160:163], a[112:115], a[176:179], v[160:163]\n"
    "v_mfma_f32_16x16x32_bf16 v[164:167], a[144:147], a[176:179], v[164:167]\n"
    "v_mfma_f32_16x16x32_bf16 v[168:171], a[84:87], a[176:179], v[168:171]\n"
    "v_mfma_f32_16x16x32_bf16 v[172:175], a[116:119], a[176:179], v[172:175]\n"
    "v_mfma_f32_16x16x32_bf16 v[176:179], a[112:115], a[180:183], v[176:179]\n"
    "v_mfma_f32_16x16x32_bf16 v[180:183], a[144:147], a[180:183], v[180:183]\n"
    "v_mfma_f32_16x16x32_bf16 v[184:187], a[84:87], a[180:183], v[184:187]\n"
    "v_mfma_f32_16x16x32_bf16 v[188:191], a[116:119], a[180:183], v[188:191]\n"
    "s_waitcnt lgkmcnt(0)\n"
    "v_mfma_f32_16x16x32_bf16 v[160:163], a[148:151], a[184:187], v[160:163]\n"
    "v_mfma_f32_16x16x32_bf16 v[164:167], a[88:91], a[184:187], v[164:167]\n"
    "v_mfma_f32_16x16x32_bf16 v[168:171], a[120:123], a[184:187], v[168:171]\n"
    "v_mfma_f32_16x16x32_bf16 v[172:175], a[152:155], a[184:187], v[172:175]\n"
    "v_mfma_f32_16x16x32_bf16 v[176:179], a[148:151], a[188:191], v[176:179]\n"
    "v_mfma_f32_16x16x32_bf16 v[180:183], a[88:91], a[188:191], v[180:183]\n"
    "v_mfma_f32_16x16x32_bf16 v[184:187], a[120:123], a[188:191], v[184:187]\n"
    "v_mfma_f32_16x16x32_bf16 v[188:191], a[152:155], a[188:191], v[188:191]\n"
    "s_waitcnt vmcnt(10)\n"
    "v_mfma_f32_16x16x32_bf16 v[0:3], v[154:157], a[224:227], v[0:3]\n"
    "s_waitcnt vmcnt(0)\n"
    "v_cmp_ne_u32_e32 vcc, s39, v144\n"
    "s_nop 3\n"
    "s_cbranch_vccnz .Lat_two_ypoll\n"
    ".Lat_two_ypoll_ret:\n"
    "s_add_u32 s61, s46, 49664\n"
    "buffer_load_dwordx4 a[192:195], v253, s[8:11], s61 offen offset:0 sc1\n"
    "buffer_load_dwordx4 a[196:199], v253, s[8:11], s61 offen offset:1024 sc1\n"
    "buffer_load_dwordx4 a[200:203], v253, s[8:11], s61 offen offset:2048 sc1\n"
    "buffer_load_dwordx4 a[204:207], v253, s[8:11], s61 offen offset:3072 sc1\n"
    "s_add_u32 s61, s45, 74240\n"
    "buffer_load_dwordx3 v[244:246], v147, s[8:11], s61 offen offset:0 sc1\n"
    "v_mfma_f32_16x16x32_bf16 v[4:7], v[152:155], a[224:227], v[4:7]\n"
    "v_mfma_f32_16x16x32_bf16 v[8:11], v[154:157], a[228:231], v[8:11]\n"
    "s_waitcnt vmcnt(7)\n"
    "v_mfma_f32_16x16x32_bf16 v[12:15], v[152:155], a[228:231], v[12:15]\n"
    "s_waitcnt vmcnt(13)\n"
    "v_mfma_f32_16x16x32_bf16 v[16:19], v[154:157], a[232:235], v[16:19]\n"
    "v_add_f32_e32 v232, v232, v236\n"
    "v_mfma_f32_16x16x32_bf16 v[20:23], v[152:155], a[232:235], v[20:23]\n"
    "v_mfma_f32_16x16x32_bf16 v[24:27], v[154:157], a[236:239], v[24:27]\n"
    "v_add_f32_e32 v233, v233, v237\n"
    "v_mfma_f32_16x16x32_bf16 v[28:31], v[152:155], a[236:239], v[28:31]\n"
    "s_waitcnt vmcnt(11)\n"
    "v_mfma_f32_16x16x32_bf16 v[0:3], v[154:157], a[240:243], v[0:3]\n"
    "v_add_f32_e32 v234, v234, v238\n"
    "v_mfma_f32_16x16x32_bf16 v[4:7], v[152:155], a[240:243], v[4:7]\n"
    "v_mfma_f32_16x16x32_bf16 v[8:11], v[154:157], a[244:247], v[8:11]\n"
    "v_add_f32_e32 v232, v232, v248\n"
    "v_mfma_f32_16x16x32_bf16 v[12:15], v[152:155], a[244:247], v[12:15]\n"
    "s_waitcnt vmcnt(9)\n"
    "v_mfma_f32_16x16x32_bf16 v[16:19], v[154:157], a[248:251], v[16:19]\n"
    "v_add_f32_e32 v233, v233, v249\n"
    "v_mfma_f32_16x16x32_bf16 v[20:23], v[152:155], a[248:251], v[20:23]\n"
    "v_mfma_f32_16x16x32_bf16 v[24:27], v[154:157], a[252:255], v[24:27]\n"
    "v_add_f32_e32 v234, v234, v250\n"
    "v_mfma_f32_16x16x32_bf16 v[28:31], v[152:155], a[252:255], v[28:31]\n"
    "s_waitcnt vmcnt(4)\n"
    "v_mfma_f32_16x16x32_bf16 v[0:3], v[154:157], a[192:195], v[0:3]\n"
    "v_mfma_f32_16x16x32_bf16 v[4:7], v[152:155], a[192:195], v[4:7]\n"
    "s_waitcnt vmcnt(3)\n"
    "v_mfma_f32_16x16x32_bf16 v[8:11], v[154:157], a[196:199], v[8:11]\n"
    "v_mfma_f32_16x16x32_bf16 v[12:15], v[152:155], a[196:199], v[12:15]\n"
    "s_waitcnt vmcnt(2)\n"
    "v_cvt_pk_bf16_f32 v0, v0, v1\n"
    "v_cvt_pk_bf16_f32 v1, v2, v3\n"
    "v_mfma_f32_16x16x32_bf16 v[16:19], v[154:157], a[200:203], v[16:19]\n"
    "v_cvt_pk_bf16_f32 v2, v4, v5\n"
    "v_cvt_pk_bf16_f32 v3, v6, v7\n"
    "v_mfma_f32_16x16x32_bf16 v[20:23], v[152:155], a[200:203], v[20:23]\n"
    "s_waitcnt vmcnt(1)\n"
    "ds_write_b128 v158, v[0:3] offset:32768\n"
    "v_mfma_f32_16x16x32_bf16 v[24:27], v[154:157], a[204:207], v[24:27]\n"
    "v_cvt_pk_bf16_f32 v8, v8, v9\n"
    "v_cvt_pk_bf16_f32 v9, v10, v11\n"
    "v_cvt_pk_bf16_f32 v10, v12, v13\n"
    "v_cvt_pk_bf16_f32 v11, v14, v15\n"
    "v_mfma_f32_16x16x32_bf16 v[28:31], v[152:155], a[204:207], v[28:31]\n"
    "ds_write_b128 v158, v[8:11] offset:33792\n"
    "v_cvt_pk_bf16_f32 v16, v16, v17\n"
    "v_cvt_pk_bf16_f32 v17, v18, v19\n"
    "v_cvt_pk_bf16_f32 v18, v20, v21\n"
    "v_cvt_pk_bf16_f32 v19, v22, v23\n"
    "ds_write_b128 v158, v[16:19] offset:34816\n"
    "v_cvt_pk_bf16_f32 v24, v24, v25\n"
    "v_cvt_pk_bf16_f32 v25, v26, v27\n"
    "v_cvt_pk_bf16_f32 v26, v28, v29\n"
    "v_cvt_pk_bf16_f32 v27, v30, v31\n"
    "ds_write_b128 v158, v[24:27] offset:35840\n"
    "s_waitcnt lgkmcnt(0)\n"
    "s_barrier\n"
    "ds_read_b128 a[96:99], v252 offset:40960\n"
    "ds_read_b128 a[100:103], v252 offset:41984\n"
    "ds_read_b128 a[104:107], v252 offset:43008\n"
    "ds_read_b128 a[108:111], v252 offset:44032\n"
    "ds_read_b128 a[112:115], v252 offset:45056\n"
    "ds_read_b128 a[116:119], v252 offset:46080\n"
    "ds_read_b128 a[120:123], v252 offset:47104\n"
    "ds_read_b128 a[124:127], v252 offset:48128\n"
    "s_waitcnt lgkmcnt(6)\n"
    "v_mfma_f32_16x16x32_bf16 v[120:123], v[56:59], a[96:99], 0\n"
    "v_mfma_f32_16x16x32_bf16 v[124:127], v[60:63], a[96:99], 0\n"
    "s_waitcnt vmcnt(0)\n"
    "ds_read_b128 a[64:67], v252 offset:32768\n"
    "v_mfma_f32_16x16x32_bf16 v[128:131], v[64:67], a[96:99], 0\n"
    "v_mfma_f32_16x16x32_bf16 v[132:135], v[68:71], a[96:99], 0\n"
    "v_add_f32_e32 v234, v234, v246\n"
    "v_mfma_f32_16x16x32_bf16 v[120:123], v[72:75], a[100:103], v[120:123]\n"
    "ds_read_b128 a[68:71], v252 offset:33792\n"
    "v_mfma_f32_16x16x32_bf16 v[124:127], v[76:79], a[100:103], v[124:127]\n"
    "v_add_f32_e32 v233, v233, v245\n"
    "v_mfma_f32_16x16x32_bf16 v[128:131], v[80:83], a[100:103], v[128:131]\n"
    "ds_read_b128 a[72:75], v252 offset:34816\n"
    "v_mfma_f32_16x16x32_bf16 v[132:135], v[84:87], a[100:103], v[132:135]\n"
    "s_waitcnt lgkmcnt(7)\n"
    "v_add_f32_e32 v232, v232, v244\n"
    "v_mfma_f32_16x16x32_bf16 v[120:123], v[88:91], a[104:107], v[120:123]\n"
    "v_mfma_f32_16x16x32_bf16 v[124:127], v[92:95], a[104:107], v[124:127]\n"
    "ds_read_b128 a[76:79], v252 offset:35840\n"
    "v_mfma_f32_16x16x32_bf16 v[128:131], v[96:99], a[104:107], v[128:131]\n"
    "s_bitcmp1_b32 %1, 0\n"
    "s_cselect_b64 vcc, -1, 0\n"
    "v_mfma_f32_16x16x32_bf16 v[132:135], v[100:103], a[104:107], v[132:135]\n"
    "ds_read_b128 a[80:83], v252 offset:36864\n"
    "v_mfma_f32_16x16x32_bf16 v[120:123], v[104:107], a[108:111], v[120:123]\n"
    "v_cndmask_b32_e32 v234, v233, v234, vcc\n"
    "v_mfma_f32_16x16x32_bf16 v[124:127], v[108:111], a[108:111], v[124:127]\n"
    "s_mov_b32 s61, 0x12800000\n"
    "v_mfma_f32_16x16x32_bf16 v[128:131], v[112:115], a[108:111], v[128:131]\n"
    "ds_read_b128 a[84:87], v252 offset:37888\n"
    "v_mfma_f32_16x16x32_bf16 v[132:135], v[116:119], a[108:111], v[132:135]\n"
    "s_waitcnt lgkmcnt(8)\n"
    "v_rcp_f32_e32 v241, v233\n"
    "v_mfma_f32_16x16x32_bf16 v[120:123], a[28:31], a[112:115], v[120:123]\n"
    "ds_read_b128 a[88:91], v252 offset:38912\n"
    "v_mfma_f32_16x16x32_bf16 v[124:127], a[60:63], a[112:115], v[124:127]\n"
    "v_rcp_f32_e32 v240, v232\n"
    "v_mfma_f32_16x16x32_bf16 v[128:131], a[0:3], a[112:115], v[128:131]\n"
    "v_mfma_f32_16x16x32_bf16 v[132:135], a[32:35], a[112:115], v[132:135]\n"
    "ds_read_b128 a[92:95], v252 offset:39936\n"
    "v_mfma_f32_16x16x32_bf16 v[120:123], a[4:7], a[116:119], v[120:123]\n"
    "v_subrev_u32_e32 v236, s61, v232\n"
    "v_mfma_f32_16x16x32_bf16 v[124:127], a[36:39], a[116:119], v[124:127]\n"
    "v_subrev_u32_e32 v237, s61, v233\n"
    "v_mfma_f32_16x16x32_bf16 v[128:131], a[8:11], a[116:119], v[128:131]\n"
    "v_mfma_f32_16x16x32_bf16 v[132:135], a[40:43], a[116:119], v[132:135]\n"
    "s_waitcnt lgkmcnt(8)\n"
    "v_subrev_u32_e32 v238, s61, v234\n"
    "v_mfma_f32_16x16x32_bf16 v[120:123], a[12:15], a[120:123], v[120:123]\n"
    "v_mfma_f32_16x16x32_bf16 v[124:127], a[44:47], a[120:123], v[124:127]\n"
    "v_max3_u32 v236, v236, v237, v238\n"
    "v_mfma_f32_16x16x32_bf16 v[128:131], a[16:19], a[120:123], v[128:131]\n"
    "v_mfma_f32_16x16x32_bf16 v[132:135], a[48:51], a[120:123], v[132:135]\n"
    "v_cmp_le_u32_e32 vcc, 0x5f000000, v236\n"
    "v_mfma_f32_16x16x32_bf16 v[120:123], a[20:23], a[124:127], v[120:123]\n"
    "v_mfma_f32_16x16x32_bf16 v[124:127], a[52:55], a[124:127], v[124:127]\n"
    "buffer_store_dword v148, v149, s[8:11], s88 offen offset:0 sc1\n"
    "v_mfma_f32_16x16x32_bf16 v[128:131], a[24:27], a[124:127], v[128:131]\n"
    "v_mfma_f32_16x16x32_bf16 v[132:135], a[56:59], a[124:127], v[132:135]\n"
    "s_waitcnt lgkmcnt(0)\n"
    "v_mfma_f32_16x16x32_bf16 v[32:35], v[56:59], a[64:67], 0\n"
    "s_cbranch_vccnz .Lat_exit\n"
    "v_mfma_f32_16x16x32_bf16 v[36:39], v[60:63], a[64:67], 0\n"
    "s_add_u32 s48, s58, 8192\n"
    "v_fma_f32 v120, v120, v241, v176\n"
    "v_mfma_f32_16x16x32_bf16 v[32:35], v[72:75], a[68:71], v[32:35]\n"
    "v_fma_f32 v121, v121, v241, v177\n"
    "v_mfma_f32_16x16x32_bf16 v[36:39], v[76:79], a[68:71], v[36:39]\n"
    "v_fma_f32 v122, v122, v241, v178\n"
    "v_mfma_f32_16x16x32_bf16 v[32:35], v[88:91], a[72:75], v[32:35]\n"
    "v_fma_f32 v123, v123, v241, v179\n"
    "v_mfma_f32_16x16x32_bf16 v[36:39], v[92:95], a[72:75], v[36:39]\n"
    "v_fma_f32 v124, v124, v241, v180\n"
    "v_mfma_f32_16x16x32_bf16 v[32:35], v[104:107], a[76:79], v[32:35]\n"
    "v_fma_f32 v125, v125, v241, v181\n"
    "v_mfma_f32_16x16x32_bf16 v[36:39], v[108:111], a[76:79], v[36:39]\n"
    "v_fma_f32 v126, v126, v241, v182\n"
    "v_fma_f32 v127, v127, v241, v183\n"
    "v_mfma_f32_16x16x32_bf16 v[32:35], a[28:31], a[80:83], v[32:35]\n"
    "v_cvt_pk_bf16_f32 v120, v120, v121\n"
    "v_mfma_f32_16x16x32_bf16 v[36:39], a[60:63], a[80:83], v[36:39]\n"
    "v_cvt_pk_bf16_f32 v121, v122, v123\n"
    "v_mfma_f32_16x16x32_bf16 v[32:35], a[4:7], a[84:87], v[32:35]\n"
    "v_cvt_pk_bf16_f32 v122, v124, v125\n"
    "v_mfma_f32_16x16x32_bf16 v[36:39], a[36:39], a[84:87], v[36:39]\n"
    "v_cvt_pk_bf16_f32 v123, v126, v127\n"
    "v_mfma_f32_16x16x32_bf16 v[32:35], a[12:15], a[88:91], v[32:35]\n"
    "buffer_store_dwordx4 v[120:123], v151, s[16:19], s48 offen offset:0 sc0 sc1\n"
    "v_fma_f32 v128, v128, v241, v184\n"
    "v_mfma_f32_16x16x32_bf16 v[36:39], a[44:47], a[88:91], v[36:39]\n"
    "v_fma_f32 v129, v129, v241, v185\n"
    "v_fma_f32 v130, v130, v241, v186\n"
    "v_mfma_f32_16x16x32_bf16 v[32:35], a[20:23], a[92:95], v[32:35]\n"
    "v_fma_f32 v131, v131, v241, v187\n"
    "v_mfma_f32_16x16x32_bf16 v[36:39], a[52:55], a[92:95], v[36:39]\n"
    "v_fma_f32 v132, v132, v241, v188\n"
    "v_mfma_f32_16x16x32_bf16 v[40:43], v[64:67], a[64:67], 0\n"
    "v_fma_f32 v133, v133, v241, v189\n"
    "v_mfma_f32_16x16x32_bf16 v[44:47], v[68:71], a[64:67], 0\n"
    "v_fma_f32 v134, v134, v241, v190\n"
    "v_fma_f32 v135, v135, v241, v191\n"
    "v_mfma_f32_16x16x32_bf16 v[40:43], v[80:83], a[68:71], v[40:43]\n"
    "v_cvt_pk_bf16_f32 v128, v128, v129\n"
    "v_mfma_f32_16x16x32_bf16 v[44:47], v[84:87], a[68:71], v[44:47]\n"
    "v_cvt_pk_bf16_f32 v129, v130, v131\n"
    "v_mfma_f32_16x16x32_bf16 v[40:43], v[96:99], a[72:75], v[40:43]\n"
    "v_cvt_pk_bf16_f32 v130, v132, v133\n"
    "v_cvt_pk_bf16_f32 v131, v134, v135\n"
    "v_mfma_f32_16x16x32_bf16 v[44:47], v[100:103], a[72:75], v[44:47]\n"
    "buffer_store_dwordx4 v[128:131], v151, s[16:19], s48 offen offset:64 sc0 sc1\n"
    "v_mfma_f32_16x16x32_bf16 v[40:43], v[112:115], a[76:79], v[40:43]\n"
    "s_add_u32 s48, s58, 0\n"
    "v_fma_f32 v32, v32, v240, v160\n"
    "v_mfma_f32_16x16x32_bf16 v[44:47], v[116:119], a[76:79], v[44:47]\n"
    "v_fma_f32 v33, v33, v240, v161\n"
    "v_mfma_f32_16x16x32_bf16 v[40:43], a[0:3], a[80:83], v[40:43]\n"
    "v_fma_f32 v34, v34, v240, v162\n"
    "v_mfma_f32_16x16x32_bf16 v[44:47], a[32:35], a[80:83], v[44:47]\n"
    "v_fma_f32 v35, v35, v240, v163\n"
    "v_mfma_f32_16x16x32_bf16 v[40:43], a[8:11], a[84:87], v[40:43]\n"
    "v_fma_f32 v36, v36, v240, v164\n"
    "v_fma_f32 v37, v37, v240, v165\n"
    "v_mfma_f32_16x16x32_bf16 v[44:47], a[40:43], a[84:87], v[44:47]\n"
    "v_fma_f32 v38, v38, v240, v166\n"
    "v_mfma_f32_16x16x32_bf16 v[40:43], a[16:19], a[88:91], v[40:43]\n"
    "v_fma_f32 v39, v39, v240, v167\n"
    "v_mfma_f32_16x16x32_bf16 v[44:47], a[48:51], a[88:91], v[44:47]\n"
    "v_cvt_pk_bf16_f32 v32, v32, v33\n"
    "v_cvt_pk_bf16_f32 v33, v34, v35\n"
    "v_mfma_f32_16x16x32_bf16 v[40:43], a[24:27], a[92:95], v[40:43]\n"
    "v_cvt_pk_bf16_f32 v34, v36, v37\n"
    "v_cvt_pk_bf16_f32 v35, v38, v39\n"
    "v_mfma_f32_16x16x32_bf16 v[44:47], a[56:59], a[92:95], v[44:47]\n"
    "buffer_store_dwordx4 v[32:35], v151, s[16:19], s48 offen offset:0 sc0 sc1\n"
    "s_nop 2\n"
    "v_fma_f32 v40, v40, v240, v168\n"
    "v_fma_f32 v41, v41, v240, v169\n"
    "v_fma_f32 v42, v42, v240, v170\n"
    "v_fma_f32 v43, v43, v240, v171\n"
    "v_fma_f32 v44, v44, v240, v172\n"
    "v_fma_f32 v45, v45, v240, v173\n"
    "v_fma_f32 v46, v46, v240, v174\n"
    "v_fma_f32 v47, v47, v240, v175\n"
    "v_cvt_pk_bf16_f32 v40, v40, v41\n"
    "v_cvt_pk_bf16_f32 v41, v42, v43\n"
    "v_cvt_pk_bf16_f32 v42, v44, v45\n"
    "v_cvt_pk_bf16_f32 v43, v46, v47\n"
    "buffer_store_dwordx4 v[40:43], v151, s[16:19], s48 offen offset:64 sc0 sc1\n"
    "s_endpgm\n"
    ".Lat_two_xpoll:\n"
    "s_mov_b32 s53, 0x400000\n"
    ".Lat_two_xpoll_l:\n"
    "s_sleep 0\n"
    "buffer_load_dword v146, v145, s[8:11], s47 offen offset:128 sc1\n"
    "s_waitcnt vmcnt(0)\n"
    "v_cmp_ne_u32_e32 vcc, s39, v146\n"
    "s_sub_u32 s53, s53, 1\n"
    "s_cbranch_scc1 .Lat_two_xpoll_ret\n"
    "s_nop 2\n"
    "s_cbranch_vccnz .Lat_two_xpoll_l\n"
    "s_branch .Lat_two_xpoll_ret\n"
    ".Lat_two_ypoll:\n"
    "s_mov_b32 s53, 0x400000\n"
    ".Lat_two_ypoll_l:\n"
    "s_sleep 0\n"
    "buffer_load_dword v144, v145, s[8:11], s52 offen offset:0 sc1\n"
    "s_waitcnt vmcnt(0)\n"
    "v_cmp_ne_u32_e32 vcc, s39, v144\n"
    "s_sub_u32 s53, s53, 1\n"
    "s_cbranch_scc1 .Lat_two_ypoll_ret\n"
    "s_nop 2\n"
    "s_cbranch_vccnz .Lat_two_ypoll_l\n"
    "s_branch .Lat_two_ypoll_ret\n"
    ".Lat_wpoll:\n"
    "s_mov_b32 s53, 0x400000\n"
    ".Lat_wpoll_l:\n"
    "s_sleep 1\n"
    "buffer_load_dword v255, v254, s[8:11], s45 offen offset:0 sc1\n"
    "s_waitcnt vmcnt(0)\n"
    "v_cmp_ne_u32_e32 vcc, s39, v255\n"
    "s_sub_u32 s53, s53, 1\n"
    "s_cbranch_scc1 .Lat_wpoll_ret\n"
    "s_nop 2\n"
    "s_cbranch_vccnz .Lat_wpoll_l\n"
    "s_branch .Lat_wpoll_ret\n"
    ".Lat_xpoll:\n"
    "s_mov_b32 s53, 0x400000\n"
    ".Lat_xpoll_l:\n"
    "s_sleep 0\n"
    "buffer_load_dword v146, v145, s[8:11], s47 offen offset:128 sc1\n"
    "s_waitcnt vmcnt(0)\n"
    "v_cmp_ne_u32_e32 vcc, s39, v146\n"
    "s_sub_u32 s53, s53, 1\n"
    "s_cbranch_scc1 .Lat_xpoll_ret\n"
    "s_nop 2\n"
    "s_cbranch_vccnz .Lat_xpoll_l\n"
    "s_branch .Lat_xpoll_ret\n"
    ".Lat_ypoll:\n"
    "s_mov_b32 s53, 0x400000\n"
    ".Lat_ypoll_l:\n"
    "s_sleep 0\n"
    "buffer_load_dword v144, v145, s[8:11], s52 offen offset:0 sc1\n"
    "s_waitcnt vmcnt(0)\n"
    "v_cmp_ne_u32_e32 vcc, s39, v144\n"
    "s_sub_u32 s53, s53, 1\n"
    "s_cbranch_scc1 .Lat_ypoll_ret\n"
    "s_nop 2\n"
    "s_cbranch_vccnz .Lat_ypoll_l\n"
    "s_branch .Lat_ypoll_ret\n"
    ".Lat_lpoll:\n"
    "s_mov_b32 s53, 0x400000\n"
    ".Lat_lpoll_l:\n"
    "s_sleep 1\n"
    "buffer_load_dword v154, off, s[8:11], s80 sc1\n"
    "s_waitcnt vmcnt(0)\n"
    "v_cmp_ne_u32_e32 vcc, s39, v154\n"
    "s_sub_u32 s53, s53, 1\n"
    "s_cbranch_scc1 .Lat_lpoll_ret\n"
    "s_nop 2\n"
    "s_cbranch_vccnz .Lat_lpoll_l\n"
    ".Lat_lpoll_ret:\n"
    "v_lshlrev_b32_e32 v253, 2, v249\n"
    "s_mov_b32 m0, s82\n"
    "s_nop 0\n"
    "buffer_load_dwordx4 v253, s[8:11], s81 offen offset:0 sc1 lds\n"
    "s_waitcnt vmcnt(0)\n"
    "s_branch .Lat_g\n"
    ".Lat_exit:\n"
    "s_waitcnt vmcnt(0)\n"
    "s_nop 15\n"
    ".Lat_end:\n"
    "s_waitcnt lgkmcnt(0)\n"
    : "+s"(ka), "+s"(b), "+s"(wid) : "s"((unsigned)(size_t)lds)
    : "v0", "v1", "v2", "v3", "v4", "v5", "v6", "v7", "v8", "v9", "v10", "v11", "v12", "v13", "v14", "v15", "v16", "v17", "v18", "v19", "v20", "v21", "v22", "v23", "v24", "v25", "v26", "v27", "v28", "v29", "v30", "v31", "v32", "v33", "v34", "v35", "v36", "v37", "v38", "v39", "v40", "v41", "v42", "v43", "v44", "v45", "v46", "v47", "v48", "v49", "v50", "v51", "v52", "v53", "v54", "v55", "v56", "v57", "v58", "v59", "v60", "v61", "v62", "v63", "v64", "v65", "v66", "v67", "v68", "v69", "v70", "v71", "v72", "v73", "v74", "v75", "v76", "v77", "v78", "v79", "v80", "v81", "v82", "v83", "v84", "v85", "v86", "v87", "v88", "v89", "v90", "v91", "v92", "v93", "v94", "v95", "v96", "v97", "v98", "v99", "v100", "v101", "v102", "v103", "v104", "v105", "v106", "v107", "v108", "v109", "v110", "v111", "v112", "v113", "v114", "v115", "v116", "v117", "v118", "v119", "v120", "v121", "v122", "v123", "v124", "v125", "v126", "v127", "v128", "v129", "v130", "v131", "v132", "v133", "v134", "v135", "v136", "v137", "v138", "v139", "v140", "v141", "v142", "v143", "v144", "v145", "v146", "v147", "v148", "v149", "v150", "v151", "v152", "v153", "v154", "v155", "v156", "v157", "v158", "v159", "v160", "v161", "v162", "v163", "v164", "v165", "v166", "v167", "v168", "v169", "v170", "v171", "v172", "v173", "v174", "v175", "v176", "v177", "v178", "v179", "v180", "v181", "v182", "v183", "v184", "v185", "v186", "v187", "v188", "v189", "v190", "v191", "v192", "v193", "v194", "v195", "v196", "v197", "v198", "v199", "v200", "v201", "v202", "v203", "v204", "v205", "v206", "v207", "v208", "v209", "v210", "v211", "v212", "v213", "v214", "v215", "v216", "v217", "v218", "v219", "v220", "v221", "v222", "v223", "v224", "v225", "v226", "v227", "v228", "v229", "v230", "v231", "v232", "v233", "v234", "v235", "v236", "v237", "v238", "v239", "v240", "v241", "v242", "v243", "v244", "v245", "v246", "v247", "v248", "v249", "v250", "v251", "v252", "v253", "v254", "v255", "a0", "a1", "a2", "a3", "a4", "a5", "a6", "a7", "a8", "a9", "a10", "a11", "a12", "a13", "a14", "a15", "a16", "a17", "a18", "a19", "a20", "a21", "a22", "a23", "a24", "a25", "a26", "a27", "a28", "a29", "a30", "a31", "a32", "a33", "a34", "a35", "a36", "a37", "a38", "a39", "a40", "a41", "a42", "a43", "a44", "a45", "a46", "a47", "a48", "a49", "a50", "a51", "a52", "a53", "a54", "a55", "a56", "a57", "a58", "a59", "a60", "a61", "a62", "a63", "a64", "a65", "a66", "a67", "a68", "a69", "a70", "a71", "a72", "a73", "a74", "a75", "a76", "a77", "a78", "a79", "a80", "a81", "a82", "a83", "a84", "a85", "a86", "a87", "a88", "a89", "a90", "a91", "a92", "a93", "a94", "a95", "a96", "a97", "a98", "a99", "a100", "a101", "a102", "a103", "a104", "a105", "a106", "a107", "a108", "a109", "a110", "a111", "a112", "a113", "a114", "a115", "a116", "a117", "a118", "a119", "a120", "a121", "a122", "a123", "a124", "a125", "a126", "a127", "a128", "a129", "a130", "a131", "a132", "a133", "a134", "a135", "a136", "a137", "a138", "a139", "a140", "a141", "a142", "a143", "a144", "a145", "a146", "a147", "a148", "a149", "a150", "a151", "a152", "a153", "a154", "a155", "a156", "a157", "a158", "a159", "a160", "a161", "a162", "a163", "a164", "a165", "a166", "a167", "a168", "a169", "a170", "a171", "a172", "a173", "a174", "a175", "a176", "a177", "a178", "a179", "a180", "a181", "a182", "a183", "a184", "a185", "a186", "a187", "a188", "a189", "a190", "a191", "a192", "a193", "a194", "a195", "a196", "a197", "a198", "a199", "a200", "a201", "a202", "a203", "a204", "a205", "a206", "a207", "a208", "a209", "a210", "a211", "a212", "a213", "a214", "a215", "a216", "a217", "a218", "a219", "a220", "a221", "a222", "a223", "a224", "a225", "a226", "a227", "a228", "a229", "a230", "a231", "a232", "a233", "a234", "a235", "a236", "a237", "a238", "a239", "a240", "a241", "a242", "a243", "a244", "a245", "a246", "a247", "a248", "a249", "a250", "a251", "a252", "a253", "a254", "a255", "s8", "s9", "s10", "s11", "s12", "s13", "s14", "s15", "s16", "s17", "s18", "s19", "s20", "s21", "s22", "s23", "s24", "s25", "s26", "s27", "s28", "s29", "s30", "s31", "s32", "s33", "s34", "s35", "s36", "s37", "s38", "s39", "s40", "s41", "s42", "s43", "s44", "s45", "s46", "s47", "s48", "s49", "s50", "s51", "s52", "s53", "s54", "s55", "s56", "s57", "s58", "s59", "s60", "s61", "s62", "s63", "s80", "s81", "s82", "s83", "s84", "s85", "s86", "s87", "s88", "s89", "s90", "vcc", "m0", "scc", "memory");
  typedef __attribute__((address_space(1))) char* __attribute__((address_space(4))) const* arg_t;
  const arg_t ap = (arg_t)ka;
  typedef const u16* cu16;
  if (b >= NATT) list_branch(lds, b - NATT, wid, (const int*)ap[1], (const int*)ap[2], (cu16)ap[3], (cu16)ap[8], (cu16)ap[9], (cu16)ap[10], (char*)ap[0]);
  else exact_rows(lds, b, wid, (char*)ap[0], (cu16)ap[4], (u16*)ap[5]);
}

extern "C" void kernel_launch(void* const* d_in, const int* in_sizes, int n_in, void* d_out, int out_size, void* d_ws,
                              size_t ws_size, hipStream_t stream) {
  const u16* x = (const u16*)d_in[0];
  const int* src = (const int*)d_in[1];
  const int* dst = (const int*)d_in[2];
  const u16* deg_inv = (const u16*)d_in[3];
  const u16* wmsg = (const u16*)d_in[4];
  const u16* wq = (const u16*)d_in[5];
  const u16* wk = (const u16*)d_in[6];
  const u16* wv = (const u16*)d_in[7];
  const u16* wout = (const u16*)d_in[8];
  char* ws = (char*)d_ws;
  (void)hipFuncSetAttribute((const void*)attn_kernel, hipFuncAttributeMaxDynamicSharedMemorySize, 163840);
  attn_kernel<<<256, 256, 163840, stream>>>(ws, src, dst, deg_inv, x, (u16*)d_out, wq, wk, wv, wmsg, wout);
}
```
